```python
import torch

def _softmax(x, axis=-1):
    return torch.softmax(x, dim=axis)

def _gelu(x):
    return torch.nn.functional.gelu(x, approximate='tanh')

def _silu(x):
    return torch.nn.functional.silu(x)

def _sigmoid(x):
    return torch.sigmoid(x)

def _layernorm(x, axis=-1, eps=1e-05):
    m = x.mean(dim=axis, keepdim=True)
    v = x.var(dim=axis, keepdim=True, unbiased=False)
    return (x - m) / torch.sqrt(v + eps)

def _scatter_add(tgt, idx, src, axis=0):
    return tgt.clone().index_add_(axis, idx, src)

def _segment_sum(src, seg, n):
    out = torch.zeros((n,) + src.shape[1:], dtype=src.dtype)
    return out.index_add_(0, seg, src)

def _pad(x, pw):
    flat = tuple((v for pair in reversed(pw) for v in pair))
    return torch.nn.functional.pad(x, flat)
kernel_name = 'adaLN_cond__memory_aug_recurrent_mem_tr__dense_activation_gelu__prompt'

def setup_inputs():

    _w = lambda a: (lambda t: t.to(torch.bfloat16) if t.is_floating_point() else t)(torch.as_tensor(a))

    def _A_setup():
        B, S, Dm = (1, 2048, 1024)
        H, Dh = (32, 128)
        M, seg = (8, 32)
        seg = min(seg, S)
        S = S // seg * seg
        return {'x': _w((lambda *s: torch.randn(*s, dtype=torch.bfloat16) * 0.05)(B, S, Dm)), 'mem0': _w((lambda *s: torch.randn(*s, dtype=torch.bfloat16) * 0.8)(B, M, Dm)), 'Wq': _w((lambda *s: torch.randn(*s, dtype=torch.bfloat16) * 1.6)(Dm, H, Dh) * Dm ** (-0.5)), 'Wk': _w((lambda *s: torch.randn(*s, dtype=torch.bfloat16) * 1.6)(Dm, H, Dh) * Dm ** (-0.5)), 'Wv': _w((lambda *s: torch.randn(*s, dtype=torch.bfloat16) * 0.8)(Dm, H, Dh) * Dm ** (-0.5)), 'Wo': _w((lambda *s: torch.randn(*s, dtype=torch.bfloat16) * 0.8)(H, Dh, Dm) * (H * Dh) ** (-0.5)), 'seg_len': seg}
    _A_in = _A_setup()

    def _B_setup():
        B, S, D, F = (1, 2048, 1024, 3584)
        return {'x': _w((lambda *s: torch.randn(*s, dtype=torch.bfloat16) * 0.05)(B, S, D)), 'w1': _w((lambda *s: torch.randn(*s, dtype=torch.bfloat16) * 1.6)(D, F) * D ** (-0.5)), 'b1': _w((lambda *s: torch.randn(*s, dtype=torch.bfloat16) * 1.6)(F) * 0.01), 'w2': _w((lambda *s: torch.randn(*s, dtype=torch.bfloat16) * 0.2)(F, D) * F ** (-0.5)), 'b2': _w((lambda *s: torch.randn(*s, dtype=torch.bfloat16) * 0.2)(D) * 0.01)}
    _B_in = _B_setup()
    return {'A_x': _A_in['x'], 'A_mem0': _A_in['mem0'], 'A_Wq': _A_in['Wq'], 'A_Wk': _A_in['Wk'], 'A_Wv': _A_in['Wv'], 'A_Wo': _A_in['Wo'], 'A_seg_len': _A_in['seg_len'], 'B_x': _B_in['x'], 'B_w1': _B_in['w1'], 'B_b1': _B_in['b1'], 'B_w2': _B_in['w2'], 'B_b2': _B_in['b2'], 'c': _w((lambda *s: torch.randn(*s, dtype=torch.bfloat16) * 0.05)(1, 1024) * 8.0), 'w_mod': _w((lambda *s: torch.randn(*s, dtype=torch.bfloat16) * 0.05)(1024, 6 * 1024) * 1024 ** (-0.5) * 8.0)}

def reference(A_x, A_mem0, A_Wq, A_Wk, A_Wv, A_Wo, A_seg_len, B_x, B_w1, B_b1, B_w2, B_b2, c, w_mod):

    def _A_ref(x, mem0, Wq, Wk, Wv, Wo, seg_len):
        B, S, Dm = x.shape
        H, Dh = (Wq.shape[1], Wq.shape[2])
        M = mem0.shape[1]
        scale = Dh ** (-0.5)
        n_seg = S // seg_len
        L = M + seg_len
        idx = torch.arange(L)
        mask = (idx[:, None] < M) | (idx[:, None] >= idx[None, :])
        mem = mem0
        outs = []
        for s in range(n_seg):
            xs = x[:, s * seg_len:(s + 1) * seg_len, :].clone()
            h = torch.concatenate([mem, xs], axis=1)
            q = torch.einsum('btd,dhe->bhte', h, Wq)
            k = torch.einsum('btd,dhe->bhte', h, Wk)
            v = torch.einsum('btd,dhe->bhte', h, Wv)
            scores = torch.einsum('bhqe,bhke->bhqk', q, k) * scale
            scores = torch.where(mask[None, None], scores, -1000000000.0)
            p = _softmax(scores, axis=-1)
            o = torch.einsum('bhqk,bhke->bhqe', p, v)
            o = torch.einsum('bhte,hed->btd', o, Wo)
            mem = o[:, :M, :].clone()
            outs.append(o[:, M:, :])
        return torch.concatenate(outs, axis=1)

    def _B_ref(x, w1, b1, w2, b2):
        h = torch.einsum('bsd,df->bsf', x, w1) + b1
        h = _gelu(h)
        return torch.einsum('bsf,fd->bsd', h, w2) + b2
    x = A_x
    mod = (_silu(c) @ w_mod).reshape(c.shape[0], 6, 1, -1)
    sa, ca, ga, sb, cb, gb = (mod[:, 0].clone(), mod[:, 1].clone(), mod[:, 2].clone(), mod[:, 3].clone(), mod[:, 4].clone(), mod[:, 5].clone())
    h = _layernorm(x, axis=-1) * (1 + ca) + sa
    x = x + ga * _A_ref(h, A_mem0, A_Wq, A_Wk, A_Wv, A_Wo, A_seg_len)
    h = _layernorm(x, axis=-1) * (1 + cb) + sb
    x = x + gb * _B_ref(h, B_w1, B_b1, B_w2, B_b2)
    return x
```

```cpp
#include <hip/hip_runtime.h>
#include <hip/hip_bf16.h>
#include <stdint.h>

typedef unsigned short u16;
typedef unsigned long long u64;
typedef __attribute__((ext_vector_type(8))) short bf16x8;
typedef __attribute__((ext_vector_type(4))) short bf16x4;
typedef __attribute__((ext_vector_type(4))) __bf16 bf16x4_t;
typedef __attribute__((ext_vector_type(8))) __bf16 v8bf;
typedef __attribute__((ext_vector_type(16))) __bf16 v16bf;
typedef __attribute__((ext_vector_type(4))) float f32x4;
typedef __attribute__((ext_vector_type(2))) float f32x2;
typedef unsigned v8u __attribute__((ext_vector_type(8)));
typedef unsigned v4u __attribute__((ext_vector_type(4)));
typedef unsigned v2u __attribute__((ext_vector_type(2)));
#define GAS __attribute__((address_space(1)))
#define LAS __attribute__((address_space(3)))
typedef GAS unsigned gu32;
#define RLX_AGENT __ATOMIC_RELAXED, __HIP_MEMORY_SCOPE_AGENT

constexpr int S_TOK = 2048, DM = 1024, NH = 32, DH = 128, MEMR = 8, SEG = 32, NSEG = 64;
constexpr int HD = NH * DH;
constexpr int QKN = 2 * HD;
constexpr int FF = 3584;

#define NTW 79
constexpr int RG = 16;
#define RGX 16
constexpr unsigned MiB = 1u << 20;
constexpr unsigned O_CTL = 0;
constexpr unsigned O_MOD = 65536;
constexpr unsigned O_POL = O_MOD + 32768;
constexpr unsigned O_MODP = 131072;
constexpr unsigned O_EP = 2 * MiB;
constexpr unsigned O_PS = O_EP + RG * 262144;
constexpr unsigned O_UM = O_PS + RG * 262144;
constexpr unsigned O_E1 = O_UM + RG * 524288;
constexpr unsigned O_H1 = O_E1 + RG * 131072;
constexpr unsigned O_UX = O_H1 + 4 * MiB;
constexpr unsigned O_D = O_UX + RGX * 2 * MiB, D_SLOT = 524288, D_X2 = 14 * 16384, D_ST = 22 * 16384;
constexpr unsigned O_SL = O_D + 32 * MiB;
constexpr unsigned WS_END = O_SL + 2 * MiB;
static_assert(WS_END <= 256 * MiB && O_UX % (2 * MiB) == 0 && O_E1 % (2 * MiB) == 0, "ws");
__device__ __forceinline__ unsigned nearg(unsigned pol, unsigned off, unsigned h) { return off + (((off >> 21) ^ pol ^ h) & 1) * 8192; }
constexpr int C_ABORT = 0;
constexpr int C_A = 32, NARM = 128;
constexpr int C_F = 64;
constexpr int C_H = C_F + 8192;
constexpr unsigned O_HW = 49152;
constexpr int H_C = 0;
constexpr int H_D = 256;
constexpr int H_P = 1024;
__device__ __forceinline__ unsigned hw(unsigned pol, int x) { return nearg(pol, O_HW, x >> 2) + (x & 3) * 2048; }

__device__ __forceinline__ float bf2f(u16 v) { return __uint_as_float(((unsigned)v) << 16); }
__device__ __forceinline__ u16 f2bf(float f) {
    __hip_bfloat16 h = __float2bfloat16(f);
    return __builtin_bit_cast(u16, h);
}
__device__ __forceinline__ float rbf(float f) { return bf2f(f2bf(f)); }

#define CODE_KB 100
#define NPR 6
#define CTC 3
__device__ __forceinline__ void code_touch(unsigned char* lds, int part) {
    u64 pc; const unsigned sz = CODE_KB * 1024;
    asm volatile("s_getpc_b64 %0" : "=s"(pc));
    const v4u r = {(unsigned)pc, (unsigned)(pc >> 32) & 0xFFFFu, sz, 0x00020000u};
    asm volatile("s_mov_b32 m0, %2\n\ts_nop 4\n\tbuffer_load_dword %0, %1, 0 offen lds" :: "v"(((threadIdx.x & 63) + 64 * part) * 128), "s"(r), "s"((unsigned)(size_t)(LAS unsigned char*)lds) : "m0");
}
__global__ __launch_bounds__(256) void k_pre(const u16* __restrict__ c, const u16* __restrict__ w_mod, const u16* __restrict__ mem0, char* __restrict__ ws) {
    __shared__ float red[32][65];
    const int tid = threadIdx.x;
    const unsigned bid = blockIdx.x;
    if (bid >= 24) {
        const int cg = (bid - 24) & 31, kq = (bid - 24) >> 5, c8 = tid & 7, kr = 128 * kq + (tid >> 3);
        v4u wv[4];
#pragma unroll
        for (int i = 0; i < 4; ++i) wv[i] = NTW & 4 ? __builtin_nontemporal_load((const v4u*)(w_mod + (size_t)(kr + 32 * i) * 6144 + cg * 64 + 8 * c8)) : *(const v4u*)(w_mod + (size_t)(kr + 32 * i) * 6144 + cg * 64 + 8 * c8);
        float acc[8] = {0.f, 0.f, 0.f, 0.f, 0.f, 0.f, 0.f, 0.f};
#pragma unroll
        for (int i = 0; i < 4; ++i) {
            const float v = bf2f(c[kr + 32 * i]), sv = rbf(v / (1.f + __expf(-v)));
#pragma unroll
            for (int j = 0; j < 4; ++j) { acc[2 * j] += sv * __uint_as_float(wv[i][j] << 16); acc[2 * j + 1] += sv * __uint_as_float(wv[i][j] & 0xFFFF0000u); }
        }
#pragma unroll
        for (int j = 0; j < 8; ++j) red[tid >> 3][8 * c8 + j] = acc[j];
        __syncthreads();
        if (tid < 64) {
            float v = 0.f;
#pragma unroll
            for (int i = 0; i < 32; ++i) v += red[i][tid];
            ((float*)(ws + O_MODP))[kq * 2048 + cg * 64 + tid] = v;
        }
        return;
    }
    if (bid >= 16) {
        if (bid < 22) *(uint4*)(ws + O_MOD + (bid - 16) * 4096 + tid * 16) = make_uint4(~0u, ~0u, ~0u, ~0u);
        if (bid == 23) { for (int i = 0; i < 4; ++i) code_touch((unsigned char*)red, i * 4 + (tid >> 6)); asm volatile("s_waitcnt vmcnt(0)"); }
        if (bid != 22 || tid) return;
        unsigned best[2] = {~0u, ~0u};
        for (int r = 0; r < NPR; ++r) {
            const u64 t0 = __builtin_readcyclecounter();
            asm volatile("global_store_dword %0, %1, %2 sc1\n\ts_waitcnt vmcnt(0)" :: "v"(O_POL + 128 + (r & 1) * 8192 + (r & 2) * 2048), "v"(0u), "s"(ws) : "memory");
            best[r & 1] = min(best[r & 1], (unsigned)(__builtin_readcyclecounter() - t0));
        }
        const unsigned xcc = __builtin_amdgcn_s_getreg((3 << 11) | 20);
        *(unsigned*)(ws + O_POL) = ((best[1] < best[0]) ^ (xcc >> 2)) & 1;
        return;
    }
    ((uint4*)(ws + O_CTL))[bid * 256 + tid] = make_uint4(0u, 0u, 0u, 0u);
    const unsigned u = (tid >> 6) * 256 + ((bid >> 1) & 3) * 64 + (tid & 63);
    uint4 m = *(const uint4*)(mem0 + (u & 7) * DM + (u >> 4) * 16 + ((u >> 3) & 1) * 8);
    m.x = min(m.x, 0xFFFFFFFEu); m.y = min(m.y, 0xFFFFFFFEu); m.z = min(m.z, 0xFFFFFFFEu); m.w = min(m.w, 0xFFFFFFFEu);
    *(uint4*)(ws + O_E1 + bid * 8192 + tid * 16) = m; *(uint4*)(ws + O_E1 + bid * 8192 + 4096 + tid * 16) = m;
}

enum { P_A, P_B, P_EU, P_DMIN, P_R, P_TP, P_TU, P_T0, P_X, P_TZ, P_TF, P_X2, P_T1, P_TS, P_TT, P_TY, P_T2, P_17, NPAR };
#define CH_PAR 164, 72, 12, 4, 52, 108, 300, 90, 36, -30u, 10, 72, 190, 30, 20, 132, 140, 0
struct Args {
    const u16* Wq; const u16* Wk; const u16* Wv; const u16* Wo;
    const u16* c; const u16* w_mod;
    const u16* x; const u16* w1; const u16* b1; const u16* w2; const u16* b2;
    u16* out; char* ws;
    unsigned mode, pad0;
    unsigned par[NPAR];
};
#define PARS auto pp_ = (__attribute__((address_space(4))) const Args*)__builtin_amdgcn_kernarg_segment_ptr(); asm volatile("" : "+s"(pp_))
#define par(i) pp_->par[i]
#define RSTAMP(sg, i) do { } while (0)
#define STAMP_WAVE 0
#define DBG(a, i) 0ull
constexpr unsigned SPIN_CAP = 1u << 22;
constexpr unsigned SENT = 0xFFFFFFFFu;

__device__ __forceinline__ void lds_barrier() { asm volatile("s_waitcnt lgkmcnt(0)\n\ts_barrier" ::: "memory"); }
__device__ __forceinline__ void wait_vm0() { asm volatile("s_waitcnt vmcnt(0)" ::: "memory"); }
typedef __bf16 bf16x2_t __attribute__((ext_vector_type(2)));
__device__ __forceinline__ unsigned pack2(float a, float b) { const f32x2 v = {a, b}; return __builtin_bit_cast(unsigned, __builtin_convertvector(v, bf16x2_t)); }
__device__ __forceinline__ float lo16f(unsigned u) { return __uint_as_float(u << 16); }
__device__ __forceinline__ float hi16f(unsigned u) { return __uint_as_float(u & 0xFFFF0000u); }
__device__ __forceinline__ unsigned umax4(v4u x) { return max(max(x.x, x.y), max(x.z, x.w)); }
__device__ __forceinline__ unsigned unsent(unsigned x) { return min(x, 0xFFFFFFFEu); }

__device__ __forceinline__ unsigned ldg4_sc1(const void* sb, unsigned vo) {
    unsigned r;
    asm volatile("global_load_dword %0, %1, %2 sc1\n\ts_waitcnt vmcnt(0)" : "=&v"(r) : "v"(vo), "s"(sb) : "memory");
    return r;
}
__device__ __forceinline__ bool spin_fail(unsigned& spins, unsigned* abortw) {
    if (++spins > SPIN_CAP) { __hip_atomic_store((gu32*)abortw, 1u, RLX_AGENT); return true; }
    if ((spins & 63u) == 0 && __builtin_amdgcn_readfirstlane(__hip_atomic_load((gu32*)abortw, RLX_AGENT)) == 1u) return true;
    return false;
}
__device__ __forceinline__ float red16_add(float x) { auto r = __builtin_amdgcn_permlane16_swap(__float_as_uint(x), __float_as_uint(x), false, false); return __uint_as_float(r[0]) + __uint_as_float(r[1]); }
__device__ __forceinline__ float red32_add(float x) { auto r = __builtin_amdgcn_permlane32_swap(__float_as_uint(x), __float_as_uint(x), false, false); return __uint_as_float(r[0]) + __uint_as_float(r[1]); }
__device__ __forceinline__ v2u tr_rd(const unsigned char* lds_base, unsigned off) {
    return __builtin_bit_cast(v2u, __builtin_amdgcn_ds_read_tr16_b64_v4bf16((LAS bf16x4_t*)(lds_base + off)));
}
__device__ __forceinline__ v4u lds16(const unsigned char* p) { return *(const LAS v4u*)p; }
__device__ __forceinline__ void dma16(const void* src, unsigned char* dst, int aux) {
    if (aux) __builtin_amdgcn_global_load_lds((const unsigned*)src, (unsigned*)dst, 16, 0, 2);
    else __builtin_amdgcn_global_load_lds((const unsigned*)src, (unsigned*)dst, 16, 0, 0);
}

__device__ __forceinline__ v4u mkrs(const void* p, unsigned bytes) { return (v4u){(unsigned)(u64)p, (unsigned)((u64)p >> 32) & 0xFFFFu, bytes, 0x00020000u}; }
template <int AUX> __device__ __forceinline__ void bdma(v4u r, unsigned vo, unsigned so, unsigned ldsaddr) {
    if (AUX == 16) asm volatile("s_mov_b32 m0, %0\n\ts_nop 0\n\tbuffer_load_dwordx4 %1, %2, %3 offen sc1 lds" :: "s"(ldsaddr), "v"(vo), "s"(r), "s"(so) : "m0");
    else if (AUX) asm volatile("s_mov_b32 m0, %0\n\ts_nop 0\n\tbuffer_load_dwordx4 %1, %2, %3 offen nt lds" :: "s"(ldsaddr), "v"(vo), "s"(r), "s"(so) : "m0");
    else asm volatile("s_mov_b32 m0, %0\n\ts_nop 0\n\tbuffer_load_dwordx4 %1, %2, %3 offen lds" :: "s"(ldsaddr), "v"(vo), "s"(r), "s"(so) : "m0");
}

template <int I> struct IC { static constexpr int v = I; };
template <int B, int E, class F> __device__ __forceinline__ void sfor(F&& f) { if constexpr (B < E) { f(IC<B>{}); sfor<B + 1, E>(f); } }
#define SB() __builtin_amdgcn_sched_barrier(0)

__device__ __forceinline__ int lane_id() { int l; asm volatile("v_mbcnt_lo_u32_b32 %0, -1, 0\n\tv_mbcnt_hi_u32_b32 %0, -1, %0" : "=v"(l)); return l; }
constexpr int LDS_BYTES = 163840;
typedef __amdgpu_buffer_rsrc_t rsrc_t;
__device__ __forceinline__ v4u bld16(rsrc_t r, unsigned off) { return __builtin_bit_cast(v4u, __builtin_amdgcn_raw_buffer_load_b128(r, off, 0, 16)); }
__device__ __forceinline__ v2u bld8(rsrc_t r, unsigned off) { return __builtin_bit_cast(v2u, __builtin_amdgcn_raw_buffer_load_b64(r, off, 0, 16)); }
__device__ __forceinline__ void bst16(rsrc_t r, unsigned off, v4u v) { __builtin_amdgcn_raw_buffer_store_b128(__builtin_bit_cast(__attribute__((ext_vector_type(4))) unsigned, v), r, off, 0, 16); }
__device__ __forceinline__ void bst8(rsrc_t r, unsigned off, v2u v) { __builtin_amdgcn_raw_buffer_store_b64(__builtin_bit_cast(__attribute__((ext_vector_type(2))) unsigned, v), r, off, 0, 16); }

constexpr int L_FLAG = LDS_BYTES - 64;
#define otid() (a.pad0 * 64 + lane_id())
__device__ __forceinline__ v4u sld16(rsrc_t r, unsigned vo, unsigned so) { return __builtin_bit_cast(v4u, __builtin_amdgcn_raw_buffer_load_b128(r, vo, so, 16)); }
__device__ __forceinline__ v4u sentv() { unsigned x = SENT; asm volatile("" : "+v"(x)); return (v4u){x, x, x, x}; }
#define BW_SLEEP 16
template <int SLP = BW_SLEEP> __device__ __forceinline__ bool wave_wait(const unsigned* p, int stride, int n, unsigned target, unsigned* abortw, unsigned char* lds = nullptr) {
    const int lane = lane_id();
    for (unsigned spins = 0;;) {
        const unsigned v = lane < n ? __hip_atomic_load((gu32*)(p + lane * stride), RLX_AGENT) : target;
        if (!__any(v < target)) { if (lds && lane == 0) *(volatile LAS unsigned*)(lds + L_FLAG + 32) = v; return true; }
        __builtin_amdgcn_s_sleep(SLP);
        if (++spins > (SPIN_CAP << 1)) { __hip_atomic_store((gu32*)abortw, 1u, RLX_AGENT); return false; }
        if ((spins & 63u) == 0 && __builtin_amdgcn_readfirstlane(__hip_atomic_load((gu32*)abortw, RLX_AGENT)) == 1u) return false;
    }
}
__device__ __forceinline__ void drained(unsigned* cnt) {
    asm volatile("s_waitcnt vmcnt(0)\n\ts_barrier" ::: "memory");
    if (threadIdx.x == 0) __hip_atomic_fetch_add((gu32*)cnt, 1u, RLX_AGENT);
}

constexpr int L_W = 0;
constexpr int L_H = 98304;
constexpr int L_KX = L_H + 40960;
constexpr int L_QX = L_KX + 8192;
constexpr int L_T = L_QX + 8192;
constexpr int L_ST = L_T + 4096;
constexpr int lsl(bool isV, int c, int n) { return isV ? (c < 6 && (n & 1) ? 2 * c + (n >> 1) : -1) : (c < 12 && n == 1 ? c : -1); }
constexpr int ridx(bool isV, int c, int n) { int r = 0; for (int i = 0; i < 4 * c + n; ++i) r += lsl(isV, i >> 2, i & 3) < 0; return r; }
#define ASM_Q \
    "s_add_u32 m0, s41, 0\ns_xor_b32 s85, s62, 0\ns_add_u32 s85, s85, s40\nbuffer_load_dwordx4 v11, s[64:67], s85 offen sc1 lds\n" \
    "buffer_load_dwordx4 v12, s[64:67], s85 offen offset:1024 sc1 lds\ns_add_u32 m0, s41, 8192\ns_xor_b32 s85, s62, 256\ns_add_u32 s85, s85, s40\n" \
    "buffer_load_dwordx4 v11, s[64:67], s85 offen sc1 lds\nbuffer_load_dwordx4 v12, s[64:67], s85 offen offset:1024 sc1 lds\ns_add_u32 m0, s41, 16384\ns_xor_b32 s85, s62, 512\n" \
    "s_add_u32 s85, s85, s40\nbuffer_load_dwordx4 v11, s[64:67], s85 offen sc1 lds\nbuffer_load_dwordx4 v12, s[64:67], s85 offen offset:1024 sc1 lds\ns_mov_b32 s83, 0x7fff0000\n" \
    "s_branch .Lq8_%=\n.Lq9_%=:\ns_waitcnt vmcnt(0)\ns_barrier\n.Lq2_%=:\ns_add_u32 s84, s40, 0x20000\ns_cmp_lt_u32 s35, 62\ns_cselect_b32 s84, s84, s40\n" \
    "ds_read_b128 v[64:67], v3 offset:0\nds_read_b128 v[68:71], v9 offset:0\nds_read_b128 v[72:75], v9 offset:4096\ns_add_u32 m0, s41, 24576\ns_xor_b32 s85, s62, 768\n" \
    "s_add_u32 s85, s85, s40\nbuffer_load_dwordx4 v11, s[64:67], s85 offen sc1 lds\nbuffer_load_dwordx4 v12, s[64:67], s85 offen offset:1024 sc1 lds\n" \
    "v_mov_b32_dpp v16, v20 quad_perm:[0,1,2,3] row_mask:0xf bank_mask:0xc\nv_mov_b32_dpp v17, v21 quad_perm:[0,1,2,3] row_mask:0xf bank_mask:0xc\n" \
    "v_mov_b32_dpp v18, v22 quad_perm:[0,1,2,3] row_mask:0xf bank_mask:0xc\nv_mov_b32_dpp v19, v23 quad_perm:[0,1,2,3] row_mask:0xf bank_mask:0xc\nv_mov_b32 v254, 0xff800000\n" \
    "v_cvt_pk_bf16_f32 v16, 0, v16\nv_cvt_pk_bf16_f32 v17, 0, v17\nv_cvt_pk_bf16_f32 v18, 0, v18\nv_cvt_pk_bf16_f32 v19, 0, v19\nv_mul_f32 v16, s33, v16\n" \
    "v_mul_f32 v17, s33, v17\nv_mul_f32 v18, s33, v18\ns_waitcnt lgkmcnt(0)\nv_mfma_f32_16x16x32_bf16 v[48:51], a[8:11], v[68:71], 0\ns_add_u32 m0, s41, 32768\n" \
    "s_xor_b32 s85, s62, 1024\nv_mfma_f32_16x16x32_bf16 v[52:55], a[8:11], v[72:75], 0\nds_read_b128 v[76:79], v3 offset:1024\ns_add_u32 s85, s85, s40\n" \
    "v_mfma_f32_16x16x32_bf16 v[56:59], a[16:19], v[68:71], 0\nds_read_b128 v[80:83], v10 offset:0\nbuffer_load_dwordx4 v11, s[64:67], s85 offen sc1 lds\n" \
    "v_mfma_f32_16x16x32_bf16 v[60:63], a[16:19], v[72:75], 0\nds_read_b128 v[84:87], v10 offset:4096\nv_mfma_f32_16x16x32_bf16 v[32:35], a[0:3], v[68:71], 0\n" \
    "buffer_load_dwordx4 v12, s[64:67], s85 offen offset:1024 sc1 lds\nv_mul_f32 v19, s33, v19\nv_cvt_pk_bf16_f32 v16, 0, v16\n" \
    "v_mfma_f32_16x16x32_bf16 v[36:39], a[0:3], v[72:75], 0\nv_cvt_pk_bf16_f32 v17, 0, v17\nv_cvt_pk_bf16_f32 v18, 0, v18\n" \
    "v_mfma_f32_16x16x32_bf16 v[40:43], v[64:67], v[68:71], 0\nv_cvt_pk_bf16_f32 v19, 0, v19\nv_cndmask_b32 v16, v254, v16, s[50:51]\n" \
    "v_mfma_f32_16x16x32_bf16 v[44:47], v[64:67], v[72:75], 0\ns_waitcnt lgkmcnt(0)\nv_cndmask_b32 v17, v254, v17, s[52:53]\nv_cndmask_b32 v18, v254, v18, s[54:55]\n" \
    "v_mfma_f32_16x16x32_bf16 v[48:51], a[12:15], v[80:83], v[48:51]\nv_cndmask_b32 v19, v254, v19, s[56:57]\nv_max3_f32 v254, v16, v17, v18\n" \
    "v_mfma_f32_16x16x32_bf16 v[52:55], a[12:15], v[84:87], v[52:55]\nds_read_b128 v[88:91], v3 offset:2048\nv_max_f32 v254, v254, v19\n" \
    "v_mfma_f32_16x16x32_bf16 v[56:59], a[20:23], v[80:83], v[56:59]\nds_read_b128 v[64:67], v9 offset:1024\n" \
    "v_max_f32_dpp v252, v254, v254 row_ror:8 row_mask:0xf bank_mask:0xf\nv_max_f32_dpp v253, v254, v254 row_ror:8 row_mask:0xf bank_mask:0xf\n" \
    "v_mfma_f32_16x16x32_bf16 v[60:63], a[20:23], v[84:87], v[60:63]\nds_read_b128 v[68:71], v9 offset:5120\nv_permlane32_swap_b32 v252, v253\n" \
    "v_mfma_f32_16x16x32_bf16 v[32:35], a[4:7], v[80:83], v[32:35]\nv_max_f32 v252, v252, v253\nv_cvt_pk_bf16_f32 v24, 0, v24\n" \
    "v_mfma_f32_16x16x32_bf16 v[36:39], a[4:7], v[84:87], v[36:39]\nv_cvt_pk_bf16_f32 v25, 0, v25\nv_cvt_pk_bf16_f32 v26, 0, v26\n" \
    "v_mfma_f32_16x16x32_bf16 v[40:43], v[76:79], v[80:83], v[40:43]\nv_cvt_pk_bf16_f32 v27, 0, v27\nv_mul_f32 v24, s33, v24\n" \
    "v_mfma_f32_16x16x32_bf16 v[44:47], v[76:79], v[84:87], v[44:47]\ns_waitcnt lgkmcnt(0)\nv_mul_f32 v25, s33, v25\nv_mul_f32 v26, s33, v26\n" \
    "v_mfma_f32_16x16x32_bf16 v[48:51], a[32:35], v[64:67], v[48:51]\nv_mul_f32 v27, s33, v27\nv_cvt_pk_bf16_f32 v24, 0, v24\n" \
    "v_mfma_f32_16x16x32_bf16 v[52:55], a[32:35], v[68:71], v[52:55]\nds_read_b128 v[72:75], v3 offset:3072\nv_cvt_pk_bf16_f32 v25, 0, v25\nv_cvt_pk_bf16_f32 v26, 0, v26\n" \
    "v_mfma_f32_16x16x32_bf16 v[56:59], a[40:43], v[64:67], v[56:59]\nds_read_b128 v[76:79], v10 offset:1024\nv_cvt_pk_bf16_f32 v27, 0, v27\nv_max3_f32 v254, v252, v24, v25\n" \
    "v_mfma_f32_16x16x32_bf16 v[60:63], a[40:43], v[68:71], v[60:63]\nds_read_b128 v[80:83], v10 offset:5120\nv_max3_f32 v252, v254, v26, v27\nv_max3_f32 v253, v254, v26, v27\n" \
    "v_mfma_f32_16x16x32_bf16 v[32:35], a[24:27], v[64:67], v[32:35]\ns_nop 0\nv_permlane16_swap_b32 v252, v253\n" \
    "v_mfma_f32_16x16x32_bf16 v[36:39], a[24:27], v[68:71], v[36:39]\nv_max_f32 v252, v252, v253\nv_sub_f32 v16, v16, v252\n" \
    "v_mfma_f32_16x16x32_bf16 v[40:43], v[88:91], v[64:67], v[40:43]\nv_sub_f32 v17, v17, v252\nv_sub_f32 v18, v18, v252\n" \
    "v_mfma_f32_16x16x32_bf16 v[44:47], v[88:91], v[68:71], v[44:47]\ns_waitcnt lgkmcnt(0)\nv_sub_f32 v19, v19, v252\nv_sub_f32 v24, v24, v252\n" \
    "v_mfma_f32_16x16x32_bf16 v[48:51], a[36:39], v[76:79], v[48:51]\nv_sub_f32 v25, v25, v252\nv_sub_f32 v26, v26, v252\n" \
    "v_mfma_f32_16x16x32_bf16 v[52:55], a[36:39], v[80:83], v[52:55]\nds_read_b128 v[64:67], v3 offset:4096\nv_sub_f32 v27, v27, v252\nv_mul_f32 v16, s34, v16\n" \
    "v_mfma_f32_16x16x32_bf16 v[56:59], a[44:47], v[76:79], v[56:59]\nds_read_b128 v[68:71], v9 offset:8192\nv_mul_f32 v17, s34, v17\nv_mul_f32 v18, s34, v18\n" \
    "v_mfma_f32_16x16x32_bf16 v[60:63], a[44:47], v[80:83], v[60:63]\nds_read_b128 v[84:87], v9 offset:12288\nv_mul_f32 v19, s34, v19\nv_mul_f32 v24, s34, v24\n" \
    "v_mfma_f32_16x16x32_bf16 v[32:35], a[28:31], v[76:79], v[32:35]\nv_mul_f32 v25, s34, v25\nv_mul_f32 v26, s34, v26\n" \
    "v_mfma_f32_16x16x32_bf16 v[36:39], a[28:31], v[80:83], v[36:39]\nv_mul_f32 v27, s34, v27\nv_exp_f32 v16, v16\n" \
    "v_mfma_f32_16x16x32_bf16 v[40:43], v[72:75], v[76:79], v[40:43]\nv_exp_f32 v17, v17\nv_mfma_f32_16x16x32_bf16 v[44:47], v[72:75], v[80:83], v[44:47]\ns_waitcnt lgkmcnt(0)\n" \
    "v_exp_f32 v18, v18\nv_mfma_f32_16x16x32_bf16 v[48:51], a[56:59], v[68:71], v[48:51]\nv_exp_f32 v19, v19\nv_mfma_f32_16x16x32_bf16 v[52:55], a[56:59], v[84:87], v[52:55]\n" \
    "ds_read_b128 v[88:91], v3 offset:5120\nv_exp_f32 v24, v24\nv_mfma_f32_16x16x32_bf16 v[56:59], a[64:67], v[68:71], v[56:59]\nds_read_b128 v[72:75], v10 offset:8192\n" \
    "v_add_f32 v254, v16, v18\nv_add_f32 v255, v17, v19\nv_mfma_f32_16x16x32_bf16 v[60:63], a[64:67], v[84:87], v[60:63]\nds_read_b128 v[76:79], v10 offset:12288\n" \
    "v_exp_f32 v25, v25\nv_mfma_f32_16x16x32_bf16 v[32:35], a[48:51], v[68:71], v[32:35]\nv_add_f32 v254, v254, v255\nv_exp_f32 v26, v26\n" \
    "v_mfma_f32_16x16x32_bf16 v[36:39], a[48:51], v[84:87], v[36:39]\nv_exp_f32 v27, v27\nv_mfma_f32_16x16x32_bf16 v[40:43], v[64:67], v[68:71], v[40:43]\n" \
    "v_add_f32_dpp v252, v254, v254 row_ror:8 row_mask:0xf bank_mask:0xf\nv_add_f32_dpp v253, v254, v254 row_ror:8 row_mask:0xf bank_mask:0xf\n" \
    "v_mfma_f32_16x16x32_bf16 v[44:47], v[64:67], v[84:87], v[44:47]\ns_waitcnt lgkmcnt(0)\nv_add_f32 v254, v24, v26\nv_add_f32 v255, v25, v27\n" \
    "v_mfma_f32_16x16x32_bf16 v[48:51], a[60:63], v[72:75], v[48:51]\nv_permlane32_swap_b32 v252, v253\nv_mfma_f32_16x16x32_bf16 v[52:55], a[60:63], v[76:79], v[52:55]\n" \
    "ds_read_b128 v[64:67], v3 offset:6144\nv_add_f32 v254, v254, v252\nv_add_f32 v255, v255, v253\nv_mfma_f32_16x16x32_bf16 v[56:59], a[68:71], v[72:75], v[56:59]\n" \
    "ds_read_b128 v[68:71], v9 offset:9216\nv_add_f32 v252, v254, v255\nv_add_f32 v253, v255, v254\nv_mfma_f32_16x16x32_bf16 v[60:63], a[68:71], v[76:79], v[60:63]\n" \
    "ds_read_b128 v[80:83], v9 offset:13312\nv_permlane16_swap_b32 v252, v253\nv_mfma_f32_16x16x32_bf16 v[32:35], a[52:55], v[72:75], v[32:35]\nv_add_f32 v252, v252, v253\n" \
    "v_rcp_f32 v252, v252\nv_mfma_f32_16x16x32_bf16 v[36:39], a[52:55], v[76:79], v[36:39]\nv_mul_f32 v24, v24, v252\nv_mul_f32 v25, v25, v252\n" \
    "v_mfma_f32_16x16x32_bf16 v[40:43], v[88:91], v[72:75], v[40:43]\nv_mul_f32 v26, v26, v252\nv_mul_f32 v27, v27, v252\n" \
    "v_mfma_f32_16x16x32_bf16 v[44:47], v[88:91], v[76:79], v[44:47]\ns_waitcnt lgkmcnt(0)\nv_cvt_pk_bf16_f32 v20, v24, v25\nv_cvt_pk_bf16_f32 v21, v26, v27\n" \
    "v_mfma_f32_16x16x32_bf16 v[48:51], a[80:83], v[68:71], v[48:51]\nv_mul_f32 v16, v16, v252\nv_mul_f32 v17, v17, v252\n" \
    "v_mfma_f32_16x16x32_bf16 v[52:55], a[80:83], v[80:83], v[52:55]\nds_read_b128 v[72:75], v3 offset:7168\nv_mul_f32 v18, v18, v252\nv_mul_f32 v19, v19, v252\n" \
    "v_mfma_f32_16x16x32_bf16 v[56:59], a[88:91], v[68:71], v[56:59]\nds_read_b128 v[76:79], v10 offset:9216\nv_permlane16_swap_b32 v20, v22\n" \
    "v_mfma_f32_16x16x32_bf16 v[60:63], a[88:91], v[80:83], v[60:63]\nds_read_b128 v[84:87], v10 offset:13312\nv_cvt_pk_bf16_f32 v16, v16, v17\nv_cvt_pk_bf16_f32 v17, v18, v19\n" \
    "v_mfma_f32_16x16x32_bf16 v[32:35], a[72:75], v[68:71], v[32:35]\nv_permlane16_swap_b32 v21, v23\nv_mfma_f32_16x16x32_bf16 v[36:39], a[72:75], v[80:83], v[36:39]\n" \
    "v_mov_b32_dpp v20, v16 quad_perm:[0,1,2,3] row_mask:0xf bank_mask:0x3\nv_mov_b32_dpp v22, v16 row_ror:8 row_mask:0xf bank_mask:0x3\n" \
    "v_mfma_f32_16x16x32_bf16 v[40:43], v[64:67], v[68:71], v[40:43]\nv_mov_b32_dpp v21, v17 quad_perm:[0,1,2,3] row_mask:0xf bank_mask:0x3\n" \
    "v_mov_b32_dpp v23, v17 row_ror:8 row_mask:0xf bank_mask:0x3\nv_mfma_f32_16x16x32_bf16 v[44:47], v[64:67], v[80:83], v[44:47]\ns_xor_b32 s74, s83, 0x2000\n" \
    "buffer_store_dwordx4 v[20:23], v7, s[64:67], s83 offen sc1\ns_waitcnt lgkmcnt(0)\nv_mfma_f32_16x16x32_bf16 v[48:51], a[84:87], v[76:79], v[48:51]\n" \
    "buffer_store_dwordx4 v[20:23], v7, s[64:67], s74 offen sc1\nv_mov_b64 v[28:29], -1\nv_mov_b64 v[30:31], -1\n" \
    "v_mfma_f32_16x16x32_bf16 v[52:55], a[84:87], v[84:87], v[52:55]\nds_read_b128 v[80:83], v3 offset:8192\ns_add_u32 s69, s35, 2\n" \
    "v_mfma_f32_16x16x32_bf16 v[56:59], a[92:95], v[76:79], v[56:59]\nds_read_b128 v[88:91], v9 offset:16384\ns_and_b32 s69, s69, 15\n" \
    "v_mfma_f32_16x16x32_bf16 v[60:63], a[92:95], v[84:87], v[60:63]\nds_read_b128 v[64:67], v9 offset:20480\ns_lshl_b32 s69, s69, 18\n" \
    "v_mfma_f32_16x16x32_bf16 v[32:35], a[76:79], v[76:79], v[32:35]\ns_add_u32 s82, s38, s69\ns_lshr_b32 s69, s82, 8\n" \
    "v_mfma_f32_16x16x32_bf16 v[36:39], a[76:79], v[84:87], v[36:39]\ns_and_b32 s69, s69, 0x2000\ns_xor_b32 s82, s82, s69\n" \
    "v_mfma_f32_16x16x32_bf16 v[40:43], v[72:75], v[76:79], v[40:43]\ns_add_u32 s69, s35, 2\ns_and_b32 s69, s69, 15\n" \
    "v_mfma_f32_16x16x32_bf16 v[44:47], v[72:75], v[84:87], v[44:47]\ns_lshl_b32 s69, s69, 18\ns_add_u32 s83, s39, s69\ns_waitcnt lgkmcnt(0)\n" \
    "v_mfma_f32_16x16x32_bf16 v[48:51], a[104:107], v[88:91], v[48:51]\ns_xor_b32 s74, s82, 0x4000\ns_xor_b32 s75, s83, 0x2000\n" \
    "v_mfma_f32_16x16x32_bf16 v[52:55], a[104:107], v[64:67], v[52:55]\nds_read_b128 v[84:87], v3 offset:9216\n" \
    "v_mfma_f32_16x16x32_bf16 v[56:59], a[112:115], v[88:91], v[56:59]\nds_read_b128 v[68:71], v10 offset:16384\n" \
    "v_mfma_f32_16x16x32_bf16 v[60:63], a[112:115], v[64:67], v[60:63]\nds_read_b128 v[72:75], v10 offset:20480\n" \
    "v_mfma_f32_16x16x32_bf16 v[32:35], a[96:99], v[88:91], v[32:35]\nv_mfma_f32_16x16x32_bf16 v[36:39], a[96:99], v[64:67], v[36:39]\n" \
    "v_mfma_f32_16x16x32_bf16 v[40:43], v[80:83], v[88:91], v[40:43]\nbuffer_store_dwordx4 v[28:31], v7, s[64:67], s82 offen sc1\n" \
    "v_mfma_f32_16x16x32_bf16 v[44:47], v[80:83], v[64:67], v[44:47]\ns_waitcnt lgkmcnt(0)\nv_mfma_f32_16x16x32_bf16 v[48:51], a[108:111], v[68:71], v[48:51]\n" \
    "v_mfma_f32_16x16x32_bf16 v[52:55], a[108:111], v[72:75], v[52:55]\nds_read_b128 v[64:67], v3 offset:10240\n" \
    "v_mfma_f32_16x16x32_bf16 v[56:59], a[116:119], v[68:71], v[56:59]\nds_read_b128 v[88:91], v9 offset:17408\n" \
    "v_mfma_f32_16x16x32_bf16 v[60:63], a[116:119], v[72:75], v[60:63]\nds_read_b128 v[76:79], v9 offset:21504\n" \
    "v_mfma_f32_16x16x32_bf16 v[32:35], a[100:103], v[68:71], v[32:35]\nbuffer_store_dwordx4 v[28:31], v7, s[64:67], s74 offen sc1\n" \
    "v_mfma_f32_16x16x32_bf16 v[36:39], a[100:103], v[72:75], v[36:39]\nv_mfma_f32_16x16x32_bf16 v[40:43], v[84:87], v[68:71], v[40:43]\n" \
    "v_mfma_f32_16x16x32_bf16 v[44:47], v[84:87], v[72:75], v[44:47]\ns_waitcnt lgkmcnt(0)\nv_mfma_f32_16x16x32_bf16 v[48:51], a[128:131], v[88:91], v[48:51]\n" \
    "v_mfma_f32_16x16x32_bf16 v[52:55], a[128:131], v[76:79], v[52:55]\nds_read_b128 v[68:71], v3 offset:11264\n" \
    "v_mfma_f32_16x16x32_bf16 v[56:59], a[136:139], v[88:91], v[56:59]\nds_read_b128 v[72:75], v10 offset:17408\nbuffer_store_dwordx4 v[28:31], v7, s[64:67], s83 offen sc1\n" \
    "v_mfma_f32_16x16x32_bf16 v[60:63], a[136:139], v[76:79], v[60:63]\nds_read_b128 v[80:83], v10 offset:21504\n" \
    "v_mfma_f32_16x16x32_bf16 v[32:35], a[120:123], v[88:91], v[32:35]\nv_mfma_f32_16x16x32_bf16 v[36:39], a[120:123], v[76:79], v[36:39]\n" \
    "v_mfma_f32_16x16x32_bf16 v[40:43], v[64:67], v[88:91], v[40:43]\nv_mfma_f32_16x16x32_bf16 v[44:47], v[64:67], v[76:79], v[44:47]\ns_waitcnt lgkmcnt(0)\n" \
    "v_mfma_f32_16x16x32_bf16 v[48:51], a[132:135], v[72:75], v[48:51]\nbuffer_store_dwordx4 v[28:31], v7, s[64:67], s75 offen sc1\ns_add_u32 s69, s35, 2\n" \
    "v_mfma_f32_16x16x32_bf16 v[52:55], a[132:135], v[80:83], v[52:55]\nds_read_b128 v[64:67], v3 offset:12288\ns_and_b32 s69, s69, 255\ns_waitcnt vmcnt(6)\ns_barrier\n" \
    "v_mfma_f32_16x16x32_bf16 v[56:59], a[140:143], v[72:75], v[56:59]\nds_read_b128 v[76:79], v9 offset:24576\ns_lshl_b32 s69, s69, 19\n" \
    "v_mfma_f32_16x16x32_bf16 v[60:63], a[140:143], v[80:83], v[60:63]\nds_read_b128 v[84:87], v9 offset:28672\ns_add_u32 m0, s41, 0\n" \
    "v_mfma_f32_16x16x32_bf16 v[32:35], a[124:127], v[72:75], v[32:35]\ns_xor_b32 s85, s62, 1280\ns_add_u32 s85, s85, s40\n" \
    "v_mfma_f32_16x16x32_bf16 v[36:39], a[124:127], v[80:83], v[36:39]\nbuffer_load_dwordx4 v11, s[64:67], s85 offen sc1 lds\ns_add_u32 s74, s63, s69\n" \
    "v_mfma_f32_16x16x32_bf16 v[40:43], v[68:71], v[72:75], v[40:43]\ns_cmp_lt_u32 s35, 62\ns_cselect_b32 s74, s74, 0x7fff0000\n" \
    "v_mfma_f32_16x16x32_bf16 v[44:47], v[68:71], v[80:83], v[44:47]\nbuffer_load_dwordx4 v12, s[64:67], s85 offen offset:1024 sc1 lds\ns_add_u32 m0, s41, 8192\n" \
    "s_waitcnt lgkmcnt(0)\nv_mfma_f32_16x16x32_bf16 v[48:51], a[152:155], v[76:79], v[48:51]\ns_xor_b32 s85, s62, 1536\ns_add_u32 s85, s85, s40\n" \
    "v_mfma_f32_16x16x32_bf16 v[52:55], a[152:155], v[84:87], v[52:55]\nds_read_b128 v[80:83], v3 offset:13312\nbuffer_load_dwordx4 v11, s[64:67], s85 offen sc1 lds\n" \
    "v_mfma_f32_16x16x32_bf16 v[56:59], a[160:163], v[76:79], v[56:59]\nds_read_b128 v[88:91], v10 offset:24576\n" \
    "v_mfma_f32_16x16x32_bf16 v[60:63], a[160:163], v[84:87], v[60:63]\nds_read_b128 v[68:71], v10 offset:28672\n" \
    "buffer_load_dwordx4 v12, s[64:67], s85 offen offset:1024 sc1 lds\nv_mfma_f32_16x16x32_bf16 v[32:35], a[144:147], v[76:79], v[32:35]\n" \
    "buffer_store_dwordx4 v[28:31], v2, s[64:67], s74 offen offset:0 sc1\nv_mfma_f32_16x16x32_bf16 v[36:39], a[144:147], v[84:87], v[36:39]\n" \
    "v_mfma_f32_16x16x32_bf16 v[40:43], v[64:67], v[76:79], v[40:43]\nv_mfma_f32_16x16x32_bf16 v[44:47], v[64:67], v[84:87], v[44:47]\ns_waitcnt lgkmcnt(0)\n" \
    "v_mfma_f32_16x16x32_bf16 v[48:51], a[156:159], v[88:91], v[48:51]\nv_mfma_f32_16x16x32_bf16 v[52:55], a[156:159], v[68:71], v[52:55]\n" \
    "ds_read_b128 v[64:67], v3 offset:14336\nv_mfma_f32_16x16x32_bf16 v[56:59], a[164:167], v[88:91], v[56:59]\nds_read_b128 v[84:87], v9 offset:25600\n" \
    "buffer_store_dwordx4 v[28:31], v2, s[64:67], s74 offen offset:1024 sc1\nv_mfma_f32_16x16x32_bf16 v[60:63], a[164:167], v[68:71], v[60:63]\n" \
    "ds_read_b128 v[72:75], v9 offset:29696\ns_and_b32 s69, s35, 15\nv_mfma_f32_16x16x32_bf16 v[32:35], a[148:151], v[88:91], v[32:35]\ns_lshl_b32 s69, s69, 18\n" \
    "s_add_u32 s82, s38, s69\nv_mfma_f32_16x16x32_bf16 v[36:39], a[148:151], v[68:71], v[36:39]\ns_lshr_b32 s69, s82, 8\ns_and_b32 s69, s69, 0x2000\n" \
    "v_mfma_f32_16x16x32_bf16 v[40:43], v[80:83], v[88:91], v[40:43]\ns_xor_b32 s82, s82, s69\ns_and_b32 s69, s35, 15\n" \
    "v_mfma_f32_16x16x32_bf16 v[44:47], v[80:83], v[68:71], v[44:47]\ns_lshl_b32 s69, s69, 18\ns_add_u32 s83, s39, s69\ns_waitcnt lgkmcnt(0)\n" \
    "v_mfma_f32_16x16x32_bf16 v[48:51], a[176:179], v[84:87], v[48:51]\nv_mfma_f32_16x16x32_bf16 v[52:55], a[176:179], v[72:75], v[52:55]\n" \
    "ds_read_b128 v[68:71], v3 offset:15360\nv_mfma_f32_16x16x32_bf16 v[56:59], a[184:187], v[84:87], v[56:59]\nds_read_b128 v[88:91], v10 offset:25600\n" \
    "v_mfma_f32_16x16x32_bf16 v[60:63], a[184:187], v[72:75], v[60:63]\nds_read_b128 v[76:79], v10 offset:29696\n" \
    "v_mfma_f32_16x16x32_bf16 v[32:35], a[168:171], v[84:87], v[32:35]\nv_mfma_f32_16x16x32_bf16 v[36:39], a[168:171], v[72:75], v[36:39]\n" \
    "v_mfma_f32_16x16x32_bf16 v[40:43], v[64:67], v[84:87], v[40:43]\nv_mfma_f32_16x16x32_bf16 v[44:47], v[64:67], v[72:75], v[44:47]\ns_waitcnt lgkmcnt(0)\n" \
    "v_mfma_f32_16x16x32_bf16 v[48:51], a[180:183], v[88:91], v[48:51]\nv_mfma_f32_16x16x32_bf16 v[52:55], a[180:183], v[76:79], v[52:55]\n" \
    "ds_read_b128 v[64:67], v3 offset:16384\nv_mfma_f32_16x16x32_bf16 v[56:59], a[188:191], v[88:91], v[56:59]\nds_read_b128 v[72:75], v9 offset:32768\n" \
    "v_mfma_f32_16x16x32_bf16 v[60:63], a[188:191], v[76:79], v[60:63]\nds_read_b128 v[80:83], v9 offset:36864\n" \
    "v_mfma_f32_16x16x32_bf16 v[32:35], a[172:175], v[88:91], v[32:35]\nv_mfma_f32_16x16x32_bf16 v[36:39], a[172:175], v[76:79], v[36:39]\n" \
    "v_mfma_f32_16x16x32_bf16 v[40:43], v[68:71], v[88:91], v[40:43]\nv_mfma_f32_16x16x32_bf16 v[44:47], v[68:71], v[76:79], v[44:47]\ns_waitcnt lgkmcnt(0)\n" \
    "v_mfma_f32_16x16x32_bf16 v[48:51], a[200:203], v[72:75], v[48:51]\nv_mfma_f32_16x16x32_bf16 v[52:55], a[200:203], v[80:83], v[52:55]\n" \
    "ds_read_b128 v[68:71], v3 offset:17408\nv_mfma_f32_16x16x32_bf16 v[56:59], a[208:211], v[72:75], v[56:59]\nds_read_b128 v[76:79], v10 offset:32768\n" \
    "v_mfma_f32_16x16x32_bf16 v[60:63], a[208:211], v[80:83], v[60:63]\nds_read_b128 v[84:87], v10 offset:36864\n" \
    "v_mfma_f32_16x16x32_bf16 v[32:35], a[192:195], v[72:75], v[32:35]\nv_mfma_f32_16x16x32_bf16 v[36:39], a[192:195], v[80:83], v[36:39]\n" \
    "v_mfma_f32_16x16x32_bf16 v[40:43], v[64:67], v[72:75], v[40:43]\nv_mfma_f32_16x16x32_bf16 v[44:47], v[64:67], v[80:83], v[44:47]\ns_waitcnt lgkmcnt(0)\n" \
    "v_mfma_f32_16x16x32_bf16 v[48:51], a[204:207], v[76:79], v[48:51]\nv_mfma_f32_16x16x32_bf16 v[52:55], a[204:207], v[84:87], v[52:55]\n" \
    "ds_read_b128 v[64:67], v3 offset:18432\nv_mfma_f32_16x16x32_bf16 v[56:59], a[212:215], v[76:79], v[56:59]\nds_read_b128 v[72:75], v9 offset:33792\n" \
    "v_mfma_f32_16x16x32_bf16 v[60:63], a[212:215], v[84:87], v[60:63]\nds_read_b128 v[80:83], v9 offset:37888\n" \
    "v_mfma_f32_16x16x32_bf16 v[32:35], a[196:199], v[76:79], v[32:35]\nv_mfma_f32_16x16x32_bf16 v[36:39], a[196:199], v[84:87], v[36:39]\n" \
    "v_mfma_f32_16x16x32_bf16 v[40:43], v[68:71], v[76:79], v[40:43]\nv_mfma_f32_16x16x32_bf16 v[44:47], v[68:71], v[84:87], v[44:47]\ns_waitcnt lgkmcnt(0)\n" \
    "v_mfma_f32_16x16x32_bf16 v[48:51], a[224:227], v[72:75], v[48:51]\nv_mfma_f32_16x16x32_bf16 v[52:55], a[224:227], v[80:83], v[52:55]\n" \
    "ds_read_b128 v[68:71], v3 offset:19456\nv_mfma_f32_16x16x32_bf16 v[56:59], a[232:235], v[72:75], v[56:59]\nds_read_b128 v[76:79], v10 offset:33792\n" \
    "v_mfma_f32_16x16x32_bf16 v[60:63], a[232:235], v[80:83], v[60:63]\nds_read_b128 v[84:87], v10 offset:37888\n" \
    "v_mfma_f32_16x16x32_bf16 v[32:35], a[216:219], v[72:75], v[32:35]\nv_mfma_f32_16x16x32_bf16 v[36:39], a[216:219], v[80:83], v[36:39]\n" \
    "v_mfma_f32_16x16x32_bf16 v[40:43], v[64:67], v[72:75], v[40:43]\nv_mfma_f32_16x16x32_bf16 v[44:47], v[64:67], v[80:83], v[44:47]\ns_waitcnt lgkmcnt(0)\n" \
    "v_mfma_f32_16x16x32_bf16 v[48:51], a[228:231], v[76:79], v[48:51]\nv_mfma_f32_16x16x32_bf16 v[52:55], a[228:231], v[84:87], v[52:55]\n" \
    "ds_read_b128 v[64:67], v3 offset:20480\ns_waitcnt vmcnt(2)\ns_barrier\nv_mfma_f32_16x16x32_bf16 v[56:59], a[236:239], v[76:79], v[56:59]\n" \
    "ds_read_b128 v[72:75], v9 offset:0\nv_mfma_f32_16x16x32_bf16 v[60:63], a[236:239], v[84:87], v[60:63]\nds_read_b128 v[80:83], v9 offset:4096\ns_add_u32 m0, s41, 16384\n" \
    "v_mfma_f32_16x16x32_bf16 v[32:35], a[220:223], v[76:79], v[32:35]\ns_xor_b32 s85, s62, 1792\ns_add_u32 s85, s85, s40\n" \
    "v_mfma_f32_16x16x32_bf16 v[36:39], a[220:223], v[84:87], v[36:39]\nbuffer_load_dwordx4 v11, s[64:67], s85 offen sc1 lds\n" \
    "v_mfma_f32_16x16x32_bf16 v[40:43], v[68:71], v[76:79], v[40:43]\nv_mfma_f32_16x16x32_bf16 v[44:47], v[68:71], v[84:87], v[44:47]\n" \
    "buffer_load_dwordx4 v12, s[64:67], s85 offen offset:1024 sc1 lds\ns_waitcnt lgkmcnt(0)\nv_mfma_f32_16x16x32_bf16 v[48:51], a[248:251], v[72:75], v[48:51]\n" \
    "v_mfma_f32_16x16x32_bf16 v[52:55], a[248:251], v[80:83], v[52:55]\nds_read_b128 v[68:71], v3 offset:21504\nv_mfma_f32_16x16x32_bf16 v[56:59], v[92:95], v[72:75], v[56:59]\n" \
    "ds_read_b128 v[76:79], v10 offset:0\nv_mfma_f32_16x16x32_bf16 v[60:63], v[92:95], v[80:83], v[60:63]\nds_read_b128 v[84:87], v10 offset:4096\n" \
    "v_mfma_f32_16x16x32_bf16 v[32:35], a[240:243], v[72:75], v[32:35]\nv_mfma_f32_16x16x32_bf16 v[36:39], a[240:243], v[80:83], v[36:39]\n" \
    "v_mfma_f32_16x16x32_bf16 v[40:43], v[64:67], v[72:75], v[40:43]\nv_mfma_f32_16x16x32_bf16 v[44:47], v[64:67], v[80:83], v[44:47]\ns_waitcnt lgkmcnt(0)\n" \
    "v_mfma_f32_16x16x32_bf16 v[48:51], a[252:255], v[76:79], v[48:51]\nv_mfma_f32_16x16x32_bf16 v[52:55], a[252:255], v[84:87], v[52:55]\n" \
    "ds_read_b128 v[64:67], v3 offset:22528\nv_mfma_f32_16x16x32_bf16 v[56:59], v[96:99], v[76:79], v[56:59]\nds_read_b128 v[72:75], v9 offset:1024\n" \
    "v_mfma_f32_16x16x32_bf16 v[60:63], v[96:99], v[84:87], v[60:63]\nds_read_b128 v[80:83], v9 offset:5120\nv_mfma_f32_16x16x32_bf16 v[32:35], a[244:247], v[76:79], v[32:35]\n" \
    "v_mfma_f32_16x16x32_bf16 v[36:39], a[244:247], v[84:87], v[36:39]\nv_mfma_f32_16x16x32_bf16 v[40:43], v[68:71], v[76:79], v[40:43]\n" \
    "v_mfma_f32_16x16x32_bf16 v[44:47], v[68:71], v[84:87], v[44:47]\ns_waitcnt lgkmcnt(0)\nv_mfma_f32_16x16x32_bf16 v[48:51], v[108:111], v[72:75], v[48:51]\n" \
    "v_mfma_f32_16x16x32_bf16 v[52:55], v[108:111], v[80:83], v[52:55]\nds_read_b128 v[68:71], v3 offset:23552\n" \
    "v_mfma_f32_16x16x32_bf16 v[56:59], v[116:119], v[72:75], v[56:59]\nds_read_b128 v[76:79], v10 offset:1024\n" \
    "v_mfma_f32_16x16x32_bf16 v[60:63], v[116:119], v[80:83], v[60:63]\nds_read_b128 v[84:87], v10 offset:5120\n" \
    "v_mfma_f32_16x16x32_bf16 v[32:35], v[100:103], v[72:75], v[32:35]\nv_mfma_f32_16x16x32_bf16 v[36:39], v[100:103], v[80:83], v[36:39]\n" \
    "v_mfma_f32_16x16x32_bf16 v[40:43], v[64:67], v[72:75], v[40:43]\nv_mfma_f32_16x16x32_bf16 v[44:47], v[64:67], v[80:83], v[44:47]\ns_waitcnt lgkmcnt(0)\n" \
    "v_mfma_f32_16x16x32_bf16 v[48:51], v[112:115], v[76:79], v[48:51]\nv_mfma_f32_16x16x32_bf16 v[52:55], v[112:115], v[84:87], v[52:55]\n" \
    "v_mfma_f32_16x16x32_bf16 v[56:59], v[120:123], v[76:79], v[56:59]\nds_read_b128 v[64:67], v9 offset:8192\n" \
    "v_mfma_f32_16x16x32_bf16 v[60:63], v[120:123], v[84:87], v[60:63]\nds_read_b128 v[72:75], v9 offset:12288\n" \
    "v_mfma_f32_16x16x32_bf16 v[32:35], v[104:107], v[76:79], v[32:35]\nv_mfma_f32_16x16x32_bf16 v[36:39], v[104:107], v[84:87], v[36:39]\n" \
    "v_mfma_f32_16x16x32_bf16 v[40:43], v[68:71], v[76:79], v[40:43]\nv_mfma_f32_16x16x32_bf16 v[44:47], v[68:71], v[84:87], v[44:47]\ns_waitcnt lgkmcnt(0)\n" \
    "v_mfma_f32_16x16x32_bf16 v[48:51], v[140:143], v[64:67], v[48:51]\nv_mfma_f32_16x16x32_bf16 v[52:55], v[140:143], v[72:75], v[52:55]\n" \
    "v_mfma_f32_16x16x32_bf16 v[56:59], v[148:151], v[64:67], v[56:59]\nds_read_b128 v[68:71], v10 offset:8192\n" \
    "v_mfma_f32_16x16x32_bf16 v[60:63], v[148:151], v[72:75], v[60:63]\nds_read_b128 v[76:79], v10 offset:12288\n" \
    "v_mfma_f32_16x16x32_bf16 v[32:35], v[124:127], v[64:67], v[32:35]\nv_mfma_f32_16x16x32_bf16 v[36:39], v[124:127], v[72:75], v[36:39]\n" \
    "v_mfma_f32_16x16x32_bf16 v[40:43], v[132:135], v[64:67], v[40:43]\nv_mfma_f32_16x16x32_bf16 v[44:47], v[132:135], v[72:75], v[44:47]\ns_waitcnt lgkmcnt(0)\n" \
    "v_mfma_f32_16x16x32_bf16 v[48:51], v[144:147], v[68:71], v[48:51]\nv_mfma_f32_16x16x32_bf16 v[52:55], v[144:147], v[76:79], v[52:55]\n" \
    "v_mfma_f32_16x16x32_bf16 v[56:59], v[152:155], v[68:71], v[56:59]\nds_read_b128 v[64:67], v9 offset:9216\n" \
    "v_mfma_f32_16x16x32_bf16 v[60:63], v[152:155], v[76:79], v[60:63]\nds_read_b128 v[72:75], v9 offset:13312\n" \
    "v_mfma_f32_16x16x32_bf16 v[32:35], v[128:131], v[68:71], v[32:35]\nv_mfma_f32_16x16x32_bf16 v[36:39], v[128:131], v[76:79], v[36:39]\n" \
    "v_mfma_f32_16x16x32_bf16 v[40:43], v[136:139], v[68:71], v[40:43]\nv_mfma_f32_16x16x32_bf16 v[44:47], v[136:139], v[76:79], v[44:47]\ns_waitcnt lgkmcnt(0)\n" \
    "v_mfma_f32_16x16x32_bf16 v[48:51], v[172:175], v[64:67], v[48:51]\nv_mfma_f32_16x16x32_bf16 v[52:55], v[172:175], v[72:75], v[52:55]\n" \
    "v_mfma_f32_16x16x32_bf16 v[56:59], v[180:183], v[64:67], v[56:59]\nds_read_b128 v[68:71], v10 offset:9216\n" \
    "v_mfma_f32_16x16x32_bf16 v[60:63], v[180:183], v[72:75], v[60:63]\nds_read_b128 v[76:79], v10 offset:13312\n" \
    "v_mfma_f32_16x16x32_bf16 v[32:35], v[156:159], v[64:67], v[32:35]\nv_mfma_f32_16x16x32_bf16 v[36:39], v[156:159], v[72:75], v[36:39]\n" \
    "v_mfma_f32_16x16x32_bf16 v[40:43], v[164:167], v[64:67], v[40:43]\nv_mfma_f32_16x16x32_bf16 v[44:47], v[164:167], v[72:75], v[44:47]\n" \
    "buffer_load_dword v13, off, s[64:67], s42 sc1\ns_waitcnt lgkmcnt(0)\nv_mfma_f32_16x16x32_bf16 v[48:51], v[176:179], v[68:71], v[48:51]\n" \
    "v_mfma_f32_16x16x32_bf16 v[52:55], v[176:179], v[76:79], v[52:55]\ns_waitcnt vmcnt(1)\ns_barrier\nv_mfma_f32_16x16x32_bf16 v[56:59], v[184:187], v[68:71], v[56:59]\n" \
    "ds_read_b128 v[64:67], v9 offset:16384\nv_mfma_f32_16x16x32_bf16 v[60:63], v[184:187], v[76:79], v[60:63]\nds_read_b128 v[72:75], v9 offset:20480\ns_add_u32 m0, s41, 0\n" \
    "v_mfma_f32_16x16x32_bf16 v[32:35], v[160:163], v[68:71], v[32:35]\ns_xor_b32 s85, s62, 0\ns_add_u32 s85, s85, s84\n" \
    "v_mfma_f32_16x16x32_bf16 v[36:39], v[160:163], v[76:79], v[36:39]\nbuffer_load_dwordx4 v11, s[64:67], s85 offen sc1 lds\n" \
    "v_mfma_f32_16x16x32_bf16 v[40:43], v[168:171], v[68:71], v[40:43]\nv_mfma_f32_16x16x32_bf16 v[44:47], v[168:171], v[76:79], v[44:47]\n" \
    "buffer_load_dwordx4 v12, s[64:67], s85 offen offset:1024 sc1 lds\ns_waitcnt lgkmcnt(0)\nv_mfma_f32_16x16x32_bf16 v[48:51], v[204:207], v[64:67], v[48:51]\n" \
    "v_mfma_f32_16x16x32_bf16 v[52:55], v[204:207], v[72:75], v[52:55]\nv_mfma_f32_16x16x32_bf16 v[56:59], v[212:215], v[64:67], v[56:59]\n" \
    "ds_read_b128 v[68:71], v10 offset:16384\nv_mfma_f32_16x16x32_bf16 v[60:63], v[212:215], v[72:75], v[60:63]\nds_read_b128 v[76:79], v10 offset:20480\n" \
    "v_mfma_f32_16x16x32_bf16 v[32:35], v[188:191], v[64:67], v[32:35]\nv_mfma_f32_16x16x32_bf16 v[36:39], v[188:191], v[72:75], v[36:39]\n" \
    "v_mfma_f32_16x16x32_bf16 v[40:43], v[196:199], v[64:67], v[40:43]\nv_mfma_f32_16x16x32_bf16 v[44:47], v[196:199], v[72:75], v[44:47]\ns_waitcnt lgkmcnt(0)\n" \
    "v_mfma_f32_16x16x32_bf16 v[48:51], v[208:211], v[68:71], v[48:51]\nv_mfma_f32_16x16x32_bf16 v[52:55], v[208:211], v[76:79], v[52:55]\n" \
    "v_mfma_f32_16x16x32_bf16 v[56:59], v[216:219], v[68:71], v[56:59]\nds_read_b128 v[64:67], v9 offset:17408\n" \
    "v_mfma_f32_16x16x32_bf16 v[60:63], v[216:219], v[76:79], v[60:63]\nds_read_b128 v[72:75], v9 offset:21504\n" \
    "v_mfma_f32_16x16x32_bf16 v[32:35], v[192:195], v[68:71], v[32:35]\nv_mfma_f32_16x16x32_bf16 v[36:39], v[192:195], v[76:79], v[36:39]\n" \
    "v_mfma_f32_16x16x32_bf16 v[40:43], v[200:203], v[68:71], v[40:43]\nv_mfma_f32_16x16x32_bf16 v[44:47], v[200:203], v[76:79], v[44:47]\ns_waitcnt lgkmcnt(0)\n" \
    "v_mfma_f32_16x16x32_bf16 v[48:51], v[236:239], v[64:67], v[48:51]\nv_mfma_f32_16x16x32_bf16 v[52:55], v[236:239], v[72:75], v[52:55]\n" \
    "v_mfma_f32_16x16x32_bf16 v[56:59], v[244:247], v[64:67], v[56:59]\nds_read_b128 v[68:71], v10 offset:17408\n" \
    "v_mfma_f32_16x16x32_bf16 v[60:63], v[244:247], v[72:75], v[60:63]\nds_read_b128 v[76:79], v10 offset:21504\n" \
    "v_mfma_f32_16x16x32_bf16 v[32:35], v[220:223], v[64:67], v[32:35]\nv_mfma_f32_16x16x32_bf16 v[36:39], v[220:223], v[72:75], v[36:39]\n" \
    "v_mfma_f32_16x16x32_bf16 v[40:43], v[228:231], v[64:67], v[40:43]\nv_mfma_f32_16x16x32_bf16 v[44:47], v[228:231], v[72:75], v[44:47]\ns_waitcnt lgkmcnt(0)\n" \
    "v_mfma_f32_16x16x32_bf16 v[48:51], v[240:243], v[68:71], v[48:51]\nv_mfma_f32_16x16x32_bf16 v[52:55], v[240:243], v[76:79], v[52:55]\n" \
    "v_mfma_f32_16x16x32_bf16 v[56:59], v[248:251], v[68:71], v[56:59]\nv_mfma_f32_16x16x32_bf16 v[60:63], v[248:251], v[76:79], v[60:63]\n" \
    "v_mfma_f32_16x16x32_bf16 v[32:35], v[224:227], v[68:71], v[32:35]\nv_mfma_f32_16x16x32_bf16 v[36:39], v[224:227], v[76:79], v[36:39]\n" \
    "v_mfma_f32_16x16x32_bf16 v[40:43], v[232:235], v[68:71], v[40:43]\nv_mfma_f32_16x16x32_bf16 v[44:47], v[232:235], v[76:79], v[44:47]\ns_barrier\n" \
    "v_cvt_pk_bf16_f32 v64, v48, v49\nv_cvt_pk_bf16_f32 v65, v50, v51\nv_cvt_pk_bf16_f32 v66, v56, v57\nv_cvt_pk_bf16_f32 v67, v58, v59\nds_write_b128 v14, v[64:67] offset:0\n" \
    "v_cvt_pk_bf16_f32 v68, v52, v53\nv_cvt_pk_bf16_f32 v69, v54, v55\nv_cvt_pk_bf16_f32 v70, v60, v61\nv_cvt_pk_bf16_f32 v71, v62, v63\n" \
    "ds_write_b128 v14, v[68:71] offset:4096\nv_cvt_pk_bf16_f32 v72, v32, v33\nv_cvt_pk_bf16_f32 v73, v34, v35\nv_cvt_pk_bf16_f32 v74, v40, v41\n" \
    "v_cvt_pk_bf16_f32 v75, v42, v43\nds_write_b128 v14, v[72:75] offset:8192\nv_cvt_pk_bf16_f32 v76, v36, v37\nv_cvt_pk_bf16_f32 v77, v38, v39\n" \
    "v_cvt_pk_bf16_f32 v78, v44, v45\nv_cvt_pk_bf16_f32 v79, v46, v47\nds_write_b128 v14, v[76:79] offset:12288\ns_add_u32 m0, s41, 8192\ns_xor_b32 s85, s62, 256\n" \
    "s_add_u32 s85, s85, s84\nbuffer_load_dwordx4 v11, s[64:67], s85 offen sc1 lds\nbuffer_load_dwordx4 v12, s[64:67], s85 offen offset:1024 sc1 lds\ns_add_u32 m0, s41, 16384\n" \
    "s_xor_b32 s85, s62, 512\ns_add_u32 s85, s85, s84\nbuffer_load_dwordx4 v11, s[64:67], s85 offen sc1 lds\nbuffer_load_dwordx4 v12, s[64:67], s85 offen offset:1024 sc1 lds\n" \
    "s_and_b32 s69, s35, 15\ns_lshl_b32 s69, s69, 17\ns_add_u32 s81, s37, s69\ns_lshr_b32 s69, s81, 8\ns_and_b32 s69, s69, 0x2000\ns_xor_b32 s81, s81, s69\ns_mov_b32 s68, 0\n" \
    "s_lshr_b32 s69, s35, 1\ns_cmp_eq_u32 s69, 5\ns_cbranch_scc1 .Lq14_%=\n.Lq15_%=:\ns_cmp_eq_u32 s35, 0\ns_cbranch_scc1 .Lq16_%=\ns_waitcnt vmcnt(6)\n.Lq17_%=:\n" \
    "v_readfirstlane_b32 s74, v13\ns_nop 0\ns_and_b32 s75, s74, 127\ns_cmp_lt_u32 s75, s35\ns_cbranch_scc1 .Lq13_%=\ns_memrealtime s[72:73]\ns_waitcnt lgkmcnt(0)\n" \
    "s_lshr_b32 s69, s74, 8\ns_sub_u32 s69, s69, s72\ns_bfe_i32 s69, s69, 0x180000\ns_add_u32 s80, s72, s69\ns_sub_u32 s76, s80, s45\ns_sub_u32 s69, s72, s76\n" \
    "s_cmp_ge_i32 s69, -8\ns_cbranch_scc1 .Lq11_%=\n.Lq18_%=:\ns_memrealtime s[72:73]\ns_waitcnt lgkmcnt(0)\ns_sub_u32 s72, s72, s76\ns_cmp_lt_i32 s72, 0\n" \
    "s_cbranch_scc1 .Lq18_%=\ns_branch .Lq10_%=\n.Lq11_%=:\ns_lshl_b32 s74, s74, 5\ns_and_b32 s74, s74, 0x1000\ns_xor_b32 s81, s81, s74\ns_lshl_b32 s74, s74, 2\n" \
    "s_xor_b32 s82, s82, s74\n.Lq12_%=:\ns_mov_b32 s68, 0\ns_cmp_eq_u32 s36, 1\ns_cbranch_scc1 .Lq5_%=\ns_cmp_eq_u32 s36, 2\ns_cbranch_scc1 .Lq6_%=\ns_cmp_eq_u32 s36, 3\n" \
    "s_cbranch_scc1 .Lq7_%=\n.Lq4_%=:\nds_read_b128 v[32:35], v3 offset:0\nds_read_b128 v[36:39], v3 offset:1024\nds_read_b128 v[40:43], v3 offset:8192\n" \
    "ds_read_b128 v[44:47], v3 offset:9216\nds_read_b128 v[48:51], v3 offset:16384\nds_read_b128 v[52:55], v3 offset:17408\nds_read_b128 v[56:59], v3 offset:2048\n" \
    "ds_read_b128 v[60:63], v3 offset:3072\nds_read_b128 v[64:67], v3 offset:4096\nds_read_b128 v[68:71], v3 offset:5120\n.Lq19_%=:\ns_xor_b32 s90, s81, 0x800\n.Lq21_%=:\n" \
    "s_memrealtime s[72:73]\ns_waitcnt lgkmcnt(0)\ns_sub_u32 s72, s72, s80\ns_cmp_lt_i32 s72, 0\ns_cbranch_scc1 .Lq21_%=\n" \
    "buffer_load_dwordx4 v[72:75], v2, s[64:67], s81 offen offset:0 sc1\nbuffer_load_dwordx4 v[76:79], v2, s[64:67], s81 offen offset:1024 sc1\n" \
    "buffer_load_dwordx4 v[80:83], v2, s[64:67], s90 offen offset:0 sc1\nbuffer_load_dwordx4 v[84:87], v2, s[64:67], s90 offen offset:1024 sc1\nv_mov_b64 v[16:17], 0\n" \
    "v_mov_b64 v[18:19], 0\nv_mov_b64 v[20:21], 0\nv_mov_b64 v[22:23], 0\nv_mov_b64 v[24:25], 0\nv_mov_b64 v[26:27], 0\nv_mov_b64 v[28:29], 0\nv_mov_b64 v[30:31], 0\n" \
    "s_waitcnt vmcnt(3)\nds_write_b128 v1, v[72:75] offset:24576\nv_smfmac_f32_16x16x64_bf16 v[20:23], v[72:75], v[32:39], v0\nv_max3_u32 v252, v72, v73, v74\n" \
    "v_smfmac_f32_16x16x64_bf16 v[16:19], v[72:75], a[0:7], v0\ns_waitcnt vmcnt(2)\nds_write_b128 v1, v[76:79] offset:28672\nv_max3_u32 v252, v252, v75, v76\n" \
    "v_smfmac_f32_16x16x64_bf16 v[24:27], v[72:75], a[8:15], v0\nv_max3_u32 v253, v77, v78, v79\nv_smfmac_f32_16x16x64_bf16 v[28:31], v[72:75], a[16:23], v0\n" \
    "s_waitcnt vmcnt(1)\nds_write_b128 v1, v[80:83] offset:32768\nv_max3_u32 v252, v252, v80, v81\nv_smfmac_f32_16x16x64_bf16 v[20:23], v[76:79], v[40:47], v0\n" \
    "v_max3_u32 v253, v253, v82, v83\nv_smfmac_f32_16x16x64_bf16 v[16:19], v[76:79], a[96:103], v0\ns_waitcnt vmcnt(0)\nds_write_b128 v1, v[84:87] offset:36864\n" \
    "v_max3_u32 v252, v252, v84, v85\nv_smfmac_f32_16x16x64_bf16 v[24:27], v[76:79], a[104:111], v0\nv_max3_u32 v253, v253, v86, v87\n" \
    "v_smfmac_f32_16x16x64_bf16 v[28:31], v[76:79], a[112:119], v0\nv_max_u32 v252, v252, v253\nv_smfmac_f32_16x16x64_bf16 v[20:23], v[80:83], v[48:55], v0\n" \
    "v_cmp_eq_u32 vcc, -1, v252\nv_smfmac_f32_16x16x64_bf16 v[16:19], v[80:83], a[192:199], v0\ns_cbranch_vccnz .Lq20_%=\ns_waitcnt lgkmcnt(0)\ns_barrier\n" \
    "ds_read_b128 v[88:91], v1 offset:25600\nds_read_b128 v[32:35], v1 offset:26624\nds_read_b128 v[36:39], v1 offset:27648\n" \
    "v_smfmac_f32_16x16x64_bf16 v[24:27], v[80:83], a[200:207], v0\nds_read_b128 v[40:43], v3 offset:6144\nv_smfmac_f32_16x16x64_bf16 v[28:31], v[80:83], a[208:215], v0\n" \
    "ds_read_b128 v[44:47], v3 offset:7168\nv_smfmac_f32_16x16x64_bf16 v[20:23], v[84:87], v[132:139], v0\nds_read_b128 v[48:51], v1 offset:29696\n" \
    "v_smfmac_f32_16x16x64_bf16 v[16:19], v[84:87], v[124:131], v0\nds_read_b128 v[72:75], v3 offset:10240\nv_smfmac_f32_16x16x64_bf16 v[24:27], v[84:87], v[140:147], v0\n" \
    "ds_read_b128 v[76:79], v3 offset:11264\nv_smfmac_f32_16x16x64_bf16 v[28:31], v[84:87], v[148:155], v0\nds_read_b128 v[52:55], v1 offset:30720\ns_waitcnt lgkmcnt(8)\n" \
    "v_smfmac_f32_16x16x64_bf16 v[20:23], v[88:91], v[56:63], v0\nds_read_b128 v[56:59], v3 offset:12288\nv_smfmac_f32_16x16x64_bf16 v[16:19], v[88:91], a[24:31], v0\n" \
    "ds_read_b128 v[60:63], v3 offset:13312\nv_smfmac_f32_16x16x64_bf16 v[24:27], v[88:91], a[32:39], v0\nds_read_b128 v[80:83], v1 offset:31744\n" \
    "v_smfmac_f32_16x16x64_bf16 v[28:31], v[88:91], a[40:47], v0\nds_read_b128 v[84:87], v3 offset:14336\ns_waitcnt lgkmcnt(11)\n" \
    "v_smfmac_f32_16x16x64_bf16 v[20:23], v[32:35], v[64:71], v0\nds_read_b128 v[88:91], v3 offset:15360\nv_smfmac_f32_16x16x64_bf16 v[16:19], v[32:35], a[48:55], v0\n" \
    "ds_read_b128 v[64:67], v1 offset:33792\ns_waitcnt lgkmcnt(10)\nv_smfmac_f32_16x16x64_bf16 v[20:23], v[36:39], v[40:47], v0\nds_read_b128 v[40:43], v3 offset:18432\n" \
    "v_smfmac_f32_16x16x64_bf16 v[16:19], v[36:39], a[72:79], v0\nds_read_b128 v[44:47], v3 offset:19456\ns_waitcnt lgkmcnt(9)\n" \
    "v_smfmac_f32_16x16x64_bf16 v[20:23], v[48:51], v[72:79], v0\nds_read_b128 v[68:71], v1 offset:34816\nv_smfmac_f32_16x16x64_bf16 v[16:19], v[48:51], a[120:127], v0\n" \
    "ds_read_b128 v[72:75], v3 offset:20480\ns_waitcnt lgkmcnt(8)\nv_smfmac_f32_16x16x64_bf16 v[20:23], v[52:55], v[56:63], v0\nds_read_b128 v[76:79], v3 offset:21504\n" \
    "v_smfmac_f32_16x16x64_bf16 v[16:19], v[52:55], a[144:151], v0\nds_read_b128 v[56:59], v1 offset:35840\ns_waitcnt lgkmcnt(7)\n" \
    "v_smfmac_f32_16x16x64_bf16 v[20:23], v[80:83], v[84:91], v0\nds_read_b128 v[84:87], v3 offset:22528\nv_smfmac_f32_16x16x64_bf16 v[16:19], v[80:83], a[168:175], v0\n" \
    "ds_read_b128 v[88:91], v3 offset:23552\ns_waitcnt lgkmcnt(6)\nv_smfmac_f32_16x16x64_bf16 v[20:23], v[64:67], v[40:47], v0\n" \
    "v_smfmac_f32_16x16x64_bf16 v[16:19], v[64:67], a[216:223], v0\nds_read_b128 v[60:63], v1 offset:37888\ns_waitcnt lgkmcnt(4)\n" \
    "v_smfmac_f32_16x16x64_bf16 v[20:23], v[68:71], v[72:79], v0\nds_read_b128 v[40:43], v1 offset:38912\nv_smfmac_f32_16x16x64_bf16 v[16:19], v[68:71], a[240:247], v0\n" \
    "ds_read_b128 v[44:47], v1 offset:39936\ns_waitcnt lgkmcnt(3)\nv_smfmac_f32_16x16x64_bf16 v[20:23], v[56:59], v[84:91], v0\n" \
    "v_smfmac_f32_16x16x64_bf16 v[16:19], v[56:59], v[100:107], v0\ns_waitcnt lgkmcnt(2)\nv_smfmac_f32_16x16x64_bf16 v[20:23], v[60:63], v[164:171], v0\ns_waitcnt lgkmcnt(1)\n" \
    "v_smfmac_f32_16x16x64_bf16 v[20:23], v[40:43], v[196:203], v0\ns_waitcnt lgkmcnt(0)\nv_smfmac_f32_16x16x64_bf16 v[20:23], v[44:47], v[228:235], v0\ns_nop 2\n" \
    "v_smfmac_f32_16x16x64_bf16 v[16:19], v[60:63], v[156:163], v0\ns_nop 2\nv_smfmac_f32_16x16x64_bf16 v[16:19], v[40:43], v[188:195], v0\nv_permlane32_swap_b32 v20, v22\n" \
    "v_smfmac_f32_16x16x64_bf16 v[16:19], v[44:47], v[220:227], v0\ns_nop 1\nv_permlane32_swap_b32 v21, v23\nv_smfmac_f32_16x16x64_bf16 v[24:27], v[32:35], a[56:63], v0\n" \
    "s_nop 1\nv_add_f32 v20, v20, v22\nv_smfmac_f32_16x16x64_bf16 v[28:31], v[32:35], a[64:71], v0\nv_add_f32 v21, v21, v23\n" \
    "v_smfmac_f32_16x16x64_bf16 v[24:27], v[36:39], a[80:87], v0\nv_mul_f32 v20, s32, v20\nv_smfmac_f32_16x16x64_bf16 v[28:31], v[36:39], a[88:95], v0\nv_mul_f32 v21, s32, v21\n" \
    "v_smfmac_f32_16x16x64_bf16 v[24:27], v[48:51], a[128:135], v0\nv_cvt_pk_bf16_f32 v20, v20, v21\nv_smfmac_f32_16x16x64_bf16 v[28:31], v[48:51], a[136:143], v0\n" \
    "ds_write_b16 v4, v20 offset:8\nv_permlane32_swap_b32 v16, v18\nv_smfmac_f32_16x16x64_bf16 v[24:27], v[52:55], a[152:159], v0\nds_write_b16_d16_hi v4, v20 offset:24\n" \
    "v_permlane32_swap_b32 v17, v19\nv_smfmac_f32_16x16x64_bf16 v[24:27], v[80:83], a[176:183], v0\nds_read_b128 v[48:51], v1 offset:40960\nv_add_f32 v16, v16, v18\n" \
    "v_smfmac_f32_16x16x64_bf16 v[24:27], v[64:67], a[224:231], v0\nds_read_b128 v[32:35], v1 offset:45056\nv_add_f32 v17, v17, v19\n" \
    "v_smfmac_f32_16x16x64_bf16 v[24:27], v[68:71], a[248:255], v0\nds_read_b128 v[36:39], v1 offset:41984\nv_mul_f32 v16, s32, v16\n" \
    "v_smfmac_f32_16x16x64_bf16 v[24:27], v[56:59], v[108:115], v0\nds_read_b128 v[72:75], v1 offset:46080\nv_mul_f32 v17, s32, v17\n" \
    "v_smfmac_f32_16x16x64_bf16 v[24:27], v[60:63], v[172:179], v0\nds_read_b128 v[76:79], v1 offset:43008\nv_cvt_pk_bf16_f32 v16, v16, v17\n" \
    "v_smfmac_f32_16x16x64_bf16 v[24:27], v[40:43], v[204:211], v0\nds_write_b16 v4, v16 offset:0\nv_smfmac_f32_16x16x64_bf16 v[24:27], v[44:47], v[236:243], v0\n" \
    "ds_write_b16_d16_hi v4, v16 offset:16\ns_nop 1\nv_smfmac_f32_16x16x64_bf16 v[28:31], v[52:55], a[160:167], v0\nds_read_b128 v[52:55], v1 offset:47104\ns_nop 1\n" \
    "v_smfmac_f32_16x16x64_bf16 v[28:31], v[80:83], a[184:191], v0\nds_read_b128 v[80:83], v1 offset:44032\nv_permlane32_swap_b32 v24, v26\n" \
    "v_smfmac_f32_16x16x64_bf16 v[28:31], v[64:67], a[232:239], v0\nds_read_b128 v[64:67], v1 offset:48128\nv_permlane32_swap_b32 v25, v27\n" \
    "v_smfmac_f32_16x16x64_bf16 v[28:31], v[68:71], v[92:99], v0\ns_waitcnt lgkmcnt(3)\ns_barrier\nds_read_b128 v[68:71], v6 offset:0\nv_add_f32 v24, v24, v26\n" \
    "v_smfmac_f32_16x16x64_bf16 v[28:31], v[56:59], v[116:123], v0\nds_read_b128 v[56:59], v6 offset:512\nv_add_f32 v25, v25, v27\n" \
    "v_smfmac_f32_16x16x64_bf16 v[28:31], v[60:63], v[180:187], v0\nds_read_b128 v[60:63], v6 offset:1024\nv_cvt_pk_bf16_f32 v24, v24, v25\n" \
    "v_smfmac_f32_16x16x64_bf16 v[28:31], v[40:43], v[212:219], v0\nds_read_b128 v[40:43], v6 offset:1536\nv_smfmac_f32_16x16x64_bf16 v[28:31], v[44:47], v[244:251], v0\n" \
    "ds_write_b16 v4, v24 offset:2048\nds_write_b16_d16_hi v4, v24 offset:2064\ns_waitcnt lgkmcnt(5)\nv_mfma_f32_16x16x32_bf16 v[16:19], v[48:51], v[68:71], 0\ns_nop 2\n" \
    "v_mfma_f32_16x16x32_bf16 v[20:23], v[32:35], v[68:71], 0\nv_permlane32_swap_b32 v28, v30\nv_permlane32_swap_b32 v29, v31\nv_add_f32 v28, v28, v30\nv_add_f32 v29, v29, v31\n" \
    "v_cvt_pk_bf16_f32 v28, v28, v29\nds_write_b16 v4, v28 offset:2056\nds_write_b16_d16_hi v4, v28 offset:2072\ns_waitcnt lgkmcnt(6)\n" \
    "v_mfma_f32_16x16x32_bf16 v[16:19], v[36:39], v[56:59], v[16:19]\nv_mfma_f32_16x16x32_bf16 v[20:23], v[72:75], v[56:59], v[20:23]\ns_waitcnt lgkmcnt(5)\n" \
    "v_mfma_f32_16x16x32_bf16 v[16:19], v[76:79], v[60:63], v[16:19]\nv_mfma_f32_16x16x32_bf16 v[20:23], v[52:55], v[60:63], v[20:23]\ns_waitcnt lgkmcnt(0)\ns_barrier\n" \
    "ds_read_b128 v[44:47], v6 offset:2048\nds_read_b128 v[84:87], v6 offset:2560\nv_mfma_f32_16x16x32_bf16 v[16:19], v[80:83], v[40:43], v[16:19]\n" \
    "ds_read_b128 v[88:91], v6 offset:3072\nds_read_b128 v[28:31], v6 offset:3584\nv_mfma_f32_16x16x32_bf16 v[20:23], v[64:67], v[40:43], v[20:23]\nv_mov_b32 v15, v15\n" \
    "v_mov_b32 v15, v15\nv_mov_b32 v15, v15\nv_mov_b32 v15, v15\nv_mov_b32 v15, v15\nv_mov_b32 v15, v15\ns_waitcnt lgkmcnt(3)\n" \
    "v_mfma_f32_16x16x32_bf16 v[24:27], v[44:47], v[68:71], 0\ns_waitcnt lgkmcnt(2)\nv_mov_b32_dpp v16, v20 quad_perm:[0,1,2,3] row_mask:0xf bank_mask:0xc\n" \
    "v_mov_b32_dpp v17, v21 quad_perm:[0,1,2,3] row_mask:0xf bank_mask:0xc\nv_mfma_f32_16x16x32_bf16 v[24:27], v[84:87], v[56:59], v[24:27]\ns_waitcnt lgkmcnt(1)\n" \
    "v_mov_b32_dpp v18, v22 quad_perm:[0,1,2,3] row_mask:0xf bank_mask:0xc\nv_mov_b32_dpp v19, v23 quad_perm:[0,1,2,3] row_mask:0xf bank_mask:0xc\n" \
    "v_mfma_f32_16x16x32_bf16 v[24:27], v[88:91], v[60:63], v[24:27]\ns_waitcnt lgkmcnt(0)\nv_max3_f32 v254, v16, v17, v18\nv_max_f32 v254, v254, v19\n" \
    "v_mfma_f32_16x16x32_bf16 v[24:27], v[28:31], v[40:43], v[24:27]\ns_nop 0\nv_max_f32_dpp v252, v254, v254 row_ror:8 row_mask:0xf bank_mask:0xf\n" \
    "v_max_f32_dpp v253, v254, v254 row_ror:8 row_mask:0xf bank_mask:0xf\ns_nop 1\nv_permlane32_swap_b32 v252, v253\nv_max_f32 v252, v252, v253\ns_xor_b32 s74, s82, 0x4000\n" \
    "v_max3_f32 v254, v252, v24, v25\nv_max3_f32 v252, v254, v26, v27\nv_max3_f32 v253, v254, v26, v27\ns_nop 1\nv_permlane16_swap_b32 v252, v253\nv_max_f32 v252, v252, v253\n" \
    "v_pk_add_f32 v[16:17], v[16:17], v[252:253] op_sel_hi:[1,0] neg_lo:[0,1] neg_hi:[0,1]\n" \
    "v_pk_add_f32 v[18:19], v[18:19], v[252:253] op_sel_hi:[1,0] neg_lo:[0,1] neg_hi:[0,1]\n" \
    "v_pk_add_f32 v[24:25], v[24:25], v[252:253] op_sel_hi:[1,0] neg_lo:[0,1] neg_hi:[0,1]\n" \
    "v_pk_add_f32 v[26:27], v[26:27], v[252:253] op_sel_hi:[1,0] neg_lo:[0,1] neg_hi:[0,1]\nv_exp_f32 v16, v16\nv_exp_f32 v17, v17\nv_exp_f32 v18, v18\nv_exp_f32 v19, v19\n" \
    "v_exp_f32 v24, v24\nv_pk_add_f32 v[254:255], v[16:17], v[18:19]\nv_exp_f32 v25, v25\nv_add_f32 v254, v254, v255\nv_exp_f32 v26, v26\nv_exp_f32 v27, v27\n" \
    "v_add_f32_dpp v252, v254, v254 row_ror:8 row_mask:0xf bank_mask:0xf\nv_add_f32_dpp v253, v254, v254 row_ror:8 row_mask:0xf bank_mask:0xf\n" \
    "v_pk_add_f32 v[254:255], v[24:25], v[26:27]\ns_nop 0\nv_permlane32_swap_b32 v252, v253\nv_pk_add_f32 v[254:255], v[254:255], v[252:253]\n" \
    "v_pk_add_f32 v[252:253], v[254:255], v[254:255] op_sel:[0,1] op_sel_hi:[1,0]\ns_nop 1\nv_permlane16_swap_b32 v252, v253\nv_add_f32 v252, v252, v253\nv_rcp_f32 v252, v252\n" \
    "s_nop 0\nv_pk_mul_f32 v[24:25], v[24:25], v[252:253] op_sel_hi:[1,0]\nv_pk_mul_f32 v[26:27], v[26:27], v[252:253] op_sel_hi:[1,0]\nv_cvt_pk_bf16_f32 v20, v24, v25\n" \
    "v_cvt_pk_bf16_f32 v21, v26, v27\nv_pk_mul_f32 v[16:17], v[16:17], v[252:253] op_sel_hi:[1,0]\nv_pk_mul_f32 v[18:19], v[18:19], v[252:253] op_sel_hi:[1,0]\n" \
    "v_permlane16_swap_b32 v20, v22\nv_cvt_pk_bf16_f32 v16, v16, v17\nv_cvt_pk_bf16_f32 v17, v18, v19\nv_permlane16_swap_b32 v21, v23\n" \
    "v_mov_b32_dpp v20, v16 quad_perm:[0,1,2,3] row_mask:0xf bank_mask:0x3\nv_mov_b32_dpp v22, v16 row_ror:8 row_mask:0xf bank_mask:0x3\n" \
    "v_mov_b32_dpp v21, v17 quad_perm:[0,1,2,3] row_mask:0xf bank_mask:0x3\nv_mov_b32_dpp v23, v17 row_ror:8 row_mask:0xf bank_mask:0x3\n" \
    "buffer_store_dwordx4 v[20:23], v7, s[64:67], s82 offen sc1\nbuffer_store_dwordx4 v[20:23], v7, s[64:67], s74 offen sc1\ns_branch .Lq3_%=\n.Lq5_%=:\n" \
    "ds_read_b128 v[32:35], v3 offset:2048\nds_read_b128 v[36:39], v3 offset:3072\nds_read_b128 v[40:43], v3 offset:10240\nds_read_b128 v[44:47], v3 offset:11264\n" \
    "ds_read_b128 v[48:51], v3 offset:18432\nds_read_b128 v[52:55], v3 offset:19456\nds_read_b128 v[56:59], v3 offset:0\nds_read_b128 v[60:63], v3 offset:1024\n" \
    "ds_read_b128 v[64:67], v3 offset:4096\nds_read_b128 v[68:71], v3 offset:5120\n.Lq23_%=:\ns_xor_b32 s90, s81, 0x800\n.Lq25_%=:\ns_memrealtime s[72:73]\n" \
    "s_waitcnt lgkmcnt(0)\ns_sub_u32 s72, s72, s80\ns_cmp_lt_i32 s72, 0\ns_cbranch_scc1 .Lq25_%=\nbuffer_load_dwordx4 v[72:75], v2, s[64:67], s81 offen offset:0 sc1\n" \
    "buffer_load_dwordx4 v[76:79], v2, s[64:67], s81 offen offset:1024 sc1\nbuffer_load_dwordx4 v[80:83], v2, s[64:67], s90 offen offset:0 sc1\n" \
    "buffer_load_dwordx4 v[84:87], v2, s[64:67], s90 offen offset:1024 sc1\nv_mov_b64 v[16:17], 0\nv_mov_b64 v[18:19], 0\nv_mov_b64 v[20:21], 0\nv_mov_b64 v[22:23], 0\n" \
    "v_mov_b64 v[24:25], 0\nv_mov_b64 v[26:27], 0\nv_mov_b64 v[28:29], 0\nv_mov_b64 v[30:31], 0\ns_waitcnt vmcnt(3)\nds_write_b128 v1, v[72:75] offset:25600\n" \
    "v_smfmac_f32_16x16x64_bf16 v[20:23], v[72:75], v[32:39], v0\nv_max3_u32 v252, v72, v73, v74\nv_smfmac_f32_16x16x64_bf16 v[16:19], v[72:75], a[24:31], v0\n" \
    "s_waitcnt vmcnt(2)\nds_write_b128 v1, v[76:79] offset:29696\nv_max3_u32 v252, v252, v75, v76\nv_smfmac_f32_16x16x64_bf16 v[24:27], v[72:75], a[32:39], v0\n" \
    "v_max3_u32 v253, v77, v78, v79\nv_smfmac_f32_16x16x64_bf16 v[28:31], v[72:75], a[40:47], v0\ns_waitcnt vmcnt(1)\nds_write_b128 v1, v[80:83] offset:33792\n" \
    "v_max3_u32 v252, v252, v80, v81\nv_smfmac_f32_16x16x64_bf16 v[20:23], v[76:79], v[40:47], v0\nv_max3_u32 v253, v253, v82, v83\n" \
    "v_smfmac_f32_16x16x64_bf16 v[16:19], v[76:79], a[120:127], v0\ns_waitcnt vmcnt(0)\nds_write_b128 v1, v[84:87] offset:37888\nv_max3_u32 v252, v252, v84, v85\n" \
    "v_smfmac_f32_16x16x64_bf16 v[24:27], v[76:79], a[128:135], v0\nv_max3_u32 v253, v253, v86, v87\nv_smfmac_f32_16x16x64_bf16 v[28:31], v[76:79], a[136:143], v0\n" \
    "v_max_u32 v252, v252, v253\nv_smfmac_f32_16x16x64_bf16 v[20:23], v[80:83], v[48:55], v0\nv_cmp_eq_u32 vcc, -1, v252\n" \
    "v_smfmac_f32_16x16x64_bf16 v[16:19], v[80:83], a[216:223], v0\ns_cbranch_vccnz .Lq24_%=\ns_waitcnt lgkmcnt(0)\ns_barrier\nds_read_b128 v[88:91], v1 offset:24576\n" \
    "ds_read_b128 v[32:35], v1 offset:26624\nds_read_b128 v[36:39], v1 offset:27648\nv_smfmac_f32_16x16x64_bf16 v[24:27], v[80:83], a[224:231], v0\n" \
    "ds_read_b128 v[40:43], v3 offset:6144\nv_smfmac_f32_16x16x64_bf16 v[28:31], v[80:83], a[232:239], v0\nds_read_b128 v[44:47], v3 offset:7168\n" \
    "v_smfmac_f32_16x16x64_bf16 v[20:23], v[84:87], v[164:171], v0\nds_read_b128 v[48:51], v1 offset:28672\nv_smfmac_f32_16x16x64_bf16 v[16:19], v[84:87], v[156:163], v0\n" \
    "ds_read_b128 v[72:75], v3 offset:8192\nv_smfmac_f32_16x16x64_bf16 v[24:27], v[84:87], v[172:179], v0\nds_read_b128 v[76:79], v3 offset:9216\n" \
    "v_smfmac_f32_16x16x64_bf16 v[28:31], v[84:87], v[180:187], v0\nds_read_b128 v[52:55], v1 offset:30720\ns_waitcnt lgkmcnt(8)\n" \
    "v_smfmac_f32_16x16x64_bf16 v[20:23], v[88:91], v[56:63], v0\nds_read_b128 v[56:59], v3 offset:12288\nv_smfmac_f32_16x16x64_bf16 v[16:19], v[88:91], a[0:7], v0\n" \
    "ds_read_b128 v[60:63], v3 offset:13312\nv_smfmac_f32_16x16x64_bf16 v[24:27], v[88:91], a[8:15], v0\nds_read_b128 v[80:83], v1 offset:31744\n" \
    "v_smfmac_f32_16x16x64_bf16 v[28:31], v[88:91], a[16:23], v0\nds_read_b128 v[84:87], v3 offset:14336\ns_waitcnt lgkmcnt(11)\n" \
    "v_smfmac_f32_16x16x64_bf16 v[20:23], v[32:35], v[64:71], v0\nds_read_b128 v[88:91], v3 offset:15360\nv_smfmac_f32_16x16x64_bf16 v[16:19], v[32:35], a[48:55], v0\n" \
    "ds_read_b128 v[64:67], v1 offset:32768\ns_waitcnt lgkmcnt(10)\nv_smfmac_f32_16x16x64_bf16 v[20:23], v[36:39], v[40:47], v0\nds_read_b128 v[40:43], v3 offset:16384\n" \
    "v_smfmac_f32_16x16x64_bf16 v[16:19], v[36:39], a[72:79], v0\nds_read_b128 v[44:47], v3 offset:17408\ns_waitcnt lgkmcnt(9)\n" \
    "v_smfmac_f32_16x16x64_bf16 v[20:23], v[48:51], v[72:79], v0\nds_read_b128 v[68:71], v1 offset:34816\nv_smfmac_f32_16x16x64_bf16 v[16:19], v[48:51], a[96:103], v0\n" \
    "ds_read_b128 v[72:75], v3 offset:20480\ns_waitcnt lgkmcnt(8)\nv_smfmac_f32_16x16x64_bf16 v[20:23], v[52:55], v[56:63], v0\nds_read_b128 v[76:79], v3 offset:21504\n" \
    "v_smfmac_f32_16x16x64_bf16 v[16:19], v[52:55], a[144:151], v0\nds_read_b128 v[56:59], v1 offset:35840\ns_waitcnt lgkmcnt(7)\n" \
    "v_smfmac_f32_16x16x64_bf16 v[20:23], v[80:83], v[84:91], v0\nds_read_b128 v[84:87], v3 offset:22528\nv_smfmac_f32_16x16x64_bf16 v[16:19], v[80:83], a[168:175], v0\n" \
    "ds_read_b128 v[88:91], v3 offset:23552\ns_waitcnt lgkmcnt(6)\nv_smfmac_f32_16x16x64_bf16 v[20:23], v[64:67], v[40:47], v0\n" \
    "v_smfmac_f32_16x16x64_bf16 v[16:19], v[64:67], a[192:199], v0\nds_read_b128 v[60:63], v1 offset:36864\ns_waitcnt lgkmcnt(4)\n" \
    "v_smfmac_f32_16x16x64_bf16 v[20:23], v[68:71], v[72:79], v0\nds_read_b128 v[40:43], v1 offset:38912\nv_smfmac_f32_16x16x64_bf16 v[16:19], v[68:71], a[240:247], v0\n" \
    "ds_read_b128 v[44:47], v1 offset:39936\ns_waitcnt lgkmcnt(3)\nv_smfmac_f32_16x16x64_bf16 v[20:23], v[56:59], v[84:91], v0\n" \
    "v_smfmac_f32_16x16x64_bf16 v[16:19], v[56:59], v[100:107], v0\ns_waitcnt lgkmcnt(2)\nv_smfmac_f32_16x16x64_bf16 v[20:23], v[60:63], v[132:139], v0\ns_waitcnt lgkmcnt(1)\n" \
    "v_smfmac_f32_16x16x64_bf16 v[20:23], v[40:43], v[196:203], v0\ns_waitcnt lgkmcnt(0)\nv_smfmac_f32_16x16x64_bf16 v[20:23], v[44:47], v[228:235], v0\ns_nop 2\n" \
    "v_smfmac_f32_16x16x64_bf16 v[16:19], v[60:63], v[124:131], v0\ns_nop 2\nv_smfmac_f32_16x16x64_bf16 v[16:19], v[40:43], v[188:195], v0\nv_permlane32_swap_b32 v20, v22\n" \
    "v_smfmac_f32_16x16x64_bf16 v[16:19], v[44:47], v[220:227], v0\ns_nop 1\nv_permlane32_swap_b32 v21, v23\nv_smfmac_f32_16x16x64_bf16 v[24:27], v[32:35], a[56:63], v0\n" \
    "s_nop 1\nv_add_f32 v20, v20, v22\nv_smfmac_f32_16x16x64_bf16 v[28:31], v[32:35], a[64:71], v0\nv_add_f32 v21, v21, v23\n" \
    "v_smfmac_f32_16x16x64_bf16 v[24:27], v[36:39], a[80:87], v0\nv_mul_f32 v20, s32, v20\nv_smfmac_f32_16x16x64_bf16 v[28:31], v[36:39], a[88:95], v0\nv_mul_f32 v21, s32, v21\n" \
    "v_smfmac_f32_16x16x64_bf16 v[24:27], v[48:51], a[104:111], v0\nv_cvt_pk_bf16_f32 v20, v20, v21\nv_smfmac_f32_16x16x64_bf16 v[28:31], v[48:51], a[112:119], v0\n" \
    "ds_write_b16 v4, v20 offset:8\nv_permlane32_swap_b32 v16, v18\nv_smfmac_f32_16x16x64_bf16 v[24:27], v[52:55], a[152:159], v0\nds_write_b16_d16_hi v4, v20 offset:24\n" \
    "v_permlane32_swap_b32 v17, v19\nv_smfmac_f32_16x16x64_bf16 v[24:27], v[80:83], a[176:183], v0\nds_read_b128 v[48:51], v1 offset:40960\nv_add_f32 v16, v16, v18\n" \
    "v_smfmac_f32_16x16x64_bf16 v[24:27], v[64:67], a[200:207], v0\nds_read_b128 v[32:35], v1 offset:45056\nv_add_f32 v17, v17, v19\n" \
    "v_smfmac_f32_16x16x64_bf16 v[24:27], v[68:71], a[248:255], v0\nds_read_b128 v[36:39], v1 offset:41984\nv_mul_f32 v16, s32, v16\n" \
    "v_smfmac_f32_16x16x64_bf16 v[24:27], v[56:59], v[108:115], v0\nds_read_b128 v[72:75], v1 offset:46080\nv_mul_f32 v17, s32, v17\n" \
    "v_smfmac_f32_16x16x64_bf16 v[24:27], v[60:63], v[140:147], v0\nds_read_b128 v[76:79], v1 offset:43008\nv_cvt_pk_bf16_f32 v16, v16, v17\n" \
    "v_smfmac_f32_16x16x64_bf16 v[24:27], v[40:43], v[204:211], v0\nds_write_b16 v4, v16 offset:0\nv_smfmac_f32_16x16x64_bf16 v[24:27], v[44:47], v[236:243], v0\n" \
    "ds_write_b16_d16_hi v4, v16 offset:16\ns_nop 1\nv_smfmac_f32_16x16x64_bf16 v[28:31], v[52:55], a[160:167], v0\nds_read_b128 v[52:55], v1 offset:47104\ns_nop 1\n" \
    "v_smfmac_f32_16x16x64_bf16 v[28:31], v[80:83], a[184:191], v0\nds_read_b128 v[80:83], v1 offset:44032\nv_permlane32_swap_b32 v24, v26\n" \
    "v_smfmac_f32_16x16x64_bf16 v[28:31], v[64:67], a[208:215], v0\nds_read_b128 v[64:67], v1 offset:48128\nv_permlane32_swap_b32 v25, v27\n" \
    "v_smfmac_f32_16x16x64_bf16 v[28:31], v[68:71], v[92:99], v0\ns_waitcnt lgkmcnt(3)\ns_barrier\nds_read_b128 v[68:71], v6 offset:0\nv_add_f32 v24, v24, v26\n" \
    "v_smfmac_f32_16x16x64_bf16 v[28:31], v[56:59], v[116:123], v0\nds_read_b128 v[56:59], v6 offset:512\nv_add_f32 v25, v25, v27\n" \
    "v_smfmac_f32_16x16x64_bf16 v[28:31], v[60:63], v[148:155], v0\nds_read_b128 v[60:63], v6 offset:1024\nv_cvt_pk_bf16_f32 v24, v24, v25\n" \
    "v_smfmac_f32_16x16x64_bf16 v[28:31], v[40:43], v[212:219], v0\nds_read_b128 v[40:43], v6 offset:1536\nv_smfmac_f32_16x16x64_bf16 v[28:31], v[44:47], v[244:251], v0\n" \
    "ds_write_b16 v4, v24 offset:2048\nds_write_b16_d16_hi v4, v24 offset:2064\ns_waitcnt lgkmcnt(5)\nv_mfma_f32_16x16x32_bf16 v[16:19], v[48:51], v[68:71], 0\ns_nop 2\n" \
    "v_mfma_f32_16x16x32_bf16 v[20:23], v[32:35], v[68:71], 0\nv_permlane32_swap_b32 v28, v30\nv_permlane32_swap_b32 v29, v31\nv_add_f32 v28, v28, v30\nv_add_f32 v29, v29, v31\n" \
    "v_cvt_pk_bf16_f32 v28, v28, v29\nds_write_b16 v4, v28 offset:2056\nds_write_b16_d16_hi v4, v28 offset:2072\ns_waitcnt lgkmcnt(6)\n" \
    "v_mfma_f32_16x16x32_bf16 v[16:19], v[36:39], v[56:59], v[16:19]\nv_mfma_f32_16x16x32_bf16 v[20:23], v[72:75], v[56:59], v[20:23]\ns_waitcnt lgkmcnt(5)\n" \
    "v_mfma_f32_16x16x32_bf16 v[16:19], v[76:79], v[60:63], v[16:19]\nv_mfma_f32_16x16x32_bf16 v[20:23], v[52:55], v[60:63], v[20:23]\ns_waitcnt lgkmcnt(0)\ns_barrier\n" \
    "ds_read_b128 v[44:47], v6 offset:2048\nds_read_b128 v[84:87], v6 offset:2560\nv_mfma_f32_16x16x32_bf16 v[16:19], v[80:83], v[40:43], v[16:19]\n" \
    "ds_read_b128 v[88:91], v6 offset:3072\nds_read_b128 v[28:31], v6 offset:3584\nv_mfma_f32_16x16x32_bf16 v[20:23], v[64:67], v[40:43], v[20:23]\nv_mov_b32 v15, v15\n" \
    "v_mov_b32 v15, v15\nv_mov_b32 v15, v15\nv_mov_b32 v15, v15\nv_mov_b32 v15, v15\nv_mov_b32 v15, v15\ns_waitcnt lgkmcnt(3)\n" \
    "v_mfma_f32_16x16x32_bf16 v[24:27], v[44:47], v[68:71], 0\ns_waitcnt lgkmcnt(2)\nv_mov_b32_dpp v16, v20 quad_perm:[0,1,2,3] row_mask:0xf bank_mask:0xc\n" \
    "v_mov_b32_dpp v17, v21 quad_perm:[0,1,2,3] row_mask:0xf bank_mask:0xc\nv_mfma_f32_16x16x32_bf16 v[24:27], v[84:87], v[56:59], v[24:27]\ns_waitcnt lgkmcnt(1)\n" \
    "v_mov_b32_dpp v18, v22 quad_perm:[0,1,2,3] row_mask:0xf bank_mask:0xc\nv_mov_b32_dpp v19, v23 quad_perm:[0,1,2,3] row_mask:0xf bank_mask:0xc\n" \
    "v_mfma_f32_16x16x32_bf16 v[24:27], v[88:91], v[60:63], v[24:27]\ns_waitcnt lgkmcnt(0)\nv_max3_f32 v254, v16, v17, v18\nv_max_f32 v254, v254, v19\n" \
    "v_mfma_f32_16x16x32_bf16 v[24:27], v[28:31], v[40:43], v[24:27]\ns_nop 0\nv_max_f32_dpp v252, v254, v254 row_ror:8 row_mask:0xf bank_mask:0xf\n" \
    "v_max_f32_dpp v253, v254, v254 row_ror:8 row_mask:0xf bank_mask:0xf\ns_nop 1\nv_permlane32_swap_b32 v252, v253\nv_max_f32 v252, v252, v253\ns_xor_b32 s74, s82, 0x4000\n" \
    "v_max3_f32 v254, v252, v24, v25\nv_max3_f32 v252, v254, v26, v27\nv_max3_f32 v253, v254, v26, v27\ns_nop 1\nv_permlane16_swap_b32 v252, v253\nv_max_f32 v252, v252, v253\n" \
    "v_pk_add_f32 v[16:17], v[16:17], v[252:253] op_sel_hi:[1,0] neg_lo:[0,1] neg_hi:[0,1]\n" \
    "v_pk_add_f32 v[18:19], v[18:19], v[252:253] op_sel_hi:[1,0] neg_lo:[0,1] neg_hi:[0,1]\n" \
    "v_pk_add_f32 v[24:25], v[24:25], v[252:253] op_sel_hi:[1,0] neg_lo:[0,1] neg_hi:[0,1]\n" \
    "v_pk_add_f32 v[26:27], v[26:27], v[252:253] op_sel_hi:[1,0] neg_lo:[0,1] neg_hi:[0,1]\nv_exp_f32 v16, v16\nv_exp_f32 v17, v17\nv_exp_f32 v18, v18\nv_exp_f32 v19, v19\n" \
    "v_exp_f32 v24, v24\nv_pk_add_f32 v[254:255], v[16:17], v[18:19]\nv_exp_f32 v25, v25\nv_add_f32 v254, v254, v255\nv_exp_f32 v26, v26\nv_exp_f32 v27, v27\n" \
    "v_add_f32_dpp v252, v254, v254 row_ror:8 row_mask:0xf bank_mask:0xf\nv_add_f32_dpp v253, v254, v254 row_ror:8 row_mask:0xf bank_mask:0xf\n" \
    "v_pk_add_f32 v[254:255], v[24:25], v[26:27]\ns_nop 0\nv_permlane32_swap_b32 v252, v253\nv_pk_add_f32 v[254:255], v[254:255], v[252:253]\n" \
    "v_pk_add_f32 v[252:253], v[254:255], v[254:255] op_sel:[0,1] op_sel_hi:[1,0]\ns_nop 1\nv_permlane16_swap_b32 v252, v253\nv_add_f32 v252, v252, v253\nv_rcp_f32 v252, v252\n" \
    "s_nop 0\nv_pk_mul_f32 v[24:25], v[24:25], v[252:253] op_sel_hi:[1,0]\nv_pk_mul_f32 v[26:27], v[26:27], v[252:253] op_sel_hi:[1,0]\nv_cvt_pk_bf16_f32 v20, v24, v25\n" \
    "v_cvt_pk_bf16_f32 v21, v26, v27\nv_pk_mul_f32 v[16:17], v[16:17], v[252:253] op_sel_hi:[1,0]\nv_pk_mul_f32 v[18:19], v[18:19], v[252:253] op_sel_hi:[1,0]\n" \
    "v_permlane16_swap_b32 v20, v22\nv_cvt_pk_bf16_f32 v16, v16, v17\nv_cvt_pk_bf16_f32 v17, v18, v19\nv_permlane16_swap_b32 v21, v23\n" \
    "v_mov_b32_dpp v20, v16 quad_perm:[0,1,2,3] row_mask:0xf bank_mask:0x3\nv_mov_b32_dpp v22, v16 row_ror:8 row_mask:0xf bank_mask:0x3\n" \
    "v_mov_b32_dpp v21, v17 quad_perm:[0,1,2,3] row_mask:0xf bank_mask:0x3\nv_mov_b32_dpp v23, v17 row_ror:8 row_mask:0xf bank_mask:0x3\n" \
    "buffer_store_dwordx4 v[20:23], v7, s[64:67], s82 offen sc1\nbuffer_store_dwordx4 v[20:23], v7, s[64:67], s74 offen sc1\ns_branch .Lq3_%=\n.Lq6_%=:\n" \
    "ds_read_b128 v[32:35], v3 offset:4096\nds_read_b128 v[36:39], v3 offset:5120\nds_read_b128 v[40:43], v3 offset:12288\nds_read_b128 v[44:47], v3 offset:13312\n" \
    "ds_read_b128 v[48:51], v3 offset:20480\nds_read_b128 v[52:55], v3 offset:21504\nds_read_b128 v[56:59], v3 offset:0\nds_read_b128 v[60:63], v3 offset:1024\n" \
    "ds_read_b128 v[64:67], v3 offset:2048\nds_read_b128 v[68:71], v3 offset:3072\n.Lq27_%=:\ns_xor_b32 s90, s81, 0x800\n.Lq29_%=:\ns_memrealtime s[72:73]\n" \
    "s_waitcnt lgkmcnt(0)\ns_sub_u32 s72, s72, s80\ns_cmp_lt_i32 s72, 0\ns_cbranch_scc1 .Lq29_%=\nbuffer_load_dwordx4 v[72:75], v2, s[64:67], s81 offen offset:0 sc1\n" \
    "buffer_load_dwordx4 v[76:79], v2, s[64:67], s81 offen offset:1024 sc1\nbuffer_load_dwordx4 v[80:83], v2, s[64:67], s90 offen offset:0 sc1\n" \
    "buffer_load_dwordx4 v[84:87], v2, s[64:67], s90 offen offset:1024 sc1\nv_mov_b64 v[16:17], 0\nv_mov_b64 v[18:19], 0\nv_mov_b64 v[20:21], 0\nv_mov_b64 v[22:23], 0\n" \
    "v_mov_b64 v[24:25], 0\nv_mov_b64 v[26:27], 0\nv_mov_b64 v[28:29], 0\nv_mov_b64 v[30:31], 0\ns_waitcnt vmcnt(3)\nds_write_b128 v1, v[72:75] offset:26624\n" \
    "v_smfmac_f32_16x16x64_bf16 v[20:23], v[72:75], v[32:39], v0\nv_max3_u32 v252, v72, v73, v74\nv_smfmac_f32_16x16x64_bf16 v[16:19], v[72:75], a[48:55], v0\n" \
    "s_waitcnt vmcnt(2)\nds_write_b128 v1, v[76:79] offset:30720\nv_max3_u32 v252, v252, v75, v76\nv_smfmac_f32_16x16x64_bf16 v[24:27], v[72:75], a[56:63], v0\n" \
    "v_max3_u32 v253, v77, v78, v79\nv_smfmac_f32_16x16x64_bf16 v[28:31], v[72:75], a[64:71], v0\ns_waitcnt vmcnt(1)\nds_write_b128 v1, v[80:83] offset:34816\n" \
    "v_max3_u32 v252, v252, v80, v81\nv_smfmac_f32_16x16x64_bf16 v[20:23], v[76:79], v[40:47], v0\nv_max3_u32 v253, v253, v82, v83\n" \
    "v_smfmac_f32_16x16x64_bf16 v[16:19], v[76:79], a[144:151], v0\ns_waitcnt vmcnt(0)\nds_write_b128 v1, v[84:87] offset:38912\nv_max3_u32 v252, v252, v84, v85\n" \
    "v_smfmac_f32_16x16x64_bf16 v[24:27], v[76:79], a[152:159], v0\nv_max3_u32 v253, v253, v86, v87\nv_smfmac_f32_16x16x64_bf16 v[28:31], v[76:79], a[160:167], v0\n" \
    "v_max_u32 v252, v252, v253\nv_smfmac_f32_16x16x64_bf16 v[20:23], v[80:83], v[48:55], v0\nv_cmp_eq_u32 vcc, -1, v252\n" \
    "v_smfmac_f32_16x16x64_bf16 v[16:19], v[80:83], a[240:247], v0\ns_cbranch_vccnz .Lq28_%=\ns_waitcnt lgkmcnt(0)\ns_barrier\nds_read_b128 v[88:91], v1 offset:24576\n" \
    "ds_read_b128 v[32:35], v1 offset:25600\nds_read_b128 v[36:39], v1 offset:27648\nv_smfmac_f32_16x16x64_bf16 v[24:27], v[80:83], a[248:255], v0\n" \
    "ds_read_b128 v[40:43], v3 offset:6144\nv_smfmac_f32_16x16x64_bf16 v[28:31], v[80:83], v[92:99], v0\nds_read_b128 v[44:47], v3 offset:7168\n" \
    "v_smfmac_f32_16x16x64_bf16 v[20:23], v[84:87], v[196:203], v0\nds_read_b128 v[48:51], v1 offset:28672\nv_smfmac_f32_16x16x64_bf16 v[16:19], v[84:87], v[188:195], v0\n" \
    "ds_read_b128 v[72:75], v3 offset:8192\nv_smfmac_f32_16x16x64_bf16 v[24:27], v[84:87], v[204:211], v0\nds_read_b128 v[76:79], v3 offset:9216\n" \
    "v_smfmac_f32_16x16x64_bf16 v[28:31], v[84:87], v[212:219], v0\nds_read_b128 v[52:55], v1 offset:29696\ns_waitcnt lgkmcnt(8)\n" \
    "v_smfmac_f32_16x16x64_bf16 v[20:23], v[88:91], v[56:63], v0\nds_read_b128 v[56:59], v3 offset:10240\nv_smfmac_f32_16x16x64_bf16 v[16:19], v[88:91], a[0:7], v0\n" \
    "ds_read_b128 v[60:63], v3 offset:11264\nv_smfmac_f32_16x16x64_bf16 v[24:27], v[88:91], a[8:15], v0\nds_read_b128 v[80:83], v1 offset:31744\n" \
    "v_smfmac_f32_16x16x64_bf16 v[28:31], v[88:91], a[16:23], v0\nds_read_b128 v[84:87], v3 offset:14336\ns_waitcnt lgkmcnt(11)\n" \
    "v_smfmac_f32_16x16x64_bf16 v[20:23], v[32:35], v[64:71], v0\nds_read_b128 v[88:91], v3 offset:15360\nv_smfmac_f32_16x16x64_bf16 v[16:19], v[32:35], a[24:31], v0\n" \
    "ds_read_b128 v[64:67], v1 offset:32768\ns_waitcnt lgkmcnt(10)\nv_smfmac_f32_16x16x64_bf16 v[20:23], v[36:39], v[40:47], v0\nds_read_b128 v[40:43], v3 offset:16384\n" \
    "v_smfmac_f32_16x16x64_bf16 v[16:19], v[36:39], a[72:79], v0\nds_read_b128 v[44:47], v3 offset:17408\ns_waitcnt lgkmcnt(9)\n" \
    "v_smfmac_f32_16x16x64_bf16 v[20:23], v[48:51], v[72:79], v0\nds_read_b128 v[68:71], v1 offset:33792\nv_smfmac_f32_16x16x64_bf16 v[16:19], v[48:51], a[96:103], v0\n" \
    "ds_read_b128 v[72:75], v3 offset:18432\ns_waitcnt lgkmcnt(8)\nv_smfmac_f32_16x16x64_bf16 v[20:23], v[52:55], v[56:63], v0\nds_read_b128 v[76:79], v3 offset:19456\n" \
    "v_smfmac_f32_16x16x64_bf16 v[16:19], v[52:55], a[120:127], v0\nds_read_b128 v[56:59], v1 offset:35840\ns_waitcnt lgkmcnt(7)\n" \
    "v_smfmac_f32_16x16x64_bf16 v[20:23], v[80:83], v[84:91], v0\nds_read_b128 v[84:87], v3 offset:22528\nv_smfmac_f32_16x16x64_bf16 v[16:19], v[80:83], a[168:175], v0\n" \
    "ds_read_b128 v[88:91], v3 offset:23552\ns_waitcnt lgkmcnt(6)\nv_smfmac_f32_16x16x64_bf16 v[20:23], v[64:67], v[40:47], v0\n" \
    "v_smfmac_f32_16x16x64_bf16 v[16:19], v[64:67], a[192:199], v0\nds_read_b128 v[60:63], v1 offset:36864\ns_waitcnt lgkmcnt(4)\n" \
    "v_smfmac_f32_16x16x64_bf16 v[20:23], v[68:71], v[72:79], v0\nds_read_b128 v[40:43], v1 offset:37888\nv_smfmac_f32_16x16x64_bf16 v[16:19], v[68:71], a[216:223], v0\n" \
    "ds_read_b128 v[44:47], v1 offset:39936\ns_waitcnt lgkmcnt(3)\nv_smfmac_f32_16x16x64_bf16 v[20:23], v[56:59], v[84:91], v0\n" \
    "v_smfmac_f32_16x16x64_bf16 v[16:19], v[56:59], v[100:107], v0\ns_waitcnt lgkmcnt(2)\nv_smfmac_f32_16x16x64_bf16 v[20:23], v[60:63], v[132:139], v0\ns_waitcnt lgkmcnt(1)\n" \
    "v_smfmac_f32_16x16x64_bf16 v[20:23], v[40:43], v[164:171], v0\ns_waitcnt lgkmcnt(0)\nv_smfmac_f32_16x16x64_bf16 v[20:23], v[44:47], v[228:235], v0\ns_nop 2\n" \
    "v_smfmac_f32_16x16x64_bf16 v[16:19], v[60:63], v[124:131], v0\ns_nop 2\nv_smfmac_f32_16x16x64_bf16 v[16:19], v[40:43], v[156:163], v0\nv_permlane32_swap_b32 v20, v22\n" \
    "v_smfmac_f32_16x16x64_bf16 v[16:19], v[44:47], v[220:227], v0\ns_nop 1\nv_permlane32_swap_b32 v21, v23\nv_smfmac_f32_16x16x64_bf16 v[24:27], v[32:35], a[32:39], v0\n" \
    "s_nop 1\nv_add_f32 v20, v20, v22\nv_smfmac_f32_16x16x64_bf16 v[28:31], v[32:35], a[40:47], v0\nv_add_f32 v21, v21, v23\n" \
    "v_smfmac_f32_16x16x64_bf16 v[24:27], v[36:39], a[80:87], v0\nv_mul_f32 v20, s32, v20\nv_smfmac_f32_16x16x64_bf16 v[28:31], v[36:39], a[88:95], v0\nv_mul_f32 v21, s32, v21\n" \
    "v_smfmac_f32_16x16x64_bf16 v[24:27], v[48:51], a[104:111], v0\nv_cvt_pk_bf16_f32 v20, v20, v21\nv_smfmac_f32_16x16x64_bf16 v[28:31], v[48:51], a[112:119], v0\n" \
    "ds_write_b16 v4, v20 offset:8\nv_permlane32_swap_b32 v16, v18\nv_smfmac_f32_16x16x64_bf16 v[24:27], v[52:55], a[128:135], v0\nds_write_b16_d16_hi v4, v20 offset:24\n" \
    "v_permlane32_swap_b32 v17, v19\nv_smfmac_f32_16x16x64_bf16 v[24:27], v[80:83], a[176:183], v0\nds_read_b128 v[48:51], v1 offset:40960\nv_add_f32 v16, v16, v18\n" \
    "v_smfmac_f32_16x16x64_bf16 v[24:27], v[64:67], a[200:207], v0\nds_read_b128 v[32:35], v1 offset:45056\nv_add_f32 v17, v17, v19\n" \
    "v_smfmac_f32_16x16x64_bf16 v[24:27], v[68:71], a[224:231], v0\nds_read_b128 v[36:39], v1 offset:41984\nv_mul_f32 v16, s32, v16\n" \
    "v_smfmac_f32_16x16x64_bf16 v[24:27], v[56:59], v[108:115], v0\nds_read_b128 v[72:75], v1 offset:46080\nv_mul_f32 v17, s32, v17\n" \
    "v_smfmac_f32_16x16x64_bf16 v[24:27], v[60:63], v[140:147], v0\nds_read_b128 v[76:79], v1 offset:43008\nv_cvt_pk_bf16_f32 v16, v16, v17\n" \
    "v_smfmac_f32_16x16x64_bf16 v[24:27], v[40:43], v[172:179], v0\nds_write_b16 v4, v16 offset:0\nv_smfmac_f32_16x16x64_bf16 v[24:27], v[44:47], v[236:243], v0\n" \
    "ds_write_b16_d16_hi v4, v16 offset:16\ns_nop 1\nv_smfmac_f32_16x16x64_bf16 v[28:31], v[52:55], a[136:143], v0\nds_read_b128 v[52:55], v1 offset:47104\ns_nop 1\n" \
    "v_smfmac_f32_16x16x64_bf16 v[28:31], v[80:83], a[184:191], v0\nds_read_b128 v[80:83], v1 offset:44032\nv_permlane32_swap_b32 v24, v26\n" \
    "v_smfmac_f32_16x16x64_bf16 v[28:31], v[64:67], a[208:215], v0\nds_read_b128 v[64:67], v1 offset:48128\nv_permlane32_swap_b32 v25, v27\n" \
    "v_smfmac_f32_16x16x64_bf16 v[28:31], v[68:71], a[232:239], v0\ns_waitcnt lgkmcnt(3)\ns_barrier\nds_read_b128 v[68:71], v6 offset:0\nv_add_f32 v24, v24, v26\n" \
    "v_smfmac_f32_16x16x64_bf16 v[28:31], v[56:59], v[116:123], v0\nds_read_b128 v[56:59], v6 offset:512\nv_add_f32 v25, v25, v27\n" \
    "v_smfmac_f32_16x16x64_bf16 v[28:31], v[60:63], v[148:155], v0\nds_read_b128 v[60:63], v6 offset:1024\nv_cvt_pk_bf16_f32 v24, v24, v25\n" \
    "v_smfmac_f32_16x16x64_bf16 v[28:31], v[40:43], v[180:187], v0\nds_read_b128 v[40:43], v6 offset:1536\nv_smfmac_f32_16x16x64_bf16 v[28:31], v[44:47], v[244:251], v0\n" \
    "ds_write_b16 v4, v24 offset:2048\nds_write_b16_d16_hi v4, v24 offset:2064\ns_waitcnt lgkmcnt(5)\nv_mfma_f32_16x16x32_bf16 v[16:19], v[48:51], v[68:71], 0\ns_nop 2\n" \
    "v_mfma_f32_16x16x32_bf16 v[20:23], v[32:35], v[68:71], 0\nv_permlane32_swap_b32 v28, v30\nv_permlane32_swap_b32 v29, v31\nv_add_f32 v28, v28, v30\nv_add_f32 v29, v29, v31\n" \
    "v_cvt_pk_bf16_f32 v28, v28, v29\nds_write_b16 v4, v28 offset:2056\nds_write_b16_d16_hi v4, v28 offset:2072\ns_waitcnt lgkmcnt(6)\n" \
    "v_mfma_f32_16x16x32_bf16 v[16:19], v[36:39], v[56:59], v[16:19]\nv_mfma_f32_16x16x32_bf16 v[20:23], v[72:75], v[56:59], v[20:23]\ns_waitcnt lgkmcnt(5)\n" \
    "v_mfma_f32_16x16x32_bf16 v[16:19], v[76:79], v[60:63], v[16:19]\nv_mfma_f32_16x16x32_bf16 v[20:23], v[52:55], v[60:63], v[20:23]\ns_waitcnt lgkmcnt(0)\ns_barrier\n" \
    "ds_read_b128 v[44:47], v6 offset:2048\nds_read_b128 v[84:87], v6 offset:2560\nv_mfma_f32_16x16x32_bf16 v[16:19], v[80:83], v[40:43], v[16:19]\n" \
    "ds_read_b128 v[88:91], v6 offset:3072\nds_read_b128 v[28:31], v6 offset:3584\nv_mfma_f32_16x16x32_bf16 v[20:23], v[64:67], v[40:43], v[20:23]\nv_mov_b32 v15, v15\n" \
    "v_mov_b32 v15, v15\nv_mov_b32 v15, v15\nv_mov_b32 v15, v15\nv_mov_b32 v15, v15\nv_mov_b32 v15, v15\ns_waitcnt lgkmcnt(3)\n" \
    "v_mfma_f32_16x16x32_bf16 v[24:27], v[44:47], v[68:71], 0\ns_waitcnt lgkmcnt(2)\nv_mov_b32_dpp v16, v20 quad_perm:[0,1,2,3] row_mask:0xf bank_mask:0xc\n" \
    "v_mov_b32_dpp v17, v21 quad_perm:[0,1,2,3] row_mask:0xf bank_mask:0xc\nv_mfma_f32_16x16x32_bf16 v[24:27], v[84:87], v[56:59], v[24:27]\ns_waitcnt lgkmcnt(1)\n" \
    "v_mov_b32_dpp v18, v22 quad_perm:[0,1,2,3] row_mask:0xf bank_mask:0xc\nv_mov_b32_dpp v19, v23 quad_perm:[0,1,2,3] row_mask:0xf bank_mask:0xc\n" \
    "v_mfma_f32_16x16x32_bf16 v[24:27], v[88:91], v[60:63], v[24:27]\ns_waitcnt lgkmcnt(0)\nv_max3_f32 v254, v16, v17, v18\nv_max_f32 v254, v254, v19\n" \
    "v_mfma_f32_16x16x32_bf16 v[24:27], v[28:31], v[40:43], v[24:27]\ns_nop 0\nv_max_f32_dpp v252, v254, v254 row_ror:8 row_mask:0xf bank_mask:0xf\n" \
    "v_max_f32_dpp v253, v254, v254 row_ror:8 row_mask:0xf bank_mask:0xf\ns_nop 1\nv_permlane32_swap_b32 v252, v253\nv_max_f32 v252, v252, v253\ns_xor_b32 s74, s82, 0x4000\n" \
    "v_max3_f32 v254, v252, v24, v25\nv_max3_f32 v252, v254, v26, v27\nv_max3_f32 v253, v254, v26, v27\ns_nop 1\nv_permlane16_swap_b32 v252, v253\nv_max_f32 v252, v252, v253\n" \
    "v_pk_add_f32 v[16:17], v[16:17], v[252:253] op_sel_hi:[1,0] neg_lo:[0,1] neg_hi:[0,1]\n" \
    "v_pk_add_f32 v[18:19], v[18:19], v[252:253] op_sel_hi:[1,0] neg_lo:[0,1] neg_hi:[0,1]\n" \
    "v_pk_add_f32 v[24:25], v[24:25], v[252:253] op_sel_hi:[1,0] neg_lo:[0,1] neg_hi:[0,1]\n" \
    "v_pk_add_f32 v[26:27], v[26:27], v[252:253] op_sel_hi:[1,0] neg_lo:[0,1] neg_hi:[0,1]\nv_exp_f32 v16, v16\nv_exp_f32 v17, v17\nv_exp_f32 v18, v18\nv_exp_f32 v19, v19\n" \
    "v_exp_f32 v24, v24\nv_pk_add_f32 v[254:255], v[16:17], v[18:19]\nv_exp_f32 v25, v25\nv_add_f32 v254, v254, v255\nv_exp_f32 v26, v26\nv_exp_f32 v27, v27\n" \
    "v_add_f32_dpp v252, v254, v254 row_ror:8 row_mask:0xf bank_mask:0xf\nv_add_f32_dpp v253, v254, v254 row_ror:8 row_mask:0xf bank_mask:0xf\n" \
    "v_pk_add_f32 v[254:255], v[24:25], v[26:27]\ns_nop 0\nv_permlane32_swap_b32 v252, v253\nv_pk_add_f32 v[254:255], v[254:255], v[252:253]\n" \
    "v_pk_add_f32 v[252:253], v[254:255], v[254:255] op_sel:[0,1] op_sel_hi:[1,0]\ns_nop 1\nv_permlane16_swap_b32 v252, v253\nv_add_f32 v252, v252, v253\nv_rcp_f32 v252, v252\n" \
    "s_nop 0\nv_pk_mul_f32 v[24:25], v[24:25], v[252:253] op_sel_hi:[1,0]\nv_pk_mul_f32 v[26:27], v[26:27], v[252:253] op_sel_hi:[1,0]\nv_cvt_pk_bf16_f32 v20, v24, v25\n" \
    "v_cvt_pk_bf16_f32 v21, v26, v27\nv_pk_mul_f32 v[16:17], v[16:17], v[252:253] op_sel_hi:[1,0]\nv_pk_mul_f32 v[18:19], v[18:19], v[252:253] op_sel_hi:[1,0]\n" \
    "v_permlane16_swap_b32 v20, v22\nv_cvt_pk_bf16_f32 v16, v16, v17\nv_cvt_pk_bf16_f32 v17, v18, v19\nv_permlane16_swap_b32 v21, v23\n" \
    "v_mov_b32_dpp v20, v16 quad_perm:[0,1,2,3] row_mask:0xf bank_mask:0x3\nv_mov_b32_dpp v22, v16 row_ror:8 row_mask:0xf bank_mask:0x3\n" \
    "v_mov_b32_dpp v21, v17 quad_perm:[0,1,2,3] row_mask:0xf bank_mask:0x3\nv_mov_b32_dpp v23, v17 row_ror:8 row_mask:0xf bank_mask:0x3\n" \
    "buffer_store_dwordx4 v[20:23], v7, s[64:67], s82 offen sc1\nbuffer_store_dwordx4 v[20:23], v7, s[64:67], s74 offen sc1\ns_branch .Lq3_%=\n.Lq7_%=:\n" \
    "ds_read_b128 v[32:35], v3 offset:6144\nds_read_b128 v[36:39], v3 offset:7168\nds_read_b128 v[40:43], v3 offset:14336\nds_read_b128 v[44:47], v3 offset:15360\n" \
    "ds_read_b128 v[48:51], v3 offset:22528\nds_read_b128 v[52:55], v3 offset:23552\nds_read_b128 v[56:59], v3 offset:0\nds_read_b128 v[60:63], v3 offset:1024\n" \
    "ds_read_b128 v[64:67], v3 offset:2048\nds_read_b128 v[68:71], v3 offset:3072\n.Lq31_%=:\ns_xor_b32 s90, s81, 0x800\n.Lq33_%=:\ns_memrealtime s[72:73]\n" \
    "s_waitcnt lgkmcnt(0)\ns_sub_u32 s72, s72, s80\ns_cmp_lt_i32 s72, 0\ns_cbranch_scc1 .Lq33_%=\nbuffer_load_dwordx4 v[72:75], v2, s[64:67], s81 offen offset:0 sc1\n" \
    "buffer_load_dwordx4 v[76:79], v2, s[64:67], s81 offen offset:1024 sc1\nbuffer_load_dwordx4 v[80:83], v2, s[64:67], s90 offen offset:0 sc1\n" \
    "buffer_load_dwordx4 v[84:87], v2, s[64:67], s90 offen offset:1024 sc1\nv_mov_b64 v[16:17], 0\nv_mov_b64 v[18:19], 0\nv_mov_b64 v[20:21], 0\nv_mov_b64 v[22:23], 0\n" \
    "v_mov_b64 v[24:25], 0\nv_mov_b64 v[26:27], 0\nv_mov_b64 v[28:29], 0\nv_mov_b64 v[30:31], 0\ns_waitcnt vmcnt(3)\nds_write_b128 v1, v[72:75] offset:27648\n" \
    "v_smfmac_f32_16x16x64_bf16 v[20:23], v[72:75], v[32:39], v0\nv_max3_u32 v252, v72, v73, v74\nv_smfmac_f32_16x16x64_bf16 v[16:19], v[72:75], a[72:79], v0\n" \
    "s_waitcnt vmcnt(2)\nds_write_b128 v1, v[76:79] offset:31744\nv_max3_u32 v252, v252, v75, v76\nv_smfmac_f32_16x16x64_bf16 v[24:27], v[72:75], a[80:87], v0\n" \
    "v_max3_u32 v253, v77, v78, v79\nv_smfmac_f32_16x16x64_bf16 v[28:31], v[72:75], a[88:95], v0\ns_waitcnt vmcnt(1)\nds_write_b128 v1, v[80:83] offset:35840\n" \
    "v_max3_u32 v252, v252, v80, v81\nv_smfmac_f32_16x16x64_bf16 v[20:23], v[76:79], v[40:47], v0\nv_max3_u32 v253, v253, v82, v83\n" \
    "v_smfmac_f32_16x16x64_bf16 v[16:19], v[76:79], a[168:175], v0\ns_waitcnt vmcnt(0)\nds_write_b128 v1, v[84:87] offset:39936\nv_max3_u32 v252, v252, v84, v85\n" \
    "v_smfmac_f32_16x16x64_bf16 v[24:27], v[76:79], a[176:183], v0\nv_max3_u32 v253, v253, v86, v87\nv_smfmac_f32_16x16x64_bf16 v[28:31], v[76:79], a[184:191], v0\n" \
    "v_max_u32 v252, v252, v253\nv_smfmac_f32_16x16x64_bf16 v[20:23], v[80:83], v[48:55], v0\nv_cmp_eq_u32 vcc, -1, v252\n" \
    "v_smfmac_f32_16x16x64_bf16 v[16:19], v[80:83], v[100:107], v0\ns_cbranch_vccnz .Lq32_%=\ns_waitcnt lgkmcnt(0)\ns_barrier\nds_read_b128 v[88:91], v1 offset:24576\n" \
    "ds_read_b128 v[32:35], v1 offset:25600\nds_read_b128 v[36:39], v1 offset:26624\nv_smfmac_f32_16x16x64_bf16 v[24:27], v[80:83], v[108:115], v0\n" \
    "ds_read_b128 v[40:43], v3 offset:4096\nv_smfmac_f32_16x16x64_bf16 v[28:31], v[80:83], v[116:123], v0\nds_read_b128 v[44:47], v3 offset:5120\n" \
    "v_smfmac_f32_16x16x64_bf16 v[20:23], v[84:87], v[228:235], v0\nds_read_b128 v[48:51], v1 offset:28672\nv_smfmac_f32_16x16x64_bf16 v[16:19], v[84:87], v[220:227], v0\n" \
    "ds_read_b128 v[72:75], v3 offset:8192\nv_smfmac_f32_16x16x64_bf16 v[24:27], v[84:87], v[236:243], v0\nds_read_b128 v[76:79], v3 offset:9216\n" \
    "v_smfmac_f32_16x16x64_bf16 v[28:31], v[84:87], v[244:251], v0\nds_read_b128 v[52:55], v1 offset:29696\ns_waitcnt lgkmcnt(8)\n" \
    "v_smfmac_f32_16x16x64_bf16 v[20:23], v[88:91], v[56:63], v0\nds_read_b128 v[56:59], v3 offset:10240\nv_smfmac_f32_16x16x64_bf16 v[16:19], v[88:91], a[0:7], v0\n" \
    "ds_read_b128 v[60:63], v3 offset:11264\nv_smfmac_f32_16x16x64_bf16 v[24:27], v[88:91], a[8:15], v0\nds_read_b128 v[80:83], v1 offset:30720\n" \
    "v_smfmac_f32_16x16x64_bf16 v[28:31], v[88:91], a[16:23], v0\nds_read_b128 v[84:87], v3 offset:12288\ns_waitcnt lgkmcnt(11)\n" \
    "v_smfmac_f32_16x16x64_bf16 v[20:23], v[32:35], v[64:71], v0\nds_read_b128 v[88:91], v3 offset:13312\nv_smfmac_f32_16x16x64_bf16 v[16:19], v[32:35], a[24:31], v0\n" \
    "ds_read_b128 v[64:67], v1 offset:32768\ns_waitcnt lgkmcnt(10)\nv_smfmac_f32_16x16x64_bf16 v[20:23], v[36:39], v[40:47], v0\nds_read_b128 v[40:43], v3 offset:16384\n" \
    "v_smfmac_f32_16x16x64_bf16 v[16:19], v[36:39], a[48:55], v0\nds_read_b128 v[44:47], v3 offset:17408\ns_waitcnt lgkmcnt(9)\n" \
    "v_smfmac_f32_16x16x64_bf16 v[20:23], v[48:51], v[72:79], v0\nds_read_b128 v[68:71], v1 offset:33792\nv_smfmac_f32_16x16x64_bf16 v[16:19], v[48:51], a[96:103], v0\n" \
    "ds_read_b128 v[72:75], v3 offset:18432\ns_waitcnt lgkmcnt(8)\nv_smfmac_f32_16x16x64_bf16 v[20:23], v[52:55], v[56:63], v0\nds_read_b128 v[76:79], v3 offset:19456\n" \
    "v_smfmac_f32_16x16x64_bf16 v[16:19], v[52:55], a[120:127], v0\nds_read_b128 v[56:59], v1 offset:34816\ns_waitcnt lgkmcnt(7)\n" \
    "v_smfmac_f32_16x16x64_bf16 v[20:23], v[80:83], v[84:91], v0\nds_read_b128 v[84:87], v3 offset:20480\nv_smfmac_f32_16x16x64_bf16 v[16:19], v[80:83], a[144:151], v0\n" \
    "ds_read_b128 v[88:91], v3 offset:21504\ns_waitcnt lgkmcnt(6)\nv_smfmac_f32_16x16x64_bf16 v[20:23], v[64:67], v[40:47], v0\n" \
    "v_smfmac_f32_16x16x64_bf16 v[16:19], v[64:67], a[192:199], v0\nds_read_b128 v[60:63], v1 offset:36864\ns_waitcnt lgkmcnt(4)\n" \
    "v_smfmac_f32_16x16x64_bf16 v[20:23], v[68:71], v[72:79], v0\nds_read_b128 v[40:43], v1 offset:37888\nv_smfmac_f32_16x16x64_bf16 v[16:19], v[68:71], a[216:223], v0\n" \
    "ds_read_b128 v[44:47], v1 offset:38912\ns_waitcnt lgkmcnt(3)\nv_smfmac_f32_16x16x64_bf16 v[20:23], v[56:59], v[84:91], v0\n" \
    "v_smfmac_f32_16x16x64_bf16 v[16:19], v[56:59], a[240:247], v0\ns_waitcnt lgkmcnt(2)\nv_smfmac_f32_16x16x64_bf16 v[20:23], v[60:63], v[132:139], v0\ns_waitcnt lgkmcnt(1)\n" \
    "v_smfmac_f32_16x16x64_bf16 v[20:23], v[40:43], v[164:171], v0\ns_waitcnt lgkmcnt(0)\nv_smfmac_f32_16x16x64_bf16 v[20:23], v[44:47], v[196:203], v0\ns_nop 2\n" \
    "v_smfmac_f32_16x16x64_bf16 v[16:19], v[60:63], v[124:131], v0\ns_nop 2\nv_smfmac_f32_16x16x64_bf16 v[16:19], v[40:43], v[156:163], v0\nv_permlane32_swap_b32 v20, v22\n" \
    "v_smfmac_f32_16x16x64_bf16 v[16:19], v[44:47], v[188:195], v0\ns_nop 1\nv_permlane32_swap_b32 v21, v23\nv_smfmac_f32_16x16x64_bf16 v[24:27], v[32:35], a[32:39], v0\n" \
    "s_nop 1\nv_add_f32 v20, v20, v22\nv_smfmac_f32_16x16x64_bf16 v[28:31], v[32:35], a[40:47], v0\nv_add_f32 v21, v21, v23\n" \
    "v_smfmac_f32_16x16x64_bf16 v[24:27], v[36:39], a[56:63], v0\nv_mul_f32 v20, s32, v20\nv_smfmac_f32_16x16x64_bf16 v[28:31], v[36:39], a[64:71], v0\nv_mul_f32 v21, s32, v21\n" \
    "v_smfmac_f32_16x16x64_bf16 v[24:27], v[48:51], a[104:111], v0\nv_cvt_pk_bf16_f32 v20, v20, v21\nv_smfmac_f32_16x16x64_bf16 v[28:31], v[48:51], a[112:119], v0\n" \
    "ds_write_b16 v4, v20 offset:8\nv_permlane32_swap_b32 v16, v18\nv_smfmac_f32_16x16x64_bf16 v[24:27], v[52:55], a[128:135], v0\nds_write_b16_d16_hi v4, v20 offset:24\n" \
    "v_permlane32_swap_b32 v17, v19\nv_smfmac_f32_16x16x64_bf16 v[24:27], v[80:83], a[152:159], v0\nds_read_b128 v[48:51], v1 offset:40960\nv_add_f32 v16, v16, v18\n" \
    "v_smfmac_f32_16x16x64_bf16 v[24:27], v[64:67], a[200:207], v0\nds_read_b128 v[32:35], v1 offset:45056\nv_add_f32 v17, v17, v19\n" \
    "v_smfmac_f32_16x16x64_bf16 v[24:27], v[68:71], a[224:231], v0\nds_read_b128 v[36:39], v1 offset:41984\nv_mul_f32 v16, s32, v16\n" \
    "v_smfmac_f32_16x16x64_bf16 v[24:27], v[56:59], a[248:255], v0\nds_read_b128 v[72:75], v1 offset:46080\nv_mul_f32 v17, s32, v17\n" \
    "v_smfmac_f32_16x16x64_bf16 v[24:27], v[60:63], v[140:147], v0\nds_read_b128 v[76:79], v1 offset:43008\nv_cvt_pk_bf16_f32 v16, v16, v17\n" \
    "v_smfmac_f32_16x16x64_bf16 v[24:27], v[40:43], v[172:179], v0\nds_write_b16 v4, v16 offset:0\nv_smfmac_f32_16x16x64_bf16 v[24:27], v[44:47], v[204:211], v0\n" \
    "ds_write_b16_d16_hi v4, v16 offset:16\ns_nop 1\nv_smfmac_f32_16x16x64_bf16 v[28:31], v[52:55], a[136:143], v0\nds_read_b128 v[52:55], v1 offset:47104\ns_nop 1\n" \
    "v_smfmac_f32_16x16x64_bf16 v[28:31], v[80:83], a[160:167], v0\nds_read_b128 v[80:83], v1 offset:44032\nv_permlane32_swap_b32 v24, v26\n" \
    "v_smfmac_f32_16x16x64_bf16 v[28:31], v[64:67], a[208:215], v0\nds_read_b128 v[64:67], v1 offset:48128\nv_permlane32_swap_b32 v25, v27\n" \
    "v_smfmac_f32_16x16x64_bf16 v[28:31], v[68:71], a[232:239], v0\ns_waitcnt lgkmcnt(3)\ns_barrier\nds_read_b128 v[68:71], v6 offset:0\nv_add_f32 v24, v24, v26\n" \
    "v_smfmac_f32_16x16x64_bf16 v[28:31], v[56:59], v[92:99], v0\nds_read_b128 v[56:59], v6 offset:512\nv_add_f32 v25, v25, v27\n" \
    "v_smfmac_f32_16x16x64_bf16 v[28:31], v[60:63], v[148:155], v0\nds_read_b128 v[60:63], v6 offset:1024\nv_cvt_pk_bf16_f32 v24, v24, v25\n" \
    "v_smfmac_f32_16x16x64_bf16 v[28:31], v[40:43], v[180:187], v0\nds_read_b128 v[40:43], v6 offset:1536\nv_smfmac_f32_16x16x64_bf16 v[28:31], v[44:47], v[212:219], v0\n" \
    "ds_write_b16 v4, v24 offset:2048\nds_write_b16_d16_hi v4, v24 offset:2064\ns_waitcnt lgkmcnt(5)\nv_mfma_f32_16x16x32_bf16 v[16:19], v[48:51], v[68:71], 0\ns_nop 2\n" \
    "v_mfma_f32_16x16x32_bf16 v[20:23], v[32:35], v[68:71], 0\nv_permlane32_swap_b32 v28, v30\nv_permlane32_swap_b32 v29, v31\nv_add_f32 v28, v28, v30\nv_add_f32 v29, v29, v31\n" \
    "v_cvt_pk_bf16_f32 v28, v28, v29\nds_write_b16 v4, v28 offset:2056\nds_write_b16_d16_hi v4, v28 offset:2072\ns_waitcnt lgkmcnt(6)\n" \
    "v_mfma_f32_16x16x32_bf16 v[16:19], v[36:39], v[56:59], v[16:19]\nv_mfma_f32_16x16x32_bf16 v[20:23], v[72:75], v[56:59], v[20:23]\ns_waitcnt lgkmcnt(5)\n" \
    "v_mfma_f32_16x16x32_bf16 v[16:19], v[76:79], v[60:63], v[16:19]\nv_mfma_f32_16x16x32_bf16 v[20:23], v[52:55], v[60:63], v[20:23]\ns_waitcnt lgkmcnt(0)\ns_barrier\n" \
    "ds_read_b128 v[44:47], v6 offset:2048\nds_read_b128 v[84:87], v6 offset:2560\nv_mfma_f32_16x16x32_bf16 v[16:19], v[80:83], v[40:43], v[16:19]\n" \
    "ds_read_b128 v[88:91], v6 offset:3072\nds_read_b128 v[28:31], v6 offset:3584\nv_mfma_f32_16x16x32_bf16 v[20:23], v[64:67], v[40:43], v[20:23]\nv_mov_b32 v15, v15\n" \
    "v_mov_b32 v15, v15\nv_mov_b32 v15, v15\nv_mov_b32 v15, v15\nv_mov_b32 v15, v15\nv_mov_b32 v15, v15\ns_waitcnt lgkmcnt(3)\n" \
    "v_mfma_f32_16x16x32_bf16 v[24:27], v[44:47], v[68:71], 0\ns_waitcnt lgkmcnt(2)\nv_mov_b32_dpp v16, v20 quad_perm:[0,1,2,3] row_mask:0xf bank_mask:0xc\n" \
    "v_mov_b32_dpp v17, v21 quad_perm:[0,1,2,3] row_mask:0xf bank_mask:0xc\nv_mfma_f32_16x16x32_bf16 v[24:27], v[84:87], v[56:59], v[24:27]\ns_waitcnt lgkmcnt(1)\n" \
    "v_mov_b32_dpp v18, v22 quad_perm:[0,1,2,3] row_mask:0xf bank_mask:0xc\nv_mov_b32_dpp v19, v23 quad_perm:[0,1,2,3] row_mask:0xf bank_mask:0xc\n" \
    "v_mfma_f32_16x16x32_bf16 v[24:27], v[88:91], v[60:63], v[24:27]\ns_waitcnt lgkmcnt(0)\nv_max3_f32 v254, v16, v17, v18\nv_max_f32 v254, v254, v19\n" \
    "v_mfma_f32_16x16x32_bf16 v[24:27], v[28:31], v[40:43], v[24:27]\ns_nop 0\nv_max_f32_dpp v252, v254, v254 row_ror:8 row_mask:0xf bank_mask:0xf\n" \
    "v_max_f32_dpp v253, v254, v254 row_ror:8 row_mask:0xf bank_mask:0xf\ns_nop 1\nv_permlane32_swap_b32 v252, v253\nv_max_f32 v252, v252, v253\ns_xor_b32 s74, s82, 0x4000\n" \
    "v_max3_f32 v254, v252, v24, v25\nv_max3_f32 v252, v254, v26, v27\nv_max3_f32 v253, v254, v26, v27\ns_nop 1\nv_permlane16_swap_b32 v252, v253\nv_max_f32 v252, v252, v253\n" \
    "v_pk_add_f32 v[16:17], v[16:17], v[252:253] op_sel_hi:[1,0] neg_lo:[0,1] neg_hi:[0,1]\n" \
    "v_pk_add_f32 v[18:19], v[18:19], v[252:253] op_sel_hi:[1,0] neg_lo:[0,1] neg_hi:[0,1]\n" \
    "v_pk_add_f32 v[24:25], v[24:25], v[252:253] op_sel_hi:[1,0] neg_lo:[0,1] neg_hi:[0,1]\n" \
    "v_pk_add_f32 v[26:27], v[26:27], v[252:253] op_sel_hi:[1,0] neg_lo:[0,1] neg_hi:[0,1]\nv_exp_f32 v16, v16\nv_exp_f32 v17, v17\nv_exp_f32 v18, v18\nv_exp_f32 v19, v19\n" \
    "v_exp_f32 v24, v24\nv_pk_add_f32 v[254:255], v[16:17], v[18:19]\nv_exp_f32 v25, v25\nv_add_f32 v254, v254, v255\nv_exp_f32 v26, v26\nv_exp_f32 v27, v27\n" \
    "v_add_f32_dpp v252, v254, v254 row_ror:8 row_mask:0xf bank_mask:0xf\nv_add_f32_dpp v253, v254, v254 row_ror:8 row_mask:0xf bank_mask:0xf\n" \
    "v_pk_add_f32 v[254:255], v[24:25], v[26:27]\ns_nop 0\nv_permlane32_swap_b32 v252, v253\nv_pk_add_f32 v[254:255], v[254:255], v[252:253]\n" \
    "v_pk_add_f32 v[252:253], v[254:255], v[254:255] op_sel:[0,1] op_sel_hi:[1,0]\ns_nop 1\nv_permlane16_swap_b32 v252, v253\nv_add_f32 v252, v252, v253\nv_rcp_f32 v252, v252\n" \
    "s_nop 0\nv_pk_mul_f32 v[24:25], v[24:25], v[252:253] op_sel_hi:[1,0]\nv_pk_mul_f32 v[26:27], v[26:27], v[252:253] op_sel_hi:[1,0]\nv_cvt_pk_bf16_f32 v20, v24, v25\n" \
    "v_cvt_pk_bf16_f32 v21, v26, v27\nv_pk_mul_f32 v[16:17], v[16:17], v[252:253] op_sel_hi:[1,0]\nv_pk_mul_f32 v[18:19], v[18:19], v[252:253] op_sel_hi:[1,0]\n" \
    "v_permlane16_swap_b32 v20, v22\nv_cvt_pk_bf16_f32 v16, v16, v17\nv_cvt_pk_bf16_f32 v17, v18, v19\nv_permlane16_swap_b32 v21, v23\n" \
    "v_mov_b32_dpp v20, v16 quad_perm:[0,1,2,3] row_mask:0xf bank_mask:0x3\nv_mov_b32_dpp v22, v16 row_ror:8 row_mask:0xf bank_mask:0x3\n" \
    "v_mov_b32_dpp v21, v17 quad_perm:[0,1,2,3] row_mask:0xf bank_mask:0x3\nv_mov_b32_dpp v23, v17 row_ror:8 row_mask:0xf bank_mask:0x3\n" \
    "buffer_store_dwordx4 v[20:23], v7, s[64:67], s82 offen sc1\nbuffer_store_dwordx4 v[20:23], v7, s[64:67], s74 offen sc1\n.Lq3_%=:\nds_read_b128 v[68:71], v5 offset:0\n" \
    "ds_read_b128 v[56:59], v5 offset:1024\nds_read_b128 v[60:63], v5 offset:2048\nds_read_b128 v[40:43], v5 offset:3072\ns_mov_b32 s40, s84\ns_add_u32 s35, s35, 2\n" \
    "v_mov_b32 v15, v15\nv_mov_b32 v15, v15\nv_mov_b32 v15, v15\nv_mov_b32 v15, v15\nv_mov_b32 v15, v15\nv_mov_b32 v15, v15\nv_mov_b32 v15, v15\nv_mov_b32 v15, v15\n" \
    "s_waitcnt lgkmcnt(3)\nv_mfma_f32_16x16x32_bf16 v[16:19], v[48:51], v[68:71], 0\nv_mfma_f32_16x16x32_bf16 v[20:23], v[32:35], v[68:71], 0\ns_waitcnt lgkmcnt(2)\n" \
    "v_mfma_f32_16x16x32_bf16 v[16:19], v[36:39], v[56:59], v[16:19]\nv_mfma_f32_16x16x32_bf16 v[20:23], v[72:75], v[56:59], v[20:23]\ns_waitcnt lgkmcnt(1)\n" \
    "v_mfma_f32_16x16x32_bf16 v[16:19], v[76:79], v[60:63], v[16:19]\nv_mfma_f32_16x16x32_bf16 v[20:23], v[52:55], v[60:63], v[20:23]\ns_waitcnt lgkmcnt(0)\n" \
    "v_mfma_f32_16x16x32_bf16 v[16:19], v[80:83], v[40:43], v[16:19]\nv_mfma_f32_16x16x32_bf16 v[20:23], v[64:67], v[40:43], v[20:23]\n" \
    "v_mfma_f32_16x16x32_bf16 v[24:27], v[44:47], v[68:71], 0\nv_mfma_f32_16x16x32_bf16 v[24:27], v[84:87], v[56:59], v[24:27]\n" \
    "v_mfma_f32_16x16x32_bf16 v[24:27], v[88:91], v[60:63], v[24:27]\nv_mfma_f32_16x16x32_bf16 v[24:27], v[28:31], v[40:43], v[24:27]\ns_cmp_lt_u32 s35, 64\n" \
    "s_cbranch_scc1 .Lq2_%=\n.Lq8_%=:\ns_nop 1\nv_mov_b32_dpp v16, v20 quad_perm:[0,1,2,3] row_mask:0xf bank_mask:0xc\n" \
    "v_mov_b32_dpp v17, v21 quad_perm:[0,1,2,3] row_mask:0xf bank_mask:0xc\nv_mov_b32_dpp v18, v22 quad_perm:[0,1,2,3] row_mask:0xf bank_mask:0xc\n" \
    "v_mov_b32_dpp v19, v23 quad_perm:[0,1,2,3] row_mask:0xf bank_mask:0xc\nv_mov_b32 v254, 0xff800000\nv_cvt_pk_bf16_f32 v16, 0, v16\nv_cvt_pk_bf16_f32 v17, 0, v17\n" \
    "v_cvt_pk_bf16_f32 v18, 0, v18\nv_cvt_pk_bf16_f32 v19, 0, v19\nv_mul_f32 v16, s33, v16\nv_mul_f32 v17, s33, v17\nv_mul_f32 v18, s33, v18\nv_mul_f32 v19, s33, v19\n" \
    "v_cvt_pk_bf16_f32 v16, 0, v16\nv_cvt_pk_bf16_f32 v17, 0, v17\nv_cvt_pk_bf16_f32 v18, 0, v18\nv_cvt_pk_bf16_f32 v19, 0, v19\nv_cndmask_b32 v16, v254, v16, s[50:51]\n" \
    "v_cndmask_b32 v17, v254, v17, s[52:53]\nv_cndmask_b32 v18, v254, v18, s[54:55]\nv_cndmask_b32 v19, v254, v19, s[56:57]\nv_max3_f32 v254, v16, v17, v18\n" \
    "v_max_f32 v254, v254, v19\ns_nop 1\nv_max_f32_dpp v252, v254, v254 row_ror:8 row_mask:0xf bank_mask:0xf\n" \
    "v_max_f32_dpp v253, v254, v254 row_ror:8 row_mask:0xf bank_mask:0xf\ns_nop 1\nv_permlane32_swap_b32 v252, v253\nv_max_f32 v252, v252, v253\nv_cvt_pk_bf16_f32 v24, 0, v24\n" \
    "v_cvt_pk_bf16_f32 v25, 0, v25\nv_cvt_pk_bf16_f32 v26, 0, v26\nv_cvt_pk_bf16_f32 v27, 0, v27\nv_mul_f32 v24, s33, v24\nv_mul_f32 v25, s33, v25\nv_mul_f32 v26, s33, v26\n" \
    "v_mul_f32 v27, s33, v27\nv_cvt_pk_bf16_f32 v24, 0, v24\nv_cvt_pk_bf16_f32 v25, 0, v25\nv_cvt_pk_bf16_f32 v26, 0, v26\nv_cvt_pk_bf16_f32 v27, 0, v27\n" \
    "v_max3_f32 v254, v252, v24, v25\nv_max3_f32 v252, v254, v26, v27\nv_max3_f32 v253, v254, v26, v27\ns_nop 1\nv_permlane16_swap_b32 v252, v253\nv_max_f32 v252, v252, v253\n" \
    "v_pk_add_f32 v[16:17], v[16:17], v[252:253] op_sel_hi:[1,0] neg_lo:[0,1] neg_hi:[0,1]\n" \
    "v_pk_add_f32 v[18:19], v[18:19], v[252:253] op_sel_hi:[1,0] neg_lo:[0,1] neg_hi:[0,1]\n" \
    "v_pk_add_f32 v[24:25], v[24:25], v[252:253] op_sel_hi:[1,0] neg_lo:[0,1] neg_hi:[0,1]\n" \
    "v_pk_add_f32 v[26:27], v[26:27], v[252:253] op_sel_hi:[1,0] neg_lo:[0,1] neg_hi:[0,1]\nv_mul_f32 v16, s34, v16\nv_mul_f32 v17, s34, v17\nv_mul_f32 v18, s34, v18\n" \
    "v_mul_f32 v19, s34, v19\nv_mul_f32 v24, s34, v24\nv_mul_f32 v25, s34, v25\nv_mul_f32 v26, s34, v26\nv_mul_f32 v27, s34, v27\nv_exp_f32 v16, v16\nv_exp_f32 v17, v17\n" \
    "v_exp_f32 v18, v18\nv_exp_f32 v19, v19\nv_exp_f32 v24, v24\nv_pk_add_f32 v[254:255], v[16:17], v[18:19]\nv_exp_f32 v25, v25\nv_add_f32 v254, v254, v255\n" \
    "v_exp_f32 v26, v26\nv_exp_f32 v27, v27\nv_add_f32_dpp v252, v254, v254 row_ror:8 row_mask:0xf bank_mask:0xf\n" \
    "v_add_f32_dpp v253, v254, v254 row_ror:8 row_mask:0xf bank_mask:0xf\nv_pk_add_f32 v[254:255], v[24:25], v[26:27]\ns_nop 0\nv_permlane32_swap_b32 v252, v253\n" \
    "v_pk_add_f32 v[254:255], v[254:255], v[252:253]\nv_pk_add_f32 v[252:253], v[254:255], v[254:255] op_sel:[0,1] op_sel_hi:[1,0]\ns_nop 1\nv_permlane16_swap_b32 v252, v253\n" \
    "v_add_f32 v252, v252, v253\nv_rcp_f32 v252, v252\ns_nop 0\nv_pk_mul_f32 v[24:25], v[24:25], v[252:253] op_sel_hi:[1,0]\n" \
    "v_pk_mul_f32 v[26:27], v[26:27], v[252:253] op_sel_hi:[1,0]\nv_cvt_pk_bf16_f32 v20, v24, v25\nv_cvt_pk_bf16_f32 v21, v26, v27\n" \
    "v_pk_mul_f32 v[16:17], v[16:17], v[252:253] op_sel_hi:[1,0]\nv_pk_mul_f32 v[18:19], v[18:19], v[252:253] op_sel_hi:[1,0]\nv_permlane16_swap_b32 v20, v22\n" \
    "v_cvt_pk_bf16_f32 v16, v16, v17\nv_cvt_pk_bf16_f32 v17, v18, v19\nv_permlane16_swap_b32 v21, v23\nv_mov_b32_dpp v20, v16 quad_perm:[0,1,2,3] row_mask:0xf bank_mask:0x3\n" \
    "v_mov_b32_dpp v22, v16 row_ror:8 row_mask:0xf bank_mask:0x3\nv_mov_b32_dpp v21, v17 quad_perm:[0,1,2,3] row_mask:0xf bank_mask:0x3\n" \
    "v_mov_b32_dpp v23, v17 row_ror:8 row_mask:0xf bank_mask:0x3\ns_xor_b32 s74, s83, 0x2000\nbuffer_store_dwordx4 v[20:23], v7, s[64:67], s83 offen sc1\n" \
    "buffer_store_dwordx4 v[20:23], v7, s[64:67], s74 offen sc1\ns_cmp_lt_u32 s35, 64\ns_cbranch_scc1 .Lq9_%=\ns_endpgm\n.Lq14_%=:\ns_mov_b32 s75, 33024\n" \
    "buffer_load_dword v253, off, s[64:67], s75 sc1\ns_waitcnt vmcnt(0)\nv_readfirstlane_b32 s74, v253\ns_nop 0\ns_cmp_ge_u32 s74, 32\ns_cbranch_scc1 .Lq15_%=\ns_sleep 8\n" \
    "s_add_u32 s68, s68, 1\ns_and_b32 s69, s68, 63\ns_cbranch_scc1 .Lq14_%=\ns_cmp_gt_u32 s68, 0x400000\ns_cbranch_scc1 .Lq1_%=\nbuffer_load_dword v253, off, s[64:67], 0 sc1\n" \
    "s_waitcnt vmcnt(0)\nv_readfirstlane_b32 s69, v253\ns_nop 0\ns_cmp_eq_u32 s69, 1\ns_cbranch_scc0 .Lq14_%=\ns_endpgm\n.Lq16_%=:\ns_memrealtime s[72:73]\n" \
    "s_waitcnt lgkmcnt(0)\ns_mov_b32 s80, s72\ns_branch .Lq12_%=\n.Lq10_%=:\nbuffer_load_dword v13, off, s[64:67], s42 sc1\ns_waitcnt vmcnt(0)\ns_branch .Lq17_%=\n.Lq13_%=:\n" \
    "s_sleep 6\ns_add_u32 s68, s68, 1\ns_and_b32 s69, s68, 63\ns_cbranch_scc1 .Lq10_%=\ns_cmp_gt_u32 s68, 0x400000\ns_cbranch_scc1 .Lq1_%=\n" \
    "buffer_load_dword v253, off, s[64:67], 0 sc1\ns_waitcnt vmcnt(0)\nv_readfirstlane_b32 s69, v253\ns_nop 0\ns_cmp_eq_u32 s69, 1\ns_cbranch_scc0 .Lq10_%=\ns_endpgm\n" \
    ".Lq20_%=:\ns_add_u32 s80, s80, s43\ns_xor_b32 s81, s81, 0x1000\ns_xor_b32 s82, s82, 0x4000\ns_cmp_eq_u32 s61, 0\ns_cbranch_scc1 .Lq22_%=\ns_lshr_b32 s69, s81, 5\n" \
    "s_and_b32 s69, s69, 128\ns_lshl_b32 s74, s80, 8\ns_add_u32 s74, s74, s69\ns_add_u32 s74, s74, s35\nv_mov_b32 v253, s74\ns_mov_b64 exec, 1\n" \
    "buffer_store_dword v253, off, s[64:67], s61 sc1\ns_mov_b64 exec, -1\n.Lq22_%=:\ns_add_u32 s68, s68, 1\ns_and_b32 s69, s68, 63\ns_cbranch_scc1 .Lq19_%=\n" \
    "s_cmp_gt_u32 s68, 0x400000\ns_cbranch_scc1 .Lq1_%=\nbuffer_load_dword v253, off, s[64:67], 0 sc1\ns_waitcnt vmcnt(0)\nv_readfirstlane_b32 s69, v253\ns_nop 0\n" \
    "s_cmp_eq_u32 s69, 1\ns_cbranch_scc0 .Lq19_%=\ns_endpgm\n.Lq24_%=:\ns_add_u32 s80, s80, s43\ns_xor_b32 s81, s81, 0x1000\ns_xor_b32 s82, s82, 0x4000\ns_cmp_eq_u32 s61, 0\n" \
    "s_cbranch_scc1 .Lq26_%=\ns_lshr_b32 s69, s81, 5\ns_and_b32 s69, s69, 128\ns_lshl_b32 s74, s80, 8\ns_add_u32 s74, s74, s69\ns_add_u32 s74, s74, s35\nv_mov_b32 v253, s74\n" \
    "s_mov_b64 exec, 1\nbuffer_store_dword v253, off, s[64:67], s61 sc1\ns_mov_b64 exec, -1\n.Lq26_%=:\ns_add_u32 s68, s68, 1\ns_and_b32 s69, s68, 63\ns_cbranch_scc1 .Lq23_%=\n" \
    "s_cmp_gt_u32 s68, 0x400000\ns_cbranch_scc1 .Lq1_%=\nbuffer_load_dword v253, off, s[64:67], 0 sc1\ns_waitcnt vmcnt(0)\nv_readfirstlane_b32 s69, v253\ns_nop 0\n" \
    "s_cmp_eq_u32 s69, 1\ns_cbranch_scc0 .Lq23_%=\ns_endpgm\n.Lq28_%=:\ns_add_u32 s80, s80, s43\ns_xor_b32 s81, s81, 0x1000\ns_xor_b32 s82, s82, 0x4000\ns_cmp_eq_u32 s61, 0\n" \
    "s_cbranch_scc1 .Lq30_%=\ns_lshr_b32 s69, s81, 5\ns_and_b32 s69, s69, 128\ns_lshl_b32 s74, s80, 8\ns_add_u32 s74, s74, s69\ns_add_u32 s74, s74, s35\nv_mov_b32 v253, s74\n" \
    "s_mov_b64 exec, 1\nbuffer_store_dword v253, off, s[64:67], s61 sc1\ns_mov_b64 exec, -1\n.Lq30_%=:\ns_add_u32 s68, s68, 1\ns_and_b32 s69, s68, 63\ns_cbranch_scc1 .Lq27_%=\n" \
    "s_cmp_gt_u32 s68, 0x400000\ns_cbranch_scc1 .Lq1_%=\nbuffer_load_dword v253, off, s[64:67], 0 sc1\ns_waitcnt vmcnt(0)\nv_readfirstlane_b32 s69, v253\ns_nop 0\n" \
    "s_cmp_eq_u32 s69, 1\ns_cbranch_scc0 .Lq27_%=\ns_endpgm\n.Lq32_%=:\ns_add_u32 s80, s80, s43\ns_xor_b32 s81, s81, 0x1000\ns_xor_b32 s82, s82, 0x4000\ns_cmp_eq_u32 s61, 0\n" \
    "s_cbranch_scc1 .Lq34_%=\ns_lshr_b32 s69, s81, 5\ns_and_b32 s69, s69, 128\ns_lshl_b32 s74, s80, 8\ns_add_u32 s74, s74, s69\ns_add_u32 s74, s74, s35\nv_mov_b32 v253, s74\n" \
    "s_mov_b64 exec, 1\nbuffer_store_dword v253, off, s[64:67], s61 sc1\ns_mov_b64 exec, -1\n.Lq34_%=:\ns_add_u32 s68, s68, 1\ns_and_b32 s69, s68, 63\ns_cbranch_scc1 .Lq31_%=\n" \
    "s_cmp_gt_u32 s68, 0x400000\ns_cbranch_scc1 .Lq1_%=\nbuffer_load_dword v253, off, s[64:67], 0 sc1\ns_waitcnt vmcnt(0)\nv_readfirstlane_b32 s69, v253\ns_nop 0\n" \
    "s_cmp_eq_u32 s69, 1\ns_cbranch_scc0 .Lq31_%=\ns_endpgm\n.Lq1_%=:\nv_mov_b32 v253, 1\nbuffer_store_dword v253, off, s[64:67], 0 sc1\ns_endpgm\n"
#define ASM_V \
    "s_add_u32 m0, s41, 0\ns_xor_b32 s85, s62, 0\ns_add_u32 s85, s85, s40\nbuffer_load_dwordx4 v11, s[64:67], s85 offen sc1 lds\n" \
    "buffer_load_dwordx4 v12, s[64:67], s85 offen offset:1024 sc1 lds\ns_add_u32 m0, s41, 8192\ns_xor_b32 s85, s62, 256\ns_add_u32 s85, s85, s40\n" \
    "buffer_load_dwordx4 v11, s[64:67], s85 offen sc1 lds\nbuffer_load_dwordx4 v12, s[64:67], s85 offen offset:1024 sc1 lds\ns_add_u32 m0, s41, 16384\ns_xor_b32 s85, s62, 512\n" \
    "s_add_u32 s85, s85, s40\nbuffer_load_dwordx4 v11, s[64:67], s85 offen sc1 lds\nbuffer_load_dwordx4 v12, s[64:67], s85 offen offset:1024 sc1 lds\ns_add_u32 m0, s41, 40960\n" \
    "s_xor_b32 s85, s62, 768\ns_add_u32 s85, s85, s40\nbuffer_load_dwordx4 v11, s[64:67], s85 offen sc1 lds\nbuffer_load_dwordx4 v12, s[64:67], s85 offen offset:1024 sc1 lds\n" \
    "s_waitcnt vmcnt(0)\ns_barrier\nds_read_b128 v[56:59], v3 offset:0\nds_read_b128 v[60:63], v9 offset:0\nds_read_b128 v[64:67], v9 offset:4096\n" \
    "ds_read_b128 v[68:71], v3 offset:1024\nds_read_b128 v[72:75], v10 offset:0\nds_read_b128 v[76:79], v10 offset:4096\ns_add_u32 s84, s40, 0x20000\ns_cmp_lt_u32 s49, 62\n" \
    "s_cselect_b32 s84, s84, s40\ns_waitcnt lgkmcnt(3)\nv_mfma_f32_16x16x32_bf16 v[16:19], a[0:3], v[60:63], 0\ns_add_u32 m0, s41, 24576\ns_xor_b32 s85, s62, 1024\n" \
    "v_mfma_f32_16x16x32_bf16 v[20:23], a[0:3], v[64:67], 0\nds_read_b128 v[80:83], v3 offset:2048\ns_add_u32 s85, s85, s40\n" \
    "v_mfma_f32_16x16x32_bf16 v[24:27], v[56:59], v[60:63], 0\nds_read_b128 v[60:63], v9 offset:1024\nbuffer_load_dwordx4 v11, s[64:67], s85 offen sc1 lds\n" \
    "v_mfma_f32_16x16x32_bf16 v[28:31], v[56:59], v[64:67], 0\nds_read_b128 v[56:59], v9 offset:5120\ns_waitcnt lgkmcnt(3)\n" \
    "v_mfma_f32_16x16x32_bf16 v[16:19], a[4:7], v[72:75], v[16:19]\nbuffer_load_dwordx4 v12, s[64:67], s85 offen offset:1024 sc1 lds\ns_add_u32 m0, s41, 32768\n" \
    "v_mfma_f32_16x16x32_bf16 v[20:23], a[4:7], v[76:79], v[20:23]\nds_read_b128 v[64:67], v3 offset:3072\ns_xor_b32 s85, s62, 1280\n" \
    "v_mfma_f32_16x16x32_bf16 v[24:27], v[68:71], v[72:75], v[24:27]\nds_read_b128 v[72:75], v10 offset:1024\ns_add_u32 s85, s85, s40\n" \
    "v_mfma_f32_16x16x32_bf16 v[28:31], v[68:71], v[76:79], v[28:31]\nds_read_b128 v[68:71], v10 offset:5120\nbuffer_load_dwordx4 v11, s[64:67], s85 offen sc1 lds\n" \
    "s_waitcnt lgkmcnt(3)\nv_mfma_f32_16x16x32_bf16 v[16:19], a[8:11], v[60:63], v[16:19]\nv_mfma_f32_16x16x32_bf16 v[20:23], a[8:11], v[56:59], v[20:23]\n" \
    "ds_read_b128 v[76:79], v3 offset:4096\nbuffer_load_dwordx4 v12, s[64:67], s85 offen offset:1024 sc1 lds\nv_mov_b64 v[252:253], -1\n" \
    "v_mfma_f32_16x16x32_bf16 v[24:27], v[80:83], v[60:63], v[24:27]\nds_read_b128 v[60:63], v9 offset:8192\nv_mov_b64 v[254:255], -1\n" \
    "v_mfma_f32_16x16x32_bf16 v[28:31], v[80:83], v[56:59], v[28:31]\nds_read_b128 v[56:59], v9 offset:12288\ns_and_b32 s69, s49, 15\ns_waitcnt lgkmcnt(3)\n" \
    "v_mfma_f32_16x16x32_bf16 v[16:19], a[12:15], v[72:75], v[16:19]\ns_lshl_b32 s69, s69, 19\ns_add_u32 s82, s38, s69\n" \
    "v_mfma_f32_16x16x32_bf16 v[20:23], a[12:15], v[68:71], v[20:23]\nds_read_b128 v[80:83], v3 offset:5120\ns_lshr_b32 s69, s82, 8\n" \
    "v_mfma_f32_16x16x32_bf16 v[24:27], v[64:67], v[72:75], v[24:27]\nds_read_b128 v[72:75], v10 offset:8192\ns_and_b32 s69, s69, 0x2000\n" \
    "v_mfma_f32_16x16x32_bf16 v[28:31], v[64:67], v[68:71], v[28:31]\nds_read_b128 v[64:67], v10 offset:12288\ns_xor_b32 s82, s82, s69\ns_waitcnt lgkmcnt(3)\n" \
    "v_mfma_f32_16x16x32_bf16 v[16:19], a[16:19], v[60:63], v[16:19]\ns_and_b32 s69, s49, 255\ns_lshl_b32 s69, s69, 19\n" \
    "v_mfma_f32_16x16x32_bf16 v[20:23], a[16:19], v[56:59], v[20:23]\nds_read_b128 v[68:71], v3 offset:6144\ns_add_u32 s74, s63, s69\n" \
    "v_mfma_f32_16x16x32_bf16 v[24:27], v[76:79], v[60:63], v[24:27]\nds_read_b128 v[60:63], v9 offset:9216\ns_cmp_lt_u32 s49, 64\ns_cselect_b32 s74, s74, 0x7fff0000\n" \
    "v_mfma_f32_16x16x32_bf16 v[28:31], v[76:79], v[56:59], v[28:31]\nds_read_b128 v[56:59], v9 offset:13312\ns_waitcnt lgkmcnt(3)\n" \
    "v_mfma_f32_16x16x32_bf16 v[16:19], a[20:23], v[72:75], v[16:19]\nv_mfma_f32_16x16x32_bf16 v[20:23], a[20:23], v[64:67], v[20:23]\nds_read_b128 v[76:79], v3 offset:7168\n" \
    "v_mfma_f32_16x16x32_bf16 v[24:27], v[80:83], v[72:75], v[24:27]\nds_read_b128 v[72:75], v10 offset:9216\nv_mfma_f32_16x16x32_bf16 v[28:31], v[80:83], v[64:67], v[28:31]\n" \
    "ds_read_b128 v[64:67], v10 offset:13312\ns_waitcnt lgkmcnt(3)\nv_mfma_f32_16x16x32_bf16 v[16:19], a[24:27], v[60:63], v[16:19]\n" \
    "buffer_store_dwordx4 v[252:255], v2, s[64:67], s74 offen offset:0 sc1\nv_mfma_f32_16x16x32_bf16 v[20:23], a[24:27], v[56:59], v[20:23]\n" \
    "ds_read_b128 v[80:83], v3 offset:8192\nv_mfma_f32_16x16x32_bf16 v[24:27], v[68:71], v[60:63], v[24:27]\nds_read_b128 v[60:63], v9 offset:16384\n" \
    "v_mfma_f32_16x16x32_bf16 v[28:31], v[68:71], v[56:59], v[28:31]\nds_read_b128 v[56:59], v9 offset:20480\ns_waitcnt lgkmcnt(3)\n" \
    "v_mfma_f32_16x16x32_bf16 v[16:19], a[28:31], v[72:75], v[16:19]\nv_mfma_f32_16x16x32_bf16 v[20:23], a[28:31], v[64:67], v[20:23]\nds_read_b128 v[68:71], v3 offset:9216\n" \
    "v_mfma_f32_16x16x32_bf16 v[24:27], v[76:79], v[72:75], v[24:27]\nds_read_b128 v[72:75], v10 offset:16384\n" \
    "buffer_store_dwordx4 v[252:255], v2, s[64:67], s74 offen offset:1024 sc1\nv_mfma_f32_16x16x32_bf16 v[28:31], v[76:79], v[64:67], v[28:31]\n" \
    "ds_read_b128 v[64:67], v10 offset:20480\ns_waitcnt lgkmcnt(3)\nv_mfma_f32_16x16x32_bf16 v[16:19], a[32:35], v[60:63], v[16:19]\n" \
    "v_mfma_f32_16x16x32_bf16 v[20:23], a[32:35], v[56:59], v[20:23]\nds_read_b128 v[76:79], v3 offset:10240\nv_mfma_f32_16x16x32_bf16 v[24:27], v[80:83], v[60:63], v[24:27]\n" \
    "ds_read_b128 v[60:63], v9 offset:17408\nv_mfma_f32_16x16x32_bf16 v[28:31], v[80:83], v[56:59], v[28:31]\nds_read_b128 v[56:59], v9 offset:21504\ns_waitcnt lgkmcnt(3)\n" \
    "v_mfma_f32_16x16x32_bf16 v[16:19], a[36:39], v[72:75], v[16:19]\nv_mfma_f32_16x16x32_bf16 v[20:23], a[36:39], v[64:67], v[20:23]\nds_read_b128 v[80:83], v3 offset:11264\n" \
    "v_mfma_f32_16x16x32_bf16 v[24:27], v[68:71], v[72:75], v[24:27]\nds_read_b128 v[72:75], v10 offset:17408\nv_mfma_f32_16x16x32_bf16 v[28:31], v[68:71], v[64:67], v[28:31]\n" \
    "ds_read_b128 v[64:67], v10 offset:21504\ns_waitcnt lgkmcnt(3)\nv_mfma_f32_16x16x32_bf16 v[16:19], a[40:43], v[60:63], v[16:19]\n" \
    "v_mfma_f32_16x16x32_bf16 v[20:23], a[40:43], v[56:59], v[20:23]\nds_read_b128 v[68:71], v3 offset:12288\ns_barrier\n" \
    "v_mfma_f32_16x16x32_bf16 v[24:27], v[76:79], v[60:63], v[24:27]\nds_read_b128 v[60:63], v9 offset:40960\nv_mfma_f32_16x16x32_bf16 v[28:31], v[76:79], v[56:59], v[28:31]\n" \
    "ds_read_b128 v[56:59], v9 offset:45056\ns_add_u32 m0, s41, 0\ns_waitcnt lgkmcnt(3)\nv_mfma_f32_16x16x32_bf16 v[16:19], a[44:47], v[72:75], v[16:19]\n" \
    "s_xor_b32 s85, s62, 1536\ns_add_u32 s85, s85, s40\nv_mfma_f32_16x16x32_bf16 v[20:23], a[44:47], v[64:67], v[20:23]\nds_read_b128 v[76:79], v3 offset:13312\n" \
    "buffer_load_dwordx4 v11, s[64:67], s85 offen sc1 lds\nv_mfma_f32_16x16x32_bf16 v[24:27], v[80:83], v[72:75], v[24:27]\nds_read_b128 v[72:75], v10 offset:40960\n" \
    "v_mfma_f32_16x16x32_bf16 v[28:31], v[80:83], v[64:67], v[28:31]\nds_read_b128 v[64:67], v10 offset:45056\nbuffer_load_dwordx4 v12, s[64:67], s85 offen offset:1024 sc1 lds\n" \
    "s_waitcnt lgkmcnt(3)\nv_mfma_f32_16x16x32_bf16 v[16:19], a[48:51], v[60:63], v[16:19]\ns_add_u32 m0, s41, 8192\ns_xor_b32 s85, s62, 1792\n" \
    "v_mfma_f32_16x16x32_bf16 v[20:23], a[48:51], v[56:59], v[20:23]\nds_read_b128 v[80:83], v3 offset:14336\ns_add_u32 s85, s85, s40\n" \
    "v_mfma_f32_16x16x32_bf16 v[24:27], v[68:71], v[60:63], v[24:27]\nds_read_b128 v[60:63], v9 offset:41984\nbuffer_load_dwordx4 v11, s[64:67], s85 offen sc1 lds\n" \
    "v_mfma_f32_16x16x32_bf16 v[28:31], v[68:71], v[56:59], v[28:31]\nds_read_b128 v[56:59], v9 offset:46080\ns_waitcnt lgkmcnt(3)\n" \
    "v_mfma_f32_16x16x32_bf16 v[16:19], a[52:55], v[72:75], v[16:19]\nbuffer_load_dwordx4 v12, s[64:67], s85 offen offset:1024 sc1 lds\n" \
    "v_mfma_f32_16x16x32_bf16 v[20:23], a[52:55], v[64:67], v[20:23]\nds_read_b128 v[68:71], v3 offset:15360\nv_mfma_f32_16x16x32_bf16 v[24:27], v[76:79], v[72:75], v[24:27]\n" \
    "ds_read_b128 v[72:75], v10 offset:41984\nv_mfma_f32_16x16x32_bf16 v[28:31], v[76:79], v[64:67], v[28:31]\nds_read_b128 v[64:67], v10 offset:46080\ns_waitcnt lgkmcnt(3)\n" \
    "v_mfma_f32_16x16x32_bf16 v[16:19], a[56:59], v[60:63], v[16:19]\nv_mfma_f32_16x16x32_bf16 v[20:23], a[56:59], v[56:59], v[20:23]\nds_read_b128 v[76:79], v3 offset:16384\n" \
    "s_waitcnt vmcnt(6)\ns_barrier\nv_mfma_f32_16x16x32_bf16 v[24:27], v[80:83], v[60:63], v[24:27]\nds_read_b128 v[60:63], v9 offset:24576\n" \
    "v_mfma_f32_16x16x32_bf16 v[28:31], v[80:83], v[56:59], v[28:31]\nds_read_b128 v[56:59], v9 offset:28672\ns_waitcnt lgkmcnt(3)\n" \
    "v_mfma_f32_16x16x32_bf16 v[16:19], a[60:63], v[72:75], v[16:19]\nv_mfma_f32_16x16x32_bf16 v[20:23], a[60:63], v[64:67], v[20:23]\nds_read_b128 v[80:83], v3 offset:17408\n" \
    "v_mfma_f32_16x16x32_bf16 v[24:27], v[68:71], v[72:75], v[24:27]\nds_read_b128 v[72:75], v10 offset:24576\nv_mfma_f32_16x16x32_bf16 v[28:31], v[68:71], v[64:67], v[28:31]\n" \
    "ds_read_b128 v[64:67], v10 offset:28672\ns_waitcnt lgkmcnt(3)\nv_mfma_f32_16x16x32_bf16 v[16:19], a[64:67], v[60:63], v[16:19]\n" \
    "v_mfma_f32_16x16x32_bf16 v[20:23], a[64:67], v[56:59], v[20:23]\nds_read_b128 v[68:71], v3 offset:18432\nv_mfma_f32_16x16x32_bf16 v[24:27], v[76:79], v[60:63], v[24:27]\n" \
    "ds_read_b128 v[60:63], v9 offset:25600\nv_mfma_f32_16x16x32_bf16 v[28:31], v[76:79], v[56:59], v[28:31]\nds_read_b128 v[56:59], v9 offset:29696\ns_waitcnt lgkmcnt(3)\n" \
    "v_mfma_f32_16x16x32_bf16 v[16:19], a[68:71], v[72:75], v[16:19]\nbuffer_store_dwordx4 v[252:255], v7, s[64:67], s82 offen sc1\ns_add_u32 s82, s82, 0x10000\n" \
    "v_mfma_f32_16x16x32_bf16 v[20:23], a[68:71], v[64:67], v[20:23]\nds_read_b128 v[76:79], v3 offset:19456\nv_mfma_f32_16x16x32_bf16 v[24:27], v[80:83], v[72:75], v[24:27]\n" \
    "ds_read_b128 v[72:75], v10 offset:25600\nv_mfma_f32_16x16x32_bf16 v[28:31], v[80:83], v[64:67], v[28:31]\nds_read_b128 v[64:67], v10 offset:29696\ns_waitcnt lgkmcnt(3)\n" \
    "v_mfma_f32_16x16x32_bf16 v[16:19], a[72:75], v[60:63], v[16:19]\nv_mfma_f32_16x16x32_bf16 v[20:23], a[72:75], v[56:59], v[20:23]\nds_read_b128 v[80:83], v3 offset:20480\n" \
    "v_mfma_f32_16x16x32_bf16 v[24:27], v[68:71], v[60:63], v[24:27]\nds_read_b128 v[60:63], v9 offset:32768\nbuffer_store_dwordx4 v[252:255], v7, s[64:67], s82 offen sc1\n" \
    "v_mfma_f32_16x16x32_bf16 v[28:31], v[68:71], v[56:59], v[28:31]\nds_read_b128 v[56:59], v9 offset:36864\ns_add_u32 s82, s82, 0x10000\ns_waitcnt lgkmcnt(3)\n" \
    "v_mfma_f32_16x16x32_bf16 v[16:19], a[76:79], v[72:75], v[16:19]\nv_mfma_f32_16x16x32_bf16 v[20:23], a[76:79], v[64:67], v[20:23]\nds_read_b128 v[68:71], v3 offset:21504\n" \
    "v_mfma_f32_16x16x32_bf16 v[24:27], v[76:79], v[72:75], v[24:27]\nds_read_b128 v[72:75], v10 offset:32768\nv_mfma_f32_16x16x32_bf16 v[28:31], v[76:79], v[64:67], v[28:31]\n" \
    "ds_read_b128 v[64:67], v10 offset:36864\ns_waitcnt lgkmcnt(3)\nv_mfma_f32_16x16x32_bf16 v[16:19], a[80:83], v[60:63], v[16:19]\n" \
    "v_mfma_f32_16x16x32_bf16 v[20:23], a[80:83], v[56:59], v[20:23]\nds_read_b128 v[76:79], v3 offset:22528\nv_mfma_f32_16x16x32_bf16 v[24:27], v[80:83], v[60:63], v[24:27]\n" \
    "ds_read_b128 v[60:63], v9 offset:33792\nv_mfma_f32_16x16x32_bf16 v[28:31], v[80:83], v[56:59], v[28:31]\nds_read_b128 v[56:59], v9 offset:37888\ns_waitcnt lgkmcnt(3)\n" \
    "v_mfma_f32_16x16x32_bf16 v[16:19], a[84:87], v[72:75], v[16:19]\nv_mfma_f32_16x16x32_bf16 v[20:23], a[84:87], v[64:67], v[20:23]\nds_read_b128 v[80:83], v3 offset:23552\n" \
    "v_mfma_f32_16x16x32_bf16 v[24:27], v[68:71], v[72:75], v[24:27]\nds_read_b128 v[72:75], v10 offset:33792\nv_mfma_f32_16x16x32_bf16 v[28:31], v[68:71], v[64:67], v[28:31]\n" \
    "ds_read_b128 v[64:67], v10 offset:37888\ns_waitcnt lgkmcnt(3)\nv_mfma_f32_16x16x32_bf16 v[16:19], a[88:91], v[60:63], v[16:19]\n" \
    "buffer_load_dword v13, off, s[64:67], s42 sc1\nv_mfma_f32_16x16x32_bf16 v[20:23], a[88:91], v[56:59], v[20:23]\ns_waitcnt vmcnt(3)\ns_barrier\n" \
    "v_mfma_f32_16x16x32_bf16 v[24:27], v[76:79], v[60:63], v[24:27]\nds_read_b128 v[60:63], v9 offset:0\nv_mfma_f32_16x16x32_bf16 v[28:31], v[76:79], v[56:59], v[28:31]\n" \
    "ds_read_b128 v[56:59], v9 offset:4096\ns_add_u32 m0, s41, 16384\ns_waitcnt lgkmcnt(2)\nv_mfma_f32_16x16x32_bf16 v[16:19], a[92:95], v[72:75], v[16:19]\n" \
    "s_xor_b32 s85, s62, 512\ns_add_u32 s85, s85, s84\nv_mfma_f32_16x16x32_bf16 v[20:23], a[92:95], v[64:67], v[20:23]\nbuffer_load_dwordx4 v11, s[64:67], s85 offen sc1 lds\n" \
    "v_mfma_f32_16x16x32_bf16 v[24:27], v[80:83], v[72:75], v[24:27]\nds_read_b128 v[68:71], v10 offset:0\nbuffer_store_dwordx4 v[252:255], v7, s[64:67], s82 offen sc1\n" \
    "v_mfma_f32_16x16x32_bf16 v[28:31], v[80:83], v[64:67], v[28:31]\nds_read_b128 v[64:67], v10 offset:4096\nbuffer_load_dwordx4 v12, s[64:67], s85 offen offset:1024 sc1 lds\n" \
    "s_waitcnt lgkmcnt(2)\nv_mfma_f32_16x16x32_bf16 v[16:19], a[96:99], v[60:63], v[16:19]\ns_add_u32 m0, s41, 40960\ns_xor_b32 s85, s62, 768\n" \
    "v_mfma_f32_16x16x32_bf16 v[20:23], a[96:99], v[56:59], v[20:23]\ns_add_u32 s85, s85, s84\nbuffer_load_dwordx4 v11, s[64:67], s85 offen sc1 lds\n" \
    "v_mfma_f32_16x16x32_bf16 v[24:27], a[104:107], v[60:63], v[24:27]\nds_read_b128 v[60:63], v9 offset:1024\ns_add_u32 s82, s82, 0x10000\n" \
    "v_mfma_f32_16x16x32_bf16 v[28:31], a[104:107], v[56:59], v[28:31]\nds_read_b128 v[56:59], v9 offset:5120\nbuffer_load_dwordx4 v12, s[64:67], s85 offen offset:1024 sc1 lds\n" \
    "s_waitcnt lgkmcnt(2)\nv_mfma_f32_16x16x32_bf16 v[16:19], a[100:103], v[68:71], v[16:19]\nv_mfma_f32_16x16x32_bf16 v[20:23], a[100:103], v[64:67], v[20:23]\n" \
    "v_mfma_f32_16x16x32_bf16 v[24:27], a[108:111], v[68:71], v[24:27]\nds_read_b128 v[68:71], v10 offset:1024\n" \
    "v_mfma_f32_16x16x32_bf16 v[28:31], a[108:111], v[64:67], v[28:31]\nds_read_b128 v[64:67], v10 offset:5120\ns_waitcnt lgkmcnt(2)\n" \
    "v_mfma_f32_16x16x32_bf16 v[16:19], a[112:115], v[60:63], v[16:19]\nbuffer_store_dwordx4 v[252:255], v7, s[64:67], s82 offen sc1\ns_add_u32 s82, s82, 0x10000\n" \
    "v_mfma_f32_16x16x32_bf16 v[20:23], a[112:115], v[56:59], v[20:23]\nv_mfma_f32_16x16x32_bf16 v[24:27], a[120:123], v[60:63], v[24:27]\n" \
    "ds_read_b128 v[60:63], v9 offset:8192\nv_mfma_f32_16x16x32_bf16 v[28:31], a[120:123], v[56:59], v[28:31]\nds_read_b128 v[56:59], v9 offset:12288\ns_waitcnt lgkmcnt(2)\n" \
    "v_mfma_f32_16x16x32_bf16 v[16:19], a[116:119], v[68:71], v[16:19]\nv_mfma_f32_16x16x32_bf16 v[20:23], a[116:119], v[64:67], v[20:23]\n" \
    "v_mfma_f32_16x16x32_bf16 v[24:27], a[124:127], v[68:71], v[24:27]\nds_read_b128 v[68:71], v10 offset:8192\n" \
    "v_mfma_f32_16x16x32_bf16 v[28:31], a[124:127], v[64:67], v[28:31]\nds_read_b128 v[64:67], v10 offset:12288\ns_waitcnt lgkmcnt(2)\n" \
    "v_mfma_f32_16x16x32_bf16 v[16:19], a[128:131], v[60:63], v[16:19]\nv_mfma_f32_16x16x32_bf16 v[20:23], a[128:131], v[56:59], v[20:23]\n" \
    "v_mfma_f32_16x16x32_bf16 v[24:27], a[136:139], v[60:63], v[24:27]\nds_read_b128 v[60:63], v9 offset:9216\n" \
    "v_mfma_f32_16x16x32_bf16 v[28:31], a[136:139], v[56:59], v[28:31]\nds_read_b128 v[56:59], v9 offset:13312\ns_waitcnt lgkmcnt(2)\n" \
    "v_mfma_f32_16x16x32_bf16 v[16:19], a[132:135], v[68:71], v[16:19]\nv_mfma_f32_16x16x32_bf16 v[20:23], a[132:135], v[64:67], v[20:23]\n" \
    "v_mfma_f32_16x16x32_bf16 v[24:27], a[140:143], v[68:71], v[24:27]\nds_read_b128 v[68:71], v10 offset:9216\n" \
    "v_mfma_f32_16x16x32_bf16 v[28:31], a[140:143], v[64:67], v[28:31]\nds_read_b128 v[64:67], v10 offset:13312\ns_waitcnt lgkmcnt(2)\n" \
    "v_mfma_f32_16x16x32_bf16 v[16:19], a[144:147], v[60:63], v[16:19]\nv_mfma_f32_16x16x32_bf16 v[20:23], a[144:147], v[56:59], v[20:23]\n" \
    "v_mfma_f32_16x16x32_bf16 v[24:27], a[152:155], v[60:63], v[24:27]\nv_mfma_f32_16x16x32_bf16 v[28:31], a[152:155], v[56:59], v[28:31]\ns_waitcnt lgkmcnt(0)\n" \
    "v_mfma_f32_16x16x32_bf16 v[16:19], a[148:151], v[68:71], v[16:19]\nv_mfma_f32_16x16x32_bf16 v[20:23], a[148:151], v[64:67], v[20:23]\n" \
    "v_mfma_f32_16x16x32_bf16 v[24:27], a[156:159], v[68:71], v[24:27]\nv_mfma_f32_16x16x32_bf16 v[28:31], a[156:159], v[64:67], v[28:31]\ns_barrier\ns_add_u32 m0, s41, 0\n" \
    "s_xor_b32 s85, s62, 0\ns_add_u32 s85, s85, s84\nbuffer_load_dwordx4 v11, s[64:67], s85 offen sc1 lds\nv_cvt_pk_bf16_f32 v56, v16, v17\nv_cvt_pk_bf16_f32 v57, v18, v19\n" \
    "v_cvt_pk_bf16_f32 v58, v24, v25\nv_cvt_pk_bf16_f32 v59, v26, v27\nds_write_b128 v14, v[56:59] offset:0\nv_cvt_pk_bf16_f32 v60, v20, v21\nv_cvt_pk_bf16_f32 v61, v22, v23\n" \
    "v_cvt_pk_bf16_f32 v62, v28, v29\nv_cvt_pk_bf16_f32 v63, v30, v31\nds_write_b128 v14, v[60:63] offset:4096\n" \
    "buffer_load_dwordx4 v12, s[64:67], s85 offen offset:1024 sc1 lds\ns_add_u32 m0, s41, 8192\ns_xor_b32 s85, s62, 256\ns_add_u32 s85, s85, s84\n" \
    "buffer_load_dwordx4 v11, s[64:67], s85 offen sc1 lds\nbuffer_load_dwordx4 v12, s[64:67], s85 offen offset:1024 sc1 lds\ns_mov_b32 s40, s84\ns_add_u32 s49, s49, 2\n" \
    "s_waitcnt vmcnt(0) lgkmcnt(0)\ns_barrier\n.Lv5_%=:\nds_read_b128 v[56:59], v6 offset:0\nds_read_b128 v[60:63], v6 offset:1024\nds_read_b128 v[64:67], v6 offset:2048\n" \
    "s_and_b32 s69, s50, 15\ns_lshl_b32 s69, s69, 21\ns_add_u32 s86, s39, s69\ns_lshr_b32 s69, s86, 8\ns_and_b32 s69, s69, 0x2000\ns_xor_b32 s86, s86, s69\n" \
    "s_add_u32 s87, s86, 0x40000\ns_add_u32 s88, s86, 0x80000\ns_add_u32 s89, s86, 0xc0000\ns_add_u32 s50, s50, 2\ns_add_u32 s84, s40, 0x20000\ns_cmp_lt_u32 s49, 62\n" \
    "s_cselect_b32 s84, s84, s40\nv_mov_b64 v[32:33], 0\nv_mov_b64 v[34:35], 0\nv_mov_b64 v[36:37], 0\nv_mov_b64 v[38:39], 0\nv_mov_b64 v[40:41], 0\nv_mov_b64 v[42:43], 0\n" \
    "v_mov_b64 v[44:45], 0\nv_mov_b64 v[46:47], 0\ns_waitcnt lgkmcnt(2)\nv_smfmac_f32_16x16x64_bf16 v[32:35], v[56:59], a[160:167], v0\nds_read_b128 v[68:71], v6 offset:3072\n" \
    "s_add_u32 m0, s41, 24576\nv_smfmac_f32_16x16x64_bf16 v[36:39], v[56:59], a[176:183], v0\nds_read_b128 v[72:75], v3 offset:0\ns_xor_b32 s85, s62, 1024\n" \
    "s_waitcnt lgkmcnt(3)\nv_smfmac_f32_16x16x64_bf16 v[32:35], v[60:63], a[168:175], v0\nds_read_b128 v[76:79], v9 offset:0\ns_add_u32 s85, s85, s40\n" \
    "v_smfmac_f32_16x16x64_bf16 v[36:39], v[60:63], a[184:191], v0\nds_read_b128 v[80:83], v9 offset:4096\nbuffer_load_dwordx4 v11, s[64:67], s85 offen sc1 lds\n" \
    "s_waitcnt lgkmcnt(4)\nv_smfmac_f32_16x16x64_bf16 v[32:35], v[64:67], a[192:199], v0\nv_smfmac_f32_16x16x64_bf16 v[36:39], v[64:67], a[208:215], v0\n" \
    "buffer_load_dwordx4 v12, s[64:67], s85 offen offset:1024 sc1 lds\ns_add_u32 m0, s41, 32768\ns_waitcnt lgkmcnt(3)\n" \
    "v_smfmac_f32_16x16x64_bf16 v[32:35], v[68:71], a[200:207], v0\ns_xor_b32 s85, s62, 1280\ns_add_u32 s85, s85, s40\n" \
    "v_smfmac_f32_16x16x64_bf16 v[36:39], v[68:71], a[216:223], v0\nbuffer_load_dwordx4 v11, s[64:67], s85 offen sc1 lds\ns_nop 0\ns_waitcnt lgkmcnt(0)\n" \
    "v_mfma_f32_16x16x32_bf16 v[16:19], a[0:3], v[76:79], 0\ns_nop 0\nv_cvt_pk_bf16_f32 v48, v32, v33\nv_cvt_pk_bf16_f32 v50, v34, v35\n" \
    "v_mfma_f32_16x16x32_bf16 v[20:23], a[0:3], v[80:83], 0\nbuffer_load_dwordx4 v12, s[64:67], s85 offen offset:1024 sc1 lds\nv_cvt_pk_bf16_f32 v49, v36, v37\n" \
    "v_cvt_pk_bf16_f32 v51, v38, v39\nv_mfma_f32_16x16x32_bf16 v[24:27], v[72:75], v[76:79], 0\nv_permlane16_swap_b32 v48, v49\n" \
    "v_mfma_f32_16x16x32_bf16 v[28:31], v[72:75], v[80:83], 0\nv_permlane16_swap_b32 v50, v51\nv_smfmac_f32_16x16x64_bf16 v[40:43], v[56:59], a[224:231], v0\n" \
    "buffer_store_dwordx4 v[48:51], v8, s[64:67], s86 offen offset:0 sc1\nv_mov_b64 v[32:33], 0\nv_smfmac_f32_16x16x64_bf16 v[44:47], v[56:59], a[240:247], v0\n" \
    "ds_read_b128 v[72:75], v3 offset:1024\nv_mov_b64 v[34:35], 0\nv_smfmac_f32_16x16x64_bf16 v[40:43], v[60:63], a[232:239], v0\nds_read_b128 v[76:79], v10 offset:0\n" \
    "v_mov_b64 v[36:37], 0\nv_smfmac_f32_16x16x64_bf16 v[44:47], v[60:63], a[248:255], v0\nds_read_b128 v[80:83], v10 offset:4096\nv_mov_b64 v[38:39], 0\n" \
    "v_smfmac_f32_16x16x64_bf16 v[40:43], v[64:67], v[92:99], v0\nv_mov_b64 v[252:253], -1\nv_smfmac_f32_16x16x64_bf16 v[44:47], v[64:67], v[108:115], v0\n" \
    "v_mov_b64 v[254:255], -1\nv_smfmac_f32_16x16x64_bf16 v[40:43], v[68:71], v[100:107], v0\ns_and_b32 s69, s49, 15\ns_lshl_b32 s69, s69, 19\n" \
    "v_smfmac_f32_16x16x64_bf16 v[44:47], v[68:71], v[116:123], v0\ns_add_u32 s82, s38, s69\ns_lshr_b32 s69, s82, 8\ns_waitcnt lgkmcnt(0)\n" \
    "v_mfma_f32_16x16x32_bf16 v[16:19], a[4:7], v[76:79], v[16:19]\ns_and_b32 s69, s69, 0x2000\nv_cvt_pk_bf16_f32 v52, v40, v41\nv_cvt_pk_bf16_f32 v54, v42, v43\n" \
    "v_mfma_f32_16x16x32_bf16 v[20:23], a[4:7], v[80:83], v[20:23]\ns_xor_b32 s82, s82, s69\nv_cvt_pk_bf16_f32 v53, v44, v45\nv_cvt_pk_bf16_f32 v55, v46, v47\n" \
    "v_mfma_f32_16x16x32_bf16 v[24:27], v[72:75], v[76:79], v[24:27]\ns_and_b32 s69, s49, 255\nv_permlane16_swap_b32 v52, v53\n" \
    "v_mfma_f32_16x16x32_bf16 v[28:31], v[72:75], v[80:83], v[28:31]\ns_lshl_b32 s69, s69, 19\nv_permlane16_swap_b32 v54, v55\n" \
    "v_smfmac_f32_16x16x64_bf16 v[32:35], v[56:59], v[124:131], v0\nbuffer_store_dwordx4 v[52:55], v8, s[64:67], s87 offen offset:0 sc1\nv_mov_b64 v[40:41], 0\n" \
    "v_smfmac_f32_16x16x64_bf16 v[36:39], v[56:59], v[140:147], v0\nds_read_b128 v[72:75], v3 offset:2048\nv_mov_b64 v[42:43], 0\n" \
    "v_smfmac_f32_16x16x64_bf16 v[32:35], v[60:63], v[132:139], v0\nds_read_b128 v[76:79], v9 offset:1024\nv_mov_b64 v[44:45], 0\n" \
    "v_smfmac_f32_16x16x64_bf16 v[36:39], v[60:63], v[148:155], v0\nds_read_b128 v[80:83], v9 offset:5120\nv_mov_b64 v[46:47], 0\n" \
    "v_smfmac_f32_16x16x64_bf16 v[32:35], v[64:67], v[156:163], v0\ns_add_u32 s74, s63, s69\ns_cmp_lt_u32 s49, 64\ns_cselect_b32 s74, s74, 0x7fff0000\n" \
    "v_smfmac_f32_16x16x64_bf16 v[36:39], v[64:67], v[172:179], v0\nv_smfmac_f32_16x16x64_bf16 v[32:35], v[68:71], v[164:171], v0\n" \
    "v_smfmac_f32_16x16x64_bf16 v[36:39], v[68:71], v[180:187], v0\ns_nop 1\ns_waitcnt lgkmcnt(0)\nv_mfma_f32_16x16x32_bf16 v[16:19], a[8:11], v[76:79], v[16:19]\ns_nop 1\n" \
    "v_mfma_f32_16x16x32_bf16 v[20:23], a[8:11], v[80:83], v[20:23]\nv_cvt_pk_bf16_f32 v48, v32, v33\nv_cvt_pk_bf16_f32 v50, v34, v35\n" \
    "v_mfma_f32_16x16x32_bf16 v[24:27], v[72:75], v[76:79], v[24:27]\nbuffer_store_dwordx4 v[252:255], v2, s[64:67], s74 offen offset:0 sc1\nv_cvt_pk_bf16_f32 v49, v36, v37\n" \
    "v_cvt_pk_bf16_f32 v51, v38, v39\nv_mfma_f32_16x16x32_bf16 v[28:31], v[72:75], v[80:83], v[28:31]\nv_permlane16_swap_b32 v48, v49\n" \
    "v_smfmac_f32_16x16x64_bf16 v[40:43], v[56:59], v[188:195], v0\nv_permlane16_swap_b32 v50, v51\nv_smfmac_f32_16x16x64_bf16 v[44:47], v[56:59], v[204:211], v0\n" \
    "ds_read_b128 v[56:59], v3 offset:3072\nbuffer_store_dwordx4 v[48:51], v8, s[64:67], s88 offen offset:0 sc1\nv_mov_b64 v[32:33], 0\n" \
    "v_smfmac_f32_16x16x64_bf16 v[40:43], v[60:63], v[196:203], v0\nds_read_b128 v[72:75], v10 offset:1024\nv_mov_b64 v[34:35], 0\n" \
    "v_smfmac_f32_16x16x64_bf16 v[44:47], v[60:63], v[212:219], v0\nds_read_b128 v[60:63], v10 offset:5120\nv_mov_b64 v[36:37], 0\n" \
    "v_smfmac_f32_16x16x64_bf16 v[40:43], v[64:67], v[220:227], v0\nbuffer_store_dwordx4 v[252:255], v2, s[64:67], s74 offen offset:1024 sc1\nv_mov_b64 v[38:39], 0\n" \
    "v_smfmac_f32_16x16x64_bf16 v[44:47], v[64:67], v[236:243], v0\nv_smfmac_f32_16x16x64_bf16 v[40:43], v[68:71], v[228:235], v0\nds_read_b128 v[64:67], v6 offset:4096\n" \
    "v_smfmac_f32_16x16x64_bf16 v[44:47], v[68:71], v[244:251], v0\ns_nop 1\ns_waitcnt lgkmcnt(1)\nv_mfma_f32_16x16x32_bf16 v[16:19], a[12:15], v[72:75], v[16:19]\n" \
    "ds_read_b128 v[68:71], v6 offset:5120\ns_nop 0\nv_cvt_pk_bf16_f32 v52, v40, v41\nv_mfma_f32_16x16x32_bf16 v[20:23], a[12:15], v[60:63], v[20:23]\n" \
    "v_cvt_pk_bf16_f32 v54, v42, v43\nv_cvt_pk_bf16_f32 v53, v44, v45\nv_mfma_f32_16x16x32_bf16 v[24:27], v[56:59], v[72:75], v[24:27]\nds_read_b128 v[72:75], v6 offset:6144\n" \
    "v_cvt_pk_bf16_f32 v55, v46, v47\nv_permlane16_swap_b32 v52, v53\nv_mfma_f32_16x16x32_bf16 v[28:31], v[56:59], v[60:63], v[28:31]\ns_waitcnt lgkmcnt(2)\n" \
    "v_permlane16_swap_b32 v54, v55\nv_smfmac_f32_16x16x64_bf16 v[32:35], v[64:67], a[160:167], v0\nds_read_b128 v[56:59], v6 offset:7168\n" \
    "buffer_store_dwordx4 v[52:55], v8, s[64:67], s89 offen offset:0 sc1\nv_mov_b64 v[40:41], 0\nv_smfmac_f32_16x16x64_bf16 v[36:39], v[64:67], a[176:183], v0\n" \
    "ds_read_b128 v[60:63], v3 offset:4096\ns_waitcnt lgkmcnt(3)\nv_mov_b64 v[42:43], 0\nv_smfmac_f32_16x16x64_bf16 v[32:35], v[68:71], a[168:175], v0\n" \
    "ds_read_b128 v[76:79], v9 offset:8192\nv_mov_b64 v[44:45], 0\nv_smfmac_f32_16x16x64_bf16 v[36:39], v[68:71], a[184:191], v0\nds_read_b128 v[80:83], v9 offset:12288\n" \
    "s_waitcnt lgkmcnt(4)\nv_mov_b64 v[46:47], 0\nv_smfmac_f32_16x16x64_bf16 v[32:35], v[72:75], a[192:199], v0\nv_smfmac_f32_16x16x64_bf16 v[36:39], v[72:75], a[208:215], v0\n" \
    "s_waitcnt lgkmcnt(3)\nv_smfmac_f32_16x16x64_bf16 v[32:35], v[56:59], a[200:207], v0\nv_smfmac_f32_16x16x64_bf16 v[36:39], v[56:59], a[216:223], v0\ns_nop 1\n" \
    "s_waitcnt lgkmcnt(0)\nv_mfma_f32_16x16x32_bf16 v[16:19], a[16:19], v[76:79], v[16:19]\ns_nop 1\nv_mfma_f32_16x16x32_bf16 v[20:23], a[16:19], v[80:83], v[20:23]\n" \
    "v_cvt_pk_bf16_f32 v48, v32, v33\nv_cvt_pk_bf16_f32 v50, v34, v35\nv_mfma_f32_16x16x32_bf16 v[24:27], v[60:63], v[76:79], v[24:27]\nv_cvt_pk_bf16_f32 v49, v36, v37\n" \
    "v_cvt_pk_bf16_f32 v51, v38, v39\nv_mfma_f32_16x16x32_bf16 v[28:31], v[60:63], v[80:83], v[28:31]\nv_permlane16_swap_b32 v48, v49\n" \
    "v_smfmac_f32_16x16x64_bf16 v[40:43], v[64:67], a[224:231], v0\nv_permlane16_swap_b32 v50, v51\nv_smfmac_f32_16x16x64_bf16 v[44:47], v[64:67], a[240:247], v0\n" \
    "ds_read_b128 v[60:63], v3 offset:5120\nbuffer_store_dwordx4 v[48:51], v8, s[64:67], s86 offen offset:256 sc1\nv_mov_b64 v[32:33], 0\n" \
    "v_smfmac_f32_16x16x64_bf16 v[40:43], v[68:71], a[232:239], v0\nds_read_b128 v[76:79], v10 offset:8192\nv_mov_b64 v[34:35], 0\n" \
    "v_smfmac_f32_16x16x64_bf16 v[44:47], v[68:71], a[248:255], v0\nds_read_b128 v[80:83], v10 offset:12288\nv_mov_b64 v[36:37], 0\n" \
    "v_smfmac_f32_16x16x64_bf16 v[40:43], v[72:75], v[92:99], v0\nv_mov_b64 v[38:39], 0\nv_smfmac_f32_16x16x64_bf16 v[44:47], v[72:75], v[108:115], v0\n" \
    "v_smfmac_f32_16x16x64_bf16 v[40:43], v[56:59], v[100:107], v0\nv_smfmac_f32_16x16x64_bf16 v[44:47], v[56:59], v[116:123], v0\ns_nop 1\ns_waitcnt lgkmcnt(0)\n" \
    "v_mfma_f32_16x16x32_bf16 v[16:19], a[20:23], v[76:79], v[16:19]\ns_nop 1\nv_mfma_f32_16x16x32_bf16 v[20:23], a[20:23], v[80:83], v[20:23]\nv_cvt_pk_bf16_f32 v52, v40, v41\n" \
    "v_cvt_pk_bf16_f32 v54, v42, v43\nv_mfma_f32_16x16x32_bf16 v[24:27], v[60:63], v[76:79], v[24:27]\nv_cvt_pk_bf16_f32 v53, v44, v45\nv_cvt_pk_bf16_f32 v55, v46, v47\n" \
    "v_mfma_f32_16x16x32_bf16 v[28:31], v[60:63], v[80:83], v[28:31]\nv_permlane16_swap_b32 v52, v53\nv_smfmac_f32_16x16x64_bf16 v[32:35], v[64:67], v[124:131], v0\n" \
    "v_permlane16_swap_b32 v54, v55\nv_smfmac_f32_16x16x64_bf16 v[36:39], v[64:67], v[140:147], v0\nds_read_b128 v[60:63], v3 offset:6144\n" \
    "buffer_store_dwordx4 v[52:55], v8, s[64:67], s87 offen offset:256 sc1\nv_mov_b64 v[40:41], 0\nv_smfmac_f32_16x16x64_bf16 v[32:35], v[68:71], v[132:139], v0\n" \
    "ds_read_b128 v[76:79], v9 offset:9216\nv_mov_b64 v[42:43], 0\nv_smfmac_f32_16x16x64_bf16 v[36:39], v[68:71], v[148:155], v0\nds_read_b128 v[80:83], v9 offset:13312\n" \
    "v_mov_b64 v[44:45], 0\nv_smfmac_f32_16x16x64_bf16 v[32:35], v[72:75], v[156:163], v0\nv_mov_b64 v[46:47], 0\nv_smfmac_f32_16x16x64_bf16 v[36:39], v[72:75], v[172:179], v0\n" \
    "v_smfmac_f32_16x16x64_bf16 v[32:35], v[56:59], v[164:171], v0\nv_smfmac_f32_16x16x64_bf16 v[36:39], v[56:59], v[180:187], v0\ns_nop 1\ns_waitcnt lgkmcnt(0)\n" \
    "v_mfma_f32_16x16x32_bf16 v[16:19], a[24:27], v[76:79], v[16:19]\ns_nop 1\nv_mfma_f32_16x16x32_bf16 v[20:23], a[24:27], v[80:83], v[20:23]\nv_cvt_pk_bf16_f32 v48, v32, v33\n" \
    "v_cvt_pk_bf16_f32 v50, v34, v35\nv_mfma_f32_16x16x32_bf16 v[24:27], v[60:63], v[76:79], v[24:27]\nv_cvt_pk_bf16_f32 v49, v36, v37\nv_cvt_pk_bf16_f32 v51, v38, v39\n" \
    "v_mfma_f32_16x16x32_bf16 v[28:31], v[60:63], v[80:83], v[28:31]\nv_permlane16_swap_b32 v48, v49\nv_smfmac_f32_16x16x64_bf16 v[40:43], v[64:67], v[188:195], v0\n" \
    "v_permlane16_swap_b32 v50, v51\nv_smfmac_f32_16x16x64_bf16 v[44:47], v[64:67], v[204:211], v0\nds_read_b128 v[60:63], v3 offset:7168\n" \
    "buffer_store_dwordx4 v[48:51], v8, s[64:67], s88 offen offset:256 sc1\nv_mov_b64 v[32:33], 0\nv_smfmac_f32_16x16x64_bf16 v[40:43], v[68:71], v[196:203], v0\n" \
    "ds_read_b128 v[64:67], v10 offset:9216\nv_mov_b64 v[34:35], 0\nv_smfmac_f32_16x16x64_bf16 v[44:47], v[68:71], v[212:219], v0\nds_read_b128 v[68:71], v10 offset:13312\n" \
    "v_mov_b64 v[36:37], 0\nv_smfmac_f32_16x16x64_bf16 v[40:43], v[72:75], v[220:227], v0\nv_mov_b64 v[38:39], 0\nv_smfmac_f32_16x16x64_bf16 v[44:47], v[72:75], v[236:243], v0\n" \
    "v_smfmac_f32_16x16x64_bf16 v[40:43], v[56:59], v[228:235], v0\nds_read_b128 v[72:75], v6 offset:128\nv_smfmac_f32_16x16x64_bf16 v[44:47], v[56:59], v[244:251], v0\n" \
    "s_nop 1\ns_waitcnt lgkmcnt(1)\nv_mfma_f32_16x16x32_bf16 v[16:19], a[28:31], v[64:67], v[16:19]\nds_read_b128 v[56:59], v6 offset:1152\ns_nop 0\n" \
    "v_cvt_pk_bf16_f32 v52, v40, v41\nv_mfma_f32_16x16x32_bf16 v[20:23], a[28:31], v[68:71], v[20:23]\nv_cvt_pk_bf16_f32 v54, v42, v43\nv_cvt_pk_bf16_f32 v53, v44, v45\n" \
    "v_mfma_f32_16x16x32_bf16 v[24:27], v[60:63], v[64:67], v[24:27]\nds_read_b128 v[64:67], v6 offset:2176\nv_cvt_pk_bf16_f32 v55, v46, v47\nv_permlane16_swap_b32 v52, v53\n" \
    "v_mfma_f32_16x16x32_bf16 v[28:31], v[60:63], v[68:71], v[28:31]\ns_waitcnt lgkmcnt(2)\nv_permlane16_swap_b32 v54, v55\n" \
    "v_smfmac_f32_16x16x64_bf16 v[32:35], v[72:75], a[160:167], v0\nds_read_b128 v[60:63], v6 offset:3200\n" \
    "buffer_store_dwordx4 v[52:55], v8, s[64:67], s89 offen offset:256 sc1\nv_mov_b64 v[40:41], 0\nv_smfmac_f32_16x16x64_bf16 v[36:39], v[72:75], a[176:183], v0\n" \
    "ds_read_b128 v[68:71], v3 offset:8192\ns_waitcnt lgkmcnt(3)\nv_mov_b64 v[42:43], 0\nv_smfmac_f32_16x16x64_bf16 v[32:35], v[56:59], a[168:175], v0\n" \
    "ds_read_b128 v[76:79], v9 offset:16384\nv_mov_b64 v[44:45], 0\nv_smfmac_f32_16x16x64_bf16 v[36:39], v[56:59], a[184:191], v0\nds_read_b128 v[80:83], v9 offset:20480\n" \
    "s_waitcnt lgkmcnt(4)\nv_mov_b64 v[46:47], 0\nv_smfmac_f32_16x16x64_bf16 v[32:35], v[64:67], a[192:199], v0\nv_smfmac_f32_16x16x64_bf16 v[36:39], v[64:67], a[208:215], v0\n" \
    "s_waitcnt lgkmcnt(3)\nv_smfmac_f32_16x16x64_bf16 v[32:35], v[60:63], a[200:207], v0\nv_smfmac_f32_16x16x64_bf16 v[36:39], v[60:63], a[216:223], v0\ns_nop 1\n" \
    "s_waitcnt lgkmcnt(0)\nv_mfma_f32_16x16x32_bf16 v[16:19], a[32:35], v[76:79], v[16:19]\ns_nop 1\nv_mfma_f32_16x16x32_bf16 v[20:23], a[32:35], v[80:83], v[20:23]\n" \
    "v_cvt_pk_bf16_f32 v48, v32, v33\nv_cvt_pk_bf16_f32 v50, v34, v35\nv_mfma_f32_16x16x32_bf16 v[24:27], v[68:71], v[76:79], v[24:27]\nv_cvt_pk_bf16_f32 v49, v36, v37\n" \
    "v_cvt_pk_bf16_f32 v51, v38, v39\nv_mfma_f32_16x16x32_bf16 v[28:31], v[68:71], v[80:83], v[28:31]\nv_permlane16_swap_b32 v48, v49\n" \
    "v_smfmac_f32_16x16x64_bf16 v[40:43], v[72:75], a[224:231], v0\nv_permlane16_swap_b32 v50, v51\nv_smfmac_f32_16x16x64_bf16 v[44:47], v[72:75], a[240:247], v0\n" \
    "ds_read_b128 v[68:71], v3 offset:9216\nbuffer_store_dwordx4 v[48:51], v8, s[64:67], s86 offen offset:512 sc1\nv_mov_b64 v[32:33], 0\n" \
    "v_smfmac_f32_16x16x64_bf16 v[40:43], v[56:59], a[232:239], v0\nds_read_b128 v[76:79], v10 offset:16384\nv_mov_b64 v[34:35], 0\n" \
    "v_smfmac_f32_16x16x64_bf16 v[44:47], v[56:59], a[248:255], v0\nds_read_b128 v[80:83], v10 offset:20480\nv_mov_b64 v[36:37], 0\n" \
    "v_smfmac_f32_16x16x64_bf16 v[40:43], v[64:67], v[92:99], v0\nv_mov_b64 v[38:39], 0\nv_smfmac_f32_16x16x64_bf16 v[44:47], v[64:67], v[108:115], v0\n" \
    "v_smfmac_f32_16x16x64_bf16 v[40:43], v[60:63], v[100:107], v0\nv_smfmac_f32_16x16x64_bf16 v[44:47], v[60:63], v[116:123], v0\ns_nop 1\ns_waitcnt lgkmcnt(0)\n" \
    "v_mfma_f32_16x16x32_bf16 v[16:19], a[36:39], v[76:79], v[16:19]\ns_nop 1\nv_mfma_f32_16x16x32_bf16 v[20:23], a[36:39], v[80:83], v[20:23]\nv_cvt_pk_bf16_f32 v52, v40, v41\n" \
    "v_cvt_pk_bf16_f32 v54, v42, v43\nv_mfma_f32_16x16x32_bf16 v[24:27], v[68:71], v[76:79], v[24:27]\nv_cvt_pk_bf16_f32 v53, v44, v45\nv_cvt_pk_bf16_f32 v55, v46, v47\n" \
    "v_mfma_f32_16x16x32_bf16 v[28:31], v[68:71], v[80:83], v[28:31]\nv_permlane16_swap_b32 v52, v53\nv_smfmac_f32_16x16x64_bf16 v[32:35], v[72:75], v[124:131], v0\n" \
    "v_permlane16_swap_b32 v54, v55\nv_smfmac_f32_16x16x64_bf16 v[36:39], v[72:75], v[140:147], v0\nds_read_b128 v[68:71], v3 offset:10240\n" \
    "buffer_store_dwordx4 v[52:55], v8, s[64:67], s87 offen offset:512 sc1\nv_mov_b64 v[40:41], 0\nv_smfmac_f32_16x16x64_bf16 v[32:35], v[56:59], v[132:139], v0\n" \
    "ds_read_b128 v[76:79], v9 offset:17408\nv_mov_b64 v[42:43], 0\nv_smfmac_f32_16x16x64_bf16 v[36:39], v[56:59], v[148:155], v0\nds_read_b128 v[80:83], v9 offset:21504\n" \
    "v_mov_b64 v[44:45], 0\nv_smfmac_f32_16x16x64_bf16 v[32:35], v[64:67], v[156:163], v0\nv_mov_b64 v[46:47], 0\nv_smfmac_f32_16x16x64_bf16 v[36:39], v[64:67], v[172:179], v0\n" \
    "v_smfmac_f32_16x16x64_bf16 v[32:35], v[60:63], v[164:171], v0\nv_smfmac_f32_16x16x64_bf16 v[36:39], v[60:63], v[180:187], v0\ns_nop 1\ns_waitcnt lgkmcnt(0)\n" \
    "v_mfma_f32_16x16x32_bf16 v[16:19], a[40:43], v[76:79], v[16:19]\ns_nop 1\nv_mfma_f32_16x16x32_bf16 v[20:23], a[40:43], v[80:83], v[20:23]\nv_cvt_pk_bf16_f32 v48, v32, v33\n" \
    "v_cvt_pk_bf16_f32 v50, v34, v35\nv_mfma_f32_16x16x32_bf16 v[24:27], v[68:71], v[76:79], v[24:27]\nv_cvt_pk_bf16_f32 v49, v36, v37\nv_cvt_pk_bf16_f32 v51, v38, v39\n" \
    "v_mfma_f32_16x16x32_bf16 v[28:31], v[68:71], v[80:83], v[28:31]\nv_permlane16_swap_b32 v48, v49\nv_smfmac_f32_16x16x64_bf16 v[40:43], v[72:75], v[188:195], v0\n" \
    "v_permlane16_swap_b32 v50, v51\nv_smfmac_f32_16x16x64_bf16 v[44:47], v[72:75], v[204:211], v0\nds_read_b128 v[68:71], v3 offset:11264\n" \
    "buffer_store_dwordx4 v[48:51], v8, s[64:67], s88 offen offset:512 sc1\nv_mov_b64 v[32:33], 0\nv_smfmac_f32_16x16x64_bf16 v[40:43], v[56:59], v[196:203], v0\n" \
    "ds_read_b128 v[72:75], v10 offset:17408\nv_mov_b64 v[34:35], 0\nv_smfmac_f32_16x16x64_bf16 v[44:47], v[56:59], v[212:219], v0\nds_read_b128 v[56:59], v10 offset:21504\n" \
    "v_mov_b64 v[36:37], 0\nv_smfmac_f32_16x16x64_bf16 v[40:43], v[64:67], v[220:227], v0\nv_mov_b64 v[38:39], 0\nv_smfmac_f32_16x16x64_bf16 v[44:47], v[64:67], v[236:243], v0\n" \
    "v_smfmac_f32_16x16x64_bf16 v[40:43], v[60:63], v[228:235], v0\nds_read_b128 v[64:67], v6 offset:4224\nv_smfmac_f32_16x16x64_bf16 v[44:47], v[60:63], v[244:251], v0\n" \
    "s_nop 1\ns_waitcnt lgkmcnt(1)\nv_mfma_f32_16x16x32_bf16 v[16:19], a[44:47], v[72:75], v[16:19]\nds_read_b128 v[60:63], v6 offset:5248\ns_nop 0\n" \
    "v_cvt_pk_bf16_f32 v52, v40, v41\nv_mfma_f32_16x16x32_bf16 v[20:23], a[44:47], v[56:59], v[20:23]\nv_cvt_pk_bf16_f32 v54, v42, v43\nv_cvt_pk_bf16_f32 v53, v44, v45\n" \
    "v_mfma_f32_16x16x32_bf16 v[24:27], v[68:71], v[72:75], v[24:27]\nds_read_b128 v[72:75], v6 offset:6272\nv_cvt_pk_bf16_f32 v55, v46, v47\nv_permlane16_swap_b32 v52, v53\n" \
    "v_mfma_f32_16x16x32_bf16 v[28:31], v[68:71], v[56:59], v[28:31]\ns_waitcnt lgkmcnt(2)\nv_permlane16_swap_b32 v54, v55\n" \
    "v_smfmac_f32_16x16x64_bf16 v[32:35], v[64:67], a[160:167], v0\nds_read_b128 v[56:59], v6 offset:7296\n" \
    "buffer_store_dwordx4 v[52:55], v8, s[64:67], s89 offen offset:512 sc1\nv_mov_b64 v[40:41], 0\nv_smfmac_f32_16x16x64_bf16 v[36:39], v[64:67], a[176:183], v0\n" \
    "ds_read_b128 v[68:71], v3 offset:12288\ns_waitcnt vmcnt(14)\ns_barrier\ns_waitcnt lgkmcnt(3)\nv_smfmac_f32_16x16x64_bf16 v[32:35], v[60:63], a[168:175], v0\n" \
    "ds_read_b128 v[76:79], v9 offset:40960\nv_mov_b64 v[42:43], 0\nv_smfmac_f32_16x16x64_bf16 v[36:39], v[60:63], a[184:191], v0\nds_read_b128 v[80:83], v9 offset:45056\n" \
    "s_add_u32 m0, s41, 0\ns_waitcnt lgkmcnt(4)\nv_smfmac_f32_16x16x64_bf16 v[32:35], v[72:75], a[192:199], v0\ns_xor_b32 s85, s62, 1536\ns_add_u32 s85, s85, s40\n" \
    "v_mov_b64 v[44:45], 0\nv_smfmac_f32_16x16x64_bf16 v[36:39], v[72:75], a[208:215], v0\nbuffer_load_dwordx4 v11, s[64:67], s85 offen sc1 lds\ns_waitcnt lgkmcnt(3)\n" \
    "v_mov_b64 v[46:47], 0\nv_smfmac_f32_16x16x64_bf16 v[32:35], v[56:59], a[200:207], v0\nv_smfmac_f32_16x16x64_bf16 v[36:39], v[56:59], a[216:223], v0\n" \
    "buffer_load_dwordx4 v12, s[64:67], s85 offen offset:1024 sc1 lds\ns_add_u32 m0, s41, 8192\ns_waitcnt lgkmcnt(0)\n" \
    "v_mfma_f32_16x16x32_bf16 v[16:19], a[48:51], v[76:79], v[16:19]\ns_xor_b32 s85, s62, 1792\ns_add_u32 s85, s85, s40\n" \
    "v_mfma_f32_16x16x32_bf16 v[20:23], a[48:51], v[80:83], v[20:23]\nbuffer_load_dwordx4 v11, s[64:67], s85 offen sc1 lds\nv_cvt_pk_bf16_f32 v48, v32, v33\n" \
    "v_cvt_pk_bf16_f32 v50, v34, v35\nv_mfma_f32_16x16x32_bf16 v[24:27], v[68:71], v[76:79], v[24:27]\nv_cvt_pk_bf16_f32 v49, v36, v37\nv_cvt_pk_bf16_f32 v51, v38, v39\n" \
    "v_mfma_f32_16x16x32_bf16 v[28:31], v[68:71], v[80:83], v[28:31]\nbuffer_load_dwordx4 v12, s[64:67], s85 offen offset:1024 sc1 lds\nv_permlane16_swap_b32 v48, v49\n" \
    "v_smfmac_f32_16x16x64_bf16 v[40:43], v[64:67], a[224:231], v0\nv_permlane16_swap_b32 v50, v51\nv_smfmac_f32_16x16x64_bf16 v[44:47], v[64:67], a[240:247], v0\n" \
    "ds_read_b128 v[68:71], v3 offset:13312\nbuffer_store_dwordx4 v[48:51], v8, s[64:67], s86 offen offset:768 sc1\nv_mov_b64 v[32:33], 0\n" \
    "v_smfmac_f32_16x16x64_bf16 v[40:43], v[60:63], a[232:239], v0\nds_read_b128 v[76:79], v10 offset:40960\nv_mov_b64 v[34:35], 0\n" \
    "v_smfmac_f32_16x16x64_bf16 v[44:47], v[60:63], a[248:255], v0\nds_read_b128 v[80:83], v10 offset:45056\nv_mov_b64 v[36:37], 0\n" \
    "v_smfmac_f32_16x16x64_bf16 v[40:43], v[72:75], v[92:99], v0\nv_mov_b64 v[38:39], 0\nv_smfmac_f32_16x16x64_bf16 v[44:47], v[72:75], v[108:115], v0\n" \
    "v_smfmac_f32_16x16x64_bf16 v[40:43], v[56:59], v[100:107], v0\nv_smfmac_f32_16x16x64_bf16 v[44:47], v[56:59], v[116:123], v0\ns_nop 1\ns_waitcnt lgkmcnt(0)\n" \
    "v_mfma_f32_16x16x32_bf16 v[16:19], a[52:55], v[76:79], v[16:19]\ns_nop 1\nv_mfma_f32_16x16x32_bf16 v[20:23], a[52:55], v[80:83], v[20:23]\nv_cvt_pk_bf16_f32 v52, v40, v41\n" \
    "v_cvt_pk_bf16_f32 v54, v42, v43\nv_mfma_f32_16x16x32_bf16 v[24:27], v[68:71], v[76:79], v[24:27]\nv_cvt_pk_bf16_f32 v53, v44, v45\nv_cvt_pk_bf16_f32 v55, v46, v47\n" \
    "v_mfma_f32_16x16x32_bf16 v[28:31], v[68:71], v[80:83], v[28:31]\nv_permlane16_swap_b32 v52, v53\nv_smfmac_f32_16x16x64_bf16 v[32:35], v[64:67], v[124:131], v0\n" \
    "v_permlane16_swap_b32 v54, v55\nv_smfmac_f32_16x16x64_bf16 v[36:39], v[64:67], v[140:147], v0\nds_read_b128 v[68:71], v3 offset:14336\n" \
    "buffer_store_dwordx4 v[52:55], v8, s[64:67], s87 offen offset:768 sc1\nv_mov_b64 v[40:41], 0\nv_smfmac_f32_16x16x64_bf16 v[32:35], v[60:63], v[132:139], v0\n" \
    "ds_read_b128 v[76:79], v9 offset:41984\nv_mov_b64 v[42:43], 0\nv_smfmac_f32_16x16x64_bf16 v[36:39], v[60:63], v[148:155], v0\nds_read_b128 v[80:83], v9 offset:46080\n" \
    "v_mov_b64 v[44:45], 0\nv_smfmac_f32_16x16x64_bf16 v[32:35], v[72:75], v[156:163], v0\nv_mov_b64 v[46:47], 0\nv_smfmac_f32_16x16x64_bf16 v[36:39], v[72:75], v[172:179], v0\n" \
    "v_smfmac_f32_16x16x64_bf16 v[32:35], v[56:59], v[164:171], v0\nv_smfmac_f32_16x16x64_bf16 v[36:39], v[56:59], v[180:187], v0\ns_nop 1\ns_waitcnt lgkmcnt(0)\n" \
    "v_mfma_f32_16x16x32_bf16 v[16:19], a[56:59], v[76:79], v[16:19]\ns_nop 1\nv_mfma_f32_16x16x32_bf16 v[20:23], a[56:59], v[80:83], v[20:23]\nv_cvt_pk_bf16_f32 v48, v32, v33\n" \
    "v_cvt_pk_bf16_f32 v50, v34, v35\nv_mfma_f32_16x16x32_bf16 v[24:27], v[68:71], v[76:79], v[24:27]\nv_cvt_pk_bf16_f32 v49, v36, v37\nv_cvt_pk_bf16_f32 v51, v38, v39\n" \
    "v_mfma_f32_16x16x32_bf16 v[28:31], v[68:71], v[80:83], v[28:31]\nv_permlane16_swap_b32 v48, v49\nv_smfmac_f32_16x16x64_bf16 v[40:43], v[64:67], v[188:195], v0\n" \
    "v_permlane16_swap_b32 v50, v51\nv_smfmac_f32_16x16x64_bf16 v[44:47], v[64:67], v[204:211], v0\nds_read_b128 v[64:67], v3 offset:15360\n" \
    "buffer_store_dwordx4 v[48:51], v8, s[64:67], s88 offen offset:768 sc1\nv_smfmac_f32_16x16x64_bf16 v[40:43], v[60:63], v[196:203], v0\n" \
    "ds_read_b128 v[68:71], v10 offset:41984\nv_smfmac_f32_16x16x64_bf16 v[44:47], v[60:63], v[212:219], v0\nds_read_b128 v[60:63], v10 offset:46080\n" \
    "v_smfmac_f32_16x16x64_bf16 v[40:43], v[72:75], v[220:227], v0\nv_smfmac_f32_16x16x64_bf16 v[44:47], v[72:75], v[236:243], v0\nds_read_b128 v[72:75], v3 offset:16384\n" \
    "v_smfmac_f32_16x16x64_bf16 v[40:43], v[56:59], v[228:235], v0\nds_read_b128 v[76:79], v9 offset:24576\nv_smfmac_f32_16x16x64_bf16 v[44:47], v[56:59], v[244:251], v0\n" \
    "ds_read_b128 v[56:59], v9 offset:28672\ns_nop 0\ns_waitcnt lgkmcnt(3)\nv_mfma_f32_16x16x32_bf16 v[16:19], a[60:63], v[68:71], v[16:19]\ns_nop 1\n" \
    "v_cvt_pk_bf16_f32 v52, v40, v41\nv_cvt_pk_bf16_f32 v54, v42, v43\nv_mfma_f32_16x16x32_bf16 v[20:23], a[60:63], v[60:63], v[20:23]\nds_read_b128 v[80:83], v3 offset:17408\n" \
    "v_cvt_pk_bf16_f32 v53, v44, v45\nv_cvt_pk_bf16_f32 v55, v46, v47\nv_mfma_f32_16x16x32_bf16 v[24:27], v[64:67], v[68:71], v[24:27]\nds_read_b128 v[68:71], v10 offset:24576\n" \
    "v_permlane16_swap_b32 v52, v53\nv_mfma_f32_16x16x32_bf16 v[28:31], v[64:67], v[60:63], v[28:31]\nds_read_b128 v[60:63], v10 offset:28672\ns_waitcnt lgkmcnt(3)\n" \
    "v_permlane16_swap_b32 v54, v55\nv_mfma_f32_16x16x32_bf16 v[16:19], a[64:67], v[76:79], v[16:19]\nbuffer_store_dwordx4 v[52:55], v8, s[64:67], s89 offen offset:768 sc1\n" \
    "v_mfma_f32_16x16x32_bf16 v[20:23], a[64:67], v[56:59], v[20:23]\nds_read_b128 v[64:67], v3 offset:18432\nv_mfma_f32_16x16x32_bf16 v[24:27], v[72:75], v[76:79], v[24:27]\n" \
    "ds_read_b128 v[76:79], v9 offset:25600\nv_mfma_f32_16x16x32_bf16 v[28:31], v[72:75], v[56:59], v[28:31]\nds_read_b128 v[56:59], v9 offset:29696\ns_waitcnt lgkmcnt(3)\n" \
    "v_mfma_f32_16x16x32_bf16 v[16:19], a[68:71], v[68:71], v[16:19]\nbuffer_store_dwordx4 v[252:255], v7, s[64:67], s82 offen sc1\ns_add_u32 s82, s82, 0x10000\n" \
    "v_mfma_f32_16x16x32_bf16 v[20:23], a[68:71], v[60:63], v[20:23]\nds_read_b128 v[72:75], v3 offset:19456\nv_mfma_f32_16x16x32_bf16 v[24:27], v[80:83], v[68:71], v[24:27]\n" \
    "ds_read_b128 v[68:71], v10 offset:25600\nv_mfma_f32_16x16x32_bf16 v[28:31], v[80:83], v[60:63], v[28:31]\nds_read_b128 v[60:63], v10 offset:29696\ns_waitcnt lgkmcnt(3)\n" \
    "v_mfma_f32_16x16x32_bf16 v[16:19], a[72:75], v[76:79], v[16:19]\nv_mfma_f32_16x16x32_bf16 v[20:23], a[72:75], v[56:59], v[20:23]\nds_read_b128 v[80:83], v3 offset:20480\n" \
    "v_mfma_f32_16x16x32_bf16 v[24:27], v[64:67], v[76:79], v[24:27]\nds_read_b128 v[76:79], v9 offset:32768\nbuffer_store_dwordx4 v[252:255], v7, s[64:67], s82 offen sc1\n" \
    "v_mfma_f32_16x16x32_bf16 v[28:31], v[64:67], v[56:59], v[28:31]\nds_read_b128 v[56:59], v9 offset:36864\ns_add_u32 s82, s82, 0x10000\ns_waitcnt lgkmcnt(3)\n" \
    "v_mfma_f32_16x16x32_bf16 v[16:19], a[76:79], v[68:71], v[16:19]\nv_mfma_f32_16x16x32_bf16 v[20:23], a[76:79], v[60:63], v[20:23]\nds_read_b128 v[64:67], v3 offset:21504\n" \
    "v_mfma_f32_16x16x32_bf16 v[24:27], v[72:75], v[68:71], v[24:27]\nds_read_b128 v[68:71], v10 offset:32768\nv_mfma_f32_16x16x32_bf16 v[28:31], v[72:75], v[60:63], v[28:31]\n" \
    "ds_read_b128 v[60:63], v10 offset:36864\ns_waitcnt lgkmcnt(3)\nv_mfma_f32_16x16x32_bf16 v[16:19], a[80:83], v[76:79], v[16:19]\n" \
    "v_mfma_f32_16x16x32_bf16 v[20:23], a[80:83], v[56:59], v[20:23]\nds_read_b128 v[72:75], v3 offset:22528\nv_mfma_f32_16x16x32_bf16 v[24:27], v[80:83], v[76:79], v[24:27]\n" \
    "ds_read_b128 v[76:79], v9 offset:33792\nv_mfma_f32_16x16x32_bf16 v[28:31], v[80:83], v[56:59], v[28:31]\nds_read_b128 v[56:59], v9 offset:37888\ns_waitcnt lgkmcnt(3)\n" \
    "v_mfma_f32_16x16x32_bf16 v[16:19], a[84:87], v[68:71], v[16:19]\nv_mfma_f32_16x16x32_bf16 v[20:23], a[84:87], v[60:63], v[20:23]\nds_read_b128 v[80:83], v3 offset:23552\n" \
    "v_mfma_f32_16x16x32_bf16 v[24:27], v[64:67], v[68:71], v[24:27]\nds_read_b128 v[68:71], v10 offset:33792\nv_mfma_f32_16x16x32_bf16 v[28:31], v[64:67], v[60:63], v[28:31]\n" \
    "ds_read_b128 v[60:63], v10 offset:37888\ns_waitcnt lgkmcnt(3)\nv_mfma_f32_16x16x32_bf16 v[16:19], a[88:91], v[76:79], v[16:19]\n" \
    "buffer_load_dword v13, off, s[64:67], s42 sc1\nv_mfma_f32_16x16x32_bf16 v[20:23], a[88:91], v[56:59], v[20:23]\ns_waitcnt vmcnt(7)\ns_barrier\n" \
    "v_mfma_f32_16x16x32_bf16 v[24:27], v[72:75], v[76:79], v[24:27]\nds_read_b128 v[64:67], v9 offset:0\nv_mfma_f32_16x16x32_bf16 v[28:31], v[72:75], v[56:59], v[28:31]\n" \
    "ds_read_b128 v[56:59], v9 offset:4096\ns_add_u32 m0, s41, 16384\ns_waitcnt lgkmcnt(2)\nv_mfma_f32_16x16x32_bf16 v[16:19], a[92:95], v[68:71], v[16:19]\n" \
    "s_xor_b32 s85, s62, 512\ns_add_u32 s85, s85, s84\nv_mfma_f32_16x16x32_bf16 v[20:23], a[92:95], v[60:63], v[20:23]\nbuffer_load_dwordx4 v11, s[64:67], s85 offen sc1 lds\n" \
    "v_mfma_f32_16x16x32_bf16 v[24:27], v[80:83], v[68:71], v[24:27]\nds_read_b128 v[68:71], v10 offset:0\nbuffer_store_dwordx4 v[252:255], v7, s[64:67], s82 offen sc1\n" \
    "v_mfma_f32_16x16x32_bf16 v[28:31], v[80:83], v[60:63], v[28:31]\nds_read_b128 v[60:63], v10 offset:4096\nbuffer_load_dwordx4 v12, s[64:67], s85 offen offset:1024 sc1 lds\n" \
    "s_waitcnt lgkmcnt(2)\nv_mfma_f32_16x16x32_bf16 v[16:19], a[96:99], v[64:67], v[16:19]\ns_add_u32 m0, s41, 40960\ns_xor_b32 s85, s62, 768\n" \
    "v_mfma_f32_16x16x32_bf16 v[20:23], a[96:99], v[56:59], v[20:23]\ns_add_u32 s85, s85, s84\nbuffer_load_dwordx4 v11, s[64:67], s85 offen sc1 lds\n" \
    "v_mfma_f32_16x16x32_bf16 v[24:27], a[104:107], v[64:67], v[24:27]\nds_read_b128 v[64:67], v9 offset:1024\ns_add_u32 s82, s82, 0x10000\n" \
    "v_mfma_f32_16x16x32_bf16 v[28:31], a[104:107], v[56:59], v[28:31]\nds_read_b128 v[56:59], v9 offset:5120\nbuffer_load_dwordx4 v12, s[64:67], s85 offen offset:1024 sc1 lds\n" \
    "s_waitcnt lgkmcnt(2)\nv_mfma_f32_16x16x32_bf16 v[16:19], a[100:103], v[68:71], v[16:19]\nv_mfma_f32_16x16x32_bf16 v[20:23], a[100:103], v[60:63], v[20:23]\n" \
    "v_mfma_f32_16x16x32_bf16 v[24:27], a[108:111], v[68:71], v[24:27]\nds_read_b128 v[68:71], v10 offset:1024\n" \
    "v_mfma_f32_16x16x32_bf16 v[28:31], a[108:111], v[60:63], v[28:31]\nds_read_b128 v[60:63], v10 offset:5120\ns_waitcnt lgkmcnt(2)\n" \
    "v_mfma_f32_16x16x32_bf16 v[16:19], a[112:115], v[64:67], v[16:19]\nv_mfma_f32_16x16x32_bf16 v[20:23], a[112:115], v[56:59], v[20:23]\n" \
    "v_mfma_f32_16x16x32_bf16 v[24:27], a[120:123], v[64:67], v[24:27]\nds_read_b128 v[64:67], v9 offset:8192\n" \
    "v_mfma_f32_16x16x32_bf16 v[28:31], a[120:123], v[56:59], v[28:31]\nds_read_b128 v[56:59], v9 offset:12288\ns_waitcnt lgkmcnt(2)\n" \
    "v_mfma_f32_16x16x32_bf16 v[16:19], a[116:119], v[68:71], v[16:19]\nv_mfma_f32_16x16x32_bf16 v[20:23], a[116:119], v[60:63], v[20:23]\n" \
    "v_mfma_f32_16x16x32_bf16 v[24:27], a[124:127], v[68:71], v[24:27]\nds_read_b128 v[68:71], v10 offset:8192\n" \
    "v_mfma_f32_16x16x32_bf16 v[28:31], a[124:127], v[60:63], v[28:31]\nds_read_b128 v[60:63], v10 offset:12288\ns_waitcnt lgkmcnt(2)\n" \
    "v_mfma_f32_16x16x32_bf16 v[16:19], a[128:131], v[64:67], v[16:19]\ns_waitcnt vmcnt(8)\nv_mov_b32 v15, s50\ns_mov_b64 exec, 1\n" \
    "buffer_store_dword v15, off, s[64:67], s48 sc1\ns_mov_b64 exec, -1\nv_mfma_f32_16x16x32_bf16 v[20:23], a[128:131], v[56:59], v[20:23]\n" \
    "v_mfma_f32_16x16x32_bf16 v[24:27], a[136:139], v[64:67], v[24:27]\nds_read_b128 v[64:67], v9 offset:9216\n" \
    "v_mfma_f32_16x16x32_bf16 v[28:31], a[136:139], v[56:59], v[28:31]\nds_read_b128 v[56:59], v9 offset:13312\ns_waitcnt lgkmcnt(2)\n" \
    "v_mfma_f32_16x16x32_bf16 v[16:19], a[132:135], v[68:71], v[16:19]\nv_mfma_f32_16x16x32_bf16 v[20:23], a[132:135], v[60:63], v[20:23]\n" \
    "v_mfma_f32_16x16x32_bf16 v[24:27], a[140:143], v[68:71], v[24:27]\nds_read_b128 v[68:71], v10 offset:9216\nbuffer_store_dwordx4 v[252:255], v7, s[64:67], s82 offen sc1\n" \
    "v_mfma_f32_16x16x32_bf16 v[28:31], a[140:143], v[60:63], v[28:31]\nds_read_b128 v[60:63], v10 offset:13312\ns_add_u32 s82, s82, 0x10000\ns_waitcnt lgkmcnt(2)\n" \
    "v_mfma_f32_16x16x32_bf16 v[16:19], a[144:147], v[64:67], v[16:19]\nv_mfma_f32_16x16x32_bf16 v[20:23], a[144:147], v[56:59], v[20:23]\n" \
    "v_mfma_f32_16x16x32_bf16 v[24:27], a[152:155], v[64:67], v[24:27]\nv_mfma_f32_16x16x32_bf16 v[28:31], a[152:155], v[56:59], v[28:31]\ns_waitcnt lgkmcnt(0)\n" \
    "v_mfma_f32_16x16x32_bf16 v[16:19], a[148:151], v[68:71], v[16:19]\nv_mfma_f32_16x16x32_bf16 v[20:23], a[148:151], v[60:63], v[20:23]\n" \
    "v_mfma_f32_16x16x32_bf16 v[24:27], a[156:159], v[68:71], v[24:27]\nv_mfma_f32_16x16x32_bf16 v[28:31], a[156:159], v[60:63], v[28:31]\ns_barrier\ns_add_u32 m0, s41, 0\n" \
    "s_xor_b32 s85, s62, 0\ns_add_u32 s85, s85, s84\nbuffer_load_dwordx4 v11, s[64:67], s85 offen sc1 lds\nv_cvt_pk_bf16_f32 v56, v16, v17\nv_cvt_pk_bf16_f32 v57, v18, v19\n" \
    "v_cvt_pk_bf16_f32 v58, v24, v25\nv_cvt_pk_bf16_f32 v59, v26, v27\nds_write_b128 v14, v[56:59] offset:0\nv_cvt_pk_bf16_f32 v60, v20, v21\nv_cvt_pk_bf16_f32 v61, v22, v23\n" \
    "v_cvt_pk_bf16_f32 v62, v28, v29\nv_cvt_pk_bf16_f32 v63, v30, v31\nds_write_b128 v14, v[60:63] offset:4096\n" \
    "buffer_load_dwordx4 v12, s[64:67], s85 offen offset:1024 sc1 lds\ns_add_u32 m0, s41, 8192\ns_xor_b32 s85, s62, 256\ns_add_u32 s85, s85, s84\n" \
    "buffer_load_dwordx4 v11, s[64:67], s85 offen sc1 lds\nbuffer_load_dwordx4 v12, s[64:67], s85 offen offset:1024 sc1 lds\ns_mov_b32 s40, s84\ns_add_u32 s49, s49, 2\n" \
    ".Lv4_%=:\ns_and_b32 s69, s35, 15\ns_lshl_b32 s69, s69, 19\ns_add_u32 s86, s38, s69\ns_lshr_b32 s69, s86, 8\ns_and_b32 s69, s69, 0x2000\ns_xor_b32 s86, s86, s69\n" \
    "s_add_u32 s87, s86, 0x10000\ns_add_u32 s88, s86, 0x20000\ns_add_u32 s89, s86, 0x30000\ns_and_b32 s69, s35, 15\ns_lshl_b32 s69, s69, 17\ns_add_u32 s81, s37, s69\n" \
    "s_lshr_b32 s69, s81, 8\ns_and_b32 s69, s69, 0x2000\ns_xor_b32 s81, s81, s69\ns_mov_b32 s68, 0\ns_lshr_b32 s69, s35, 1\ns_cmp_eq_u32 s69, 5\ns_cbranch_scc1 .Lv16_%=\n" \
    ".Lv17_%=:\ns_cmp_eq_u32 s35, 0\ns_cbranch_scc1 .Lv18_%=\ns_waitcnt vmcnt(11)\n.Lv19_%=:\nv_readfirstlane_b32 s74, v13\ns_nop 0\ns_and_b32 s75, s74, 127\n" \
    "s_cmp_lt_u32 s75, s35\ns_cbranch_scc1 .Lv15_%=\ns_memrealtime s[72:73]\ns_waitcnt lgkmcnt(0)\ns_lshr_b32 s69, s74, 8\ns_sub_u32 s69, s69, s72\n" \
    "s_bfe_i32 s69, s69, 0x180000\ns_add_u32 s80, s72, s69\ns_sub_u32 s76, s80, s45\ns_sub_u32 s69, s72, s76\ns_cmp_ge_i32 s69, -8\ns_cbranch_scc1 .Lv13_%=\n.Lv20_%=:\n" \
    "s_memrealtime s[72:73]\ns_waitcnt lgkmcnt(0)\ns_sub_u32 s72, s72, s76\ns_cmp_lt_i32 s72, 0\ns_cbranch_scc1 .Lv20_%=\ns_branch .Lv12_%=\n.Lv13_%=:\ns_lshl_b32 s74, s74, 5\n" \
    "s_and_b32 s74, s74, 0x1000\ns_xor_b32 s81, s81, s74\n.Lv14_%=:\ns_mov_b32 s68, 0\ns_cmp_eq_u32 s36, 1\ns_cbranch_scc1 .Lv9_%=\ns_cmp_eq_u32 s36, 2\n" \
    "s_cbranch_scc1 .Lv10_%=\ns_cmp_eq_u32 s36, 3\ns_cbranch_scc1 .Lv11_%=\n.Lv8_%=:\nds_read_b128 v[24:27], v3 offset:0\nds_read_b128 v[28:31], v3 offset:1024\n" \
    "ds_read_b128 v[32:35], v3 offset:8192\nds_read_b128 v[36:39], v3 offset:9216\nds_read_b128 v[40:43], v3 offset:16384\nds_read_b128 v[44:47], v3 offset:17408\n" \
    "ds_read_b128 v[48:51], v3 offset:2048\nds_read_b128 v[52:55], v3 offset:3072\nds_read_b128 v[56:59], v3 offset:4096\nds_read_b128 v[60:63], v3 offset:5120\n" \
    "ds_read_b128 v[64:67], v3 offset:6144\nds_read_b128 v[68:71], v3 offset:7168\n.Lv21_%=:\ns_xor_b32 s90, s81, 0x800\n.Lv23_%=:\ns_memrealtime s[72:73]\n" \
    "s_waitcnt lgkmcnt(0)\ns_sub_u32 s72, s72, s80\ns_cmp_lt_i32 s72, 0\ns_cbranch_scc1 .Lv23_%=\nbuffer_load_dwordx4 v[72:75], v2, s[64:67], s81 offen offset:0 sc1\n" \
    "buffer_load_dwordx4 v[76:79], v2, s[64:67], s81 offen offset:1024 sc1\nbuffer_load_dwordx4 v[80:83], v2, s[64:67], s90 offen offset:0 sc1\n" \
    "buffer_load_dwordx4 v[84:87], v2, s[64:67], s90 offen offset:1024 sc1\nv_mov_b64 v[16:17], 0\nv_mov_b64 v[18:19], 0\nv_mov_b64 v[20:21], 0\nv_mov_b64 v[22:23], 0\n" \
    "s_waitcnt vmcnt(3)\nds_write_b128 v1, v[72:75] offset:24576\nv_smfmac_f32_16x16x64_bf16 v[20:23], v[72:75], v[24:31], v0\nv_max3_u32 v252, v72, v73, v74\n" \
    "v_smfmac_f32_16x16x64_bf16 v[16:19], v[72:75], a[0:7], v0\ns_waitcnt vmcnt(2)\nds_write_b128 v1, v[76:79] offset:28672\nv_max3_u32 v252, v252, v75, v76\n" \
    "v_max3_u32 v253, v77, v78, v79\nv_smfmac_f32_16x16x64_bf16 v[20:23], v[76:79], v[32:39], v0\ns_waitcnt vmcnt(1)\nds_write_b128 v1, v[80:83] offset:32768\n" \
    "v_max3_u32 v252, v252, v80, v81\nv_max3_u32 v253, v253, v82, v83\nv_smfmac_f32_16x16x64_bf16 v[16:19], v[76:79], a[32:39], v0\ns_waitcnt vmcnt(0)\n" \
    "ds_write_b128 v1, v[84:87] offset:36864\nv_max3_u32 v252, v252, v84, v85\nv_max3_u32 v253, v253, v86, v87\nv_max_u32 v252, v252, v253\nv_cmp_eq_u32 vcc, -1, v252\ns_nop 0\n" \
    "s_cbranch_vccnz .Lv22_%=\ns_waitcnt lgkmcnt(0)\ns_barrier\nds_read_b128 v[88:91], v1 offset:25600\nds_read_b128 v[24:27], v1 offset:26624\n" \
    "ds_read_b128 v[28:31], v1 offset:27648\nds_read_b128 v[32:35], v1 offset:29696\nds_read_b128 v[72:75], v3 offset:10240\nds_read_b128 v[76:79], v3 offset:11264\n" \
    "v_smfmac_f32_16x16x64_bf16 v[20:23], v[80:83], v[40:47], v0\nds_read_b128 v[36:39], v1 offset:30720\nv_smfmac_f32_16x16x64_bf16 v[16:19], v[80:83], a[64:71], v0\n" \
    "ds_read_b128 v[40:43], v3 offset:12288\nv_smfmac_f32_16x16x64_bf16 v[20:23], v[84:87], a[104:111], v0\nds_read_b128 v[44:47], v3 offset:13312\n" \
    "v_smfmac_f32_16x16x64_bf16 v[16:19], v[84:87], a[96:103], v0\nds_read_b128 v[80:83], v1 offset:31744\ns_waitcnt lgkmcnt(9)\n" \
    "v_smfmac_f32_16x16x64_bf16 v[20:23], v[88:91], v[48:55], v0\nds_read_b128 v[48:51], v3 offset:14336\nv_smfmac_f32_16x16x64_bf16 v[16:19], v[88:91], a[8:15], v0\n" \
    "ds_read_b128 v[52:55], v3 offset:15360\ns_waitcnt lgkmcnt(10)\nv_smfmac_f32_16x16x64_bf16 v[20:23], v[24:27], v[56:63], v0\nds_read_b128 v[56:59], v1 offset:33792\n" \
    "v_smfmac_f32_16x16x64_bf16 v[16:19], v[24:27], a[16:23], v0\nds_read_b128 v[84:87], v3 offset:18432\ns_waitcnt lgkmcnt(11)\n" \
    "v_smfmac_f32_16x16x64_bf16 v[20:23], v[28:31], v[64:71], v0\nds_read_b128 v[88:91], v3 offset:19456\nv_smfmac_f32_16x16x64_bf16 v[16:19], v[28:31], a[24:31], v0\n" \
    "ds_read_b128 v[24:27], v1 offset:34816\ns_waitcnt lgkmcnt(10)\nv_smfmac_f32_16x16x64_bf16 v[20:23], v[32:35], v[72:79], v0\nds_read_b128 v[60:63], v3 offset:20480\n" \
    "v_smfmac_f32_16x16x64_bf16 v[16:19], v[32:35], a[40:47], v0\nds_read_b128 v[64:67], v3 offset:21504\ns_waitcnt lgkmcnt(9)\n" \
    "v_smfmac_f32_16x16x64_bf16 v[20:23], v[36:39], v[40:47], v0\nds_read_b128 v[28:31], v1 offset:35840\nv_smfmac_f32_16x16x64_bf16 v[16:19], v[36:39], a[48:55], v0\n" \
    "ds_read_b128 v[32:35], v3 offset:22528\ns_waitcnt lgkmcnt(8)\nv_smfmac_f32_16x16x64_bf16 v[20:23], v[80:83], v[48:55], v0\nds_read_b128 v[36:39], v3 offset:23552\n" \
    "v_smfmac_f32_16x16x64_bf16 v[16:19], v[80:83], a[56:63], v0\nds_read_b128 v[40:43], v1 offset:37888\ns_waitcnt lgkmcnt(7)\n" \
    "v_smfmac_f32_16x16x64_bf16 v[20:23], v[56:59], v[84:91], v0\nds_read_b128 v[44:47], v1 offset:38912\ns_waitcnt lgkmcnt(5)\n" \
    "v_smfmac_f32_16x16x64_bf16 v[20:23], v[24:27], v[60:67], v0\nds_read_b128 v[48:51], v1 offset:39936\ns_waitcnt lgkmcnt(3)\n" \
    "v_smfmac_f32_16x16x64_bf16 v[20:23], v[28:31], v[32:39], v0\ns_waitcnt lgkmcnt(2)\nv_smfmac_f32_16x16x64_bf16 v[20:23], v[40:43], a[120:127], v0\ns_waitcnt lgkmcnt(1)\n" \
    "v_smfmac_f32_16x16x64_bf16 v[20:23], v[44:47], a[136:143], v0\ns_waitcnt lgkmcnt(0)\nv_smfmac_f32_16x16x64_bf16 v[20:23], v[48:51], a[152:159], v0\ns_nop 2\n" \
    "v_smfmac_f32_16x16x64_bf16 v[16:19], v[56:59], a[72:79], v0\ns_nop 2\nv_smfmac_f32_16x16x64_bf16 v[16:19], v[24:27], a[80:87], v0\nv_permlane32_swap_b32 v20, v22\n" \
    "v_smfmac_f32_16x16x64_bf16 v[16:19], v[28:31], a[88:95], v0\nv_permlane32_swap_b32 v21, v23\nv_smfmac_f32_16x16x64_bf16 v[16:19], v[40:43], a[112:119], v0\n" \
    "v_add_f32 v20, v20, v22\nv_smfmac_f32_16x16x64_bf16 v[16:19], v[44:47], a[128:135], v0\nv_add_f32 v21, v21, v23\n" \
    "v_smfmac_f32_16x16x64_bf16 v[16:19], v[48:51], a[144:151], v0\nv_cvt_pk_bf16_f32 v20, v20, v21\nds_write_b16 v4, v20 offset:8\nds_write_b16_d16_hi v4, v20 offset:24\n" \
    "v_mov_b32 v15, v15\nv_mov_b32 v15, v15\nv_mov_b32 v15, v15\nv_mov_b32 v15, v15\nv_mov_b32 v15, v15\nv_permlane32_swap_b32 v16, v18\nv_permlane32_swap_b32 v17, v19\n" \
    "v_pk_add_f32 v[16:17], v[16:17], v[18:19]\nv_cvt_pk_bf16_f32 v16, v16, v17\nds_write_b16 v4, v16 offset:0\nds_write_b16_d16_hi v4, v16 offset:16\nv_mov_b64 v[30:31], 0\n" \
    "v_mov_b64 v[28:29], 0\nv_mov_b64 v[26:27], 0\nv_mov_b64 v[24:25], 0\nv_mov_b64 v[70:71], 0\nv_mov_b64 v[68:69], 0\nv_mov_b64 v[66:67], 0\nv_mov_b64 v[64:65], 0\n" \
    "v_mov_b64 v[22:23], 0\nv_mov_b64 v[20:21], 0\nv_mov_b64 v[18:19], 0\nv_mov_b32 v15, v15\ns_waitcnt lgkmcnt(0)\ns_barrier\nv_mov_b64 v[16:17], 0\ns_branch .Lv7_%=\n" \
    ".Lv9_%=:\nds_read_b128 v[24:27], v3 offset:2048\nds_read_b128 v[28:31], v3 offset:3072\nds_read_b128 v[32:35], v3 offset:10240\nds_read_b128 v[36:39], v3 offset:11264\n" \
    "ds_read_b128 v[40:43], v3 offset:18432\nds_read_b128 v[44:47], v3 offset:19456\nds_read_b128 v[48:51], v3 offset:0\nds_read_b128 v[52:55], v3 offset:1024\n" \
    "ds_read_b128 v[56:59], v3 offset:4096\nds_read_b128 v[60:63], v3 offset:5120\nds_read_b128 v[64:67], v3 offset:6144\nds_read_b128 v[68:71], v3 offset:7168\n.Lv24_%=:\n" \
    "s_xor_b32 s90, s81, 0x800\n.Lv26_%=:\ns_memrealtime s[72:73]\ns_waitcnt lgkmcnt(0)\ns_sub_u32 s72, s72, s80\ns_cmp_lt_i32 s72, 0\ns_cbranch_scc1 .Lv26_%=\n" \
    "buffer_load_dwordx4 v[72:75], v2, s[64:67], s81 offen offset:0 sc1\nbuffer_load_dwordx4 v[76:79], v2, s[64:67], s81 offen offset:1024 sc1\n" \
    "buffer_load_dwordx4 v[80:83], v2, s[64:67], s90 offen offset:0 sc1\nbuffer_load_dwordx4 v[84:87], v2, s[64:67], s90 offen offset:1024 sc1\nv_mov_b64 v[16:17], 0\n" \
    "v_mov_b64 v[18:19], 0\nv_mov_b64 v[20:21], 0\nv_mov_b64 v[22:23], 0\ns_waitcnt vmcnt(3)\nds_write_b128 v1, v[72:75] offset:25600\n" \
    "v_smfmac_f32_16x16x64_bf16 v[20:23], v[72:75], v[24:31], v0\nv_max3_u32 v252, v72, v73, v74\nv_smfmac_f32_16x16x64_bf16 v[16:19], v[72:75], a[8:15], v0\n" \
    "s_waitcnt vmcnt(2)\nds_write_b128 v1, v[76:79] offset:29696\nv_max3_u32 v252, v252, v75, v76\nv_max3_u32 v253, v77, v78, v79\n" \
    "v_smfmac_f32_16x16x64_bf16 v[20:23], v[76:79], v[32:39], v0\ns_waitcnt vmcnt(1)\nds_write_b128 v1, v[80:83] offset:33792\nv_max3_u32 v252, v252, v80, v81\n" \
    "v_max3_u32 v253, v253, v82, v83\nv_smfmac_f32_16x16x64_bf16 v[16:19], v[76:79], a[40:47], v0\ns_waitcnt vmcnt(0)\nds_write_b128 v1, v[84:87] offset:37888\n" \
    "v_max3_u32 v252, v252, v84, v85\nv_max3_u32 v253, v253, v86, v87\nv_max_u32 v252, v252, v253\nv_cmp_eq_u32 vcc, -1, v252\ns_nop 0\ns_cbranch_vccnz .Lv25_%=\n" \
    "s_waitcnt lgkmcnt(0)\ns_barrier\nds_read_b128 v[88:91], v1 offset:24576\nds_read_b128 v[24:27], v1 offset:26624\nds_read_b128 v[28:31], v1 offset:27648\n" \
    "ds_read_b128 v[32:35], v1 offset:28672\nds_read_b128 v[72:75], v3 offset:8192\nds_read_b128 v[76:79], v3 offset:9216\n" \
    "v_smfmac_f32_16x16x64_bf16 v[20:23], v[80:83], v[40:47], v0\nds_read_b128 v[36:39], v1 offset:30720\nv_smfmac_f32_16x16x64_bf16 v[16:19], v[80:83], a[72:79], v0\n" \
    "ds_read_b128 v[40:43], v3 offset:12288\nv_smfmac_f32_16x16x64_bf16 v[20:23], v[84:87], a[120:127], v0\nds_read_b128 v[44:47], v3 offset:13312\n" \
    "v_smfmac_f32_16x16x64_bf16 v[16:19], v[84:87], a[112:119], v0\nds_read_b128 v[80:83], v1 offset:31744\ns_waitcnt lgkmcnt(9)\n" \
    "v_smfmac_f32_16x16x64_bf16 v[20:23], v[88:91], v[48:55], v0\nds_read_b128 v[48:51], v3 offset:14336\nv_smfmac_f32_16x16x64_bf16 v[16:19], v[88:91], a[0:7], v0\n" \
    "ds_read_b128 v[52:55], v3 offset:15360\ns_waitcnt lgkmcnt(10)\nv_smfmac_f32_16x16x64_bf16 v[20:23], v[24:27], v[56:63], v0\nds_read_b128 v[56:59], v1 offset:32768\n" \
    "v_smfmac_f32_16x16x64_bf16 v[16:19], v[24:27], a[16:23], v0\nds_read_b128 v[84:87], v3 offset:16384\ns_waitcnt lgkmcnt(11)\n" \
    "v_smfmac_f32_16x16x64_bf16 v[20:23], v[28:31], v[64:71], v0\nds_read_b128 v[88:91], v3 offset:17408\nv_smfmac_f32_16x16x64_bf16 v[16:19], v[28:31], a[24:31], v0\n" \
    "ds_read_b128 v[24:27], v1 offset:34816\ns_waitcnt lgkmcnt(10)\nv_smfmac_f32_16x16x64_bf16 v[20:23], v[32:35], v[72:79], v0\nds_read_b128 v[60:63], v3 offset:20480\n" \
    "v_smfmac_f32_16x16x64_bf16 v[16:19], v[32:35], a[32:39], v0\nds_read_b128 v[64:67], v3 offset:21504\ns_waitcnt lgkmcnt(9)\n" \
    "v_smfmac_f32_16x16x64_bf16 v[20:23], v[36:39], v[40:47], v0\nds_read_b128 v[28:31], v1 offset:35840\nv_smfmac_f32_16x16x64_bf16 v[16:19], v[36:39], a[48:55], v0\n" \
    "ds_read_b128 v[32:35], v3 offset:22528\ns_waitcnt lgkmcnt(8)\nv_smfmac_f32_16x16x64_bf16 v[20:23], v[80:83], v[48:55], v0\nds_read_b128 v[36:39], v3 offset:23552\n" \
    "v_smfmac_f32_16x16x64_bf16 v[16:19], v[80:83], a[56:63], v0\nds_read_b128 v[40:43], v1 offset:36864\ns_waitcnt lgkmcnt(7)\n" \
    "v_smfmac_f32_16x16x64_bf16 v[20:23], v[56:59], v[84:91], v0\nds_read_b128 v[44:47], v1 offset:38912\ns_waitcnt lgkmcnt(5)\n" \
    "v_smfmac_f32_16x16x64_bf16 v[20:23], v[24:27], v[60:67], v0\nds_read_b128 v[48:51], v1 offset:39936\ns_waitcnt lgkmcnt(3)\n" \
    "v_smfmac_f32_16x16x64_bf16 v[20:23], v[28:31], v[32:39], v0\ns_waitcnt lgkmcnt(2)\nv_smfmac_f32_16x16x64_bf16 v[20:23], v[40:43], a[104:111], v0\ns_waitcnt lgkmcnt(1)\n" \
    "v_smfmac_f32_16x16x64_bf16 v[20:23], v[44:47], a[136:143], v0\ns_waitcnt lgkmcnt(0)\nv_smfmac_f32_16x16x64_bf16 v[20:23], v[48:51], a[152:159], v0\ns_nop 2\n" \
    "v_smfmac_f32_16x16x64_bf16 v[16:19], v[56:59], a[64:71], v0\ns_nop 2\nv_smfmac_f32_16x16x64_bf16 v[16:19], v[24:27], a[80:87], v0\nv_permlane32_swap_b32 v20, v22\n" \
    "v_smfmac_f32_16x16x64_bf16 v[16:19], v[28:31], a[88:95], v0\nv_permlane32_swap_b32 v21, v23\nv_smfmac_f32_16x16x64_bf16 v[16:19], v[40:43], a[96:103], v0\n" \
    "v_add_f32 v20, v20, v22\nv_smfmac_f32_16x16x64_bf16 v[16:19], v[44:47], a[128:135], v0\nv_add_f32 v21, v21, v23\n" \
    "v_smfmac_f32_16x16x64_bf16 v[16:19], v[48:51], a[144:151], v0\nv_cvt_pk_bf16_f32 v20, v20, v21\nds_write_b16 v4, v20 offset:8\nds_write_b16_d16_hi v4, v20 offset:24\n" \
    "v_mov_b32 v15, v15\nv_mov_b32 v15, v15\nv_mov_b32 v15, v15\nv_mov_b32 v15, v15\nv_mov_b32 v15, v15\nv_permlane32_swap_b32 v16, v18\nv_permlane32_swap_b32 v17, v19\n" \
    "v_pk_add_f32 v[16:17], v[16:17], v[18:19]\nv_cvt_pk_bf16_f32 v16, v16, v17\nds_write_b16 v4, v16 offset:0\nds_write_b16_d16_hi v4, v16 offset:16\nv_mov_b64 v[30:31], 0\n" \
    "v_mov_b64 v[28:29], 0\nv_mov_b64 v[26:27], 0\nv_mov_b64 v[24:25], 0\nv_mov_b64 v[70:71], 0\nv_mov_b64 v[68:69], 0\nv_mov_b64 v[66:67], 0\nv_mov_b64 v[64:65], 0\n" \
    "v_mov_b64 v[22:23], 0\nv_mov_b64 v[20:21], 0\nv_mov_b64 v[18:19], 0\nv_mov_b32 v15, v15\ns_waitcnt lgkmcnt(0)\ns_barrier\nv_mov_b64 v[16:17], 0\ns_branch .Lv7_%=\n" \
    ".Lv10_%=:\nds_read_b128 v[24:27], v3 offset:4096\nds_read_b128 v[28:31], v3 offset:5120\nds_read_b128 v[32:35], v3 offset:12288\nds_read_b128 v[36:39], v3 offset:13312\n" \
    "ds_read_b128 v[40:43], v3 offset:20480\nds_read_b128 v[44:47], v3 offset:21504\nds_read_b128 v[48:51], v3 offset:0\nds_read_b128 v[52:55], v3 offset:1024\n" \
    "ds_read_b128 v[56:59], v3 offset:2048\nds_read_b128 v[60:63], v3 offset:3072\nds_read_b128 v[64:67], v3 offset:6144\nds_read_b128 v[68:71], v3 offset:7168\n.Lv27_%=:\n" \
    "s_xor_b32 s90, s81, 0x800\n.Lv29_%=:\ns_memrealtime s[72:73]\ns_waitcnt lgkmcnt(0)\ns_sub_u32 s72, s72, s80\ns_cmp_lt_i32 s72, 0\ns_cbranch_scc1 .Lv29_%=\n" \
    "buffer_load_dwordx4 v[72:75], v2, s[64:67], s81 offen offset:0 sc1\nbuffer_load_dwordx4 v[76:79], v2, s[64:67], s81 offen offset:1024 sc1\n" \
    "buffer_load_dwordx4 v[80:83], v2, s[64:67], s90 offen offset:0 sc1\nbuffer_load_dwordx4 v[84:87], v2, s[64:67], s90 offen offset:1024 sc1\nv_mov_b64 v[16:17], 0\n" \
    "v_mov_b64 v[18:19], 0\nv_mov_b64 v[20:21], 0\nv_mov_b64 v[22:23], 0\ns_waitcnt vmcnt(3)\nds_write_b128 v1, v[72:75] offset:26624\n" \
    "v_smfmac_f32_16x16x64_bf16 v[20:23], v[72:75], v[24:31], v0\nv_max3_u32 v252, v72, v73, v74\nv_smfmac_f32_16x16x64_bf16 v[16:19], v[72:75], a[16:23], v0\n" \
    "s_waitcnt vmcnt(2)\nds_write_b128 v1, v[76:79] offset:30720\nv_max3_u32 v252, v252, v75, v76\nv_max3_u32 v253, v77, v78, v79\n" \
    "v_smfmac_f32_16x16x64_bf16 v[20:23], v[76:79], v[32:39], v0\ns_waitcnt vmcnt(1)\nds_write_b128 v1, v[80:83] offset:34816\nv_max3_u32 v252, v252, v80, v81\n" \
    "v_max3_u32 v253, v253, v82, v83\nv_smfmac_f32_16x16x64_bf16 v[16:19], v[76:79], a[48:55], v0\ns_waitcnt vmcnt(0)\nds_write_b128 v1, v[84:87] offset:38912\n" \
    "v_max3_u32 v252, v252, v84, v85\nv_max3_u32 v253, v253, v86, v87\nv_max_u32 v252, v252, v253\nv_cmp_eq_u32 vcc, -1, v252\ns_nop 0\ns_cbranch_vccnz .Lv28_%=\n" \
    "s_waitcnt lgkmcnt(0)\ns_barrier\nds_read_b128 v[88:91], v1 offset:24576\nds_read_b128 v[24:27], v1 offset:25600\nds_read_b128 v[28:31], v1 offset:27648\n" \
    "ds_read_b128 v[32:35], v1 offset:28672\nds_read_b128 v[72:75], v3 offset:8192\nds_read_b128 v[76:79], v3 offset:9216\n" \
    "v_smfmac_f32_16x16x64_bf16 v[20:23], v[80:83], v[40:47], v0\nds_read_b128 v[36:39], v1 offset:29696\nv_smfmac_f32_16x16x64_bf16 v[16:19], v[80:83], a[80:87], v0\n" \
    "ds_read_b128 v[40:43], v3 offset:10240\nv_smfmac_f32_16x16x64_bf16 v[20:23], v[84:87], a[136:143], v0\nds_read_b128 v[44:47], v3 offset:11264\n" \
    "v_smfmac_f32_16x16x64_bf16 v[16:19], v[84:87], a[128:135], v0\nds_read_b128 v[80:83], v1 offset:31744\ns_waitcnt lgkmcnt(9)\n" \
    "v_smfmac_f32_16x16x64_bf16 v[20:23], v[88:91], v[48:55], v0\nds_read_b128 v[48:51], v3 offset:14336\nv_smfmac_f32_16x16x64_bf16 v[16:19], v[88:91], a[0:7], v0\n" \
    "ds_read_b128 v[52:55], v3 offset:15360\ns_waitcnt lgkmcnt(10)\nv_smfmac_f32_16x16x64_bf16 v[20:23], v[24:27], v[56:63], v0\nds_read_b128 v[56:59], v1 offset:32768\n" \
    "v_smfmac_f32_16x16x64_bf16 v[16:19], v[24:27], a[8:15], v0\nds_read_b128 v[84:87], v3 offset:16384\ns_waitcnt lgkmcnt(11)\n" \
    "v_smfmac_f32_16x16x64_bf16 v[20:23], v[28:31], v[64:71], v0\nds_read_b128 v[88:91], v3 offset:17408\nv_smfmac_f32_16x16x64_bf16 v[16:19], v[28:31], a[24:31], v0\n" \
    "ds_read_b128 v[24:27], v1 offset:33792\ns_waitcnt lgkmcnt(10)\nv_smfmac_f32_16x16x64_bf16 v[20:23], v[32:35], v[72:79], v0\nds_read_b128 v[60:63], v3 offset:18432\n" \
    "v_smfmac_f32_16x16x64_bf16 v[16:19], v[32:35], a[32:39], v0\nds_read_b128 v[64:67], v3 offset:19456\ns_waitcnt lgkmcnt(9)\n" \
    "v_smfmac_f32_16x16x64_bf16 v[20:23], v[36:39], v[40:47], v0\nds_read_b128 v[28:31], v1 offset:35840\nv_smfmac_f32_16x16x64_bf16 v[16:19], v[36:39], a[40:47], v0\n" \
    "ds_read_b128 v[32:35], v3 offset:22528\ns_waitcnt lgkmcnt(8)\nv_smfmac_f32_16x16x64_bf16 v[20:23], v[80:83], v[48:55], v0\nds_read_b128 v[36:39], v3 offset:23552\n" \
    "v_smfmac_f32_16x16x64_bf16 v[16:19], v[80:83], a[56:63], v0\nds_read_b128 v[40:43], v1 offset:36864\ns_waitcnt lgkmcnt(7)\n" \
    "v_smfmac_f32_16x16x64_bf16 v[20:23], v[56:59], v[84:91], v0\nds_read_b128 v[44:47], v1 offset:37888\ns_waitcnt lgkmcnt(5)\n" \
    "v_smfmac_f32_16x16x64_bf16 v[20:23], v[24:27], v[60:67], v0\nds_read_b128 v[48:51], v1 offset:39936\ns_waitcnt lgkmcnt(3)\n" \
    "v_smfmac_f32_16x16x64_bf16 v[20:23], v[28:31], v[32:39], v0\ns_waitcnt lgkmcnt(2)\nv_smfmac_f32_16x16x64_bf16 v[20:23], v[40:43], a[104:111], v0\ns_waitcnt lgkmcnt(1)\n" \
    "v_smfmac_f32_16x16x64_bf16 v[20:23], v[44:47], a[120:127], v0\ns_waitcnt lgkmcnt(0)\nv_smfmac_f32_16x16x64_bf16 v[20:23], v[48:51], a[152:159], v0\ns_nop 2\n" \
    "v_smfmac_f32_16x16x64_bf16 v[16:19], v[56:59], a[64:71], v0\ns_nop 2\nv_smfmac_f32_16x16x64_bf16 v[16:19], v[24:27], a[72:79], v0\nv_permlane32_swap_b32 v20, v22\n" \
    "v_smfmac_f32_16x16x64_bf16 v[16:19], v[28:31], a[88:95], v0\nv_permlane32_swap_b32 v21, v23\nv_smfmac_f32_16x16x64_bf16 v[16:19], v[40:43], a[96:103], v0\n" \
    "v_add_f32 v20, v20, v22\nv_smfmac_f32_16x16x64_bf16 v[16:19], v[44:47], a[112:119], v0\nv_add_f32 v21, v21, v23\n" \
    "v_smfmac_f32_16x16x64_bf16 v[16:19], v[48:51], a[144:151], v0\nv_cvt_pk_bf16_f32 v20, v20, v21\nds_write_b16 v4, v20 offset:8\nds_write_b16_d16_hi v4, v20 offset:24\n" \
    "v_mov_b32 v15, v15\nv_mov_b32 v15, v15\nv_mov_b32 v15, v15\nv_mov_b32 v15, v15\nv_mov_b32 v15, v15\nv_permlane32_swap_b32 v16, v18\nv_permlane32_swap_b32 v17, v19\n" \
    "v_pk_add_f32 v[16:17], v[16:17], v[18:19]\nv_cvt_pk_bf16_f32 v16, v16, v17\nds_write_b16 v4, v16 offset:0\nds_write_b16_d16_hi v4, v16 offset:16\nv_mov_b64 v[30:31], 0\n" \
    "v_mov_b64 v[28:29], 0\nv_mov_b64 v[26:27], 0\nv_mov_b64 v[24:25], 0\nv_mov_b64 v[70:71], 0\nv_mov_b64 v[68:69], 0\nv_mov_b64 v[66:67], 0\nv_mov_b64 v[64:65], 0\n" \
    "v_mov_b64 v[22:23], 0\nv_mov_b64 v[20:21], 0\nv_mov_b64 v[18:19], 0\nv_mov_b32 v15, v15\ns_waitcnt lgkmcnt(0)\ns_barrier\nv_mov_b64 v[16:17], 0\ns_branch .Lv7_%=\n" \
    ".Lv11_%=:\nds_read_b128 v[24:27], v3 offset:6144\nds_read_b128 v[28:31], v3 offset:7168\nds_read_b128 v[32:35], v3 offset:14336\nds_read_b128 v[36:39], v3 offset:15360\n" \
    "ds_read_b128 v[40:43], v3 offset:22528\nds_read_b128 v[44:47], v3 offset:23552\nds_read_b128 v[48:51], v3 offset:0\nds_read_b128 v[52:55], v3 offset:1024\n" \
    "ds_read_b128 v[56:59], v3 offset:2048\nds_read_b128 v[60:63], v3 offset:3072\nds_read_b128 v[64:67], v3 offset:4096\nds_read_b128 v[68:71], v3 offset:5120\n.Lv30_%=:\n" \
    "s_xor_b32 s90, s81, 0x800\n.Lv32_%=:\ns_memrealtime s[72:73]\ns_waitcnt lgkmcnt(0)\ns_sub_u32 s72, s72, s80\ns_cmp_lt_i32 s72, 0\ns_cbranch_scc1 .Lv32_%=\n" \
    "buffer_load_dwordx4 v[72:75], v2, s[64:67], s81 offen offset:0 sc1\nbuffer_load_dwordx4 v[76:79], v2, s[64:67], s81 offen offset:1024 sc1\n" \
    "buffer_load_dwordx4 v[80:83], v2, s[64:67], s90 offen offset:0 sc1\nbuffer_load_dwordx4 v[84:87], v2, s[64:67], s90 offen offset:1024 sc1\nv_mov_b64 v[16:17], 0\n" \
    "v_mov_b64 v[18:19], 0\nv_mov_b64 v[20:21], 0\nv_mov_b64 v[22:23], 0\ns_waitcnt vmcnt(3)\nds_write_b128 v1, v[72:75] offset:27648\n" \
    "v_smfmac_f32_16x16x64_bf16 v[20:23], v[72:75], v[24:31], v0\nv_max3_u32 v252, v72, v73, v74\nv_smfmac_f32_16x16x64_bf16 v[16:19], v[72:75], a[24:31], v0\n" \
    "s_waitcnt vmcnt(2)\nds_write_b128 v1, v[76:79] offset:31744\nv_max3_u32 v252, v252, v75, v76\nv_max3_u32 v253, v77, v78, v79\n" \
    "v_smfmac_f32_16x16x64_bf16 v[20:23], v[76:79], v[32:39], v0\ns_waitcnt vmcnt(1)\nds_write_b128 v1, v[80:83] offset:35840\nv_max3_u32 v252, v252, v80, v81\n" \
    "v_max3_u32 v253, v253, v82, v83\nv_smfmac_f32_16x16x64_bf16 v[16:19], v[76:79], a[56:63], v0\ns_waitcnt vmcnt(0)\nds_write_b128 v1, v[84:87] offset:39936\n" \
    "v_max3_u32 v252, v252, v84, v85\nv_max3_u32 v253, v253, v86, v87\nv_max_u32 v252, v252, v253\nv_cmp_eq_u32 vcc, -1, v252\ns_nop 0\ns_cbranch_vccnz .Lv31_%=\n" \
    "s_waitcnt lgkmcnt(0)\ns_barrier\nds_read_b128 v[88:91], v1 offset:24576\nds_read_b128 v[24:27], v1 offset:25600\nds_read_b128 v[28:31], v1 offset:26624\n" \
    "ds_read_b128 v[32:35], v1 offset:28672\nds_read_b128 v[72:75], v3 offset:8192\nds_read_b128 v[76:79], v3 offset:9216\n" \
    "v_smfmac_f32_16x16x64_bf16 v[20:23], v[80:83], v[40:47], v0\nds_read_b128 v[36:39], v1 offset:29696\nv_smfmac_f32_16x16x64_bf16 v[16:19], v[80:83], a[88:95], v0\n" \
    "ds_read_b128 v[40:43], v3 offset:10240\nv_smfmac_f32_16x16x64_bf16 v[20:23], v[84:87], a[152:159], v0\nds_read_b128 v[44:47], v3 offset:11264\n" \
    "v_smfmac_f32_16x16x64_bf16 v[16:19], v[84:87], a[144:151], v0\nds_read_b128 v[80:83], v1 offset:30720\ns_waitcnt lgkmcnt(9)\n" \
    "v_smfmac_f32_16x16x64_bf16 v[20:23], v[88:91], v[48:55], v0\nds_read_b128 v[48:51], v3 offset:12288\nv_smfmac_f32_16x16x64_bf16 v[16:19], v[88:91], a[0:7], v0\n" \
    "ds_read_b128 v[52:55], v3 offset:13312\ns_waitcnt lgkmcnt(10)\nv_smfmac_f32_16x16x64_bf16 v[20:23], v[24:27], v[56:63], v0\nds_read_b128 v[56:59], v1 offset:32768\n" \
    "v_smfmac_f32_16x16x64_bf16 v[16:19], v[24:27], a[8:15], v0\nds_read_b128 v[84:87], v3 offset:16384\ns_waitcnt lgkmcnt(11)\n" \
    "v_smfmac_f32_16x16x64_bf16 v[20:23], v[28:31], v[64:71], v0\nds_read_b128 v[88:91], v3 offset:17408\nv_smfmac_f32_16x16x64_bf16 v[16:19], v[28:31], a[16:23], v0\n" \
    "ds_read_b128 v[24:27], v1 offset:33792\ns_waitcnt lgkmcnt(10)\nv_smfmac_f32_16x16x64_bf16 v[20:23], v[32:35], v[72:79], v0\nds_read_b128 v[60:63], v3 offset:18432\n" \
    "v_smfmac_f32_16x16x64_bf16 v[16:19], v[32:35], a[32:39], v0\nds_read_b128 v[64:67], v3 offset:19456\ns_waitcnt lgkmcnt(9)\n" \
    "v_smfmac_f32_16x16x64_bf16 v[20:23], v[36:39], v[40:47], v0\nds_read_b128 v[28:31], v1 offset:34816\nv_smfmac_f32_16x16x64_bf16 v[16:19], v[36:39], a[40:47], v0\n" \
    "ds_read_b128 v[32:35], v3 offset:20480\ns_waitcnt lgkmcnt(8)\nv_smfmac_f32_16x16x64_bf16 v[20:23], v[80:83], v[48:55], v0\nds_read_b128 v[36:39], v3 offset:21504\n" \
    "v_smfmac_f32_16x16x64_bf16 v[16:19], v[80:83], a[48:55], v0\nds_read_b128 v[40:43], v1 offset:36864\ns_waitcnt lgkmcnt(7)\n" \
    "v_smfmac_f32_16x16x64_bf16 v[20:23], v[56:59], v[84:91], v0\nds_read_b128 v[44:47], v1 offset:37888\ns_waitcnt lgkmcnt(5)\n" \
    "v_smfmac_f32_16x16x64_bf16 v[20:23], v[24:27], v[60:67], v0\nds_read_b128 v[48:51], v1 offset:38912\ns_waitcnt lgkmcnt(3)\n" \
    "v_smfmac_f32_16x16x64_bf16 v[20:23], v[28:31], v[32:39], v0\ns_waitcnt lgkmcnt(2)\nv_smfmac_f32_16x16x64_bf16 v[20:23], v[40:43], a[104:111], v0\ns_waitcnt lgkmcnt(1)\n" \
    "v_smfmac_f32_16x16x64_bf16 v[20:23], v[44:47], a[120:127], v0\ns_waitcnt lgkmcnt(0)\nv_smfmac_f32_16x16x64_bf16 v[20:23], v[48:51], a[136:143], v0\ns_nop 2\n" \
    "v_smfmac_f32_16x16x64_bf16 v[16:19], v[56:59], a[64:71], v0\ns_nop 2\nv_smfmac_f32_16x16x64_bf16 v[16:19], v[24:27], a[72:79], v0\nv_permlane32_swap_b32 v20, v22\n" \
    "v_smfmac_f32_16x16x64_bf16 v[16:19], v[28:31], a[80:87], v0\nv_permlane32_swap_b32 v21, v23\nv_smfmac_f32_16x16x64_bf16 v[16:19], v[40:43], a[96:103], v0\n" \
    "v_add_f32 v20, v20, v22\nv_smfmac_f32_16x16x64_bf16 v[16:19], v[44:47], a[112:119], v0\nv_add_f32 v21, v21, v23\n" \
    "v_smfmac_f32_16x16x64_bf16 v[16:19], v[48:51], a[128:135], v0\nv_cvt_pk_bf16_f32 v20, v20, v21\nds_write_b16 v4, v20 offset:8\nds_write_b16_d16_hi v4, v20 offset:24\n" \
    "v_mov_b32 v15, v15\nv_mov_b32 v15, v15\nv_mov_b32 v15, v15\nv_mov_b32 v15, v15\nv_mov_b32 v15, v15\nv_permlane32_swap_b32 v16, v18\nv_permlane32_swap_b32 v17, v19\n" \
    "v_pk_add_f32 v[16:17], v[16:17], v[18:19]\nv_cvt_pk_bf16_f32 v16, v16, v17\nds_write_b16 v4, v16 offset:0\nds_write_b16_d16_hi v4, v16 offset:16\nv_mov_b64 v[30:31], 0\n" \
    "v_mov_b64 v[28:29], 0\nv_mov_b64 v[26:27], 0\nv_mov_b64 v[24:25], 0\nv_mov_b64 v[70:71], 0\nv_mov_b64 v[68:69], 0\nv_mov_b64 v[66:67], 0\nv_mov_b64 v[64:65], 0\n" \
    "v_mov_b64 v[22:23], 0\nv_mov_b64 v[20:21], 0\nv_mov_b64 v[18:19], 0\nv_mov_b32 v15, v15\ns_waitcnt lgkmcnt(0)\ns_barrier\nv_mov_b64 v[16:17], 0\n.Lv7_%=:\n" \
    "ds_read_b128 v[32:35], v5 offset:0\nv_mov_b64 v[48:49], 0\nds_read_b128 v[36:39], v5 offset:512\nv_mov_b64 v[50:51], 0\nds_read_b128 v[40:43], v5 offset:1024\n" \
    "v_mov_b64 v[52:53], 0\nds_read_b128 v[44:47], v5 offset:1536\nv_mov_b64 v[54:55], 0\ns_waitcnt lgkmcnt(3)\nv_smfmac_f32_16x16x64_bf16 v[16:19], v[32:35], a[160:167], v0\n" \
    "v_smfmac_f32_16x16x64_bf16 v[20:23], v[32:35], a[176:183], v0\ns_waitcnt lgkmcnt(2)\nv_smfmac_f32_16x16x64_bf16 v[16:19], v[36:39], a[168:175], v0\n" \
    "v_smfmac_f32_16x16x64_bf16 v[20:23], v[36:39], a[184:191], v0\ns_waitcnt lgkmcnt(1)\nv_smfmac_f32_16x16x64_bf16 v[16:19], v[40:43], a[192:199], v0\n" \
    "v_smfmac_f32_16x16x64_bf16 v[20:23], v[40:43], a[208:215], v0\ns_waitcnt lgkmcnt(0)\nv_smfmac_f32_16x16x64_bf16 v[16:19], v[44:47], a[200:207], v0\n" \
    "v_smfmac_f32_16x16x64_bf16 v[20:23], v[44:47], a[216:223], v0\ns_nop 2\nv_smfmac_f32_16x16x64_bf16 v[24:27], v[32:35], a[224:231], v0\ns_nop 2\n" \
    "v_cvt_pk_bf16_f32 v72, v16, v17\nv_smfmac_f32_16x16x64_bf16 v[28:31], v[32:35], a[240:247], v0\nv_cvt_pk_bf16_f32 v74, v18, v19\n" \
    "v_smfmac_f32_16x16x64_bf16 v[24:27], v[36:39], a[232:239], v0\nv_cvt_pk_bf16_f32 v73, v20, v21\nv_smfmac_f32_16x16x64_bf16 v[28:31], v[36:39], a[248:255], v0\n" \
    "v_cvt_pk_bf16_f32 v75, v22, v23\nv_smfmac_f32_16x16x64_bf16 v[24:27], v[40:43], v[92:99], v0\nv_permlane16_swap_b32 v72, v73\n" \
    "v_smfmac_f32_16x16x64_bf16 v[28:31], v[40:43], v[108:115], v0\nv_permlane16_swap_b32 v74, v75\nv_smfmac_f32_16x16x64_bf16 v[24:27], v[44:47], v[100:107], v0\n" \
    "buffer_store_dwordx4 v[72:75], v7, s[64:67], s86 offen offset:0 sc1\nv_smfmac_f32_16x16x64_bf16 v[28:31], v[44:47], v[116:123], v0\ns_nop 2\n" \
    "v_smfmac_f32_16x16x64_bf16 v[64:67], v[32:35], v[124:131], v0\ns_nop 1\nv_cvt_pk_bf16_f32 v76, v24, v25\nv_smfmac_f32_16x16x64_bf16 v[68:71], v[32:35], v[140:147], v0\n" \
    "v_cvt_pk_bf16_f32 v78, v26, v27\nv_smfmac_f32_16x16x64_bf16 v[64:67], v[36:39], v[132:139], v0\nv_cvt_pk_bf16_f32 v77, v28, v29\n" \
    "v_smfmac_f32_16x16x64_bf16 v[68:71], v[36:39], v[148:155], v0\nv_cvt_pk_bf16_f32 v79, v30, v31\nv_smfmac_f32_16x16x64_bf16 v[64:67], v[40:43], v[156:163], v0\n" \
    "v_permlane16_swap_b32 v76, v77\nv_smfmac_f32_16x16x64_bf16 v[68:71], v[40:43], v[172:179], v0\nv_permlane16_swap_b32 v78, v79\n" \
    "v_smfmac_f32_16x16x64_bf16 v[64:67], v[44:47], v[164:171], v0\nbuffer_store_dwordx4 v[76:79], v7, s[64:67], s87 offen offset:0 sc1\n" \
    "v_smfmac_f32_16x16x64_bf16 v[68:71], v[44:47], v[180:187], v0\ns_nop 2\nv_smfmac_f32_16x16x64_bf16 v[48:51], v[32:35], v[188:195], v0\ns_nop 1\n" \
    "v_cvt_pk_bf16_f32 v72, v64, v65\nv_smfmac_f32_16x16x64_bf16 v[52:55], v[32:35], v[204:211], v0\nv_cvt_pk_bf16_f32 v74, v66, v67\n" \
    "v_smfmac_f32_16x16x64_bf16 v[48:51], v[36:39], v[196:203], v0\nv_cvt_pk_bf16_f32 v73, v68, v69\nv_smfmac_f32_16x16x64_bf16 v[52:55], v[36:39], v[212:219], v0\n" \
    "v_cvt_pk_bf16_f32 v75, v70, v71\nv_smfmac_f32_16x16x64_bf16 v[48:51], v[40:43], v[220:227], v0\nv_permlane16_swap_b32 v72, v73\n" \
    "v_smfmac_f32_16x16x64_bf16 v[52:55], v[40:43], v[236:243], v0\nv_permlane16_swap_b32 v74, v75\nv_smfmac_f32_16x16x64_bf16 v[48:51], v[44:47], v[228:235], v0\n" \
    "buffer_store_dwordx4 v[72:75], v7, s[64:67], s88 offen offset:0 sc1\nv_smfmac_f32_16x16x64_bf16 v[52:55], v[44:47], v[244:251], v0\ns_nop 5\n" \
    "v_cvt_pk_bf16_f32 v76, v48, v49\nv_cvt_pk_bf16_f32 v78, v50, v51\nv_cvt_pk_bf16_f32 v77, v52, v53\nv_cvt_pk_bf16_f32 v79, v54, v55\ns_nop 0\n" \
    "v_permlane16_swap_b32 v76, v77\nv_permlane16_swap_b32 v78, v79\nbuffer_store_dwordx4 v[76:79], v7, s[64:67], s89 offen offset:0 sc1\ns_add_u32 s35, s35, 2\n" \
    "s_cmp_lt_u32 s35, 62\ns_cbranch_scc1 .Lv5_%=\ns_cmp_lt_u32 s35, 64\ns_cbranch_scc0 .Lv6_%=\nds_read_b128 v[56:59], v6 offset:0\nds_read_b128 v[60:63], v6 offset:1024\n" \
    "ds_read_b128 v[64:67], v6 offset:2048\ns_and_b32 s69, s50, 15\ns_lshl_b32 s69, s69, 21\ns_add_u32 s86, s39, s69\ns_lshr_b32 s69, s86, 8\ns_and_b32 s69, s69, 0x2000\n" \
    "s_xor_b32 s86, s86, s69\ns_add_u32 s87, s86, 0x40000\ns_add_u32 s88, s86, 0x80000\ns_add_u32 s89, s86, 0xc0000\ns_add_u32 s50, s50, 2\nv_mov_b64 v[32:33], 0\n" \
    "v_mov_b64 v[34:35], 0\nv_mov_b64 v[36:37], 0\nv_mov_b64 v[38:39], 0\nv_mov_b64 v[40:41], 0\nv_mov_b64 v[42:43], 0\nv_mov_b64 v[44:45], 0\nv_mov_b64 v[46:47], 0\n" \
    "s_waitcnt lgkmcnt(2)\nv_smfmac_f32_16x16x64_bf16 v[32:35], v[56:59], a[160:167], v0\nds_read_b128 v[68:71], v6 offset:3072\n" \
    "v_smfmac_f32_16x16x64_bf16 v[36:39], v[56:59], a[176:183], v0\ns_waitcnt lgkmcnt(2)\nv_smfmac_f32_16x16x64_bf16 v[32:35], v[60:63], a[168:175], v0\n" \
    "v_smfmac_f32_16x16x64_bf16 v[36:39], v[60:63], a[184:191], v0\ns_waitcnt lgkmcnt(1)\nv_smfmac_f32_16x16x64_bf16 v[32:35], v[64:67], a[192:199], v0\n" \
    "v_smfmac_f32_16x16x64_bf16 v[36:39], v[64:67], a[208:215], v0\ns_waitcnt lgkmcnt(0)\nv_smfmac_f32_16x16x64_bf16 v[32:35], v[68:71], a[200:207], v0\n" \
    "v_smfmac_f32_16x16x64_bf16 v[36:39], v[68:71], a[216:223], v0\ns_nop 2\nv_smfmac_f32_16x16x64_bf16 v[40:43], v[56:59], a[224:231], v0\ns_nop 2\n" \
    "v_cvt_pk_bf16_f32 v48, v32, v33\nv_cvt_pk_bf16_f32 v50, v34, v35\nv_smfmac_f32_16x16x64_bf16 v[44:47], v[56:59], a[240:247], v0\nv_cvt_pk_bf16_f32 v49, v36, v37\n" \
    "v_cvt_pk_bf16_f32 v51, v38, v39\nv_smfmac_f32_16x16x64_bf16 v[40:43], v[60:63], a[232:239], v0\nv_permlane16_swap_b32 v48, v49\n" \
    "v_smfmac_f32_16x16x64_bf16 v[44:47], v[60:63], a[248:255], v0\nv_permlane16_swap_b32 v50, v51\nv_smfmac_f32_16x16x64_bf16 v[40:43], v[64:67], v[92:99], v0\n" \
    "buffer_store_dwordx4 v[48:51], v8, s[64:67], s86 offen offset:0 sc1\nv_mov_b64 v[32:33], 0\nv_mov_b64 v[34:35], 0\n" \
    "v_smfmac_f32_16x16x64_bf16 v[44:47], v[64:67], v[108:115], v0\nv_mov_b64 v[36:37], 0\nv_mov_b64 v[38:39], 0\nv_smfmac_f32_16x16x64_bf16 v[40:43], v[68:71], v[100:107], v0\n" \
    "v_smfmac_f32_16x16x64_bf16 v[44:47], v[68:71], v[116:123], v0\ns_nop 2\nv_smfmac_f32_16x16x64_bf16 v[32:35], v[56:59], v[124:131], v0\ns_nop 2\n" \
    "v_cvt_pk_bf16_f32 v52, v40, v41\nv_cvt_pk_bf16_f32 v54, v42, v43\nv_smfmac_f32_16x16x64_bf16 v[36:39], v[56:59], v[140:147], v0\nv_cvt_pk_bf16_f32 v53, v44, v45\n" \
    "v_cvt_pk_bf16_f32 v55, v46, v47\nv_smfmac_f32_16x16x64_bf16 v[32:35], v[60:63], v[132:139], v0\nv_permlane16_swap_b32 v52, v53\n" \
    "v_smfmac_f32_16x16x64_bf16 v[36:39], v[60:63], v[148:155], v0\nv_permlane16_swap_b32 v54, v55\nv_smfmac_f32_16x16x64_bf16 v[32:35], v[64:67], v[156:163], v0\n" \
    "buffer_store_dwordx4 v[52:55], v8, s[64:67], s87 offen offset:0 sc1\nv_mov_b64 v[40:41], 0\nv_mov_b64 v[42:43], 0\n" \
    "v_smfmac_f32_16x16x64_bf16 v[36:39], v[64:67], v[172:179], v0\nv_mov_b64 v[44:45], 0\nv_mov_b64 v[46:47], 0\nv_smfmac_f32_16x16x64_bf16 v[32:35], v[68:71], v[164:171], v0\n" \
    "v_smfmac_f32_16x16x64_bf16 v[36:39], v[68:71], v[180:187], v0\ns_nop 2\nv_smfmac_f32_16x16x64_bf16 v[40:43], v[56:59], v[188:195], v0\ns_nop 2\n" \
    "v_cvt_pk_bf16_f32 v48, v32, v33\nv_cvt_pk_bf16_f32 v50, v34, v35\nv_smfmac_f32_16x16x64_bf16 v[44:47], v[56:59], v[204:211], v0\nv_cvt_pk_bf16_f32 v49, v36, v37\n" \
    "v_cvt_pk_bf16_f32 v51, v38, v39\nv_smfmac_f32_16x16x64_bf16 v[40:43], v[60:63], v[196:203], v0\nds_read_b128 v[56:59], v6 offset:4096\nv_permlane16_swap_b32 v48, v49\n" \
    "v_smfmac_f32_16x16x64_bf16 v[44:47], v[60:63], v[212:219], v0\nv_permlane16_swap_b32 v50, v51\nv_smfmac_f32_16x16x64_bf16 v[40:43], v[64:67], v[220:227], v0\n" \
    "ds_read_b128 v[60:63], v6 offset:5120\nbuffer_store_dwordx4 v[48:51], v8, s[64:67], s88 offen offset:0 sc1\nv_mov_b64 v[32:33], 0\nv_mov_b64 v[34:35], 0\n" \
    "v_smfmac_f32_16x16x64_bf16 v[44:47], v[64:67], v[236:243], v0\nv_mov_b64 v[36:37], 0\nv_mov_b64 v[38:39], 0\nv_smfmac_f32_16x16x64_bf16 v[40:43], v[68:71], v[228:235], v0\n" \
    "ds_read_b128 v[64:67], v6 offset:6144\nv_smfmac_f32_16x16x64_bf16 v[44:47], v[68:71], v[244:251], v0\ns_nop 2\ns_waitcnt lgkmcnt(2)\n" \
    "v_smfmac_f32_16x16x64_bf16 v[32:35], v[56:59], a[160:167], v0\nds_read_b128 v[68:71], v6 offset:7168\nv_cvt_pk_bf16_f32 v52, v40, v41\nv_cvt_pk_bf16_f32 v54, v42, v43\n" \
    "v_smfmac_f32_16x16x64_bf16 v[36:39], v[56:59], a[176:183], v0\ns_waitcnt lgkmcnt(2)\nv_cvt_pk_bf16_f32 v53, v44, v45\nv_cvt_pk_bf16_f32 v55, v46, v47\n" \
    "v_smfmac_f32_16x16x64_bf16 v[32:35], v[60:63], a[168:175], v0\nv_permlane16_swap_b32 v52, v53\nv_smfmac_f32_16x16x64_bf16 v[36:39], v[60:63], a[184:191], v0\n" \
    "s_waitcnt lgkmcnt(1)\nv_permlane16_swap_b32 v54, v55\nv_smfmac_f32_16x16x64_bf16 v[32:35], v[64:67], a[192:199], v0\n" \
    "buffer_store_dwordx4 v[52:55], v8, s[64:67], s89 offen offset:0 sc1\nv_mov_b64 v[40:41], 0\nv_mov_b64 v[42:43], 0\n" \
    "v_smfmac_f32_16x16x64_bf16 v[36:39], v[64:67], a[208:215], v0\ns_waitcnt lgkmcnt(0)\nv_mov_b64 v[44:45], 0\nv_mov_b64 v[46:47], 0\n" \
    "v_smfmac_f32_16x16x64_bf16 v[32:35], v[68:71], a[200:207], v0\nv_smfmac_f32_16x16x64_bf16 v[36:39], v[68:71], a[216:223], v0\ns_nop 2\n" \
    "v_smfmac_f32_16x16x64_bf16 v[40:43], v[56:59], a[224:231], v0\ns_nop 2\nv_cvt_pk_bf16_f32 v48, v32, v33\nv_cvt_pk_bf16_f32 v50, v34, v35\n" \
    "v_smfmac_f32_16x16x64_bf16 v[44:47], v[56:59], a[240:247], v0\nv_cvt_pk_bf16_f32 v49, v36, v37\nv_cvt_pk_bf16_f32 v51, v38, v39\n" \
    "v_smfmac_f32_16x16x64_bf16 v[40:43], v[60:63], a[232:239], v0\nv_permlane16_swap_b32 v48, v49\nv_smfmac_f32_16x16x64_bf16 v[44:47], v[60:63], a[248:255], v0\n" \
    "v_permlane16_swap_b32 v50, v51\nv_smfmac_f32_16x16x64_bf16 v[40:43], v[64:67], v[92:99], v0\nbuffer_store_dwordx4 v[48:51], v8, s[64:67], s86 offen offset:256 sc1\n" \
    "v_mov_b64 v[32:33], 0\nv_mov_b64 v[34:35], 0\nv_smfmac_f32_16x16x64_bf16 v[44:47], v[64:67], v[108:115], v0\nv_mov_b64 v[36:37], 0\nv_mov_b64 v[38:39], 0\n" \
    "v_smfmac_f32_16x16x64_bf16 v[40:43], v[68:71], v[100:107], v0\nv_smfmac_f32_16x16x64_bf16 v[44:47], v[68:71], v[116:123], v0\ns_nop 2\n" \
    "v_smfmac_f32_16x16x64_bf16 v[32:35], v[56:59], v[124:131], v0\ns_nop 2\nv_cvt_pk_bf16_f32 v52, v40, v41\nv_cvt_pk_bf16_f32 v54, v42, v43\n" \
    "v_smfmac_f32_16x16x64_bf16 v[36:39], v[56:59], v[140:147], v0\nv_cvt_pk_bf16_f32 v53, v44, v45\nv_cvt_pk_bf16_f32 v55, v46, v47\n" \
    "v_smfmac_f32_16x16x64_bf16 v[32:35], v[60:63], v[132:139], v0\nv_permlane16_swap_b32 v52, v53\nv_smfmac_f32_16x16x64_bf16 v[36:39], v[60:63], v[148:155], v0\n" \
    "v_permlane16_swap_b32 v54, v55\nv_smfmac_f32_16x16x64_bf16 v[32:35], v[64:67], v[156:163], v0\nbuffer_store_dwordx4 v[52:55], v8, s[64:67], s87 offen offset:256 sc1\n" \
    "v_mov_b64 v[40:41], 0\nv_mov_b64 v[42:43], 0\nv_smfmac_f32_16x16x64_bf16 v[36:39], v[64:67], v[172:179], v0\nv_mov_b64 v[44:45], 0\nv_mov_b64 v[46:47], 0\n" \
    "v_smfmac_f32_16x16x64_bf16 v[32:35], v[68:71], v[164:171], v0\nv_smfmac_f32_16x16x64_bf16 v[36:39], v[68:71], v[180:187], v0\ns_nop 2\n" \
    "v_smfmac_f32_16x16x64_bf16 v[40:43], v[56:59], v[188:195], v0\ns_nop 2\nv_cvt_pk_bf16_f32 v48, v32, v33\nv_cvt_pk_bf16_f32 v50, v34, v35\n" \
    "v_smfmac_f32_16x16x64_bf16 v[44:47], v[56:59], v[204:211], v0\nv_cvt_pk_bf16_f32 v49, v36, v37\nv_cvt_pk_bf16_f32 v51, v38, v39\n" \
    "v_smfmac_f32_16x16x64_bf16 v[40:43], v[60:63], v[196:203], v0\nds_read_b128 v[56:59], v6 offset:128\nv_permlane16_swap_b32 v48, v49\n" \
    "v_smfmac_f32_16x16x64_bf16 v[44:47], v[60:63], v[212:219], v0\nv_permlane16_swap_b32 v50, v51\nv_smfmac_f32_16x16x64_bf16 v[40:43], v[64:67], v[220:227], v0\n" \
    "ds_read_b128 v[60:63], v6 offset:1152\nbuffer_store_dwordx4 v[48:51], v8, s[64:67], s88 offen offset:256 sc1\nv_mov_b64 v[32:33], 0\nv_mov_b64 v[34:35], 0\n" \
    "v_smfmac_f32_16x16x64_bf16 v[44:47], v[64:67], v[236:243], v0\nv_mov_b64 v[36:37], 0\nv_mov_b64 v[38:39], 0\nv_smfmac_f32_16x16x64_bf16 v[40:43], v[68:71], v[228:235], v0\n" \
    "ds_read_b128 v[64:67], v6 offset:2176\nv_smfmac_f32_16x16x64_bf16 v[44:47], v[68:71], v[244:251], v0\ns_nop 2\ns_waitcnt lgkmcnt(2)\n" \
    "v_smfmac_f32_16x16x64_bf16 v[32:35], v[56:59], a[160:167], v0\nds_read_b128 v[68:71], v6 offset:3200\nv_cvt_pk_bf16_f32 v52, v40, v41\nv_cvt_pk_bf16_f32 v54, v42, v43\n" \
    "v_smfmac_f32_16x16x64_bf16 v[36:39], v[56:59], a[176:183], v0\ns_waitcnt lgkmcnt(2)\nv_cvt_pk_bf16_f32 v53, v44, v45\nv_cvt_pk_bf16_f32 v55, v46, v47\n" \
    "v_smfmac_f32_16x16x64_bf16 v[32:35], v[60:63], a[168:175], v0\nv_permlane16_swap_b32 v52, v53\nv_smfmac_f32_16x16x64_bf16 v[36:39], v[60:63], a[184:191], v0\n" \
    "s_waitcnt lgkmcnt(1)\nv_permlane16_swap_b32 v54, v55\nv_smfmac_f32_16x16x64_bf16 v[32:35], v[64:67], a[192:199], v0\n" \
    "buffer_store_dwordx4 v[52:55], v8, s[64:67], s89 offen offset:256 sc1\nv_mov_b64 v[40:41], 0\nv_mov_b64 v[42:43], 0\n" \
    "v_smfmac_f32_16x16x64_bf16 v[36:39], v[64:67], a[208:215], v0\ns_waitcnt lgkmcnt(0)\nv_mov_b64 v[44:45], 0\nv_mov_b64 v[46:47], 0\n" \
    "v_smfmac_f32_16x16x64_bf16 v[32:35], v[68:71], a[200:207], v0\nv_smfmac_f32_16x16x64_bf16 v[36:39], v[68:71], a[216:223], v0\ns_nop 2\n" \
    "v_smfmac_f32_16x16x64_bf16 v[40:43], v[56:59], a[224:231], v0\ns_nop 2\nv_cvt_pk_bf16_f32 v48, v32, v33\nv_cvt_pk_bf16_f32 v50, v34, v35\n" \
    "v_smfmac_f32_16x16x64_bf16 v[44:47], v[56:59], a[240:247], v0\nv_cvt_pk_bf16_f32 v49, v36, v37\nv_cvt_pk_bf16_f32 v51, v38, v39\n" \
    "v_smfmac_f32_16x16x64_bf16 v[40:43], v[60:63], a[232:239], v0\nv_permlane16_swap_b32 v48, v49\nv_smfmac_f32_16x16x64_bf16 v[44:47], v[60:63], a[248:255], v0\n" \
    "v_permlane16_swap_b32 v50, v51\nv_smfmac_f32_16x16x64_bf16 v[40:43], v[64:67], v[92:99], v0\nbuffer_store_dwordx4 v[48:51], v8, s[64:67], s86 offen offset:512 sc1\n" \
    "v_mov_b64 v[32:33], 0\nv_mov_b64 v[34:35], 0\nv_smfmac_f32_16x16x64_bf16 v[44:47], v[64:67], v[108:115], v0\nv_mov_b64 v[36:37], 0\nv_mov_b64 v[38:39], 0\n" \
    "v_smfmac_f32_16x16x64_bf16 v[40:43], v[68:71], v[100:107], v0\nv_smfmac_f32_16x16x64_bf16 v[44:47], v[68:71], v[116:123], v0\ns_nop 2\n" \
    "v_smfmac_f32_16x16x64_bf16 v[32:35], v[56:59], v[124:131], v0\ns_nop 2\nv_cvt_pk_bf16_f32 v52, v40, v41\nv_cvt_pk_bf16_f32 v54, v42, v43\n" \
    "v_smfmac_f32_16x16x64_bf16 v[36:39], v[56:59], v[140:147], v0\nv_cvt_pk_bf16_f32 v53, v44, v45\nv_cvt_pk_bf16_f32 v55, v46, v47\n" \
    "v_smfmac_f32_16x16x64_bf16 v[32:35], v[60:63], v[132:139], v0\nv_permlane16_swap_b32 v52, v53\nv_smfmac_f32_16x16x64_bf16 v[36:39], v[60:63], v[148:155], v0\n" \
    "v_permlane16_swap_b32 v54, v55\nv_smfmac_f32_16x16x64_bf16 v[32:35], v[64:67], v[156:163], v0\nbuffer_store_dwordx4 v[52:55], v8, s[64:67], s87 offen offset:512 sc1\n" \
    "v_mov_b64 v[40:41], 0\nv_mov_b64 v[42:43], 0\nv_smfmac_f32_16x16x64_bf16 v[36:39], v[64:67], v[172:179], v0\nv_mov_b64 v[44:45], 0\nv_mov_b64 v[46:47], 0\n" \
    "v_smfmac_f32_16x16x64_bf16 v[32:35], v[68:71], v[164:171], v0\nv_smfmac_f32_16x16x64_bf16 v[36:39], v[68:71], v[180:187], v0\ns_nop 2\n" \
    "v_smfmac_f32_16x16x64_bf16 v[40:43], v[56:59], v[188:195], v0\ns_nop 2\nv_cvt_pk_bf16_f32 v48, v32, v33\nv_cvt_pk_bf16_f32 v50, v34, v35\n" \
    "v_smfmac_f32_16x16x64_bf16 v[44:47], v[56:59], v[204:211], v0\nv_cvt_pk_bf16_f32 v49, v36, v37\nv_cvt_pk_bf16_f32 v51, v38, v39\n" \
    "v_smfmac_f32_16x16x64_bf16 v[40:43], v[60:63], v[196:203], v0\nds_read_b128 v[56:59], v6 offset:4224\nv_permlane16_swap_b32 v48, v49\n" \
    "v_smfmac_f32_16x16x64_bf16 v[44:47], v[60:63], v[212:219], v0\nv_permlane16_swap_b32 v50, v51\nv_smfmac_f32_16x16x64_bf16 v[40:43], v[64:67], v[220:227], v0\n" \
    "ds_read_b128 v[60:63], v6 offset:5248\nbuffer_store_dwordx4 v[48:51], v8, s[64:67], s88 offen offset:512 sc1\nv_mov_b64 v[32:33], 0\nv_mov_b64 v[34:35], 0\n" \
    "v_smfmac_f32_16x16x64_bf16 v[44:47], v[64:67], v[236:243], v0\nv_mov_b64 v[36:37], 0\nv_mov_b64 v[38:39], 0\nv_smfmac_f32_16x16x64_bf16 v[40:43], v[68:71], v[228:235], v0\n" \
    "ds_read_b128 v[64:67], v6 offset:6272\nv_smfmac_f32_16x16x64_bf16 v[44:47], v[68:71], v[244:251], v0\ns_nop 2\ns_waitcnt lgkmcnt(2)\n" \
    "v_smfmac_f32_16x16x64_bf16 v[32:35], v[56:59], a[160:167], v0\nds_read_b128 v[68:71], v6 offset:7296\nv_cvt_pk_bf16_f32 v52, v40, v41\nv_cvt_pk_bf16_f32 v54, v42, v43\n" \
    "v_smfmac_f32_16x16x64_bf16 v[36:39], v[56:59], a[176:183], v0\ns_waitcnt lgkmcnt(2)\nv_cvt_pk_bf16_f32 v53, v44, v45\nv_cvt_pk_bf16_f32 v55, v46, v47\n" \
    "v_smfmac_f32_16x16x64_bf16 v[32:35], v[60:63], a[168:175], v0\nv_permlane16_swap_b32 v52, v53\nv_smfmac_f32_16x16x64_bf16 v[36:39], v[60:63], a[184:191], v0\n" \
    "s_waitcnt lgkmcnt(1)\nv_permlane16_swap_b32 v54, v55\nv_smfmac_f32_16x16x64_bf16 v[32:35], v[64:67], a[192:199], v0\n" \
    "buffer_store_dwordx4 v[52:55], v8, s[64:67], s89 offen offset:512 sc1\nv_mov_b64 v[40:41], 0\nv_mov_b64 v[42:43], 0\n" \
    "v_smfmac_f32_16x16x64_bf16 v[36:39], v[64:67], a[208:215], v0\ns_waitcnt lgkmcnt(0)\nv_mov_b64 v[44:45], 0\nv_mov_b64 v[46:47], 0\n" \
    "v_smfmac_f32_16x16x64_bf16 v[32:35], v[68:71], a[200:207], v0\nv_smfmac_f32_16x16x64_bf16 v[36:39], v[68:71], a[216:223], v0\ns_nop 2\n" \
    "v_smfmac_f32_16x16x64_bf16 v[40:43], v[56:59], a[224:231], v0\ns_nop 2\nv_cvt_pk_bf16_f32 v48, v32, v33\nv_cvt_pk_bf16_f32 v50, v34, v35\n" \
    "v_smfmac_f32_16x16x64_bf16 v[44:47], v[56:59], a[240:247], v0\nv_cvt_pk_bf16_f32 v49, v36, v37\nv_cvt_pk_bf16_f32 v51, v38, v39\n" \
    "v_smfmac_f32_16x16x64_bf16 v[40:43], v[60:63], a[232:239], v0\nv_permlane16_swap_b32 v48, v49\nv_smfmac_f32_16x16x64_bf16 v[44:47], v[60:63], a[248:255], v0\n" \
    "v_permlane16_swap_b32 v50, v51\nv_smfmac_f32_16x16x64_bf16 v[40:43], v[64:67], v[92:99], v0\nbuffer_store_dwordx4 v[48:51], v8, s[64:67], s86 offen offset:768 sc1\n" \
    "v_mov_b64 v[32:33], 0\nv_mov_b64 v[34:35], 0\nv_smfmac_f32_16x16x64_bf16 v[44:47], v[64:67], v[108:115], v0\nv_mov_b64 v[36:37], 0\nv_mov_b64 v[38:39], 0\n" \
    "v_smfmac_f32_16x16x64_bf16 v[40:43], v[68:71], v[100:107], v0\nv_smfmac_f32_16x16x64_bf16 v[44:47], v[68:71], v[116:123], v0\ns_nop 2\n" \
    "v_smfmac_f32_16x16x64_bf16 v[32:35], v[56:59], v[124:131], v0\ns_nop 2\nv_cvt_pk_bf16_f32 v52, v40, v41\nv_cvt_pk_bf16_f32 v54, v42, v43\n" \
    "v_smfmac_f32_16x16x64_bf16 v[36:39], v[56:59], v[140:147], v0\nv_cvt_pk_bf16_f32 v53, v44, v45\nv_cvt_pk_bf16_f32 v55, v46, v47\n" \
    "v_smfmac_f32_16x16x64_bf16 v[32:35], v[60:63], v[132:139], v0\nv_permlane16_swap_b32 v52, v53\nv_smfmac_f32_16x16x64_bf16 v[36:39], v[60:63], v[148:155], v0\n" \
    "v_permlane16_swap_b32 v54, v55\nv_smfmac_f32_16x16x64_bf16 v[32:35], v[64:67], v[156:163], v0\nbuffer_store_dwordx4 v[52:55], v8, s[64:67], s87 offen offset:768 sc1\n" \
    "v_mov_b64 v[40:41], 0\nv_mov_b64 v[42:43], 0\nv_smfmac_f32_16x16x64_bf16 v[36:39], v[64:67], v[172:179], v0\nv_mov_b64 v[44:45], 0\nv_mov_b64 v[46:47], 0\n" \
    "v_smfmac_f32_16x16x64_bf16 v[32:35], v[68:71], v[164:171], v0\nv_smfmac_f32_16x16x64_bf16 v[36:39], v[68:71], v[180:187], v0\ns_nop 2\n" \
    "v_smfmac_f32_16x16x64_bf16 v[40:43], v[56:59], v[188:195], v0\ns_nop 2\nv_cvt_pk_bf16_f32 v48, v32, v33\nv_cvt_pk_bf16_f32 v50, v34, v35\n" \
    "v_smfmac_f32_16x16x64_bf16 v[44:47], v[56:59], v[204:211], v0\nv_cvt_pk_bf16_f32 v49, v36, v37\nv_cvt_pk_bf16_f32 v51, v38, v39\n" \
    "v_smfmac_f32_16x16x64_bf16 v[40:43], v[60:63], v[196:203], v0\nv_permlane16_swap_b32 v48, v49\nv_smfmac_f32_16x16x64_bf16 v[44:47], v[60:63], v[212:219], v0\n" \
    "v_permlane16_swap_b32 v50, v51\nv_smfmac_f32_16x16x64_bf16 v[40:43], v[64:67], v[220:227], v0\nbuffer_store_dwordx4 v[48:51], v8, s[64:67], s88 offen offset:768 sc1\n" \
    "v_smfmac_f32_16x16x64_bf16 v[44:47], v[64:67], v[236:243], v0\nv_smfmac_f32_16x16x64_bf16 v[40:43], v[68:71], v[228:235], v0\n" \
    "v_smfmac_f32_16x16x64_bf16 v[44:47], v[68:71], v[244:251], v0\ns_nop 6\nv_cvt_pk_bf16_f32 v52, v40, v41\nv_cvt_pk_bf16_f32 v54, v42, v43\nv_cvt_pk_bf16_f32 v53, v44, v45\n" \
    "v_cvt_pk_bf16_f32 v55, v46, v47\ns_nop 0\nv_permlane16_swap_b32 v52, v53\nv_permlane16_swap_b32 v54, v55\n" \
    "buffer_store_dwordx4 v[52:55], v8, s[64:67], s89 offen offset:768 sc1\nbuffer_load_dword v13, off, s[64:67], s42 sc1\ns_waitcnt vmcnt(0)\nv_mov_b32 v15, s50\n" \
    "s_mov_b64 exec, 1\nbuffer_store_dword v15, off, s[64:67], s48 sc1\ns_mov_b64 exec, -1\ns_branch .Lv4_%=\n.Lv6_%=:\ns_endpgm\n.Lv16_%=:\ns_mov_b32 s75, 33024\n" \
    "buffer_load_dword v253, off, s[64:67], s75 sc1\ns_waitcnt vmcnt(0)\nv_readfirstlane_b32 s74, v253\ns_nop 0\ns_cmp_ge_u32 s74, 32\ns_cbranch_scc1 .Lv17_%=\ns_sleep 8\n" \
    "s_add_u32 s68, s68, 1\ns_and_b32 s69, s68, 63\ns_cbranch_scc1 .Lv16_%=\ns_cmp_gt_u32 s68, 0x400000\ns_cbranch_scc1 .Lv1_%=\nbuffer_load_dword v253, off, s[64:67], 0 sc1\n" \
    "s_waitcnt vmcnt(0)\nv_readfirstlane_b32 s69, v253\ns_nop 0\ns_cmp_eq_u32 s69, 1\ns_cbranch_scc0 .Lv16_%=\ns_endpgm\n.Lv18_%=:\ns_memrealtime s[72:73]\n" \
    "s_waitcnt lgkmcnt(0)\ns_mov_b32 s80, s72\ns_branch .Lv14_%=\n.Lv12_%=:\nbuffer_load_dword v13, off, s[64:67], s42 sc1\ns_waitcnt vmcnt(0)\ns_branch .Lv19_%=\n.Lv15_%=:\n" \
    "s_sleep 6\ns_add_u32 s68, s68, 1\ns_and_b32 s69, s68, 63\ns_cbranch_scc1 .Lv12_%=\ns_cmp_gt_u32 s68, 0x400000\ns_cbranch_scc1 .Lv1_%=\n" \
    "buffer_load_dword v253, off, s[64:67], 0 sc1\ns_waitcnt vmcnt(0)\nv_readfirstlane_b32 s69, v253\ns_nop 0\ns_cmp_eq_u32 s69, 1\ns_cbranch_scc0 .Lv12_%=\ns_endpgm\n" \
    ".Lv22_%=:\ns_add_u32 s80, s80, s43\ns_xor_b32 s81, s81, 0x1000\ns_add_u32 s68, s68, 1\ns_and_b32 s69, s68, 63\ns_cbranch_scc1 .Lv21_%=\ns_cmp_gt_u32 s68, 0x400000\n" \
    "s_cbranch_scc1 .Lv1_%=\nbuffer_load_dword v253, off, s[64:67], 0 sc1\ns_waitcnt vmcnt(0)\nv_readfirstlane_b32 s69, v253\ns_nop 0\ns_cmp_eq_u32 s69, 1\n" \
    "s_cbranch_scc0 .Lv21_%=\ns_endpgm\n.Lv25_%=:\ns_add_u32 s80, s80, s43\ns_xor_b32 s81, s81, 0x1000\ns_add_u32 s68, s68, 1\ns_and_b32 s69, s68, 63\ns_cbranch_scc1 .Lv24_%=\n" \
    "s_cmp_gt_u32 s68, 0x400000\ns_cbranch_scc1 .Lv1_%=\nbuffer_load_dword v253, off, s[64:67], 0 sc1\ns_waitcnt vmcnt(0)\nv_readfirstlane_b32 s69, v253\ns_nop 0\n" \
    "s_cmp_eq_u32 s69, 1\ns_cbranch_scc0 .Lv24_%=\ns_endpgm\n.Lv28_%=:\ns_add_u32 s80, s80, s43\ns_xor_b32 s81, s81, 0x1000\ns_add_u32 s68, s68, 1\ns_and_b32 s69, s68, 63\n" \
    "s_cbranch_scc1 .Lv27_%=\ns_cmp_gt_u32 s68, 0x400000\ns_cbranch_scc1 .Lv1_%=\nbuffer_load_dword v253, off, s[64:67], 0 sc1\ns_waitcnt vmcnt(0)\n" \
    "v_readfirstlane_b32 s69, v253\ns_nop 0\ns_cmp_eq_u32 s69, 1\ns_cbranch_scc0 .Lv27_%=\ns_endpgm\n.Lv31_%=:\ns_add_u32 s80, s80, s43\ns_xor_b32 s81, s81, 0x1000\n" \
    "s_add_u32 s68, s68, 1\ns_and_b32 s69, s68, 63\ns_cbranch_scc1 .Lv30_%=\ns_cmp_gt_u32 s68, 0x400000\ns_cbranch_scc1 .Lv1_%=\nbuffer_load_dword v253, off, s[64:67], 0 sc1\n" \
    "s_waitcnt vmcnt(0)\nv_readfirstlane_b32 s69, v253\ns_nop 0\ns_cmp_eq_u32 s69, 1\ns_cbranch_scc0 .Lv30_%=\ns_endpgm\n.Lv1_%=:\nv_mov_b32 v253, 1\n" \
    "buffer_store_dword v253, off, s[64:67], 0 sc1\ns_endpgm\n"
#define ASM_R \
    ".Lr2_%=:\ns_and_b32 s69, s32, 15\ns_lshl_b32 s69, s69, 21\ns_add_u32 s82, s42, s69\ns_lshr_b32 s69, s82, 8\ns_and_b32 s69, s69, 0x2000\ns_xor_b32 s82, s82, s69\n" \
    "s_and_b32 s69, s32, 15\ns_lshl_b32 s69, s69, 19\ns_add_u32 s83, s43, s69\ns_lshr_b32 s69, s83, 8\ns_and_b32 s69, s69, 0x2000\ns_xor_b32 s83, s83, s69\n" \
    "s_and_b32 s69, s32, 15\ns_lshl_b32 s69, s69, 18\ns_add_u32 s84, s44, s69\ns_lshr_b32 s69, s84, 8\ns_and_b32 s69, s69, 0x2000\ns_xor_b32 s84, s84, s69\n" \
    "s_and_b32 s69, s32, 15\ns_lshl_b32 s69, s69, 18\ns_add_u32 s94, s45, s69\ns_lshr_b32 s69, s94, 8\ns_and_b32 s69, s69, 0x2000\ns_xor_b32 s94, s94, s69\n" \
    "s_add_u32 s69, s32, 1\ns_and_b32 s69, s69, 15\ns_lshl_b32 s69, s69, 17\ns_add_u32 s92, s46, s69\ns_and_b32 s69, s32, 63\ns_lshl_b32 s69, s69, 19\ns_add_u32 s93, s47, s69\n" \
    "s_mov_b32 s68, 0\ns_cmp_lt_u32 s32, 2\ns_cbranch_scc1 .Lr6_%=\ns_sub_u32 s77, s32, 1\n.Lr4_%=:\nds_read_b32 v253, v10\ns_waitcnt lgkmcnt(0)\nv_readfirstlane_b32 s74, v253\n" \
    "s_nop 0\ns_and_b32 s75, s74, 127\ns_cmp_lt_u32 s75, s77\ns_cbranch_scc1 .Lr5_%=\ns_cmp_gt_u32 s75, s77\ns_cbranch_scc1 .Lr6_%=\ns_memrealtime s[72:73]\n" \
    "s_waitcnt lgkmcnt(0)\ns_lshr_b32 s69, s74, 8\ns_sub_u32 s69, s69, s72\ns_bfe_i32 s69, s69, 0x180000\ns_add_u32 s76, s72, s69\ns_add_u32 s76, s76, s58\n.Lr21_%=:\n" \
    "s_memrealtime s[72:73]\ns_waitcnt lgkmcnt(0)\ns_sub_u32 s72, s72, s76\ns_cmp_lt_i32 s72, 0\ns_cbranch_scc1 .Lr21_%=\n.Lr6_%=:\n" \
    "buffer_load_dword v253, v8, s[64:67], 0 offen sc1\ns_waitcnt vmcnt(0)\nv_cmp_ge_u32 vcc, s32, v253\ns_nop 0\ns_cbranch_vccnz .Lr7_%=\ns_cmp_eq_u32 s32, 0\n" \
    "s_cbranch_scc1 .Lr15_%=\n.Lr13_%=:\nds_read_b32 v253, v10\ns_waitcnt lgkmcnt(0)\nv_readfirstlane_b32 s74, v253\ns_nop 0\ns_and_b32 s75, s74, 127\ns_cmp_lt_u32 s75, s32\n" \
    "s_cbranch_scc1 .Lr14_%=\ns_and_b32 s57, s74, 128\ns_memrealtime s[72:73]\ns_waitcnt lgkmcnt(0)\ns_lshr_b32 s69, s74, 8\ns_sub_u32 s69, s69, s72\n" \
    "s_bfe_i32 s69, s69, 0x180000\ns_add_u32 s56, s72, s69\ns_sub_u32 s76, s56, s40\n.Lr22_%=:\ns_memrealtime s[72:73]\ns_waitcnt lgkmcnt(0)\ns_sub_u32 s72, s72, s76\n" \
    "s_cmp_lt_i32 s72, 0\ns_cbranch_scc1 .Lr22_%=\n.Lr15_%=:\nbuffer_load_dwordx4 a[0:3], v1, s[64:67], s82 offen offset:0 sc1\n" \
    "buffer_load_dwordx4 a[4:7], v1, s[64:67], s82 offen offset:1024 sc1\nbuffer_load_dwordx4 a[8:11], v1, s[64:67], s82 offen offset:2048 sc1\n" \
    "buffer_load_dwordx4 a[12:15], v1, s[64:67], s82 offen offset:3072 sc1\ns_add_u32 s82, s82, 4096\nbuffer_load_dwordx4 a[16:19], v1, s[64:67], s82 offen offset:0 sc1\n" \
    "buffer_load_dwordx4 a[20:23], v1, s[64:67], s82 offen offset:1024 sc1\nbuffer_load_dwordx4 a[24:27], v1, s[64:67], s82 offen offset:2048 sc1\n" \
    "buffer_load_dwordx4 a[28:31], v1, s[64:67], s82 offen offset:3072 sc1\ns_add_u32 s82, s82, 12288\nbuffer_load_dwordx4 a[32:35], v1, s[64:67], s82 offen offset:0 sc1\n" \
    "buffer_load_dwordx4 a[36:39], v1, s[64:67], s82 offen offset:1024 sc1\nbuffer_load_dwordx4 a[40:43], v1, s[64:67], s82 offen offset:2048 sc1\n" \
    "buffer_load_dwordx4 a[44:47], v1, s[64:67], s82 offen offset:3072 sc1\ns_add_u32 s82, s82, 4096\nbuffer_load_dwordx4 a[48:51], v1, s[64:67], s82 offen offset:0 sc1\n" \
    "buffer_load_dwordx4 a[52:55], v1, s[64:67], s82 offen offset:1024 sc1\nbuffer_load_dwordx4 a[56:59], v1, s[64:67], s82 offen offset:2048 sc1\n" \
    "buffer_load_dwordx4 a[60:63], v1, s[64:67], s82 offen offset:3072 sc1\ns_add_u32 s82, s82, 12288\nbuffer_load_dwordx4 a[64:67], v1, s[64:67], s82 offen offset:0 sc1\n" \
    "buffer_load_dwordx4 a[68:71], v1, s[64:67], s82 offen offset:1024 sc1\nbuffer_load_dwordx4 a[72:75], v1, s[64:67], s82 offen offset:2048 sc1\n" \
    "buffer_load_dwordx4 a[76:79], v1, s[64:67], s82 offen offset:3072 sc1\ns_add_u32 s82, s82, 4096\nbuffer_load_dwordx4 a[80:83], v1, s[64:67], s82 offen offset:0 sc1\n" \
    "buffer_load_dwordx4 a[84:87], v1, s[64:67], s82 offen offset:1024 sc1\nbuffer_load_dwordx4 a[88:91], v1, s[64:67], s82 offen offset:2048 sc1\n" \
    "buffer_load_dwordx4 a[92:95], v1, s[64:67], s82 offen offset:3072 sc1\ns_add_u32 s82, s82, 12288\nbuffer_load_dwordx4 a[96:99], v1, s[64:67], s82 offen offset:0 sc1\n" \
    "buffer_load_dwordx4 a[100:103], v1, s[64:67], s82 offen offset:1024 sc1\nbuffer_load_dwordx4 a[104:107], v1, s[64:67], s82 offen offset:2048 sc1\n" \
    "buffer_load_dwordx4 a[108:111], v1, s[64:67], s82 offen offset:3072 sc1\ns_add_u32 s82, s82, 4096\nbuffer_load_dwordx4 a[112:115], v1, s[64:67], s82 offen offset:0 sc1\n" \
    "buffer_load_dwordx4 a[116:119], v1, s[64:67], s82 offen offset:1024 sc1\nbuffer_load_dwordx4 a[120:123], v1, s[64:67], s82 offen offset:2048 sc1\n" \
    "buffer_load_dwordx4 a[124:127], v1, s[64:67], s82 offen offset:3072 sc1\nglobal_load_dwordx4 v[224:227], v7, s[54:55]\ns_cmp_lg_u32 s32, 0\ns_cbranch_scc1 .Lr3_%=\n" \
    "s_memrealtime s[72:73]\ns_waitcnt lgkmcnt(0)\ns_add_u32 s72, s72, s41\n.Lr20_%=:\ns_add_u32 s56, s56, s38\ns_xor_b32 s57, s57, 128\ns_sub_u32 s69, s56, s72\n" \
    "s_cmp_lt_i32 s69, 0\ns_cbranch_scc1 .Lr20_%=\ns_add_u32 s76, s56, s35\ns_cmp_eq_u32 s48, 0\ns_cbranch_scc1 .Lr23_%=\ns_lshl_b32 s74, s76, 8\ns_add_u32 s74, s74, s57\n" \
    "s_add_u32 s74, s74, s32\ns_add_u32 s74, s74, 1\nv_mov_b32 v253, s74\ns_xor_b32 s69, s48, 512\ns_mov_b64 exec, 1\nbuffer_store_dword v253, off, s[64:67], s69 sc1\n" \
    "s_mov_b64 exec, -1\n.Lr23_%=:\n.Lr3_%=:\ns_waitcnt vmcnt(0)\ns_sub_u32 s76, s56, s59\n.Lr25_%=:\ns_memrealtime s[72:73]\ns_waitcnt lgkmcnt(0)\ns_sub_u32 s72, s72, s76\n" \
    "s_cmp_lt_i32 s72, 0\ns_cbranch_scc1 .Lr25_%=\nbuffer_load_dwordx4 v[24:27], off, s[64:67], s60 sc1\ns_sub_u32 s76, s56, s34\ns_mov_b32 s77, 0\ns_waitcnt vmcnt(0)\n" \
    "v_readfirstlane_b32 s74, v24\ns_nop 0\ns_and_b32 s75, s74, 127\ns_cmp_lg_u32 s75, s32\ns_cbranch_scc1 .Lr26_%=\ns_lshr_b32 s69, s74, 8\ns_sub_u32 s69, s69, s76\n" \
    "s_bfe_i32 s69, s69, 0x180000\ns_cmp_le_i32 s69, s77\ns_cbranch_scc1 .Lr26_%=\ns_mov_b32 s77, s69\ns_and_b32 s57, s74, 128\n.Lr26_%=:\nv_readfirstlane_b32 s74, v25\n" \
    "s_nop 0\ns_and_b32 s75, s74, 127\ns_cmp_lg_u32 s75, s32\ns_cbranch_scc1 .Lr27_%=\ns_lshr_b32 s69, s74, 8\ns_sub_u32 s69, s69, s76\ns_bfe_i32 s69, s69, 0x180000\n" \
    "s_cmp_le_i32 s69, s77\ns_cbranch_scc1 .Lr27_%=\ns_mov_b32 s77, s69\ns_and_b32 s57, s74, 128\n.Lr27_%=:\nv_readfirstlane_b32 s74, v26\ns_nop 0\ns_and_b32 s75, s74, 127\n" \
    "s_cmp_lg_u32 s75, s32\ns_cbranch_scc1 .Lr28_%=\ns_lshr_b32 s69, s74, 8\ns_sub_u32 s69, s69, s76\ns_bfe_i32 s69, s69, 0x180000\ns_cmp_le_i32 s69, s77\n" \
    "s_cbranch_scc1 .Lr28_%=\ns_mov_b32 s77, s69\ns_and_b32 s57, s74, 128\n.Lr28_%=:\nv_readfirstlane_b32 s74, v27\ns_nop 0\ns_and_b32 s75, s74, 127\ns_cmp_lg_u32 s75, s32\n" \
    "s_cbranch_scc1 .Lr29_%=\ns_lshr_b32 s69, s74, 8\ns_sub_u32 s69, s69, s76\ns_bfe_i32 s69, s69, 0x180000\ns_cmp_le_i32 s69, s77\ns_cbranch_scc1 .Lr29_%=\ns_mov_b32 s77, s69\n" \
    "s_and_b32 s57, s74, 128\n.Lr29_%=:\ns_cmp_eq_u32 s77, 0\ns_cbranch_scc1 .Lr24_%=\ns_cmp_eq_u32 s32, 0\ns_cbranch_scc1 .Lr24_%=\ns_add_u32 s56, s56, s77\n" \
    "s_add_u32 s76, s56, s35\ns_cmp_eq_u32 s48, 0\ns_cbranch_scc1 .Lr30_%=\ns_lshl_b32 s74, s76, 8\ns_add_u32 s74, s74, s57\ns_add_u32 s74, s74, s32\ns_add_u32 s74, s74, 1\n" \
    "v_mov_b32 v253, s74\ns_xor_b32 s69, s48, 512\ns_mov_b64 exec, 1\nbuffer_store_dword v253, off, s[64:67], s69 sc1\ns_mov_b64 exec, -1\n.Lr30_%=:\n.Lr24_%=:\n" \
    "s_lshl_b32 s69, s57, 7\ns_xor_b32 s84, s84, s69\ns_lshl_b32 s69, s57, 5\ns_xor_b32 s92, s92, s69\ns_add_u32 s75, s83, 4096\ns_mov_b32 s68, 0\n.Lr8_%=:\n" \
    "s_add_u32 s85, s84, 0x8000\ns_add_u32 s86, s84, 0x10000\ns_add_u32 s87, s84, 0x18000\ns_sub_u32 s76, s56, s36\n.Lr31_%=:\ns_memrealtime s[72:73]\ns_waitcnt lgkmcnt(0)\n" \
    "s_sub_u32 s72, s72, s76\ns_cmp_lt_i32 s72, 0\ns_cbranch_scc1 .Lr31_%=\nbuffer_load_dwordx4 v[32:35], v1, s[64:67], s83 offen offset:0 sc1\n" \
    "buffer_load_dwordx4 v[36:39], v1, s[64:67], s83 offen offset:1024 sc1\nbuffer_load_dwordx4 v[40:43], v1, s[64:67], s83 offen offset:2048 sc1\n" \
    "buffer_load_dwordx4 v[44:47], v1, s[64:67], s83 offen offset:3072 sc1\nbuffer_load_dwordx4 v[48:51], v1, s[64:67], s75 offen offset:0 sc1\n" \
    "buffer_load_dwordx4 v[52:55], v1, s[64:67], s75 offen offset:1024 sc1\nbuffer_load_dwordx4 v[56:59], v1, s[64:67], s75 offen offset:2048 sc1\n" \
    "buffer_load_dwordx4 v[60:63], v1, s[64:67], s75 offen offset:3072 sc1\ns_cmp_eq_u32 s32, 63\ns_cbranch_scc1 .Lr10_%=\n.Lr32_%=:\ns_memrealtime s[72:73]\n" \
    "s_waitcnt lgkmcnt(0)\ns_sub_u32 s72, s72, s56\ns_cmp_lt_i32 s72, 0\ns_cbranch_scc1 .Lr32_%=\nbuffer_load_dwordx4 v[64:67], v1, s[64:67], s84 offen offset:0 sc1\n" \
    "buffer_load_dwordx4 v[68:71], v1, s[64:67], s84 offen offset:1024 sc1\nbuffer_load_dwordx4 v[72:75], v1, s[64:67], s84 offen offset:2048 sc1\n" \
    "buffer_load_dwordx4 v[76:79], v1, s[64:67], s84 offen offset:3072 sc1\nbuffer_load_dwordx2 v[80:81], v2, s[64:67], s84 offen offset:0 sc1\n" \
    "buffer_load_dwordx2 v[82:83], v2, s[64:67], s84 offen offset:128 sc1\nbuffer_load_dwordx4 v[84:87], v1, s[64:67], s85 offen offset:0 sc1\n" \
    "buffer_load_dwordx4 v[88:91], v1, s[64:67], s85 offen offset:1024 sc1\nbuffer_load_dwordx4 v[92:95], v1, s[64:67], s85 offen offset:2048 sc1\n" \
    "buffer_load_dwordx4 v[96:99], v1, s[64:67], s85 offen offset:3072 sc1\nbuffer_load_dwordx2 v[100:101], v2, s[64:67], s85 offen offset:0 sc1\n" \
    "buffer_load_dwordx2 v[102:103], v2, s[64:67], s85 offen offset:128 sc1\nbuffer_load_dwordx4 v[104:107], v1, s[64:67], s86 offen offset:0 sc1\n" \
    "buffer_load_dwordx4 v[108:111], v1, s[64:67], s86 offen offset:1024 sc1\nbuffer_load_dwordx4 v[112:115], v1, s[64:67], s86 offen offset:2048 sc1\n" \
    "buffer_load_dwordx4 v[116:119], v1, s[64:67], s86 offen offset:3072 sc1\nbuffer_load_dwordx2 v[120:121], v2, s[64:67], s86 offen offset:0 sc1\n" \
    "buffer_load_dwordx2 v[122:123], v2, s[64:67], s86 offen offset:128 sc1\nbuffer_load_dwordx4 v[124:127], v1, s[64:67], s87 offen offset:0 sc1\n" \
    "buffer_load_dwordx4 v[128:131], v1, s[64:67], s87 offen offset:1024 sc1\nbuffer_load_dwordx4 v[132:135], v1, s[64:67], s87 offen offset:2048 sc1\n" \
    "buffer_load_dwordx4 v[136:139], v1, s[64:67], s87 offen offset:3072 sc1\nbuffer_load_dwordx2 v[140:141], v2, s[64:67], s87 offen offset:0 sc1\n" \
    "buffer_load_dwordx2 v[142:143], v2, s[64:67], s87 offen offset:128 sc1\nv_mov_b64 v[24:25], 0\nv_mov_b64 v[26:27], 0\ns_xor_b32 s77, s92, 0x1000\ns_waitcnt vmcnt(23)\n" \
    "v_smfmac_f32_16x16x64_bf16 v[24:27], v[64:67], a[0:7], v0\ns_waitcnt vmcnt(22)\nv_smfmac_f32_16x16x64_bf16 v[24:27], v[68:71], a[8:15], v0\ns_waitcnt vmcnt(21)\n" \
    "v_smfmac_f32_16x16x64_bf16 v[24:27], v[72:75], a[16:23], v0\ns_waitcnt vmcnt(20)\nv_smfmac_f32_16x16x64_bf16 v[24:27], v[76:79], a[24:31], v0\ns_waitcnt vmcnt(18)\n" \
    "v_smfmac_f32_16x16x64_bf16 v[24:27], v[80:83], v[32:39], v0\ns_waitcnt vmcnt(17)\nv_smfmac_f32_16x16x64_bf16 v[24:27], v[84:87], a[32:39], v0\ns_waitcnt vmcnt(16)\n" \
    "v_smfmac_f32_16x16x64_bf16 v[24:27], v[88:91], a[40:47], v0\ns_waitcnt vmcnt(15)\nv_smfmac_f32_16x16x64_bf16 v[24:27], v[92:95], a[48:55], v0\ns_waitcnt vmcnt(14)\n" \
    "v_smfmac_f32_16x16x64_bf16 v[24:27], v[96:99], a[56:63], v0\ns_waitcnt vmcnt(12)\nv_smfmac_f32_16x16x64_bf16 v[24:27], v[100:103], v[40:47], v0\ns_waitcnt vmcnt(11)\n" \
    "v_smfmac_f32_16x16x64_bf16 v[24:27], v[104:107], a[64:71], v0\ns_waitcnt vmcnt(10)\nv_smfmac_f32_16x16x64_bf16 v[24:27], v[108:111], a[72:79], v0\ns_waitcnt vmcnt(9)\n" \
    "v_smfmac_f32_16x16x64_bf16 v[24:27], v[112:115], a[80:87], v0\ns_waitcnt vmcnt(8)\nv_smfmac_f32_16x16x64_bf16 v[24:27], v[116:119], a[88:95], v0\ns_waitcnt vmcnt(6)\n" \
    "v_smfmac_f32_16x16x64_bf16 v[24:27], v[120:123], v[48:55], v0\ns_waitcnt vmcnt(5)\nv_smfmac_f32_16x16x64_bf16 v[24:27], v[124:127], a[96:103], v0\ns_waitcnt vmcnt(4)\n" \
    "v_smfmac_f32_16x16x64_bf16 v[24:27], v[128:131], a[104:111], v0\ns_waitcnt vmcnt(3)\nv_smfmac_f32_16x16x64_bf16 v[24:27], v[132:135], a[112:119], v0\ns_waitcnt vmcnt(2)\n" \
    "v_smfmac_f32_16x16x64_bf16 v[24:27], v[136:139], a[120:127], v0\ns_waitcnt vmcnt(0)\nv_smfmac_f32_16x16x64_bf16 v[24:27], v[140:143], v[56:63], v0\ns_nop 7\n" \
    "v_permlane32_swap_b32 v24, v26\nv_permlane32_swap_b32 v25, v27\nv_pk_add_f32 v[24:25], v[24:25], v[26:27]\nv_cvt_pk_bf16_f32 v26, v24, v25\nds_write_b32 v3, v26\n" \
    "ds_read_b64_tr_b16 v[28:29], v4\nds_read_b64_tr_b16 v[30:31], v4 offset:64\nv_cmp_u_f32 vcc, v24, v25\ns_nop 0\ns_cbranch_vccnz .Lr17_%=\n.Lr16_%=:\ns_waitcnt lgkmcnt(0)\n" \
    "buffer_store_dwordx4 v[28:31], v6, s[64:67], s92 offen sc1\nbuffer_store_dwordx4 v[28:31], v6, s[64:67], s77 offen sc1\n.Lr10_%=:\ns_memrealtime s[72:73]\n" \
    "s_waitcnt lgkmcnt(0)\ns_add_u32 s76, s56, s35\n.Lr34_%=:\ns_sub_u32 s69, s76, s72\ns_cmp_ge_i32 s69, s37\ns_cbranch_scc1 .Lr35_%=\ns_add_u32 s76, s76, s38\n" \
    "s_xor_b32 s57, s57, 128\ns_branch .Lr34_%=\n.Lr35_%=:\ns_add_u32 s76, s76, s34\ns_lshl_b32 s74, s76, 8\ns_add_u32 s74, s74, s57\ns_add_u32 s74, s74, s32\n" \
    "s_add_u32 s74, s74, 1\nv_mov_b32 v253, s74\nds_write_b32 v10, v253\ns_add_u32 s76, s76, s35\ns_cmp_eq_u32 s48, 0\ns_cbranch_scc1 .Lr36_%=\ns_lshl_b32 s74, s76, 8\n" \
    "s_add_u32 s74, s74, s57\ns_add_u32 s74, s74, s32\ns_add_u32 s74, s74, 2\nv_mov_b32 v253, s74\ns_xor_b32 s69, s48, 0\ns_mov_b64 exec, 1\n" \
    "buffer_store_dword v253, off, s[64:67], s69 sc1\ns_mov_b64 exec, -1\n.Lr36_%=:\ns_cmp_ge_u32 s32, 62\ns_cselect_b32 s76, s61, s39\ns_add_u32 s76, s76, s56\n" \
    "s_mov_b32 s68, 0\ns_mov_b32 s80, 0\n.Lr37_%=:\ns_memrealtime s[72:73]\ns_waitcnt lgkmcnt(0)\ns_sub_u32 s72, s72, s76\ns_cmp_lt_i32 s72, 0\ns_cbranch_scc1 .Lr37_%=\n" \
    ".Lr11_%=:\ns_add_u32 s76, s94, 0x20000\nbuffer_load_dword v250, v13, s[64:67], s94 offen sc1\nbuffer_load_dword v251, v13, s[64:67], s76 offen sc1\nv_mov_b64 v[24:25], 0\n" \
    "v_mov_b64 v[26:27], 0\nv_mov_b64 v[28:29], 0\nv_mov_b64 v[30:31], 0\nv_mov_b32 v252, 0\ns_xor_b32 s69, s49, 0x0\ns_add_u32 s84, s94, s69\ns_add_u32 s85, s84, 0x4000\n" \
    "s_add_u32 s86, s84, 0x8000\ns_add_u32 s87, s84, 0xc000\nbuffer_load_dwordx4 v[64:67], v1, s[64:67], s84 offen offset:0 sc1\n" \
    "buffer_load_dwordx4 v[68:71], v1, s[64:67], s84 offen offset:1024 sc1\nbuffer_load_dwordx4 v[72:75], v1, s[64:67], s84 offen offset:2048 sc1\n" \
    "buffer_load_dwordx4 v[76:79], v1, s[64:67], s84 offen offset:3072 sc1\nbuffer_load_dwordx2 v[80:81], v2, s[64:67], s84 offen offset:0 sc1\n" \
    "buffer_load_dwordx2 v[82:83], v2, s[64:67], s84 offen offset:128 sc1\nbuffer_load_dwordx4 v[84:87], v1, s[64:67], s85 offen offset:0 sc1\n" \
    "buffer_load_dwordx4 v[88:91], v1, s[64:67], s85 offen offset:1024 sc1\nbuffer_load_dwordx4 v[92:95], v1, s[64:67], s85 offen offset:2048 sc1\n" \
    "buffer_load_dwordx4 v[96:99], v1, s[64:67], s85 offen offset:3072 sc1\nbuffer_load_dwordx2 v[100:101], v2, s[64:67], s85 offen offset:0 sc1\n" \
    "buffer_load_dwordx2 v[102:103], v2, s[64:67], s85 offen offset:128 sc1\nbuffer_load_dwordx4 v[104:107], v1, s[64:67], s86 offen offset:0 sc1\n" \
    "buffer_load_dwordx4 v[108:111], v1, s[64:67], s86 offen offset:1024 sc1\nbuffer_load_dwordx4 v[112:115], v1, s[64:67], s86 offen offset:2048 sc1\n" \
    "buffer_load_dwordx4 v[116:119], v1, s[64:67], s86 offen offset:3072 sc1\nbuffer_load_dwordx2 v[120:121], v2, s[64:67], s86 offen offset:0 sc1\n" \
    "buffer_load_dwordx2 v[122:123], v2, s[64:67], s86 offen offset:128 sc1\nbuffer_load_dwordx4 v[124:127], v1, s[64:67], s87 offen offset:0 sc1\n" \
    "buffer_load_dwordx4 v[128:131], v1, s[64:67], s87 offen offset:1024 sc1\nbuffer_load_dwordx4 v[132:135], v1, s[64:67], s87 offen offset:2048 sc1\n" \
    "buffer_load_dwordx4 v[136:139], v1, s[64:67], s87 offen offset:3072 sc1\nbuffer_load_dwordx2 v[140:141], v2, s[64:67], s87 offen offset:0 sc1\n" \
    "buffer_load_dwordx2 v[142:143], v2, s[64:67], s87 offen offset:128 sc1\ns_xor_b32 s69, s49, 0x10000\ns_add_u32 s88, s94, s69\ns_add_u32 s89, s88, 0x4000\n" \
    "s_add_u32 s90, s88, 0x8000\ns_add_u32 s91, s88, 0xc000\nbuffer_load_dwordx4 v[144:147], v1, s[64:67], s88 offen offset:0 sc1\n" \
    "buffer_load_dwordx4 v[148:151], v1, s[64:67], s88 offen offset:1024 sc1\nbuffer_load_dwordx4 v[152:155], v1, s[64:67], s88 offen offset:2048 sc1\n" \
    "buffer_load_dwordx4 v[156:159], v1, s[64:67], s88 offen offset:3072 sc1\nbuffer_load_dwordx2 v[160:161], v2, s[64:67], s88 offen offset:0 sc1\n" \
    "buffer_load_dwordx2 v[162:163], v2, s[64:67], s88 offen offset:128 sc1\nbuffer_load_dwordx4 v[164:167], v1, s[64:67], s89 offen offset:0 sc1\n" \
    "buffer_load_dwordx4 v[168:171], v1, s[64:67], s89 offen offset:1024 sc1\nbuffer_load_dwordx4 v[172:175], v1, s[64:67], s89 offen offset:2048 sc1\n" \
    "buffer_load_dwordx4 v[176:179], v1, s[64:67], s89 offen offset:3072 sc1\nbuffer_load_dwordx2 v[180:181], v2, s[64:67], s89 offen offset:0 sc1\n" \
    "buffer_load_dwordx2 v[182:183], v2, s[64:67], s89 offen offset:128 sc1\nbuffer_load_dwordx4 v[184:187], v1, s[64:67], s90 offen offset:0 sc1\n" \
    "buffer_load_dwordx4 v[188:191], v1, s[64:67], s90 offen offset:1024 sc1\ns_waitcnt vmcnt(38)\nv_max_u32 v250, v250, v251\nv_cmp_eq_u32 vcc, -1, v250\ns_nop 0\n" \
    "s_cbranch_vccnz .Lr38_%=\ns_waitcnt vmcnt(37)\nv_smfmac_f32_16x16x64_bf16 v[24:27], v[64:67], a[0:7], v0\n" \
    "buffer_load_dwordx4 v[192:195], v1, s[64:67], s90 offen offset:2048 sc1\ns_waitcnt vmcnt(37)\nv_smfmac_f32_16x16x64_bf16 v[24:27], v[68:71], a[8:15], v0\n" \
    "buffer_load_dwordx4 v[196:199], v1, s[64:67], s90 offen offset:3072 sc1\ns_waitcnt vmcnt(37)\nv_smfmac_f32_16x16x64_bf16 v[24:27], v[72:75], a[16:23], v0\n" \
    "buffer_load_dwordx2 v[200:201], v2, s[64:67], s90 offen offset:0 sc1\nbuffer_load_dwordx2 v[202:203], v2, s[64:67], s90 offen offset:128 sc1\ns_waitcnt vmcnt(38)\n" \
    "v_smfmac_f32_16x16x64_bf16 v[24:27], v[76:79], a[24:31], v0\nbuffer_load_dwordx4 v[204:207], v1, s[64:67], s91 offen offset:0 sc1\ns_waitcnt vmcnt(37)\n" \
    "v_smfmac_f32_16x16x64_bf16 v[24:27], v[80:83], v[32:39], v0\nbuffer_load_dwordx4 v[208:211], v1, s[64:67], s91 offen offset:1024 sc1\ns_waitcnt vmcnt(37)\n" \
    "v_smfmac_f32_16x16x64_bf16 v[24:27], v[84:87], a[32:39], v0\nbuffer_load_dwordx4 v[212:215], v1, s[64:67], s91 offen offset:2048 sc1\ns_waitcnt vmcnt(37)\n" \
    "v_smfmac_f32_16x16x64_bf16 v[24:27], v[88:91], a[40:47], v0\nbuffer_load_dwordx4 v[216:219], v1, s[64:67], s91 offen offset:3072 sc1\ns_waitcnt vmcnt(37)\n" \
    "v_smfmac_f32_16x16x64_bf16 v[24:27], v[92:95], a[48:55], v0\nbuffer_load_dwordx2 v[220:221], v2, s[64:67], s91 offen offset:0 sc1\n" \
    "buffer_load_dwordx2 v[222:223], v2, s[64:67], s91 offen offset:128 sc1\ns_xor_b32 s69, s49, 0x20000\ns_waitcnt vmcnt(38)\n" \
    "v_smfmac_f32_16x16x64_bf16 v[24:27], v[96:99], a[56:63], v0\ns_add_u32 s84, s94, s69\ns_add_u32 s85, s84, 0x4000\ns_add_u32 s86, s84, 0x8000\ns_waitcnt vmcnt(36)\n" \
    "v_smfmac_f32_16x16x64_bf16 v[24:27], v[100:103], v[40:47], v0\ns_add_u32 s87, s84, 0xc000\nbuffer_load_dwordx4 v[64:67], v1, s[64:67], s84 offen offset:0 sc1\n" \
    "buffer_load_dwordx4 v[68:71], v1, s[64:67], s84 offen offset:1024 sc1\ns_waitcnt vmcnt(37)\nv_smfmac_f32_16x16x64_bf16 v[24:27], v[104:107], a[64:71], v0\n" \
    "buffer_load_dwordx4 v[72:75], v1, s[64:67], s84 offen offset:2048 sc1\ns_waitcnt vmcnt(37)\nv_smfmac_f32_16x16x64_bf16 v[24:27], v[108:111], a[72:79], v0\n" \
    "buffer_load_dwordx4 v[76:79], v1, s[64:67], s84 offen offset:3072 sc1\ns_waitcnt vmcnt(37)\nv_smfmac_f32_16x16x64_bf16 v[24:27], v[112:115], a[80:87], v0\n" \
    "buffer_load_dwordx2 v[80:81], v2, s[64:67], s84 offen offset:0 sc1\nbuffer_load_dwordx2 v[82:83], v2, s[64:67], s84 offen offset:128 sc1\ns_waitcnt vmcnt(38)\n" \
    "v_smfmac_f32_16x16x64_bf16 v[24:27], v[116:119], a[88:95], v0\nbuffer_load_dwordx4 v[84:87], v1, s[64:67], s85 offen offset:0 sc1\ns_waitcnt vmcnt(37)\n" \
    "v_smfmac_f32_16x16x64_bf16 v[24:27], v[120:123], v[48:55], v0\nbuffer_load_dwordx4 v[88:91], v1, s[64:67], s85 offen offset:1024 sc1\ns_waitcnt vmcnt(37)\n" \
    "v_smfmac_f32_16x16x64_bf16 v[24:27], v[124:127], a[96:103], v0\nbuffer_load_dwordx4 v[92:95], v1, s[64:67], s85 offen offset:2048 sc1\ns_waitcnt vmcnt(37)\n" \
    "v_smfmac_f32_16x16x64_bf16 v[24:27], v[128:131], a[104:111], v0\nbuffer_load_dwordx4 v[96:99], v1, s[64:67], s85 offen offset:3072 sc1\ns_waitcnt vmcnt(37)\n" \
    "v_smfmac_f32_16x16x64_bf16 v[24:27], v[132:135], a[112:119], v0\nbuffer_load_dwordx2 v[100:101], v2, s[64:67], s85 offen offset:0 sc1\n" \
    "buffer_load_dwordx2 v[102:103], v2, s[64:67], s85 offen offset:128 sc1\ns_waitcnt vmcnt(38)\nv_smfmac_f32_16x16x64_bf16 v[24:27], v[136:139], a[120:127], v0\n" \
    "buffer_load_dwordx4 v[104:107], v1, s[64:67], s86 offen offset:0 sc1\ns_waitcnt vmcnt(37)\nv_smfmac_f32_16x16x64_bf16 v[24:27], v[140:143], v[56:63], v0\n" \
    "buffer_load_dwordx4 v[108:111], v1, s[64:67], s86 offen offset:1024 sc1\ns_nop 1\ns_waitcnt vmcnt(37)\nv_smfmac_f32_16x16x64_bf16 v[28:31], v[144:147], a[0:7], v0\n" \
    "buffer_load_dwordx4 v[112:115], v1, s[64:67], s86 offen offset:2048 sc1\ns_nop 1\ns_waitcnt vmcnt(37)\nv_permlane32_swap_b32 v24, v26\n" \
    "v_smfmac_f32_16x16x64_bf16 v[28:31], v[148:151], a[8:15], v0\nbuffer_load_dwordx4 v[116:119], v1, s[64:67], s86 offen offset:3072 sc1\ns_waitcnt vmcnt(37)\n" \
    "v_permlane32_swap_b32 v25, v27\nv_smfmac_f32_16x16x64_bf16 v[28:31], v[152:155], a[16:23], v0\nbuffer_load_dwordx2 v[120:121], v2, s[64:67], s86 offen offset:0 sc1\n" \
    "buffer_load_dwordx2 v[122:123], v2, s[64:67], s86 offen offset:128 sc1\ns_waitcnt vmcnt(38)\nv_add_f32 v24, v24, v26\n" \
    "v_smfmac_f32_16x16x64_bf16 v[28:31], v[156:159], a[24:31], v0\nbuffer_load_dwordx4 v[124:127], v1, s[64:67], s87 offen offset:0 sc1\ns_waitcnt vmcnt(37)\n" \
    "v_add_f32 v25, v25, v27\nv_smfmac_f32_16x16x64_bf16 v[28:31], v[160:163], v[32:39], v0\nbuffer_load_dwordx4 v[128:131], v1, s[64:67], s87 offen offset:1024 sc1\n" \
    "s_waitcnt vmcnt(37)\nv_fma_f32 v252, v24, v25, v252\nv_smfmac_f32_16x16x64_bf16 v[28:31], v[164:167], a[32:39], v0\n" \
    "buffer_load_dwordx4 v[132:135], v1, s[64:67], s87 offen offset:2048 sc1\ns_waitcnt vmcnt(37)\nv_cvt_pk_bf16_f32 v24, v24, v25\n" \
    "v_smfmac_f32_16x16x64_bf16 v[28:31], v[168:171], a[40:47], v0\nbuffer_load_dwordx4 v[136:139], v1, s[64:67], s87 offen offset:3072 sc1\nds_write_b32 v3, v24 offset:0\n" \
    "s_waitcnt vmcnt(37)\nv_mov_b64 v[26:27], 0\nv_smfmac_f32_16x16x64_bf16 v[28:31], v[172:175], a[48:55], v0\n" \
    "buffer_load_dwordx2 v[140:141], v2, s[64:67], s87 offen offset:0 sc1\nbuffer_load_dwordx2 v[142:143], v2, s[64:67], s87 offen offset:128 sc1\ns_xor_b32 s69, s49, 0x30000\n" \
    "s_waitcnt vmcnt(38)\nv_mov_b64 v[24:25], 0\nv_smfmac_f32_16x16x64_bf16 v[28:31], v[176:179], a[56:63], v0\ns_add_u32 s88, s94, s69\ns_add_u32 s89, s88, 0x4000\n" \
    "s_add_u32 s90, s88, 0x8000\ns_waitcnt vmcnt(36)\nv_smfmac_f32_16x16x64_bf16 v[28:31], v[180:183], v[40:47], v0\ns_add_u32 s91, s88, 0xc000\n" \
    "buffer_load_dwordx4 v[144:147], v1, s[64:67], s88 offen offset:0 sc1\nbuffer_load_dwordx4 v[148:151], v1, s[64:67], s88 offen offset:1024 sc1\ns_waitcnt vmcnt(37)\n" \
    "v_smfmac_f32_16x16x64_bf16 v[28:31], v[184:187], a[64:71], v0\nbuffer_load_dwordx4 v[152:155], v1, s[64:67], s88 offen offset:2048 sc1\ns_waitcnt vmcnt(37)\n" \
    "v_smfmac_f32_16x16x64_bf16 v[28:31], v[188:191], a[72:79], v0\nbuffer_load_dwordx4 v[156:159], v1, s[64:67], s88 offen offset:3072 sc1\ns_waitcnt vmcnt(37)\n" \
    "v_smfmac_f32_16x16x64_bf16 v[28:31], v[192:195], a[80:87], v0\nbuffer_load_dwordx2 v[160:161], v2, s[64:67], s88 offen offset:0 sc1\n" \
    "buffer_load_dwordx2 v[162:163], v2, s[64:67], s88 offen offset:128 sc1\ns_waitcnt vmcnt(38)\nv_smfmac_f32_16x16x64_bf16 v[28:31], v[196:199], a[88:95], v0\n" \
    "buffer_load_dwordx4 v[164:167], v1, s[64:67], s89 offen offset:0 sc1\ns_waitcnt vmcnt(37)\nv_smfmac_f32_16x16x64_bf16 v[28:31], v[200:203], v[48:55], v0\n" \
    "buffer_load_dwordx4 v[168:171], v1, s[64:67], s89 offen offset:1024 sc1\ns_waitcnt vmcnt(37)\nv_smfmac_f32_16x16x64_bf16 v[28:31], v[204:207], a[96:103], v0\n" \
    "buffer_load_dwordx4 v[172:175], v1, s[64:67], s89 offen offset:2048 sc1\ns_waitcnt vmcnt(37)\nv_smfmac_f32_16x16x64_bf16 v[28:31], v[208:211], a[104:111], v0\n" \
    "buffer_load_dwordx4 v[176:179], v1, s[64:67], s89 offen offset:3072 sc1\ns_waitcnt vmcnt(37)\nv_smfmac_f32_16x16x64_bf16 v[28:31], v[212:215], a[112:119], v0\n" \
    "buffer_load_dwordx2 v[180:181], v2, s[64:67], s89 offen offset:0 sc1\nbuffer_load_dwordx2 v[182:183], v2, s[64:67], s89 offen offset:128 sc1\ns_waitcnt vmcnt(38)\n" \
    "v_smfmac_f32_16x16x64_bf16 v[28:31], v[216:219], a[120:127], v0\nbuffer_load_dwordx4 v[184:187], v1, s[64:67], s90 offen offset:0 sc1\ns_waitcnt vmcnt(37)\n" \
    "v_smfmac_f32_16x16x64_bf16 v[28:31], v[220:223], v[56:63], v0\nbuffer_load_dwordx4 v[188:191], v1, s[64:67], s90 offen offset:1024 sc1\ns_nop 1\ns_waitcnt vmcnt(37)\n" \
    "v_smfmac_f32_16x16x64_bf16 v[24:27], v[64:67], a[0:7], v0\nbuffer_load_dwordx4 v[192:195], v1, s[64:67], s90 offen offset:2048 sc1\ns_nop 1\ns_waitcnt vmcnt(37)\n" \
    "v_permlane32_swap_b32 v28, v30\nv_smfmac_f32_16x16x64_bf16 v[24:27], v[68:71], a[8:15], v0\nbuffer_load_dwordx4 v[196:199], v1, s[64:67], s90 offen offset:3072 sc1\n" \
    "s_waitcnt vmcnt(37)\nv_permlane32_swap_b32 v29, v31\nv_smfmac_f32_16x16x64_bf16 v[24:27], v[72:75], a[16:23], v0\n" \
    "buffer_load_dwordx2 v[200:201], v2, s[64:67], s90 offen offset:0 sc1\nbuffer_load_dwordx2 v[202:203], v2, s[64:67], s90 offen offset:128 sc1\ns_waitcnt vmcnt(38)\n" \
    "v_add_f32 v28, v28, v30\nv_smfmac_f32_16x16x64_bf16 v[24:27], v[76:79], a[24:31], v0\nbuffer_load_dwordx4 v[204:207], v1, s[64:67], s91 offen offset:0 sc1\n" \
    "s_waitcnt vmcnt(37)\nv_add_f32 v29, v29, v31\nv_smfmac_f32_16x16x64_bf16 v[24:27], v[80:83], v[32:39], v0\n" \
    "buffer_load_dwordx4 v[208:211], v1, s[64:67], s91 offen offset:1024 sc1\ns_waitcnt vmcnt(37)\nv_fma_f32 v252, v28, v29, v252\n" \
    "v_smfmac_f32_16x16x64_bf16 v[24:27], v[84:87], a[32:39], v0\nbuffer_load_dwordx4 v[212:215], v1, s[64:67], s91 offen offset:2048 sc1\ns_waitcnt vmcnt(37)\n" \
    "v_cvt_pk_bf16_f32 v28, v28, v29\nv_smfmac_f32_16x16x64_bf16 v[24:27], v[88:91], a[40:47], v0\nbuffer_load_dwordx4 v[216:219], v1, s[64:67], s91 offen offset:3072 sc1\n" \
    "ds_write_b32 v3, v28 offset:256\ns_waitcnt vmcnt(37)\nv_mov_b64 v[30:31], 0\nv_smfmac_f32_16x16x64_bf16 v[24:27], v[92:95], a[48:55], v0\n" \
    "buffer_load_dwordx2 v[220:221], v2, s[64:67], s91 offen offset:0 sc1\nbuffer_load_dwordx2 v[222:223], v2, s[64:67], s91 offen offset:128 sc1\ns_waitcnt vmcnt(38)\n" \
    "v_mov_b64 v[28:29], 0\nv_smfmac_f32_16x16x64_bf16 v[24:27], v[96:99], a[56:63], v0\ns_waitcnt vmcnt(36)\nv_smfmac_f32_16x16x64_bf16 v[24:27], v[100:103], v[40:47], v0\n" \
    "s_waitcnt vmcnt(35)\nv_smfmac_f32_16x16x64_bf16 v[24:27], v[104:107], a[64:71], v0\ns_waitcnt vmcnt(34)\nv_smfmac_f32_16x16x64_bf16 v[24:27], v[108:111], a[72:79], v0\n" \
    "s_waitcnt vmcnt(33)\nv_smfmac_f32_16x16x64_bf16 v[24:27], v[112:115], a[80:87], v0\ns_waitcnt vmcnt(32)\nv_smfmac_f32_16x16x64_bf16 v[24:27], v[116:119], a[88:95], v0\n" \
    "s_waitcnt vmcnt(30)\nv_smfmac_f32_16x16x64_bf16 v[24:27], v[120:123], v[48:55], v0\ns_waitcnt vmcnt(29)\nv_smfmac_f32_16x16x64_bf16 v[24:27], v[124:127], a[96:103], v0\n" \
    "s_waitcnt vmcnt(28)\nv_smfmac_f32_16x16x64_bf16 v[24:27], v[128:131], a[104:111], v0\ns_waitcnt vmcnt(27)\nv_smfmac_f32_16x16x64_bf16 v[24:27], v[132:135], a[112:119], v0\n" \
    "s_waitcnt vmcnt(26)\nv_smfmac_f32_16x16x64_bf16 v[24:27], v[136:139], a[120:127], v0\ns_waitcnt vmcnt(24)\nv_smfmac_f32_16x16x64_bf16 v[24:27], v[140:143], v[56:63], v0\n" \
    "s_nop 2\ns_waitcnt vmcnt(23)\nv_smfmac_f32_16x16x64_bf16 v[28:31], v[144:147], a[0:7], v0\ns_nop 2\ns_waitcnt vmcnt(22)\nv_permlane32_swap_b32 v24, v26\n" \
    "v_smfmac_f32_16x16x64_bf16 v[28:31], v[148:151], a[8:15], v0\ns_waitcnt vmcnt(21)\nv_permlane32_swap_b32 v25, v27\n" \
    "v_smfmac_f32_16x16x64_bf16 v[28:31], v[152:155], a[16:23], v0\ns_waitcnt vmcnt(20)\nv_add_f32 v24, v24, v26\nv_smfmac_f32_16x16x64_bf16 v[28:31], v[156:159], a[24:31], v0\n" \
    "s_waitcnt vmcnt(18)\nv_add_f32 v25, v25, v27\nv_smfmac_f32_16x16x64_bf16 v[28:31], v[160:163], v[32:39], v0\ns_waitcnt vmcnt(17)\nv_fma_f32 v252, v24, v25, v252\n" \
    "v_smfmac_f32_16x16x64_bf16 v[28:31], v[164:167], a[32:39], v0\ns_waitcnt vmcnt(16)\nv_cvt_pk_bf16_f32 v24, v24, v25\n" \
    "v_smfmac_f32_16x16x64_bf16 v[28:31], v[168:171], a[40:47], v0\nds_write_b32 v3, v24 offset:512\ns_waitcnt vmcnt(15)\nv_mov_b64 v[26:27], 0\n" \
    "v_smfmac_f32_16x16x64_bf16 v[28:31], v[172:175], a[48:55], v0\ns_waitcnt vmcnt(14)\nv_mov_b64 v[24:25], 0\nv_smfmac_f32_16x16x64_bf16 v[28:31], v[176:179], a[56:63], v0\n" \
    "s_waitcnt vmcnt(12)\nv_smfmac_f32_16x16x64_bf16 v[28:31], v[180:183], v[40:47], v0\ns_waitcnt vmcnt(11)\nv_smfmac_f32_16x16x64_bf16 v[28:31], v[184:187], a[64:71], v0\n" \
    "s_waitcnt vmcnt(10)\nv_smfmac_f32_16x16x64_bf16 v[28:31], v[188:191], a[72:79], v0\ns_waitcnt vmcnt(9)\nv_smfmac_f32_16x16x64_bf16 v[28:31], v[192:195], a[80:87], v0\n" \
    "s_waitcnt vmcnt(8)\nv_smfmac_f32_16x16x64_bf16 v[28:31], v[196:199], a[88:95], v0\ns_waitcnt vmcnt(6)\nv_smfmac_f32_16x16x64_bf16 v[28:31], v[200:203], v[48:55], v0\n" \
    "s_waitcnt vmcnt(5)\nv_smfmac_f32_16x16x64_bf16 v[28:31], v[204:207], a[96:103], v0\ns_waitcnt vmcnt(4)\nv_smfmac_f32_16x16x64_bf16 v[28:31], v[208:211], a[104:111], v0\n" \
    "s_waitcnt vmcnt(3)\nv_smfmac_f32_16x16x64_bf16 v[28:31], v[212:215], a[112:119], v0\ns_waitcnt vmcnt(2)\nv_smfmac_f32_16x16x64_bf16 v[28:31], v[216:219], a[120:127], v0\n" \
    "s_waitcnt vmcnt(0)\nv_smfmac_f32_16x16x64_bf16 v[28:31], v[220:223], v[56:63], v0\ns_nop 7\nv_permlane32_swap_b32 v28, v30\nv_permlane32_swap_b32 v29, v31\n" \
    "v_add_f32 v28, v28, v30\nv_add_f32 v29, v29, v31\nv_fma_f32 v252, v28, v29, v252\nv_cvt_pk_bf16_f32 v28, v28, v29\nv_cmp_u_f32 vcc, v252, v252\n" \
    "ds_write_b32 v3, v28 offset:768\nds_read_b64_tr_b16 v[24:25], v5\nds_read_b64_tr_b16 v[26:27], v5 offset:64\ns_cbranch_vccnz .Lr12_%=\n.Lr18_%=:\ns_cmp_lt_u32 s32, 4\n" \
    "s_cbranch_scc1 .Lr39_%=\n.Lr40_%=:\nv_lshlrev_b32 v72, 16, v224\nv_and_b32 v73, 0xffff0000, v224\nv_lshlrev_b32 v74, 16, v225\nv_and_b32 v75, 0xffff0000, v225\n" \
    "v_lshlrev_b32 v76, 16, v226\nv_and_b32 v77, 0xffff0000, v226\nv_lshlrev_b32 v78, 16, v227\nv_and_b32 v79, 0xffff0000, v227\ns_xor_b32 s77, s93, 0x2000\n" \
    "s_waitcnt lgkmcnt(0)\nv_lshlrev_b32 v64, 16, v24\nv_and_b32 v65, 0xffff0000, v24\nv_lshlrev_b32 v66, 16, v25\nv_and_b32 v67, 0xffff0000, v25\nv_lshlrev_b32 v68, 16, v26\n" \
    "v_and_b32 v69, 0xffff0000, v26\nv_lshlrev_b32 v70, 16, v27\nv_and_b32 v71, 0xffff0000, v27\nv_mul_f32 v64, v64, v16\nv_mul_f32 v65, v65, v17\nv_mul_f32 v66, v66, v18\n" \
    "v_mul_f32 v67, v67, v19\nv_mul_f32 v68, v68, v20\nv_mul_f32 v69, v69, v21\nv_mul_f32 v70, v70, v22\nv_mul_f32 v71, v71, v23\nv_cvt_pk_bf16_f32 v64, 0, v64\n" \
    "v_cvt_pk_bf16_f32 v65, 0, v65\nv_cvt_pk_bf16_f32 v66, 0, v66\nv_cvt_pk_bf16_f32 v67, 0, v67\nv_cvt_pk_bf16_f32 v68, 0, v68\nv_cvt_pk_bf16_f32 v69, 0, v69\n" \
    "v_cvt_pk_bf16_f32 v70, 0, v70\nv_cvt_pk_bf16_f32 v71, 0, v71\nv_add_f32 v64, v64, v72\nv_add_f32 v65, v65, v73\nv_add_f32 v66, v66, v74\nv_add_f32 v67, v67, v75\n" \
    "v_add_f32 v68, v68, v76\nv_add_f32 v69, v69, v77\nv_add_f32 v70, v70, v78\nv_add_f32 v71, v71, v79\nv_cvt_pk_bf16_f32 v24, v64, v65\nv_cvt_pk_bf16_f32 v25, v66, v67\n" \
    "v_cvt_pk_bf16_f32 v26, v68, v69\nv_cvt_pk_bf16_f32 v27, v70, v71\nv_min_u32 v28, -2, v24\nv_min_u32 v29, -2, v25\nv_min_u32 v30, -2, v26\nv_min_u32 v31, -2, v27\n" \
    "s_cmp_lt_u32 s32, 62\ns_cbranch_scc1 .Lr41_%=\nbuffer_store_dwordx4 v[28:31], v1, s[64:67], s93 offen sc1\nbuffer_store_dwordx4 v[28:31], v1, s[64:67], s77 offen sc1\n" \
    ".Lr41_%=:\nv_lshlrev_b32 v64, 16, v24\nv_and_b32 v65, 0xffff0000, v24\nv_lshlrev_b32 v66, 16, v25\nv_and_b32 v67, 0xffff0000, v25\nv_lshlrev_b32 v68, 16, v26\n" \
    "v_and_b32 v69, 0xffff0000, v26\nv_lshlrev_b32 v70, 16, v27\nv_and_b32 v71, 0xffff0000, v27\nv_add_f32 v72, v64, v65\nv_add_f32 v73, v66, v67\nv_add_f32 v74, v68, v69\n" \
    "v_add_f32 v75, v70, v71\nv_add_f32 v72, v72, v73\nv_add_f32 v74, v74, v75\nv_add_f32 v76, v72, v74\nv_add_f32 v77, v72, v74\ns_nop 1\nv_permlane32_swap_b32 v76, v77\n" \
    "v_add_f32 v76, v76, v77\nv_mul_f32 v76, 0x3d800000, v76\nv_sub_f32 v64, v64, v76\nv_sub_f32 v65, v65, v76\nv_sub_f32 v66, v66, v76\nv_sub_f32 v67, v67, v76\n" \
    "v_sub_f32 v68, v68, v76\nv_sub_f32 v69, v69, v76\nv_sub_f32 v70, v70, v76\nv_sub_f32 v71, v71, v76\nv_mul_f32 v72, v64, v64\nv_mul_f32 v73, v65, v65\n" \
    "v_fmac_f32 v72, v66, v66\nv_fmac_f32 v73, v67, v67\nv_fmac_f32 v72, v68, v68\nv_fmac_f32 v73, v69, v69\nv_fmac_f32 v72, v70, v70\nv_fmac_f32 v73, v71, v71\n" \
    "v_add_f32 v74, v72, v73\nv_add_f32 v75, v72, v73\ns_nop 1\nv_permlane32_swap_b32 v74, v75\nv_add_f32 v77, v74, v75\nv_min_u32 v76, -2, v76\nv_min_u32 v77, -2, v77\n" \
    "s_cmp_ge_u32 s32, 62\ns_cbranch_scc1 .Lr42_%=\ns_add_u32 s76, s32, 2\n.Lr44_%=:\nds_read_b32 v253, v10\ns_waitcnt lgkmcnt(0)\nv_readfirstlane_b32 s74, v253\ns_nop 0\n" \
    "s_and_b32 s75, s74, 127\ns_cmp_lt_u32 s75, s76\ns_cbranch_scc1 .Lr45_%=\nbuffer_store_dwordx4 v[28:31], v1, s[64:67], s93 offen sc1\n" \
    "buffer_store_dwordx4 v[28:31], v1, s[64:67], s77 offen sc1\n.Lr42_%=:\nbuffer_store_dwordx2 v[76:77], v9, s[64:67], s93 offen sc1\ns_cmp_ge_u32 s32, 56\n" \
    "s_cbranch_scc1 .Lr46_%=\nv_mov_b64 v[80:81], -1\nv_mov_b64 v[82:83], -1\ns_add_u32 s69, s32, 9\ns_and_b32 s69, s69, 15\ns_lshl_b32 s69, s69, 17\ns_add_u32 s92, s46, s69\n" \
    "s_xor_b32 s74, s92, 0x1000\nbuffer_store_dwordx4 v[80:83], v6, s[64:67], s92 offen sc1\nbuffer_store_dwordx4 v[80:83], v6, s[64:67], s74 offen sc1\n.Lr46_%=:\n" \
    "v_add_u32 v7, 0x40000, v7\ns_add_u32 s32, s32, 4\ns_cmp_lt_u32 s32, 64\ns_cbranch_scc1 .Lr2_%=\ns_endpgm\n.Lr5_%=:\ns_sleep 2\ns_add_u32 s68, s68, 1\n" \
    "s_and_b32 s69, s68, 63\ns_cbranch_scc1 .Lr4_%=\ns_cmp_gt_u32 s68, 0x400000\ns_cbranch_scc1 .Lr1_%=\nbuffer_load_dword v253, off, s[64:67], 0 sc1\ns_waitcnt vmcnt(0)\n" \
    "v_readfirstlane_b32 s69, v253\ns_nop 0\ns_cmp_eq_u32 s69, 1\ns_cbranch_scc0 .Lr4_%=\ns_endpgm\n.Lr7_%=:\ns_sleep 6\ns_add_u32 s68, s68, 1\ns_and_b32 s69, s68, 63\n" \
    "s_cbranch_scc1 .Lr6_%=\ns_cmp_gt_u32 s68, 0x400000\ns_cbranch_scc1 .Lr1_%=\nbuffer_load_dword v253, off, s[64:67], 0 sc1\ns_waitcnt vmcnt(0)\n" \
    "v_readfirstlane_b32 s69, v253\ns_nop 0\ns_cmp_eq_u32 s69, 1\ns_cbranch_scc0 .Lr6_%=\ns_endpgm\n.Lr14_%=:\ns_sleep 2\ns_add_u32 s68, s68, 1\ns_and_b32 s69, s68, 63\n" \
    "s_cbranch_scc1 .Lr13_%=\ns_cmp_gt_u32 s68, 0x400000\ns_cbranch_scc1 .Lr1_%=\nbuffer_load_dword v253, off, s[64:67], 0 sc1\ns_waitcnt vmcnt(0)\n" \
    "v_readfirstlane_b32 s69, v253\ns_nop 0\ns_cmp_eq_u32 s69, 1\ns_cbranch_scc0 .Lr13_%=\ns_endpgm\n.Lr17_%=:\ns_cmp_lt_u32 s68, 3\ns_cbranch_scc1 .Lr9_%=\n" \
    "v_max3_u32 v252, v32, v33, v34\nv_max3_u32 v253, v35, v36, v37\nv_max3_u32 v252, v252, v38, v39\nv_max3_u32 v253, v253, v40, v41\nv_max3_u32 v252, v252, v42, v43\n" \
    "v_max3_u32 v253, v253, v44, v45\nv_max3_u32 v252, v252, v46, v47\nv_max3_u32 v253, v253, v48, v49\nv_max3_u32 v252, v252, v50, v51\nv_max3_u32 v253, v253, v52, v53\n" \
    "v_max3_u32 v252, v252, v54, v55\nv_max3_u32 v253, v253, v56, v57\nv_max3_u32 v252, v252, v58, v59\nv_max3_u32 v253, v253, v60, v61\nv_max3_u32 v252, v252, v62, v63\n" \
    "v_max3_u32 v252, v252, v64, v65\nv_max3_u32 v253, v253, v66, v67\nv_max3_u32 v252, v252, v68, v69\nv_max3_u32 v253, v253, v70, v71\nv_max3_u32 v252, v252, v72, v73\n" \
    "v_max3_u32 v253, v253, v74, v75\nv_max3_u32 v252, v252, v76, v77\nv_max3_u32 v253, v253, v78, v79\nv_max3_u32 v252, v252, v80, v81\nv_max3_u32 v253, v253, v82, v83\n" \
    "v_max3_u32 v252, v252, v84, v85\nv_max3_u32 v253, v253, v86, v87\nv_max3_u32 v252, v252, v88, v89\nv_max3_u32 v253, v253, v90, v91\nv_max3_u32 v252, v252, v92, v93\n" \
    "v_max3_u32 v253, v253, v94, v95\nv_max3_u32 v252, v252, v96, v97\nv_max3_u32 v253, v253, v98, v99\nv_max3_u32 v252, v252, v100, v101\nv_max3_u32 v253, v253, v102, v103\n" \
    "v_max3_u32 v252, v252, v104, v105\nv_max3_u32 v253, v253, v106, v107\nv_max3_u32 v252, v252, v108, v109\nv_max3_u32 v253, v253, v110, v111\n" \
    "v_max3_u32 v252, v252, v112, v113\nv_max3_u32 v253, v253, v114, v115\nv_max3_u32 v252, v252, v116, v117\nv_max3_u32 v253, v253, v118, v119\n" \
    "v_max3_u32 v252, v252, v120, v121\nv_max3_u32 v253, v253, v122, v123\nv_max3_u32 v252, v252, v124, v125\nv_max3_u32 v253, v253, v126, v127\n" \
    "v_max3_u32 v252, v252, v128, v129\nv_max3_u32 v253, v253, v130, v131\nv_max3_u32 v252, v252, v132, v133\nv_max3_u32 v253, v253, v134, v135\n" \
    "v_max3_u32 v252, v252, v136, v137\nv_max3_u32 v253, v253, v138, v139\nv_max3_u32 v252, v252, v140, v141\nv_max3_u32 v253, v253, v142, v143\nv_max_u32 v252, v252, v253\n" \
    "v_cmp_eq_u32 vcc, -1, v252\ns_nop 0\ns_cbranch_vccz .Lr16_%=\n.Lr9_%=:\ns_add_u32 s56, s56, s38\ns_xor_b32 s57, s57, 128\ns_xor_b32 s84, s84, 0x4000\n" \
    "s_xor_b32 s92, s92, 0x1000\ns_add_u32 s76, s56, s35\ns_cmp_eq_u32 s48, 0\ns_cbranch_scc1 .Lr33_%=\ns_lshl_b32 s74, s76, 8\ns_add_u32 s74, s74, s57\n" \
    "s_add_u32 s74, s74, s32\ns_add_u32 s74, s74, 1\nv_mov_b32 v253, s74\ns_xor_b32 s69, s48, 512\ns_mov_b64 exec, 1\nbuffer_store_dword v253, off, s[64:67], s69 sc1\n" \
    "s_mov_b64 exec, -1\n.Lr33_%=:\ns_add_u32 s68, s68, 1\ns_and_b32 s69, s68, 63\ns_cbranch_scc1 .Lr8_%=\ns_cmp_gt_u32 s68, 0x400000\ns_cbranch_scc1 .Lr1_%=\n" \
    "buffer_load_dword v253, off, s[64:67], 0 sc1\ns_waitcnt vmcnt(0)\nv_readfirstlane_b32 s69, v253\ns_nop 0\ns_cmp_eq_u32 s69, 1\ns_cbranch_scc0 .Lr8_%=\ns_endpgm\n.Lr38_%=:\n" \
    "s_waitcnt vmcnt(0)\ns_add_u32 s68, s68, 1\ns_and_b32 s69, s68, 63\ns_cbranch_scc1 .Lr11_%=\ns_cmp_gt_u32 s68, 0x400000\ns_cbranch_scc1 .Lr1_%=\n" \
    "buffer_load_dword v253, off, s[64:67], 0 sc1\ns_waitcnt vmcnt(0)\nv_readfirstlane_b32 s69, v253\ns_nop 0\ns_cmp_eq_u32 s69, 1\ns_cbranch_scc0 .Lr11_%=\ns_endpgm\n" \
    ".Lr12_%=:\ns_cmp_lg_u32 s80, 0\ns_cbranch_scc1 .Lr18_%=\ns_sleep 4\nbuffer_load_dwordx4 v[32:35], v1, s[64:67], s83 offen offset:0 sc1\n" \
    "buffer_load_dwordx4 v[36:39], v1, s[64:67], s83 offen offset:1024 sc1\nbuffer_load_dwordx4 v[40:43], v1, s[64:67], s83 offen offset:2048 sc1\n" \
    "buffer_load_dwordx4 v[44:47], v1, s[64:67], s83 offen offset:3072 sc1\nbuffer_load_dwordx4 v[48:51], v1, s[64:67], s75 offen offset:0 sc1\n" \
    "buffer_load_dwordx4 v[52:55], v1, s[64:67], s75 offen offset:1024 sc1\nbuffer_load_dwordx4 v[56:59], v1, s[64:67], s75 offen offset:2048 sc1\n" \
    "buffer_load_dwordx4 v[60:63], v1, s[64:67], s75 offen offset:3072 sc1\ns_waitcnt vmcnt(0)\nv_max3_u32 v252, v32, v33, v34\nv_max3_u32 v253, v35, v36, v37\n" \
    "v_max3_u32 v252, v252, v38, v39\nv_max3_u32 v253, v253, v40, v41\nv_max3_u32 v252, v252, v42, v43\nv_max3_u32 v253, v253, v44, v45\nv_max3_u32 v252, v252, v46, v47\n" \
    "v_max3_u32 v253, v253, v48, v49\nv_max3_u32 v252, v252, v50, v51\nv_max3_u32 v253, v253, v52, v53\nv_max3_u32 v252, v252, v54, v55\nv_max3_u32 v253, v253, v56, v57\n" \
    "v_max3_u32 v252, v252, v58, v59\nv_max3_u32 v253, v253, v60, v61\nv_max3_u32 v252, v252, v62, v63\ns_mov_b32 s76, s94\n.Lr19_%=:\ns_add_u32 s84, s76, 0x0\n" \
    "s_add_u32 s85, s76, 0x4000\ns_add_u32 s86, s76, 0x8000\ns_add_u32 s87, s76, 0xc000\nbuffer_load_dwordx4 v[64:67], v1, s[64:67], s84 offen offset:0 sc1\n" \
    "buffer_load_dwordx4 v[68:71], v1, s[64:67], s84 offen offset:1024 sc1\nbuffer_load_dwordx4 v[72:75], v1, s[64:67], s84 offen offset:2048 sc1\n" \
    "buffer_load_dwordx4 v[76:79], v1, s[64:67], s84 offen offset:3072 sc1\nbuffer_load_dwordx2 v[80:81], v2, s[64:67], s84 offen offset:0 sc1\n" \
    "buffer_load_dwordx2 v[82:83], v2, s[64:67], s84 offen offset:128 sc1\nbuffer_load_dwordx4 v[84:87], v1, s[64:67], s85 offen offset:0 sc1\n" \
    "buffer_load_dwordx4 v[88:91], v1, s[64:67], s85 offen offset:1024 sc1\nbuffer_load_dwordx4 v[92:95], v1, s[64:67], s85 offen offset:2048 sc1\n" \
    "buffer_load_dwordx4 v[96:99], v1, s[64:67], s85 offen offset:3072 sc1\nbuffer_load_dwordx2 v[100:101], v2, s[64:67], s85 offen offset:0 sc1\n" \
    "buffer_load_dwordx2 v[102:103], v2, s[64:67], s85 offen offset:128 sc1\nbuffer_load_dwordx4 v[104:107], v1, s[64:67], s86 offen offset:0 sc1\n" \
    "buffer_load_dwordx4 v[108:111], v1, s[64:67], s86 offen offset:1024 sc1\nbuffer_load_dwordx4 v[112:115], v1, s[64:67], s86 offen offset:2048 sc1\n" \
    "buffer_load_dwordx4 v[116:119], v1, s[64:67], s86 offen offset:3072 sc1\nbuffer_load_dwordx2 v[120:121], v2, s[64:67], s86 offen offset:0 sc1\n" \
    "buffer_load_dwordx2 v[122:123], v2, s[64:67], s86 offen offset:128 sc1\nbuffer_load_dwordx4 v[124:127], v1, s[64:67], s87 offen offset:0 sc1\n" \
    "buffer_load_dwordx4 v[128:131], v1, s[64:67], s87 offen offset:1024 sc1\nbuffer_load_dwordx4 v[132:135], v1, s[64:67], s87 offen offset:2048 sc1\n" \
    "buffer_load_dwordx4 v[136:139], v1, s[64:67], s87 offen offset:3072 sc1\nbuffer_load_dwordx2 v[140:141], v2, s[64:67], s87 offen offset:0 sc1\n" \
    "buffer_load_dwordx2 v[142:143], v2, s[64:67], s87 offen offset:128 sc1\ns_waitcnt vmcnt(0)\nv_max3_u32 v252, v252, v64, v65\nv_max3_u32 v253, v253, v66, v67\n" \
    "v_max3_u32 v252, v252, v68, v69\nv_max3_u32 v253, v253, v70, v71\nv_max3_u32 v252, v252, v72, v73\nv_max3_u32 v253, v253, v74, v75\nv_max3_u32 v252, v252, v76, v77\n" \
    "v_max3_u32 v253, v253, v78, v79\nv_max3_u32 v252, v252, v80, v81\nv_max3_u32 v253, v253, v82, v83\nv_max3_u32 v252, v252, v84, v85\nv_max3_u32 v253, v253, v86, v87\n" \
    "v_max3_u32 v252, v252, v88, v89\nv_max3_u32 v253, v253, v90, v91\nv_max3_u32 v252, v252, v92, v93\nv_max3_u32 v253, v253, v94, v95\nv_max3_u32 v252, v252, v96, v97\n" \
    "v_max3_u32 v253, v253, v98, v99\nv_max3_u32 v252, v252, v100, v101\nv_max3_u32 v253, v253, v102, v103\nv_max3_u32 v252, v252, v104, v105\n" \
    "v_max3_u32 v253, v253, v106, v107\nv_max3_u32 v252, v252, v108, v109\nv_max3_u32 v253, v253, v110, v111\nv_max3_u32 v252, v252, v112, v113\n" \
    "v_max3_u32 v253, v253, v114, v115\nv_max3_u32 v252, v252, v116, v117\nv_max3_u32 v253, v253, v118, v119\nv_max3_u32 v252, v252, v120, v121\n" \
    "v_max3_u32 v253, v253, v122, v123\nv_max3_u32 v252, v252, v124, v125\nv_max3_u32 v253, v253, v126, v127\nv_max3_u32 v252, v252, v128, v129\n" \
    "v_max3_u32 v253, v253, v130, v131\nv_max3_u32 v252, v252, v132, v133\nv_max3_u32 v253, v253, v134, v135\nv_max3_u32 v252, v252, v136, v137\n" \
    "v_max3_u32 v253, v253, v138, v139\nv_max3_u32 v252, v252, v140, v141\nv_max3_u32 v253, v253, v142, v143\ns_add_u32 s76, s76, 0x10000\ns_sub_u32 s69, s76, s94\n" \
    "s_cmp_lt_u32 s69, 0x40000\ns_cbranch_scc1 .Lr19_%=\nv_max_u32 v252, v252, v253\nv_cmp_eq_u32 vcc, -1, v252\ns_nop 0\ns_cmp_eq_u64 vcc, 0\ns_cselect_b32 s80, 1, 0\n" \
    "s_add_u32 s68, s68, 1\ns_and_b32 s69, s68, 63\ns_cbranch_scc1 .Lr11_%=\ns_cmp_gt_u32 s68, 0x400000\ns_cbranch_scc1 .Lr1_%=\nbuffer_load_dword v253, off, s[64:67], 0 sc1\n" \
    "s_waitcnt vmcnt(0)\nv_readfirstlane_b32 s69, v253\ns_nop 0\ns_cmp_eq_u32 s69, 1\ns_cbranch_scc0 .Lr11_%=\ns_endpgm\n.Lr39_%=:\n" \
    "buffer_load_dwordx4 v[16:19], v12, s[64:67], 0 offen offset:0 sc1\nbuffer_load_dwordx4 v[20:23], v12, s[64:67], 0 offen offset:16 sc1\ns_waitcnt vmcnt(0)\n" \
    "v_max3_u32 v252, v16, v17, v18\nv_max3_u32 v253, v19, v20, v21\nv_max3_u32 v252, v252, v22, v23\nv_max_u32 v252, v252, v253\nv_cmp_eq_u32 vcc, -1, v252\ns_nop 0\n" \
    "s_cbranch_vccz .Lr40_%=\ns_sleep 8\ns_add_u32 s68, s68, 1\ns_and_b32 s69, s68, 63\ns_cbranch_scc1 .Lr39_%=\ns_cmp_gt_u32 s68, 0x400000\ns_cbranch_scc1 .Lr1_%=\n" \
    "buffer_load_dword v253, off, s[64:67], 0 sc1\ns_waitcnt vmcnt(0)\nv_readfirstlane_b32 s69, v253\ns_nop 0\ns_cmp_eq_u32 s69, 1\ns_cbranch_scc0 .Lr39_%=\ns_endpgm\n" \
    ".Lr45_%=:\ns_sleep 2\ns_add_u32 s68, s68, 1\ns_and_b32 s69, s68, 63\ns_cbranch_scc1 .Lr44_%=\ns_cmp_gt_u32 s68, 0x400000\ns_cbranch_scc1 .Lr1_%=\n" \
    "buffer_load_dword v253, off, s[64:67], 0 sc1\ns_waitcnt vmcnt(0)\nv_readfirstlane_b32 s69, v253\ns_nop 0\ns_cmp_eq_u32 s69, 1\ns_cbranch_scc0 .Lr44_%=\ns_endpgm\n.Lr1_%=:\n" \
    "v_mov_b32 v253, 1\nbuffer_store_dword v253, off, s[64:67], 0 sc1\ns_endpgm\n"
#define ASM_F1 \
    "s_mov_b32 s90, 0x7fff0000\ns_mov_b32 s91, 0x7fff0000\ns_mov_b32 s92, 0x7fff0000\ns_mov_b32 s93, 0x7fff0000\ns_mov_b32 s80, 0\ns_mov_b32 s81, 0\ns_mov_b32 s68, 0\n.Lf2_%=:\n" \
    "s_xor_b32 s85, s38, 512\ns_and_b32 s69, s32, 63\ns_lshl_b32 s69, s69, 19\ns_add_u32 s82, s36, s69\ns_lshr_b32 s69, s82, 8\ns_and_b32 s69, s69, 0x2000\n" \
    "s_xor_b32 s82, s82, s69\ns_add_u32 s69, s32, 1\ns_and_b32 s69, s69, 63\ns_lshl_b32 s69, s69, 19\ns_add_u32 s83, s36, s69\ns_lshr_b32 s69, s83, 8\n" \
    "s_and_b32 s69, s69, 0x2000\ns_xor_b32 s83, s83, s69\ns_add_u32 s84, s37, s80\ns_cmp_eq_u32 s81, 1\ns_cbranch_scc1 .Lf6_%=\ns_add_u32 s77, s32, 2\ns_cmp_eq_u32 s81, 2\n" \
    "s_cbranch_scc1 .Lf5_%=\n.Lf3_%=:\nbuffer_load_dword v150, off, s[64:67], s38 sc1\ns_waitcnt vmcnt(0)\nv_and_b32 v112, 127, v150\nv_cmp_gt_u32 vcc, s77, v112\ns_nop 0\n" \
    "s_cbranch_vccnz .Lf4_%=\n.Lf5_%=:\nv_and_b32 v112, 127, v150\n.Lf11_%=:\ns_sleep 1\ns_memrealtime s[72:73]\ns_waitcnt lgkmcnt(0)\ns_cmp_ge_u32 s77, 64\n" \
    "s_cselect_b32 s69, s60, s34\ns_sub_u32 s72, s72, s69\ns_lshl_b32 s72, s72, 8\nv_sub_u32 v113, s72, v150\nv_cmp_gt_i32 s[74:75], 0, v113\nv_cmp_eq_u32 vcc, s77, v112\n" \
    "s_nop 0\ns_and_b64 vcc, vcc, s[74:75]\ns_cbranch_scc1 .Lf11_%=\n.Lf12_%=:\nbuffer_load_dword v151, v2, s[64:67], s82 offen sc1\ns_add_u32 m0, s84, 0\n" \
    "s_add_u32 s69, s82, s48\nbuffer_load_dwordx4 v0, s[64:67], s69 offen offset:0 sc1 lds\nbuffer_load_dwordx4 v0, s[64:67], s69 offen offset:1024 sc1 lds\n" \
    "s_add_u32 m0, s84, 8192\ns_add_u32 s69, s82, s49\nbuffer_load_dwordx4 v0, s[64:67], s69 offen offset:0 sc1 lds\n" \
    "buffer_load_dwordx4 v0, s[64:67], s69 offen offset:1024 sc1 lds\ns_add_u32 m0, s84, 16384\ns_add_u32 s69, s82, s50\n" \
    "buffer_load_dwordx4 v0, s[64:67], s69 offen offset:0 sc1 lds\nbuffer_load_dwordx4 v0, s[64:67], s69 offen offset:1024 sc1 lds\n.Lf6_%=:\ns_waitcnt vmcnt(6)\n" \
    "v_cmp_eq_u32 vcc, -1, v151\ns_nop 0\ns_cbranch_vccnz .Lf13_%=\ns_cmp_lg_u32 s33, 0\ns_cbranch_scc1 .Lf14_%=\ns_memrealtime s[72:73]\ns_waitcnt lgkmcnt(0)\n" \
    "s_lshl_b32 s72, s72, 8\ns_add_u32 s72, s72, s32\ns_add_u32 s72, s72, 1\nv_mov_b32 v112, s72\ns_mov_b64 exec, 1\nbuffer_store_dword v112, off, s[64:67], s39 sc1\n" \
    "s_mov_b64 exec, -1\n.Lf14_%=:\ns_add_u32 s77, s32, 3\ns_add_u32 m0, s84, 24576\ns_add_u32 s69, s82, s51\nbuffer_load_dwordx4 v0, s[64:67], s69 offen offset:0 sc1 lds\n" \
    "buffer_load_dwordx4 v0, s[64:67], s69 offen offset:1024 sc1 lds\ns_waitcnt vmcnt(6)\ns_barrier\nds_read_b128 v[64:67], v1 offset:0\nds_read_b128 v[68:71], v1 offset:1024\n" \
    "ds_read_b128 v[72:75], v1 offset:2048\nds_read_b128 v[76:79], v1 offset:3072\ns_waitcnt lgkmcnt(3)\nv_mfma_f32_32x32x16_bf16 v[48:63], a[0:3], v[64:67], 0\n" \
    "ds_read_b128 v[80:83], v1 offset:4096\ns_add_u32 m0, s84, 32768\ns_add_u32 s69, s82, s52\nbuffer_load_dwordx4 v0, s[64:67], s69 offen offset:0 sc1 lds\n" \
    "s_waitcnt lgkmcnt(3)\nv_mfma_f32_32x32x16_bf16 v[48:63], a[4:7], v[68:71], v[48:63]\nds_read_b128 v[84:87], v1 offset:5120\ns_waitcnt lgkmcnt(3)\n" \
    "v_mfma_f32_32x32x16_bf16 v[48:63], a[8:11], v[72:75], v[48:63]\nds_read_b128 v[88:91], v1 offset:6144\nbuffer_load_dwordx4 v0, s[64:67], s69 offen offset:1024 sc1 lds\n" \
    "s_waitcnt lgkmcnt(3)\nv_mfma_f32_32x32x16_bf16 v[48:63], a[12:15], v[76:79], v[48:63]\nds_read_b128 v[92:95], v1 offset:7168\ns_waitcnt vmcnt(6) lgkmcnt(3)\ns_barrier\n" \
    "v_mfma_f32_32x32x16_bf16 v[48:63], a[16:19], v[80:83], v[48:63]\nds_read_b128 v[64:67], v1 offset:8192\ns_add_u32 s69, s82, s41\n" \
    "buffer_load_dwordx4 v[96:99], v0, s[64:67], s69 offen offset:0 sc1\ns_waitcnt lgkmcnt(3)\nv_mfma_f32_32x32x16_bf16 v[48:63], a[20:23], v[84:87], v[48:63]\n" \
    "ds_read_b128 v[68:71], v1 offset:9216\ns_waitcnt lgkmcnt(3)\nv_mfma_f32_32x32x16_bf16 v[48:63], a[24:27], v[88:91], v[48:63]\nds_read_b128 v[72:75], v1 offset:10240\n" \
    "buffer_load_dwordx4 v[100:103], v0, s[64:67], s69 offen offset:1024 sc1\ns_waitcnt lgkmcnt(3)\nv_mfma_f32_32x32x16_bf16 v[48:63], a[28:31], v[92:95], v[48:63]\n" \
    "ds_read_b128 v[76:79], v1 offset:11264\ns_waitcnt lgkmcnt(3)\nv_mfma_f32_32x32x16_bf16 v[48:63], a[32:35], v[64:67], v[48:63]\nds_read_b128 v[80:83], v1 offset:12288\n" \
    "buffer_load_dwordx4 v[104:107], v0, s[64:67], s69 offen offset:2048 sc1\ns_waitcnt lgkmcnt(3)\nv_mfma_f32_32x32x16_bf16 v[48:63], a[36:39], v[68:71], v[48:63]\n" \
    "ds_read_b128 v[84:87], v1 offset:13312\ns_waitcnt lgkmcnt(3)\nv_mfma_f32_32x32x16_bf16 v[48:63], a[40:43], v[72:75], v[48:63]\nds_read_b128 v[88:91], v1 offset:14336\n" \
    "buffer_load_dwordx4 v[108:111], v0, s[64:67], s69 offen offset:3072 sc1\ns_waitcnt lgkmcnt(3)\nv_mfma_f32_32x32x16_bf16 v[48:63], a[44:47], v[76:79], v[48:63]\n" \
    "ds_read_b128 v[92:95], v1 offset:15360\ns_waitcnt vmcnt(6) lgkmcnt(3)\ns_barrier\nv_mfma_f32_32x32x16_bf16 v[48:63], a[48:51], v[80:83], v[48:63]\n" \
    "ds_read_b128 v[64:67], v1 offset:16384\ns_add_u32 m0, s84, 40960\ns_add_u32 s69, s82, s53\nbuffer_load_dwordx4 v0, s[64:67], s69 offen offset:0 sc1 lds\n" \
    "s_waitcnt lgkmcnt(3)\nv_mfma_f32_32x32x16_bf16 v[48:63], a[52:55], v[84:87], v[48:63]\nds_read_b128 v[68:71], v1 offset:17408\ns_waitcnt lgkmcnt(3)\n" \
    "v_mfma_f32_32x32x16_bf16 v[48:63], a[56:59], v[88:91], v[48:63]\nds_read_b128 v[72:75], v1 offset:18432\nbuffer_load_dwordx4 v0, s[64:67], s69 offen offset:1024 sc1 lds\n" \
    "s_waitcnt lgkmcnt(3)\nv_mfma_f32_32x32x16_bf16 v[48:63], a[60:63], v[92:95], v[48:63]\nds_read_b128 v[76:79], v1 offset:19456\ns_waitcnt lgkmcnt(3)\n" \
    "v_mfma_f32_32x32x16_bf16 v[48:63], a[64:67], v[64:67], v[48:63]\nds_read_b128 v[80:83], v1 offset:20480\ns_add_u32 m0, s84, 49152\ns_add_u32 s69, s82, s54\n" \
    "buffer_load_dwordx4 v0, s[64:67], s69 offen offset:0 sc1 lds\ns_waitcnt lgkmcnt(3)\nv_mfma_f32_32x32x16_bf16 v[48:63], a[68:71], v[68:71], v[48:63]\n" \
    "ds_read_b128 v[84:87], v1 offset:21504\ns_waitcnt lgkmcnt(3)\nv_mfma_f32_32x32x16_bf16 v[48:63], a[72:75], v[72:75], v[48:63]\nds_read_b128 v[88:91], v1 offset:22528\n" \
    "buffer_load_dwordx4 v0, s[64:67], s69 offen offset:1024 sc1 lds\ns_waitcnt lgkmcnt(3)\nv_mfma_f32_32x32x16_bf16 v[48:63], a[76:79], v[76:79], v[48:63]\n" \
    "ds_read_b128 v[92:95], v1 offset:23552\ns_waitcnt lgkmcnt(3)\nv_mfma_f32_32x32x16_bf16 v[48:63], a[80:83], v[80:83], v[48:63]\nds_read_b128 v[64:67], v1 offset:24576\n" \
    "s_add_u32 m0, s84, 57344\ns_add_u32 s69, s82, s55\nbuffer_load_dwordx4 v0, s[64:67], s69 offen offset:0 sc1 lds\ns_waitcnt lgkmcnt(3)\n" \
    "v_mfma_f32_32x32x16_bf16 v[48:63], a[84:87], v[84:87], v[48:63]\nds_read_b128 v[68:71], v1 offset:25600\ns_waitcnt lgkmcnt(3)\n" \
    "v_mfma_f32_32x32x16_bf16 v[48:63], a[88:91], v[88:91], v[48:63]\nds_read_b128 v[72:75], v1 offset:26624\nbuffer_load_dwordx4 v0, s[64:67], s69 offen offset:1024 sc1 lds\n" \
    "s_waitcnt lgkmcnt(3)\nv_mfma_f32_32x32x16_bf16 v[48:63], a[92:95], v[92:95], v[48:63]\nds_read_b128 v[76:79], v1 offset:27648\ns_waitcnt lgkmcnt(3)\n" \
    "v_mfma_f32_32x32x16_bf16 v[48:63], a[96:99], v[64:67], v[48:63]\nds_read_b128 v[80:83], v1 offset:28672\ns_and_b32 s69, s32, 63\ns_lshl_b32 s69, s69, 19\n" \
    "s_add_u32 s82, s40, s69\ns_lshr_b32 s69, s82, 8\ns_and_b32 s69, s69, 0x2000\ns_waitcnt lgkmcnt(3)\nv_mfma_f32_32x32x16_bf16 v[48:63], a[100:103], v[68:71], v[48:63]\n" \
    "ds_read_b128 v[84:87], v1 offset:29696\ns_xor_b32 s82, s82, s69\ns_waitcnt lgkmcnt(3)\nv_mfma_f32_32x32x16_bf16 v[48:63], a[104:107], v[72:75], v[48:63]\n" \
    "ds_read_b128 v[88:91], v1 offset:30720\ns_waitcnt vmcnt(6)\ns_waitcnt lgkmcnt(3)\nv_max3_u32 v116, v96, v97, v98\nv_max3_u32 v116, v116, v99, v100\n" \
    "v_max3_u32 v116, v116, v101, v102\nv_max3_u32 v116, v116, v103, v104\nv_max3_u32 v116, v116, v105, v106\nv_mfma_f32_32x32x16_bf16 v[48:63], a[108:111], v[76:79], v[48:63]\n" \
    "ds_read_b128 v[92:95], v1 offset:31744\ns_waitcnt vmcnt(4) lgkmcnt(3)\ns_barrier\nv_max3_u32 v116, v116, v107, v108\nv_max3_u32 v116, v116, v109, v110\n" \
    "v_max_u32 v116, v116, v111\nv_cmp_eq_u32 s[70:71], -1, v116\nv_sub_f32 v114, v96, v100\nv_mfma_f32_32x32x16_bf16 v[48:63], a[112:115], v[80:83], v[48:63]\n" \
    "ds_read_b128 v[64:67], v1 offset:32768\ns_waitcnt lgkmcnt(3)\nv_add_f32 v96, v96, v100\nv_add_f32 v97, v97, v101\nv_mul_f32 v114, v114, v114\nv_mul_f32 v96, 0.5, v96\n" \
    "v_fmac_f32 v97, 0x41000000, v114\nv_mfma_f32_32x32x16_bf16 v[48:63], a[116:119], v[84:87], v[48:63]\nds_read_b128 v[68:71], v1 offset:33792\ns_waitcnt lgkmcnt(3)\n" \
    "v_sub_f32 v114, v104, v108\nv_add_f32 v104, v104, v108\nv_add_f32 v105, v105, v109\nv_mul_f32 v114, v114, v114\nv_mul_f32 v104, 0.5, v104\n" \
    "v_mfma_f32_32x32x16_bf16 v[48:63], a[120:123], v[88:91], v[48:63]\nds_read_b128 v[72:75], v1 offset:34816\ns_waitcnt lgkmcnt(3)\nv_fmac_f32 v105, 0x41000000, v114\n" \
    "v_sub_f32 v114, v96, v104\nv_add_f32 v96, v96, v104\nv_add_f32 v97, v97, v105\nv_mul_f32 v114, v114, v114\n" \
    "v_mfma_f32_32x32x16_bf16 v[48:63], a[124:127], v[92:95], v[48:63]\nds_read_b128 v[76:79], v1 offset:35840\ns_waitcnt lgkmcnt(3)\nv_mul_f32 v96, 0.5, v96\n" \
    "v_fmac_f32 v97, 0x41800000, v114\nv_sub_f32 v114, v98, v102\nv_add_f32 v98, v98, v102\nv_add_f32 v99, v99, v103\n" \
    "v_mfma_f32_32x32x16_bf16 v[48:63], a[128:131], v[64:67], v[48:63]\nds_read_b128 v[80:83], v1 offset:36864\ns_waitcnt lgkmcnt(3)\nv_mul_f32 v114, v114, v114\n" \
    "v_mul_f32 v98, 0.5, v98\nv_fmac_f32 v99, 0x41000000, v114\nv_sub_f32 v114, v106, v110\nv_add_f32 v106, v106, v110\n" \
    "v_mfma_f32_32x32x16_bf16 v[48:63], a[132:135], v[68:71], v[48:63]\nds_read_b128 v[84:87], v1 offset:37888\ns_waitcnt lgkmcnt(3)\nv_add_f32 v107, v107, v111\n" \
    "v_mul_f32 v114, v114, v114\nv_mul_f32 v106, 0.5, v106\nv_fmac_f32 v107, 0x41000000, v114\nv_sub_f32 v114, v98, v106\n" \
    "v_mfma_f32_32x32x16_bf16 v[48:63], a[136:139], v[72:75], v[48:63]\nds_read_b128 v[88:91], v1 offset:38912\ns_waitcnt lgkmcnt(3)\nv_add_f32 v98, v98, v106\n" \
    "v_add_f32 v99, v99, v107\nv_mul_f32 v114, v114, v114\nv_mul_f32 v98, 0.5, v98\nv_fmac_f32 v99, 0x41800000, v114\n" \
    "v_mfma_f32_32x32x16_bf16 v[48:63], a[140:143], v[76:79], v[48:63]\nds_read_b128 v[92:95], v1 offset:39936\ns_waitcnt lgkmcnt(3)\nv_mov_b64 v[100:101], v[96:97]\n" \
    "v_mov_b64 v[102:103], v[98:99]\nv_mfma_f32_32x32x16_bf16 v[48:63], a[144:147], v[80:83], v[48:63]\nds_read_b128 v[64:67], v1 offset:40960\ns_waitcnt lgkmcnt(3)\n" \
    "v_permlane16_swap_b32 v96, v100\nv_permlane16_swap_b32 v97, v101\nv_permlane16_swap_b32 v98, v102\nv_mfma_f32_32x32x16_bf16 v[48:63], a[148:151], v[84:87], v[48:63]\n" \
    "ds_read_b128 v[68:71], v1 offset:41984\ns_waitcnt lgkmcnt(3)\nv_permlane16_swap_b32 v99, v103\nv_sub_f32 v114, v96, v100\nv_add_f32 v96, v96, v100\n" \
    "v_add_f32 v97, v97, v101\nv_mfma_f32_32x32x16_bf16 v[48:63], a[152:155], v[88:91], v[48:63]\nds_read_b128 v[72:75], v1 offset:43008\ns_waitcnt lgkmcnt(3)\n" \
    "v_mul_f32 v114, v114, v114\nv_mul_f32 v96, 0.5, v96\nv_fmac_f32 v97, 0x42000000, v114\nv_sub_f32 v114, v98, v102\nv_add_f32 v98, v98, v102\n" \
    "v_mfma_f32_32x32x16_bf16 v[48:63], a[156:159], v[92:95], v[48:63]\nds_read_b128 v[76:79], v1 offset:44032\ns_waitcnt lgkmcnt(3)\nv_add_f32 v99, v99, v103\n" \
    "v_mul_f32 v114, v114, v114\nv_mul_f32 v98, 0.5, v98\nv_fmac_f32 v99, 0x42000000, v114\nv_mov_b64 v[100:101], v[96:97]\n" \
    "v_mfma_f32_32x32x16_bf16 v[48:63], a[160:163], v[64:67], v[48:63]\nds_read_b128 v[80:83], v1 offset:45056\nbuffer_load_dword v150, off, s[64:67], s85 sc1\n" \
    "s_waitcnt lgkmcnt(3)\nv_mov_b64 v[102:103], v[98:99]\nv_permlane32_swap_b32 v96, v100\nv_permlane32_swap_b32 v97, v101\n" \
    "v_mfma_f32_32x32x16_bf16 v[48:63], a[164:167], v[68:71], v[48:63]\nds_read_b128 v[84:87], v1 offset:46080\ns_waitcnt lgkmcnt(3)\nv_permlane32_swap_b32 v98, v102\n" \
    "v_permlane32_swap_b32 v99, v103\nv_sub_f32 v114, v96, v100\nv_mfma_f32_32x32x16_bf16 v[48:63], a[168:171], v[72:75], v[48:63]\nds_read_b128 v[88:91], v1 offset:47104\n" \
    "s_waitcnt lgkmcnt(3)\nv_add_f32 v96, v96, v100\nv_add_f32 v97, v97, v101\nv_mul_f32 v114, v114, v114\nv_mul_f32 v96, 0.5, v96\nv_fmac_f32 v97, 0x42800000, v114\n" \
    "v_mfma_f32_32x32x16_bf16 v[48:63], a[172:175], v[76:79], v[48:63]\nds_read_b128 v[92:95], v1 offset:48128\ns_waitcnt vmcnt(1) lgkmcnt(3)\ns_barrier\n" \
    "v_sub_f32 v114, v98, v102\nv_add_f32 v98, v98, v102\nv_add_f32 v99, v99, v103\nv_mul_f32 v114, v114, v114\nv_mul_f32 v98, 0.5, v98\n" \
    "v_mfma_f32_32x32x16_bf16 v[48:63], a[176:179], v[80:83], v[48:63]\nds_read_b128 v[64:67], v1 offset:49152\ns_waitcnt lgkmcnt(3)\nv_fmac_f32 v99, 0x42800000, v114\n" \
    "v_mfma_f32_32x32x16_bf16 v[48:63], a[180:183], v[84:87], v[48:63]\nds_read_b128 v[68:71], v1 offset:50176\ns_cmp_lg_u64 s[70:71], 0\ns_cselect_b32 s76, -1, 0\n" \
    "s_waitcnt lgkmcnt(3)\nv_min_u32 v96, -2, v96\nv_min_u32 v98, -2, v98\nv_or_b32 v96, s76, v96\nv_or_b32 v98, s76, v98\n" \
    "v_mfma_f32_32x32x16_bf16 v[48:63], a[184:187], v[88:91], v[48:63]\nds_read_b128 v[72:75], v1 offset:51200\nds_write_b128 v3, v[96:99]\ns_waitcnt lgkmcnt(4)\n" \
    "v_mfma_f32_32x32x16_bf16 v[48:63], a[188:191], v[92:95], v[48:63]\nds_read_b128 v[76:79], v1 offset:52224\ns_waitcnt lgkmcnt(4)\n" \
    "v_mfma_f32_32x32x16_bf16 v[48:63], a[192:195], v[64:67], v[48:63]\nds_read_b128 v[80:83], v1 offset:53248\ns_waitcnt lgkmcnt(2)\ns_barrier\n" \
    "ds_read_b64 v[96:97], v4 offset:0\nds_read_b64 v[98:99], v4 offset:256\nds_read_b64 v[100:101], v4 offset:512\n" \
    "v_mfma_f32_32x32x16_bf16 v[48:63], a[196:199], v[68:71], v[48:63]\nds_read_b128 v[84:87], v1 offset:54272\nds_read_b64 v[102:103], v4 offset:768\n" \
    "v_mfma_f32_32x32x16_bf16 v[48:63], a[200:203], v[72:75], v[48:63]\nds_read_b128 v[88:91], v1 offset:55296\ns_waitcnt lgkmcnt(7)\n" \
    "v_mfma_f32_32x32x16_bf16 v[48:63], a[204:207], v[76:79], v[48:63]\nds_read_b128 v[92:95], v1 offset:56320\ns_waitcnt lgkmcnt(2)\nv_max3_u32 v116, v96, v98, v100\n" \
    "v_max_u32 v116, v116, v102\nv_cmp_eq_u32 s[70:71], -1, v116\nv_sub_f32 v114, v96, v98\nv_add_f32 v96, v96, v98\n" \
    "v_mfma_f32_32x32x16_bf16 v[48:63], a[208:211], v[80:83], v[48:63]\nds_read_b128 v[64:67], v1 offset:57344\nv_add_f32 v97, v97, v99\nv_mul_f32 v114, v114, v114\n" \
    "v_mul_f32 v96, 0.5, v96\nv_fmac_f32 v97, 0x43000000, v114\nv_sub_f32 v114, v100, v102\nv_mfma_f32_32x32x16_bf16 v[48:63], a[212:215], v[84:87], v[48:63]\n" \
    "ds_read_b128 v[68:71], v1 offset:58368\ns_waitcnt lgkmcnt(3)\nv_add_f32 v100, v100, v102\nv_add_f32 v101, v101, v103\nv_mul_f32 v114, v114, v114\n" \
    "v_mul_f32 v100, 0.5, v100\nv_fmac_f32 v101, 0x43000000, v114\nv_mfma_f32_32x32x16_bf16 v[48:63], a[216:219], v[88:91], v[48:63]\nds_read_b128 v[72:75], v1 offset:59392\n" \
    "s_waitcnt lgkmcnt(3)\nv_sub_f32 v114, v96, v100\nv_add_f32 v96, v96, v100\nv_add_f32 v97, v97, v101\nv_mul_f32 v114, v114, v114\nv_mul_f32 v96, 0.5, v96\n" \
    "v_mfma_f32_32x32x16_bf16 v[48:63], a[220:223], v[92:95], v[48:63]\nds_read_b128 v[76:79], v1 offset:60416\ns_waitcnt lgkmcnt(3)\nv_fmac_f32 v97, 0x43800000, v114\n" \
    "v_mul_f32 v98, 0x3a800000, v97\nv_cvt_pk_bf16_f32 v96, 0, v96\nv_cvt_pk_bf16_f32 v98, 0, v98\nv_add_f32 v98, 0x3727c5ac, v98\n" \
    "v_mfma_f32_32x32x16_bf16 v[48:63], a[224:227], v[64:67], v[48:63]\nds_read_b128 v[80:83], v1 offset:61440\ns_waitcnt lgkmcnt(3)\nv_cvt_pk_bf16_f32 v98, 0, v98\n" \
    "v_sqrt_f32 v98, v98\nv_mfma_f32_32x32x16_bf16 v[48:63], a[228:231], v[68:71], v[48:63]\nds_read_b128 v[84:87], v1 offset:62464\ns_waitcnt lgkmcnt(3)\n" \
    "v_cvt_pk_bf16_f32 v98, 0, v98\nv_rcp_f32 v98, v98\nv_mfma_f32_32x32x16_bf16 v[48:63], a[232:235], v[72:75], v[48:63]\nds_read_b128 v[88:91], v1 offset:63488\n" \
    "s_waitcnt vmcnt(0)\ns_waitcnt lgkmcnt(3)\nv_and_b32 v112, 127, v150\nv_cmp_gt_u32 vcc, s77, v112\nv_mfma_f32_32x32x16_bf16 v[48:63], a[236:239], v[76:79], v[48:63]\n" \
    "ds_read_b128 v[92:95], v1 offset:64512\ns_cmp_eq_u64 vcc, 0\ns_cselect_b32 s81, 1, 0\ns_waitcnt lgkmcnt(3)\n" \
    "v_mfma_f32_32x32x16_bf16 v[48:63], a[240:243], v[80:83], v[48:63]\ns_waitcnt lgkmcnt(2)\nv_mfma_f32_32x32x16_bf16 v[48:63], a[244:247], v[84:87], v[48:63]\n" \
    "s_waitcnt lgkmcnt(0)\ns_memrealtime s[72:73]\nv_mfma_f32_32x32x16_bf16 v[48:63], a[248:251], v[88:91], v[48:63]\n" \
    "v_mfma_f32_32x32x16_bf16 v[48:63], a[252:255], v[92:95], v[48:63]\ns_waitcnt lgkmcnt(0)\ns_cmp_ge_u32 s77, 64\ns_cselect_b32 s69, s60, s34\ns_sub_u32 s72, s72, s69\n" \
    "s_lshl_b32 s72, s72, 8\nv_sub_u32 v113, s72, v150\nv_cmp_gt_i32 s[74:75], 0, v113\nv_cmp_eq_u32 vcc, s77, v112\ns_nop 0\ns_and_b64 vcc, vcc, s[74:75]\n" \
    "s_cselect_b32 s69, 1, 0\ns_lshl_b32 s81, s81, s69\ns_cmp_lg_u64 s[70:71], 0\ns_cbranch_scc1 .Lf9_%=\nv_cmp_u_f32 vcc, v48, v48\ns_nop 0\ns_cbranch_vccnz .Lf7_%=\n.Lf8_%=:\n" \
    "s_cmp_eq_u32 s81, 1\ns_cbranch_scc1 .Lf15_%=\nv_pk_fma_f32 v[48:49], v[16:17], v[96:97], v[48:49] op_sel_hi:[1,0,1] neg_lo:[0,1,0] neg_hi:[0,1,0]\n" \
    "v_pk_fma_f32 v[50:51], v[18:19], v[96:97], v[50:51] op_sel_hi:[1,0,1] neg_lo:[0,1,0] neg_hi:[0,1,0]\n" \
    "v_pk_fma_f32 v[52:53], v[20:21], v[96:97], v[52:53] op_sel_hi:[1,0,1] neg_lo:[0,1,0] neg_hi:[0,1,0]\n" \
    "v_pk_fma_f32 v[54:55], v[22:23], v[96:97], v[54:55] op_sel_hi:[1,0,1] neg_lo:[0,1,0] neg_hi:[0,1,0]\nv_pk_fma_f32 v[48:49], v[48:49], v[98:99], v[32:33] op_sel_hi:[1,0,1]\n" \
    "v_pk_fma_f32 v[50:51], v[50:51], v[98:99], v[34:35] op_sel_hi:[1,0,1]\nv_pk_fma_f32 v[52:53], v[52:53], v[98:99], v[36:37] op_sel_hi:[1,0,1]\n" \
    "v_pk_fma_f32 v[54:55], v[54:55], v[98:99], v[38:39] op_sel_hi:[1,0,1]\nv_pk_mul_f32 v[72:73], v[48:49], v[48:49]\nv_pk_mul_f32 v[74:75], v[50:51], v[50:51]\n" \
    "v_pk_mul_f32 v[76:77], v[52:53], v[52:53]\nv_pk_mul_f32 v[78:79], v[54:55], v[54:55]\nv_pk_fma_f32 v[72:73], v[72:73], v[6:7], v[8:9]\n" \
    "v_pk_fma_f32 v[74:75], v[74:75], v[6:7], v[8:9]\nv_pk_fma_f32 v[76:77], v[76:77], v[6:7], v[8:9]\nv_pk_fma_f32 v[78:79], v[78:79], v[6:7], v[8:9]\n" \
    "v_pk_mul_f32 v[72:73], v[72:73], v[48:49]\nv_pk_mul_f32 v[74:75], v[74:75], v[50:51]\nv_pk_mul_f32 v[76:77], v[76:77], v[52:53]\nv_pk_mul_f32 v[78:79], v[78:79], v[54:55]\n" \
    "v_exp_f32 v72, v72\nv_exp_f32 v73, v73\nv_exp_f32 v74, v74\nv_exp_f32 v75, v75\nv_exp_f32 v76, v76\nv_exp_f32 v77, v77\nv_exp_f32 v78, v78\nv_exp_f32 v79, v79\n" \
    "v_pk_add_f32 v[72:73], v[72:73], v[10:11]\nv_pk_add_f32 v[74:75], v[74:75], v[10:11]\nv_pk_add_f32 v[76:77], v[76:77], v[10:11]\nv_pk_add_f32 v[78:79], v[78:79], v[10:11]\n" \
    "v_rcp_f32 v72, v72\nv_rcp_f32 v73, v73\nv_rcp_f32 v74, v74\nv_rcp_f32 v75, v75\nv_rcp_f32 v76, v76\nv_rcp_f32 v77, v77\nv_rcp_f32 v78, v78\nv_rcp_f32 v79, v79\n" \
    "v_pk_mul_f32 v[72:73], v[72:73], v[48:49]\nv_pk_mul_f32 v[74:75], v[74:75], v[50:51]\nv_pk_mul_f32 v[76:77], v[76:77], v[52:53]\nv_pk_mul_f32 v[78:79], v[78:79], v[54:55]\n" \
    "v_cvt_pk_bf16_f32 v64, v72, v73\nv_cvt_pk_bf16_f32 v65, v74, v75\nv_cvt_pk_bf16_f32 v66, v76, v77\nv_cvt_pk_bf16_f32 v67, v78, v79\nv_min_u32 v64, -2, v64\n" \
    "v_min_u32 v65, -2, v65\nv_min_u32 v66, -2, v66\nv_min_u32 v67, -2, v67\nbuffer_store_dwordx4 v[64:67], v0, s[64:67], s82 offen offset:0 sc1\n" \
    "v_pk_fma_f32 v[56:57], v[24:25], v[96:97], v[56:57] op_sel_hi:[1,0,1] neg_lo:[0,1,0] neg_hi:[0,1,0]\n" \
    "v_pk_fma_f32 v[58:59], v[26:27], v[96:97], v[58:59] op_sel_hi:[1,0,1] neg_lo:[0,1,0] neg_hi:[0,1,0]\n" \
    "v_pk_fma_f32 v[60:61], v[28:29], v[96:97], v[60:61] op_sel_hi:[1,0,1] neg_lo:[0,1,0] neg_hi:[0,1,0]\n" \
    "v_pk_fma_f32 v[62:63], v[30:31], v[96:97], v[62:63] op_sel_hi:[1,0,1] neg_lo:[0,1,0] neg_hi:[0,1,0]\nv_pk_fma_f32 v[56:57], v[56:57], v[98:99], v[40:41] op_sel_hi:[1,0,1]\n" \
    "v_pk_fma_f32 v[58:59], v[58:59], v[98:99], v[42:43] op_sel_hi:[1,0,1]\nv_pk_fma_f32 v[60:61], v[60:61], v[98:99], v[44:45] op_sel_hi:[1,0,1]\n" \
    "v_pk_fma_f32 v[62:63], v[62:63], v[98:99], v[46:47] op_sel_hi:[1,0,1]\nv_pk_mul_f32 v[72:73], v[56:57], v[56:57]\nv_pk_mul_f32 v[74:75], v[58:59], v[58:59]\n" \
    "v_pk_mul_f32 v[76:77], v[60:61], v[60:61]\nv_pk_mul_f32 v[78:79], v[62:63], v[62:63]\nv_pk_fma_f32 v[72:73], v[72:73], v[6:7], v[8:9]\n" \
    "v_pk_fma_f32 v[74:75], v[74:75], v[6:7], v[8:9]\nv_pk_fma_f32 v[76:77], v[76:77], v[6:7], v[8:9]\nv_pk_fma_f32 v[78:79], v[78:79], v[6:7], v[8:9]\n" \
    "v_pk_mul_f32 v[72:73], v[72:73], v[56:57]\nv_pk_mul_f32 v[74:75], v[74:75], v[58:59]\nv_pk_mul_f32 v[76:77], v[76:77], v[60:61]\nv_pk_mul_f32 v[78:79], v[78:79], v[62:63]\n" \
    "v_exp_f32 v72, v72\nv_exp_f32 v73, v73\nv_exp_f32 v74, v74\nv_exp_f32 v75, v75\nv_exp_f32 v76, v76\nv_exp_f32 v77, v77\nv_exp_f32 v78, v78\nv_exp_f32 v79, v79\n" \
    "v_pk_add_f32 v[72:73], v[72:73], v[10:11]\nv_pk_add_f32 v[74:75], v[74:75], v[10:11]\nv_pk_add_f32 v[76:77], v[76:77], v[10:11]\nv_pk_add_f32 v[78:79], v[78:79], v[10:11]\n" \
    "v_rcp_f32 v72, v72\nv_rcp_f32 v73, v73\nv_rcp_f32 v74, v74\nv_rcp_f32 v75, v75\nv_rcp_f32 v76, v76\nv_rcp_f32 v77, v77\nv_rcp_f32 v78, v78\nv_rcp_f32 v79, v79\n" \
    "v_pk_mul_f32 v[72:73], v[72:73], v[56:57]\nv_pk_mul_f32 v[74:75], v[74:75], v[58:59]\nv_pk_mul_f32 v[76:77], v[76:77], v[60:61]\nv_pk_mul_f32 v[78:79], v[78:79], v[62:63]\n" \
    "v_cvt_pk_bf16_f32 v68, v72, v73\nv_cvt_pk_bf16_f32 v69, v74, v75\nv_cvt_pk_bf16_f32 v70, v76, v77\nv_cvt_pk_bf16_f32 v71, v78, v79\nv_min_u32 v68, -2, v68\n" \
    "v_min_u32 v69, -2, v69\nv_min_u32 v70, -2, v70\nv_min_u32 v71, -2, v71\nbuffer_store_dwordx4 v[68:71], v0, s[64:67], s82 offen offset:1024 sc1\n.Lf16_%=:\n" \
    "s_mov_b32 s38, s85\ns_xor_b32 s80, s80, 0x10000\nv_xor_b32 v1, 0x10000, v1\ns_mov_b32 s68, 0\ns_add_u32 s32, s32, 1\ns_cmp_lt_u32 s32, 64\ns_cbranch_scc1 .Lf2_%=\n" \
    "s_endpgm\n.Lf4_%=:\ns_sleep 8\ns_add_u32 s68, s68, 1\ns_and_b32 s69, s68, 63\ns_cbranch_scc1 .Lf3_%=\ns_cmp_gt_u32 s68, 0x400000\ns_cbranch_scc1 .Lf1_%=\n" \
    "buffer_load_dword v253, off, s[64:67], 0 sc1\ns_waitcnt vmcnt(0)\nv_readfirstlane_b32 s69, v253\ns_nop 0\ns_cmp_eq_u32 s69, 1\ns_cbranch_scc0 .Lf3_%=\ns_endpgm\n.Lf13_%=:\n" \
    "s_waitcnt vmcnt(0)\ns_add_u32 s68, s68, 1\ns_and_b32 s69, s68, 63\ns_cbranch_scc1 .Lf12_%=\ns_cmp_gt_u32 s68, 0x400000\ns_cbranch_scc1 .Lf1_%=\n" \
    "buffer_load_dword v253, off, s[64:67], 0 sc1\ns_waitcnt vmcnt(0)\nv_readfirstlane_b32 s69, v253\ns_nop 0\ns_cmp_eq_u32 s69, 1\ns_cbranch_scc0 .Lf12_%=\ns_endpgm\n" \
    ".Lf15_%=:\ns_xor_b32 s84, s84, 0x10000\nbuffer_load_dword v151, v2, s[64:67], s83 offen sc1\n" \
    "v_pk_fma_f32 v[48:49], v[16:17], v[96:97], v[48:49] op_sel_hi:[1,0,1] neg_lo:[0,1,0] neg_hi:[0,1,0]\n" \
    "v_pk_fma_f32 v[50:51], v[18:19], v[96:97], v[50:51] op_sel_hi:[1,0,1] neg_lo:[0,1,0] neg_hi:[0,1,0]\n" \
    "v_pk_fma_f32 v[52:53], v[20:21], v[96:97], v[52:53] op_sel_hi:[1,0,1] neg_lo:[0,1,0] neg_hi:[0,1,0]\n" \
    "v_pk_fma_f32 v[54:55], v[22:23], v[96:97], v[54:55] op_sel_hi:[1,0,1] neg_lo:[0,1,0] neg_hi:[0,1,0]\nv_pk_fma_f32 v[48:49], v[48:49], v[98:99], v[32:33] op_sel_hi:[1,0,1]\n" \
    "v_pk_fma_f32 v[50:51], v[50:51], v[98:99], v[34:35] op_sel_hi:[1,0,1]\nv_pk_fma_f32 v[52:53], v[52:53], v[98:99], v[36:37] op_sel_hi:[1,0,1]\n" \
    "v_pk_fma_f32 v[54:55], v[54:55], v[98:99], v[38:39] op_sel_hi:[1,0,1]\nv_pk_mul_f32 v[72:73], v[48:49], v[48:49]\nv_pk_mul_f32 v[74:75], v[50:51], v[50:51]\n" \
    "v_pk_mul_f32 v[76:77], v[52:53], v[52:53]\nv_pk_mul_f32 v[78:79], v[54:55], v[54:55]\ns_add_u32 m0, s84, 0\ns_add_u32 s69, s83, s48\n" \
    "buffer_load_dwordx4 v0, s[64:67], s69 offen offset:0 sc1 lds\nv_pk_fma_f32 v[72:73], v[72:73], v[6:7], v[8:9]\nv_pk_fma_f32 v[74:75], v[74:75], v[6:7], v[8:9]\n" \
    "v_pk_fma_f32 v[76:77], v[76:77], v[6:7], v[8:9]\nv_pk_fma_f32 v[78:79], v[78:79], v[6:7], v[8:9]\nv_pk_mul_f32 v[72:73], v[72:73], v[48:49]\n" \
    "v_pk_mul_f32 v[74:75], v[74:75], v[50:51]\nv_pk_mul_f32 v[76:77], v[76:77], v[52:53]\nv_pk_mul_f32 v[78:79], v[78:79], v[54:55]\nv_exp_f32 v72, v72\nv_exp_f32 v73, v73\n" \
    "v_exp_f32 v74, v74\nv_exp_f32 v75, v75\nbuffer_load_dwordx4 v0, s[64:67], s69 offen offset:1024 sc1 lds\nv_exp_f32 v76, v76\nv_exp_f32 v77, v77\nv_exp_f32 v78, v78\n" \
    "v_exp_f32 v79, v79\nv_pk_add_f32 v[72:73], v[72:73], v[10:11]\nv_pk_add_f32 v[74:75], v[74:75], v[10:11]\nv_pk_add_f32 v[76:77], v[76:77], v[10:11]\n" \
    "v_pk_add_f32 v[78:79], v[78:79], v[10:11]\nv_rcp_f32 v72, v72\nv_rcp_f32 v73, v73\nv_rcp_f32 v74, v74\nv_rcp_f32 v75, v75\ns_add_u32 m0, s84, 8192\n" \
    "s_add_u32 s69, s83, s49\nbuffer_load_dwordx4 v0, s[64:67], s69 offen offset:0 sc1 lds\nv_rcp_f32 v76, v76\nv_rcp_f32 v77, v77\nv_rcp_f32 v78, v78\nv_rcp_f32 v79, v79\n" \
    "v_pk_mul_f32 v[72:73], v[72:73], v[48:49]\nv_pk_mul_f32 v[74:75], v[74:75], v[50:51]\nv_pk_mul_f32 v[76:77], v[76:77], v[52:53]\nv_pk_mul_f32 v[78:79], v[78:79], v[54:55]\n" \
    "v_cvt_pk_bf16_f32 v64, v72, v73\nv_cvt_pk_bf16_f32 v65, v74, v75\nv_cvt_pk_bf16_f32 v66, v76, v77\nv_cvt_pk_bf16_f32 v67, v78, v79\n" \
    "buffer_load_dwordx4 v0, s[64:67], s69 offen offset:1024 sc1 lds\nv_min_u32 v64, -2, v64\nv_min_u32 v65, -2, v65\nv_min_u32 v66, -2, v66\nv_min_u32 v67, -2, v67\n" \
    "buffer_store_dwordx4 v[64:67], v0, s[64:67], s82 offen offset:0 sc1\nv_pk_fma_f32 v[56:57], v[24:25], v[96:97], v[56:57] op_sel_hi:[1,0,1] neg_lo:[0,1,0] neg_hi:[0,1,0]\n" \
    "v_pk_fma_f32 v[58:59], v[26:27], v[96:97], v[58:59] op_sel_hi:[1,0,1] neg_lo:[0,1,0] neg_hi:[0,1,0]\n" \
    "v_pk_fma_f32 v[60:61], v[28:29], v[96:97], v[60:61] op_sel_hi:[1,0,1] neg_lo:[0,1,0] neg_hi:[0,1,0]\n" \
    "v_pk_fma_f32 v[62:63], v[30:31], v[96:97], v[62:63] op_sel_hi:[1,0,1] neg_lo:[0,1,0] neg_hi:[0,1,0]\nv_pk_fma_f32 v[56:57], v[56:57], v[98:99], v[40:41] op_sel_hi:[1,0,1]\n" \
    "v_pk_fma_f32 v[58:59], v[58:59], v[98:99], v[42:43] op_sel_hi:[1,0,1]\nv_pk_fma_f32 v[60:61], v[60:61], v[98:99], v[44:45] op_sel_hi:[1,0,1]\ns_add_u32 m0, s84, 16384\n" \
    "s_add_u32 s69, s83, s50\nbuffer_load_dwordx4 v0, s[64:67], s69 offen offset:0 sc1 lds\nv_pk_fma_f32 v[62:63], v[62:63], v[98:99], v[46:47] op_sel_hi:[1,0,1]\n" \
    "v_pk_mul_f32 v[72:73], v[56:57], v[56:57]\nv_pk_mul_f32 v[74:75], v[58:59], v[58:59]\nv_pk_mul_f32 v[76:77], v[60:61], v[60:61]\nv_pk_mul_f32 v[78:79], v[62:63], v[62:63]\n" \
    "v_pk_fma_f32 v[72:73], v[72:73], v[6:7], v[8:9]\nv_pk_fma_f32 v[74:75], v[74:75], v[6:7], v[8:9]\nv_pk_fma_f32 v[76:77], v[76:77], v[6:7], v[8:9]\n" \
    "v_pk_fma_f32 v[78:79], v[78:79], v[6:7], v[8:9]\nv_pk_mul_f32 v[72:73], v[72:73], v[56:57]\nv_pk_mul_f32 v[74:75], v[74:75], v[58:59]\n" \
    "v_pk_mul_f32 v[76:77], v[76:77], v[60:61]\nbuffer_load_dwordx4 v0, s[64:67], s69 offen offset:1024 sc1 lds\nv_pk_mul_f32 v[78:79], v[78:79], v[62:63]\nv_exp_f32 v72, v72\n" \
    "v_exp_f32 v73, v73\nv_exp_f32 v74, v74\nv_exp_f32 v75, v75\nv_exp_f32 v76, v76\nv_exp_f32 v77, v77\nv_exp_f32 v78, v78\nv_exp_f32 v79, v79\n" \
    "v_pk_add_f32 v[72:73], v[72:73], v[10:11]\nv_pk_add_f32 v[74:75], v[74:75], v[10:11]\nv_pk_add_f32 v[76:77], v[76:77], v[10:11]\nv_pk_add_f32 v[78:79], v[78:79], v[10:11]\n" \
    "v_rcp_f32 v72, v72\nv_rcp_f32 v73, v73\nv_rcp_f32 v74, v74\nv_rcp_f32 v75, v75\nv_rcp_f32 v76, v76\nv_rcp_f32 v77, v77\nv_rcp_f32 v78, v78\nv_rcp_f32 v79, v79\n" \
    "v_pk_mul_f32 v[72:73], v[72:73], v[56:57]\nv_pk_mul_f32 v[74:75], v[74:75], v[58:59]\nv_pk_mul_f32 v[76:77], v[76:77], v[60:61]\nv_pk_mul_f32 v[78:79], v[78:79], v[62:63]\n" \
    "v_cvt_pk_bf16_f32 v68, v72, v73\nv_cvt_pk_bf16_f32 v69, v74, v75\nv_cvt_pk_bf16_f32 v70, v76, v77\nv_cvt_pk_bf16_f32 v71, v78, v79\nv_min_u32 v68, -2, v68\n" \
    "v_min_u32 v69, -2, v69\nv_min_u32 v70, -2, v70\nv_min_u32 v71, -2, v71\nbuffer_store_dwordx4 v[68:71], v0, s[64:67], s82 offen offset:1024 sc1\ns_branch .Lf16_%=\n" \
    ".Lf7_%=:\nv_mov_b32 v120, 0\nv_mov_b32 v121, v1\ns_mov_b32 s76, 16\n.Lf10_%=:\nds_read_b128 v[124:127], v121 offset:0\nds_read_b128 v[128:131], v121 offset:1024\n" \
    "ds_read_b128 v[132:135], v121 offset:2048\nds_read_b128 v[136:139], v121 offset:3072\nv_add_u32 v121, 0x1000, v121\ns_waitcnt lgkmcnt(0)\n" \
    "v_max3_u32 v120, v120, v124, v125\nv_max3_u32 v120, v120, v126, v127\nv_max3_u32 v120, v120, v128, v129\nv_max3_u32 v120, v120, v130, v131\n" \
    "v_max3_u32 v120, v120, v132, v133\nv_max3_u32 v120, v120, v134, v135\nv_max3_u32 v120, v120, v136, v137\nv_max3_u32 v120, v120, v138, v139\ns_sub_u32 s76, s76, 1\n" \
    "s_cmp_lg_u32 s76, 0\ns_cbranch_scc1 .Lf10_%=\nv_cmp_eq_u32 vcc, -1, v120\ns_nop 0\ns_cbranch_vccz .Lf8_%=\n.Lf9_%=:\ns_mov_b32 s81, 0\ns_barrier\ns_sleep 4\n" \
    "s_add_u32 s68, s68, 1\ns_and_b32 s69, s68, 63\ns_cbranch_scc1 .Lf2_%=\ns_cmp_gt_u32 s68, 0x400000\ns_cbranch_scc1 .Lf1_%=\nbuffer_load_dword v253, off, s[64:67], 0 sc1\n" \
    "s_waitcnt vmcnt(0)\nv_readfirstlane_b32 s69, v253\ns_nop 0\ns_cmp_eq_u32 s69, 1\ns_cbranch_scc0 .Lf2_%=\ns_endpgm\n.Lf1_%=:\nv_mov_b32 v253, 1\n" \
    "buffer_store_dword v253, off, s[64:67], 0 sc1\ns_endpgm\n"
#define ASM_F2 \
    "s_mov_b32 s90, 0x7fff0000\ns_mov_b32 s91, 0x7fff0000\ns_mov_b32 s92, 0x7fff0000\ns_mov_b32 s93, 0x7fff0000\ns_mov_b32 s80, 0\ns_mov_b32 s81, 0\ns_mov_b32 s68, 0\n.Lg2_%=:\n" \
    "s_xor_b32 s85, s38, 512\ns_and_b32 s69, s32, 63\ns_lshl_b32 s69, s69, 19\ns_add_u32 s82, s36, s69\ns_lshr_b32 s69, s82, 8\ns_and_b32 s69, s69, 0x2000\n" \
    "s_xor_b32 s82, s82, s69\ns_add_u32 s69, s32, 1\ns_and_b32 s69, s69, 63\ns_lshl_b32 s69, s69, 19\ns_add_u32 s83, s36, s69\ns_lshr_b32 s69, s83, 8\n" \
    "s_and_b32 s69, s69, 0x2000\ns_xor_b32 s83, s83, s69\ns_add_u32 s84, s37, s80\ns_cmp_eq_u32 s81, 1\ns_cbranch_scc1 .Lg6_%=\ns_add_u32 s77, s32, 1\ns_cmp_eq_u32 s81, 2\n" \
    "s_cbranch_scc1 .Lg5_%=\n.Lg3_%=:\nbuffer_load_dword v150, v3, s[64:67], 0 offen sc1\ns_waitcnt vmcnt(0)\nv_and_b32 v112, 127, v150\nv_cmp_gt_u32 vcc, s77, v112\ns_nop 0\n" \
    "s_cbranch_vccnz .Lg4_%=\n.Lg5_%=:\nv_and_b32 v112, 127, v150\n.Lg11_%=:\ns_sleep 1\ns_memrealtime s[72:73]\ns_waitcnt lgkmcnt(0)\ns_cmp_ge_u32 s77, 64\n" \
    "s_cselect_b32 s69, s60, s34\ns_sub_u32 s72, s72, s69\ns_lshl_b32 s72, s72, 8\nv_sub_u32 v113, s72, v150\nv_cmp_gt_i32 s[74:75], 0, v113\nv_cmp_eq_u32 vcc, s77, v112\n" \
    "s_nop 0\ns_and_b64 vcc, vcc, s[74:75]\ns_cbranch_scc1 .Lg11_%=\n.Lg12_%=:\nbuffer_load_dword v151, v2, s[64:67], s82 offen sc1\ns_add_u32 m0, s84, 0\n" \
    "s_add_u32 s69, s82, s48\nbuffer_load_dwordx4 v0, s[64:67], s69 offen offset:0 sc1 lds\nbuffer_load_dwordx4 v0, s[64:67], s69 offen offset:1024 sc1 lds\n" \
    "s_add_u32 m0, s84, 8192\ns_add_u32 s69, s82, s49\nbuffer_load_dwordx4 v0, s[64:67], s69 offen offset:0 sc1 lds\n" \
    "buffer_load_dwordx4 v0, s[64:67], s69 offen offset:1024 sc1 lds\ns_add_u32 m0, s84, 16384\ns_add_u32 s69, s82, s50\n" \
    "buffer_load_dwordx4 v0, s[64:67], s69 offen offset:0 sc1 lds\nbuffer_load_dwordx4 v0, s[64:67], s69 offen offset:1024 sc1 lds\n.Lg6_%=:\ns_waitcnt vmcnt(6)\n" \
    "v_cmp_eq_u32 vcc, -1, v151\ns_nop 0\ns_cbranch_vccnz .Lg13_%=\ns_add_u32 s77, s32, 2\ns_add_u32 m0, s84, 24576\ns_add_u32 s69, s82, s51\n" \
    "buffer_load_dwordx4 v0, s[64:67], s69 offen offset:0 sc1 lds\nbuffer_load_dwordx4 v0, s[64:67], s69 offen offset:1024 sc1 lds\ns_waitcnt vmcnt(6)\ns_barrier\n" \
    "ds_read_b128 v[64:67], v1 offset:0\nds_read_b128 v[68:71], v1 offset:1024\nds_read_b128 v[72:75], v1 offset:2048\nds_read_b128 v[76:79], v1 offset:3072\n" \
    "s_waitcnt lgkmcnt(3)\nv_mfma_f32_32x32x16_bf16 v[48:63], a[0:3], v[64:67], 0\nds_read_b128 v[80:83], v1 offset:4096\ns_add_u32 m0, s84, 32768\ns_add_u32 s69, s82, s52\n" \
    "buffer_load_dwordx4 v0, s[64:67], s69 offen offset:0 sc1 lds\ns_waitcnt lgkmcnt(3)\nv_mfma_f32_32x32x16_bf16 v[48:63], a[4:7], v[68:71], v[48:63]\n" \
    "ds_read_b128 v[84:87], v1 offset:5120\ns_waitcnt lgkmcnt(3)\nv_mfma_f32_32x32x16_bf16 v[48:63], a[8:11], v[72:75], v[48:63]\nds_read_b128 v[88:91], v1 offset:6144\n" \
    "buffer_load_dwordx4 v0, s[64:67], s69 offen offset:1024 sc1 lds\ns_waitcnt lgkmcnt(3)\nv_mfma_f32_32x32x16_bf16 v[48:63], a[12:15], v[76:79], v[48:63]\n" \
    "ds_read_b128 v[92:95], v1 offset:7168\ns_waitcnt vmcnt(6) lgkmcnt(3)\ns_barrier\nv_mfma_f32_32x32x16_bf16 v[48:63], a[16:19], v[80:83], v[48:63]\n" \
    "ds_read_b128 v[64:67], v1 offset:8192\ns_add_u32 m0, s84, 40960\ns_add_u32 s69, s82, s53\nbuffer_load_dwordx4 v0, s[64:67], s69 offen offset:0 sc1 lds\n" \
    "s_waitcnt lgkmcnt(3)\nv_mfma_f32_32x32x16_bf16 v[48:63], a[20:23], v[84:87], v[48:63]\nds_read_b128 v[68:71], v1 offset:9216\ns_waitcnt lgkmcnt(3)\n" \
    "v_mfma_f32_32x32x16_bf16 v[48:63], a[24:27], v[88:91], v[48:63]\nds_read_b128 v[72:75], v1 offset:10240\nbuffer_load_dwordx4 v0, s[64:67], s69 offen offset:1024 sc1 lds\n" \
    "s_waitcnt lgkmcnt(3)\nv_mfma_f32_32x32x16_bf16 v[48:63], a[28:31], v[92:95], v[48:63]\nds_read_b128 v[76:79], v1 offset:11264\ns_waitcnt lgkmcnt(3)\n" \
    "v_mfma_f32_32x32x16_bf16 v[48:63], a[32:35], v[64:67], v[48:63]\nds_read_b128 v[80:83], v1 offset:12288\ns_add_u32 m0, s84, 49152\ns_add_u32 s69, s82, s54\n" \
    "buffer_load_dwordx4 v0, s[64:67], s69 offen offset:0 sc1 lds\ns_waitcnt lgkmcnt(3)\nv_mfma_f32_32x32x16_bf16 v[48:63], a[36:39], v[68:71], v[48:63]\n" \
    "ds_read_b128 v[84:87], v1 offset:13312\ns_waitcnt lgkmcnt(3)\nv_mfma_f32_32x32x16_bf16 v[48:63], a[40:43], v[72:75], v[48:63]\nds_read_b128 v[88:91], v1 offset:14336\n" \
    "buffer_load_dwordx4 v0, s[64:67], s69 offen offset:1024 sc1 lds\ns_waitcnt lgkmcnt(3)\nv_mfma_f32_32x32x16_bf16 v[48:63], a[44:47], v[76:79], v[48:63]\n" \
    "ds_read_b128 v[92:95], v1 offset:15360\ns_waitcnt vmcnt(6) lgkmcnt(3)\ns_barrier\nv_mfma_f32_32x32x16_bf16 v[48:63], a[48:51], v[80:83], v[48:63]\n" \
    "ds_read_b128 v[64:67], v1 offset:16384\ns_and_b32 s69, s32, 3\ns_lshl_b32 s69, s69, 19\ns_add_u32 s86, s56, s69\ns_lshr_b32 s69, s86, 8\ns_and_b32 s69, s69, 0x2000\n" \
    "s_waitcnt lgkmcnt(3)\nv_mfma_f32_32x32x16_bf16 v[48:63], a[52:55], v[84:87], v[48:63]\nds_read_b128 v[68:71], v1 offset:17408\ns_xor_b32 s86, s86, s69\n" \
    "s_and_b32 s69, s32, 3\ns_lshl_b32 s69, s69, 19\ns_add_u32 s87, s57, s69\ns_lshr_b32 s69, s87, 8\ns_waitcnt lgkmcnt(3)\n" \
    "v_mfma_f32_32x32x16_bf16 v[48:63], a[56:59], v[88:91], v[48:63]\nds_read_b128 v[72:75], v1 offset:18432\ns_and_b32 s69, s69, 0x2000\ns_xor_b32 s87, s87, s69\n" \
    "s_and_b32 s69, s32, 3\ns_lshl_b32 s69, s69, 19\ns_add_u32 s88, s58, s69\ns_waitcnt lgkmcnt(3)\nv_mfma_f32_32x32x16_bf16 v[48:63], a[60:63], v[92:95], v[48:63]\n" \
    "ds_read_b128 v[76:79], v1 offset:19456\ns_lshr_b32 s69, s88, 8\ns_and_b32 s69, s69, 0x2000\ns_xor_b32 s88, s88, s69\ns_and_b32 s69, s32, 3\ns_lshl_b32 s69, s69, 19\n" \
    "s_waitcnt lgkmcnt(3)\nv_mfma_f32_32x32x16_bf16 v[48:63], a[64:67], v[64:67], v[48:63]\nds_read_b128 v[80:83], v1 offset:20480\ns_add_u32 s89, s59, s69\n" \
    "s_lshr_b32 s69, s89, 8\ns_and_b32 s69, s69, 0x2000\ns_xor_b32 s89, s89, s69\ns_bfe_u32 s78, s32, 0x10002\ns_waitcnt lgkmcnt(3)\n" \
    "v_mfma_f32_32x32x16_bf16 v[48:63], a[68:71], v[68:71], v[48:63]\nds_read_b128 v[84:87], v1 offset:21504\ns_waitcnt lgkmcnt(3)\n" \
    "v_mfma_f32_32x32x16_bf16 v[48:63], a[72:75], v[72:75], v[48:63]\nds_read_b128 v[88:91], v1 offset:22528\nbuffer_load_dwordx2 v[108:109], v4, s[64:67], s93 offen sc1\n" \
    "s_waitcnt lgkmcnt(3)\nv_mfma_f32_32x32x16_bf16 v[48:63], a[76:79], v[76:79], v[48:63]\nds_read_b128 v[92:95], v1 offset:23552\n" \
    "buffer_load_dwordx4 v[96:99], v0, s[64:67], s90 offen sc1\ns_waitcnt lgkmcnt(3)\nv_mfma_f32_32x32x16_bf16 v[48:63], a[80:83], v[80:83], v[48:63]\n" \
    "ds_read_b128 v[64:67], v1 offset:24576\nbuffer_load_dwordx4 v[100:103], v0, s[64:67], s91 offen sc1\ns_waitcnt lgkmcnt(3)\n" \
    "v_mfma_f32_32x32x16_bf16 v[48:63], a[84:87], v[84:87], v[48:63]\nds_read_b128 v[68:71], v1 offset:25600\nbuffer_load_dwordx4 v[104:107], v0, s[64:67], s92 offen sc1\n" \
    "s_waitcnt lgkmcnt(3)\nv_mfma_f32_32x32x16_bf16 v[48:63], a[88:91], v[88:91], v[48:63]\nds_read_b128 v[72:75], v1 offset:26624\ns_waitcnt lgkmcnt(3)\n" \
    "v_mfma_f32_32x32x16_bf16 v[48:63], a[92:95], v[92:95], v[48:63]\nds_read_b128 v[76:79], v1 offset:27648\ns_waitcnt lgkmcnt(3)\n" \
    "v_mfma_f32_32x32x16_bf16 v[48:63], a[96:99], v[64:67], v[48:63]\nds_read_b128 v[80:83], v1 offset:28672\ns_waitcnt lgkmcnt(3)\n" \
    "v_mfma_f32_32x32x16_bf16 v[48:63], a[100:103], v[68:71], v[48:63]\nds_read_b128 v[84:87], v1 offset:29696\ns_waitcnt lgkmcnt(3)\n" \
    "v_mfma_f32_32x32x16_bf16 v[48:63], a[104:107], v[72:75], v[48:63]\nds_read_b128 v[88:91], v1 offset:30720\ns_waitcnt lgkmcnt(3)\n" \
    "v_mfma_f32_32x32x16_bf16 v[48:63], a[108:111], v[76:79], v[48:63]\nds_read_b128 v[92:95], v1 offset:31744\ns_waitcnt vmcnt(6) lgkmcnt(3)\ns_barrier\n" \
    "v_mfma_f32_32x32x16_bf16 v[48:63], a[112:115], v[80:83], v[48:63]\nds_read_b128 v[64:67], v1 offset:32768\ns_waitcnt lgkmcnt(3)\n" \
    "v_mfma_f32_32x32x16_bf16 v[48:63], a[116:119], v[84:87], v[48:63]\nds_read_b128 v[68:71], v1 offset:33792\ns_waitcnt lgkmcnt(3)\n" \
    "v_mfma_f32_32x32x16_bf16 v[48:63], a[120:123], v[88:91], v[48:63]\nds_read_b128 v[72:75], v1 offset:34816\ns_waitcnt lgkmcnt(3)\n" \
    "v_mfma_f32_32x32x16_bf16 v[48:63], a[124:127], v[92:95], v[48:63]\nds_read_b128 v[76:79], v1 offset:35840\ns_waitcnt lgkmcnt(3)\n" \
    "v_mfma_f32_32x32x16_bf16 v[48:63], a[128:131], v[64:67], v[48:63]\nds_read_b128 v[80:83], v1 offset:36864\nbuffer_load_dword v150, v3, s[64:67], 0 offen sc1\n" \
    "s_waitcnt lgkmcnt(3)\nv_mfma_f32_32x32x16_bf16 v[48:63], a[132:135], v[68:71], v[48:63]\nds_read_b128 v[84:87], v1 offset:37888\ns_waitcnt lgkmcnt(3)\n" \
    "v_mfma_f32_32x32x16_bf16 v[48:63], a[136:139], v[72:75], v[48:63]\nds_read_b128 v[88:91], v1 offset:38912\ns_waitcnt lgkmcnt(3)\n" \
    "v_mfma_f32_32x32x16_bf16 v[48:63], a[140:143], v[76:79], v[48:63]\nds_read_b128 v[92:95], v1 offset:39936\ns_waitcnt lgkmcnt(3)\n" \
    "v_mfma_f32_32x32x16_bf16 v[48:63], a[144:147], v[80:83], v[48:63]\nds_read_b128 v[64:67], v1 offset:40960\ns_waitcnt lgkmcnt(3)\n" \
    "v_mfma_f32_32x32x16_bf16 v[48:63], a[148:151], v[84:87], v[48:63]\nds_read_b128 v[68:71], v1 offset:41984\ns_waitcnt vmcnt(1)\ns_waitcnt lgkmcnt(3)\n" \
    "v_max_u32 v116, v108, v109\nv_cmp_eq_u32 s[70:71], -1, v116\nv_mfma_f32_32x32x16_bf16 v[48:63], a[152:155], v[88:91], v[48:63]\nds_read_b128 v[72:75], v1 offset:43008\n" \
    "s_waitcnt lgkmcnt(3)\nv_cndmask_b32 v116, 0, 1, s[70:71]\nv_xor_b32 v96, s79, v96\nv_xor_b32 v97, s79, v97\nv_xor_b32 v98, s79, v98\nv_xor_b32 v99, s79, v99\n" \
    "v_mfma_f32_32x32x16_bf16 v[48:63], a[156:159], v[92:95], v[48:63]\nds_read_b128 v[76:79], v1 offset:44032\ns_waitcnt lgkmcnt(3)\nv_xor_b32 v100, s79, v100\n" \
    "v_xor_b32 v101, s79, v101\nv_xor_b32 v102, s79, v102\nv_xor_b32 v103, s79, v103\nv_xor_b32 v104, s79, v104\n" \
    "v_mfma_f32_32x32x16_bf16 v[48:63], a[160:163], v[64:67], v[48:63]\nds_read_b128 v[80:83], v1 offset:45056\ns_waitcnt lgkmcnt(3)\nv_xor_b32 v105, s79, v105\n" \
    "v_xor_b32 v106, s79, v106\nv_xor_b32 v107, s79, v107\nv_or3_b32 v116, v116, v96, v97\nv_or3_b32 v116, v116, v98, v99\n" \
    "v_mfma_f32_32x32x16_bf16 v[48:63], a[164:167], v[68:71], v[48:63]\nds_read_b128 v[84:87], v1 offset:46080\ns_waitcnt lgkmcnt(3)\nv_or3_b32 v116, v116, v100, v101\n" \
    "v_or3_b32 v116, v116, v102, v103\nv_or3_b32 v116, v116, v104, v105\nv_or3_b32 v116, v116, v106, v107\nv_and_b32 v116, 1, v116\n" \
    "v_mfma_f32_32x32x16_bf16 v[48:63], a[168:171], v[72:75], v[48:63]\nds_read_b128 v[88:91], v1 offset:47104\ns_waitcnt lgkmcnt(3)\nv_cmp_eq_u32 s[70:71], 1, v116\n" \
    "v_add_f32 v96, v96, v16\nv_add_f32 v97, v97, v17\nv_add_f32 v98, v98, v18\nv_add_f32 v99, v99, v19\nv_mfma_f32_32x32x16_bf16 v[48:63], a[172:175], v[76:79], v[48:63]\n" \
    "ds_read_b128 v[92:95], v1 offset:48128\ns_waitcnt lgkmcnt(3)\ns_barrier\nv_add_f32 v100, v100, v104\nv_add_f32 v101, v101, v105\nv_add_f32 v102, v102, v106\n" \
    "v_add_f32 v103, v103, v107\nv_add_f32 v96, v96, v100\nv_mfma_f32_32x32x16_bf16 v[48:63], a[176:179], v[80:83], v[48:63]\nds_read_b128 v[64:67], v1 offset:49152\n" \
    "s_waitcnt lgkmcnt(3)\nv_add_f32 v97, v97, v101\nv_add_f32 v98, v98, v102\nv_add_f32 v99, v99, v103\nv_cvt_pk_bf16_f32 v96, 0, v96\nv_cvt_pk_bf16_f32 v97, 0, v97\n" \
    "v_mfma_f32_32x32x16_bf16 v[48:63], a[180:183], v[84:87], v[48:63]\nds_read_b128 v[68:71], v1 offset:50176\ns_waitcnt lgkmcnt(3)\nv_cvt_pk_bf16_f32 v98, 0, v98\n" \
    "v_cvt_pk_bf16_f32 v99, 0, v99\nv_add_f32 v96, v96, v8\nv_add_f32 v97, v97, v9\nv_add_f32 v98, v98, v10\nv_mfma_f32_32x32x16_bf16 v[48:63], a[184:187], v[88:91], v[48:63]\n" \
    "ds_read_b128 v[72:75], v1 offset:51200\ns_waitcnt lgkmcnt(3)\nv_add_f32 v99, v99, v11\nv_cvt_pk_bf16_f32 v96, 0, v96\nv_cvt_pk_bf16_f32 v97, 0, v97\n" \
    "v_cvt_pk_bf16_f32 v98, 0, v98\nv_cvt_pk_bf16_f32 v99, 0, v99\nv_mfma_f32_32x32x16_bf16 v[48:63], a[188:191], v[92:95], v[48:63]\nds_read_b128 v[76:79], v1 offset:52224\n" \
    "s_waitcnt lgkmcnt(3)\nv_mul_f32 v96, v96, v12\nv_mul_f32 v97, v97, v13\nv_mul_f32 v98, v98, v14\nv_mul_f32 v99, v99, v15\nv_cvt_pk_bf16_f32 v96, 0, v96\n" \
    "v_mfma_f32_32x32x16_bf16 v[48:63], a[192:195], v[64:67], v[48:63]\nds_read_b128 v[80:83], v1 offset:53248\ns_waitcnt lgkmcnt(3)\nv_cvt_pk_bf16_f32 v97, 0, v97\n" \
    "v_cvt_pk_bf16_f32 v98, 0, v98\nv_cvt_pk_bf16_f32 v99, 0, v99\nv_lshlrev_b32 v100, 16, v108\nv_and_b32 v101, 0xffff0000, v108\n" \
    "v_mfma_f32_32x32x16_bf16 v[48:63], a[196:199], v[68:71], v[48:63]\nds_read_b128 v[84:87], v1 offset:54272\ns_waitcnt lgkmcnt(3)\nv_lshlrev_b32 v102, 16, v109\n" \
    "v_and_b32 v103, 0xffff0000, v109\nv_add_f32 v96, v96, v100\nv_add_f32 v97, v97, v101\nv_add_f32 v98, v98, v102\n" \
    "v_mfma_f32_32x32x16_bf16 v[48:63], a[200:203], v[72:75], v[48:63]\nds_read_b128 v[88:91], v1 offset:55296\ns_waitcnt vmcnt(0)\ns_waitcnt lgkmcnt(3)\n" \
    "v_and_b32 v112, 127, v150\nv_cmp_gt_u32 vcc, s77, v112\nv_add_f32 v99, v99, v103\nv_cvt_pk_bf16_f32 v116, v96, v97\nv_cvt_pk_bf16_f32 v117, v98, v99\n" \
    "v_mfma_f32_32x32x16_bf16 v[48:63], a[204:207], v[76:79], v[48:63]\nds_read_b128 v[92:95], v1 offset:56320\ns_cmp_eq_u64 vcc, 0\ns_cselect_b32 s81, 1, 0\n" \
    "s_waitcnt lgkmcnt(3)\nv_mfma_f32_32x32x16_bf16 v[48:63], a[208:211], v[80:83], v[48:63]\ns_waitcnt lgkmcnt(2)\n" \
    "v_mfma_f32_32x32x16_bf16 v[48:63], a[212:215], v[84:87], v[48:63]\ns_waitcnt lgkmcnt(0)\ns_memrealtime s[72:73]\n" \
    "v_mfma_f32_32x32x16_bf16 v[48:63], a[216:219], v[88:91], v[48:63]\nv_mfma_f32_32x32x16_bf16 v[48:63], a[220:223], v[92:95], v[48:63]\ns_waitcnt lgkmcnt(0)\n" \
    "s_cmp_ge_u32 s77, 64\ns_cselect_b32 s69, s60, s34\ns_sub_u32 s72, s72, s69\ns_lshl_b32 s72, s72, 8\nv_sub_u32 v113, s72, v150\nv_cmp_gt_i32 s[74:75], 0, v113\n" \
    "v_cmp_eq_u32 vcc, s77, v112\ns_nop 0\ns_and_b64 vcc, vcc, s[74:75]\ns_cselect_b32 s69, 1, 0\ns_lshl_b32 s81, s81, s69\ns_nop 7\ns_cmp_eq_u32 s90, 0x7fff0000\n" \
    "s_cbranch_scc1 .Lg15_%=\ns_cmp_lg_u64 s[70:71], 0\ns_cbranch_scc1 .Lg16_%=\n.Lg17_%=:\nglobal_store_dwordx2 v5, v[116:117], s[46:47]\nv_add_u32 v5, 0x10000, v5\n" \
    "s_mov_b32 s90, 0x7fff0000\ns_mov_b32 s91, 0x7fff0000\ns_mov_b32 s92, 0x7fff0000\ns_mov_b32 s93, 0x7fff0000\n.Lg15_%=:\ns_nop 1\nv_cmp_u_f32 vcc, v48, v48\ns_nop 0\n" \
    "s_cbranch_vccnz .Lg7_%=\n.Lg8_%=:\nv_and_or_b32 v52, v52, -2, s78\nv_and_or_b32 v53, v53, -2, s78\nv_and_or_b32 v54, v54, -2, s78\nv_and_or_b32 v55, v55, -2, s78\n" \
    "buffer_store_dwordx4 v[52:55], v0, s[64:67], s87 offen sc1\nv_and_or_b32 v56, v56, -2, s78\nv_and_or_b32 v57, v57, -2, s78\nv_and_or_b32 v58, v58, -2, s78\n" \
    "v_and_or_b32 v59, v59, -2, s78\nbuffer_store_dwordx4 v[56:59], v0, s[64:67], s88 offen sc1\nv_and_or_b32 v60, v60, -2, s78\nv_and_or_b32 v61, v61, -2, s78\n" \
    "v_and_or_b32 v62, v62, -2, s78\nv_and_or_b32 v63, v63, -2, s78\nbuffer_store_dwordx4 v[60:63], v0, s[64:67], s89 offen sc1\ns_cmp_lg_u32 s81, 1\ns_cbranch_scc1 .Lg20_%=\n" \
    "s_xor_b32 s84, s84, 0x10000\nbuffer_load_dword v151, v2, s[64:67], s83 offen sc1\ns_add_u32 m0, s84, 0\ns_add_u32 s69, s83, s48\n" \
    "buffer_load_dwordx4 v0, s[64:67], s69 offen offset:0 sc1 lds\nbuffer_load_dwordx4 v0, s[64:67], s69 offen offset:1024 sc1 lds\ns_add_u32 m0, s84, 8192\n" \
    "s_add_u32 s69, s83, s49\nbuffer_load_dwordx4 v0, s[64:67], s69 offen offset:0 sc1 lds\nbuffer_load_dwordx4 v0, s[64:67], s69 offen offset:1024 sc1 lds\n" \
    "s_add_u32 m0, s84, 16384\ns_add_u32 s69, s83, s50\nbuffer_load_dwordx4 v0, s[64:67], s69 offen offset:0 sc1 lds\n" \
    "buffer_load_dwordx4 v0, s[64:67], s69 offen offset:1024 sc1 lds\n.Lg20_%=:\nv_mov_b64 v[16:17], v[48:49]\nv_mov_b64 v[18:19], v[50:51]\ns_add_u32 s90, s86, 0x1000\n" \
    "s_add_u32 s91, s86, 0x4000\ns_add_u32 s92, s86, 0x5000\ns_mov_b32 s93, s82\ns_mov_b32 s79, s78\ns_cmp_lt_u32 s32, 63\ns_cbranch_scc1 .Lg14_%=\ns_memrealtime s[72:73]\n" \
    "s_waitcnt lgkmcnt(0)\ns_add_u32 s76, s72, s35\n.Lg21_%=:\ns_memrealtime s[72:73]\ns_waitcnt lgkmcnt(0)\ns_sub_u32 s72, s72, s76\ns_cmp_lt_i32 s72, 0\n" \
    "s_cbranch_scc1 .Lg21_%=\n.Lg18_%=:\nbuffer_load_dwordx2 v[108:109], v4, s[64:67], s93 offen sc1\nbuffer_load_dwordx4 v[96:99], v0, s[64:67], s90 offen sc1\n" \
    "buffer_load_dwordx4 v[100:103], v0, s[64:67], s91 offen sc1\nbuffer_load_dwordx4 v[104:107], v0, s[64:67], s92 offen sc1\ns_waitcnt vmcnt(0)\nv_max_u32 v116, v108, v109\n" \
    "v_cmp_eq_u32 s[70:71], -1, v116\ns_nop 1\nv_cndmask_b32 v116, 0, 1, s[70:71]\nv_xor_b32 v96, s79, v96\nv_xor_b32 v97, s79, v97\nv_xor_b32 v98, s79, v98\n" \
    "v_xor_b32 v99, s79, v99\nv_xor_b32 v100, s79, v100\nv_xor_b32 v101, s79, v101\nv_xor_b32 v102, s79, v102\nv_xor_b32 v103, s79, v103\nv_xor_b32 v104, s79, v104\n" \
    "v_xor_b32 v105, s79, v105\nv_xor_b32 v106, s79, v106\nv_xor_b32 v107, s79, v107\nv_or3_b32 v116, v116, v96, v97\nv_or3_b32 v116, v116, v98, v99\n" \
    "v_or3_b32 v116, v116, v100, v101\nv_or3_b32 v116, v116, v102, v103\nv_or3_b32 v116, v116, v104, v105\nv_or3_b32 v116, v116, v106, v107\nv_and_b32 v116, 1, v116\n" \
    "v_cmp_eq_u32 s[70:71], 1, v116\nv_pk_add_f32 v[96:97], v[96:97], v[16:17]\nv_pk_add_f32 v[98:99], v[98:99], v[18:19]\nv_pk_add_f32 v[100:101], v[100:101], v[104:105]\n" \
    "v_pk_add_f32 v[102:103], v[102:103], v[106:107]\nv_pk_add_f32 v[96:97], v[96:97], v[100:101]\nv_pk_add_f32 v[98:99], v[98:99], v[102:103]\nv_cvt_pk_bf16_f32 v96, 0, v96\n" \
    "v_cvt_pk_bf16_f32 v97, 0, v97\nv_cvt_pk_bf16_f32 v98, 0, v98\nv_cvt_pk_bf16_f32 v99, 0, v99\nv_pk_add_f32 v[96:97], v[96:97], v[8:9]\n" \
    "v_pk_add_f32 v[98:99], v[98:99], v[10:11]\nv_cvt_pk_bf16_f32 v96, 0, v96\nv_cvt_pk_bf16_f32 v97, 0, v97\nv_cvt_pk_bf16_f32 v98, 0, v98\nv_cvt_pk_bf16_f32 v99, 0, v99\n" \
    "v_pk_mul_f32 v[96:97], v[96:97], v[12:13]\nv_pk_mul_f32 v[98:99], v[98:99], v[14:15]\nv_cvt_pk_bf16_f32 v96, 0, v96\nv_cvt_pk_bf16_f32 v97, 0, v97\n" \
    "v_cvt_pk_bf16_f32 v98, 0, v98\nv_cvt_pk_bf16_f32 v99, 0, v99\nv_lshlrev_b32 v100, 16, v108\nv_and_b32 v101, 0xffff0000, v108\nv_lshlrev_b32 v102, 16, v109\n" \
    "v_and_b32 v103, 0xffff0000, v109\nv_pk_add_f32 v[96:97], v[96:97], v[100:101]\nv_pk_add_f32 v[98:99], v[98:99], v[102:103]\nv_cvt_pk_bf16_f32 v116, v96, v97\n" \
    "v_cvt_pk_bf16_f32 v117, v98, v99\ns_cmp_lg_u64 s[70:71], 0\ns_cbranch_scc1 .Lg19_%=\nglobal_store_dwordx2 v5, v[116:117], s[46:47]\nv_add_u32 v5, 0x10000, v5\n" \
    "s_mov_b32 s90, 0x7fff0000\ns_mov_b32 s91, 0x7fff0000\ns_mov_b32 s92, 0x7fff0000\ns_mov_b32 s93, 0x7fff0000\n.Lg14_%=:\ns_mov_b32 s38, s85\ns_xor_b32 s80, s80, 0x10000\n" \
    "v_xor_b32 v1, 0x10000, v1\ns_mov_b32 s68, 0\ns_add_u32 s32, s32, 1\ns_cmp_lt_u32 s32, 64\ns_cbranch_scc1 .Lg2_%=\ns_endpgm\n.Lg4_%=:\ns_sleep 8\ns_add_u32 s68, s68, 1\n" \
    "s_and_b32 s69, s68, 63\ns_cbranch_scc1 .Lg3_%=\ns_cmp_gt_u32 s68, 0x400000\ns_cbranch_scc1 .Lg1_%=\nbuffer_load_dword v253, off, s[64:67], 0 sc1\ns_waitcnt vmcnt(0)\n" \
    "v_readfirstlane_b32 s69, v253\ns_nop 0\ns_cmp_eq_u32 s69, 1\ns_cbranch_scc0 .Lg3_%=\ns_endpgm\n.Lg13_%=:\ns_waitcnt vmcnt(0)\ns_add_u32 s68, s68, 1\n" \
    "s_and_b32 s69, s68, 63\ns_cbranch_scc1 .Lg12_%=\ns_cmp_gt_u32 s68, 0x400000\ns_cbranch_scc1 .Lg1_%=\nbuffer_load_dword v253, off, s[64:67], 0 sc1\ns_waitcnt vmcnt(0)\n" \
    "v_readfirstlane_b32 s69, v253\ns_nop 0\ns_cmp_eq_u32 s69, 1\ns_cbranch_scc0 .Lg12_%=\ns_endpgm\n.Lg16_%=:\ns_sleep 4\n" \
    "buffer_load_dwordx2 v[108:109], v4, s[64:67], s93 offen sc1\nbuffer_load_dwordx4 v[96:99], v0, s[64:67], s90 offen sc1\n" \
    "buffer_load_dwordx4 v[100:103], v0, s[64:67], s91 offen sc1\nbuffer_load_dwordx4 v[104:107], v0, s[64:67], s92 offen sc1\ns_waitcnt vmcnt(0)\nv_max_u32 v116, v108, v109\n" \
    "v_cmp_eq_u32 s[70:71], -1, v116\ns_nop 1\nv_cndmask_b32 v116, 0, 1, s[70:71]\nv_xor_b32 v96, s79, v96\nv_xor_b32 v97, s79, v97\nv_xor_b32 v98, s79, v98\n" \
    "v_xor_b32 v99, s79, v99\nv_xor_b32 v100, s79, v100\nv_xor_b32 v101, s79, v101\nv_xor_b32 v102, s79, v102\nv_xor_b32 v103, s79, v103\nv_xor_b32 v104, s79, v104\n" \
    "v_xor_b32 v105, s79, v105\nv_xor_b32 v106, s79, v106\nv_xor_b32 v107, s79, v107\nv_or3_b32 v116, v116, v96, v97\nv_or3_b32 v116, v116, v98, v99\n" \
    "v_or3_b32 v116, v116, v100, v101\nv_or3_b32 v116, v116, v102, v103\nv_or3_b32 v116, v116, v104, v105\nv_or3_b32 v116, v116, v106, v107\nv_and_b32 v116, 1, v116\n" \
    "v_cmp_eq_u32 s[70:71], 1, v116\nv_pk_add_f32 v[96:97], v[96:97], v[16:17]\nv_pk_add_f32 v[98:99], v[98:99], v[18:19]\nv_pk_add_f32 v[100:101], v[100:101], v[104:105]\n" \
    "v_pk_add_f32 v[102:103], v[102:103], v[106:107]\nv_pk_add_f32 v[96:97], v[96:97], v[100:101]\nv_pk_add_f32 v[98:99], v[98:99], v[102:103]\nv_cvt_pk_bf16_f32 v96, 0, v96\n" \
    "v_cvt_pk_bf16_f32 v97, 0, v97\nv_cvt_pk_bf16_f32 v98, 0, v98\nv_cvt_pk_bf16_f32 v99, 0, v99\nv_pk_add_f32 v[96:97], v[96:97], v[8:9]\n" \
    "v_pk_add_f32 v[98:99], v[98:99], v[10:11]\nv_cvt_pk_bf16_f32 v96, 0, v96\nv_cvt_pk_bf16_f32 v97, 0, v97\nv_cvt_pk_bf16_f32 v98, 0, v98\nv_cvt_pk_bf16_f32 v99, 0, v99\n" \
    "v_pk_mul_f32 v[96:97], v[96:97], v[12:13]\nv_pk_mul_f32 v[98:99], v[98:99], v[14:15]\nv_cvt_pk_bf16_f32 v96, 0, v96\nv_cvt_pk_bf16_f32 v97, 0, v97\n" \
    "v_cvt_pk_bf16_f32 v98, 0, v98\nv_cvt_pk_bf16_f32 v99, 0, v99\nv_lshlrev_b32 v100, 16, v108\nv_and_b32 v101, 0xffff0000, v108\nv_lshlrev_b32 v102, 16, v109\n" \
    "v_and_b32 v103, 0xffff0000, v109\nv_pk_add_f32 v[96:97], v[96:97], v[100:101]\nv_pk_add_f32 v[98:99], v[98:99], v[102:103]\nv_cvt_pk_bf16_f32 v116, v96, v97\n" \
    "v_cvt_pk_bf16_f32 v117, v98, v99\ns_cmp_lg_u64 s[70:71], 0\ns_cbranch_scc0 .Lg17_%=\ns_add_u32 s68, s68, 1\ns_and_b32 s69, s68, 63\ns_cbranch_scc1 .Lg16_%=\n" \
    "s_cmp_gt_u32 s68, 0x400000\ns_cbranch_scc1 .Lg1_%=\nbuffer_load_dword v253, off, s[64:67], 0 sc1\ns_waitcnt vmcnt(0)\nv_readfirstlane_b32 s69, v253\ns_nop 0\n" \
    "s_cmp_eq_u32 s69, 1\ns_cbranch_scc0 .Lg16_%=\ns_endpgm\n.Lg19_%=:\ns_sleep 4\ns_add_u32 s68, s68, 1\ns_and_b32 s69, s68, 63\ns_cbranch_scc1 .Lg18_%=\n" \
    "s_cmp_gt_u32 s68, 0x400000\ns_cbranch_scc1 .Lg1_%=\nbuffer_load_dword v253, off, s[64:67], 0 sc1\ns_waitcnt vmcnt(0)\nv_readfirstlane_b32 s69, v253\ns_nop 0\n" \
    "s_cmp_eq_u32 s69, 1\ns_cbranch_scc0 .Lg18_%=\ns_endpgm\n.Lg7_%=:\nv_mov_b32 v120, 0\nv_mov_b32 v121, v1\ns_mov_b32 s76, 14\n.Lg10_%=:\n" \
    "ds_read_b128 v[124:127], v121 offset:0\nds_read_b128 v[128:131], v121 offset:1024\nds_read_b128 v[132:135], v121 offset:2048\nds_read_b128 v[136:139], v121 offset:3072\n" \
    "v_add_u32 v121, 0x1000, v121\ns_waitcnt lgkmcnt(0)\nv_max3_u32 v120, v120, v124, v125\nv_max3_u32 v120, v120, v126, v127\nv_max3_u32 v120, v120, v128, v129\n" \
    "v_max3_u32 v120, v120, v130, v131\nv_max3_u32 v120, v120, v132, v133\nv_max3_u32 v120, v120, v134, v135\nv_max3_u32 v120, v120, v136, v137\n" \
    "v_max3_u32 v120, v120, v138, v139\ns_sub_u32 s76, s76, 1\ns_cmp_lg_u32 s76, 0\ns_cbranch_scc1 .Lg10_%=\nv_cmp_eq_u32 vcc, -1, v120\ns_nop 0\ns_cbranch_vccz .Lg8_%=\n" \
    ".Lg9_%=:\ns_mov_b32 s81, 0\ns_barrier\ns_sleep 4\ns_add_u32 s68, s68, 1\ns_and_b32 s69, s68, 63\ns_cbranch_scc1 .Lg2_%=\ns_cmp_gt_u32 s68, 0x400000\n" \
    "s_cbranch_scc1 .Lg1_%=\nbuffer_load_dword v253, off, s[64:67], 0 sc1\ns_waitcnt vmcnt(0)\nv_readfirstlane_b32 s69, v253\ns_nop 0\ns_cmp_eq_u32 s69, 1\n" \
    "s_cbranch_scc0 .Lg2_%=\ns_endpgm\n.Lg1_%=:\nv_mov_b32 v253, 1\nbuffer_store_dword v253, off, s[64:67], 0 sc1\ns_endpgm\n"
typedef unsigned v32u __attribute__((ext_vector_type(32)));
typedef unsigned v16u __attribute__((ext_vector_type(16)));
#define ORD 1
template <bool isV>
__device__ __forceinline__ void s1_main(const Args& a, unsigned char* lds, const int xg, const int ii) {
    const int wave = a.pad0, head = xg * 4 + (ii >> 2), set = (ii >> 1) & 1;
    char* const ws = a.ws;
    unsigned* const ctl = (unsigned*)(ws + O_CTL);
    const unsigned pol = __builtin_amdgcn_readfirstlane(*(const unsigned*)(ws + O_POL));
    const unsigned pubb = isV ? O_UM + (wave & 1) * 262144 + head * 256 : O_EP + ((wave & 1) * 4 + (head >> 3)) * 32768;
    const unsigned psb = isV ? O_UX + (wave & 1) * MiB + (head >> 3) * 16384 + (head & 7) * 1024 : O_PS + (wave * 4 + (head >> 3)) * 16384;
    v16bf w[52], pend[12];
    v4u rX = mkrs(isV ? a.Wv : wave < 2 ? a.Wq : a.Wk, DM * HD * 2), rO = mkrs(a.Wo, DM * HD * 2);
    unsigned bo[4];
#pragma unroll
    for (int i = 0; i < 4; ++i) {
        const int lane = lane_id(), row = lane >> 3, ch = (lane & 7) ^ ((2 * (i & 1) + (row >> 2)) << 1);
        bo[i] = i < 2 ? (((isV ? 64 * (wave >> 1) : 0) + row) * HD + head * DH + 64 * (wave & 1) + 8 * ch) * 2 : ((head * DH + row) * DM + 256 * wave + 8 * ch) * 2;
    }
    asm volatile("s_nop 4" : "+s"(rX), "+s"(rO));
    const int kx = ORD * set;
    {
        const int lane = lane_id(), l15 = lane & 15, gb = lane >> 4;
        auto bufp = [&](int u, int wv) { const int b = (u + 4) % 5; return lds + (b < 3 ? L_W + wv * 24576 + b * 8192 : L_H + wv * 16384 + (b - 3) * 8192); };
        auto issue = [&](auto uu) {
            constexpr int u = decltype(uu)::v, c = u;
            constexpr bool wo = isV && c >= 8;
            const unsigned d = (unsigned)(size_t)(LAS unsigned char*)bufp(u, wave);
#pragma unroll
            for (int r8 = 0; r8 < 8; ++r8)
                bdma<(NTW & (u < 4 ? 1 : u < 8 ? 2 : 64))>(wo ? rO : rX, wo ? bo[2 + (r8 & 1)] : bo[r8 & 1], wo ? ((64 * (c & 1) + 8 * r8) * DM + 64 * ((c - 8) >> 1)) * 2 : (((isV ? 128 : 64) * c + 8 * r8) ^ (512 * kx)) * HD * 2, d + r8 * 1024);
        };
        const unsigned q = l15 >> 2, p = lane & 3;
        const unsigned row0 = 16 * (gb >> 1) + 4 * (gb & 1) + (q & 1) + 8 * (q >> 1);
        const unsigned sw = ((gb & 1) | ((q >> 1) << 1)) << 1;
        auto flush = [&](int j) {
            const v8u v = __builtin_bit_cast(v8u, pend[j]);
            unsigned char* d = lds + L_W + wave * 24576 + j * 2048 + lane * 16;
            *(LAS v4u*)d = (v4u){v[0], v[1], v[2], v[3]};
            *(LAS v4u*)(d + 1024) = (v4u){v[4], v[5], v[6], v[7]};
        };
        sfor<0, 5>(issue);
        sfor<0, 16>([&](auto cc) {
            constexpr int u = decltype(cc)::v, c = u;
            asm volatile("s_waitcnt vmcnt(%0)\n\ts_barrier" :: "n"(u == 0 ? 32 : u < 13 ? 24 : 8 * (15 - u)) : "memory");
            if constexpr (u > 0 && u + 4 < 16) issue(IC<u + 4>{});
            if (u >= 12 && u < 15) { flush(4 * u - 48); flush(4 * u - 47); flush(4 * u - 46); flush(4 * u - 45); }
            v16bf t[4];
#pragma unroll
            for (int n = 0; n < 4; ++n) {
                constexpr bool pk = isV && c >= 8;
                const unsigned char* buf = bufp(u, pk ? wave : (n & 2) + (wave >> 1));
                const unsigned o = pk ? (32 * (n & 1) + 16 * (gb & 1) + 4 * (gb >> 1) + (q & 1)) * 128 + (((4 * (n >> 1) + 2 * (q >> 1) + (p >> 1)) ^ (gb & 2)) << 4) + (p & 1) * 8
                                      : row0 * 128 + (((4 * (wave & 1) + 2 * (n & 1) + (p >> 1)) ^ sw) << 4) + (p & 1) * 8;
                const unsigned o2 = pk ? (o ^ 64) + 8 * 128 : o + 32 * 128;
                const v2u r0 = tr_rd(buf, o), r1 = tr_rd(buf, o + 2 * 128), r2 = tr_rd(buf, o2), r3 = tr_rd(buf, o2 + 2 * 128);
                const v8u v = {r0.x, r0.y, r1.x, r1.y, r2.x, r2.y, r3.x, r3.y};
                t[n] = __builtin_bit_cast(v16bf, v);
            }
            asm volatile("s_waitcnt lgkmcnt(0)" ::: "memory");
            sfor<0, 4>([&](auto nn) {
                constexpr int n = decltype(nn)::v, j = lsl(isV, c, n), r = ridx(isV, c, n);
                if constexpr (j >= 0) { pend[j] = t[n]; asm volatile("" : "+v"(pend[j])); }
                else {
                    w[r] = t[n];
                    if constexpr (r < 32) asm volatile("" : "+a"(w[r])); else asm volatile("" : "+v"(w[r]));
                }
            });
        });
    }
    RSTAMP(74, 0);
    if (!wave_wait<8>(ctl + C_A, 1, 1, NARM, ctl + C_ABORT)) asm volatile("s_endpgm");
    lds_barrier();
    RSTAMP(74, 1);
    const unsigned lb0 = (unsigned)(size_t)(LAS unsigned char*)lds;
    const unsigned lane = lane_id(), l15 = lane & 15, g = lane >> 4, q = l15 >> 2, p = lane & 3, l16 = lane * 16, to = g * 128 + (l15 & 7) * 16, xb = ((g & 1) << 1) | (g >> 1);
    const unsigned r = 8 * wave + (lane >> 3), ao = r * 2048 + 16 * ((lane & 7) ^ ((r >> 1) & 7)) + 1024, ra = lb0 + L_H + (l15 >> 3) * 2048 + (l15 & 7) * 128;
    const v16u c0 = {(lane & 8) ? 0xEEEEu : 0x4444u, lb0 + L_H + l16, l16, lb0 + L_W + wave * 24576 + l16, lb0 + L_T + wave * 512 + q * 128 + (g & 1) * 64 + (g >> 1) * 32 + p * 2,
                     lb0 + (isV ? L_T + to : L_QX + (wave >> 1) * 4096 + (wave & 1) * 128 + g * 128 + to), isV ? lb0 + L_QX + g * 128 + to : lb0 + L_T + to,
                     isV ? xb * 16384 + l15 * 16 : l15 < 8 ? (head & 7) * 512 + g * 128 + l15 * 16 : g ? 0x7FFF0000u : 4096 + (head & 7) * 128 + (l15 & 7) * 16, isV ? xb * 65536 + l15 * 16 : 0xFFFFFFFEu,
                     ra + 16 * (g ^ (l15 >> 1)), ra + 16 * ((4 + g) ^ (l15 >> 1)), ao, ao - 896,
                     0u, lb0 + (isV ? L_QX : L_KX) + wave * 1024 + l16, 0u};
    const unsigned sd = ((pol ^ (wave >> 1)) & 1) * 8192;
    PARS;
    const v16u s0 = {0x3e0293eeu, 0x3db504f3u, 0x3fb8aa3bu, (unsigned)set, (unsigned)wave, O_E1 + (((xg >> 1) & 1) * 4 + wave) * 16384 + ((pol ^ (xg >> 2)) & 1) * 8192 + 2048 * kx, pubb + sd, psb + (isV ? sd : 0),
                     O_H1 - 1024 + set * 65536, lb0 + L_H + wave * 2048, hw(pol, xg) + H_C + 512 * set, par(P_R), par(P_A) + par(P_B), par(P_X), lb0 + L_ST + (wave == STAMP_WAVE ? 0 : MiB), lb0 + L_ST};
    u64 cm[4];
#pragma unroll
    for (int i = 0; i < 4; ++i) cm[i] = __ballot(16 * (l15 >> 3) + 4 * g + i <= 8 * wave + (l15 & 7));
    const u64 dbg = DBG(a, blockIdx.x);
    const v16u s1 = {isV ? (unsigned)(C_F + 32 * (128 * set + 32 * wave + head)) * 4 : 0x00FFFFFFu, isV ? set : 0x00FF00FFu, isV ? set : (unsigned)cm[0], (unsigned)(cm[0] >> 32), (unsigned)cm[1], (unsigned)(cm[1] >> 32),
                     (unsigned)cm[2], (unsigned)(cm[2] >> 32), (unsigned)cm[3], (unsigned)(cm[3] >> 32), (unsigned)dbg, (unsigned)(dbg >> 32), (unsigned)(L_QX - L_T + wave * 2048), ii < 4 ? hw(pol, xg) + H_D + 4 * wave : 0u, 1024u * kx,
                     head < (isV ? 20 : 28) ? O_D + (isV ? D_X2 : 0) + head * 8192 + wave * 2048 : 0x70000000u};
    const v4u rws = mkrs(ws, WS_END);
    v32u ww[13];
#pragma unroll
    for (int i = 0; i < 13; ++i) {
        const v8u x0 = __builtin_bit_cast(v8u, w[4 * i]), x1 = __builtin_bit_cast(v8u, w[4 * i + 1]), x2 = __builtin_bit_cast(v8u, w[4 * i + 2]), x3 = __builtin_bit_cast(v8u, w[4 * i + 3]);
        ww[i] = __builtin_shufflevector(__builtin_shufflevector(x0, x1, 0, 1, 2, 3, 4, 5, 6, 7, 8, 9, 10, 11, 12, 13, 14, 15), __builtin_shufflevector(x2, x3, 0, 1, 2, 3, 4, 5, 6, 7, 8, 9, 10, 11, 12, 13, 14, 15),
                                        0, 1, 2, 3, 4, 5, 6, 7, 8, 9, 10, 11, 12, 13, 14, 15, 16, 17, 18, 19, 20, 21, 22, 23, 24, 25, 26, 27, 28, 29, 30, 31);
    }
#define S1_CALL(TXT) asm volatile("s_nop 4\n" TXT :: "{v[0:15]}"(c0), "{s[32:47]}"(s0), "{s[48:63]}"(s1), "{s[64:67]}"(rws), \
              "{a[0:31]}"(ww[0]), "{a[32:63]}"(ww[1]), "{a[64:95]}"(ww[2]), "{a[96:127]}"(ww[3]), "{a[128:159]}"(ww[4]), "{a[160:191]}"(ww[5]), "{a[192:223]}"(ww[6]), "{a[224:255]}"(ww[7]), \
              "{v[92:123]}"(ww[8]), "{v[124:155]}"(ww[9]), "{v[156:187]}"(ww[10]), "{v[188:219]}"(ww[11]), "{v[220:251]}"(ww[12]) : "memory", "s95", "v253")
    if constexpr (isV) S1_CALL(ASM_V); else S1_CALL(ASM_Q);
}

__device__ __forceinline__ float gelu_tanh(float v) {
    const float e = v * (-2.302208198f - 0.1029432397f * (v * v));
    return v * __builtin_amdgcn_rcpf(1.f + __builtin_amdgcn_exp2f(e));
}
__device__ __forceinline__ v4u unsent4(v4u v) { return (v4u){unsent(v.x), unsent(v.y), unsent(v.z), unsent(v.w)}; }
__device__ __forceinline__ unsigned rt_now() { return (unsigned)__builtin_amdgcn_s_memrealtime(); }
__device__ __forceinline__ void rt_delay(unsigned ticks  , unsigned t0 = rt_now()) {
    while ((unsigned)__builtin_amdgcn_s_memrealtime() - t0 < ticks) __builtin_amdgcn_s_sleep(2);
}
__device__ __forceinline__ float wave_sum(float x) {
    x += __uint_as_float(__builtin_amdgcn_update_dpp(0, __float_as_uint(x), 0xB1, 0xF, 0xF, false));
    x += __uint_as_float(__builtin_amdgcn_update_dpp(0, __float_as_uint(x), 0x4E, 0xF, 0xF, false));
    x += __uint_as_float(__builtin_amdgcn_update_dpp(0, __float_as_uint(x), 0x124, 0xF, 0xF, false));
    x += __uint_as_float(__builtin_amdgcn_update_dpp(0, __float_as_uint(x), 0x128, 0xF, 0xF, false));
    return red32_add(red16_add(x));
}
__device__ __forceinline__ void ln_row(const v4u (&xr)[2], const f32x4 (&c1)[4], const f32x4 (&sh)[4], v4u (&o)[2]) {
    f32x2 v[8];
    f32x2 s2 = {0.f, 0.f};
#pragma unroll
    for (int j = 0; j < 8; ++j) { v[j] = (f32x2){lo16f(xr[j >> 2][j & 3]), hi16f(xr[j >> 2][j & 3])}; s2 += v[j]; }
    const float mean = wave_sum(s2[0] + s2[1]) * (1.f / DM);
    f32x2 q2 = {0.f, 0.f};
#pragma unroll
    for (int j = 0; j < 8; ++j) { const f32x2 d = v[j] - mean; q2 += d * d; }
    const float vs = wave_sum(q2[0] + q2[1]);
    const float mb = rbf(mean), vb = rbf(vs * (1.f / DM));
    const float rsd = 1.f / rbf(sqrtf(rbf(vb + 1e-5f)));
    auto r2 = [](f32x2 z) { const unsigned u = pack2(z[0], z[1]); return (f32x2){lo16f(u), hi16f(u)}; };
#pragma unroll
    for (int j = 0; j < 8; ++j) {
        const f32x2 cc = {c1[j >> 1][2 * (j & 1)], c1[j >> 1][2 * (j & 1) + 1]}, ss = {sh[j >> 1][2 * (j & 1)], sh[j >> 1][2 * (j & 1) + 1]};
        const f32x2 y = r2(r2(r2(v[j] - mb) * rsd) * cc) + ss;
        o[j >> 2][j & 3] = pack2(y[0], y[1]);
    }
}
template <int N> __device__ __forceinline__ void ld_mod(rsrc_t rs, unsigned* ctl, const int (&col)[N], f32x4* m) {
    for (unsigned spins = 0;;) {
        unsigned mx = 0;
#pragma unroll
        for (int i = 0; i < N; ++i) { const v4u v = bld16(rs, O_MOD + col[i] * 4); mx = max(mx, umax4(v)); m[i] = __builtin_bit_cast(f32x4, v); }
        if (!__any(mx == SENT)) break;
        __builtin_amdgcn_s_sleep(6);
        if (spin_fail(spins, ctl + C_ABORT)) asm volatile("s_endpgm");
    }
}
__device__ __forceinline__ void set_m0(unsigned v) { asm volatile("s_mov_b32 m0, %0" :: "s"(v) : "m0"); }
__device__ __forceinline__ void mod8(const Args& a, int col, f32x4 (&m)[2]) {
    f32x4 p[8][2];
#pragma unroll
    for (int k = 0; k < 16; ++k) p[k >> 1][k & 1] = *(const f32x4*)(a.ws + O_MODP + ((k >> 1) * 2048 + col + 4 * (k & 1)) * 4);
#pragma unroll
    for (int h = 0; h < 2; ++h) {
        const f32x4 t = ((p[0][h] + p[1][h]) + (p[2][h] + p[3][h])) + ((p[4][h] + p[5][h]) + (p[6][h] + p[7][h]));
#pragma unroll
        for (int i = 0; i < 4; ++i) { float v = rbf(t[i]); if (col >= DM) v = rbf(1.f + v); m[h][i] = v; }
    }
}
__device__ __forceinline__ void mod_cols(const Args& a, rsrc_t rs, unsigned char* lds, int cg) {
    const int tid = otid();
    v4u wv[32];
#pragma unroll
    for (int i = 0; i < 32; ++i) wv[i] = __builtin_nontemporal_load((const v4u*)(a.w_mod + (size_t)((tid >> 3) + 32 * i) * 6144 + cg * 64 + 8 * (tid & 7)));
    {
        const v2u cr = *(const v2u*)(a.c + 4 * tid);
        f32x4 sv = {lo16f(cr.x), hi16f(cr.x), lo16f(cr.y), hi16f(cr.y)};
#pragma unroll
        for (int i = 0; i < 4; ++i) sv[i] = rbf(sv[i] / (1.f + __expf(-sv[i])));
        *(LAS f32x4*)(lds + 16384 + tid * 16) = sv;
    }
    lds_barrier();
    f32x2 acc[4] = {{0.f, 0.f}, {0.f, 0.f}, {0.f, 0.f}, {0.f, 0.f}};
#pragma unroll
    for (int i = 0; i < 32; ++i) {
        const float sv = *(const LAS float*)(lds + 16384 + ((tid >> 3) + 32 * i) * 4);
#pragma unroll
        for (int j = 0; j < 4; ++j) acc[j] += sv * (f32x2){lo16f(wv[i][j]), hi16f(wv[i][j])};
    }
    unsigned char* red = lds + 20480;
    *(LAS f32x4*)(red + (tid >> 3) * 272 + (tid & 7) * 32) = (f32x4){acc[0][0], acc[0][1], acc[1][0], acc[1][1]};
    *(LAS f32x4*)(red + (tid >> 3) * 272 + (tid & 7) * 32 + 16) = (f32x4){acc[2][0], acc[2][1], acc[3][0], acc[3][1]};
    lds_barrier();
    if (tid < 64) {
        f32x4 t = {0.f, 0.f, 0.f, 0.f};
#pragma unroll
        for (int i = 0; i < 8; ++i) t += *(const LAS f32x4*)(red + ((tid >> 4) * 8 + i) * 272 + (tid & 15) * 16);
#pragma unroll
        for (int i = 0; i < 4; ++i) { float v = rbf(red32_add(red16_add(t[i]))); if ((cg >> 4) == 4) v = rbf(1.f + v); t[i] = v; }
        if (tid < 16) bst16(rs, O_MOD + (cg * 64 + 4 * tid) * 4, unsent4(__builtin_bit_cast(v4u, t)));
    }
}
#define TD_A 300
#define TD_L 1150
constexpr int NARM0 = 5120;
__device__ __forceinline__ unsigned arm0(unsigned v) {
    if (v < 1280) return (v < 640 ? O_EP : O_PS - MiB) + v / 5 * 8192 + v % 5 * 1024;
    if (v < 2304) return O_E1 + 131072 + (v - 1280) * 1024;
    if (v < 3072) return O_D + (v - 2304) / 384 * D_SLOT + (v - 2304) % 384 * 1024;
    return O_SL + (v - 3072) * 1024;
}
__device__ __forceinline__ void f_start(const Args& a, rsrc_t rs, unsigned char* lds, int idx  , unsigned t0) {
    const int tid = otid(), lane = tid & 63, wave = a.pad0;
    unsigned* const ctl = (unsigned*)(a.ws + O_CTL);
    const int r0 = 4 * idx + wave;
    v4u xr[6][2];
    auto ldx = [&](int k, int row) { for (int c = 0; c < 2; ++c) xr[k][c] = *(const v4u*)(a.x + (size_t)row * DM + 512 * c + 8 * lane); };
    ldx(0, r0);
    {
        f32x4 m[2];
        mod8(a, 8 * tid, m);
        *(LAS f32x4*)(lds + 32 * tid) = m[0]; *(LAS f32x4*)(lds + 32 * tid + 16) = m[1];
    }
    lds_barrier();
    f32x4 c1[4], sh[4];
#pragma unroll
    for (int i = 0; i < 4; ++i) { const int col = 512 * (i >> 1) + 8 * lane + 4 * (i & 1); sh[i] = *(const LAS f32x4*)(lds + col * 4); c1[i] = *(const LAS f32x4*)(lds + (DM + col) * 4); }
    auto row = [&](int k, int r) {
        v4u o[2];
        ln_row(xr[k], c1, sh, o);
#pragma unroll
        for (int c = 0; c < 2; ++c) {
            const bool od = lane & 1;
            const unsigned y0 = __builtin_amdgcn_update_dpp(0, od ? o[c].x : o[c].z, 0xB1, 0xF, 0xF, false), y1 = __builtin_amdgcn_update_dpp(0, od ? o[c].y : o[c].w, 0xB1, 0xF, 0xF, false);
            bst16(rs, O_H1 + (unsigned)(r * DM + 512 * c + 8 * lane) * 2, od ? (v4u){y0, o[c].z, y1, o[c].w} : (v4u){o[c].x, y0, o[c].y, y1});
        }
    };
    row(0, r0);
    if (idx >= 64) {
        rt_delay(TD_A, t0);
        for (unsigned v = r0 - 256; v < NARM0; v += 256) bst16(rs, arm0(v) + lane * 16, sentv());
    }
    drained(ctl + C_A);
    if (idx >= 64) return;
    RSTAMP(75, 2);
    rt_delay(TD_L, t0);
    mod_cols(a, rs, lds, idx + 32);
    if (idx >= 32) return;
    for (int h = 0; h < 2; ++h) {
#pragma unroll
        for (int k = 0; k < 6; ++k) ldx(k, 512 + r0 + 128 * (6 * h + k));
#pragma unroll
        for (int k = 0; k < 6; ++k) row(k, 512 + r0 + 128 * (6 * h + k));
    }
    drained(ctl + C_H);
}
#define TD_W 1300
__device__ __forceinline__ unsigned r_ew(unsigned pol, unsigned lane) { return ((lane >> 4) & 1) * 65536 + (((O_E1 >> 21) ^ pol ^ (lane >> 5)) & 1) * 8192 + (lane & 15) * 16; }
__device__ __forceinline__ unsigned r_e1(int cb) { return O_E1 + ((cb >> 2) & 3) * 16384 + (cb >> 4) * 1024 + (cb & 3) * 256; }
__device__ __forceinline__ unsigned r_db(int cb) { return D_X2 + (cb >> 3) * 16384 + (cb & 7) * 1024; }
__device__ __forceinline__ unsigned r_st(int cb, unsigned lane) { return D_ST + (cb >> 5) * 16384 + (lane >> 5) * 8192 + (cb & 31) * 256 + (lane & 31) * 8; }
__device__ __forceinline__ void r_main(const Args& a, rsrc_t rs, unsigned char* lds, int xg, int i8, unsigned* ctl) {
    const unsigned lane = lane_id(), wave = a.pad0, l15 = lane & 15, g = lane >> 4, q = l15 >> 2, p = lane & 3, cb = 8 * xg + i8;
    const unsigned pol = __builtin_amdgcn_readfirstlane(*(const unsigned*)(a.ws + O_POL)), lb = (unsigned)(size_t)(LAS unsigned char*)lds;
    *(volatile LAS unsigned*)(lds + 4096) = 0u;
    if (!wave_wait<64>(ctl + C_A, 1, 1, NARM, ctl + C_ABORT)) asm volatile("s_endpgm");
    lds_barrier();
    RSTAMP(74, 0);
    const v16u c0 = {(lane & 8) ? 0xEEEEu : 0x4444u, lane * 16, 4096 + 2 * g * 128 + (l15 & 7) * 16 + (l15 >> 3) * 8, lb + wave * 1024 + l15 * 16 + (g & 1) * 8 + (g >> 1) * 4,
                     lb + wave * 1024 + (8 * (p >> 1) + q) * 16 + (p & 1) * 8, lb + wave * 1024 + ((2 * (g & 1) + (p >> 1)) ^ (cb & 3)) * 256 + (8 * (g >> 1) + q) * 16 + (p & 1) * 8,
                     r_ew(pol, lane), (wave * 32 + (lane & 31)) * 2048 + cb * 32 + (lane >> 5) * 16, (C_F + 32 * (128 * (wave & 1) + 32 * (cb >> 4) + (lane & 31))) * 4, r_st(cb, lane) - r_db(cb), lb + 4096, 0xFFFEFFFEu,
                     O_MOD + (2 * DM + 16 * cb + 8 * (lane >> 5)) * 4, (lane >> 3) * 16384 + 4096 + (lane & 7) * 128, 0u, 0u};
    const unsigned sd = ((pol ^ (xg >> 2)) & 1) * 8192;
    PARS;
    const v16u s0 = {wave, wave, par(P_A), par(P_B), par(P_EU), par(P_DMIN), par(P_R), par(P_TP), par(P_TU), par(P_T0),
                     O_UX + (cb & 31) * 65536 + sd, O_UM + (cb & 31) * 16384 + sd, O_EP + ((xg >> 1) & 1) * 131072 + sd, O_PS + sd, r_e1(cb), O_D + r_db(cb)};
    const u64 dbg = DBG(a, blockIdx.x);
    const v8u s1 = {i8 ? 0u : hw(pol, xg) + H_C + 512 * (wave & 1), (cb & 3) * 65536, lb + 8192 + wave * 32, lb + 8192, (unsigned)dbg, (unsigned)(dbg >> 32), (unsigned)(u64)a.x, (unsigned)((u64)a.x >> 32)};
    const v4u rws = mkrs(a.ws, WS_END);
    const unsigned n0 = __builtin_amdgcn_readfirstlane(rt_now() / par(P_R));
    const v8u s2 = {n0 * par(P_R), (n0 & 1) * 128, par(P_TF), par(P_X2), hw(pol, xg) + H_D, par(P_TT), 0u, 0u};
    asm volatile("s_nop 4\n" ASM_R :: "{v[0:15]}"(c0), "{s[32:47]}"(s0), "{s[48:55]}"(s1), "{s[56:63]}"(s2), "{s[64:67]}"(rws) : "memory", "s95", "v253", "a255");
}

typedef __attribute__((ext_vector_type(16))) float f32x16;
template <bool is2> __device__ __forceinline__ void f_main(const Args& a, rsrc_t rs, unsigned char* lds, int xg, int kq, int cg, unsigned* ctl) {
    constexpr int L_C = 131072, L_S = L_C + 8192, NG = is2 ? 7 : 8, LD = is2 ? DM : FF;
    const int tid = otid(), lane = tid & 63, wave = a.pad0, l15 = lane & 15, g = lane >> 4;
    const unsigned lb = (unsigned)(size_t)(LAS unsigned char*)lds, q = l15 >> 2, p = lane & 3, l16 = lane * 16;
    const unsigned pol = __builtin_amdgcn_readfirstlane(*(const unsigned*)(a.ws + O_POL));
    auto gr = [&](int G) { const int x = G + 2 * ((blockIdx.x >> 3) & 3); return x >= NG ? x - NG : x; };
    lds_barrier();
    v4u w[64];
    f32x16 u1 = {}, u2 = {};
    {
        v4u rW = mkrs(is2 ? a.w2 : a.w1, DM * FF * 2);
        unsigned bo[2];
#pragma unroll
        for (int i = 0; i < 2; ++i) bo[i] = (g * LD + 16 * ((l15 >> 1) ^ (g | i << 2)) + 8 * (lane & 1)) * 2;
        asm volatile("s_nop 4" : "+s"(rW));
        auto issue = [&](int G) {
            const unsigned r0 = 896 * kq + 128 * gr(G) + 32 * wave;
#pragma unroll
            for (int j = 0; j < 8; ++j) bdma<(NTW & 8)>(rW, bo[(j >> 1) & 1], ((r0 + 4 * j) * LD + 128 * cg) * 2, lb + (G & 3) * 32768 + wave * 8192 + j * 1024);
        };
        issue(0); issue(1); issue(2); issue(3);
        if (!is2) {
            f32x4 md[2];
            ld_mod<2>(rs, ctl, {4 * DM + 4 * tid, 3 * DM + 4 * tid}, md);
            *(LAS f32x4*)(lds + L_C + 16 * tid) = md[0]; *(LAS v2u*)(lds + L_C + 4096 + 8 * tid) = (v2u){pack2(md[1][0], md[1][1]), pack2(md[1][2], md[1][3])};
            asm volatile("s_waitcnt lgkmcnt(0)" ::: "memory");
        }
        const unsigned ox = is2 ? (4 * (g >> 1) + q) * 256 + 32 * ((2 * (kq ^ (2 * (g & 1) + (p >> 1))) + (wave >> 1)) ^ q) + 16 * (wave & 1) + 8 * (p & 1)
                                : (8 * (g >> 1) + q) * 256 + 32 * ((2 * wave + (g & 1)) ^ (q | (g >> 1) << 2)) + 8 * p;
        sfor<0, NG>([&](auto gg) {
            constexpr int G = decltype(gg)::v;
            asm volatile("s_waitcnt vmcnt(%0)\n\ts_barrier" :: "n"(G < NG - 3 ? 24 : 8 * (NG - 1 - G)) : "memory");
#pragma unroll
            for (int k = 0; k < 8; ++k) {
                const unsigned o = (G & 3) * 32768 + k * 4096;
                const v2u lo = tr_rd(lds, o + ox), hi = tr_rd(lds, o + (is2 ? (ox ^ 128) + 2048 : ox + 1024));
                w[8 * G + k] = (v4u){lo.x, lo.y, hi.x, hi.y};
                if constexpr (!is2) {
                    const unsigned k0 = 128 * gr(G) + 16 * k + 8 * (g >> 1);
                    const f32x4 ca = *(const LAS f32x4*)(lds + L_C + 4 * k0), cb = *(const LAS f32x4*)(lds + L_C + 4 * k0 + 16);
                    u2 = __builtin_amdgcn_mfma_f32_32x32x16_bf16(__builtin_bit_cast(v8bf, w[8 * G + k]), __builtin_bit_cast(v8bf, lds16(lds + L_C + 4096 + 2 * k0)), u2, 0, 0, 0);
                    w[8 * G + k] = (v4u){pack2(lo16f(lo.x) * ca[0], hi16f(lo.x) * ca[1]), pack2(lo16f(lo.y) * ca[2], hi16f(lo.y) * ca[3]), pack2(lo16f(hi.x) * cb[0], hi16f(hi.x) * cb[1]), pack2(lo16f(hi.y) * cb[2], hi16f(hi.y) * cb[3])};
                    u1 = __builtin_amdgcn_mfma_f32_32x32x16_bf16(__builtin_bit_cast(v8bf, w[8 * G + k]), __builtin_bit_cast(v8bf, (v4u){0x3F803F80u, 0x3F803F80u, 0x3F803F80u, 0x3F803F80u}), u1, 0, 0, 0);
                }
            }
            if constexpr (!is2) asm volatile("" : "+v"(u1), "+v"(u2));
#pragma unroll
            for (int k = 8 * G; k < 8 * G + 8; ++k) asm volatile("" : "+a"(w[k]));
            lds_barrier();
            if (G + 4 < NG) issue(G + 4);
        });
    }
    const unsigned sd = ((pol ^ (xg >> 2)) & 1) * 8192;
    v16u c0 = {l16, lb + l16, is2 ? min(lane, 6) * 16384 + wave * 2048 + 2044 : D_ST - D_X2 + (l15 >> 3) * 16384 + ((l15 & 6) * 4 + 2 * wave + (lane & 1)) * 256, lb + L_S + wave * 256 + l15 * 16, lb + L_S + (lane & 31) * 8, 0u, 0xbdd2d3e8u, 0xbdd2d3e8u, 0xc0135761u, 0xc0135761u, 0x3f800000u, 0x3f800000u, 0u, 0u, 0u, 0u};
    if (is2) {
        const int col = 128 * cg + 32 * kq + 8 * wave + 4 * (lane >> 5);
        const v2u br = *(const v2u*)(a.b2 + col);
        f32x4 gv;
        ld_mod<1>(rs, ctl, {5 * DM + col}, &gv);
        const int f1 = 7 * (kq & 1) + min(lane, 6);
        c0[3] = hw(pol, (xg & 4) + (f1 >> 2)) + H_P + 256 * (f1 & 3); c0[4] = r_db(col >> 4) - (kq & 1) * 7 * 16384 + (wave & 1) * 512 + (lane & 31) * 16 + (lane >> 5) * 8; c0[5] = ((lane & 31) * DM + col) * 2;
        c0[8] = br.x << 16; c0[9] = br.x & 0xFFFF0000u; c0[10] = br.y << 16; c0[11] = br.y & 0xFFFF0000u;
#pragma unroll
        for (int i = 0; i < 4; ++i) c0[12 + i] = __float_as_uint(gv[i]);
    } else {
#pragma unroll
        for (int h = 0; h < 4; ++h) {
            const v2u br = *(const v2u*)(a.b1 + 128 * cg + 32 * wave + 8 * h + 4 * (g >> 1));
            u2[4 * h] += lo16f(br.x); u2[4 * h + 1] += hi16f(br.x); u2[4 * h + 2] += lo16f(br.y); u2[4 * h + 3] += hi16f(br.y);
        }
    }
    if (!wave_wait<8>(ctl + C_A, 1, 1, NARM, ctl + C_ABORT)) asm volatile("s_endpgm");
    RSTAMP(74, 0);
    const u64 dbg = DBG(a, blockIdx.x);
    PARS;
    const v16u s0 = {0u, (unsigned)wave, is2 ? par(P_T1) : -par(P_TZ), par(P_TS), O_D + (is2 ? (kq & 1) * 7 * 16384 : D_X2) + sd, lb + wave * 2048, hw(pol, xg) + H_C, hw(pol, xg) + H_P + 256 * ((blockIdx.x >> 3) & 3),
                     O_D + (cg >= 14 ? cg - 14 : cg) * 16384 + wave * 2048 + sd, D_ST - D_X2 + (wave >> 1) * 16384 + (wave & 1) * 4096, lb + L_S + 2048 + (wave == STAMP_WAVE ? 0 : MiB), lb + L_S + 2048,
                     (unsigned)dbg, (unsigned)(dbg >> 32), (unsigned)(u64)a.out, (unsigned)((u64)a.out >> 32)};
    v16u s1;
#pragma unroll
    for (int i = 0; i < 16; ++i) {
        const int o = kq ^ (i & 3);
        s1[i] = i < 8 ? gr(i) * 16384 + wave * 2048 : O_SL + ((cg * 2 + (o & 1)) * 2 + ((i >> 1) & 1)) * 16384 + ((pol ^ (o >> 1)) & 1) * 8192 + (i & 1) * 4096 + wave * 1024;
    }
    s1[12] = is2 ? par(P_T2) : -par(P_TY);
    const v4u rws = mkrs(a.ws, WS_END);
    v32u ww[8];
#pragma unroll
    for (int i = 0; i < NG; ++i) {
        const v16u x0 = __builtin_shufflevector(__builtin_shufflevector(w[8 * i], w[8 * i + 1], 0, 1, 2, 3, 4, 5, 6, 7), __builtin_shufflevector(w[8 * i + 2], w[8 * i + 3], 0, 1, 2, 3, 4, 5, 6, 7), 0, 1, 2, 3, 4, 5, 6, 7, 8, 9, 10, 11, 12, 13, 14, 15);
        const v16u x1 = __builtin_shufflevector(__builtin_shufflevector(w[8 * i + 4], w[8 * i + 5], 0, 1, 2, 3, 4, 5, 6, 7), __builtin_shufflevector(w[8 * i + 6], w[8 * i + 7], 0, 1, 2, 3, 4, 5, 6, 7), 0, 1, 2, 3, 4, 5, 6, 7, 8, 9, 10, 11, 12, 13, 14, 15);
        ww[i] = __builtin_shufflevector(x0, x1, 0, 1, 2, 3, 4, 5, 6, 7, 8, 9, 10, 11, 12, 13, 14, 15, 16, 17, 18, 19, 20, 21, 22, 23, 24, 25, 26, 27, 28, 29, 30, 31);
    }
#define F_IN "{v[0:15]}"(c0), "{s[32:47]}"(s0), "{s[48:63]}"(s1), "{s[64:67]}"(rws), "{a[0:31]}"(ww[0]), "{a[32:63]}"(ww[1]), "{a[64:95]}"(ww[2]), "{a[96:127]}"(ww[3]), "{a[128:159]}"(ww[4]), "{a[160:191]}"(ww[5]), "{a[192:223]}"(ww[6])
    if constexpr (is2) asm volatile("s_nop 4\n" ASM_F2 :: F_IN : "memory", "s95");
    else asm volatile("s_nop 7\ns_nop 7\n" ASM_F1 :: F_IN, "{a[224:255]}"(ww[7]), "{v[16:31]}"(u1), "{v[32:47]}"(u2) : "memory", "s95");
}

__global__ void __launch_bounds__(256, 1) k_main(Args) {
    extern __shared__ __attribute__((aligned(16))) unsigned char lds[];
    const int xg = blockIdx.x & 7, ii = blockIdx.x >> 3;
    int wv = __builtin_amdgcn_readfirstlane(threadIdx.x >> 6); asm volatile("" : "+s"(wv));
    if (blockIdx.x >= 224) code_touch(lds + LDS_BYTES - 1024, ((blockIdx.x >> 3) - 28) * 4 + wv);
    auto args = [&]() { PARS; Args r = *(const Args*)pp_; r.pad0 = wv; r.mode = blockIdx.x; return r; };
    const Args a = args();
    RSTAMP(75, 0);
    if (ii < 16) {
        if (ii & 1) s1_main<true>(args(), lds, xg, ii); else s1_main<false>(args(), lds, xg, ii);
        return;
    }
    const rsrc_t rs = __builtin_amdgcn_make_buffer_rsrc((void*)a.ws, (short)0, (int)WS_END, 0x00020000);
    unsigned* const ctl = (unsigned*)(a.ws + O_CTL);
    const int i4 = ii & 3, cg = 4 * (xg & 3) + i4;
    const unsigned t0 = rt_now();
    f_start(args(), rs, lds, (blockIdx.x - 64) & 127, t0);
    if (ii < 24) {
        rt_delay(TD_W, t0);
        r_main(args(), rs, lds, xg, ii - 16, ctl);
        return;
    }
    RSTAMP(75, 1);
    if (ii >= 28 && cg >= 14) return;
    if (ii < 28) f_main<true>(args(), rs, lds, xg, xg >> 1, 4 * (xg & 1) + i4, ctl);
    else f_main<false>(args(), rs, lds, xg, 0, 14 * (xg >> 2) + cg, ctl);
}

static const unsigned g_par[NPAR] = {CH_PAR};
extern "C" void kernel_launch(void* const* d_in, const int* in_sizes, int n_in,
                              void* d_out, int out_size, void* d_ws, size_t ws_size,
                              hipStream_t stream) {
    Args a;
    a.x = (const u16*)d_in[0]; a.Wq = (const u16*)d_in[2]; a.Wk = (const u16*)d_in[3];
    a.Wv = (const u16*)d_in[4]; a.Wo = (const u16*)d_in[5];
    a.w1 = (const u16*)d_in[8]; a.b1 = (const u16*)d_in[9]; a.w2 = (const u16*)d_in[10]; a.b2 = (const u16*)d_in[11];
    a.out = (u16*)d_out; a.ws = (char*)d_ws; a.mode = 0; a.pad0 = 0;
    for (int i = 0; i < NPAR; ++i) a.par[i] = g_par[i];
    a.c = (const u16*)d_in[12]; a.w_mod = (const u16*)d_in[13];
    k_pre<<<280, 256, 0, stream>>>(a.c, a.w_mod, (const u16*)d_in[1], a.ws);
    hipFuncSetAttribute((const void*)k_main, hipFuncAttributeMaxDynamicSharedMemorySize, LDS_BYTES);
    k_main<<<256, 256, LDS_BYTES, stream>>>(a);
}
```

```cpp
#include <hip/hip_runtime.h>
#include <hip/hip_bf16.h>
#include <stdint.h>

typedef unsigned short u16;
typedef unsigned long long u64;
typedef __attribute__((ext_vector_type(8))) short bf16x8;
typedef __attribute__((ext_vector_type(4))) short bf16x4;
typedef __attribute__((ext_vector_type(4))) __bf16 bf16x4_t;
typedef __attribute__((ext_vector_type(8))) __bf16 v8bf;
typedef __attribute__((ext_vector_type(16))) __bf16 v16bf;
typedef __attribute__((ext_vector_type(4))) float f32x4;
typedef __attribute__((ext_vector_type(2))) float f32x2;
typedef unsigned v8u __attribute__((ext_vector_type(8)));
typedef unsigned v4u __attribute__((ext_vector_type(4)));
typedef unsigned v2u __attribute__((ext_vector_type(2)));
#define GAS __attribute__((address_space(1)))
#define LAS __attribute__((address_space(3)))
typedef GAS unsigned gu32;
#define RLX_AGENT __ATOMIC_RELAXED, __HIP_MEMORY_SCOPE_AGENT

constexpr int S_TOK = 2048, DM = 1024, NH = 32, DH = 128, MEMR = 8, SEG = 32, NSEG = 64;
constexpr int HD = NH * DH;
constexpr int QKN = 2 * HD;
constexpr int FF = 3584;

#define NTW 79
constexpr int RG = 16;
#define RGX 16
constexpr unsigned MiB = 1u << 20;
constexpr unsigned O_CTL = 0;
constexpr unsigned O_MOD = 65536;
constexpr unsigned O_POL = O_MOD + 32768;
constexpr unsigned O_MODP = 131072;
constexpr unsigned O_EP = 2 * MiB;
constexpr unsigned O_PS = O_EP + RG * 262144;
constexpr unsigned O_UM = O_PS + RG * 262144;
constexpr unsigned O_E1 = O_UM + RG * 524288;
constexpr unsigned O_H1 = O_E1 + RG * 131072;
constexpr unsigned O_UX = O_H1 + 4 * MiB;
constexpr unsigned O_D = O_UX + RGX * 2 * MiB, D_SLOT = 524288, D_X2 = 14 * 16384, D_ST = 22 * 16384;
constexpr unsigned O_SL = O_D + 32 * MiB;
constexpr unsigned WS_END = O_SL + 2 * MiB;
static_assert(WS_END <= 256 * MiB && O_UX % (2 * MiB) == 0 && O_E1 % (2 * MiB) == 0, "ws");
__device__ __forceinline__ unsigned nearg(unsigned pol, unsigned off, unsigned h) { return off + (((off >> 21) ^ pol ^ h) & 1) * 8192; }
constexpr int C_ABORT = 0;
constexpr int C_A = 32, NARM = 128;
constexpr int C_F = 64;
constexpr int C_H = C_F + 8192;
constexpr unsigned O_HW = 49152;
constexpr int H_C = 0;
constexpr int H_D = 256;
constexpr int H_P = 1024;
__device__ __forceinline__ unsigned hw(unsigned pol, int x) { return nearg(pol, O_HW, x >> 2) + (x & 3) * 2048; }

__device__ __forceinline__ float bf2f(u16 v) { return __uint_as_float(((unsigned)v) << 16); }
__device__ __forceinline__ u16 f2bf(float f) {
    __hip_bfloat16 h = __float2bfloat16(f);
    return __builtin_bit_cast(u16, h);
}
__device__ __forceinline__ float rbf(float f) { return bf2f(f2bf(f)); }

#define CODE_KB 100
#define NPR 6
#define CTC 3
__device__ __forceinline__ void code_touch(unsigned char* lds, int part) {
    u64 pc; const unsigned sz = CODE_KB * 1024;
    asm volatile("s_getpc_b64 %0" : "=s"(pc));
    const v4u r = {(unsigned)pc, (unsigned)(pc >> 32) & 0xFFFFu, sz, 0x00020000u};
    asm volatile("s_mov_b32 m0, %2\n\ts_nop 4\n\tbuffer_load_dword %0, %1, 0 offen lds" :: "v"(((threadIdx.x & 63) + 64 * part) * 128), "s"(r), "s"((unsigned)(size_t)(LAS unsigned char*)lds) : "m0");
}
__global__ __launch_bounds__(256) void k_pre(const u16* __restrict__ c, const u16* __restrict__ w_mod, const u16* __restrict__ mem0, char* __restrict__ ws) {
    __shared__ float red[32][65];
    const int tid = threadIdx.x;
    const unsigned bid = blockIdx.x;
    if (bid >= 24) {
        const int cg = (bid - 24) & 31, kq = (bid - 24) >> 5, c8 = tid & 7, kr = 128 * kq + (tid >> 3);
        v4u wv[4];
#pragma unroll
        for (int i = 0; i < 4; ++i) wv[i] = NTW & 4 ? __builtin_nontemporal_load((const v4u*)(w_mod + (size_t)(kr + 32 * i) * 6144 + cg * 64 + 8 * c8)) : *(const v4u*)(w_mod + (size_t)(kr + 32 * i) * 6144 + cg * 64 + 8 * c8);
        float acc[8] = {0.f, 0.f, 0.f, 0.f, 0.f, 0.f, 0.f, 0.f};
#pragma unroll
        for (int i = 0; i < 4; ++i) {
            const float v = bf2f(c[kr + 32 * i]), sv = rbf(v / (1.f + __expf(-v)));
#pragma unroll
            for (int j = 0; j < 4; ++j) { acc[2 * j] += sv * __uint_as_float(wv[i][j] << 16); acc[2 * j + 1] += sv * __uint_as_float(wv[i][j] & 0xFFFF0000u); }
        }
#pragma unroll
        for (int j = 0; j < 8; ++j) red[tid >> 3][8 * c8 + j] = acc[j];
        __syncthreads();
        if (tid < 64) {
            float v = 0.f;
#pragma unroll
            for (int i = 0; i < 32; ++i) v += red[i][tid];
            ((float*)(ws + O_MODP))[kq * 2048 + cg * 64 + tid] = v;
        }
        return;
    }
    if (bid >= 16) {
        if (bid < 22) *(uint4*)(ws + O_MOD + (bid - 16) * 4096 + tid * 16) = make_uint4(~0u, ~0u, ~0u, ~0u);
        if (bid == 23) { for (int i = 0; i < 4; ++i) code_touch((unsigned char*)red, i * 4 + (tid >> 6)); asm volatile("s_waitcnt vmcnt(0)"); }
        if (bid != 22 || tid) return;
        unsigned best[2] = {~0u, ~0u};
        for (int r = 0; r < NPR; ++r) {
            const u64 t0 = __builtin_readcyclecounter();
            asm volatile("global_store_dword %0, %1, %2 sc1\n\ts_waitcnt vmcnt(0)" :: "v"(O_POL + 128 + (r & 1) * 8192 + (r & 2) * 2048), "v"(0u), "s"(ws) : "memory");
            best[r & 1] = min(best[r & 1], (unsigned)(__builtin_readcyclecounter() - t0));
        }
        const unsigned xcc = __builtin_amdgcn_s_getreg((3 << 11) | 20);
        *(unsigned*)(ws + O_POL) = ((best[1] < best[0]) ^ (xcc >> 2)) & 1;
        return;
    }
    ((uint4*)(ws + O_CTL))[bid * 256 + tid] = make_uint4(0u, 0u, 0u, 0u);
    const unsigned u = (tid >> 6) * 256 + ((bid >> 1) & 3) * 64 + (tid & 63);
    uint4 m = *(const uint4*)(mem0 + (u & 7) * DM + (u >> 4) * 16 + ((u >> 3) & 1) * 8);
    m.x = min(m.x, 0xFFFFFFFEu); m.y = min(m.y, 0xFFFFFFFEu); m.z = min(m.z, 0xFFFFFFFEu); m.w = min(m.w, 0xFFFFFFFEu);
    *(uint4*)(ws + O_E1 + bid * 8192 + tid * 16) = m; *(uint4*)(ws + O_E1 + bid * 8192 + 4096 + tid * 16) = m;
}

enum { P_A, P_B, P_EU, P_DMIN, P_R, P_TP, P_TU, P_T0, P_X, P_TZ, P_TF, P_X2, P_T1, P_TS, P_TT, P_TY, P_T2, P_17, NPAR };
#define CH_PAR 164, 72, 12, 4, 52, 108, 300, 90, 36, -30u, 10, 72, 190, 30, 20, 132, 140, 0
struct Args {
    const u16* Wq; const u16* Wk; const u16* Wv; const u16* Wo;
    const u16* c; const u16* w_mod;
    const u16* x; const u16* w1; const u16* b1; const u16* w2; const u16* b2;
    u16* out; char* ws;
    unsigned mode, pad0;
};
#define PARS auto pp_ = (__attribute__((address_space(4))) const Args*)__builtin_amdgcn_kernarg_segment_ptr(); asm volatile("" : "+s"(pp_))
constexpr unsigned PARV[NPAR] = {CH_PAR};
#define par(i) PARV[i]
#define RSTAMP(sg, i) do { } while (0)
#define STAMP_WAVE 0
#define DBG(a, i) 0ull
constexpr unsigned SPIN_CAP = 1u << 22;
constexpr unsigned SENT = 0xFFFFFFFFu;

__device__ __forceinline__ void lds_barrier() { asm volatile("s_waitcnt lgkmcnt(0)\n\ts_barrier" ::: "memory"); }
__device__ __forceinline__ void wait_vm0() { asm volatile("s_waitcnt vmcnt(0)" ::: "memory"); }
typedef __bf16 bf16x2_t __attribute__((ext_vector_type(2)));
__device__ __forceinline__ unsigned pack2(float a, float b) { const f32x2 v = {a, b}; return __builtin_bit_cast(unsigned, __builtin_convertvector(v, bf16x2_t)); }
__device__ __forceinline__ float lo16f(unsigned u) { return __uint_as_float(u << 16); }
__device__ __forceinline__ float hi16f(unsigned u) { return __uint_as_float(u & 0xFFFF0000u); }
__device__ __forceinline__ unsigned umax4(v4u x) { return max(max(x.x, x.y), max(x.z, x.w)); }
__device__ __forceinline__ unsigned unsent(unsigned x) { return min(x, 0xFFFFFFFEu); }

__device__ __forceinline__ unsigned ldg4_sc1(const void* sb, unsigned vo) {
    unsigned r;
    asm volatile("global_load_dword %0, %1, %2 sc1\n\ts_waitcnt vmcnt(0)" : "=&v"(r) : "v"(vo), "s"(sb) : "memory");
    return r;
}
__device__ __forceinline__ bool spin_fail(unsigned& spins, unsigned* abortw) {
    if (++spins > SPIN_CAP) { __hip_atomic_store((gu32*)abortw, 1u, RLX_AGENT); return true; }
    if ((spins & 63u) == 0 && __builtin_amdgcn_readfirstlane(__hip_atomic_load((gu32*)abortw, RLX_AGENT)) == 1u) return true;
    return false;
}
__device__ __forceinline__ float red16_add(float x) { auto r = __builtin_amdgcn_permlane16_swap(__float_as_uint(x), __float_as_uint(x), false, false); return __uint_as_float(r[0]) + __uint_as_float(r[1]); }
__device__ __forceinline__ float red32_add(float x) { auto r = __builtin_amdgcn_permlane32_swap(__float_as_uint(x), __float_as_uint(x), false, false); return __uint_as_float(r[0]) + __uint_as_float(r[1]); }
__device__ __forceinline__ v2u tr_rd(const unsigned char* lds_base, unsigned off) {
    return __builtin_bit_cast(v2u, __builtin_amdgcn_ds_read_tr16_b64_v4bf16((LAS bf16x4_t*)(lds_base + off)));
}
__device__ __forceinline__ v4u lds16(const unsigned char* p) { return *(const LAS v4u*)p; }
__device__ __forceinline__ void dma16(const void* src, unsigned char* dst, int aux) {
    if (aux) __builtin_amdgcn_global_load_lds((const unsigned*)src, (unsigned*)dst, 16, 0, 2);
    else __builtin_amdgcn_global_load_lds((const unsigned*)src, (unsigned*)dst, 16, 0, 0);
}

__device__ __forceinline__ v4u mkrs(const void* p, unsigned bytes) { return (v4u){(unsigned)(u64)p, (unsigned)((u64)p >> 32) & 0xFFFFu, bytes, 0x00020000u}; }
template <int AUX> __device__ __forceinline__ void bdma(v4u r, unsigned vo, unsigned so, unsigned ldsaddr) {
    if (AUX == 16) asm volatile("s_mov_b32 m0, %0\n\ts_nop 0\n\tbuffer_load_dwordx4 %1, %2, %3 offen sc1 lds" :: "s"(ldsaddr), "v"(vo), "s"(r), "s"(so) : "m0");
    else if (AUX) asm volatile("s_mov_b32 m0, %0\n\ts_nop 0\n\tbuffer_load_dwordx4 %1, %2, %3 offen nt lds" :: "s"(ldsaddr), "v"(vo), "s"(r), "s"(so) : "m0");
    else asm volatile("s_mov_b32 m0, %0\n\ts_nop 0\n\tbuffer_load_dwordx4 %1, %2, %3 offen lds" :: "s"(ldsaddr), "v"(vo), "s"(r), "s"(so) : "m0");
}

template <int I> struct IC { static constexpr int v = I; };
template <int B, int E, class F> __device__ __forceinline__ void sfor(F&& f) { if constexpr (B < E) { f(IC<B>{}); sfor<B + 1, E>(f); } }
#define SB() __builtin_amdgcn_sched_barrier(0)

__device__ __forceinline__ int lane_id() { int l; asm volatile("v_mbcnt_lo_u32_b32 %0, -1, 0\n\tv_mbcnt_hi_u32_b32 %0, -1, %0" : "=v"(l)); return l; }
constexpr int LDS_BYTES = 163840;
typedef __amdgpu_buffer_rsrc_t rsrc_t;
__device__ __forceinline__ v4u bld16(rsrc_t r, unsigned off) { return __builtin_bit_cast(v4u, __builtin_amdgcn_raw_buffer_load_b128(r, off, 0, 16)); }
__device__ __forceinline__ v2u bld8(rsrc_t r, unsigned off) { return __builtin_bit_cast(v2u, __builtin_amdgcn_raw_buffer_load_b64(r, off, 0, 16)); }
__device__ __forceinline__ void bst16(rsrc_t r, unsigned off, v4u v) { __builtin_amdgcn_raw_buffer_store_b128(__builtin_bit_cast(__attribute__((ext_vector_type(4))) unsigned, v), r, off, 0, 16); }
__device__ __forceinline__ void bst8(rsrc_t r, unsigned off, v2u v) { __builtin_amdgcn_raw_buffer_store_b64(__builtin_bit_cast(__attribute__((ext_vector_type(2))) unsigned, v), r, off, 0, 16); }

constexpr int L_FLAG = LDS_BYTES - 64;
#define otid() (a.pad0 * 64 + lane_id())
__device__ __forceinline__ v4u sld16(rsrc_t r, unsigned vo, unsigned so) { return __builtin_bit_cast(v4u, __builtin_amdgcn_raw_buffer_load_b128(r, vo, so, 16)); }
__device__ __forceinline__ v4u sentv() { unsigned x = SENT; asm volatile("" : "+v"(x)); return (v4u){x, x, x, x}; }
#define BW_SLEEP 16
template <int SLP = BW_SLEEP> __device__ __forceinline__ bool wave_wait(const unsigned* p, int stride, int n, unsigned target, unsigned* abortw, unsigned char* lds = nullptr) {
    const int lane = lane_id();
    for (unsigned spins = 0;;) {
        const unsigned v = lane < n ? __hip_atomic_load((gu32*)(p + lane * stride), RLX_AGENT) : target;
        if (!__any(v < target)) { if (lds && lane == 0) *(volatile LAS unsigned*)(lds + L_FLAG + 32) = v; return true; }
        __builtin_amdgcn_s_sleep(SLP);
        if (++spins > (SPIN_CAP << 1)) { __hip_atomic_store((gu32*)abortw, 1u, RLX_AGENT); return false; }
        if ((spins & 63u) == 0 && __builtin_amdgcn_readfirstlane(__hip_atomic_load((gu32*)abortw, RLX_AGENT)) == 1u) return false;
    }
}
__device__ __forceinline__ void drained(unsigned* cnt) {
    asm volatile("s_waitcnt vmcnt(0)\n\ts_barrier" ::: "memory");
    if (threadIdx.x == 0) __hip_atomic_fetch_add((gu32*)cnt, 1u, RLX_AGENT);
}

constexpr int L_W = 0;
constexpr int L_H = 98304;
constexpr int L_KX = L_H + 40960;
constexpr int L_QX = L_KX + 8192;
constexpr int L_T = L_QX + 8192;
constexpr int L_ST = L_T + 4096;
constexpr int lsl(bool isV, int c, int n) { return isV ? (c < 6 && (n & 1) ? 2 * c + (n >> 1) : -1) : (c < 12 && n == 1 ? c : -1); }
constexpr int ridx(bool isV, int c, int n) { int r = 0; for (int i = 0; i < 4 * c + n; ++i) r += lsl(isV, i >> 2, i & 3) < 0; return r; }
#define ASM_Q \
    "s_add_u32 m0, s41, 0\ns_xor_b32 s85, s62, 0\ns_add_u32 s85, s85, s40\nbuffer_load_dwordx4 v11, s[64:67], s85 offen sc1 lds\n" \
    "buffer_load_dwordx4 v12, s[64:67], s85 offen offset:1024 sc1 lds\ns_add_u32 m0, s41, 8192\ns_xor_b32 s85, s62, 256\ns_add_u32 s85, s85, s40\n" \
    "buffer_load_dwordx4 v11, s[64:67], s85 offen sc1 lds\nbuffer_load_dwordx4 v12, s[64:67], s85 offen offset:1024 sc1 lds\ns_add_u32 m0, s41, 16384\ns_xor_b32 s85, s62, 512\n" \
    "s_add_u32 s85, s85, s40\nbuffer_load_dwordx4 v11, s[64:67], s85 offen sc1 lds\nbuffer_load_dwordx4 v12, s[64:67], s85 offen offset:1024 sc1 lds\ns_mov_b32 s83, 0x7fff0000\n" \
    "s_branch .Lq8_%=\n.Lq9_%=:\ns_waitcnt vmcnt(0)\ns_barrier\n.Lq2_%=:\ns_add_u32 s84, s40, 0x20000\ns_cmp_lt_u32 s35, 62\ns_cselect_b32 s84, s84, s40\n" \
    "ds_read_b128 v[64:67], v3 offset:0\nds_read_b128 v[68:71], v9 offset:0\nds_read_b128 v[72:75], v9 offset:4096\ns_add_u32 m0, s41, 24576\ns_xor_b32 s85, s62, 768\n" \
    "s_add_u32 s85, s85, s40\nbuffer_load_dwordx4 v11, s[64:67], s85 offen sc1 lds\nbuffer_load_dwordx4 v12, s[64:67], s85 offen offset:1024 sc1 lds\n" \
    "v_mov_b32_dpp v16, v20 quad_perm:[0,1,2,3] row_mask:0xf bank_mask:0xc\nv_mov_b32_dpp v17, v21 quad_perm:[0,1,2,3] row_mask:0xf bank_mask:0xc\n" \
    "v_mov_b32_dpp v18, v22 quad_perm:[0,1,2,3] row_mask:0xf bank_mask:0xc\nv_mov_b32_dpp v19, v23 quad_perm:[0,1,2,3] row_mask:0xf bank_mask:0xc\nv_mov_b32 v254, 0xff800000\n" \
    "v_cvt_pk_bf16_f32 v16, 0, v16\nv_cvt_pk_bf16_f32 v17, 0, v17\nv_cvt_pk_bf16_f32 v18, 0, v18\nv_cvt_pk_bf16_f32 v19, 0, v19\nv_mul_f32 v16, s33, v16\n" \
    "v_mul_f32 v17, s33, v17\nv_mul_f32 v18, s33, v18\ns_waitcnt lgkmcnt(0)\nv_mfma_f32_16x16x32_bf16 v[48:51], a[8:11], v[68:71], 0\ns_add_u32 m0, s41, 32768\n" \
    "s_xor_b32 s85, s62, 1024\nv_mfma_f32_16x16x32_bf16 v[52:55], a[8:11], v[72:75], 0\nds_read_b128 v[76:79], v3 offset:1024\ns_add_u32 s85, s85, s40\n" \
    "v_mfma_f32_16x16x32_bf16 v[56:59], a[16:19], v[68:71], 0\nds_read_b128 v[80:83], v10 offset:0\nbuffer_load_dwordx4 v11, s[64:67], s85 offen sc1 lds\n" \
    "v_mfma_f32_16x16x32_bf16 v[60:63], a[16:19], v[72:75], 0\nds_read_b128 v[84:87], v10 offset:4096\nv_mfma_f32_16x16x32_bf16 v[32:35], a[0:3], v[68:71], 0\n" \
    "buffer_load_dwordx4 v12, s[64:67], s85 offen offset:1024 sc1 lds\nv_mul_f32 v19, s33, v19\nv_cvt_pk_bf16_f32 v16, 0, v16\n" \
    "v_mfma_f32_16x16x32_bf16 v[36:39], a[0:3], v[72:75], 0\nv_cvt_pk_bf16_f32 v17, 0, v17\nv_cvt_pk_bf16_f32 v18, 0, v18\n" \
    "v_mfma_f32_16x16x32_bf16 v[40:43], v[64:67], v[68:71], 0\nv_cvt_pk_bf16_f32 v19, 0, v19\nv_cndmask_b32 v16, v254, v16, s[50:51]\n" \
    "v_mfma_f32_16x16x32_bf16 v[44:47], v[64:67], v[72:75], 0\ns_waitcnt lgkmcnt(0)\nv_cndmask_b32 v17, v254, v17, s[52:53]\nv_cndmask_b32 v18, v254, v18, s[54:55]\n" \
    "v_mfma_f32_16x16x32_bf16 v[48:51], a[12:15], v[80:83], v[48:51]\nv_cndmask_b32 v19, v254, v19, s[56:57]\nv_max3_f32 v254, v16, v17, v18\n" \
    "v_mfma_f32_16x16x32_bf16 v[52:55], a[12:15], v[84:87], v[52:55]\nds_read_b128 v[88:91], v3 offset:2048\nv_max_f32 v254, v254, v19\n" \
    "v_mfma_f32_16x16x32_bf16 v[56:59], a[20:23], v[80:83], v[56:59]\nds_read_b128 v[64:67], v9 offset:1024\n" \
    "v_max_f32_dpp v252, v254, v254 row_ror:8 row_mask:0xf bank_mask:0xf\nv_max_f32_dpp v253, v254, v254 row_ror:8 row_mask:0xf bank_mask:0xf\n" \
    "v_mfma_f32_16x16x32_bf16 v[60:63], a[20:23], v[84:87], v[60:63]\nds_read_b128 v[68:71], v9 offset:5120\nv_permlane32_swap_b32 v252, v253\n" \
    "v_mfma_f32_16x16x32_bf16 v[32:35], a[4:7], v[80:83], v[32:35]\nv_max_f32 v252, v252, v253\nv_cvt_pk_bf16_f32 v24, 0, v24\n" \
    "v_mfma_f32_16x16x32_bf16 v[36:39], a[4:7], v[84:87], v[36:39]\nv_cvt_pk_bf16_f32 v25, 0, v25\nv_cvt_pk_bf16_f32 v26, 0, v26\n" \
    "v_mfma_f32_16x16x32_bf16 v[40:43], v[76:79], v[80:83], v[40:43]\nv_cvt_pk_bf16_f32 v27, 0, v27\nv_mul_f32 v24, s33, v24\n" \
    "v_mfma_f32_16x16x32_bf16 v[44:47], v[76:79], v[84:87], v[44:47]\ns_waitcnt lgkmcnt(0)\nv_mul_f32 v25, s33, v25\nv_mul_f32 v26, s33, v26\n" \
    "v_mfma_f32_16x16x32_bf16 v[48:51], a[32:35], v[64:67], v[48:51]\nv_mul_f32 v27, s33, v27\nv_cvt_pk_bf16_f32 v24, 0, v24\n" \
    "v_mfma_f32_16x16x32_bf16 v[52:55], a[32:35], v[68:71], v[52:55]\nds_read_b128 v[72:75], v3 offset:3072\nv_cvt_pk_bf16_f32 v25, 0, v25\nv_cvt_pk_bf16_f32 v26, 0, v26\n" \
    "v_mfma_f32_16x16x32_bf16 v[56:59], a[40:43], v[64:67], v[56:59]\nds_read_b128 v[76:79], v10 offset:1024\nv_cvt_pk_bf16_f32 v27, 0, v27\nv_max3_f32 v254, v252, v24, v25\n" \
    "v_mfma_f32_16x16x32_bf16 v[60:63], a[40:43], v[68:71], v[60:63]\nds_read_b128 v[80:83], v10 offset:5120\nv_max3_f32 v252, v254, v26, v27\nv_max3_f32 v253, v254, v26, v27\n" \
    "v_mfma_f32_16x16x32_bf16 v[32:35], a[24:27], v[64:67], v[32:35]\ns_nop 0\nv_permlane16_swap_b32 v252, v253\n" \
    "v_mfma_f32_16x16x32_bf16 v[36:39], a[24:27], v[68:71], v[36:39]\nv_max_f32 v252, v252, v253\nv_sub_f32 v16, v16, v252\n" \
    "v_mfma_f32_16x16x32_bf16 v[40:43], v[88:91], v[64:67], v[40:43]\nv_sub_f32 v17, v17, v252\nv_sub_f32 v18, v18, v252\n" \
    "v_mfma_f32_16x16x32_bf16 v[44:47], v[88:91], v[68:71], v[44:47]\ns_waitcnt lgkmcnt(0)\nv_sub_f32 v19, v19, v252\nv_sub_f32 v24, v24, v252\n" \
    "v_mfma_f32_16x16x32_bf16 v[48:51], a[36:39], v[76:79], v[48:51]\nv_sub_f32 v25, v25, v252\nv_sub_f32 v26, v26, v252\n" \
    "v_mfma_f32_16x16x32_bf16 v[52:55], a[36:39], v[80:83], v[52:55]\nds_read_b128 v[64:67], v3 offset:4096\nv_sub_f32 v27, v27, v252\nv_mul_f32 v16, s34, v16\n" \
    "v_mfma_f32_16x16x32_bf16 v[56:59], a[44:47], v[76:79], v[56:59]\nds_read_b128 v[68:71], v9 offset:8192\nv_mul_f32 v17, s34, v17\nv_mul_f32 v18, s34, v18\n" \
    "v_mfma_f32_16x16x32_bf16 v[60:63], a[44:47], v[80:83], v[60:63]\nds_read_b128 v[84:87], v9 offset:12288\nv_mul_f32 v19, s34, v19\nv_mul_f32 v24, s34, v24\n" \
    "v_mfma_f32_16x16x32_bf16 v[32:35], a[28:31], v[76:79], v[32:35]\nv_mul_f32 v25, s34, v25\nv_mul_f32 v26, s34, v26\n" \
    "v_mfma_f32_16x16x32_bf16 v[36:39], a[28:31], v[80:83], v[36:39]\nv_mul_f32 v27, s34, v27\nv_exp_f32 v16, v16\n" \
    "v_mfma_f32_16x16x32_bf16 v[40:43], v[72:75], v[76:79], v[40:43]\nv_exp_f32 v17, v17\nv_mfma_f32_16x16x32_bf16 v[44:47], v[72:75], v[80:83], v[44:47]\ns_waitcnt lgkmcnt(0)\n" \
    "v_exp_f32 v18, v18\nv_mfma_f32_16x16x32_bf16 v[48:51], a[56:59], v[68:71], v[48:51]\nv_exp_f32 v19, v19\nv_mfma_f32_16x16x32_bf16 v[52:55], a[56:59], v[84:87], v[52:55]\n" \
    "ds_read_b128 v[88:91], v3 offset:5120\nv_exp_f32 v24, v24\nv_mfma_f32_16x16x32_bf16 v[56:59], a[64:67], v[68:71], v[56:59]\nds_read_b128 v[72:75], v10 offset:8192\n" \
    "v_add_f32 v254, v16, v18\nv_add_f32 v255, v17, v19\nv_mfma_f32_16x16x32_bf16 v[60:63], a[64:67], v[84:87], v[60:63]\nds_read_b128 v[76:79], v10 offset:12288\n" \
    "v_exp_f32 v25, v25\nv_mfma_f32_16x16x32_bf16 v[32:35], a[48:51], v[68:71], v[32:35]\nv_add_f32 v254, v254, v255\nv_exp_f32 v26, v26\n" \
    "v_mfma_f32_16x16x32_bf16 v[36:39], a[48:51], v[84:87], v[36:39]\nv_exp_f32 v27, v27\nv_mfma_f32_16x16x32_bf16 v[40:43], v[64:67], v[68:71], v[40:43]\n" \
    "v_add_f32_dpp v252, v254, v254 row_ror:8 row_mask:0xf bank_mask:0xf\nv_add_f32_dpp v253, v254, v254 row_ror:8 row_mask:0xf bank_mask:0xf\n" \
    "v_mfma_f32_16x16x32_bf16 v[44:47], v[64:67], v[84:87], v[44:47]\ns_waitcnt lgkmcnt(0)\nv_add_f32 v254, v24, v26\nv_add_f32 v255, v25, v27\n" \
    "v_mfma_f32_16x16x32_bf16 v[48:51], a[60:63], v[72:75], v[48:51]\nv_permlane32_swap_b32 v252, v253\nv_mfma_f32_16x16x32_bf16 v[52:55], a[60:63], v[76:79], v[52:55]\n" \
    "ds_read_b128 v[64:67], v3 offset:6144\nv_add_f32 v254, v254, v252\nv_add_f32 v255, v255, v253\nv_mfma_f32_16x16x32_bf16 v[56:59], a[68:71], v[72:75], v[56:59]\n" \
    "ds_read_b128 v[68:71], v9 offset:9216\nv_add_f32 v252, v254, v255\nv_add_f32 v253, v255, v254\nv_mfma_f32_16x16x32_bf16 v[60:63], a[68:71], v[76:79], v[60:63]\n" \
    "ds_read_b128 v[80:83], v9 offset:13312\nv_permlane16_swap_b32 v252, v253\nv_mfma_f32_16x16x32_bf16 v[32:35], a[52:55], v[72:75], v[32:35]\nv_add_f32 v252, v252, v253\n" \
    "v_rcp_f32 v252, v252\nv_mfma_f32_16x16x32_bf16 v[36:39], a[52:55], v[76:79], v[36:39]\nv_mul_f32 v24, v24, v252\nv_mul_f32 v25, v25, v252\n" \
    "v_mfma_f32_16x16x32_bf16 v[40:43], v[88:91], v[72:75], v[40:43]\nv_mul_f32 v26, v26, v252\nv_mul_f32 v27, v27, v252\n" \
    "v_mfma_f32_16x16x32_bf16 v[44:47], v[88:91], v[76:79], v[44:47]\ns_waitcnt lgkmcnt(0)\nv_cvt_pk_bf16_f32 v20, v24, v25\nv_cvt_pk_bf16_f32 v21, v26, v27\n" \
    "v_mfma_f32_16x16x32_bf16 v[48:51], a[80:83], v[68:71], v[48:51]\nv_mul_f32 v16, v16, v252\nv_mul_f32 v17, v17, v252\n" \
    "v_mfma_f32_16x16x32_bf16 v[52:55], a[80:83], v[80:83], v[52:55]\nds_read_b128 v[72:75], v3 offset:7168\nv_mul_f32 v18, v18, v252\nv_mul_f32 v19, v19, v252\n" \
    "v_mfma_f32_16x16x32_bf16 v[56:59], a[88:91], v[68:71], v[56:59]\nds_read_b128 v[76:79], v10 offset:9216\nv_permlane16_swap_b32 v20, v22\n" \
    "v_mfma_f32_16x16x32_bf16 v[60:63], a[88:91], v[80:83], v[60:63]\nds_read_b128 v[84:87], v10 offset:13312\nv_cvt_pk_bf16_f32 v16, v16, v17\nv_cvt_pk_bf16_f32 v17, v18, v19\n" \
    "v_mfma_f32_16x16x32_bf16 v[32:35], a[72:75], v[68:71], v[32:35]\nv_permlane16_swap_b32 v21, v23\nv_mfma_f32_16x16x32_bf16 v[36:39], a[72:75], v[80:83], v[36:39]\n" \
    "v_mov_b32_dpp v20, v16 quad_perm:[0,1,2,3] row_mask:0xf bank_mask:0x3\nv_mov_b32_dpp v22, v16 row_ror:8 row_mask:0xf bank_mask:0x3\n" \
    "v_mfma_f32_16x16x32_bf16 v[40:43], v[64:67], v[68:71], v[40:43]\nv_mov_b32_dpp v21, v17 quad_perm:[0,1,2,3] row_mask:0xf bank_mask:0x3\n" \
    "v_mov_b32_dpp v23, v17 row_ror:8 row_mask:0xf bank_mask:0x3\nv_mfma_f32_16x16x32_bf16 v[44:47], v[64:67], v[80:83], v[44:47]\ns_xor_b32 s74, s83, 0x2000\n" \
    "buffer_store_dwordx4 v[20:23], v7, s[64:67], s83 offen sc1\ns_waitcnt lgkmcnt(0)\nv_mfma_f32_16x16x32_bf16 v[48:51], a[84:87], v[76:79], v[48:51]\n" \
    "buffer_store_dwordx4 v[20:23], v7, s[64:67], s74 offen sc1\nv_mov_b64 v[28:29], -1\nv_mov_b64 v[30:31], -1\n" \
    "v_mfma_f32_16x16x32_bf16 v[52:55], a[84:87], v[84:87], v[52:55]\nds_read_b128 v[80:83], v3 offset:8192\ns_add_u32 s69, s35, 2\n" \
    "v_mfma_f32_16x16x32_bf16 v[56:59], a[92:95], v[76:79], v[56:59]\nds_read_b128 v[88:91], v9 offset:16384\ns_and_b32 s69, s69, 15\n" \
    "v_mfma_f32_16x16x32_bf16 v[60:63], a[92:95], v[84:87], v[60:63]\nds_read_b128 v[64:67], v9 offset:20480\ns_lshl_b32 s69, s69, 18\n" \
    "v_mfma_f32_16x16x32_bf16 v[32:35], a[76:79], v[76:79], v[32:35]\ns_add_u32 s82, s38, s69\ns_lshr_b32 s69, s82, 8\n" \
    "v_mfma_f32_16x16x32_bf16 v[36:39], a[76:79], v[84:87], v[36:39]\ns_and_b32 s69, s69, 0x2000\ns_xor_b32 s82, s82, s69\n" \
    "v_mfma_f32_16x16x32_bf16 v[40:43], v[72:75], v[76:79], v[40:43]\ns_add_u32 s69, s35, 2\ns_and_b32 s69, s69, 15\n" \
    "v_mfma_f32_16x16x32_bf16 v[44:47], v[72:75], v[84:87], v[44:47]\ns_lshl_b32 s69, s69, 18\ns_add_u32 s83, s39, s69\ns_waitcnt lgkmcnt(0)\n" \
    "v_mfma_f32_16x16x32_bf16 v[48:51], a[104:107], v[88:91], v[48:51]\ns_xor_b32 s74, s82, 0x4000\ns_xor_b32 s75, s83, 0x2000\n" \
    "v_mfma_f32_16x16x32_bf16 v[52:55], a[104:107], v[64:67], v[52:55]\nds_read_b128 v[84:87], v3 offset:9216\n" \
    "v_mfma_f32_16x16x32_bf16 v[56:59], a[112:115], v[88:91], v[56:59]\nds_read_b128 v[68:71], v10 offset:16384\n" \
    "v_mfma_f32_16x16x32_bf16 v[60:63], a[112:115], v[64:67], v[60:63]\nds_read_b128 v[72:75], v10 offset:20480\n" \
    "v_mfma_f32_16x16x32_bf16 v[32:35], a[96:99], v[88:91], v[32:35]\nv_mfma_f32_16x16x32_bf16 v[36:39], a[96:99], v[64:67], v[36:39]\n" \
    "v_mfma_f32_16x16x32_bf16 v[40:43], v[80:83], v[88:91], v[40:43]\nbuffer_store_dwordx4 v[28:31], v7, s[64:67], s82 offen sc1\n" \
    "v_mfma_f32_16x16x32_bf16 v[44:47], v[80:83], v[64:67], v[44:47]\ns_waitcnt lgkmcnt(0)\nv_mfma_f32_16x16x32_bf16 v[48:51], a[108:111], v[68:71], v[48:51]\n" \
    "v_mfma_f32_16x16x32_bf16 v[52:55], a[108:111], v[72:75], v[52:55]\nds_read_b128 v[64:67], v3 offset:10240\n" \
    "v_mfma_f32_16x16x32_bf16 v[56:59], a[116:119], v[68:71], v[56:59]\nds_read_b128 v[88:91], v9 offset:17408\n" \
    "v_mfma_f32_16x16x32_bf16 v[60:63], a[116:119], v[72:75], v[60:63]\nds_read_b128 v[76:79], v9 offset:21504\n" \
    "v_mfma_f32_16x16x32_bf16 v[32:35], a[100:103], v[68:71], v[32:35]\nbuffer_store_dwordx4 v[28:31], v7, s[64:67], s74 offen sc1\n" \
    "v_mfma_f32_16x16x32_bf16 v[36:39], a[100:103], v[72:75], v[36:39]\nv_mfma_f32_16x16x32_bf16 v[40:43], v[84:87], v[68:71], v[40:43]\n" \
    "v_mfma_f32_16x16x32_bf16 v[44:47], v[84:87], v[72:75], v[44:47]\ns_waitcnt lgkmcnt(0)\nv_mfma_f32_16x16x32_bf16 v[48:51], a[128:131], v[88:91], v[48:51]\n" \
    "v_mfma_f32_16x16x32_bf16 v[52:55], a[128:131], v[76:79], v[52:55]\nds_read_b128 v[68:71], v3 offset:11264\n" \
    "v_mfma_f32_16x16x32_bf16 v[56:59], a[136:139], v[88:91], v[56:59]\nds_read_b128 v[72:75], v10 offset:17408\nbuffer_store_dwordx4 v[28:31], v7, s[64:67], s83 offen sc1\n" \
    "v_mfma_f32_16x16x32_bf16 v[60:63], a[136:139], v[76:79], v[60:63]\nds_read_b128 v[80:83], v10 offset:21504\n" \
    "v_mfma_f32_16x16x32_bf16 v[32:35], a[120:123], v[88:91], v[32:35]\nv_mfma_f32_16x16x32_bf16 v[36:39], a[120:123], v[76:79], v[36:39]\n" \
    "v_mfma_f32_16x16x32_bf16 v[40:43], v[64:67], v[88:91], v[40:43]\nv_mfma_f32_16x16x32_bf16 v[44:47], v[64:67], v[76:79], v[44:47]\ns_waitcnt lgkmcnt(0)\n" \
    "v_mfma_f32_16x16x32_bf16 v[48:51], a[132:135], v[72:75], v[48:51]\nbuffer_store_dwordx4 v[28:31], v7, s[64:67], s75 offen sc1\ns_add_u32 s69, s35, 2\n" \
    "v_mfma_f32_16x16x32_bf16 v[52:55], a[132:135], v[80:83], v[52:55]\nds_read_b128 v[64:67], v3 offset:12288\ns_and_b32 s69, s69, 255\ns_waitcnt vmcnt(6)\ns_barrier\n" \
    "v_mfma_f32_16x16x32_bf16 v[56:59], a[140:143], v[72:75], v[56:59]\nds_read_b128 v[76:79], v9 offset:24576\ns_lshl_b32 s69, s69, 19\n" \
    "v_mfma_f32_16x16x32_bf16 v[60:63], a[140:143], v[80:83], v[60:63]\nds_read_b128 v[84:87], v9 offset:28672\ns_add_u32 m0, s41, 0\n" \
    "v_mfma_f32_16x16x32_bf16 v[32:35], a[124:127], v[72:75], v[32:35]\ns_xor_b32 s85, s62, 1280\ns_add_u32 s85, s85, s40\n" \
    "v_mfma_f32_16x16x32_bf16 v[36:39], a[124:127], v[80:83], v[36:39]\nbuffer_load_dwordx4 v11, s[64:67], s85 offen sc1 lds\ns_add_u32 s74, s63, s69\n" \
    "v_mfma_f32_16x16x32_bf16 v[40:43], v[68:71], v[72:75], v[40:43]\ns_cmp_lt_u32 s35, 62\ns_cselect_b32 s74, s74, 0x7fff0000\n" \
    "v_mfma_f32_16x16x32_bf16 v[44:47], v[68:71], v[80:83], v[44:47]\nbuffer_load_dwordx4 v12, s[64:67], s85 offen offset:1024 sc1 lds\ns_add_u32 m0, s41, 8192\n" \
    "s_waitcnt lgkmcnt(0)\nv_mfma_f32_16x16x32_bf16 v[48:51], a[152:155], v[76:79], v[48:51]\ns_xor_b32 s85, s62, 1536\ns_add_u32 s85, s85, s40\n" \
    "v_mfma_f32_16x16x32_bf16 v[52:55], a[152:155], v[84:87], v[52:55]\nds_read_b128 v[80:83], v3 offset:13312\nbuffer_load_dwordx4 v11, s[64:67], s85 offen sc1 lds\n" \
    "v_mfma_f32_16x16x32_bf16 v[56:59], a[160:163], v[76:79], v[56:59]\nds_read_b128 v[88:91], v10 offset:24576\n" \
    "v_mfma_f32_16x16x32_bf16 v[60:63], a[160:163], v[84:87], v[60:63]\nds_read_b128 v[68:71], v10 offset:28672\n" \
    "buffer_load_dwordx4 v12, s[64:67], s85 offen offset:1024 sc1 lds\nv_mfma_f32_16x16x32_bf16 v[32:35], a[144:147], v[76:79], v[32:35]\n" \
    "buffer_store_dwordx4 v[28:31], v2, s[64:67], s74 offen offset:0 sc1\nv_mfma_f32_16x16x32_bf16 v[36:39], a[144:147], v[84:87], v[36:39]\n" \
    "v_mfma_f32_16x16x32_bf16 v[40:43], v[64:67], v[76:79], v[40:43]\nv_mfma_f32_16x16x32_bf16 v[44:47], v[64:67], v[84:87], v[44:47]\ns_waitcnt lgkmcnt(0)\n" \
    "v_mfma_f32_16x16x32_bf16 v[48:51], a[156:159], v[88:91], v[48:51]\nv_mfma_f32_16x16x32_bf16 v[52:55], a[156:159], v[68:71], v[52:55]\n" \
    "ds_read_b128 v[64:67], v3 offset:14336\nv_mfma_f32_16x16x32_bf16 v[56:59], a[164:167], v[88:91], v[56:59]\nds_read_b128 v[84:87], v9 offset:25600\n" \
    "buffer_store_dwordx4 v[28:31], v2, s[64:67], s74 offen offset:1024 sc1\nv_mfma_f32_16x16x32_bf16 v[60:63], a[164:167], v[68:71], v[60:63]\n" \
    "ds_read_b128 v[72:75], v9 offset:29696\ns_and_b32 s69, s35, 15\nv_mfma_f32_16x16x32_bf16 v[32:35], a[148:151], v[88:91], v[32:35]\ns_lshl_b32 s69, s69, 18\n" \
    "s_add_u32 s82, s38, s69\nv_mfma_f32_16x16x32_bf16 v[36:39], a[148:151], v[68:71], v[36:39]\ns_lshr_b32 s69, s82, 8\ns_and_b32 s69, s69, 0x2000\n" \
    "v_mfma_f32_16x16x32_bf16 v[40:43], v[80:83], v[88:91], v[40:43]\ns_xor_b32 s82, s82, s69\ns_and_b32 s69, s35, 15\n" \
    "v_mfma_f32_16x16x32_bf16 v[44:47], v[80:83], v[68:71], v[44:47]\ns_lshl_b32 s69, s69, 18\ns_add_u32 s83, s39, s69\ns_waitcnt lgkmcnt(0)\n" \
    "v_mfma_f32_16x16x32_bf16 v[48:51], a[176:179], v[84:87], v[48:51]\nv_mfma_f32_16x16x32_bf16 v[52:55], a[176:179], v[72:75], v[52:55]\n" \
    "ds_read_b128 v[68:71], v3 offset:15360\nv_mfma_f32_16x16x32_bf16 v[56:59], a[184:187], v[84:87], v[56:59]\nds_read_b128 v[88:91], v10 offset:25600\n" \
    "v_mfma_f32_16x16x32_bf16 v[60:63], a[184:187], v[72:75], v[60:63]\nds_read_b128 v[76:79], v10 offset:29696\n" \
    "v_mfma_f32_16x16x32_bf16 v[32:35], a[168:171], v[84:87], v[32:35]\nv_mfma_f32_16x16x32_bf16 v[36:39], a[168:171], v[72:75], v[36:39]\n" \
    "v_mfma_f32_16x16x32_bf16 v[40:43], v[64:67], v[84:87], v[40:43]\nv_mfma_f32_16x16x32_bf16 v[44:47], v[64:67], v[72:75], v[44:47]\ns_waitcnt lgkmcnt(0)\n" \
    "v_mfma_f32_16x16x32_bf16 v[48:51], a[180:183], v[88:91], v[48:51]\nv_mfma_f32_16x16x32_bf16 v[52:55], a[180:183], v[76:79], v[52:55]\n" \
    "ds_read_b128 v[64:67], v3 offset:16384\nv_mfma_f32_16x16x32_bf16 v[56:59], a[188:191], v[88:91], v[56:59]\nds_read_b128 v[72:75], v9 offset:32768\n" \
    "v_mfma_f32_16x16x32_bf16 v[60:63], a[188:191], v[76:79], v[60:63]\nds_read_b128 v[80:83], v9 offset:36864\n" \
    "v_mfma_f32_16x16x32_bf16 v[32:35], a[172:175], v[88:91], v[32:35]\nv_mfma_f32_16x16x32_bf16 v[36:39], a[172:175], v[76:79], v[36:39]\n" \
    "v_mfma_f32_16x16x32_bf16 v[40:43], v[68:71], v[88:91], v[40:43]\nv_mfma_f32_16x16x32_bf16 v[44:47], v[68:71], v[76:79], v[44:47]\ns_waitcnt lgkmcnt(0)\n" \
    "v_mfma_f32_16x16x32_bf16 v[48:51], a[200:203], v[72:75], v[48:51]\nv_mfma_f32_16x16x32_bf16 v[52:55], a[200:203], v[80:83], v[52:55]\n" \
    "ds_read_b128 v[68:71], v3 offset:17408\nv_mfma_f32_16x16x32_bf16 v[56:59], a[208:211], v[72:75], v[56:59]\nds_read_b128 v[76:79], v10 offset:32768\n" \
    "v_mfma_f32_16x16x32_bf16 v[60:63], a[208:211], v[80:83], v[60:63]\nds_read_b128 v[84:87], v10 offset:36864\n" \
    "v_mfma_f32_16x16x32_bf16 v[32:35], a[192:195], v[72:75], v[32:35]\nv_mfma_f32_16x16x32_bf16 v[36:39], a[192:195], v[80:83], v[36:39]\n" \
    "v_mfma_f32_16x16x32_bf16 v[40:43], v[64:67], v[72:75], v[40:43]\nv_mfma_f32_16x16x32_bf16 v[44:47], v[64:67], v[80:83], v[44:47]\ns_waitcnt lgkmcnt(0)\n" \
    "v_mfma_f32_16x16x32_bf16 v[48:51], a[204:207], v[76:79], v[48:51]\nv_mfma_f32_16x16x32_bf16 v[52:55], a[204:207], v[84:87], v[52:55]\n" \
    "ds_read_b128 v[64:67], v3 offset:18432\nv_mfma_f32_16x16x32_bf16 v[56:59], a[212:215], v[76:79], v[56:59]\nds_read_b128 v[72:75], v9 offset:33792\n" \
    "v_mfma_f32_16x16x32_bf16 v[60:63], a[212:215], v[84:87], v[60:63]\nds_read_b128 v[80:83], v9 offset:37888\n" \
    "v_mfma_f32_16x16x32_bf16 v[32:35], a[196:199], v[76:79], v[32:35]\nv_mfma_f32_16x16x32_bf16 v[36:39], a[196:199], v[84:87], v[36:39]\n" \
    "v_mfma_f32_16x16x32_bf16 v[40:43], v[68:71], v[76:79], v[40:43]\nv_mfma_f32_16x16x32_bf16 v[44:47], v[68:71], v[84:87], v[44:47]\ns_waitcnt lgkmcnt(0)\n" \
    "v_mfma_f32_16x16x32_bf16 v[48:51], a[224:227], v[72:75], v[48:51]\nv_mfma_f32_16x16x32_bf16 v[52:55], a[224:227], v[80:83], v[52:55]\n" \
    "ds_read_b128 v[68:71], v3 offset:19456\nv_mfma_f32_16x16x32_bf16 v[56:59], a[232:235], v[72:75], v[56:59]\nds_read_b128 v[76:79], v10 offset:33792\n" \
    "v_mfma_f32_16x16x32_bf16 v[60:63], a[232:235], v[80:83], v[60:63]\nds_read_b128 v[84:87], v10 offset:37888\n" \
    "v_mfma_f32_16x16x32_bf16 v[32:35], a[216:219], v[72:75], v[32:35]\nv_mfma_f32_16x16x32_bf16 v[36:39], a[216:219], v[80:83], v[36:39]\n" \
    "v_mfma_f32_16x16x32_bf16 v[40:43], v[64:67], v[72:75], v[40:43]\nv_mfma_f32_16x16x32_bf16 v[44:47], v[64:67], v[80:83], v[44:47]\ns_waitcnt lgkmcnt(0)\n" \
    "v_mfma_f32_16x16x32_bf16 v[48:51], a[228:231], v[76:79], v[48:51]\nv_mfma_f32_16x16x32_bf16 v[52:55], a[228:231], v[84:87], v[52:55]\n" \
    "ds_read_b128 v[64:67], v3 offset:20480\ns_waitcnt vmcnt(2)\ns_barrier\nv_mfma_f32_16x16x32_bf16 v[56:59], a[236:239], v[76:79], v[56:59]\n" \
    "ds_read_b128 v[72:75], v9 offset:0\nv_mfma_f32_16x16x32_bf16 v[60:63], a[236:239], v[84:87], v[60:63]\nds_read_b128 v[80:83], v9 offset:4096\ns_add_u32 m0, s41, 16384\n" \
    "v_mfma_f32_16x16x32_bf16 v[32:35], a[220:223], v[76:79], v[32:35]\ns_xor_b32 s85, s62, 1792\ns_add_u32 s85, s85, s40\n" \
    "v_mfma_f32_16x16x32_bf16 v[36:39], a[220:223], v[84:87], v[36:39]\nbuffer_load_dwordx4 v11, s[64:67], s85 offen sc1 lds\n" \
    "v_mfma_f32_16x16x32_bf16 v[40:43], v[68:71], v[76:79], v[40:43]\nv_mfma_f32_16x16x32_bf16 v[44:47], v[68:71], v[84:87], v[44:47]\n" \
    "buffer_load_dwordx4 v12, s[64:67], s85 offen offset:1024 sc1 lds\ns_waitcnt lgkmcnt(0)\nv_mfma_f32_16x16x32_bf16 v[48:51], a[248:251], v[72:75], v[48:51]\n" \
    "v_mfma_f32_16x16x32_bf16 v[52:55], a[248:251], v[80:83], v[52:55]\nds_read_b128 v[68:71], v3 offset:21504\nv_mfma_f32_16x16x32_bf16 v[56:59], v[92:95], v[72:75], v[56:59]\n" \
    "ds_read_b128 v[76:79], v10 offset:0\nv_mfma_f32_16x16x32_bf16 v[60:63], v[92:95], v[80:83], v[60:63]\nds_read_b128 v[84:87], v10 offset:4096\n" \
    "v_mfma_f32_16x16x32_bf16 v[32:35], a[240:243], v[72:75], v[32:35]\nv_mfma_f32_16x16x32_bf16 v[36:39], a[240:243], v[80:83], v[36:39]\n" \
    "v_mfma_f32_16x16x32_bf16 v[40:43], v[64:67], v[72:75], v[40:43]\nv_mfma_f32_16x16x32_bf16 v[44:47], v[64:67], v[80:83], v[44:47]\ns_waitcnt lgkmcnt(0)\n" \
    "v_mfma_f32_16x16x32_bf16 v[48:51], a[252:255], v[76:79], v[48:51]\nv_mfma_f32_16x16x32_bf16 v[52:55], a[252:255], v[84:87], v[52:55]\n" \
    "ds_read_b128 v[64:67], v3 offset:22528\nv_mfma_f32_16x16x32_bf16 v[56:59], v[96:99], v[76:79], v[56:59]\nds_read_b128 v[72:75], v9 offset:1024\n" \
    "v_mfma_f32_16x16x32_bf16 v[60:63], v[96:99], v[84:87], v[60:63]\nds_read_b128 v[80:83], v9 offset:5120\nv_mfma_f32_16x16x32_bf16 v[32:35], a[244:247], v[76:79], v[32:35]\n" \
    "v_mfma_f32_16x16x32_bf16 v[36:39], a[244:247], v[84:87], v[36:39]\nv_mfma_f32_16x16x32_bf16 v[40:43], v[68:71], v[76:79], v[40:43]\n" \
    "v_mfma_f32_16x16x32_bf16 v[44:47], v[68:71], v[84:87], v[44:47]\ns_waitcnt lgkmcnt(0)\nv_mfma_f32_16x16x32_bf16 v[48:51], v[108:111], v[72:75], v[48:51]\n" \
    "v_mfma_f32_16x16x32_bf16 v[52:55], v[108:111], v[80:83], v[52:55]\nds_read_b128 v[68:71], v3 offset:23552\n" \
    "v_mfma_f32_16x16x32_bf16 v[56:59], v[116:119], v[72:75], v[56:59]\nds_read_b128 v[76:79], v10 offset:1024\n" \
    "v_mfma_f32_16x16x32_bf16 v[60:63], v[116:119], v[80:83], v[60:63]\nds_read_b128 v[84:87], v10 offset:5120\n" \
    "v_mfma_f32_16x16x32_bf16 v[32:35], v[100:103], v[72:75], v[32:35]\nv_mfma_f32_16x16x32_bf16 v[36:39], v[100:103], v[80:83], v[36:39]\n" \
    "v_mfma_f32_16x16x32_bf16 v[40:43], v[64:67], v[72:75], v[40:43]\nv_mfma_f32_16x16x32_bf16 v[44:47], v[64:67], v[80:83], v[44:47]\ns_waitcnt lgkmcnt(0)\n" \
    "v_mfma_f32_16x16x32_bf16 v[48:51], v[112:115], v[76:79], v[48:51]\nv_mfma_f32_16x16x32_bf16 v[52:55], v[112:115], v[84:87], v[52:55]\n" \
    "v_mfma_f32_16x16x32_bf16 v[56:59], v[120:123], v[76:79], v[56:59]\nds_read_b128 v[64:67], v9 offset:8192\n" \
    "v_mfma_f32_16x16x32_bf16 v[60:63], v[120:123], v[84:87], v[60:63]\nds_read_b128 v[72:75], v9 offset:12288\n" \
    "v_mfma_f32_16x16x32_bf16 v[32:35], v[104:107], v[76:79], v[32:35]\nv_mfma_f32_16x16x32_bf16 v[36:39], v[104:107], v[84:87], v[36:39]\n" \
    "v_mfma_f32_16x16x32_bf16 v[40:43], v[68:71], v[76:79], v[40:43]\nv_mfma_f32_16x16x32_bf16 v[44:47], v[68:71], v[84:87], v[44:47]\ns_waitcnt lgkmcnt(0)\n" \
    "v_mfma_f32_16x16x32_bf16 v[48:51], v[140:143], v[64:67], v[48:51]\nv_mfma_f32_16x16x32_bf16 v[52:55], v[140:143], v[72:75], v[52:55]\n" \
    "v_mfma_f32_16x16x32_bf16 v[56:59], v[148:151], v[64:67], v[56:59]\nds_read_b128 v[68:71], v10 offset:8192\n" \
    "v_mfma_f32_16x16x32_bf16 v[60:63], v[148:151], v[72:75], v[60:63]\nds_read_b128 v[76:79], v10 offset:12288\n" \
    "v_mfma_f32_16x16x32_bf16 v[32:35], v[124:127], v[64:67], v[32:35]\nv_mfma_f32_16x16x32_bf16 v[36:39], v[124:127], v[72:75], v[36:39]\n" \
    "v_mfma_f32_16x16x32_bf16 v[40:43], v[132:135], v[64:67], v[40:43]\nv_mfma_f32_16x16x32_bf16 v[44:47], v[132:135], v[72:75], v[44:47]\ns_waitcnt lgkmcnt(0)\n" \
    "v_mfma_f32_16x16x32_bf16 v[48:51], v[144:147], v[68:71], v[48:51]\nv_mfma_f32_16x16x32_bf16 v[52:55], v[144:147], v[76:79], v[52:55]\n" \
    "v_mfma_f32_16x16x32_bf16 v[56:59], v[152:155], v[68:71], v[56:59]\nds_read_b128 v[64:67], v9 offset:9216\n" \
    "v_mfma_f32_16x16x32_bf16 v[60:63], v[152:155], v[76:79], v[60:63]\nds_read_b128 v[72:75], v9 offset:13312\n" \
    "v_mfma_f32_16x16x32_bf16 v[32:35], v[128:131], v[68:71], v[32:35]\nv_mfma_f32_16x16x32_bf16 v[36:39], v[128:131], v[76:79], v[36:39]\n" \
    "v_mfma_f32_16x16x32_bf16 v[40:43], v[136:139], v[68:71], v[40:43]\nv_mfma_f32_16x16x32_bf16 v[44:47], v[136:139], v[76:79], v[44:47]\ns_waitcnt lgkmcnt(0)\n" \
    "v_mfma_f32_16x16x32_bf16 v[48:51], v[172:175], v[64:67], v[48:51]\nv_mfma_f32_16x16x32_bf16 v[52:55], v[172:175], v[72:75], v[52:55]\n" \
    "v_mfma_f32_16x16x32_bf16 v[56:59], v[180:183], v[64:67], v[56:59]\nds_read_b128 v[68:71], v10 offset:9216\n" \
    "v_mfma_f32_16x16x32_bf16 v[60:63], v[180:183], v[72:75], v[60:63]\nds_read_b128 v[76:79], v10 offset:13312\n" \
    "v_mfma_f32_16x16x32_bf16 v[32:35], v[156:159], v[64:67], v[32:35]\nv_mfma_f32_16x16x32_bf16 v[36:39], v[156:159], v[72:75], v[36:39]\n" \
    "v_mfma_f32_16x16x32_bf16 v[40:43], v[164:167], v[64:67], v[40:43]\nv_mfma_f32_16x16x32_bf16 v[44:47], v[164:167], v[72:75], v[44:47]\n" \
    "buffer_load_dword v13, off, s[64:67], s42 sc1\ns_waitcnt lgkmcnt(0)\nv_mfma_f32_16x16x32_bf16 v[48:51], v[176:179], v[68:71], v[48:51]\n" \
    "v_mfma_f32_16x16x32_bf16 v[52:55], v[176:179], v[76:79], v[52:55]\ns_waitcnt vmcnt(1)\ns_barrier\nv_mfma_f32_16x16x32_bf16 v[56:59], v[184:187], v[68:71], v[56:59]\n" \
    "ds_read_b128 v[64:67], v9 offset:16384\nv_mfma_f32_16x16x32_bf16 v[60:63], v[184:187], v[76:79], v[60:63]\nds_read_b128 v[72:75], v9 offset:20480\ns_add_u32 m0, s41, 0\n" \
    "v_mfma_f32_16x16x32_bf16 v[32:35], v[160:163], v[68:71], v[32:35]\ns_xor_b32 s85, s62, 0\ns_add_u32 s85, s85, s84\n" \
    "v_mfma_f32_16x16x32_bf16 v[36:39], v[160:163], v[76:79], v[36:39]\nbuffer_load_dwordx4 v11, s[64:67], s85 offen sc1 lds\n" \
    "v_mfma_f32_16x16x32_bf16 v[40:43], v[168:171], v[68:71], v[40:43]\nv_mfma_f32_16x16x32_bf16 v[44:47], v[168:171], v[76:79], v[44:47]\n" \
    "buffer_load_dwordx4 v12, s[64:67], s85 offen offset:1024 sc1 lds\ns_waitcnt lgkmcnt(0)\nv_mfma_f32_16x16x32_bf16 v[48:51], v[204:207], v[64:67], v[48:51]\n" \
    "v_mfma_f32_16x16x32_bf16 v[52:55], v[204:207], v[72:75], v[52:55]\nv_mfma_f32_16x16x32_bf16 v[56:59], v[212:215], v[64:67], v[56:59]\n" \
    "ds_read_b128 v[68:71], v10 offset:16384\nv_mfma_f32_16x16x32_bf16 v[60:63], v[212:215], v[72:75], v[60:63]\nds_read_b128 v[76:79], v10 offset:20480\n" \
    "v_mfma_f32_16x16x32_bf16 v[32:35], v[188:191], v[64:67], v[32:35]\nv_mfma_f32_16x16x32_bf16 v[36:39], v[188:191], v[72:75], v[36:39]\n" \
    "v_mfma_f32_16x16x32_bf16 v[40:43], v[196:199], v[64:67], v[40:43]\nv_mfma_f32_16x16x32_bf16 v[44:47], v[196:199], v[72:75], v[44:47]\ns_waitcnt lgkmcnt(0)\n" \
    "v_mfma_f32_16x16x32_bf16 v[48:51], v[208:211], v[68:71], v[48:51]\nv_mfma_f32_16x16x32_bf16 v[52:55], v[208:211], v[76:79], v[52:55]\n" \
    "v_mfma_f32_16x16x32_bf16 v[56:59], v[216:219], v[68:71], v[56:59]\nds_read_b128 v[64:67], v9 offset:17408\n" \
    "v_mfma_f32_16x16x32_bf16 v[60:63], v[216:219], v[76:79], v[60:63]\nds_read_b128 v[72:75], v9 offset:21504\n" \
    "v_mfma_f32_16x16x32_bf16 v[32:35], v[192:195], v[68:71], v[32:35]\nv_mfma_f32_16x16x32_bf16 v[36:39], v[192:195], v[76:79], v[36:39]\n" \
    "v_mfma_f32_16x16x32_bf16 v[40:43], v[200:203], v[68:71], v[40:43]\nv_mfma_f32_16x16x32_bf16 v[44:47], v[200:203], v[76:79], v[44:47]\ns_waitcnt lgkmcnt(0)\n" \
    "v_mfma_f32_16x16x32_bf16 v[48:51], v[236:239], v[64:67], v[48:51]\nv_mfma_f32_16x16x32_bf16 v[52:55], v[236:239], v[72:75], v[52:55]\n" \
    "v_mfma_f32_16x16x32_bf16 v[56:59], v[244:247], v[64:67], v[56:59]\nds_read_b128 v[68:71], v10 offset:17408\n" \
    "v_mfma_f32_16x16x32_bf16 v[60:63], v[244:247], v[72:75], v[60:63]\nds_read_b128 v[76:79], v10 offset:21504\n" \
    "v_mfma_f32_16x16x32_bf16 v[32:35], v[220:223], v[64:67], v[32:35]\nv_mfma_f32_16x16x32_bf16 v[36:39], v[220:223], v[72:75], v[36:39]\n" \
    "v_mfma_f32_16x16x32_bf16 v[40:43], v[228:231], v[64:67], v[40:43]\nv_mfma_f32_16x16x32_bf16 v[44:47], v[228:231], v[72:75], v[44:47]\ns_waitcnt lgkmcnt(0)\n" \
    "v_mfma_f32_16x16x32_bf16 v[48:51], v[240:243], v[68:71], v[48:51]\nv_mfma_f32_16x16x32_bf16 v[52:55], v[240:243], v[76:79], v[52:55]\n" \
    "v_mfma_f32_16x16x32_bf16 v[56:59], v[248:251], v[68:71], v[56:59]\nv_mfma_f32_16x16x32_bf16 v[60:63], v[248:251], v[76:79], v[60:63]\n" \
    "v_mfma_f32_16x16x32_bf16 v[32:35], v[224:227], v[68:71], v[32:35]\nv_mfma_f32_16x16x32_bf16 v[36:39], v[224:227], v[76:79], v[36:39]\n" \
    "v_mfma_f32_16x16x32_bf16 v[40:43], v[232:235], v[68:71], v[40:43]\nv_mfma_f32_16x16x32_bf16 v[44:47], v[232:235], v[76:79], v[44:47]\ns_barrier\n" \
    "v_cvt_pk_bf16_f32 v64, v48, v49\nv_cvt_pk_bf16_f32 v65, v50, v51\nv_cvt_pk_bf16_f32 v66, v56, v57\nv_cvt_pk_bf16_f32 v67, v58, v59\nds_write_b128 v14, v[64:67] offset:0\n" \
    "v_cvt_pk_bf16_f32 v68, v52, v53\nv_cvt_pk_bf16_f32 v69, v54, v55\nv_cvt_pk_bf16_f32 v70, v60, v61\nv_cvt_pk_bf16_f32 v71, v62, v63\n" \
    "ds_write_b128 v14, v[68:71] offset:4096\nv_cvt_pk_bf16_f32 v72, v32, v33\nv_cvt_pk_bf16_f32 v73, v34, v35\nv_cvt_pk_bf16_f32 v74, v40, v41\n" \
    "v_cvt_pk_bf16_f32 v75, v42, v43\nds_write_b128 v14, v[72:75] offset:8192\nv_cvt_pk_bf16_f32 v76, v36, v37\nv_cvt_pk_bf16_f32 v77, v38, v39\n" \
    "v_cvt_pk_bf16_f32 v78, v44, v45\nv_cvt_pk_bf16_f32 v79, v46, v47\nds_write_b128 v14, v[76:79] offset:12288\ns_add_u32 m0, s41, 8192\ns_xor_b32 s85, s62, 256\n" \
    "s_add_u32 s85, s85, s84\nbuffer_load_dwordx4 v11, s[64:67], s85 offen sc1 lds\nbuffer_load_dwordx4 v12, s[64:67], s85 offen offset:1024 sc1 lds\ns_add_u32 m0, s41, 16384\n" \
    "s_xor_b32 s85, s62, 512\ns_add_u32 s85, s85, s84\nbuffer_load_dwordx4 v11, s[64:67], s85 offen sc1 lds\nbuffer_load_dwordx4 v12, s[64:67], s85 offen offset:1024 sc1 lds\n" \
    "s_and_b32 s69, s35, 15\ns_lshl_b32 s69, s69, 17\ns_add_u32 s81, s37, s69\ns_lshr_b32 s69, s81, 8\ns_and_b32 s69, s69, 0x2000\ns_xor_b32 s81, s81, s69\ns_mov_b32 s68, 0\n" \
    "s_lshr_b32 s69, s35, 1\ns_cmp_eq_u32 s69, 5\ns_cbranch_scc1 .Lq14_%=\n.Lq15_%=:\ns_cmp_eq_u32 s35, 0\ns_cbranch_scc1 .Lq16_%=\ns_waitcnt vmcnt(6)\n.Lq17_%=:\n" \
    "v_readfirstlane_b32 s74, v13\ns_nop 0\ns_and_b32 s75, s74, 127\ns_cmp_lt_u32 s75, s35\ns_cbranch_scc1 .Lq13_%=\ns_memrealtime s[72:73]\ns_waitcnt lgkmcnt(0)\n" \
    "s_lshr_b32 s69, s74, 8\ns_sub_u32 s69, s69, s72\ns_bfe_i32 s69, s69, 0x180000\ns_add_u32 s80, s72, s69\ns_sub_u32 s76, s80, s45\ns_sub_u32 s69, s72, s76\n" \
    "s_cmp_ge_i32 s69, -8\ns_cbranch_scc1 .Lq11_%=\n.Lq18_%=:\ns_memrealtime s[72:73]\ns_waitcnt lgkmcnt(0)\ns_sub_u32 s72, s72, s76\ns_cmp_lt_i32 s72, 0\n" \
    "s_cbranch_scc1 .Lq18_%=\ns_branch .Lq10_%=\n.Lq11_%=:\ns_lshl_b32 s74, s74, 5\ns_and_b32 s74, s74, 0x1000\ns_xor_b32 s81, s81, s74\ns_lshl_b32 s74, s74, 2\n" \
    "s_xor_b32 s82, s82, s74\n.Lq12_%=:\ns_mov_b32 s68, 0\ns_cmp_eq_u32 s36, 1\ns_cbranch_scc1 .Lq5_%=\ns_cmp_eq_u32 s36, 2\ns_cbranch_scc1 .Lq6_%=\ns_cmp_eq_u32 s36, 3\n" \
    "s_cbranch_scc1 .Lq7_%=\n.Lq4_%=:\nds_read_b128 v[32:35], v3 offset:0\nds_read_b128 v[36:39], v3 offset:1024\nds_read_b128 v[40:43], v3 offset:8192\n" \
    "ds_read_b128 v[44:47], v3 offset:9216\nds_read_b128 v[48:51], v3 offset:16384\nds_read_b128 v[52:55], v3 offset:17408\nds_read_b128 v[56:59], v3 offset:2048\n" \
    "ds_read_b128 v[60:63], v3 offset:3072\nds_read_b128 v[64:67], v3 offset:4096\nds_read_b128 v[68:71], v3 offset:5120\n.Lq19_%=:\ns_xor_b32 s90, s81, 0x800\n.Lq21_%=:\n" \
    "s_memrealtime s[72:73]\ns_waitcnt lgkmcnt(0)\ns_sub_u32 s72, s72, s80\ns_cmp_lt_i32 s72, 0\ns_cbranch_scc1 .Lq21_%=\n" \
    "buffer_load_dwordx4 v[72:75], v2, s[64:67], s81 offen offset:0 sc1\nbuffer_load_dwordx4 v[76:79], v2, s[64:67], s81 offen offset:1024 sc1\n" \
    "buffer_load_dwordx4 v[80:83], v2, s[64:67], s90 offen offset:0 sc1\nbuffer_load_dwordx4 v[84:87], v2, s[64:67], s90 offen offset:1024 sc1\nv_mov_b64 v[16:17], 0\n" \
    "v_mov_b64 v[18:19], 0\nv_mov_b64 v[20:21], 0\nv_mov_b64 v[22:23], 0\nv_mov_b64 v[24:25], 0\nv_mov_b64 v[26:27], 0\nv_mov_b64 v[28:29], 0\nv_mov_b64 v[30:31], 0\n" \
    "s_waitcnt vmcnt(3)\nds_write_b128 v1, v[72:75] offset:24576\nv_smfmac_f32_16x16x64_bf16 v[20:23], v[72:75], v[32:39], v0\nv_max3_u32 v252, v72, v73, v74\n" \
    "v_smfmac_f32_16x16x64_bf16 v[16:19], v[72:75], a[0:7], v0\ns_waitcnt vmcnt(2)\nds_write_b128 v1, v[76:79] offset:28672\nv_max3_u32 v252, v252, v75, v76\n" \
    "v_smfmac_f32_16x16x64_bf16 v[24:27], v[72:75], a[8:15], v0\nv_max3_u32 v253, v77, v78, v79\nv_smfmac_f32_16x16x64_bf16 v[28:31], v[72:75], a[16:23], v0\n" \
    "s_waitcnt vmcnt(1)\nds_write_b128 v1, v[80:83] offset:32768\nv_max3_u32 v252, v252, v80, v81\nv_smfmac_f32_16x16x64_bf16 v[20:23], v[76:79], v[40:47], v0\n" \
    "v_max3_u32 v253, v253, v82, v83\nv_smfmac_f32_16x16x64_bf16 v[16:19], v[76:79], a[96:103], v0\ns_waitcnt vmcnt(0)\nds_write_b128 v1, v[84:87] offset:36864\n" \
    "v_max3_u32 v252, v252, v84, v85\nv_smfmac_f32_16x16x64_bf16 v[24:27], v[76:79], a[104:111], v0\nv_max3_u32 v253, v253, v86, v87\n" \
    "v_smfmac_f32_16x16x64_bf16 v[28:31], v[76:79], a[112:119], v0\nv_max_u32 v252, v252, v253\nv_smfmac_f32_16x16x64_bf16 v[20:23], v[80:83], v[48:55], v0\n" \
    "v_cmp_eq_u32 vcc, -1, v252\nv_smfmac_f32_16x16x64_bf16 v[16:19], v[80:83], a[192:199], v0\ns_cbranch_vccnz .Lq20_%=\ns_waitcnt lgkmcnt(0)\ns_barrier\n" \
    "ds_read_b128 v[88:91], v1 offset:25600\nds_read_b128 v[32:35], v1 offset:26624\nds_read_b128 v[36:39], v1 offset:27648\n" \
    "v_smfmac_f32_16x16x64_bf16 v[24:27], v[80:83], a[200:207], v0\nds_read_b128 v[40:43], v3 offset:6144\nv_smfmac_f32_16x16x64_bf16 v[28:31], v[80:83], a[208:215], v0\n" \
    "ds_read_b128 v[44:47], v3 offset:7168\nv_smfmac_f32_16x16x64_bf16 v[20:23], v[84:87], v[132:139], v0\nds_read_b128 v[48:51], v1 offset:29696\n" \
    "v_smfmac_f32_16x16x64_bf16 v[16:19], v[84:87], v[124:131], v0\nds_read_b128 v[72:75], v3 offset:10240\nv_smfmac_f32_16x16x64_bf16 v[24:27], v[84:87], v[140:147], v0\n" \
    "ds_read_b128 v[76:79], v3 offset:11264\nv_smfmac_f32_16x16x64_bf16 v[28:31], v[84:87], v[148:155], v0\nds_read_b128 v[52:55], v1 offset:30720\ns_waitcnt lgkmcnt(8)\n" \
    "v_smfmac_f32_16x16x64_bf16 v[20:23], v[88:91], v[56:63], v0\nds_read_b128 v[56:59], v3 offset:12288\nv_smfmac_f32_16x16x64_bf16 v[16:19], v[88:91], a[24:31], v0\n" \
    "ds_read_b128 v[60:63], v3 offset:13312\nv_smfmac_f32_16x16x64_bf16 v[24:27], v[88:91], a[32:39], v0\nds_read_b128 v[80:83], v1 offset:31744\n" \
    "v_smfmac_f32_16x16x64_bf16 v[28:31], v[88:91], a[40:47], v0\nds_read_b128 v[84:87], v3 offset:14336\ns_waitcnt lgkmcnt(11)\n" \
    "v_smfmac_f32_16x16x64_bf16 v[20:23], v[32:35], v[64:71], v0\nds_read_b128 v[88:91], v3 offset:15360\nv_smfmac_f32_16x16x64_bf16 v[16:19], v[32:35], a[48:55], v0\n" \
    "ds_read_b128 v[64:67], v1 offset:33792\ns_waitcnt lgkmcnt(10)\nv_smfmac_f32_16x16x64_bf16 v[20:23], v[36:39], v[40:47], v0\nds_read_b128 v[40:43], v3 offset:18432\n" \
    "v_smfmac_f32_16x16x64_bf16 v[16:19], v[36:39], a[72:79], v0\nds_read_b128 v[44:47], v3 offset:19456\ns_waitcnt lgkmcnt(9)\n" \
    "v_smfmac_f32_16x16x64_bf16 v[20:23], v[48:51], v[72:79], v0\nds_read_b128 v[68:71], v1 offset:34816\nv_smfmac_f32_16x16x64_bf16 v[16:19], v[48:51], a[120:127], v0\n" \
    "ds_read_b128 v[72:75], v3 offset:20480\ns_waitcnt lgkmcnt(8)\nv_smfmac_f32_16x16x64_bf16 v[20:23], v[52:55], v[56:63], v0\nds_read_b128 v[76:79], v3 offset:21504\n" \
    "v_smfmac_f32_16x16x64_bf16 v[16:19], v[52:55], a[144:151], v0\nds_read_b128 v[56:59], v1 offset:35840\ns_waitcnt lgkmcnt(7)\n" \
    "v_smfmac_f32_16x16x64_bf16 v[20:23], v[80:83], v[84:91], v0\nds_read_b128 v[84:87], v3 offset:22528\nv_smfmac_f32_16x16x64_bf16 v[16:19], v[80:83], a[168:175], v0\n" \
    "ds_read_b128 v[88:91], v3 offset:23552\ns_waitcnt lgkmcnt(6)\nv_smfmac_f32_16x16x64_bf16 v[20:23], v[64:67], v[40:47], v0\n" \
    "v_smfmac_f32_16x16x64_bf16 v[16:19], v[64:67], a[216:223], v0\nds_read_b128 v[60:63], v1 offset:37888\ns_waitcnt lgkmcnt(4)\n" \
    "v_smfmac_f32_16x16x64_bf16 v[20:23], v[68:71], v[72:79], v0\nds_read_b128 v[40:43], v1 offset:38912\nv_smfmac_f32_16x16x64_bf16 v[16:19], v[68:71], a[240:247], v0\n" \
    "ds_read_b128 v[44:47], v1 offset:39936\ns_waitcnt lgkmcnt(3)\nv_smfmac_f32_16x16x64_bf16 v[20:23], v[56:59], v[84:91], v0\n" \
    "v_smfmac_f32_16x16x64_bf16 v[16:19], v[56:59], v[100:107], v0\ns_waitcnt lgkmcnt(2)\nv_smfmac_f32_16x16x64_bf16 v[20:23], v[60:63], v[164:171], v0\ns_waitcnt lgkmcnt(1)\n" \
    "v_smfmac_f32_16x16x64_bf16 v[20:23], v[40:43], v[196:203], v0\ns_waitcnt lgkmcnt(0)\nv_smfmac_f32_16x16x64_bf16 v[20:23], v[44:47], v[228:235], v0\ns_nop 2\n" \
    "v_smfmac_f32_16x16x64_bf16 v[16:19], v[60:63], v[156:163], v0\ns_nop 2\nv_smfmac_f32_16x16x64_bf16 v[16:19], v[40:43], v[188:195], v0\nv_permlane32_swap_b32 v20, v22\n" \
    "v_smfmac_f32_16x16x64_bf16 v[16:19], v[44:47], v[220:227], v0\ns_nop 1\nv_permlane32_swap_b32 v21, v23\nv_smfmac_f32_16x16x64_bf16 v[24:27], v[32:35], a[56:63], v0\n" \
    "s_nop 1\nv_add_f32 v20, v20, v22\nv_smfmac_f32_16x16x64_bf16 v[28:31], v[32:35], a[64:71], v0\nv_add_f32 v21, v21, v23\n" \
    "v_smfmac_f32_16x16x64_bf16 v[24:27], v[36:39], a[80:87], v0\nv_mul_f32 v20, s32, v20\nv_smfmac_f32_16x16x64_bf16 v[28:31], v[36:39], a[88:95], v0\nv_mul_f32 v21, s32, v21\n" \
    "v_smfmac_f32_16x16x64_bf16 v[24:27], v[48:51], a[128:135], v0\nv_cvt_pk_bf16_f32 v20, v20, v21\nv_smfmac_f32_16x16x64_bf16 v[28:31], v[48:51], a[136:143], v0\n" \
    "ds_write_b16 v4, v20 offset:8\nv_permlane32_swap_b32 v16, v18\nv_smfmac_f32_16x16x64_bf16 v[24:27], v[52:55], a[152:159], v0\nds_write_b16_d16_hi v4, v20 offset:24\n" \
    "v_permlane32_swap_b32 v17, v19\nv_smfmac_f32_16x16x64_bf16 v[24:27], v[80:83], a[176:183], v0\nds_read_b128 v[48:51], v1 offset:40960\nv_add_f32 v16, v16, v18\n" \
    "v_smfmac_f32_16x16x64_bf16 v[24:27], v[64:67], a[224:231], v0\nds_read_b128 v[32:35], v1 offset:45056\nv_add_f32 v17, v17, v19\n" \
    "v_smfmac_f32_16x16x64_bf16 v[24:27], v[68:71], a[248:255], v0\nds_read_b128 v[36:39], v1 offset:41984\nv_mul_f32 v16, s32, v16\n" \
    "v_smfmac_f32_16x16x64_bf16 v[24:27], v[56:59], v[108:115], v0\nds_read_b128 v[72:75], v1 offset:46080\nv_mul_f32 v17, s32, v17\n" \
    "v_smfmac_f32_16x16x64_bf16 v[24:27], v[60:63], v[172:179], v0\nds_read_b128 v[76:79], v1 offset:43008\nv_cvt_pk_bf16_f32 v16, v16, v17\n" \
    "v_smfmac_f32_16x16x64_bf16 v[24:27], v[40:43], v[204:211], v0\nds_write_b16 v4, v16 offset:0\nv_smfmac_f32_16x16x64_bf16 v[24:27], v[44:47], v[236:243], v0\n" \
    "ds_write_b16_d16_hi v4, v16 offset:16\ns_nop 1\nv_smfmac_f32_16x16x64_bf16 v[28:31], v[52:55], a[160:167], v0\nds_read_b128 v[52:55], v1 offset:47104\ns_nop 1\n" \
    "v_smfmac_f32_16x16x64_bf16 v[28:31], v[80:83], a[184:191], v0\nds_read_b128 v[80:83], v1 offset:44032\nv_permlane32_swap_b32 v24, v26\n" \
    "v_smfmac_f32_16x16x64_bf16 v[28:31], v[64:67], a[232:239], v0\nds_read_b128 v[64:67], v1 offset:48128\nv_permlane32_swap_b32 v25, v27\n" \
    "v_smfmac_f32_16x16x64_bf16 v[28:31], v[68:71], v[92:99], v0\ns_waitcnt lgkmcnt(3)\ns_barrier\nds_read_b128 v[68:71], v6 offset:0\nv_add_f32 v24, v24, v26\n" \
    "v_smfmac_f32_16x16x64_bf16 v[28:31], v[56:59], v[116:123], v0\nds_read_b128 v[56:59], v6 offset:512\nv_add_f32 v25, v25, v27\n" \
    "v_smfmac_f32_16x16x64_bf16 v[28:31], v[60:63], v[180:187], v0\nds_read_b128 v[60:63], v6 offset:1024\nv_cvt_pk_bf16_f32 v24, v24, v25\n" \
    "v_smfmac_f32_16x16x64_bf16 v[28:31], v[40:43], v[212:219], v0\nds_read_b128 v[40:43], v6 offset:1536\nv_smfmac_f32_16x16x64_bf16 v[28:31], v[44:47], v[244:251], v0\n" \
    "ds_write_b16 v4, v24 offset:2048\nds_write_b16_d16_hi v4, v24 offset:2064\ns_waitcnt lgkmcnt(5)\nv_mfma_f32_16x16x32_bf16 v[16:19], v[48:51], v[68:71], 0\ns_nop 2\n" \
    "v_mfma_f32_16x16x32_bf16 v[20:23], v[32:35], v[68:71], 0\nv_permlane32_swap_b32 v28, v30\nv_permlane32_swap_b32 v29, v31\nv_add_f32 v28, v28, v30\nv_add_f32 v29, v29, v31\n" \
    "v_cvt_pk_bf16_f32 v28, v28, v29\nds_write_b16 v4, v28 offset:2056\nds_write_b16_d16_hi v4, v28 offset:2072\ns_waitcnt lgkmcnt(6)\n" \
    "v_mfma_f32_16x16x32_bf16 v[16:19], v[36:39], v[56:59], v[16:19]\nv_mfma_f32_16x16x32_bf16 v[20:23], v[72:75], v[56:59], v[20:23]\ns_waitcnt lgkmcnt(5)\n" \
    "v_mfma_f32_16x16x32_bf16 v[16:19], v[76:79], v[60:63], v[16:19]\nv_mfma_f32_16x16x32_bf16 v[20:23], v[52:55], v[60:63], v[20:23]\ns_waitcnt lgkmcnt(0)\ns_barrier\n" \
    "ds_read_b128 v[44:47], v6 offset:2048\nds_read_b128 v[84:87], v6 offset:2560\nv_mfma_f32_16x16x32_bf16 v[16:19], v[80:83], v[40:43], v[16:19]\n" \
    "ds_read_b128 v[88:91], v6 offset:3072\nds_read_b128 v[28:31], v6 offset:3584\nv_mfma_f32_16x16x32_bf16 v[20:23], v[64:67], v[40:43], v[20:23]\nv_mov_b32 v15, v15\n" \
    "v_mov_b32 v15, v15\nv_mov_b32 v15, v15\nv_mov_b32 v15, v15\nv_mov_b32 v15, v15\nv_mov_b32 v15, v15\ns_waitcnt lgkmcnt(3)\n" \
    "v_mfma_f32_16x16x32_bf16 v[24:27], v[44:47], v[68:71], 0\ns_waitcnt lgkmcnt(2)\nv_mov_b32_dpp v16, v20 quad_perm:[0,1,2,3] row_mask:0xf bank_mask:0xc\n" \
    "v_mov_b32_dpp v17, v21 quad_perm:[0,1,2,3] row_mask:0xf bank_mask:0xc\nv_mfma_f32_16x16x32_bf16 v[24:27], v[84:87], v[56:59], v[24:27]\ns_waitcnt lgkmcnt(1)\n" \
    "v_mov_b32_dpp v18, v22 quad_perm:[0,1,2,3] row_mask:0xf bank_mask:0xc\nv_mov_b32_dpp v19, v23 quad_perm:[0,1,2,3] row_mask:0xf bank_mask:0xc\n" \
    "v_mfma_f32_16x16x32_bf16 v[24:27], v[88:91], v[60:63], v[24:27]\ns_waitcnt lgkmcnt(0)\nv_max3_f32 v254, v16, v17, v18\nv_max_f32 v254, v254, v19\n" \
    "v_mfma_f32_16x16x32_bf16 v[24:27], v[28:31], v[40:43], v[24:27]\ns_nop 0\nv_max_f32_dpp v252, v254, v254 row_ror:8 row_mask:0xf bank_mask:0xf\n" \
    "v_max_f32_dpp v253, v254, v254 row_ror:8 row_mask:0xf bank_mask:0xf\ns_nop 1\nv_permlane32_swap_b32 v252, v253\nv_max_f32 v252, v252, v253\ns_xor_b32 s74, s82, 0x4000\n" \
    "v_max3_f32 v254, v252, v24, v25\nv_max3_f32 v252, v254, v26, v27\nv_max3_f32 v253, v254, v26, v27\ns_nop 1\nv_permlane16_swap_b32 v252, v253\nv_max_f32 v252, v252, v253\n" \
    "v_pk_add_f32 v[16:17], v[16:17], v[252:253] op_sel_hi:[1,0] neg_lo:[0,1] neg_hi:[0,1]\n" \
    "v_pk_add_f32 v[18:19], v[18:19], v[252:253] op_sel_hi:[1,0] neg_lo:[0,1] neg_hi:[0,1]\n" \
    "v_pk_add_f32 v[24:25], v[24:25], v[252:253] op_sel_hi:[1,0] neg_lo:[0,1] neg_hi:[0,1]\n" \
    "v_pk_add_f32 v[26:27], v[26:27], v[252:253] op_sel_hi:[1,0] neg_lo:[0,1] neg_hi:[0,1]\nv_exp_f32 v16, v16\nv_exp_f32 v17, v17\nv_exp_f32 v18, v18\nv_exp_f32 v19, v19\n" \
    "v_exp_f32 v24, v24\nv_pk_add_f32 v[254:255], v[16:17], v[18:19]\nv_exp_f32 v25, v25\nv_add_f32 v254, v254, v255\nv_exp_f32 v26, v26\nv_exp_f32 v27, v27\n" \
    "v_add_f32_dpp v252, v254, v254 row_ror:8 row_mask:0xf bank_mask:0xf\nv_add_f32_dpp v253, v254, v254 row_ror:8 row_mask:0xf bank_mask:0xf\n" \
    "v_pk_add_f32 v[254:255], v[24:25], v[26:27]\ns_nop 0\nv_permlane32_swap_b32 v252, v253\nv_pk_add_f32 v[254:255], v[254:255], v[252:253]\n" \
    "v_pk_add_f32 v[252:253], v[254:255], v[254:255] op_sel:[0,1] op_sel_hi:[1,0]\ns_nop 1\nv_permlane16_swap_b32 v252, v253\nv_add_f32 v252, v252, v253\nv_rcp_f32 v252, v252\n" \
    "s_nop 0\nv_pk_mul_f32 v[24:25], v[24:25], v[252:253] op_sel_hi:[1,0]\nv_pk_mul_f32 v[26:27], v[26:27], v[252:253] op_sel_hi:[1,0]\nv_cvt_pk_bf16_f32 v20, v24, v25\n" \
    "v_cvt_pk_bf16_f32 v21, v26, v27\nv_pk_mul_f32 v[16:17], v[16:17], v[252:253] op_sel_hi:[1,0]\nv_pk_mul_f32 v[18:19], v[18:19], v[252:253] op_sel_hi:[1,0]\n" \
    "v_permlane16_swap_b32 v20, v22\nv_cvt_pk_bf16_f32 v16, v16, v17\nv_cvt_pk_bf16_f32 v17, v18, v19\nv_permlane16_swap_b32 v21, v23\n" \
    "v_mov_b32_dpp v20, v16 quad_perm:[0,1,2,3] row_mask:0xf bank_mask:0x3\nv_mov_b32_dpp v22, v16 row_ror:8 row_mask:0xf bank_mask:0x3\n" \
    "v_mov_b32_dpp v21, v17 quad_perm:[0,1,2,3] row_mask:0xf bank_mask:0x3\nv_mov_b32_dpp v23, v17 row_ror:8 row_mask:0xf bank_mask:0x3\n" \
    "buffer_store_dwordx4 v[20:23], v7, s[64:67], s82 offen sc1\nbuffer_store_dwordx4 v[20:23], v7, s[64:67], s74 offen sc1\ns_branch .Lq3_%=\n.Lq5_%=:\n" \
    "ds_read_b128 v[32:35], v3 offset:2048\nds_read_b128 v[36:39], v3 offset:3072\nds_read_b128 v[40:43], v3 offset:10240\nds_read_b128 v[44:47], v3 offset:11264\n" \
    "ds_read_b128 v[48:51], v3 offset:18432\nds_read_b128 v[52:55], v3 offset:19456\nds_read_b128 v[56:59], v3 offset:0\nds_read_b128 v[60:63], v3 offset:1024\n" \
    "ds_read_b128 v[64:67], v3 offset:4096\nds_read_b128 v[68:71], v3 offset:5120\n.Lq23_%=:\ns_xor_b32 s90, s81, 0x800\n.Lq25_%=:\ns_memrealtime s[72:73]\n" \
    "s_waitcnt lgkmcnt(0)\ns_sub_u32 s72, s72, s80\ns_cmp_lt_i32 s72, 0\ns_cbranch_scc1 .Lq25_%=\nbuffer_load_dwordx4 v[72:75], v2, s[64:67], s81 offen offset:0 sc1\n" \
    "buffer_load_dwordx4 v[76:79], v2, s[64:67], s81 offen offset:1024 sc1\nbuffer_load_dwordx4 v[80:83], v2, s[64:67], s90 offen offset:0 sc1\n" \
    "buffer_load_dwordx4 v[84:87], v2, s[64:67], s90 offen offset:1024 sc1\nv_mov_b64 v[16:17], 0\nv_mov_b64 v[18:19], 0\nv_mov_b64 v[20:21], 0\nv_mov_b64 v[22:23], 0\n" \
    "v_mov_b64 v[24:25], 0\nv_mov_b64 v[26:27], 0\nv_mov_b64 v[28:29], 0\nv_mov_b64 v[30:31], 0\ns_waitcnt vmcnt(3)\nds_write_b128 v1, v[72:75] offset:25600\n" \
    "v_smfmac_f32_16x16x64_bf16 v[20:23], v[72:75], v[32:39], v0\nv_max3_u32 v252, v72, v73, v74\nv_smfmac_f32_16x16x64_bf16 v[16:19], v[72:75], a[24:31], v0\n" \
    "s_waitcnt vmcnt(2)\nds_write_b128 v1, v[76:79] offset:29696\nv_max3_u32 v252, v252, v75, v76\nv_smfmac_f32_16x16x64_bf16 v[24:27], v[72:75], a[32:39], v0\n" \
    "v_max3_u32 v253, v77, v78, v79\nv_smfmac_f32_16x16x64_bf16 v[28:31], v[72:75], a[40:47], v0\ns_waitcnt vmcnt(1)\nds_write_b128 v1, v[80:83] offset:33792\n" \
    "v_max3_u32 v252, v252, v80, v81\nv_smfmac_f32_16x16x64_bf16 v[20:23], v[76:79], v[40:47], v0\nv_max3_u32 v253, v253, v82, v83\n" \
    "v_smfmac_f32_16x16x64_bf16 v[16:19], v[76:79], a[120:127], v0\ns_waitcnt vmcnt(0)\nds_write_b128 v1, v[84:87] offset:37888\nv_max3_u32 v252, v252, v84, v85\n" \
    "v_smfmac_f32_16x16x64_bf16 v[24:27], v[76:79], a[128:135], v0\nv_max3_u32 v253, v253, v86, v87\nv_smfmac_f32_16x16x64_bf16 v[28:31], v[76:79], a[136:143], v0\n" \
    "v_max_u32 v252, v252, v253\nv_smfmac_f32_16x16x64_bf16 v[20:23], v[80:83], v[48:55], v0\nv_cmp_eq_u32 vcc, -1, v252\n" \
    "v_smfmac_f32_16x16x64_bf16 v[16:19], v[80:83], a[216:223], v0\ns_cbranch_vccnz .Lq24_%=\ns_waitcnt lgkmcnt(0)\ns_barrier\nds_read_b128 v[88:91], v1 offset:24576\n" \
    "ds_read_b128 v[32:35], v1 offset:26624\nds_read_b128 v[36:39], v1 offset:27648\nv_smfmac_f32_16x16x64_bf16 v[24:27], v[80:83], a[224:231], v0\n" \
    "ds_read_b128 v[40:43], v3 offset:6144\nv_smfmac_f32_16x16x64_bf16 v[28:31], v[80:83], a[232:239], v0\nds_read_b128 v[44:47], v3 offset:7168\n" \
    "v_smfmac_f32_16x16x64_bf16 v[20:23], v[84:87], v[164:171], v0\nds_read_b128 v[48:51], v1 offset:28672\nv_smfmac_f32_16x16x64_bf16 v[16:19], v[84:87], v[156:163], v0\n" \
    "ds_read_b128 v[72:75], v3 offset:8192\nv_smfmac_f32_16x16x64_bf16 v[24:27], v[84:87], v[172:179], v0\nds_read_b128 v[76:79], v3 offset:9216\n" \
    "v_smfmac_f32_16x16x64_bf16 v[28:31], v[84:87], v[180:187], v0\nds_read_b128 v[52:55], v1 offset:30720\ns_waitcnt lgkmcnt(8)\n" \
    "v_smfmac_f32_16x16x64_bf16 v[20:23], v[88:91], v[56:63], v0\nds_read_b128 v[56:59], v3 offset:12288\nv_smfmac_f32_16x16x64_bf16 v[16:19], v[88:91], a[0:7], v0\n" \
    "ds_read_b128 v[60:63], v3 offset:13312\nv_smfmac_f32_16x16x64_bf16 v[24:27], v[88:91], a[8:15], v0\nds_read_b128 v[80:83], v1 offset:31744\n" \
    "v_smfmac_f32_16x16x64_bf16 v[28:31], v[88:91], a[16:23], v0\nds_read_b128 v[84:87], v3 offset:14336\ns_waitcnt lgkmcnt(11)\n" \
    "v_smfmac_f32_16x16x64_bf16 v[20:23], v[32:35], v[64:71], v0\nds_read_b128 v[88:91], v3 offset:15360\nv_smfmac_f32_16x16x64_bf16 v[16:19], v[32:35], a[48:55], v0\n" \
    "ds_read_b128 v[64:67], v1 offset:32768\ns_waitcnt lgkmcnt(10)\nv_smfmac_f32_16x16x64_bf16 v[20:23], v[36:39], v[40:47], v0\nds_read_b128 v[40:43], v3 offset:16384\n" \
    "v_smfmac_f32_16x16x64_bf16 v[16:19], v[36:39], a[72:79], v0\nds_read_b128 v[44:47], v3 offset:17408\ns_waitcnt lgkmcnt(9)\n" \
    "v_smfmac_f32_16x16x64_bf16 v[20:23], v[48:51], v[72:79], v0\nds_read_b128 v[68:71], v1 offset:34816\nv_smfmac_f32_16x16x64_bf16 v[16:19], v[48:51], a[96:103], v0\n" \
    "ds_read_b128 v[72:75], v3 offset:20480\ns_waitcnt lgkmcnt(8)\nv_smfmac_f32_16x16x64_bf16 v[20:23], v[52:55], v[56:63], v0\nds_read_b128 v[76:79], v3 offset:21504\n" \
    "v_smfmac_f32_16x16x64_bf16 v[16:19], v[52:55], a[144:151], v0\nds_read_b128 v[56:59], v1 offset:35840\ns_waitcnt lgkmcnt(7)\n" \
    "v_smfmac_f32_16x16x64_bf16 v[20:23], v[80:83], v[84:91], v0\nds_read_b128 v[84:87], v3 offset:22528\nv_smfmac_f32_16x16x64_bf16 v[16:19], v[80:83], a[168:175], v0\n" \
    "ds_read_b128 v[88:91], v3 offset:23552\ns_waitcnt lgkmcnt(6)\nv_smfmac_f32_16x16x64_bf16 v[20:23], v[64:67], v[40:47], v0\n" \
    "v_smfmac_f32_16x16x64_bf16 v[16:19], v[64:67], a[192:199], v0\nds_read_b128 v[60:63], v1 offset:36864\ns_waitcnt lgkmcnt(4)\n" \
    "v_smfmac_f32_16x16x64_bf16 v[20:23], v[68:71], v[72:79], v0\nds_read_b128 v[40:43], v1 offset:38912\nv_smfmac_f32_16x16x64_bf16 v[16:19], v[68:71], a[240:247], v0\n" \
    "ds_read_b128 v[44:47], v1 offset:39936\ns_waitcnt lgkmcnt(3)\nv_smfmac_f32_16x16x64_bf16 v[20:23], v[56:59], v[84:91], v0\n" \
    "v_smfmac_f32_16x16x64_bf16 v[16:19], v[56:59], v[100:107], v0\ns_waitcnt lgkmcnt(2)\nv_smfmac_f32_16x16x64_bf16 v[20:23], v[60:63], v[132:139], v0\ns_waitcnt lgkmcnt(1)\n" \
    "v_smfmac_f32_16x16x64_bf16 v[20:23], v[40:43], v[196:203], v0\ns_waitcnt lgkmcnt(0)\nv_smfmac_f32_16x16x64_bf16 v[20:23], v[44:47], v[228:235], v0\ns_nop 2\n" \
    "v_smfmac_f32_16x16x64_bf16 v[16:19], v[60:63], v[124:131], v0\ns_nop 2\nv_smfmac_f32_16x16x64_bf16 v[16:19], v[40:43], v[188:195], v0\nv_permlane32_swap_b32 v20, v22\n" \
    "v_smfmac_f32_16x16x64_bf16 v[16:19], v[44:47], v[220:227], v0\ns_nop 1\nv_permlane32_swap_b32 v21, v23\nv_smfmac_f32_16x16x64_bf16 v[24:27], v[32:35], a[56:63], v0\n" \
    "s_nop 1\nv_add_f32 v20, v20, v22\nv_smfmac_f32_16x16x64_bf16 v[28:31], v[32:35], a[64:71], v0\nv_add_f32 v21, v21, v23\n" \
    "v_smfmac_f32_16x16x64_bf16 v[24:27], v[36:39], a[80:87], v0\nv_mul_f32 v20, s32, v20\nv_smfmac_f32_16x16x64_bf16 v[28:31], v[36:39], a[88:95], v0\nv_mul_f32 v21, s32, v21\n" \
    "v_smfmac_f32_16x16x64_bf16 v[24:27], v[48:51], a[104:111], v0\nv_cvt_pk_bf16_f32 v20, v20, v21\nv_smfmac_f32_16x16x64_bf16 v[28:31], v[48:51], a[112:119], v0\n" \
    "ds_write_b16 v4, v20 offset:8\nv_permlane32_swap_b32 v16, v18\nv_smfmac_f32_16x16x64_bf16 v[24:27], v[52:55], a[152:159], v0\nds_write_b16_d16_hi v4, v20 offset:24\n" \
    "v_permlane32_swap_b32 v17, v19\nv_smfmac_f32_16x16x64_bf16 v[24:27], v[80:83], a[176:183], v0\nds_read_b128 v[48:51], v1 offset:40960\nv_add_f32 v16, v16, v18\n" \
    "v_smfmac_f32_16x16x64_bf16 v[24:27], v[64:67], a[200:207], v0\nds_read_b128 v[32:35], v1 offset:45056\nv_add_f32 v17, v17, v19\n" \
    "v_smfmac_f32_16x16x64_bf16 v[24:27], v[68:71], a[248:255], v0\nds_read_b128 v[36:39], v1 offset:41984\nv_mul_f32 v16, s32, v16\n" \
    "v_smfmac_f32_16x16x64_bf16 v[24:27], v[56:59], v[108:115], v0\nds_read_b128 v[72:75], v1 offset:46080\nv_mul_f32 v17, s32, v17\n" \
    "v_smfmac_f32_16x16x64_bf16 v[24:27], v[60:63], v[140:147], v0\nds_read_b128 v[76:79], v1 offset:43008\nv_cvt_pk_bf16_f32 v16, v16, v17\n" \
    "v_smfmac_f32_16x16x64_bf16 v[24:27], v[40:43], v[204:211], v0\nds_write_b16 v4, v16 offset:0\nv_smfmac_f32_16x16x64_bf16 v[24:27], v[44:47], v[236:243], v0\n" \
    "ds_write_b16_d16_hi v4, v16 offset:16\ns_nop 1\nv_smfmac_f32_16x16x64_bf16 v[28:31], v[52:55], a[160:167], v0\nds_read_b128 v[52:55], v1 offset:47104\ns_nop 1\n" \
    "v_smfmac_f32_16x16x64_bf16 v[28:31], v[80:83], a[184:191], v0\nds_read_b128 v[80:83], v1 offset:44032\nv_permlane32_swap_b32 v24, v26\n" \
    "v_smfmac_f32_16x16x64_bf16 v[28:31], v[64:67], a[208:215], v0\nds_read_b128 v[64:67], v1 offset:48128\nv_permlane32_swap_b32 v25, v27\n" \
    "v_smfmac_f32_16x16x64_bf16 v[28:31], v[68:71], v[92:99], v0\ns_waitcnt lgkmcnt(3)\ns_barrier\nds_read_b128 v[68:71], v6 offset:0\nv_add_f32 v24, v24, v26\n" \
    "v_smfmac_f32_16x16x64_bf16 v[28:31], v[56:59], v[116:123], v0\nds_read_b128 v[56:59], v6 offset:512\nv_add_f32 v25, v25, v27\n" \
    "v_smfmac_f32_16x16x64_bf16 v[28:31], v[60:63], v[148:155], v0\nds_read_b128 v[60:63], v6 offset:1024\nv_cvt_pk_bf16_f32 v24, v24, v25\n" \
    "v_smfmac_f32_16x16x64_bf16 v[28:31], v[40:43], v[212:219], v0\nds_read_b128 v[40:43], v6 offset:1536\nv_smfmac_f32_16x16x64_bf16 v[28:31], v[44:47], v[244:251], v0\n" \
    "ds_write_b16 v4, v24 offset:2048\nds_write_b16_d16_hi v4, v24 offset:2064\ns_waitcnt lgkmcnt(5)\nv_mfma_f32_16x16x32_bf16 v[16:19], v[48:51], v[68:71], 0\ns_nop 2\n" \
    "v_mfma_f32_16x16x32_bf16 v[20:23], v[32:35], v[68:71], 0\nv_permlane32_swap_b32 v28, v30\nv_permlane32_swap_b32 v29, v31\nv_add_f32 v28, v28, v30\nv_add_f32 v29, v29, v31\n" \
    "v_cvt_pk_bf16_f32 v28, v28, v29\nds_write_b16 v4, v28 offset:2056\nds_write_b16_d16_hi v4, v28 offset:2072\ns_waitcnt lgkmcnt(6)\n" \
    "v_mfma_f32_16x16x32_bf16 v[16:19], v[36:39], v[56:59], v[16:19]\nv_mfma_f32_16x16x32_bf16 v[20:23], v[72:75], v[56:59], v[20:23]\ns_waitcnt lgkmcnt(5)\n" \
    "v_mfma_f32_16x16x32_bf16 v[16:19], v[76:79], v[60:63], v[16:19]\nv_mfma_f32_16x16x32_bf16 v[20:23], v[52:55], v[60:63], v[20:23]\ns_waitcnt lgkmcnt(0)\ns_barrier\n" \
    "ds_read_b128 v[44:47], v6 offset:2048\nds_read_b128 v[84:87], v6 offset:2560\nv_mfma_f32_16x16x32_bf16 v[16:19], v[80:83], v[40:43], v[16:19]\n" \
    "ds_read_b128 v[88:91], v6 offset:3072\nds_read_b128 v[28:31], v6 offset:3584\nv_mfma_f32_16x16x32_bf16 v[20:23], v[64:67], v[40:43], v[20:23]\nv_mov_b32 v15, v15\n" \
    "v_mov_b32 v15, v15\nv_mov_b32 v15, v15\nv_mov_b32 v15, v15\nv_mov_b32 v15, v15\nv_mov_b32 v15, v15\ns_waitcnt lgkmcnt(3)\n" \
    "v_mfma_f32_16x16x32_bf16 v[24:27], v[44:47], v[68:71], 0\ns_waitcnt lgkmcnt(2)\nv_mov_b32_dpp v16, v20 quad_perm:[0,1,2,3] row_mask:0xf bank_mask:0xc\n" \
    "v_mov_b32_dpp v17, v21 quad_perm:[0,1,2,3] row_mask:0xf bank_mask:0xc\nv_mfma_f32_16x16x32_bf16 v[24:27], v[84:87], v[56:59], v[24:27]\ns_waitcnt lgkmcnt(1)\n" \
    "v_mov_b32_dpp v18, v22 quad_perm:[0,1,2,3] row_mask:0xf bank_mask:0xc\nv_mov_b32_dpp v19, v23 quad_perm:[0,1,2,3] row_mask:0xf bank_mask:0xc\n" \
    "v_mfma_f32_16x16x32_bf16 v[24:27], v[88:91], v[60:63], v[24:27]\ns_waitcnt lgkmcnt(0)\nv_max3_f32 v254, v16, v17, v18\nv_max_f32 v254, v254, v19\n" \
    "v_mfma_f32_16x16x32_bf16 v[24:27], v[28:31], v[40:43], v[24:27]\ns_nop 0\nv_max_f32_dpp v252, v254, v254 row_ror:8 row_mask:0xf bank_mask:0xf\n" \
    "v_max_f32_dpp v253, v254, v254 row_ror:8 row_mask:0xf bank_mask:0xf\ns_nop 1\nv_permlane32_swap_b32 v252, v253\nv_max_f32 v252, v252, v253\ns_xor_b32 s74, s82, 0x4000\n" \
    "v_max3_f32 v254, v252, v24, v25\nv_max3_f32 v252, v254, v26, v27\nv_max3_f32 v253, v254, v26, v27\ns_nop 1\nv_permlane16_swap_b32 v252, v253\nv_max_f32 v252, v252, v253\n" \
    "v_pk_add_f32 v[16:17], v[16:17], v[252:253] op_sel_hi:[1,0] neg_lo:[0,1] neg_hi:[0,1]\n" \
    "v_pk_add_f32 v[18:19], v[18:19], v[252:253] op_sel_hi:[1,0] neg_lo:[0,1] neg_hi:[0,1]\n" \
    "v_pk_add_f32 v[24:25], v[24:25], v[252:253] op_sel_hi:[1,0] neg_lo:[0,1] neg_hi:[0,1]\n" \
    "v_pk_add_f32 v[26:27], v[26:27], v[252:253] op_sel_hi:[1,0] neg_lo:[0,1] neg_hi:[0,1]\nv_exp_f32 v16, v16\nv_exp_f32 v17, v17\nv_exp_f32 v18, v18\nv_exp_f32 v19, v19\n" \
    "v_exp_f32 v24, v24\nv_pk_add_f32 v[254:255], v[16:17], v[18:19]\nv_exp_f32 v25, v25\nv_add_f32 v254, v254, v255\nv_exp_f32 v26, v26\nv_exp_f32 v27, v27\n" \
    "v_add_f32_dpp v252, v254, v254 row_ror:8 row_mask:0xf bank_mask:0xf\nv_add_f32_dpp v253, v254, v254 row_ror:8 row_mask:0xf bank_mask:0xf\n" \
    "v_pk_add_f32 v[254:255], v[24:25], v[26:27]\ns_nop 0\nv_permlane32_swap_b32 v252, v253\nv_pk_add_f32 v[254:255], v[254:255], v[252:253]\n" \
    "v_pk_add_f32 v[252:253], v[254:255], v[254:255] op_sel:[0,1] op_sel_hi:[1,0]\ns_nop 1\nv_permlane16_swap_b32 v252, v253\nv_add_f32 v252, v252, v253\nv_rcp_f32 v252, v252\n" \
    "s_nop 0\nv_pk_mul_f32 v[24:25], v[24:25], v[252:253] op_sel_hi:[1,0]\nv_pk_mul_f32 v[26:27], v[26:27], v[252:253] op_sel_hi:[1,0]\nv_cvt_pk_bf16_f32 v20, v24, v25\n" \
    "v_cvt_pk_bf16_f32 v21, v26, v27\nv_pk_mul_f32 v[16:17], v[16:17], v[252:253] op_sel_hi:[1,0]\nv_pk_mul_f32 v[18:19], v[18:19], v[252:253] op_sel_hi:[1,0]\n" \
    "v_permlane16_swap_b32 v20, v22\nv_cvt_pk_bf16_f32 v16, v16, v17\nv_cvt_pk_bf16_f32 v17, v18, v19\nv_permlane16_swap_b32 v21, v23\n" \
    "v_mov_b32_dpp v20, v16 quad_perm:[0,1,2,3] row_mask:0xf bank_mask:0x3\nv_mov_b32_dpp v22, v16 row_ror:8 row_mask:0xf bank_mask:0x3\n" \
    "v_mov_b32_dpp v21, v17 quad_perm:[0,1,2,3] row_mask:0xf bank_mask:0x3\nv_mov_b32_dpp v23, v17 row_ror:8 row_mask:0xf bank_mask:0x3\n" \
    "buffer_store_dwordx4 v[20:23], v7, s[64:67], s82 offen sc1\nbuffer_store_dwordx4 v[20:23], v7, s[64:67], s74 offen sc1\ns_branch .Lq3_%=\n.Lq6_%=:\n" \
    "ds_read_b128 v[32:35], v3 offset:4096\nds_read_b128 v[36:39], v3 offset:5120\nds_read_b128 v[40:43], v3 offset:12288\nds_read_b128 v[44:47], v3 offset:13312\n" \
    "ds_read_b128 v[48:51], v3 offset:20480\nds_read_b128 v[52:55], v3 offset:21504\nds_read_b128 v[56:59], v3 offset:0\nds_read_b128 v[60:63], v3 offset:1024\n" \
    "ds_read_b128 v[64:67], v3 offset:2048\nds_read_b128 v[68:71], v3 offset:3072\n.Lq27_%=:\ns_xor_b32 s90, s81, 0x800\n.Lq29_%=:\ns_memrealtime s[72:73]\n" \
    "s_waitcnt lgkmcnt(0)\ns_sub_u32 s72, s72, s80\ns_cmp_lt_i32 s72, 0\ns_cbranch_scc1 .Lq29_%=\nbuffer_load_dwordx4 v[72:75], v2, s[64:67], s81 offen offset:0 sc1\n" \
    "buffer_load_dwordx4 v[76:79], v2, s[64:67], s81 offen offset:1024 sc1\nbuffer_load_dwordx4 v[80:83], v2, s[64:67], s90 offen offset:0 sc1\n" \
    "buffer_load_dwordx4 v[84:87], v2, s[64:67], s90 offen offset:1024 sc1\nv_mov_b64 v[16:17], 0\nv_mov_b64 v[18:19], 0\nv_mov_b64 v[20:21], 0\nv_mov_b64 v[22:23], 0\n" \
    "v_mov_b64 v[24:25], 0\nv_mov_b64 v[26:27], 0\nv_mov_b64 v[28:29], 0\nv_mov_b64 v[30:31], 0\ns_waitcnt vmcnt(3)\nds_write_b128 v1, v[72:75] offset:26624\n" \
    "v_smfmac_f32_16x16x64_bf16 v[20:23], v[72:75], v[32:39], v0\nv_max3_u32 v252, v72, v73, v74\nv_smfmac_f32_16x16x64_bf16 v[16:19], v[72:75], a[48:55], v0\n" \
    "s_waitcnt vmcnt(2)\nds_write_b128 v1, v[76:79] offset:30720\nv_max3_u32 v252, v252, v75, v76\nv_smfmac_f32_16x16x64_bf16 v[24:27], v[72:75], a[56:63], v0\n" \
    "v_max3_u32 v253, v77, v78, v79\nv_smfmac_f32_16x16x64_bf16 v[28:31], v[72:75], a[64:71], v0\ns_waitcnt vmcnt(1)\nds_write_b128 v1, v[80:83] offset:34816\n" \
    "v_max3_u32 v252, v252, v80, v81\nv_smfmac_f32_16x16x64_bf16 v[20:23], v[76:79], v[40:47], v0\nv_max3_u32 v253, v253, v82, v83\n" \
    "v_smfmac_f32_16x16x64_bf16 v[16:19], v[76:79], a[144:151], v0\ns_waitcnt vmcnt(0)\nds_write_b128 v1, v[84:87] offset:38912\nv_max3_u32 v252, v252, v84, v85\n" \
    "v_smfmac_f32_16x16x64_bf16 v[24:27], v[76:79], a[152:159], v0\nv_max3_u32 v253, v253, v86, v87\nv_smfmac_f32_16x16x64_bf16 v[28:31], v[76:79], a[160:167], v0\n" \
    "v_max_u32 v252, v252, v253\nv_smfmac_f32_16x16x64_bf16 v[20:23], v[80:83], v[48:55], v0\nv_cmp_eq_u32 vcc, -1, v252\n" \
    "v_smfmac_f32_16x16x64_bf16 v[16:19], v[80:83], a[240:247], v0\ns_cbranch_vccnz .Lq28_%=\ns_waitcnt lgkmcnt(0)\ns_barrier\nds_read_b128 v[88:91], v1 offset:24576\n" \
    "ds_read_b128 v[32:35], v1 offset:25600\nds_read_b128 v[36:39], v1 offset:27648\nv_smfmac_f32_16x16x64_bf16 v[24:27], v[80:83], a[248:255], v0\n" \
    "ds_read_b128 v[40:43], v3 offset:6144\nv_smfmac_f32_16x16x64_bf16 v[28:31], v[80:83], v[92:99], v0\nds_read_b128 v[44:47], v3 offset:7168\n" \
    "v_smfmac_f32_16x16x64_bf16 v[20:23], v[84:87], v[196:203], v0\nds_read_b128 v[48:51], v1 offset:28672\nv_smfmac_f32_16x16x64_bf16 v[16:19], v[84:87], v[188:195], v0\n" \
    "ds_read_b128 v[72:75], v3 offset:8192\nv_smfmac_f32_16x16x64_bf16 v[24:27], v[84:87], v[204:211], v0\nds_read_b128 v[76:79], v3 offset:9216\n" \
    "v_smfmac_f32_16x16x64_bf16 v[28:31], v[84:87], v[212:219], v0\nds_read_b128 v[52:55], v1 offset:29696\ns_waitcnt lgkmcnt(8)\n" \
    "v_smfmac_f32_16x16x64_bf16 v[20:23], v[88:91], v[56:63], v0\nds_read_b128 v[56:59], v3 offset:10240\nv_smfmac_f32_16x16x64_bf16 v[16:19], v[88:91], a[0:7], v0\n" \
    "ds_read_b128 v[60:63], v3 offset:11264\nv_smfmac_f32_16x16x64_bf16 v[24:27], v[88:91], a[8:15], v0\nds_read_b128 v[80:83], v1 offset:31744\n" \
    "v_smfmac_f32_16x16x64_bf16 v[28:31], v[88:91], a[16:23], v0\nds_read_b128 v[84:87], v3 offset:14336\ns_waitcnt lgkmcnt(11)\n" \
    "v_smfmac_f32_16x16x64_bf16 v[20:23], v[32:35], v[64:71], v0\nds_read_b128 v[88:91], v3 offset:15360\nv_smfmac_f32_16x16x64_bf16 v[16:19], v[32:35], a[24:31], v0\n" \
    "ds_read_b128 v[64:67], v1 offset:32768\ns_waitcnt lgkmcnt(10)\nv_smfmac_f32_16x16x64_bf16 v[20:23], v[36:39], v[40:47], v0\nds_read_b128 v[40:43], v3 offset:16384\n" \
    "v_smfmac_f32_16x16x64_bf16 v[16:19], v[36:39], a[72:79], v0\nds_read_b128 v[44:47], v3 offset:17408\ns_waitcnt lgkmcnt(9)\n" \
    "v_smfmac_f32_16x16x64_bf16 v[20:23], v[48:51], v[72:79], v0\nds_read_b128 v[68:71], v1 offset:33792\nv_smfmac_f32_16x16x64_bf16 v[16:19], v[48:51], a[96:103], v0\n" \
    "ds_read_b128 v[72:75], v3 offset:18432\ns_waitcnt lgkmcnt(8)\nv_smfmac_f32_16x16x64_bf16 v[20:23], v[52:55], v[56:63], v0\nds_read_b128 v[76:79], v3 offset:19456\n" \
    "v_smfmac_f32_16x16x64_bf16 v[16:19], v[52:55], a[120:127], v0\nds_read_b128 v[56:59], v1 offset:35840\ns_waitcnt lgkmcnt(7)\n" \
    "v_smfmac_f32_16x16x64_bf16 v[20:23], v[80:83], v[84:91], v0\nds_read_b128 v[84:87], v3 offset:22528\nv_smfmac_f32_16x16x64_bf16 v[16:19], v[80:83], a[168:175], v0\n" \
    "ds_read_b128 v[88:91], v3 offset:23552\ns_waitcnt lgkmcnt(6)\nv_smfmac_f32_16x16x64_bf16 v[20:23], v[64:67], v[40:47], v0\n" \
    "v_smfmac_f32_16x16x64_bf16 v[16:19], v[64:67], a[192:199], v0\nds_read_b128 v[60:63], v1 offset:36864\ns_waitcnt lgkmcnt(4)\n" \
    "v_smfmac_f32_16x16x64_bf16 v[20:23], v[68:71], v[72:79], v0\nds_read_b128 v[40:43], v1 offset:37888\nv_smfmac_f32_16x16x64_bf16 v[16:19], v[68:71], a[216:223], v0\n" \
    "ds_read_b128 v[44:47], v1 offset:39936\ns_waitcnt lgkmcnt(3)\nv_smfmac_f32_16x16x64_bf16 v[20:23], v[56:59], v[84:91], v0\n" \
    "v_smfmac_f32_16x16x64_bf16 v[16:19], v[56:59], v[100:107], v0\ns_waitcnt lgkmcnt(2)\nv_smfmac_f32_16x16x64_bf16 v[20:23], v[60:63], v[132:139], v0\ns_waitcnt lgkmcnt(1)\n" \
    "v_smfmac_f32_16x16x64_bf16 v[20:23], v[40:43], v[164:171], v0\ns_waitcnt lgkmcnt(0)\nv_smfmac_f32_16x16x64_bf16 v[20:23], v[44:47], v[228:235], v0\ns_nop 2\n" \
    "v_smfmac_f32_16x16x64_bf16 v[16:19], v[60:63], v[124:131], v0\ns_nop 2\nv_smfmac_f32_16x16x64_bf16 v[16:19], v[40:43], v[156:163], v0\nv_permlane32_swap_b32 v20, v22\n" \
    "v_smfmac_f32_16x16x64_bf16 v[16:19], v[44:47], v[220:227], v0\ns_nop 1\nv_permlane32_swap_b32 v21, v23\nv_smfmac_f32_16x16x64_bf16 v[24:27], v[32:35], a[32:39], v0\n" \
    "s_nop 1\nv_add_f32 v20, v20, v22\nv_smfmac_f32_16x16x64_bf16 v[28:31], v[32:35], a[40:47], v0\nv_add_f32 v21, v21, v23\n" \
    "v_smfmac_f32_16x16x64_bf16 v[24:27], v[36:39], a[80:87], v0\nv_mul_f32 v20, s32, v20\nv_smfmac_f32_16x16x64_bf16 v[28:31], v[36:39], a[88:95], v0\nv_mul_f32 v21, s32, v21\n" \
    "v_smfmac_f32_16x16x64_bf16 v[24:27], v[48:51], a[104:111], v0\nv_cvt_pk_bf16_f32 v20, v20, v21\nv_smfmac_f32_16x16x64_bf16 v[28:31], v[48:51], a[112:119], v0\n" \
    "ds_write_b16 v4, v20 offset:8\nv_permlane32_swap_b32 v16, v18\nv_smfmac_f32_16x16x64_bf16 v[24:27], v[52:55], a[128:135], v0\nds_write_b16_d16_hi v4, v20 offset:24\n" \
    "v_permlane32_swap_b32 v17, v19\nv_smfmac_f32_16x16x64_bf16 v[24:27], v[80:83], a[176:183], v0\nds_read_b128 v[48:51], v1 offset:40960\nv_add_f32 v16, v16, v18\n" \
    "v_smfmac_f32_16x16x64_bf16 v[24:27], v[64:67], a[200:207], v0\nds_read_b128 v[32:35], v1 offset:45056\nv_add_f32 v17, v17, v19\n" \
    "v_smfmac_f32_16x16x64_bf16 v[24:27], v[68:71], a[224:231], v0\nds_read_b128 v[36:39], v1 offset:41984\nv_mul_f32 v16, s32, v16\n" \
    "v_smfmac_f32_16x16x64_bf16 v[24:27], v[56:59], v[108:115], v0\nds_read_b128 v[72:75], v1 offset:46080\nv_mul_f32 v17, s32, v17\n" \
    "v_smfmac_f32_16x16x64_bf16 v[24:27], v[60:63], v[140:147], v0\nds_read_b128 v[76:79], v1 offset:43008\nv_cvt_pk_bf16_f32 v16, v16, v17\n" \
    "v_smfmac_f32_16x16x64_bf16 v[24:27], v[40:43], v[172:179], v0\nds_write_b16 v4, v16 offset:0\nv_smfmac_f32_16x16x64_bf16 v[24:27], v[44:47], v[236:243], v0\n" \
    "ds_write_b16_d16_hi v4, v16 offset:16\ns_nop 1\nv_smfmac_f32_16x16x64_bf16 v[28:31], v[52:55], a[136:143], v0\nds_read_b128 v[52:55], v1 offset:47104\ns_nop 1\n" \
    "v_smfmac_f32_16x16x64_bf16 v[28:31], v[80:83], a[184:191], v0\nds_read_b128 v[80:83], v1 offset:44032\nv_permlane32_swap_b32 v24, v26\n" \
    "v_smfmac_f32_16x16x64_bf16 v[28:31], v[64:67], a[208:215], v0\nds_read_b128 v[64:67], v1 offset:48128\nv_permlane32_swap_b32 v25, v27\n" \
    "v_smfmac_f32_16x16x64_bf16 v[28:31], v[68:71], a[232:239], v0\ns_waitcnt lgkmcnt(3)\ns_barrier\nds_read_b128 v[68:71], v6 offset:0\nv_add_f32 v24, v24, v26\n" \
    "v_smfmac_f32_16x16x64_bf16 v[28:31], v[56:59], v[116:123], v0\nds_read_b128 v[56:59], v6 offset:512\nv_add_f32 v25, v25, v27\n" \
    "v_smfmac_f32_16x16x64_bf16 v[28:31], v[60:63], v[148:155], v0\nds_read_b128 v[60:63], v6 offset:1024\nv_cvt_pk_bf16_f32 v24, v24, v25\n" \
    "v_smfmac_f32_16x16x64_bf16 v[28:31], v[40:43], v[180:187], v0\nds_read_b128 v[40:43], v6 offset:1536\nv_smfmac_f32_16x16x64_bf16 v[28:31], v[44:47], v[244:251], v0\n" \
    "ds_write_b16 v4, v24 offset:2048\nds_write_b16_d16_hi v4, v24 offset:2064\ns_waitcnt lgkmcnt(5)\nv_mfma_f32_16x16x32_bf16 v[16:19], v[48:51], v[68:71], 0\ns_nop 2\n" \
    "v_mfma_f32_16x16x32_bf16 v[20:23], v[32:35], v[68:71], 0\nv_permlane32_swap_b32 v28, v30\nv_permlane32_swap_b32 v29, v31\nv_add_f32 v28, v28, v30\nv_add_f32 v29, v29, v31\n" \
    "v_cvt_pk_bf16_f32 v28, v28, v29\nds_write_b16 v4, v28 offset:2056\nds_write_b16_d16_hi v4, v28 offset:2072\ns_waitcnt lgkmcnt(6)\n" \
    "v_mfma_f32_16x16x32_bf16 v[16:19], v[36:39], v[56:59], v[16:19]\nv_mfma_f32_16x16x32_bf16 v[20:23], v[72:75], v[56:59], v[20:23]\ns_waitcnt lgkmcnt(5)\n" \
    "v_mfma_f32_16x16x32_bf16 v[16:19], v[76:79], v[60:63], v[16:19]\nv_mfma_f32_16x16x32_bf16 v[20:23], v[52:55], v[60:63], v[20:23]\ns_waitcnt lgkmcnt(0)\ns_barrier\n" \
    "ds_read_b128 v[44:47], v6 offset:2048\nds_read_b128 v[84:87], v6 offset:2560\nv_mfma_f32_16x16x32_bf16 v[16:19], v[80:83], v[40:43], v[16:19]\n" \
    "ds_read_b128 v[88:91], v6 offset:3072\nds_read_b128 v[28:31], v6 offset:3584\nv_mfma_f32_16x16x32_bf16 v[20:23], v[64:67], v[40:43], v[20:23]\nv_mov_b32 v15, v15\n" \
    "v_mov_b32 v15, v15\nv_mov_b32 v15, v15\nv_mov_b32 v15, v15\nv_mov_b32 v15, v15\nv_mov_b32 v15, v15\ns_waitcnt lgkmcnt(3)\n" \
    "v_mfma_f32_16x16x32_bf16 v[24:27], v[44:47], v[68:71], 0\ns_waitcnt lgkmcnt(2)\nv_mov_b32_dpp v16, v20 quad_perm:[0,1,2,3] row_mask:0xf bank_mask:0xc\n" \
    "v_mov_b32_dpp v17, v21 quad_perm:[0,1,2,3] row_mask:0xf bank_mask:0xc\nv_mfma_f32_16x16x32_bf16 v[24:27], v[84:87], v[56:59], v[24:27]\ns_waitcnt lgkmcnt(1)\n" \
    "v_mov_b32_dpp v18, v22 quad_perm:[0,1,2,3] row_mask:0xf bank_mask:0xc\nv_mov_b32_dpp v19, v23 quad_perm:[0,1,2,3] row_mask:0xf bank_mask:0xc\n" \
    "v_mfma_f32_16x16x32_bf16 v[24:27], v[88:91], v[60:63], v[24:27]\ns_waitcnt lgkmcnt(0)\nv_max3_f32 v254, v16, v17, v18\nv_max_f32 v254, v254, v19\n" \
    "v_mfma_f32_16x16x32_bf16 v[24:27], v[28:31], v[40:43], v[24:27]\ns_nop 0\nv_max_f32_dpp v252, v254, v254 row_ror:8 row_mask:0xf bank_mask:0xf\n" \
    "v_max_f32_dpp v253, v254, v254 row_ror:8 row_mask:0xf bank_mask:0xf\ns_nop 1\nv_permlane32_swap_b32 v252, v253\nv_max_f32 v252, v252, v253\ns_xor_b32 s74, s82, 0x4000\n" \
    "v_max3_f32 v254, v252, v24, v25\nv_max3_f32 v252, v254, v26, v27\nv_max3_f32 v253, v254, v26, v27\ns_nop 1\nv_permlane16_swap_b32 v252, v253\nv_max_f32 v252, v252, v253\n" \
    "v_pk_add_f32 v[16:17], v[16:17], v[252:253] op_sel_hi:[1,0] neg_lo:[0,1] neg_hi:[0,1]\n" \
    "v_pk_add_f32 v[18:19], v[18:19], v[252:253] op_sel_hi:[1,0] neg_lo:[0,1] neg_hi:[0,1]\n" \
    "v_pk_add_f32 v[24:25], v[24:25], v[252:253] op_sel_hi:[1,0] neg_lo:[0,1] neg_hi:[0,1]\n" \
    "v_pk_add_f32 v[26:27], v[26:27], v[252:253] op_sel_hi:[1,0] neg_lo:[0,1] neg_hi:[0,1]\nv_exp_f32 v16, v16\nv_exp_f32 v17, v17\nv_exp_f32 v18, v18\nv_exp_f32 v19, v19\n" \
    "v_exp_f32 v24, v24\nv_pk_add_f32 v[254:255], v[16:17], v[18:19]\nv_exp_f32 v25, v25\nv_add_f32 v254, v254, v255\nv_exp_f32 v26, v26\nv_exp_f32 v27, v27\n" \
    "v_add_f32_dpp v252, v254, v254 row_ror:8 row_mask:0xf bank_mask:0xf\nv_add_f32_dpp v253, v254, v254 row_ror:8 row_mask:0xf bank_mask:0xf\n" \
    "v_pk_add_f32 v[254:255], v[24:25], v[26:27]\ns_nop 0\nv_permlane32_swap_b32 v252, v253\nv_pk_add_f32 v[254:255], v[254:255], v[252:253]\n" \
    "v_pk_add_f32 v[252:253], v[254:255], v[254:255] op_sel:[0,1] op_sel_hi:[1,0]\ns_nop 1\nv_permlane16_swap_b32 v252, v253\nv_add_f32 v252, v252, v253\nv_rcp_f32 v252, v252\n" \
    "s_nop 0\nv_pk_mul_f32 v[24:25], v[24:25], v[252:253] op_sel_hi:[1,0]\nv_pk_mul_f32 v[26:27], v[26:27], v[252:253] op_sel_hi:[1,0]\nv_cvt_pk_bf16_f32 v20, v24, v25\n" \
    "v_cvt_pk_bf16_f32 v21, v26, v27\nv_pk_mul_f32 v[16:17], v[16:17], v[252:253] op_sel_hi:[1,0]\nv_pk_mul_f32 v[18:19], v[18:19], v[252:253] op_sel_hi:[1,0]\n" \
    "v_permlane16_swap_b32 v20, v22\nv_cvt_pk_bf16_f32 v16, v16, v17\nv_cvt_pk_bf16_f32 v17, v18, v19\nv_permlane16_swap_b32 v21, v23\n" \
    "v_mov_b32_dpp v20, v16 quad_perm:[0,1,2,3] row_mask:0xf bank_mask:0x3\nv_mov_b32_dpp v22, v16 row_ror:8 row_mask:0xf bank_mask:0x3\n" \
    "v_mov_b32_dpp v21, v17 quad_perm:[0,1,2,3] row_mask:0xf bank_mask:0x3\nv_mov_b32_dpp v23, v17 row_ror:8 row_mask:0xf bank_mask:0x3\n" \
    "buffer_store_dwordx4 v[20:23], v7, s[64:67], s82 offen sc1\nbuffer_store_dwordx4 v[20:23], v7, s[64:67], s74 offen sc1\ns_branch .Lq3_%=\n.Lq7_%=:\n" \
    "ds_read_b128 v[32:35], v3 offset:6144\nds_read_b128 v[36:39], v3 offset:7168\nds_read_b128 v[40:43], v3 offset:14336\nds_read_b128 v[44:47], v3 offset:15360\n" \
    "ds_read_b128 v[48:51], v3 offset:22528\nds_read_b128 v[52:55], v3 offset:23552\nds_read_b128 v[56:59], v3 offset:0\nds_read_b128 v[60:63], v3 offset:1024\n" \
    "ds_read_b128 v[64:67], v3 offset:2048\nds_read_b128 v[68:71], v3 offset:3072\n.Lq31_%=:\ns_xor_b32 s90, s81, 0x800\n.Lq33_%=:\ns_memrealtime s[72:73]\n" \
    "s_waitcnt lgkmcnt(0)\ns_sub_u32 s72, s72, s80\ns_cmp_lt_i32 s72, 0\ns_cbranch_scc1 .Lq33_%=\nbuffer_load_dwordx4 v[72:75], v2, s[64:67], s81 offen offset:0 sc1\n" \
    "buffer_load_dwordx4 v[76:79], v2, s[64:67], s81 offen offset:1024 sc1\nbuffer_load_dwordx4 v[80:83], v2, s[64:67], s90 offen offset:0 sc1\n" \
    "buffer_load_dwordx4 v[84:87], v2, s[64:67], s90 offen offset:1024 sc1\nv_mov_b64 v[16:17], 0\nv_mov_b64 v[18:19], 0\nv_mov_b64 v[20:21], 0\nv_mov_b64 v[22:23], 0\n" \
    "v_mov_b64 v[24:25], 0\nv_mov_b64 v[26:27], 0\nv_mov_b64 v[28:29], 0\nv_mov_b64 v[30:31], 0\ns_waitcnt vmcnt(3)\nds_write_b128 v1, v[72:75] offset:27648\n" \
    "v_smfmac_f32_16x16x64_bf16 v[20:23], v[72:75], v[32:39], v0\nv_max3_u32 v252, v72, v73, v74\nv_smfmac_f32_16x16x64_bf16 v[16:19], v[72:75], a[72:79], v0\n" \
    "s_waitcnt vmcnt(2)\nds_write_b128 v1, v[76:79] offset:31744\nv_max3_u32 v252, v252, v75, v76\nv_smfmac_f32_16x16x64_bf16 v[24:27], v[72:75], a[80:87], v0\n" \
    "v_max3_u32 v253, v77, v78, v79\nv_smfmac_f32_16x16x64_bf16 v[28:31], v[72:75], a[88:95], v0\ns_waitcnt vmcnt(1)\nds_write_b128 v1, v[80:83] offset:35840\n" \
    "v_max3_u32 v252, v252, v80, v81\nv_smfmac_f32_16x16x64_bf16 v[20:23], v[76:79], v[40:47], v0\nv_max3_u32 v253, v253, v82, v83\n" \
    "v_smfmac_f32_16x16x64_bf16 v[16:19], v[76:79], a[168:175], v0\ns_waitcnt vmcnt(0)\nds_write_b128 v1, v[84:87] offset:39936\nv_max3_u32 v252, v252, v84, v85\n" \
    "v_smfmac_f32_16x16x64_bf16 v[24:27], v[76:79], a[176:183], v0\nv_max3_u32 v253, v253, v86, v87\nv_smfmac_f32_16x16x64_bf16 v[28:31], v[76:79], a[184:191], v0\n" \
    "v_max_u32 v252, v252, v253\nv_smfmac_f32_16x16x64_bf16 v[20:23], v[80:83], v[48:55], v0\nv_cmp_eq_u32 vcc, -1, v252\n" \
    "v_smfmac_f32_16x16x64_bf16 v[16:19], v[80:83], v[100:107], v0\ns_cbranch_vccnz .Lq32_%=\ns_waitcnt lgkmcnt(0)\ns_barrier\nds_read_b128 v[88:91], v1 offset:24576\n" \
    "ds_read_b128 v[32:35], v1 offset:25600\nds_read_b128 v[36:39], v1 offset:26624\nv_smfmac_f32_16x16x64_bf16 v[24:27], v[80:83], v[108:115], v0\n" \
    "ds_read_b128 v[40:43], v3 offset:4096\nv_smfmac_f32_16x16x64_bf16 v[28:31], v[80:83], v[116:123], v0\nds_read_b128 v[44:47], v3 offset:5120\n" \
    "v_smfmac_f32_16x16x64_bf16 v[20:23], v[84:87], v[228:235], v0\nds_read_b128 v[48:51], v1 offset:28672\nv_smfmac_f32_16x16x64_bf16 v[16:19], v[84:87], v[220:227], v0\n" \
    "ds_read_b128 v[72:75], v3 offset:8192\nv_smfmac_f32_16x16x64_bf16 v[24:27], v[84:87], v[236:243], v0\nds_read_b128 v[76:79], v3 offset:9216\n" \
    "v_smfmac_f32_16x16x64_bf16 v[28:31], v[84:87], v[244:251], v0\nds_read_b128 v[52:55], v1 offset:29696\ns_waitcnt lgkmcnt(8)\n" \
    "v_smfmac_f32_16x16x64_bf16 v[20:23], v[88:91], v[56:63], v0\nds_read_b128 v[56:59], v3 offset:10240\nv_smfmac_f32_16x16x64_bf16 v[16:19], v[88:91], a[0:7], v0\n" \
    "ds_read_b128 v[60:63], v3 offset:11264\nv_smfmac_f32_16x16x64_bf16 v[24:27], v[88:91], a[8:15], v0\nds_read_b128 v[80:83], v1 offset:30720\n" \
    "v_smfmac_f32_16x16x64_bf16 v[28:31], v[88:91], a[16:23], v0\nds_read_b128 v[84:87], v3 offset:12288\ns_waitcnt lgkmcnt(11)\n" \
    "v_smfmac_f32_16x16x64_bf16 v[20:23], v[32:35], v[64:71], v0\nds_read_b128 v[88:91], v3 offset:13312\nv_smfmac_f32_16x16x64_bf16 v[16:19], v[32:35], a[24:31], v0\n" \
    "ds_read_b128 v[64:67], v1 offset:32768\ns_waitcnt lgkmcnt(10)\nv_smfmac_f32_16x16x64_bf16 v[20:23], v[36:39], v[40:47], v0\nds_read_b128 v[40:43], v3 offset:16384\n" \
    "v_smfmac_f32_16x16x64_bf16 v[16:19], v[36:39], a[48:55], v0\nds_read_b128 v[44:47], v3 offset:17408\ns_waitcnt lgkmcnt(9)\n" \
    "v_smfmac_f32_16x16x64_bf16 v[20:23], v[48:51], v[72:79], v0\nds_read_b128 v[68:71], v1 offset:33792\nv_smfmac_f32_16x16x64_bf16 v[16:19], v[48:51], a[96:103], v0\n" \
    "ds_read_b128 v[72:75], v3 offset:18432\ns_waitcnt lgkmcnt(8)\nv_smfmac_f32_16x16x64_bf16 v[20:23], v[52:55], v[56:63], v0\nds_read_b128 v[76:79], v3 offset:19456\n" \
    "v_smfmac_f32_16x16x64_bf16 v[16:19], v[52:55], a[120:127], v0\nds_read_b128 v[56:59], v1 offset:34816\ns_waitcnt lgkmcnt(7)\n" \
    "v_smfmac_f32_16x16x64_bf16 v[20:23], v[80:83], v[84:91], v0\nds_read_b128 v[84:87], v3 offset:20480\nv_smfmac_f32_16x16x64_bf16 v[16:19], v[80:83], a[144:151], v0\n" \
    "ds_read_b128 v[88:91], v3 offset:21504\ns_waitcnt lgkmcnt(6)\nv_smfmac_f32_16x16x64_bf16 v[20:23], v[64:67], v[40:47], v0\n" \
    "v_smfmac_f32_16x16x64_bf16 v[16:19], v[64:67], a[192:199], v0\nds_read_b128 v[60:63], v1 offset:36864\ns_waitcnt lgkmcnt(4)\n" \
    "v_smfmac_f32_16x16x64_bf16 v[20:23], v[68:71], v[72:79], v0\nds_read_b128 v[40:43], v1 offset:37888\nv_smfmac_f32_16x16x64_bf16 v[16:19], v[68:71], a[216:223], v0\n" \
    "ds_read_b128 v[44:47], v1 offset:38912\ns_waitcnt lgkmcnt(3)\nv_smfmac_f32_16x16x64_bf16 v[20:23], v[56:59], v[84:91], v0\n" \
    "v_smfmac_f32_16x16x64_bf16 v[16:19], v[56:59], a[240:247], v0\ns_waitcnt lgkmcnt(2)\nv_smfmac_f32_16x16x64_bf16 v[20:23], v[60:63], v[132:139], v0\ns_waitcnt lgkmcnt(1)\n" \
    "v_smfmac_f32_16x16x64_bf16 v[20:23], v[40:43], v[164:171], v0\ns_waitcnt lgkmcnt(0)\nv_smfmac_f32_16x16x64_bf16 v[20:23], v[44:47], v[196:203], v0\ns_nop 2\n" \
    "v_smfmac_f32_16x16x64_bf16 v[16:19], v[60:63], v[124:131], v0\ns_nop 2\nv_smfmac_f32_16x16x64_bf16 v[16:19], v[40:43], v[156:163], v0\nv_permlane32_swap_b32 v20, v22\n" \
    "v_smfmac_f32_16x16x64_bf16 v[16:19], v[44:47], v[188:195], v0\ns_nop 1\nv_permlane32_swap_b32 v21, v23\nv_smfmac_f32_16x16x64_bf16 v[24:27], v[32:35], a[32:39], v0\n" \
    "s_nop 1\nv_add_f32 v20, v20, v22\nv_smfmac_f32_16x16x64_bf16 v[28:31], v[32:35], a[40:47], v0\nv_add_f32 v21, v21, v23\n" \
    "v_smfmac_f32_16x16x64_bf16 v[24:27], v[36:39], a[56:63], v0\nv_mul_f32 v20, s32, v20\nv_smfmac_f32_16x16x64_bf16 v[28:31], v[36:39], a[64:71], v0\nv_mul_f32 v21, s32, v21\n" \
    "v_smfmac_f32_16x16x64_bf16 v[24:27], v[48:51], a[104:111], v0\nv_cvt_pk_bf16_f32 v20, v20, v21\nv_smfmac_f32_16x16x64_bf16 v[28:31], v[48:51], a[112:119], v0\n" \
    "ds_write_b16 v4, v20 offset:8\nv_permlane32_swap_b32 v16, v18\nv_smfmac_f32_16x16x64_bf16 v[24:27], v[52:55], a[128:135], v0\nds_write_b16_d16_hi v4, v20 offset:24\n" \
    "v_permlane32_swap_b32 v17, v19\nv_smfmac_f32_16x16x64_bf16 v[24:27], v[80:83], a[152:159], v0\nds_read_b128 v[48:51], v1 offset:40960\nv_add_f32 v16, v16, v18\n" \
    "v_smfmac_f32_16x16x64_bf16 v[24:27], v[64:67], a[200:207], v0\nds_read_b128 v[32:35], v1 offset:45056\nv_add_f32 v17, v17, v19\n" \
    "v_smfmac_f32_16x16x64_bf16 v[24:27], v[68:71], a[224:231], v0\nds_read_b128 v[36:39], v1 offset:41984\nv_mul_f32 v16, s32, v16\n" \
    "v_smfmac_f32_16x16x64_bf16 v[24:27], v[56:59], a[248:255], v0\nds_read_b128 v[72:75], v1 offset:46080\nv_mul_f32 v17, s32, v17\n" \
    "v_smfmac_f32_16x16x64_bf16 v[24:27], v[60:63], v[140:147], v0\nds_read_b128 v[76:79], v1 offset:43008\nv_cvt_pk_bf16_f32 v16, v16, v17\n" \
    "v_smfmac_f32_16x16x64_bf16 v[24:27], v[40:43], v[172:179], v0\nds_write_b16 v4, v16 offset:0\nv_smfmac_f32_16x16x64_bf16 v[24:27], v[44:47], v[204:211], v0\n" \
    "ds_write_b16_d16_hi v4, v16 offset:16\ns_nop 1\nv_smfmac_f32_16x16x64_bf16 v[28:31], v[52:55], a[136:143], v0\nds_read_b128 v[52:55], v1 offset:47104\ns_nop 1\n" \
    "v_smfmac_f32_16x16x64_bf16 v[28:31], v[80:83], a[160:167], v0\nds_read_b128 v[80:83], v1 offset:44032\nv_permlane32_swap_b32 v24, v26\n" \
    "v_smfmac_f32_16x16x64_bf16 v[28:31], v[64:67], a[208:215], v0\nds_read_b128 v[64:67], v1 offset:48128\nv_permlane32_swap_b32 v25, v27\n" \
    "v_smfmac_f32_16x16x64_bf16 v[28:31], v[68:71], a[232:239], v0\ns_waitcnt lgkmcnt(3)\ns_barrier\nds_read_b128 v[68:71], v6 offset:0\nv_add_f32 v24, v24, v26\n" \
    "v_smfmac_f32_16x16x64_bf16 v[28:31], v[56:59], v[92:99], v0\nds_read_b128 v[56:59], v6 offset:512\nv_add_f32 v25, v25, v27\n" \
    "v_smfmac_f32_16x16x64_bf16 v[28:31], v[60:63], v[148:155], v0\nds_read_b128 v[60:63], v6 offset:1024\nv_cvt_pk_bf16_f32 v24, v24, v25\n" \
    "v_smfmac_f32_16x16x64_bf16 v[28:31], v[40:43], v[180:187], v0\nds_read_b128 v[40:43], v6 offset:1536\nv_smfmac_f32_16x16x64_bf16 v[28:31], v[44:47], v[212:219], v0\n" \
    "ds_write_b16 v4, v24 offset:2048\nds_write_b16_d16_hi v4, v24 offset:2064\ns_waitcnt lgkmcnt(5)\nv_mfma_f32_16x16x32_bf16 v[16:19], v[48:51], v[68:71], 0\ns_nop 2\n" \
    "v_mfma_f32_16x16x32_bf16 v[20:23], v[32:35], v[68:71], 0\nv_permlane32_swap_b32 v28, v30\nv_permlane32_swap_b32 v29, v31\nv_add_f32 v28, v28, v30\nv_add_f32 v29, v29, v31\n" \
    "v_cvt_pk_bf16_f32 v28, v28, v29\nds_write_b16 v4, v28 offset:2056\nds_write_b16_d16_hi v4, v28 offset:2072\ns_waitcnt lgkmcnt(6)\n" \
    "v_mfma_f32_16x16x32_bf16 v[16:19], v[36:39], v[56:59], v[16:19]\nv_mfma_f32_16x16x32_bf16 v[20:23], v[72:75], v[56:59], v[20:23]\ns_waitcnt lgkmcnt(5)\n" \
    "v_mfma_f32_16x16x32_bf16 v[16:19], v[76:79], v[60:63], v[16:19]\nv_mfma_f32_16x16x32_bf16 v[20:23], v[52:55], v[60:63], v[20:23]\ns_waitcnt lgkmcnt(0)\ns_barrier\n" \
    "ds_read_b128 v[44:47], v6 offset:2048\nds_read_b128 v[84:87], v6 offset:2560\nv_mfma_f32_16x16x32_bf16 v[16:19], v[80:83], v[40:43], v[16:19]\n" \
    "ds_read_b128 v[88:91], v6 offset:3072\nds_read_b128 v[28:31], v6 offset:3584\nv_mfma_f32_16x16x32_bf16 v[20:23], v[64:67], v[40:43], v[20:23]\nv_mov_b32 v15, v15\n" \
    "v_mov_b32 v15, v15\nv_mov_b32 v15, v15\nv_mov_b32 v15, v15\nv_mov_b32 v15, v15\nv_mov_b32 v15, v15\ns_waitcnt lgkmcnt(3)\n" \
    "v_mfma_f32_16x16x32_bf16 v[24:27], v[44:47], v[68:71], 0\ns_waitcnt lgkmcnt(2)\nv_mov_b32_dpp v16, v20 quad_perm:[0,1,2,3] row_mask:0xf bank_mask:0xc\n" \
    "v_mov_b32_dpp v17, v21 quad_perm:[0,1,2,3] row_mask:0xf bank_mask:0xc\nv_mfma_f32_16x16x32_bf16 v[24:27], v[84:87], v[56:59], v[24:27]\ns_waitcnt lgkmcnt(1)\n" \
    "v_mov_b32_dpp v18, v22 quad_perm:[0,1,2,3] row_mask:0xf bank_mask:0xc\nv_mov_b32_dpp v19, v23 quad_perm:[0,1,2,3] row_mask:0xf bank_mask:0xc\n" \
    "v_mfma_f32_16x16x32_bf16 v[24:27], v[88:91], v[60:63], v[24:27]\ns_waitcnt lgkmcnt(0)\nv_max3_f32 v254, v16, v17, v18\nv_max_f32 v254, v254, v19\n" \
    "v_mfma_f32_16x16x32_bf16 v[24:27], v[28:31], v[40:43], v[24:27]\ns_nop 0\nv_max_f32_dpp v252, v254, v254 row_ror:8 row_mask:0xf bank_mask:0xf\n" \
    "v_max_f32_dpp v253, v254, v254 row_ror:8 row_mask:0xf bank_mask:0xf\ns_nop 1\nv_permlane32_swap_b32 v252, v253\nv_max_f32 v252, v252, v253\ns_xor_b32 s74, s82, 0x4000\n" \
    "v_max3_f32 v254, v252, v24, v25\nv_max3_f32 v252, v254, v26, v27\nv_max3_f32 v253, v254, v26, v27\ns_nop 1\nv_permlane16_swap_b32 v252, v253\nv_max_f32 v252, v252, v253\n" \
    "v_pk_add_f32 v[16:17], v[16:17], v[252:253] op_sel_hi:[1,0] neg_lo:[0,1] neg_hi:[0,1]\n" \
    "v_pk_add_f32 v[18:19], v[18:19], v[252:253] op_sel_hi:[1,0] neg_lo:[0,1] neg_hi:[0,1]\n" \
    "v_pk_add_f32 v[24:25], v[24:25], v[252:253] op_sel_hi:[1,0] neg_lo:[0,1] neg_hi:[0,1]\n" \
    "v_pk_add_f32 v[26:27], v[26:27], v[252:253] op_sel_hi:[1,0] neg_lo:[0,1] neg_hi:[0,1]\nv_exp_f32 v16, v16\nv_exp_f32 v17, v17\nv_exp_f32 v18, v18\nv_exp_f32 v19, v19\n" \
    "v_exp_f32 v24, v24\nv_pk_add_f32 v[254:255], v[16:17], v[18:19]\nv_exp_f32 v25, v25\nv_add_f32 v254, v254, v255\nv_exp_f32 v26, v26\nv_exp_f32 v27, v27\n" \
    "v_add_f32_dpp v252, v254, v254 row_ror:8 row_mask:0xf bank_mask:0xf\nv_add_f32_dpp v253, v254, v254 row_ror:8 row_mask:0xf bank_mask:0xf\n" \
    "v_pk_add_f32 v[254:255], v[24:25], v[26:27]\ns_nop 0\nv_permlane32_swap_b32 v252, v253\nv_pk_add_f32 v[254:255], v[254:255], v[252:253]\n" \
    "v_pk_add_f32 v[252:253], v[254:255], v[254:255] op_sel:[0,1] op_sel_hi:[1,0]\ns_nop 1\nv_permlane16_swap_b32 v252, v253\nv_add_f32 v252, v252, v253\nv_rcp_f32 v252, v252\n" \
    "s_nop 0\nv_pk_mul_f32 v[24:25], v[24:25], v[252:253] op_sel_hi:[1,0]\nv_pk_mul_f32 v[26:27], v[26:27], v[252:253] op_sel_hi:[1,0]\nv_cvt_pk_bf16_f32 v20, v24, v25\n" \
    "v_cvt_pk_bf16_f32 v21, v26, v27\nv_pk_mul_f32 v[16:17], v[16:17], v[252:253] op_sel_hi:[1,0]\nv_pk_mul_f32 v[18:19], v[18:19], v[252:253] op_sel_hi:[1,0]\n" \
    "v_permlane16_swap_b32 v20, v22\nv_cvt_pk_bf16_f32 v16, v16, v17\nv_cvt_pk_bf16_f32 v17, v18, v19\nv_permlane16_swap_b32 v21, v23\n" \
    "v_mov_b32_dpp v20, v16 quad_perm:[0,1,2,3] row_mask:0xf bank_mask:0x3\nv_mov_b32_dpp v22, v16 row_ror:8 row_mask:0xf bank_mask:0x3\n" \
    "v_mov_b32_dpp v21, v17 quad_perm:[0,1,2,3] row_mask:0xf bank_mask:0x3\nv_mov_b32_dpp v23, v17 row_ror:8 row_mask:0xf bank_mask:0x3\n" \
    "buffer_store_dwordx4 v[20:23], v7, s[64:67], s82 offen sc1\nbuffer_store_dwordx4 v[20:23], v7, s[64:67], s74 offen sc1\n.Lq3_%=:\nds_read_b128 v[68:71], v5 offset:0\n" \
    "ds_read_b128 v[56:59], v5 offset:1024\nds_read_b128 v[60:63], v5 offset:2048\nds_read_b128 v[40:43], v5 offset:3072\ns_mov_b32 s40, s84\ns_add_u32 s35, s35, 2\n" \
    "v_mov_b32 v15, v15\nv_mov_b32 v15, v15\nv_mov_b32 v15, v15\nv_mov_b32 v15, v15\nv_mov_b32 v15, v15\nv_mov_b32 v15, v15\nv_mov_b32 v15, v15\nv_mov_b32 v15, v15\n" \
    "s_waitcnt lgkmcnt(3)\nv_mfma_f32_16x16x32_bf16 v[16:19], v[48:51], v[68:71], 0\nv_mfma_f32_16x16x32_bf16 v[20:23], v[32:35], v[68:71], 0\ns_waitcnt lgkmcnt(2)\n" \
    "v_mfma_f32_16x16x32_bf16 v[16:19], v[36:39], v[56:59], v[16:19]\nv_mfma_f32_16x16x32_bf16 v[20:23], v[72:75], v[56:59], v[20:23]\ns_waitcnt lgkmcnt(1)\n" \
    "v_mfma_f32_16x16x32_bf16 v[16:19], v[76:79], v[60:63], v[16:19]\nv_mfma_f32_16x16x32_bf16 v[20:23], v[52:55], v[60:63], v[20:23]\ns_waitcnt lgkmcnt(0)\n" \
    "v_mfma_f32_16x16x32_bf16 v[16:19], v[80:83], v[40:43], v[16:19]\nv_mfma_f32_16x16x32_bf16 v[20:23], v[64:67], v[40:43], v[20:23]\n" \
    "v_mfma_f32_16x16x32_bf16 v[24:27], v[44:47], v[68:71], 0\nv_mfma_f32_16x16x32_bf16 v[24:27], v[84:87], v[56:59], v[24:27]\n" \
    "v_mfma_f32_16x16x32_bf16 v[24:27], v[88:91], v[60:63], v[24:27]\nv_mfma_f32_16x16x32_bf16 v[24:27], v[28:31], v[40:43], v[24:27]\ns_cmp_lt_u32 s35, 64\n" \
    "s_cbranch_scc1 .Lq2_%=\n.Lq8_%=:\ns_nop 1\nv_mov_b32_dpp v16, v20 quad_perm:[0,1,2,3] row_mask:0xf bank_mask:0xc\n" \
    "v_mov_b32_dpp v17, v21 quad_perm:[0,1,2,3] row_mask:0xf bank_mask:0xc\nv_mov_b32_dpp v18, v22 quad_perm:[0,1,2,3] row_mask:0xf bank_mask:0xc\n" \
    "v_mov_b32_dpp v19, v23 quad_perm:[0,1,2,3] row_mask:0xf bank_mask:0xc\nv_mov_b32 v254, 0xff800000\nv_cvt_pk_bf16_f32 v16, 0, v16\nv_cvt_pk_bf16_f32 v17, 0, v17\n" \
    "v_cvt_pk_bf16_f32 v18, 0, v18\nv_cvt_pk_bf16_f32 v19, 0, v19\nv_mul_f32 v16, s33, v16\nv_mul_f32 v17, s33, v17\nv_mul_f32 v18, s33, v18\nv_mul_f32 v19, s33, v19\n" \
    "v_cvt_pk_bf16_f32 v16, 0, v16\nv_cvt_pk_bf16_f32 v17, 0, v17\nv_cvt_pk_bf16_f32 v18, 0, v18\nv_cvt_pk_bf16_f32 v19, 0, v19\nv_cndmask_b32 v16, v254, v16, s[50:51]\n" \
    "v_cndmask_b32 v17, v254, v17, s[52:53]\nv_cndmask_b32 v18, v254, v18, s[54:55]\nv_cndmask_b32 v19, v254, v19, s[56:57]\nv_max3_f32 v254, v16, v17, v18\n" \
    "v_max_f32 v254, v254, v19\ns_nop 1\nv_max_f32_dpp v252, v254, v254 row_ror:8 row_mask:0xf bank_mask:0xf\n" \
    "v_max_f32_dpp v253, v254, v254 row_ror:8 row_mask:0xf bank_mask:0xf\ns_nop 1\nv_permlane32_swap_b32 v252, v253\nv_max_f32 v252, v252, v253\nv_cvt_pk_bf16_f32 v24, 0, v24\n" \
    "v_cvt_pk_bf16_f32 v25, 0, v25\nv_cvt_pk_bf16_f32 v26, 0, v26\nv_cvt_pk_bf16_f32 v27, 0, v27\nv_mul_f32 v24, s33, v24\nv_mul_f32 v25, s33, v25\nv_mul_f32 v26, s33, v26\n" \
    "v_mul_f32 v27, s33, v27\nv_cvt_pk_bf16_f32 v24, 0, v24\nv_cvt_pk_bf16_f32 v25, 0, v25\nv_cvt_pk_bf16_f32 v26, 0, v26\nv_cvt_pk_bf16_f32 v27, 0, v27\n" \
    "v_max3_f32 v254, v252, v24, v25\nv_max3_f32 v252, v254, v26, v27\nv_max3_f32 v253, v254, v26, v27\ns_nop 1\nv_permlane16_swap_b32 v252, v253\nv_max_f32 v252, v252, v253\n" \
    "v_pk_add_f32 v[16:17], v[16:17], v[252:253] op_sel_hi:[1,0] neg_lo:[0,1] neg_hi:[0,1]\n" \
    "v_pk_add_f32 v[18:19], v[18:19], v[252:253] op_sel_hi:[1,0] neg_lo:[0,1] neg_hi:[0,1]\n" \
    "v_pk_add_f32 v[24:25], v[24:25], v[252:253] op_sel_hi:[1,0] neg_lo:[0,1] neg_hi:[0,1]\n" \
    "v_pk_add_f32 v[26:27], v[26:27], v[252:253] op_sel_hi:[1,0] neg_lo:[0,1] neg_hi:[0,1]\nv_mul_f32 v16, s34, v16\nv_mul_f32 v17, s34, v17\nv_mul_f32 v18, s34, v18\n" \
    "v_mul_f32 v19, s34, v19\nv_mul_f32 v24, s34, v24\nv_mul_f32 v25, s34, v25\nv_mul_f32 v26, s34, v26\nv_mul_f32 v27, s34, v27\nv_exp_f32 v16, v16\nv_exp_f32 v17, v17\n" \
    "v_exp_f32 v18, v18\nv_exp_f32 v19, v19\nv_exp_f32 v24, v24\nv_pk_add_f32 v[254:255], v[16:17], v[18:19]\nv_exp_f32 v25, v25\nv_add_f32 v254, v254, v255\n" \
    "v_exp_f32 v26, v26\nv_exp_f32 v27, v27\nv_add_f32_dpp v252, v254, v254 row_ror:8 row_mask:0xf bank_mask:0xf\n" \
    "v_add_f32_dpp v253, v254, v254 row_ror:8 row_mask:0xf bank_mask:0xf\nv_pk_add_f32 v[254:255], v[24:25], v[26:27]\ns_nop 0\nv_permlane32_swap_b32 v252, v253\n" \
    "v_pk_add_f32 v[254:255], v[254:255], v[252:253]\nv_pk_add_f32 v[252:253], v[254:255], v[254:255] op_sel:[0,1] op_sel_hi:[1,0]\ns_nop 1\nv_permlane16_swap_b32 v252, v253\n" \
    "v_add_f32 v252, v252, v253\nv_rcp_f32 v252, v252\ns_nop 0\nv_pk_mul_f32 v[24:25], v[24:25], v[252:253] op_sel_hi:[1,0]\n" \
    "v_pk_mul_f32 v[26:27], v[26:27], v[252:253] op_sel_hi:[1,0]\nv_cvt_pk_bf16_f32 v20, v24, v25\nv_cvt_pk_bf16_f32 v21, v26, v27\n" \
    "v_pk_mul_f32 v[16:17], v[16:17], v[252:253] op_sel_hi:[1,0]\nv_pk_mul_f32 v[18:19], v[18:19], v[252:253] op_sel_hi:[1,0]\nv_permlane16_swap_b32 v20, v22\n" \
    "v_cvt_pk_bf16_f32 v16, v16, v17\nv_cvt_pk_bf16_f32 v17, v18, v19\nv_permlane16_swap_b32 v21, v23\nv_mov_b32_dpp v20, v16 quad_perm:[0,1,2,3] row_mask:0xf bank_mask:0x3\n" \
    "v_mov_b32_dpp v22, v16 row_ror:8 row_mask:0xf bank_mask:0x3\nv_mov_b32_dpp v21, v17 quad_perm:[0,1,2,3] row_mask:0xf bank_mask:0x3\n" \
    "v_mov_b32_dpp v23, v17 row_ror:8 row_mask:0xf bank_mask:0x3\ns_xor_b32 s74, s83, 0x2000\nbuffer_store_dwordx4 v[20:23], v7, s[64:67], s83 offen sc1\n" \
    "buffer_store_dwordx4 v[20:23], v7, s[64:67], s74 offen sc1\ns_cmp_lt_u32 s35, 64\ns_cbranch_scc1 .Lq9_%=\ns_endpgm\n.Lq14_%=:\ns_mov_b32 s75, 33024\n" \
    "buffer_load_dword v253, off, s[64:67], s75 sc1\ns_waitcnt vmcnt(0)\nv_readfirstlane_b32 s74, v253\ns_nop 0\ns_cmp_ge_u32 s74, 32\ns_cbranch_scc1 .Lq15_%=\ns_sleep 8\n" \
    "s_add_u32 s68, s68, 1\ns_and_b32 s69, s68, 63\ns_cbranch_scc1 .Lq14_%=\ns_cmp_gt_u32 s68, 0x400000\ns_cbranch_scc1 .Lq1_%=\nbuffer_load_dword v253, off, s[64:67], 0 sc1\n" \
    "s_waitcnt vmcnt(0)\nv_readfirstlane_b32 s69, v253\ns_nop 0\ns_cmp_eq_u32 s69, 1\ns_cbranch_scc0 .Lq14_%=\ns_endpgm\n.Lq16_%=:\ns_memrealtime s[72:73]\n" \
    "s_waitcnt lgkmcnt(0)\ns_mov_b32 s80, s72\ns_branch .Lq12_%=\n.Lq10_%=:\nbuffer_load_dword v13, off, s[64:67], s42 sc1\ns_waitcnt vmcnt(0)\ns_branch .Lq17_%=\n.Lq13_%=:\n" \
    "s_sleep 6\ns_add_u32 s68, s68, 1\ns_and_b32 s69, s68, 63\ns_cbranch_scc1 .Lq10_%=\ns_cmp_gt_u32 s68, 0x400000\ns_cbranch_scc1 .Lq1_%=\n" \
    "buffer_load_dword v253, off, s[64:67], 0 sc1\ns_waitcnt vmcnt(0)\nv_readfirstlane_b32 s69, v253\ns_nop 0\ns_cmp_eq_u32 s69, 1\ns_cbranch_scc0 .Lq10_%=\ns_endpgm\n" \
    ".Lq20_%=:\ns_add_u32 s80, s80, s43\ns_xor_b32 s81, s81, 0x1000\ns_xor_b32 s82, s82, 0x4000\ns_cmp_eq_u32 s61, 0\ns_cbranch_scc1 .Lq22_%=\ns_lshr_b32 s69, s81, 5\n" \
    "s_and_b32 s69, s69, 128\ns_lshl_b32 s74, s80, 8\ns_add_u32 s74, s74, s69\ns_add_u32 s74, s74, s35\nv_mov_b32 v253, s74\ns_mov_b64 exec, 1\n" \
    "buffer_store_dword v253, off, s[64:67], s61 sc1\ns_mov_b64 exec, -1\n.Lq22_%=:\ns_add_u32 s68, s68, 1\ns_and_b32 s69, s68, 63\ns_cbranch_scc1 .Lq19_%=\n" \
    "s_cmp_gt_u32 s68, 0x400000\ns_cbranch_scc1 .Lq1_%=\nbuffer_load_dword v253, off, s[64:67], 0 sc1\ns_waitcnt vmcnt(0)\nv_readfirstlane_b32 s69, v253\ns_nop 0\n" \
    "s_cmp_eq_u32 s69, 1\ns_cbranch_scc0 .Lq19_%=\ns_endpgm\n.Lq24_%=:\ns_add_u32 s80, s80, s43\ns_xor_b32 s81, s81, 0x1000\ns_xor_b32 s82, s82, 0x4000\ns_cmp_eq_u32 s61, 0\n" \
    "s_cbranch_scc1 .Lq26_%=\ns_lshr_b32 s69, s81, 5\ns_and_b32 s69, s69, 128\ns_lshl_b32 s74, s80, 8\ns_add_u32 s74, s74, s69\ns_add_u32 s74, s74, s35\nv_mov_b32 v253, s74\n" \
    "s_mov_b64 exec, 1\nbuffer_store_dword v253, off, s[64:67], s61 sc1\ns_mov_b64 exec, -1\n.Lq26_%=:\ns_add_u32 s68, s68, 1\ns_and_b32 s69, s68, 63\ns_cbranch_scc1 .Lq23_%=\n" \
    "s_cmp_gt_u32 s68, 0x400000\ns_cbranch_scc1 .Lq1_%=\nbuffer_load_dword v253, off, s[64:67], 0 sc1\ns_waitcnt vmcnt(0)\nv_readfirstlane_b32 s69, v253\ns_nop 0\n" \
    "s_cmp_eq_u32 s69, 1\ns_cbranch_scc0 .Lq23_%=\ns_endpgm\n.Lq28_%=:\ns_add_u32 s80, s80, s43\ns_xor_b32 s81, s81, 0x1000\ns_xor_b32 s82, s82, 0x4000\ns_cmp_eq_u32 s61, 0\n" \
    "s_cbranch_scc1 .Lq30_%=\ns_lshr_b32 s69, s81, 5\ns_and_b32 s69, s69, 128\ns_lshl_b32 s74, s80, 8\ns_add_u32 s74, s74, s69\ns_add_u32 s74, s74, s35\nv_mov_b32 v253, s74\n" \
    "s_mov_b64 exec, 1\nbuffer_store_dword v253, off, s[64:67], s61 sc1\ns_mov_b64 exec, -1\n.Lq30_%=:\ns_add_u32 s68, s68, 1\ns_and_b32 s69, s68, 63\ns_cbranch_scc1 .Lq27_%=\n" \
    "s_cmp_gt_u32 s68, 0x400000\ns_cbranch_scc1 .Lq1_%=\nbuffer_load_dword v253, off, s[64:67], 0 sc1\ns_waitcnt vmcnt(0)\nv_readfirstlane_b32 s69, v253\ns_nop 0\n" \
    "s_cmp_eq_u32 s69, 1\ns_cbranch_scc0 .Lq27_%=\ns_endpgm\n.Lq32_%=:\ns_add_u32 s80, s80, s43\ns_xor_b32 s81, s81, 0x1000\ns_xor_b32 s82, s82, 0x4000\ns_cmp_eq_u32 s61, 0\n" \
    "s_cbranch_scc1 .Lq34_%=\ns_lshr_b32 s69, s81, 5\ns_and_b32 s69, s69, 128\ns_lshl_b32 s74, s80, 8\ns_add_u32 s74, s74, s69\ns_add_u32 s74, s74, s35\nv_mov_b32 v253, s74\n" \
    "s_mov_b64 exec, 1\nbuffer_store_dword v253, off, s[64:67], s61 sc1\ns_mov_b64 exec, -1\n.Lq34_%=:\ns_add_u32 s68, s68, 1\ns_and_b32 s69, s68, 63\ns_cbranch_scc1 .Lq31_%=\n" \
    "s_cmp_gt_u32 s68, 0x400000\ns_cbranch_scc1 .Lq1_%=\nbuffer_load_dword v253, off, s[64:67], 0 sc1\ns_waitcnt vmcnt(0)\nv_readfirstlane_b32 s69, v253\ns_nop 0\n" \
    "s_cmp_eq_u32 s69, 1\ns_cbranch_scc0 .Lq31_%=\ns_endpgm\n.Lq1_%=:\nv_mov_b32 v253, 1\nbuffer_store_dword v253, off, s[64:67], 0 sc1\ns_endpgm\n"
#define ASM_V \
    "s_add_u32 m0, s41, 0\ns_xor_b32 s85, s62, 0\ns_add_u32 s85, s85, s40\nbuffer_load_dwordx4 v11, s[64:67], s85 offen sc1 lds\n" \
    "buffer_load_dwordx4 v12, s[64:67], s85 offen offset:1024 sc1 lds\ns_add_u32 m0, s41, 8192\ns_xor_b32 s85, s62, 256\ns_add_u32 s85, s85, s40\n" \
    "buffer_load_dwordx4 v11, s[64:67], s85 offen sc1 lds\nbuffer_load_dwordx4 v12, s[64:67], s85 offen offset:1024 sc1 lds\ns_add_u32 m0, s41, 16384\ns_xor_b32 s85, s62, 512\n" \
    "s_add_u32 s85, s85, s40\nbuffer_load_dwordx4 v11, s[64:67], s85 offen sc1 lds\nbuffer_load_dwordx4 v12, s[64:67], s85 offen offset:1024 sc1 lds\ns_add_u32 m0, s41, 40960\n" \
    "s_xor_b32 s85, s62, 768\ns_add_u32 s85, s85, s40\nbuffer_load_dwordx4 v11, s[64:67], s85 offen sc1 lds\nbuffer_load_dwordx4 v12, s[64:67], s85 offen offset:1024 sc1 lds\n" \
    "s_waitcnt vmcnt(0)\ns_barrier\nds_read_b128 v[56:59], v3 offset:0\nds_read_b128 v[60:63], v9 offset:0\nds_read_b128 v[64:67], v9 offset:4096\n" \
    "ds_read_b128 v[68:71], v3 offset:1024\nds_read_b128 v[72:75], v10 offset:0\nds_read_b128 v[76:79], v10 offset:4096\ns_add_u32 s84, s40, 0x20000\ns_cmp_lt_u32 s49, 62\n" \
    "s_cselect_b32 s84, s84, s40\ns_waitcnt lgkmcnt(3)\nv_mfma_f32_16x16x32_bf16 v[16:19], a[0:3], v[60:63], 0\ns_add_u32 m0, s41, 24576\ns_xor_b32 s85, s62, 1024\n" \
    "v_mfma_f32_16x16x32_bf16 v[20:23], a[0:3], v[64:67], 0\nds_read_b128 v[80:83], v3 offset:2048\ns_add_u32 s85, s85, s40\n" \
    "v_mfma_f32_16x16x32_bf16 v[24:27], v[56:59], v[60:63], 0\nds_read_b128 v[60:63], v9 offset:1024\nbuffer_load_dwordx4 v11, s[64:67], s85 offen sc1 lds\n" \
    "v_mfma_f32_16x16x32_bf16 v[28:31], v[56:59], v[64:67], 0\nds_read_b128 v[56:59], v9 offset:5120\ns_waitcnt lgkmcnt(3)\n" \
    "v_mfma_f32_16x16x32_bf16 v[16:19], a[4:7], v[72:75], v[16:19]\nbuffer_load_dwordx4 v12, s[64:67], s85 offen offset:1024 sc1 lds\ns_add_u32 m0, s41, 32768\n" \
    "v_mfma_f32_16x16x32_bf16 v[20:23], a[4:7], v[76:79], v[20:23]\nds_read_b128 v[64:67], v3 offset:3072\ns_xor_b32 s85, s62, 1280\n" \
    "v_mfma_f32_16x16x32_bf16 v[24:27], v[68:71], v[72:75], v[24:27]\nds_read_b128 v[72:75], v10 offset:1024\ns_add_u32 s85, s85, s40\n" \
    "v_mfma_f32_16x16x32_bf16 v[28:31], v[68:71], v[76:79], v[28:31]\nds_read_b128 v[68:71], v10 offset:5120\nbuffer_load_dwordx4 v11, s[64:67], s85 offen sc1 lds\n" \
    "s_waitcnt lgkmcnt(3)\nv_mfma_f32_16x16x32_bf16 v[16:19], a[8:11], v[60:63], v[16:19]\nv_mfma_f32_16x16x32_bf16 v[20:23], a[8:11], v[56:59], v[20:23]\n" \
    "ds_read_b128 v[76:79], v3 offset:4096\nbuffer_load_dwordx4 v12, s[64:67], s85 offen offset:1024 sc1 lds\nv_mov_b64 v[252:253], -1\n" \
    "v_mfma_f32_16x16x32_bf16 v[24:27], v[80:83], v[60:63], v[24:27]\nds_read_b128 v[60:63], v9 offset:8192\nv_mov_b64 v[254:255], -1\n" \
    "v_mfma_f32_16x16x32_bf16 v[28:31], v[80:83], v[56:59], v[28:31]\nds_read_b128 v[56:59], v9 offset:12288\ns_and_b32 s69, s49, 15\ns_waitcnt lgkmcnt(3)\n" \
    "v_mfma_f32_16x16x32_bf16 v[16:19], a[12:15], v[72:75], v[16:19]\ns_lshl_b32 s69, s69, 19\ns_add_u32 s82, s38, s69\n" \
    "v_mfma_f32_16x16x32_bf16 v[20:23], a[12:15], v[68:71], v[20:23]\nds_read_b128 v[80:83], v3 offset:5120\ns_lshr_b32 s69, s82, 8\n" \
    "v_mfma_f32_16x16x32_bf16 v[24:27], v[64:67], v[72:75], v[24:27]\nds_read_b128 v[72:75], v10 offset:8192\ns_and_b32 s69, s69, 0x2000\n" \
    "v_mfma_f32_16x16x32_bf16 v[28:31], v[64:67], v[68:71], v[28:31]\nds_read_b128 v[64:67], v10 offset:12288\ns_xor_b32 s82, s82, s69\ns_waitcnt lgkmcnt(3)\n" \
    "v_mfma_f32_16x16x32_bf16 v[16:19], a[16:19], v[60:63], v[16:19]\ns_and_b32 s69, s49, 255\ns_lshl_b32 s69, s69, 19\n" \
    "v_mfma_f32_16x16x32_bf16 v[20:23], a[16:19], v[56:59], v[20:23]\nds_read_b128 v[68:71], v3 offset:6144\ns_add_u32 s74, s63, s69\n" \
    "v_mfma_f32_16x16x32_bf16 v[24:27], v[76:79], v[60:63], v[24:27]\nds_read_b128 v[60:63], v9 offset:9216\ns_cmp_lt_u32 s49, 64\ns_cselect_b32 s74, s74, 0x7fff0000\n" \
    "v_mfma_f32_16x16x32_bf16 v[28:31], v[76:79], v[56:59], v[28:31]\nds_read_b128 v[56:59], v9 offset:13312\ns_waitcnt lgkmcnt(3)\n" \
    "v_mfma_f32_16x16x32_bf16 v[16:19], a[20:23], v[72:75], v[16:19]\nv_mfma_f32_16x16x32_bf16 v[20:23], a[20:23], v[64:67], v[20:23]\nds_read_b128 v[76:79], v3 offset:7168\n" \
    "v_mfma_f32_16x16x32_bf16 v[24:27], v[80:83], v[72:75], v[24:27]\nds_read_b128 v[72:75], v10 offset:9216\nv_mfma_f32_16x16x32_bf16 v[28:31], v[80:83], v[64:67], v[28:31]\n" \
    "ds_read_b128 v[64:67], v10 offset:13312\ns_waitcnt lgkmcnt(3)\nv_mfma_f32_16x16x32_bf16 v[16:19], a[24:27], v[60:63], v[16:19]\n" \
    "buffer_store_dwordx4 v[252:255], v2, s[64:67], s74 offen offset:0 sc1\nv_mfma_f32_16x16x32_bf16 v[20:23], a[24:27], v[56:59], v[20:23]\n" \
    "ds_read_b128 v[80:83], v3 offset:8192\nv_mfma_f32_16x16x32_bf16 v[24:27], v[68:71], v[60:63], v[24:27]\nds_read_b128 v[60:63], v9 offset:16384\n" \
    "v_mfma_f32_16x16x32_bf16 v[28:31], v[68:71], v[56:59], v[28:31]\nds_read_b128 v[56:59], v9 offset:20480\ns_waitcnt lgkmcnt(3)\n" \
    "v_mfma_f32_16x16x32_bf16 v[16:19], a[28:31], v[72:75], v[16:19]\nv_mfma_f32_16x16x32_bf16 v[20:23], a[28:31], v[64:67], v[20:23]\nds_read_b128 v[68:71], v3 offset:9216\n" \
    "v_mfma_f32_16x16x32_bf16 v[24:27], v[76:79], v[72:75], v[24:27]\nds_read_b128 v[72:75], v10 offset:16384\n" \
    "buffer_store_dwordx4 v[252:255], v2, s[64:67], s74 offen offset:1024 sc1\nv_mfma_f32_16x16x32_bf16 v[28:31], v[76:79], v[64:67], v[28:31]\n" \
    "ds_read_b128 v[64:67], v10 offset:20480\ns_waitcnt lgkmcnt(3)\nv_mfma_f32_16x16x32_bf16 v[16:19], a[32:35], v[60:63], v[16:19]\n" \
    "v_mfma_f32_16x16x32_bf16 v[20:23], a[32:35], v[56:59], v[20:23]\nds_read_b128 v[76:79], v3 offset:10240\nv_mfma_f32_16x16x32_bf16 v[24:27], v[80:83], v[60:63], v[24:27]\n" \
    "ds_read_b128 v[60:63], v9 offset:17408\nv_mfma_f32_16x16x32_bf16 v[28:31], v[80:83], v[56:59], v[28:31]\nds_read_b128 v[56:59], v9 offset:21504\ns_waitcnt lgkmcnt(3)\n" \
    "v_mfma_f32_16x16x32_bf16 v[16:19], a[36:39], v[72:75], v[16:19]\nv_mfma_f32_16x16x32_bf16 v[20:23], a[36:39], v[64:67], v[20:23]\nds_read_b128 v[80:83], v3 offset:11264\n" \
    "v_mfma_f32_16x16x32_bf16 v[24:27], v[68:71], v[72:75], v[24:27]\nds_read_b128 v[72:75], v10 offset:17408\nv_mfma_f32_16x16x32_bf16 v[28:31], v[68:71], v[64:67], v[28:31]\n" \
    "ds_read_b128 v[64:67], v10 offset:21504\ns_waitcnt lgkmcnt(3)\nv_mfma_f32_16x16x32_bf16 v[16:19], a[40:43], v[60:63], v[16:19]\n" \
    "v_mfma_f32_16x16x32_bf16 v[20:23], a[40:43], v[56:59], v[20:23]\nds_read_b128 v[68:71], v3 offset:12288\ns_barrier\n" \
    "v_mfma_f32_16x16x32_bf16 v[24:27], v[76:79], v[60:63], v[24:27]\nds_read_b128 v[60:63], v9 offset:40960\nv_mfma_f32_16x16x32_bf16 v[28:31], v[76:79], v[56:59], v[28:31]\n" \
    "ds_read_b128 v[56:59], v9 offset:45056\ns_add_u32 m0, s41, 0\ns_waitcnt lgkmcnt(3)\nv_mfma_f32_16x16x32_bf16 v[16:19], a[44:47], v[72:75], v[16:19]\n" \
    "s_xor_b32 s85, s62, 1536\ns_add_u32 s85, s85, s40\nv_mfma_f32_16x16x32_bf16 v[20:23], a[44:47], v[64:67], v[20:23]\nds_read_b128 v[76:79], v3 offset:13312\n" \
    "buffer_load_dwordx4 v11, s[64:67], s85 offen sc1 lds\nv_mfma_f32_16x16x32_bf16 v[24:27], v[80:83], v[72:75], v[24:27]\nds_read_b128 v[72:75], v10 offset:40960\n" \
    "v_mfma_f32_16x16x32_bf16 v[28:31], v[80:83], v[64:67], v[28:31]\nds_read_b128 v[64:67], v10 offset:45056\nbuffer_load_dwordx4 v12, s[64:67], s85 offen offset:1024 sc1 lds\n" \
    "s_waitcnt lgkmcnt(3)\nv_mfma_f32_16x16x32_bf16 v[16:19], a[48:51], v[60:63], v[16:19]\ns_add_u32 m0, s41, 8192\ns_xor_b32 s85, s62, 1792\n" \
    "v_mfma_f32_16x16x32_bf16 v[20:23], a[48:51], v[56:59], v[20:23]\nds_read_b128 v[80:83], v3 offset:14336\ns_add_u32 s85, s85, s40\n" \
    "v_mfma_f32_16x16x32_bf16 v[24:27], v[68:71], v[60:63], v[24:27]\nds_read_b128 v[60:63], v9 offset:41984\nbuffer_load_dwordx4 v11, s[64:67], s85 offen sc1 lds\n" \
    "v_mfma_f32_16x16x32_bf16 v[28:31], v[68:71], v[56:59], v[28:31]\nds_read_b128 v[56:59], v9 offset:46080\ns_waitcnt lgkmcnt(3)\n" \
    "v_mfma_f32_16x16x32_bf16 v[16:19], a[52:55], v[72:75], v[16:19]\nbuffer_load_dwordx4 v12, s[64:67], s85 offen offset:1024 sc1 lds\n" \
    "v_mfma_f32_16x16x32_bf16 v[20:23], a[52:55], v[64:67], v[20:23]\nds_read_b128 v[68:71], v3 offset:15360\nv_mfma_f32_16x16x32_bf16 v[24:27], v[76:79], v[72:75], v[24:27]\n" \
    "ds_read_b128 v[72:75], v10 offset:41984\nv_mfma_f32_16x16x32_bf16 v[28:31], v[76:79], v[64:67], v[28:31]\nds_read_b128 v[64:67], v10 offset:46080\ns_waitcnt lgkmcnt(3)\n" \
    "v_mfma_f32_16x16x32_bf16 v[16:19], a[56:59], v[60:63], v[16:19]\nv_mfma_f32_16x16x32_bf16 v[20:23], a[56:59], v[56:59], v[20:23]\nds_read_b128 v[76:79], v3 offset:16384\n" \
    "s_waitcnt vmcnt(6)\ns_barrier\nv_mfma_f32_16x16x32_bf16 v[24:27], v[80:83], v[60:63], v[24:27]\nds_read_b128 v[60:63], v9 offset:24576\n" \
    "v_mfma_f32_16x16x32_bf16 v[28:31], v[80:83], v[56:59], v[28:31]\nds_read_b128 v[56:59], v9 offset:28672\ns_waitcnt lgkmcnt(3)\n" \
    "v_mfma_f32_16x16x32_bf16 v[16:19], a[60:63], v[72:75], v[16:19]\nv_mfma_f32_16x16x32_bf16 v[20:23], a[60:63], v[64:67], v[20:23]\nds_read_b128 v[80:83], v3 offset:17408\n" \
    "v_mfma_f32_16x16x32_bf16 v[24:27], v[68:71], v[72:75], v[24:27]\nds_read_b128 v[72:75], v10 offset:24576\nv_mfma_f32_16x16x32_bf16 v[28:31], v[68:71], v[64:67], v[28:31]\n" \
    "ds_read_b128 v[64:67], v10 offset:28672\ns_waitcnt lgkmcnt(3)\nv_mfma_f32_16x16x32_bf16 v[16:19], a[64:67], v[60:63], v[16:19]\n" \
    "v_mfma_f32_16x16x32_bf16 v[20:23], a[64:67], v[56:59], v[20:23]\nds_read_b128 v[68:71], v3 offset:18432\nv_mfma_f32_16x16x32_bf16 v[24:27], v[76:79], v[60:63], v[24:27]\n" \
    "ds_read_b128 v[60:63], v9 offset:25600\nv_mfma_f32_16x16x32_bf16 v[28:31], v[76:79], v[56:59], v[28:31]\nds_read_b128 v[56:59], v9 offset:29696\ns_waitcnt lgkmcnt(3)\n" \
    "v_mfma_f32_16x16x32_bf16 v[16:19], a[68:71], v[72:75], v[16:19]\nbuffer_store_dwordx4 v[252:255], v7, s[64:67], s82 offen sc1\ns_add_u32 s82, s82, 0x10000\n" \
    "v_mfma_f32_16x16x32_bf16 v[20:23], a[68:71], v[64:67], v[20:23]\nds_read_b128 v[76:79], v3 offset:19456\nv_mfma_f32_16x16x32_bf16 v[24:27], v[80:83], v[72:75], v[24:27]\n" \
    "ds_read_b128 v[72:75], v10 offset:25600\nv_mfma_f32_16x16x32_bf16 v[28:31], v[80:83], v[64:67], v[28:31]\nds_read_b128 v[64:67], v10 offset:29696\ns_waitcnt lgkmcnt(3)\n" \
    "v_mfma_f32_16x16x32_bf16 v[16:19], a[72:75], v[60:63], v[16:19]\nv_mfma_f32_16x16x32_bf16 v[20:23], a[72:75], v[56:59], v[20:23]\nds_read_b128 v[80:83], v3 offset:20480\n" \
    "v_mfma_f32_16x16x32_bf16 v[24:27], v[68:71], v[60:63], v[24:27]\nds_read_b128 v[60:63], v9 offset:32768\nbuffer_store_dwordx4 v[252:255], v7, s[64:67], s82 offen sc1\n" \
    "v_mfma_f32_16x16x32_bf16 v[28:31], v[68:71], v[56:59], v[28:31]\nds_read_b128 v[56:59], v9 offset:36864\ns_add_u32 s82, s82, 0x10000\ns_waitcnt lgkmcnt(3)\n" \
    "v_mfma_f32_16x16x32_bf16 v[16:19], a[76:79], v[72:75], v[16:19]\nv_mfma_f32_16x16x32_bf16 v[20:23], a[76:79], v[64:67], v[20:23]\nds_read_b128 v[68:71], v3 offset:21504\n" \
    "v_mfma_f32_16x16x32_bf16 v[24:27], v[76:79], v[72:75], v[24:27]\nds_read_b128 v[72:75], v10 offset:32768\nv_mfma_f32_16x16x32_bf16 v[28:31], v[76:79], v[64:67], v[28:31]\n" \
    "ds_read_b128 v[64:67], v10 offset:36864\ns_waitcnt lgkmcnt(3)\nv_mfma_f32_16x16x32_bf16 v[16:19], a[80:83], v[60:63], v[16:19]\n" \
    "v_mfma_f32_16x16x32_bf16 v[20:23], a[80:83], v[56:59], v[20:23]\nds_read_b128 v[76:79], v3 offset:22528\nv_mfma_f32_16x16x32_bf16 v[24:27], v[80:83], v[60:63], v[24:27]\n" \
    "ds_read_b128 v[60:63], v9 offset:33792\nv_mfma_f32_16x16x32_bf16 v[28:31], v[80:83], v[56:59], v[28:31]\nds_read_b128 v[56:59], v9 offset:37888\ns_waitcnt lgkmcnt(3)\n" \
    "v_mfma_f32_16x16x32_bf16 v[16:19], a[84:87], v[72:75], v[16:19]\nv_mfma_f32_16x16x32_bf16 v[20:23], a[84:87], v[64:67], v[20:23]\nds_read_b128 v[80:83], v3 offset:23552\n" \
    "v_mfma_f32_16x16x32_bf16 v[24:27], v[68:71], v[72:75], v[24:27]\nds_read_b128 v[72:75], v10 offset:33792\nv_mfma_f32_16x16x32_bf16 v[28:31], v[68:71], v[64:67], v[28:31]\n" \
    "ds_read_b128 v[64:67], v10 offset:37888\ns_waitcnt lgkmcnt(3)\nv_mfma_f32_16x16x32_bf16 v[16:19], a[88:91], v[60:63], v[16:19]\n" \
    "buffer_load_dword v13, off, s[64:67], s42 sc1\nv_mfma_f32_16x16x32_bf16 v[20:23], a[88:91], v[56:59], v[20:23]\ns_waitcnt vmcnt(3)\ns_barrier\n" \
    "v_mfma_f32_16x16x32_bf16 v[24:27], v[76:79], v[60:63], v[24:27]\nds_read_b128 v[60:63], v9 offset:0\nv_mfma_f32_16x16x32_bf16 v[28:31], v[76:79], v[56:59], v[28:31]\n" \
    "ds_read_b128 v[56:59], v9 offset:4096\ns_add_u32 m0, s41, 16384\ns_waitcnt lgkmcnt(2)\nv_mfma_f32_16x16x32_bf16 v[16:19], a[92:95], v[72:75], v[16:19]\n" \
    "s_xor_b32 s85, s62, 512\ns_add_u32 s85, s85, s84\nv_mfma_f32_16x16x32_bf16 v[20:23], a[92:95], v[64:67], v[20:23]\nbuffer_load_dwordx4 v11, s[64:67], s85 offen sc1 lds\n" \
    "v_mfma_f32_16x16x32_bf16 v[24:27], v[80:83], v[72:75], v[24:27]\nds_read_b128 v[68:71], v10 offset:0\nbuffer_store_dwordx4 v[252:255], v7, s[64:67], s82 offen sc1\n" \
    "v_mfma_f32_16x16x32_bf16 v[28:31], v[80:83], v[64:67], v[28:31]\nds_read_b128 v[64:67], v10 offset:4096\nbuffer_load_dwordx4 v12, s[64:67], s85 offen offset:1024 sc1 lds\n" \
    "s_waitcnt lgkmcnt(2)\nv_mfma_f32_16x16x32_bf16 v[16:19], a[96:99], v[60:63], v[16:19]\ns_add_u32 m0, s41, 40960\ns_xor_b32 s85, s62, 768\n" \
    "v_mfma_f32_16x16x32_bf16 v[20:23], a[96:99], v[56:59], v[20:23]\ns_add_u32 s85, s85, s84\nbuffer_load_dwordx4 v11, s[64:67], s85 offen sc1 lds\n" \
    "v_mfma_f32_16x16x32_bf16 v[24:27], a[104:107], v[60:63], v[24:27]\nds_read_b128 v[60:63], v9 offset:1024\ns_add_u32 s82, s82, 0x10000\n" \
    "v_mfma_f32_16x16x32_bf16 v[28:31], a[104:107], v[56:59], v[28:31]\nds_read_b128 v[56:59], v9 offset:5120\nbuffer_load_dwordx4 v12, s[64:67], s85 offen offset:1024 sc1 lds\n" \
    "s_waitcnt lgkmcnt(2)\nv_mfma_f32_16x16x32_bf16 v[16:19], a[100:103], v[68:71], v[16:19]\nv_mfma_f32_16x16x32_bf16 v[20:23], a[100:103], v[64:67], v[20:23]\n" \
    "v_mfma_f32_16x16x32_bf16 v[24:27], a[108:111], v[68:71], v[24:27]\nds_read_b128 v[68:71], v10 offset:1024\n" \
    "v_mfma_f32_16x16x32_bf16 v[28:31], a[108:111], v[64:67], v[28:31]\nds_read_b128 v[64:67], v10 offset:5120\ns_waitcnt lgkmcnt(2)\n" \
    "v_mfma_f32_16x16x32_bf16 v[16:19], a[112:115], v[60:63], v[16:19]\nbuffer_store_dwordx4 v[252:255], v7, s[64:67], s82 offen sc1\ns_add_u32 s82, s82, 0x10000\n" \
    "v_mfma_f32_16x16x32_bf16 v[20:23], a[112:115], v[56:59], v[20:23]\nv_mfma_f32_16x16x32_bf16 v[24:27], a[120:123], v[60:63], v[24:27]\n" \
    "ds_read_b128 v[60:63], v9 offset:8192\nv_mfma_f32_16x16x32_bf16 v[28:31], a[120:123], v[56:59], v[28:31]\nds_read_b128 v[56:59], v9 offset:12288\ns_waitcnt lgkmcnt(2)\n" \
    "v_mfma_f32_16x16x32_bf16 v[16:19], a[116:119], v[68:71], v[16:19]\nv_mfma_f32_16x16x32_bf16 v[20:23], a[116:119], v[64:67], v[20:23]\n" \
    "v_mfma_f32_16x16x32_bf16 v[24:27], a[124:127], v[68:71], v[24:27]\nds_read_b128 v[68:71], v10 offset:8192\n" \
    "v_mfma_f32_16x16x32_bf16 v[28:31], a[124:127], v[64:67], v[28:31]\nds_read_b128 v[64:67], v10 offset:12288\ns_waitcnt lgkmcnt(2)\n" \
    "v_mfma_f32_16x16x32_bf16 v[16:19], a[128:131], v[60:63], v[16:19]\nv_mfma_f32_16x16x32_bf16 v[20:23], a[128:131], v[56:59], v[20:23]\n" \
    "v_mfma_f32_16x16x32_bf16 v[24:27], a[136:139], v[60:63], v[24:27]\nds_read_b128 v[60:63], v9 offset:9216\n" \
    "v_mfma_f32_16x16x32_bf16 v[28:31], a[136:139], v[56:59], v[28:31]\nds_read_b128 v[56:59], v9 offset:13312\ns_waitcnt lgkmcnt(2)\n" \
    "v_mfma_f32_16x16x32_bf16 v[16:19], a[132:135], v[68:71], v[16:19]\nv_mfma_f32_16x16x32_bf16 v[20:23], a[132:135], v[64:67], v[20:23]\n" \
    "v_mfma_f32_16x16x32_bf16 v[24:27], a[140:143], v[68:71], v[24:27]\nds_read_b128 v[68:71], v10 offset:9216\n" \
    "v_mfma_f32_16x16x32_bf16 v[28:31], a[140:143], v[64:67], v[28:31]\nds_read_b128 v[64:67], v10 offset:13312\ns_waitcnt lgkmcnt(2)\n" \
    "v_mfma_f32_16x16x32_bf16 v[16:19], a[144:147], v[60:63], v[16:19]\nv_mfma_f32_16x16x32_bf16 v[20:23], a[144:147], v[56:59], v[20:23]\n" \
    "v_mfma_f32_16x16x32_bf16 v[24:27], a[152:155], v[60:63], v[24:27]\nv_mfma_f32_16x16x32_bf16 v[28:31], a[152:155], v[56:59], v[28:31]\ns_waitcnt lgkmcnt(0)\n" \
    "v_mfma_f32_16x16x32_bf16 v[16:19], a[148:151], v[68:71], v[16:19]\nv_mfma_f32_16x16x32_bf16 v[20:23], a[148:151], v[64:67], v[20:23]\n" \
    "v_mfma_f32_16x16x32_bf16 v[24:27], a[156:159], v[68:71], v[24:27]\nv_mfma_f32_16x16x32_bf16 v[28:31], a[156:159], v[64:67], v[28:31]\ns_barrier\ns_add_u32 m0, s41, 0\n" \
    "s_xor_b32 s85, s62, 0\ns_add_u32 s85, s85, s84\nbuffer_load_dwordx4 v11, s[64:67], s85 offen sc1 lds\nv_cvt_pk_bf16_f32 v56, v16, v17\nv_cvt_pk_bf16_f32 v57, v18, v19\n" \
    "v_cvt_pk_bf16_f32 v58, v24, v25\nv_cvt_pk_bf16_f32 v59, v26, v27\nds_write_b128 v14, v[56:59] offset:0\nv_cvt_pk_bf16_f32 v60, v20, v21\nv_cvt_pk_bf16_f32 v61, v22, v23\n" \
    "v_cvt_pk_bf16_f32 v62, v28, v29\nv_cvt_pk_bf16_f32 v63, v30, v31\nds_write_b128 v14, v[60:63] offset:4096\n" \
    "buffer_load_dwordx4 v12, s[64:67], s85 offen offset:1024 sc1 lds\ns_add_u32 m0, s41, 8192\ns_xor_b32 s85, s62, 256\ns_add_u32 s85, s85, s84\n" \
    "buffer_load_dwordx4 v11, s[64:67], s85 offen sc1 lds\nbuffer_load_dwordx4 v12, s[64:67], s85 offen offset:1024 sc1 lds\ns_mov_b32 s40, s84\ns_add_u32 s49, s49, 2\n" \
    "s_waitcnt vmcnt(0) lgkmcnt(0)\ns_barrier\n.Lv5_%=:\nds_read_b128 v[56:59], v6 offset:0\nds_read_b128 v[60:63], v6 offset:1024\nds_read_b128 v[64:67], v6 offset:2048\n" \
    "s_and_b32 s69, s50, 15\ns_lshl_b32 s69, s69, 21\ns_add_u32 s86, s39, s69\ns_lshr_b32 s69, s86, 8\ns_and_b32 s69, s69, 0x2000\ns_xor_b32 s86, s86, s69\n" \
    "s_add_u32 s87, s86, 0x40000\ns_add_u32 s88, s86, 0x80000\ns_add_u32 s89, s86, 0xc0000\ns_add_u32 s50, s50, 2\ns_add_u32 s84, s40, 0x20000\ns_cmp_lt_u32 s49, 62\n" \
    "s_cselect_b32 s84, s84, s40\nv_mov_b64 v[32:33], 0\nv_mov_b64 v[34:35], 0\nv_mov_b64 v[36:37], 0\nv_mov_b64 v[38:39], 0\nv_mov_b64 v[40:41], 0\nv_mov_b64 v[42:43], 0\n" \
    "v_mov_b64 v[44:45], 0\nv_mov_b64 v[46:47], 0\ns_waitcnt lgkmcnt(2)\nv_smfmac_f32_16x16x64_bf16 v[32:35], v[56:59], a[160:167], v0\nds_read_b128 v[68:71], v6 offset:3072\n" \
    "s_add_u32 m0, s41, 24576\nv_smfmac_f32_16x16x64_bf16 v[36:39], v[56:59], a[176:183], v0\nds_read_b128 v[72:75], v3 offset:0\ns_xor_b32 s85, s62, 1024\n" \
    "s_waitcnt lgkmcnt(3)\nv_smfmac_f32_16x16x64_bf16 v[32:35], v[60:63], a[168:175], v0\nds_read_b128 v[76:79], v9 offset:0\ns_add_u32 s85, s85, s40\n" \
    "v_smfmac_f32_16x16x64_bf16 v[36:39], v[60:63], a[184:191], v0\nds_read_b128 v[80:83], v9 offset:4096\nbuffer_load_dwordx4 v11, s[64:67], s85 offen sc1 lds\n" \
    "s_waitcnt lgkmcnt(4)\nv_smfmac_f32_16x16x64_bf16 v[32:35], v[64:67], a[192:199], v0\nv_smfmac_f32_16x16x64_bf16 v[36:39], v[64:67], a[208:215], v0\n" \
    "buffer_load_dwordx4 v12, s[64:67], s85 offen offset:1024 sc1 lds\ns_add_u32 m0, s41, 32768\ns_waitcnt lgkmcnt(3)\n" \
    "v_smfmac_f32_16x16x64_bf16 v[32:35], v[68:71], a[200:207], v0\ns_xor_b32 s85, s62, 1280\ns_add_u32 s85, s85, s40\n" \
    "v_smfmac_f32_16x16x64_bf16 v[36:39], v[68:71], a[216:223], v0\nbuffer_load_dwordx4 v11, s[64:67], s85 offen sc1 lds\ns_nop 0\ns_waitcnt lgkmcnt(0)\n" \
    "v_mfma_f32_16x16x32_bf16 v[16:19], a[0:3], v[76:79], 0\ns_nop 0\nv_cvt_pk_bf16_f32 v48, v32, v33\nv_cvt_pk_bf16_f32 v50, v34, v35\n" \
    "v_mfma_f32_16x16x32_bf16 v[20:23], a[0:3], v[80:83], 0\nbuffer_load_dwordx4 v12, s[64:67], s85 offen offset:1024 sc1 lds\nv_cvt_pk_bf16_f32 v49, v36, v37\n" \
    "v_cvt_pk_bf16_f32 v51, v38, v39\nv_mfma_f32_16x16x32_bf16 v[24:27], v[72:75], v[76:79], 0\nv_permlane16_swap_b32 v48, v49\n" \
    "v_mfma_f32_16x16x32_bf16 v[28:31], v[72:75], v[80:83], 0\nv_permlane16_swap_b32 v50, v51\nv_smfmac_f32_16x16x64_bf16 v[40:43], v[56:59], a[224:231], v0\n" \
    "buffer_store_dwordx4 v[48:51], v8, s[64:67], s86 offen offset:0 sc1\nv_mov_b64 v[32:33], 0\nv_smfmac_f32_16x16x64_bf16 v[44:47], v[56:59], a[240:247], v0\n" \
    "ds_read_b128 v[72:75], v3 offset:1024\nv_mov_b64 v[34:35], 0\nv_smfmac_f32_16x16x64_bf16 v[40:43], v[60:63], a[232:239], v0\nds_read_b128 v[76:79], v10 offset:0\n" \
    "v_mov_b64 v[36:37], 0\nv_smfmac_f32_16x16x64_bf16 v[44:47], v[60:63], a[248:255], v0\nds_read_b128 v[80:83], v10 offset:4096\nv_mov_b64 v[38:39], 0\n" \
    "v_smfmac_f32_16x16x64_bf16 v[40:43], v[64:67], v[92:99], v0\nv_mov_b64 v[252:253], -1\nv_smfmac_f32_16x16x64_bf16 v[44:47], v[64:67], v[108:115], v0\n" \
    "v_mov_b64 v[254:255], -1\nv_smfmac_f32_16x16x64_bf16 v[40:43], v[68:71], v[100:107], v0\ns_and_b32 s69, s49, 15\ns_lshl_b32 s69, s69, 19\n" \
    "v_smfmac_f32_16x16x64_bf16 v[44:47], v[68:71], v[116:123], v0\ns_add_u32 s82, s38, s69\ns_lshr_b32 s69, s82, 8\ns_waitcnt lgkmcnt(0)\n" \
    "v_mfma_f32_16x16x32_bf16 v[16:19], a[4:7], v[76:79], v[16:19]\ns_and_b32 s69, s69, 0x2000\nv_cvt_pk_bf16_f32 v52, v40, v41\nv_cvt_pk_bf16_f32 v54, v42, v43\n" \
    "v_mfma_f32_16x16x32_bf16 v[20:23], a[4:7], v[80:83], v[20:23]\ns_xor_b32 s82, s82, s69\nv_cvt_pk_bf16_f32 v53, v44, v45\nv_cvt_pk_bf16_f32 v55, v46, v47\n" \
    "v_mfma_f32_16x16x32_bf16 v[24:27], v[72:75], v[76:79], v[24:27]\ns_and_b32 s69, s49, 255\nv_permlane16_swap_b32 v52, v53\n" \
    "v_mfma_f32_16x16x32_bf16 v[28:31], v[72:75], v[80:83], v[28:31]\ns_lshl_b32 s69, s69, 19\nv_permlane16_swap_b32 v54, v55\n" \
    "v_smfmac_f32_16x16x64_bf16 v[32:35], v[56:59], v[124:131], v0\nbuffer_store_dwordx4 v[52:55], v8, s[64:67], s87 offen offset:0 sc1\nv_mov_b64 v[40:41], 0\n" \
    "v_smfmac_f32_16x16x64_bf16 v[36:39], v[56:59], v[140:147], v0\nds_read_b128 v[72:75], v3 offset:2048\nv_mov_b64 v[42:43], 0\n" \
    "v_smfmac_f32_16x16x64_bf16 v[32:35], v[60:63], v[132:139], v0\nds_read_b128 v[76:79], v9 offset:1024\nv_mov_b64 v[44:45], 0\n" \
    "v_smfmac_f32_16x16x64_bf16 v[36:39], v[60:63], v[148:155], v0\nds_read_b128 v[80:83], v9 offset:5120\nv_mov_b64 v[46:47], 0\n" \
    "v_smfmac_f32_16x16x64_bf16 v[32:35], v[64:67], v[156:163], v0\ns_add_u32 s74, s63, s69\ns_cmp_lt_u32 s49, 64\ns_cselect_b32 s74, s74, 0x7fff0000\n" \
    "v_smfmac_f32_16x16x64_bf16 v[36:39], v[64:67], v[172:179], v0\nv_smfmac_f32_16x16x64_bf16 v[32:35], v[68:71], v[164:171], v0\n" \
    "v_smfmac_f32_16x16x64_bf16 v[36:39], v[68:71], v[180:187], v0\ns_nop 1\ns_waitcnt lgkmcnt(0)\nv_mfma_f32_16x16x32_bf16 v[16:19], a[8:11], v[76:79], v[16:19]\ns_nop 1\n" \
    "v_mfma_f32_16x16x32_bf16 v[20:23], a[8:11], v[80:83], v[20:23]\nv_cvt_pk_bf16_f32 v48, v32, v33\nv_cvt_pk_bf16_f32 v50, v34, v35\n" \
    "v_mfma_f32_16x16x32_bf16 v[24:27], v[72:75], v[76:79], v[24:27]\nbuffer_store_dwordx4 v[252:255], v2, s[64:67], s74 offen offset:0 sc1\nv_cvt_pk_bf16_f32 v49, v36, v37\n" \
    "v_cvt_pk_bf16_f32 v51, v38, v39\nv_mfma_f32_16x16x32_bf16 v[28:31], v[72:75], v[80:83], v[28:31]\nv_permlane16_swap_b32 v48, v49\n" \
    "v_smfmac_f32_16x16x64_bf16 v[40:43], v[56:59], v[188:195], v0\nv_permlane16_swap_b32 v50, v51\nv_smfmac_f32_16x16x64_bf16 v[44:47], v[56:59], v[204:211], v0\n" \
    "ds_read_b128 v[56:59], v3 offset:3072\nbuffer_store_dwordx4 v[48:51], v8, s[64:67], s88 offen offset:0 sc1\nv_mov_b64 v[32:33], 0\n" \
    "v_smfmac_f32_16x16x64_bf16 v[40:43], v[60:63], v[196:203], v0\nds_read_b128 v[72:75], v10 offset:1024\nv_mov_b64 v[34:35], 0\n" \
    "v_smfmac_f32_16x16x64_bf16 v[44:47], v[60:63], v[212:219], v0\nds_read_b128 v[60:63], v10 offset:5120\nv_mov_b64 v[36:37], 0\n" \
    "v_smfmac_f32_16x16x64_bf16 v[40:43], v[64:67], v[220:227], v0\nbuffer_store_dwordx4 v[252:255], v2, s[64:67], s74 offen offset:1024 sc1\nv_mov_b64 v[38:39], 0\n" \
    "v_smfmac_f32_16x16x64_bf16 v[44:47], v[64:67], v[236:243], v0\nv_smfmac_f32_16x16x64_bf16 v[40:43], v[68:71], v[228:235], v0\nds_read_b128 v[64:67], v6 offset:4096\n" \
    "v_smfmac_f32_16x16x64_bf16 v[44:47], v[68:71], v[244:251], v0\ns_nop 1\ns_waitcnt lgkmcnt(1)\nv_mfma_f32_16x16x32_bf16 v[16:19], a[12:15], v[72:75], v[16:19]\n" \
    "ds_read_b128 v[68:71], v6 offset:5120\ns_nop 0\nv_cvt_pk_bf16_f32 v52, v40, v41\nv_mfma_f32_16x16x32_bf16 v[20:23], a[12:15], v[60:63], v[20:23]\n" \
    "v_cvt_pk_bf16_f32 v54, v42, v43\nv_cvt_pk_bf16_f32 v53, v44, v45\nv_mfma_f32_16x16x32_bf16 v[24:27], v[56:59], v[72:75], v[24:27]\nds_read_b128 v[72:75], v6 offset:6144\n" \
    "v_cvt_pk_bf16_f32 v55, v46, v47\nv_permlane16_swap_b32 v52, v53\nv_mfma_f32_16x16x32_bf16 v[28:31], v[56:59], v[60:63], v[28:31]\ns_waitcnt lgkmcnt(2)\n" \
    "v_permlane16_swap_b32 v54, v55\nv_smfmac_f32_16x16x64_bf16 v[32:35], v[64:67], a[160:167], v0\nds_read_b128 v[56:59], v6 offset:7168\n" \
    "buffer_store_dwordx4 v[52:55], v8, s[64:67], s89 offen offset:0 sc1\nv_mov_b64 v[40:41], 0\nv_smfmac_f32_16x16x64_bf16 v[36:39], v[64:67], a[176:183], v0\n" \
    "ds_read_b128 v[60:63], v3 offset:4096\ns_waitcnt lgkmcnt(3)\nv_mov_b64 v[42:43], 0\nv_smfmac_f32_16x16x64_bf16 v[32:35], v[68:71], a[168:175], v0\n" \
    "ds_read_b128 v[76:79], v9 offset:8192\nv_mov_b64 v[44:45], 0\nv_smfmac_f32_16x16x64_bf16 v[36:39], v[68:71], a[184:191], v0\nds_read_b128 v[80:83], v9 offset:12288\n" \
    "s_waitcnt lgkmcnt(4)\nv_mov_b64 v[46:47], 0\nv_smfmac_f32_16x16x64_bf16 v[32:35], v[72:75], a[192:199], v0\nv_smfmac_f32_16x16x64_bf16 v[36:39], v[72:75], a[208:215], v0\n" \
    "s_waitcnt lgkmcnt(3)\nv_smfmac_f32_16x16x64_bf16 v[32:35], v[56:59], a[200:207], v0\nv_smfmac_f32_16x16x64_bf16 v[36:39], v[56:59], a[216:223], v0\ns_nop 1\n" \
    "s_waitcnt lgkmcnt(0)\nv_mfma_f32_16x16x32_bf16 v[16:19], a[16:19], v[76:79], v[16:19]\ns_nop 1\nv_mfma_f32_16x16x32_bf16 v[20:23], a[16:19], v[80:83], v[20:23]\n" \
    "v_cvt_pk_bf16_f32 v48, v32, v33\nv_cvt_pk_bf16_f32 v50, v34, v35\nv_mfma_f32_16x16x32_bf16 v[24:27], v[60:63], v[76:79], v[24:27]\nv_cvt_pk_bf16_f32 v49, v36, v37\n" \
    "v_cvt_pk_bf16_f32 v51, v38, v39\nv_mfma_f32_16x16x32_bf16 v[28:31], v[60:63], v[80:83], v[28:31]\nv_permlane16_swap_b32 v48, v49\n" \
    "v_smfmac_f32_16x16x64_bf16 v[40:43], v[64:67], a[224:231], v0\nv_permlane16_swap_b32 v50, v51\nv_smfmac_f32_16x16x64_bf16 v[44:47], v[64:67], a[240:247], v0\n" \
    "ds_read_b128 v[60:63], v3 offset:5120\nbuffer_store_dwordx4 v[48:51], v8, s[64:67], s86 offen offset:256 sc1\nv_mov_b64 v[32:33], 0\n" \
    "v_smfmac_f32_16x16x64_bf16 v[40:43], v[68:71], a[232:239], v0\nds_read_b128 v[76:79], v10 offset:8192\nv_mov_b64 v[34:35], 0\n" \
    "v_smfmac_f32_16x16x64_bf16 v[44:47], v[68:71], a[248:255], v0\nds_read_b128 v[80:83], v10 offset:12288\nv_mov_b64 v[36:37], 0\n" \
    "v_smfmac_f32_16x16x64_bf16 v[40:43], v[72:75], v[92:99], v0\nv_mov_b64 v[38:39], 0\nv_smfmac_f32_16x16x64_bf16 v[44:47], v[72:75], v[108:115], v0\n" \
    "v_smfmac_f32_16x16x64_bf16 v[40:43], v[56:59], v[100:107], v0\nv_smfmac_f32_16x16x64_bf16 v[44:47], v[56:59], v[116:123], v0\ns_nop 1\ns_waitcnt lgkmcnt(0)\n" \
    "v_mfma_f32_16x16x32_bf16 v[16:19], a[20:23], v[76:79], v[16:19]\ns_nop 1\nv_mfma_f32_16x16x32_bf16 v[20:23], a[20:23], v[80:83], v[20:23]\nv_cvt_pk_bf16_f32 v52, v40, v41\n" \
    "v_cvt_pk_bf16_f32 v54, v42, v43\nv_mfma_f32_16x16x32_bf16 v[24:27], v[60:63], v[76:79], v[24:27]\nv_cvt_pk_bf16_f32 v53, v44, v45\nv_cvt_pk_bf16_f32 v55, v46, v47\n" \
    "v_mfma_f32_16x16x32_bf16 v[28:31], v[60:63], v[80:83], v[28:31]\nv_permlane16_swap_b32 v52, v53\nv_smfmac_f32_16x16x64_bf16 v[32:35], v[64:67], v[124:131], v0\n" \
    "v_permlane16_swap_b32 v54, v55\nv_smfmac_f32_16x16x64_bf16 v[36:39], v[64:67], v[140:147], v0\nds_read_b128 v[60:63], v3 offset:6144\n" \
    "buffer_store_dwordx4 v[52:55], v8, s[64:67], s87 offen offset:256 sc1\nv_mov_b64 v[40:41], 0\nv_smfmac_f32_16x16x64_bf16 v[32:35], v[68:71], v[132:139], v0\n" \
    "ds_read_b128 v[76:79], v9 offset:9216\nv_mov_b64 v[42:43], 0\nv_smfmac_f32_16x16x64_bf16 v[36:39], v[68:71], v[148:155], v0\nds_read_b128 v[80:83], v9 offset:13312\n" \
    "v_mov_b64 v[44:45], 0\nv_smfmac_f32_16x16x64_bf16 v[32:35], v[72:75], v[156:163], v0\nv_mov_b64 v[46:47], 0\nv_smfmac_f32_16x16x64_bf16 v[36:39], v[72:75], v[172:179], v0\n" \
    "v_smfmac_f32_16x16x64_bf16 v[32:35], v[56:59], v[164:171], v0\nv_smfmac_f32_16x16x64_bf16 v[36:39], v[56:59], v[180:187], v0\ns_nop 1\ns_waitcnt lgkmcnt(0)\n" \
    "v_mfma_f32_16x16x32_bf16 v[16:19], a[24:27], v[76:79], v[16:19]\ns_nop 1\nv_mfma_f32_16x16x32_bf16 v[20:23], a[24:27], v[80:83], v[20:23]\nv_cvt_pk_bf16_f32 v48, v32, v33\n" \
    "v_cvt_pk_bf16_f32 v50, v34, v35\nv_mfma_f32_16x16x32_bf16 v[24:27], v[60:63], v[76:79], v[24:27]\nv_cvt_pk_bf16_f32 v49, v36, v37\nv_cvt_pk_bf16_f32 v51, v38, v39\n" \
    "v_mfma_f32_16x16x32_bf16 v[28:31], v[60:63], v[80:83], v[28:31]\nv_permlane16_swap_b32 v48, v49\nv_smfmac_f32_16x16x64_bf16 v[40:43], v[64:67], v[188:195], v0\n" \
    "v_permlane16_swap_b32 v50, v51\nv_smfmac_f32_16x16x64_bf16 v[44:47], v[64:67], v[204:211], v0\nds_read_b128 v[60:63], v3 offset:7168\n" \
    "buffer_store_dwordx4 v[48:51], v8, s[64:67], s88 offen offset:256 sc1\nv_mov_b64 v[32:33], 0\nv_smfmac_f32_16x16x64_bf16 v[40:43], v[68:71], v[196:203], v0\n" \
    "ds_read_b128 v[64:67], v10 offset:9216\nv_mov_b64 v[34:35], 0\nv_smfmac_f32_16x16x64_bf16 v[44:47], v[68:71], v[212:219], v0\nds_read_b128 v[68:71], v10 offset:13312\n" \
    "v_mov_b64 v[36:37], 0\nv_smfmac_f32_16x16x64_bf16 v[40:43], v[72:75], v[220:227], v0\nv_mov_b64 v[38:39], 0\nv_smfmac_f32_16x16x64_bf16 v[44:47], v[72:75], v[236:243], v0\n" \
    "v_smfmac_f32_16x16x64_bf16 v[40:43], v[56:59], v[228:235], v0\nds_read_b128 v[72:75], v6 offset:128\nv_smfmac_f32_16x16x64_bf16 v[44:47], v[56:59], v[244:251], v0\n" \
    "s_nop 1\ns_waitcnt lgkmcnt(1)\nv_mfma_f32_16x16x32_bf16 v[16:19], a[28:31], v[64:67], v[16:19]\nds_read_b128 v[56:59], v6 offset:1152\ns_nop 0\n" \
    "v_cvt_pk_bf16_f32 v52, v40, v41\nv_mfma_f32_16x16x32_bf16 v[20:23], a[28:31], v[68:71], v[20:23]\nv_cvt_pk_bf16_f32 v54, v42, v43\nv_cvt_pk_bf16_f32 v53, v44, v45\n" \
    "v_mfma_f32_16x16x32_bf16 v[24:27], v[60:63], v[64:67], v[24:27]\nds_read_b128 v[64:67], v6 offset:2176\nv_cvt_pk_bf16_f32 v55, v46, v47\nv_permlane16_swap_b32 v52, v53\n" \
    "v_mfma_f32_16x16x32_bf16 v[28:31], v[60:63], v[68:71], v[28:31]\ns_waitcnt lgkmcnt(2)\nv_permlane16_swap_b32 v54, v55\n" \
    "v_smfmac_f32_16x16x64_bf16 v[32:35], v[72:75], a[160:167], v0\nds_read_b128 v[60:63], v6 offset:3200\n" \
    "buffer_store_dwordx4 v[52:55], v8, s[64:67], s89 offen offset:256 sc1\nv_mov_b64 v[40:41], 0\nv_smfmac_f32_16x16x64_bf16 v[36:39], v[72:75], a[176:183], v0\n" \
    "ds_read_b128 v[68:71], v3 offset:8192\ns_waitcnt lgkmcnt(3)\nv_mov_b64 v[42:43], 0\nv_smfmac_f32_16x16x64_bf16 v[32:35], v[56:59], a[168:175], v0\n" \
    "ds_read_b128 v[76:79], v9 offset:16384\nv_mov_b64 v[44:45], 0\nv_smfmac_f32_16x16x64_bf16 v[36:39], v[56:59], a[184:191], v0\nds_read_b128 v[80:83], v9 offset:20480\n" \
    "s_waitcnt lgkmcnt(4)\nv_mov_b64 v[46:47], 0\nv_smfmac_f32_16x16x64_bf16 v[32:35], v[64:67], a[192:199], v0\nv_smfmac_f32_16x16x64_bf16 v[36:39], v[64:67], a[208:215], v0\n" \
    "s_waitcnt lgkmcnt(3)\nv_smfmac_f32_16x16x64_bf16 v[32:35], v[60:63], a[200:207], v0\nv_smfmac_f32_16x16x64_bf16 v[36:39], v[60:63], a[216:223], v0\ns_nop 1\n" \
    "s_waitcnt lgkmcnt(0)\nv_mfma_f32_16x16x32_bf16 v[16:19], a[32:35], v[76:79], v[16:19]\ns_nop 1\nv_mfma_f32_16x16x32_bf16 v[20:23], a[32:35], v[80:83], v[20:23]\n" \
    "v_cvt_pk_bf16_f32 v48, v32, v33\nv_cvt_pk_bf16_f32 v50, v34, v35\nv_mfma_f32_16x16x32_bf16 v[24:27], v[68:71], v[76:79], v[24:27]\nv_cvt_pk_bf16_f32 v49, v36, v37\n" \
    "v_cvt_pk_bf16_f32 v51, v38, v39\nv_mfma_f32_16x16x32_bf16 v[28:31], v[68:71], v[80:83], v[28:31]\nv_permlane16_swap_b32 v48, v49\n" \
    "v_smfmac_f32_16x16x64_bf16 v[40:43], v[72:75], a[224:231], v0\nv_permlane16_swap_b32 v50, v51\nv_smfmac_f32_16x16x64_bf16 v[44:47], v[72:75], a[240:247], v0\n" \
    "ds_read_b128 v[68:71], v3 offset:9216\nbuffer_store_dwordx4 v[48:51], v8, s[64:67], s86 offen offset:512 sc1\nv_mov_b64 v[32:33], 0\n" \
    "v_smfmac_f32_16x16x64_bf16 v[40:43], v[56:59], a[232:239], v0\nds_read_b128 v[76:79], v10 offset:16384\nv_mov_b64 v[34:35], 0\n" \
    "v_smfmac_f32_16x16x64_bf16 v[44:47], v[56:59], a[248:255], v0\nds_read_b128 v[80:83], v10 offset:20480\nv_mov_b64 v[36:37], 0\n" \
    "v_smfmac_f32_16x16x64_bf16 v[40:43], v[64:67], v[92:99], v0\nv_mov_b64 v[38:39], 0\nv_smfmac_f32_16x16x64_bf16 v[44:47], v[64:67], v[108:115], v0\n" \
    "v_smfmac_f32_16x16x64_bf16 v[40:43], v[60:63], v[100:107], v0\nv_smfmac_f32_16x16x64_bf16 v[44:47], v[60:63], v[116:123], v0\ns_nop 1\ns_waitcnt lgkmcnt(0)\n" \
    "v_mfma_f32_16x16x32_bf16 v[16:19], a[36:39], v[76:79], v[16:19]\ns_nop 1\nv_mfma_f32_16x16x32_bf16 v[20:23], a[36:39], v[80:83], v[20:23]\nv_cvt_pk_bf16_f32 v52, v40, v41\n" \
    "v_cvt_pk_bf16_f32 v54, v42, v43\nv_mfma_f32_16x16x32_bf16 v[24:27], v[68:71], v[76:79], v[24:27]\nv_cvt_pk_bf16_f32 v53, v44, v45\nv_cvt_pk_bf16_f32 v55, v46, v47\n" \
    "v_mfma_f32_16x16x32_bf16 v[28:31], v[68:71], v[80:83], v[28:31]\nv_permlane16_swap_b32 v52, v53\nv_smfmac_f32_16x16x64_bf16 v[32:35], v[72:75], v[124:131], v0\n" \
    "v_permlane16_swap_b32 v54, v55\nv_smfmac_f32_16x16x64_bf16 v[36:39], v[72:75], v[140:147], v0\nds_read_b128 v[68:71], v3 offset:10240\n" \
    "buffer_store_dwordx4 v[52:55], v8, s[64:67], s87 offen offset:512 sc1\nv_mov_b64 v[40:41], 0\nv_smfmac_f32_16x16x64_bf16 v[32:35], v[56:59], v[132:139], v0\n" \
    "ds_read_b128 v[76:79], v9 offset:17408\nv_mov_b64 v[42:43], 0\nv_smfmac_f32_16x16x64_bf16 v[36:39], v[56:59], v[148:155], v0\nds_read_b128 v[80:83], v9 offset:21504\n" \
    "v_mov_b64 v[44:45], 0\nv_smfmac_f32_16x16x64_bf16 v[32:35], v[64:67], v[156:163], v0\nv_mov_b64 v[46:47], 0\nv_smfmac_f32_16x16x64_bf16 v[36:39], v[64:67], v[172:179], v0\n" \
    "v_smfmac_f32_16x16x64_bf16 v[32:35], v[60:63], v[164:171], v0\nv_smfmac_f32_16x16x64_bf16 v[36:39], v[60:63], v[180:187], v0\ns_nop 1\ns_waitcnt lgkmcnt(0)\n" \
    "v_mfma_f32_16x16x32_bf16 v[16:19], a[40:43], v[76:79], v[16:19]\ns_nop 1\nv_mfma_f32_16x16x32_bf16 v[20:23], a[40:43], v[80:83], v[20:23]\nv_cvt_pk_bf16_f32 v48, v32, v33\n" \
    "v_cvt_pk_bf16_f32 v50, v34, v35\nv_mfma_f32_16x16x32_bf16 v[24:27], v[68:71], v[76:79], v[24:27]\nv_cvt_pk_bf16_f32 v49, v36, v37\nv_cvt_pk_bf16_f32 v51, v38, v39\n" \
    "v_mfma_f32_16x16x32_bf16 v[28:31], v[68:71], v[80:83], v[28:31]\nv_permlane16_swap_b32 v48, v49\nv_smfmac_f32_16x16x64_bf16 v[40:43], v[72:75], v[188:195], v0\n" \
    "v_permlane16_swap_b32 v50, v51\nv_smfmac_f32_16x16x64_bf16 v[44:47], v[72:75], v[204:211], v0\nds_read_b128 v[68:71], v3 offset:11264\n" \
    "buffer_store_dwordx4 v[48:51], v8, s[64:67], s88 offen offset:512 sc1\nv_mov_b64 v[32:33], 0\nv_smfmac_f32_16x16x64_bf16 v[40:43], v[56:59], v[196:203], v0\n" \
    "ds_read_b128 v[72:75], v10 offset:17408\nv_mov_b64 v[34:35], 0\nv_smfmac_f32_16x16x64_bf16 v[44:47], v[56:59], v[212:219], v0\nds_read_b128 v[56:59], v10 offset:21504\n" \
    "v_mov_b64 v[36:37], 0\nv_smfmac_f32_16x16x64_bf16 v[40:43], v[64:67], v[220:227], v0\nv_mov_b64 v[38:39], 0\nv_smfmac_f32_16x16x64_bf16 v[44:47], v[64:67], v[236:243], v0\n" \
    "v_smfmac_f32_16x16x64_bf16 v[40:43], v[60:63], v[228:235], v0\nds_read_b128 v[64:67], v6 offset:4224\nv_smfmac_f32_16x16x64_bf16 v[44:47], v[60:63], v[244:251], v0\n" \
    "s_nop 1\ns_waitcnt lgkmcnt(1)\nv_mfma_f32_16x16x32_bf16 v[16:19], a[44:47], v[72:75], v[16:19]\nds_read_b128 v[60:63], v6 offset:5248\ns_nop 0\n" \
    "v_cvt_pk_bf16_f32 v52, v40, v41\nv_mfma_f32_16x16x32_bf16 v[20:23], a[44:47], v[56:59], v[20:23]\nv_cvt_pk_bf16_f32 v54, v42, v43\nv_cvt_pk_bf16_f32 v53, v44, v45\n" \
    "v_mfma_f32_16x16x32_bf16 v[24:27], v[68:71], v[72:75], v[24:27]\nds_read_b128 v[72:75], v6 offset:6272\nv_cvt_pk_bf16_f32 v55, v46, v47\nv_permlane16_swap_b32 v52, v53\n" \
    "v_mfma_f32_16x16x32_bf16 v[28:31], v[68:71], v[56:59], v[28:31]\ns_waitcnt lgkmcnt(2)\nv_permlane16_swap_b32 v54, v55\n" \
    "v_smfmac_f32_16x16x64_bf16 v[32:35], v[64:67], a[160:167], v0\nds_read_b128 v[56:59], v6 offset:7296\n" \
    "buffer_store_dwordx4 v[52:55], v8, s[64:67], s89 offen offset:512 sc1\nv_mov_b64 v[40:41], 0\nv_smfmac_f32_16x16x64_bf16 v[36:39], v[64:67], a[176:183], v0\n" \
    "ds_read_b128 v[68:71], v3 offset:12288\ns_waitcnt vmcnt(14)\ns_barrier\ns_waitcnt lgkmcnt(3)\nv_smfmac_f32_16x16x64_bf16 v[32:35], v[60:63], a[168:175], v0\n" \
    "ds_read_b128 v[76:79], v9 offset:40960\nv_mov_b64 v[42:43], 0\nv_smfmac_f32_16x16x64_bf16 v[36:39], v[60:63], a[184:191], v0\nds_read_b128 v[80:83], v9 offset:45056\n" \
    "s_add_u32 m0, s41, 0\ns_waitcnt lgkmcnt(4)\nv_smfmac_f32_16x16x64_bf16 v[32:35], v[72:75], a[192:199], v0\ns_xor_b32 s85, s62, 1536\ns_add_u32 s85, s85, s40\n" \
    "v_mov_b64 v[44:45], 0\nv_smfmac_f32_16x16x64_bf16 v[36:39], v[72:75], a[208:215], v0\nbuffer_load_dwordx4 v11, s[64:67], s85 offen sc1 lds\ns_waitcnt lgkmcnt(3)\n" \
    "v_mov_b64 v[46:47], 0\nv_smfmac_f32_16x16x64_bf16 v[32:35], v[56:59], a[200:207], v0\nv_smfmac_f32_16x16x64_bf16 v[36:39], v[56:59], a[216:223], v0\n" \
    "buffer_load_dwordx4 v12, s[64:67], s85 offen offset:1024 sc1 lds\ns_add_u32 m0, s41, 8192\ns_waitcnt lgkmcnt(0)\n" \
    "v_mfma_f32_16x16x32_bf16 v[16:19], a[48:51], v[76:79], v[16:19]\ns_xor_b32 s85, s62, 1792\ns_add_u32 s85, s85, s40\n" \
    "v_mfma_f32_16x16x32_bf16 v[20:23], a[48:51], v[80:83], v[20:23]\nbuffer_load_dwordx4 v11, s[64:67], s85 offen sc1 lds\nv_cvt_pk_bf16_f32 v48, v32, v33\n" \
    "v_cvt_pk_bf16_f32 v50, v34, v35\nv_mfma_f32_16x16x32_bf16 v[24:27], v[68:71], v[76:79], v[24:27]\nv_cvt_pk_bf16_f32 v49, v36, v37\nv_cvt_pk_bf16_f32 v51, v38, v39\n" \
    "v_mfma_f32_16x16x32_bf16 v[28:31], v[68:71], v[80:83], v[28:31]\nbuffer_load_dwordx4 v12, s[64:67], s85 offen offset:1024 sc1 lds\nv_permlane16_swap_b32 v48, v49\n" \
    "v_smfmac_f32_16x16x64_bf16 v[40:43], v[64:67], a[224:231], v0\nv_permlane16_swap_b32 v50, v51\nv_smfmac_f32_16x16x64_bf16 v[44:47], v[64:67], a[240:247], v0\n" \
    "ds_read_b128 v[68:71], v3 offset:13312\nbuffer_store_dwordx4 v[48:51], v8, s[64:67], s86 offen offset:768 sc1\nv_mov_b64 v[32:33], 0\n" \
    "v_smfmac_f32_16x16x64_bf16 v[40:43], v[60:63], a[232:239], v0\nds_read_b128 v[76:79], v10 offset:40960\nv_mov_b64 v[34:35], 0\n" \
    "v_smfmac_f32_16x16x64_bf16 v[44:47], v[60:63], a[248:255], v0\nds_read_b128 v[80:83], v10 offset:45056\nv_mov_b64 v[36:37], 0\n" \
    "v_smfmac_f32_16x16x64_bf16 v[40:43], v[72:75], v[92:99], v0\nv_mov_b64 v[38:39], 0\nv_smfmac_f32_16x16x64_bf16 v[44:47], v[72:75], v[108:115], v0\n" \
    "v_smfmac_f32_16x16x64_bf16 v[40:43], v[56:59], v[100:107], v0\nv_smfmac_f32_16x16x64_bf16 v[44:47], v[56:59], v[116:123], v0\ns_nop 1\ns_waitcnt lgkmcnt(0)\n" \
    "v_mfma_f32_16x16x32_bf16 v[16:19], a[52:55], v[76:79], v[16:19]\ns_nop 1\nv_mfma_f32_16x16x32_bf16 v[20:23], a[52:55], v[80:83], v[20:23]\nv_cvt_pk_bf16_f32 v52, v40, v41\n" \
    "v_cvt_pk_bf16_f32 v54, v42, v43\nv_mfma_f32_16x16x32_bf16 v[24:27], v[68:71], v[76:79], v[24:27]\nv_cvt_pk_bf16_f32 v53, v44, v45\nv_cvt_pk_bf16_f32 v55, v46, v47\n" \
    "v_mfma_f32_16x16x32_bf16 v[28:31], v[68:71], v[80:83], v[28:31]\nv_permlane16_swap_b32 v52, v53\nv_smfmac_f32_16x16x64_bf16 v[32:35], v[64:67], v[124:131], v0\n" \
    "v_permlane16_swap_b32 v54, v55\nv_smfmac_f32_16x16x64_bf16 v[36:39], v[64:67], v[140:147], v0\nds_read_b128 v[68:71], v3 offset:14336\n" \
    "buffer_store_dwordx4 v[52:55], v8, s[64:67], s87 offen offset:768 sc1\nv_mov_b64 v[40:41], 0\nv_smfmac_f32_16x16x64_bf16 v[32:35], v[60:63], v[132:139], v0\n" \
    "ds_read_b128 v[76:79], v9 offset:41984\nv_mov_b64 v[42:43], 0\nv_smfmac_f32_16x16x64_bf16 v[36:39], v[60:63], v[148:155], v0\nds_read_b128 v[80:83], v9 offset:46080\n" \
    "v_mov_b64 v[44:45], 0\nv_smfmac_f32_16x16x64_bf16 v[32:35], v[72:75], v[156:163], v0\nv_mov_b64 v[46:47], 0\nv_smfmac_f32_16x16x64_bf16 v[36:39], v[72:75], v[172:179], v0\n" \
    "v_smfmac_f32_16x16x64_bf16 v[32:35], v[56:59], v[164:171], v0\nv_smfmac_f32_16x16x64_bf16 v[36:39], v[56:59], v[180:187], v0\ns_nop 1\ns_waitcnt lgkmcnt(0)\n" \
    "v_mfma_f32_16x16x32_bf16 v[16:19], a[56:59], v[76:79], v[16:19]\ns_nop 1\nv_mfma_f32_16x16x32_bf16 v[20:23], a[56:59], v[80:83], v[20:23]\nv_cvt_pk_bf16_f32 v48, v32, v33\n" \
    "v_cvt_pk_bf16_f32 v50, v34, v35\nv_mfma_f32_16x16x32_bf16 v[24:27], v[68:71], v[76:79], v[24:27]\nv_cvt_pk_bf16_f32 v49, v36, v37\nv_cvt_pk_bf16_f32 v51, v38, v39\n" \
    "v_mfma_f32_16x16x32_bf16 v[28:31], v[68:71], v[80:83], v[28:31]\nv_permlane16_swap_b32 v48, v49\nv_smfmac_f32_16x16x64_bf16 v[40:43], v[64:67], v[188:195], v0\n" \
    "v_permlane16_swap_b32 v50, v51\nv_smfmac_f32_16x16x64_bf16 v[44:47], v[64:67], v[204:211], v0\nds_read_b128 v[64:67], v3 offset:15360\n" \
    "buffer_store_dwordx4 v[48:51], v8, s[64:67], s88 offen offset:768 sc1\nv_smfmac_f32_16x16x64_bf16 v[40:43], v[60:63], v[196:203], v0\n" \
    "ds_read_b128 v[68:71], v10 offset:41984\nv_smfmac_f32_16x16x64_bf16 v[44:47], v[60:63], v[212:219], v0\nds_read_b128 v[60:63], v10 offset:46080\n" \
    "v_smfmac_f32_16x16x64_bf16 v[40:43], v[72:75], v[220:227], v0\nv_smfmac_f32_16x16x64_bf16 v[44:47], v[72:75], v[236:243], v0\nds_read_b128 v[72:75], v3 offset:16384\n" \
    "v_smfmac_f32_16x16x64_bf16 v[40:43], v[56:59], v[228:235], v0\nds_read_b128 v[76:79], v9 offset:24576\nv_smfmac_f32_16x16x64_bf16 v[44:47], v[56:59], v[244:251], v0\n" \
    "ds_read_b128 v[56:59], v9 offset:28672\ns_nop 0\ns_waitcnt lgkmcnt(3)\nv_mfma_f32_16x16x32_bf16 v[16:19], a[60:63], v[68:71], v[16:19]\ns_nop 1\n" \
    "v_cvt_pk_bf16_f32 v52, v40, v41\nv_cvt_pk_bf16_f32 v54, v42, v43\nv_mfma_f32_16x16x32_bf16 v[20:23], a[60:63], v[60:63], v[20:23]\nds_read_b128 v[80:83], v3 offset:17408\n" \
    "v_cvt_pk_bf16_f32 v53, v44, v45\nv_cvt_pk_bf16_f32 v55, v46, v47\nv_mfma_f32_16x16x32_bf16 v[24:27], v[64:67], v[68:71], v[24:27]\nds_read_b128 v[68:71], v10 offset:24576\n" \
    "v_permlane16_swap_b32 v52, v53\nv_mfma_f32_16x16x32_bf16 v[28:31], v[64:67], v[60:63], v[28:31]\nds_read_b128 v[60:63], v10 offset:28672\ns_waitcnt lgkmcnt(3)\n" \
    "v_permlane16_swap_b32 v54, v55\nv_mfma_f32_16x16x32_bf16 v[16:19], a[64:67], v[76:79], v[16:19]\nbuffer_store_dwordx4 v[52:55], v8, s[64:67], s89 offen offset:768 sc1\n" \
    "v_mfma_f32_16x16x32_bf16 v[20:23], a[64:67], v[56:59], v[20:23]\nds_read_b128 v[64:67], v3 offset:18432\nv_mfma_f32_16x16x32_bf16 v[24:27], v[72:75], v[76:79], v[24:27]\n" \
    "ds_read_b128 v[76:79], v9 offset:25600\nv_mfma_f32_16x16x32_bf16 v[28:31], v[72:75], v[56:59], v[28:31]\nds_read_b128 v[56:59], v9 offset:29696\ns_waitcnt lgkmcnt(3)\n" \
    "v_mfma_f32_16x16x32_bf16 v[16:19], a[68:71], v[68:71], v[16:19]\nbuffer_store_dwordx4 v[252:255], v7, s[64:67], s82 offen sc1\ns_add_u32 s82, s82, 0x10000\n" \
    "v_mfma_f32_16x16x32_bf16 v[20:23], a[68:71], v[60:63], v[20:23]\nds_read_b128 v[72:75], v3 offset:19456\nv_mfma_f32_16x16x32_bf16 v[24:27], v[80:83], v[68:71], v[24:27]\n" \
    "ds_read_b128 v[68:71], v10 offset:25600\nv_mfma_f32_16x16x32_bf16 v[28:31], v[80:83], v[60:63], v[28:31]\nds_read_b128 v[60:63], v10 offset:29696\ns_waitcnt lgkmcnt(3)\n" \
    "v_mfma_f32_16x16x32_bf16 v[16:19], a[72:75], v[76:79], v[16:19]\nv_mfma_f32_16x16x32_bf16 v[20:23], a[72:75], v[56:59], v[20:23]\nds_read_b128 v[80:83], v3 offset:20480\n" \
    "v_mfma_f32_16x16x32_bf16 v[24:27], v[64:67], v[76:79], v[24:27]\nds_read_b128 v[76:79], v9 offset:32768\nbuffer_store_dwordx4 v[252:255], v7, s[64:67], s82 offen sc1\n" \
    "v_mfma_f32_16x16x32_bf16 v[28:31], v[64:67], v[56:59], v[28:31]\nds_read_b128 v[56:59], v9 offset:36864\ns_add_u32 s82, s82, 0x10000\ns_waitcnt lgkmcnt(3)\n" \
    "v_mfma_f32_16x16x32_bf16 v[16:19], a[76:79], v[68:71], v[16:19]\nv_mfma_f32_16x16x32_bf16 v[20:23], a[76:79], v[60:63], v[20:23]\nds_read_b128 v[64:67], v3 offset:21504\n" \
    "v_mfma_f32_16x16x32_bf16 v[24:27], v[72:75], v[68:71], v[24:27]\nds_read_b128 v[68:71], v10 offset:32768\nv_mfma_f32_16x16x32_bf16 v[28:31], v[72:75], v[60:63], v[28:31]\n" \
    "ds_read_b128 v[60:63], v10 offset:36864\ns_waitcnt lgkmcnt(3)\nv_mfma_f32_16x16x32_bf16 v[16:19], a[80:83], v[76:79], v[16:19]\n" \
    "v_mfma_f32_16x16x32_bf16 v[20:23], a[80:83], v[56:59], v[20:23]\nds_read_b128 v[72:75], v3 offset:22528\nv_mfma_f32_16x16x32_bf16 v[24:27], v[80:83], v[76:79], v[24:27]\n" \
    "ds_read_b128 v[76:79], v9 offset:33792\nv_mfma_f32_16x16x32_bf16 v[28:31], v[80:83], v[56:59], v[28:31]\nds_read_b128 v[56:59], v9 offset:37888\ns_waitcnt lgkmcnt(3)\n" \
    "v_mfma_f32_16x16x32_bf16 v[16:19], a[84:87], v[68:71], v[16:19]\nv_mfma_f32_16x16x32_bf16 v[20:23], a[84:87], v[60:63], v[20:23]\nds_read_b128 v[80:83], v3 offset:23552\n" \
    "v_mfma_f32_16x16x32_bf16 v[24:27], v[64:67], v[68:71], v[24:27]\nds_read_b128 v[68:71], v10 offset:33792\nv_mfma_f32_16x16x32_bf16 v[28:31], v[64:67], v[60:63], v[28:31]\n" \
    "ds_read_b128 v[60:63], v10 offset:37888\ns_waitcnt lgkmcnt(3)\nv_mfma_f32_16x16x32_bf16 v[16:19], a[88:91], v[76:79], v[16:19]\n" \
    "buffer_load_dword v13, off, s[64:67], s42 sc1\nv_mfma_f32_16x16x32_bf16 v[20:23], a[88:91], v[56:59], v[20:23]\ns_waitcnt vmcnt(7)\ns_barrier\n" \
    "v_mfma_f32_16x16x32_bf16 v[24:27], v[72:75], v[76:79], v[24:27]\nds_read_b128 v[64:67], v9 offset:0\nv_mfma_f32_16x16x32_bf16 v[28:31], v[72:75], v[56:59], v[28:31]\n" \
    "ds_read_b128 v[56:59], v9 offset:4096\ns_add_u32 m0, s41, 16384\ns_waitcnt lgkmcnt(2)\nv_mfma_f32_16x16x32_bf16 v[16:19], a[92:95], v[68:71], v[16:19]\n" \
    "s_xor_b32 s85, s62, 512\ns_add_u32 s85, s85, s84\nv_mfma_f32_16x16x32_bf16 v[20:23], a[92:95], v[60:63], v[20:23]\nbuffer_load_dwordx4 v11, s[64:67], s85 offen sc1 lds\n" \
    "v_mfma_f32_16x16x32_bf16 v[24:27], v[80:83], v[68:71], v[24:27]\nds_read_b128 v[68:71], v10 offset:0\nbuffer_store_dwordx4 v[252:255], v7, s[64:67], s82 offen sc1\n" \
    "v_mfma_f32_16x16x32_bf16 v[28:31], v[80:83], v[60:63], v[28:31]\nds_read_b128 v[60:63], v10 offset:4096\nbuffer_load_dwordx4 v12, s[64:67], s85 offen offset:1024 sc1 lds\n" \
    "s_waitcnt lgkmcnt(2)\nv_mfma_f32_16x16x32_bf16 v[16:19], a[96:99], v[64:67], v[16:19]\ns_add_u32 m0, s41, 40960\ns_xor_b32 s85, s62, 768\n" \
    "v_mfma_f32_16x16x32_bf16 v[20:23], a[96:99], v[56:59], v[20:23]\ns_add_u32 s85, s85, s84\nbuffer_load_dwordx4 v11, s[64:67], s85 offen sc1 lds\n" \
    "v_mfma_f32_16x16x32_bf16 v[24:27], a[104:107], v[64:67], v[24:27]\nds_read_b128 v[64:67], v9 offset:1024\ns_add_u32 s82, s82, 0x10000\n" \
    "v_mfma_f32_16x16x32_bf16 v[28:31], a[104:107], v[56:59], v[28:31]\nds_read_b128 v[56:59], v9 offset:5120\nbuffer_load_dwordx4 v12, s[64:67], s85 offen offset:1024 sc1 lds\n" \
    "s_waitcnt lgkmcnt(2)\nv_mfma_f32_16x16x32_bf16 v[16:19], a[100:103], v[68:71], v[16:19]\nv_mfma_f32_16x16x32_bf16 v[20:23], a[100:103], v[60:63], v[20:23]\n" \
    "v_mfma_f32_16x16x32_bf16 v[24:27], a[108:111], v[68:71], v[24:27]\nds_read_b128 v[68:71], v10 offset:1024\n" \
    "v_mfma_f32_16x16x32_bf16 v[28:31], a[108:111], v[60:63], v[28:31]\nds_read_b128 v[60:63], v10 offset:5120\ns_waitcnt lgkmcnt(2)\n" \
    "v_mfma_f32_16x16x32_bf16 v[16:19], a[112:115], v[64:67], v[16:19]\nv_mfma_f32_16x16x32_bf16 v[20:23], a[112:115], v[56:59], v[20:23]\n" \
    "v_mfma_f32_16x16x32_bf16 v[24:27], a[120:123], v[64:67], v[24:27]\nds_read_b128 v[64:67], v9 offset:8192\n" \
    "v_mfma_f32_16x16x32_bf16 v[28:31], a[120:123], v[56:59], v[28:31]\nds_read_b128 v[56:59], v9 offset:12288\ns_waitcnt lgkmcnt(2)\n" \
    "v_mfma_f32_16x16x32_bf16 v[16:19], a[116:119], v[68:71], v[16:19]\nv_mfma_f32_16x16x32_bf16 v[20:23], a[116:119], v[60:63], v[20:23]\n" \
    "v_mfma_f32_16x16x32_bf16 v[24:27], a[124:127], v[68:71], v[24:27]\nds_read_b128 v[68:71], v10 offset:8192\n" \
    "v_mfma_f32_16x16x32_bf16 v[28:31], a[124:127], v[60:63], v[28:31]\nds_read_b128 v[60:63], v10 offset:12288\ns_waitcnt lgkmcnt(2)\n" \
    "v_mfma_f32_16x16x32_bf16 v[16:19], a[128:131], v[64:67], v[16:19]\ns_waitcnt vmcnt(8)\nv_mov_b32 v15, s50\ns_mov_b64 exec, 1\n" \
    "buffer_store_dword v15, off, s[64:67], s48 sc1\ns_mov_b64 exec, -1\nv_mfma_f32_16x16x32_bf16 v[20:23], a[128:131], v[56:59], v[20:23]\n" \
    "v_mfma_f32_16x16x32_bf16 v[24:27], a[136:139], v[64:67], v[24:27]\nds_read_b128 v[64:67], v9 offset:9216\n" \
    "v_mfma_f32_16x16x32_bf16 v[28:31], a[136:139], v[56:59], v[28:31]\nds_read_b128 v[56:59], v9 offset:13312\ns_waitcnt lgkmcnt(2)\n" \
    "v_mfma_f32_16x16x32_bf16 v[16:19], a[132:135], v[68:71], v[16:19]\nv_mfma_f32_16x16x32_bf16 v[20:23], a[132:135], v[60:63], v[20:23]\n" \
    "v_mfma_f32_16x16x32_bf16 v[24:27], a[140:143], v[68:71], v[24:27]\nds_read_b128 v[68:71], v10 offset:9216\nbuffer_store_dwordx4 v[252:255], v7, s[64:67], s82 offen sc1\n" \
    "v_mfma_f32_16x16x32_bf16 v[28:31], a[140:143], v[60:63], v[28:31]\nds_read_b128 v[60:63], v10 offset:13312\ns_add_u32 s82, s82, 0x10000\ns_waitcnt lgkmcnt(2)\n" \
    "v_mfma_f32_16x16x32_bf16 v[16:19], a[144:147], v[64:67], v[16:19]\nv_mfma_f32_16x16x32_bf16 v[20:23], a[144:147], v[56:59], v[20:23]\n" \
    "v_mfma_f32_16x16x32_bf16 v[24:27], a[152:155], v[64:67], v[24:27]\nv_mfma_f32_16x16x32_bf16 v[28:31], a[152:155], v[56:59], v[28:31]\ns_waitcnt lgkmcnt(0)\n" \
    "v_mfma_f32_16x16x32_bf16 v[16:19], a[148:151], v[68:71], v[16:19]\nv_mfma_f32_16x16x32_bf16 v[20:23], a[148:151], v[60:63], v[20:23]\n" \
    "v_mfma_f32_16x16x32_bf16 v[24:27], a[156:159], v[68:71], v[24:27]\nv_mfma_f32_16x16x32_bf16 v[28:31], a[156:159], v[60:63], v[28:31]\ns_barrier\ns_add_u32 m0, s41, 0\n" \
    "s_xor_b32 s85, s62, 0\ns_add_u32 s85, s85, s84\nbuffer_load_dwordx4 v11, s[64:67], s85 offen sc1 lds\nv_cvt_pk_bf16_f32 v56, v16, v17\nv_cvt_pk_bf16_f32 v57, v18, v19\n" \
    "v_cvt_pk_bf16_f32 v58, v24, v25\nv_cvt_pk_bf16_f32 v59, v26, v27\nds_write_b128 v14, v[56:59] offset:0\nv_cvt_pk_bf16_f32 v60, v20, v21\nv_cvt_pk_bf16_f32 v61, v22, v23\n" \
    "v_cvt_pk_bf16_f32 v62, v28, v29\nv_cvt_pk_bf16_f32 v63, v30, v31\nds_write_b128 v14, v[60:63] offset:4096\n" \
    "buffer_load_dwordx4 v12, s[64:67], s85 offen offset:1024 sc1 lds\ns_add_u32 m0, s41, 8192\ns_xor_b32 s85, s62, 256\ns_add_u32 s85, s85, s84\n" \
    "buffer_load_dwordx4 v11, s[64:67], s85 offen sc1 lds\nbuffer_load_dwordx4 v12, s[64:67], s85 offen offset:1024 sc1 lds\ns_mov_b32 s40, s84\ns_add_u32 s49, s49, 2\n" \
    ".Lv4_%=:\ns_and_b32 s69, s35, 15\ns_lshl_b32 s69, s69, 19\ns_add_u32 s86, s38, s69\ns_lshr_b32 s69, s86, 8\ns_and_b32 s69, s69, 0x2000\ns_xor_b32 s86, s86, s69\n" \
    "s_add_u32 s87, s86, 0x10000\ns_add_u32 s88, s86, 0x20000\ns_add_u32 s89, s86, 0x30000\ns_and_b32 s69, s35, 15\ns_lshl_b32 s69, s69, 17\ns_add_u32 s81, s37, s69\n" \
    "s_lshr_b32 s69, s81, 8\ns_and_b32 s69, s69, 0x2000\ns_xor_b32 s81, s81, s69\ns_mov_b32 s68, 0\ns_lshr_b32 s69, s35, 1\ns_cmp_eq_u32 s69, 5\ns_cbranch_scc1 .Lv16_%=\n" \
    ".Lv17_%=:\ns_cmp_eq_u32 s35, 0\ns_cbranch_scc1 .Lv18_%=\ns_waitcnt vmcnt(11)\n.Lv19_%=:\nv_readfirstlane_b32 s74, v13\ns_nop 0\ns_and_b32 s75, s74, 127\n" \
    "s_cmp_lt_u32 s75, s35\ns_cbranch_scc1 .Lv15_%=\ns_memrealtime s[72:73]\ns_waitcnt lgkmcnt(0)\ns_lshr_b32 s69, s74, 8\ns_sub_u32 s69, s69, s72\n" \
    "s_bfe_i32 s69, s69, 0x180000\ns_add_u32 s80, s72, s69\ns_sub_u32 s76, s80, s45\ns_sub_u32 s69, s72, s76\ns_cmp_ge_i32 s69, -8\ns_cbranch_scc1 .Lv13_%=\n.Lv20_%=:\n" \
    "s_memrealtime s[72:73]\ns_waitcnt lgkmcnt(0)\ns_sub_u32 s72, s72, s76\ns_cmp_lt_i32 s72, 0\ns_cbranch_scc1 .Lv20_%=\ns_branch .Lv12_%=\n.Lv13_%=:\ns_lshl_b32 s74, s74, 5\n" \
    "s_and_b32 s74, s74, 0x1000\ns_xor_b32 s81, s81, s74\n.Lv14_%=:\ns_mov_b32 s68, 0\ns_cmp_eq_u32 s36, 1\ns_cbranch_scc1 .Lv9_%=\ns_cmp_eq_u32 s36, 2\n" \
    "s_cbranch_scc1 .Lv10_%=\ns_cmp_eq_u32 s36, 3\ns_cbranch_scc1 .Lv11_%=\n.Lv8_%=:\nds_read_b128 v[24:27], v3 offset:0\nds_read_b128 v[28:31], v3 offset:1024\n" \
    "ds_read_b128 v[32:35], v3 offset:8192\nds_read_b128 v[36:39], v3 offset:9216\nds_read_b128 v[40:43], v3 offset:16384\nds_read_b128 v[44:47], v3 offset:17408\n" \
    "ds_read_b128 v[48:51], v3 offset:2048\nds_read_b128 v[52:55], v3 offset:3072\nds_read_b128 v[56:59], v3 offset:4096\nds_read_b128 v[60:63], v3 offset:5120\n" \
    "ds_read_b128 v[64:67], v3 offset:6144\nds_read_b128 v[68:71], v3 offset:7168\n.Lv21_%=:\ns_xor_b32 s90, s81, 0x800\n.Lv23_%=:\ns_memrealtime s[72:73]\n" \
    "s_waitcnt lgkmcnt(0)\ns_sub_u32 s72, s72, s80\ns_cmp_lt_i32 s72, 0\ns_cbranch_scc1 .Lv23_%=\nbuffer_load_dwordx4 v[72:75], v2, s[64:67], s81 offen offset:0 sc1\n" \
    "buffer_load_dwordx4 v[76:79], v2, s[64:67], s81 offen offset:1024 sc1\nbuffer_load_dwordx4 v[80:83], v2, s[64:67], s90 offen offset:0 sc1\n" \
    "buffer_load_dwordx4 v[84:87], v2, s[64:67], s90 offen offset:1024 sc1\nv_mov_b64 v[16:17], 0\nv_mov_b64 v[18:19], 0\nv_mov_b64 v[20:21], 0\nv_mov_b64 v[22:23], 0\n" \
    "s_waitcnt vmcnt(3)\nds_write_b128 v1, v[72:75] offset:24576\nv_smfmac_f32_16x16x64_bf16 v[20:23], v[72:75], v[24:31], v0\nv_max3_u32 v252, v72, v73, v74\n" \
    "v_smfmac_f32_16x16x64_bf16 v[16:19], v[72:75], a[0:7], v0\ns_waitcnt vmcnt(2)\nds_write_b128 v1, v[76:79] offset:28672\nv_max3_u32 v252, v252, v75, v76\n" \
    "v_max3_u32 v253, v77, v78, v79\nv_smfmac_f32_16x16x64_bf16 v[20:23], v[76:79], v[32:39], v0\ns_waitcnt vmcnt(1)\nds_write_b128 v1, v[80:83] offset:32768\n" \
    "v_max3_u32 v252, v252, v80, v81\nv_max3_u32 v253, v253, v82, v83\nv_smfmac_f32_16x16x64_bf16 v[16:19], v[76:79], a[32:39], v0\ns_waitcnt vmcnt(0)\n" \
    "ds_write_b128 v1, v[84:87] offset:36864\nv_max3_u32 v252, v252, v84, v85\nv_max3_u32 v253, v253, v86, v87\nv_max_u32 v252, v252, v253\nv_cmp_eq_u32 vcc, -1, v252\ns_nop 0\n" \
    "s_cbranch_vccnz .Lv22_%=\ns_waitcnt lgkmcnt(0)\ns_barrier\nds_read_b128 v[88:91], v1 offset:25600\nds_read_b128 v[24:27], v1 offset:26624\n" \
    "ds_read_b128 v[28:31], v1 offset:27648\nds_read_b128 v[32:35], v1 offset:29696\nds_read_b128 v[72:75], v3 offset:10240\nds_read_b128 v[76:79], v3 offset:11264\n" \
    "v_smfmac_f32_16x16x64_bf16 v[20:23], v[80:83], v[40:47], v0\nds_read_b128 v[36:39], v1 offset:30720\nv_smfmac_f32_16x16x64_bf16 v[16:19], v[80:83], a[64:71], v0\n" \
    "ds_read_b128 v[40:43], v3 offset:12288\nv_smfmac_f32_16x16x64_bf16 v[20:23], v[84:87], a[104:111], v0\nds_read_b128 v[44:47], v3 offset:13312\n" \
    "v_smfmac_f32_16x16x64_bf16 v[16:19], v[84:87], a[96:103], v0\nds_read_b128 v[80:83], v1 offset:31744\ns_waitcnt lgkmcnt(9)\n" \
    "v_smfmac_f32_16x16x64_bf16 v[20:23], v[88:91], v[48:55], v0\nds_read_b128 v[48:51], v3 offset:14336\nv_smfmac_f32_16x16x64_bf16 v[16:19], v[88:91], a[8:15], v0\n" \
    "ds_read_b128 v[52:55], v3 offset:15360\ns_waitcnt lgkmcnt(10)\nv_smfmac_f32_16x16x64_bf16 v[20:23], v[24:27], v[56:63], v0\nds_read_b128 v[56:59], v1 offset:33792\n" \
    "v_smfmac_f32_16x16x64_bf16 v[16:19], v[24:27], a[16:23], v0\nds_read_b128 v[84:87], v3 offset:18432\ns_waitcnt lgkmcnt(11)\n" \
    "v_smfmac_f32_16x16x64_bf16 v[20:23], v[28:31], v[64:71], v0\nds_read_b128 v[88:91], v3 offset:19456\nv_smfmac_f32_16x16x64_bf16 v[16:19], v[28:31], a[24:31], v0\n" \
    "ds_read_b128 v[24:27], v1 offset:34816\ns_waitcnt lgkmcnt(10)\nv_smfmac_f32_16x16x64_bf16 v[20:23], v[32:35], v[72:79], v0\nds_read_b128 v[60:63], v3 offset:20480\n" \
    "v_smfmac_f32_16x16x64_bf16 v[16:19], v[32:35], a[40:47], v0\nds_read_b128 v[64:67], v3 offset:21504\ns_waitcnt lgkmcnt(9)\n" \
    "v_smfmac_f32_16x16x64_bf16 v[20:23], v[36:39], v[40:47], v0\nds_read_b128 v[28:31], v1 offset:35840\nv_smfmac_f32_16x16x64_bf16 v[16:19], v[36:39], a[48:55], v0\n" \
    "ds_read_b128 v[32:35], v3 offset:22528\ns_waitcnt lgkmcnt(8)\nv_smfmac_f32_16x16x64_bf16 v[20:23], v[80:83], v[48:55], v0\nds_read_b128 v[36:39], v3 offset:23552\n" \
    "v_smfmac_f32_16x16x64_bf16 v[16:19], v[80:83], a[56:63], v0\nds_read_b128 v[40:43], v1 offset:37888\ns_waitcnt lgkmcnt(7)\n" \
    "v_smfmac_f32_16x16x64_bf16 v[20:23], v[56:59], v[84:91], v0\nds_read_b128 v[44:47], v1 offset:38912\ns_waitcnt lgkmcnt(5)\n" \
    "v_smfmac_f32_16x16x64_bf16 v[20:23], v[24:27], v[60:67], v0\nds_read_b128 v[48:51], v1 offset:39936\ns_waitcnt lgkmcnt(3)\n" \
    "v_smfmac_f32_16x16x64_bf16 v[20:23], v[28:31], v[32:39], v0\ns_waitcnt lgkmcnt(2)\nv_smfmac_f32_16x16x64_bf16 v[20:23], v[40:43], a[120:127], v0\ns_waitcnt lgkmcnt(1)\n" \
    "v_smfmac_f32_16x16x64_bf16 v[20:23], v[44:47], a[136:143], v0\ns_waitcnt lgkmcnt(0)\nv_smfmac_f32_16x16x64_bf16 v[20:23], v[48:51], a[152:159], v0\ns_nop 2\n" \
    "v_smfmac_f32_16x16x64_bf16 v[16:19], v[56:59], a[72:79], v0\ns_nop 2\nv_smfmac_f32_16x16x64_bf16 v[16:19], v[24:27], a[80:87], v0\nv_permlane32_swap_b32 v20, v22\n" \
    "v_smfmac_f32_16x16x64_bf16 v[16:19], v[28:31], a[88:95], v0\nv_permlane32_swap_b32 v21, v23\nv_smfmac_f32_16x16x64_bf16 v[16:19], v[40:43], a[112:119], v0\n" \
    "v_add_f32 v20, v20, v22\nv_smfmac_f32_16x16x64_bf16 v[16:19], v[44:47], a[128:135], v0\nv_add_f32 v21, v21, v23\n" \
    "v_smfmac_f32_16x16x64_bf16 v[16:19], v[48:51], a[144:151], v0\nv_cvt_pk_bf16_f32 v20, v20, v21\nds_write_b16 v4, v20 offset:8\nds_write_b16_d16_hi v4, v20 offset:24\n" \
    "v_mov_b32 v15, v15\nv_mov_b32 v15, v15\nv_mov_b32 v15, v15\nv_mov_b32 v15, v15\nv_mov_b32 v15, v15\nv_permlane32_swap_b32 v16, v18\nv_permlane32_swap_b32 v17, v19\n" \
    "v_pk_add_f32 v[16:17], v[16:17], v[18:19]\nv_cvt_pk_bf16_f32 v16, v16, v17\nds_write_b16 v4, v16 offset:0\nds_write_b16_d16_hi v4, v16 offset:16\nv_mov_b64 v[30:31], 0\n" \
    "v_mov_b64 v[28:29], 0\nv_mov_b64 v[26:27], 0\nv_mov_b64 v[24:25], 0\nv_mov_b64 v[70:71], 0\nv_mov_b64 v[68:69], 0\nv_mov_b64 v[66:67], 0\nv_mov_b64 v[64:65], 0\n" \
    "v_mov_b64 v[22:23], 0\nv_mov_b64 v[20:21], 0\nv_mov_b64 v[18:19], 0\nv_mov_b32 v15, v15\ns_waitcnt lgkmcnt(0)\ns_barrier\nv_mov_b64 v[16:17], 0\ns_branch .Lv7_%=\n" \
    ".Lv9_%=:\nds_read_b128 v[24:27], v3 offset:2048\nds_read_b128 v[28:31], v3 offset:3072\nds_read_b128 v[32:35], v3 offset:10240\nds_read_b128 v[36:39], v3 offset:11264\n" \
    "ds_read_b128 v[40:43], v3 offset:18432\nds_read_b128 v[44:47], v3 offset:19456\nds_read_b128 v[48:51], v3 offset:0\nds_read_b128 v[52:55], v3 offset:1024\n" \
    "ds_read_b128 v[56:59], v3 offset:4096\nds_read_b128 v[60:63], v3 offset:5120\nds_read_b128 v[64:67], v3 offset:6144\nds_read_b128 v[68:71], v3 offset:7168\n.Lv24_%=:\n" \
    "s_xor_b32 s90, s81, 0x800\n.Lv26_%=:\ns_memrealtime s[72:73]\ns_waitcnt lgkmcnt(0)\ns_sub_u32 s72, s72, s80\ns_cmp_lt_i32 s72, 0\ns_cbranch_scc1 .Lv26_%=\n" \
    "buffer_load_dwordx4 v[72:75], v2, s[64:67], s81 offen offset:0 sc1\nbuffer_load_dwordx4 v[76:79], v2, s[64:67], s81 offen offset:1024 sc1\n" \
    "buffer_load_dwordx4 v[80:83], v2, s[64:67], s90 offen offset:0 sc1\nbuffer_load_dwordx4 v[84:87], v2, s[64:67], s90 offen offset:1024 sc1\nv_mov_b64 v[16:17], 0\n" \
    "v_mov_b64 v[18:19], 0\nv_mov_b64 v[20:21], 0\nv_mov_b64 v[22:23], 0\ns_waitcnt vmcnt(3)\nds_write_b128 v1, v[72:75] offset:25600\n" \
    "v_smfmac_f32_16x16x64_bf16 v[20:23], v[72:75], v[24:31], v0\nv_max3_u32 v252, v72, v73, v74\nv_smfmac_f32_16x16x64_bf16 v[16:19], v[72:75], a[8:15], v0\n" \
    "s_waitcnt vmcnt(2)\nds_write_b128 v1, v[76:79] offset:29696\nv_max3_u32 v252, v252, v75, v76\nv_max3_u32 v253, v77, v78, v79\n" \
    "v_smfmac_f32_16x16x64_bf16 v[20:23], v[76:79], v[32:39], v0\ns_waitcnt vmcnt(1)\nds_write_b128 v1, v[80:83] offset:33792\nv_max3_u32 v252, v252, v80, v81\n" \
    "v_max3_u32 v253, v253, v82, v83\nv_smfmac_f32_16x16x64_bf16 v[16:19], v[76:79], a[40:47], v0\ns_waitcnt vmcnt(0)\nds_write_b128 v1, v[84:87] offset:37888\n" \
    "v_max3_u32 v252, v252, v84, v85\nv_max3_u32 v253, v253, v86, v87\nv_max_u32 v252, v252, v253\nv_cmp_eq_u32 vcc, -1, v252\ns_nop 0\ns_cbranch_vccnz .Lv25_%=\n" \
    "s_waitcnt lgkmcnt(0)\ns_barrier\nds_read_b128 v[88:91], v1 offset:24576\nds_read_b128 v[24:27], v1 offset:26624\nds_read_b128 v[28:31], v1 offset:27648\n" \
    "ds_read_b128 v[32:35], v1 offset:28672\nds_read_b128 v[72:75], v3 offset:8192\nds_read_b128 v[76:79], v3 offset:9216\n" \
    "v_smfmac_f32_16x16x64_bf16 v[20:23], v[80:83], v[40:47], v0\nds_read_b128 v[36:39], v1 offset:30720\nv_smfmac_f32_16x16x64_bf16 v[16:19], v[80:83], a[72:79], v0\n" \
    "ds_read_b128 v[40:43], v3 offset:12288\nv_smfmac_f32_16x16x64_bf16 v[20:23], v[84:87], a[120:127], v0\nds_read_b128 v[44:47], v3 offset:13312\n" \
    "v_smfmac_f32_16x16x64_bf16 v[16:19], v[84:87], a[112:119], v0\nds_read_b128 v[80:83], v1 offset:31744\ns_waitcnt lgkmcnt(9)\n" \
    "v_smfmac_f32_16x16x64_bf16 v[20:23], v[88:91], v[48:55], v0\nds_read_b128 v[48:51], v3 offset:14336\nv_smfmac_f32_16x16x64_bf16 v[16:19], v[88:91], a[0:7], v0\n" \
    "ds_read_b128 v[52:55], v3 offset:15360\ns_waitcnt lgkmcnt(10)\nv_smfmac_f32_16x16x64_bf16 v[20:23], v[24:27], v[56:63], v0\nds_read_b128 v[56:59], v1 offset:32768\n" \
    "v_smfmac_f32_16x16x64_bf16 v[16:19], v[24:27], a[16:23], v0\nds_read_b128 v[84:87], v3 offset:16384\ns_waitcnt lgkmcnt(11)\n" \
    "v_smfmac_f32_16x16x64_bf16 v[20:23], v[28:31], v[64:71], v0\nds_read_b128 v[88:91], v3 offset:17408\nv_smfmac_f32_16x16x64_bf16 v[16:19], v[28:31], a[24:31], v0\n" \
    "ds_read_b128 v[24:27], v1 offset:34816\ns_waitcnt lgkmcnt(10)\nv_smfmac_f32_16x16x64_bf16 v[20:23], v[32:35], v[72:79], v0\nds_read_b128 v[60:63], v3 offset:20480\n" \
    "v_smfmac_f32_16x16x64_bf16 v[16:19], v[32:35], a[32:39], v0\nds_read_b128 v[64:67], v3 offset:21504\ns_waitcnt lgkmcnt(9)\n" \
    "v_smfmac_f32_16x16x64_bf16 v[20:23], v[36:39], v[40:47], v0\nds_read_b128 v[28:31], v1 offset:35840\nv_smfmac_f32_16x16x64_bf16 v[16:19], v[36:39], a[48:55], v0\n" \
    "ds_read_b128 v[32:35], v3 offset:22528\ns_waitcnt lgkmcnt(8)\nv_smfmac_f32_16x16x64_bf16 v[20:23], v[80:83], v[48:55], v0\nds_read_b128 v[36:39], v3 offset:23552\n" \
    "v_smfmac_f32_16x16x64_bf16 v[16:19], v[80:83], a[56:63], v0\nds_read_b128 v[40:43], v1 offset:36864\ns_waitcnt lgkmcnt(7)\n" \
    "v_smfmac_f32_16x16x64_bf16 v[20:23], v[56:59], v[84:91], v0\nds_read_b128 v[44:47], v1 offset:38912\ns_waitcnt lgkmcnt(5)\n" \
    "v_smfmac_f32_16x16x64_bf16 v[20:23], v[24:27], v[60:67], v0\nds_read_b128 v[48:51], v1 offset:39936\ns_waitcnt lgkmcnt(3)\n" \
    "v_smfmac_f32_16x16x64_bf16 v[20:23], v[28:31], v[32:39], v0\ns_waitcnt lgkmcnt(2)\nv_smfmac_f32_16x16x64_bf16 v[20:23], v[40:43], a[104:111], v0\ns_waitcnt lgkmcnt(1)\n" \
    "v_smfmac_f32_16x16x64_bf16 v[20:23], v[44:47], a[136:143], v0\ns_waitcnt lgkmcnt(0)\nv_smfmac_f32_16x16x64_bf16 v[20:23], v[48:51], a[152:159], v0\ns_nop 2\n" \
    "v_smfmac_f32_16x16x64_bf16 v[16:19], v[56:59], a[64:71], v0\ns_nop 2\nv_smfmac_f32_16x16x64_bf16 v[16:19], v[24:27], a[80:87], v0\nv_permlane32_swap_b32 v20, v22\n" \
    "v_smfmac_f32_16x16x64_bf16 v[16:19], v[28:31], a[88:95], v0\nv_permlane32_swap_b32 v21, v23\nv_smfmac_f32_16x16x64_bf16 v[16:19], v[40:43], a[96:103], v0\n" \
    "v_add_f32 v20, v20, v22\nv_smfmac_f32_16x16x64_bf16 v[16:19], v[44:47], a[128:135], v0\nv_add_f32 v21, v21, v23\n" \
    "v_smfmac_f32_16x16x64_bf16 v[16:19], v[48:51], a[144:151], v0\nv_cvt_pk_bf16_f32 v20, v20, v21\nds_write_b16 v4, v20 offset:8\nds_write_b16_d16_hi v4, v20 offset:24\n" \
    "v_mov_b32 v15, v15\nv_mov_b32 v15, v15\nv_mov_b32 v15, v15\nv_mov_b32 v15, v15\nv_mov_b32 v15, v15\nv_permlane32_swap_b32 v16, v18\nv_permlane32_swap_b32 v17, v19\n" \
    "v_pk_add_f32 v[16:17], v[16:17], v[18:19]\nv_cvt_pk_bf16_f32 v16, v16, v17\nds_write_b16 v4, v16 offset:0\nds_write_b16_d16_hi v4, v16 offset:16\nv_mov_b64 v[30:31], 0\n" \
    "v_mov_b64 v[28:29], 0\nv_mov_b64 v[26:27], 0\nv_mov_b64 v[24:25], 0\nv_mov_b64 v[70:71], 0\nv_mov_b64 v[68:69], 0\nv_mov_b64 v[66:67], 0\nv_mov_b64 v[64:65], 0\n" \
    "v_mov_b64 v[22:23], 0\nv_mov_b64 v[20:21], 0\nv_mov_b64 v[18:19], 0\nv_mov_b32 v15, v15\ns_waitcnt lgkmcnt(0)\ns_barrier\nv_mov_b64 v[16:17], 0\ns_branch .Lv7_%=\n" \
    ".Lv10_%=:\nds_read_b128 v[24:27], v3 offset:4096\nds_read_b128 v[28:31], v3 offset:5120\nds_read_b128 v[32:35], v3 offset:12288\nds_read_b128 v[36:39], v3 offset:13312\n" \
    "ds_read_b128 v[40:43], v3 offset:20480\nds_read_b128 v[44:47], v3 offset:21504\nds_read_b128 v[48:51], v3 offset:0\nds_read_b128 v[52:55], v3 offset:1024\n" \
    "ds_read_b128 v[56:59], v3 offset:2048\nds_read_b128 v[60:63], v3 offset:3072\nds_read_b128 v[64:67], v3 offset:6144\nds_read_b128 v[68:71], v3 offset:7168\n.Lv27_%=:\n" \
    "s_xor_b32 s90, s81, 0x800\n.Lv29_%=:\ns_memrealtime s[72:73]\ns_waitcnt lgkmcnt(0)\ns_sub_u32 s72, s72, s80\ns_cmp_lt_i32 s72, 0\ns_cbranch_scc1 .Lv29_%=\n" \
    "buffer_load_dwordx4 v[72:75], v2, s[64:67], s81 offen offset:0 sc1\nbuffer_load_dwordx4 v[76:79], v2, s[64:67], s81 offen offset:1024 sc1\n" \
    "buffer_load_dwordx4 v[80:83], v2, s[64:67], s90 offen offset:0 sc1\nbuffer_load_dwordx4 v[84:87], v2, s[64:67], s90 offen offset:1024 sc1\nv_mov_b64 v[16:17], 0\n" \
    "v_mov_b64 v[18:19], 0\nv_mov_b64 v[20:21], 0\nv_mov_b64 v[22:23], 0\ns_waitcnt vmcnt(3)\nds_write_b128 v1, v[72:75] offset:26624\n" \
    "v_smfmac_f32_16x16x64_bf16 v[20:23], v[72:75], v[24:31], v0\nv_max3_u32 v252, v72, v73, v74\nv_smfmac_f32_16x16x64_bf16 v[16:19], v[72:75], a[16:23], v0\n" \
    "s_waitcnt vmcnt(2)\nds_write_b128 v1, v[76:79] offset:30720\nv_max3_u32 v252, v252, v75, v76\nv_max3_u32 v253, v77, v78, v79\n" \
    "v_smfmac_f32_16x16x64_bf16 v[20:23], v[76:79], v[32:39], v0\ns_waitcnt vmcnt(1)\nds_write_b128 v1, v[80:83] offset:34816\nv_max3_u32 v252, v252, v80, v81\n" \
    "v_max3_u32 v253, v253, v82, v83\nv_smfmac_f32_16x16x64_bf16 v[16:19], v[76:79], a[48:55], v0\ns_waitcnt vmcnt(0)\nds_write_b128 v1, v[84:87] offset:38912\n" \
    "v_max3_u32 v252, v252, v84, v85\nv_max3_u32 v253, v253, v86, v87\nv_max_u32 v252, v252, v253\nv_cmp_eq_u32 vcc, -1, v252\ns_nop 0\ns_cbranch_vccnz .Lv28_%=\n" \
    "s_waitcnt lgkmcnt(0)\ns_barrier\nds_read_b128 v[88:91], v1 offset:24576\nds_read_b128 v[24:27], v1 offset:25600\nds_read_b128 v[28:31], v1 offset:27648\n" \
    "ds_read_b128 v[32:35], v1 offset:28672\nds_read_b128 v[72:75], v3 offset:8192\nds_read_b128 v[76:79], v3 offset:9216\n" \
    "v_smfmac_f32_16x16x64_bf16 v[20:23], v[80:83], v[40:47], v0\nds_read_b128 v[36:39], v1 offset:29696\nv_smfmac_f32_16x16x64_bf16 v[16:19], v[80:83], a[80:87], v0\n" \
    "ds_read_b128 v[40:43], v3 offset:10240\nv_smfmac_f32_16x16x64_bf16 v[20:23], v[84:87], a[136:143], v0\nds_read_b128 v[44:47], v3 offset:11264\n" \
    "v_smfmac_f32_16x16x64_bf16 v[16:19], v[84:87], a[128:135], v0\nds_read_b128 v[80:83], v1 offset:31744\ns_waitcnt lgkmcnt(9)\n" \
    "v_smfmac_f32_16x16x64_bf16 v[20:23], v[88:91], v[48:55], v0\nds_read_b128 v[48:51], v3 offset:14336\nv_smfmac_f32_16x16x64_bf16 v[16:19], v[88:91], a[0:7], v0\n" \
    "ds_read_b128 v[52:55], v3 offset:15360\ns_waitcnt lgkmcnt(10)\nv_smfmac_f32_16x16x64_bf16 v[20:23], v[24:27], v[56:63], v0\nds_read_b128 v[56:59], v1 offset:32768\n" \
    "v_smfmac_f32_16x16x64_bf16 v[16:19], v[24:27], a[8:15], v0\nds_read_b128 v[84:87], v3 offset:16384\ns_waitcnt lgkmcnt(11)\n" \
    "v_smfmac_f32_16x16x64_bf16 v[20:23], v[28:31], v[64:71], v0\nds_read_b128 v[88:91], v3 offset:17408\nv_smfmac_f32_16x16x64_bf16 v[16:19], v[28:31], a[24:31], v0\n" \
    "ds_read_b128 v[24:27], v1 offset:33792\ns_waitcnt lgkmcnt(10)\nv_smfmac_f32_16x16x64_bf16 v[20:23], v[32:35], v[72:79], v0\nds_read_b128 v[60:63], v3 offset:18432\n" \
    "v_smfmac_f32_16x16x64_bf16 v[16:19], v[32:35], a[32:39], v0\nds_read_b128 v[64:67], v3 offset:19456\ns_waitcnt lgkmcnt(9)\n" \
    "v_smfmac_f32_16x16x64_bf16 v[20:23], v[36:39], v[40:47], v0\nds_read_b128 v[28:31], v1 offset:35840\nv_smfmac_f32_16x16x64_bf16 v[16:19], v[36:39], a[40:47], v0\n" \
    "ds_read_b128 v[32:35], v3 offset:22528\ns_waitcnt lgkmcnt(8)\nv_smfmac_f32_16x16x64_bf16 v[20:23], v[80:83], v[48:55], v0\nds_read_b128 v[36:39], v3 offset:23552\n" \
    "v_smfmac_f32_16x16x64_bf16 v[16:19], v[80:83], a[56:63], v0\nds_read_b128 v[40:43], v1 offset:36864\ns_waitcnt lgkmcnt(7)\n" \
    "v_smfmac_f32_16x16x64_bf16 v[20:23], v[56:59], v[84:91], v0\nds_read_b128 v[44:47], v1 offset:37888\ns_waitcnt lgkmcnt(5)\n" \
    "v_smfmac_f32_16x16x64_bf16 v[20:23], v[24:27], v[60:67], v0\nds_read_b128 v[48:51], v1 offset:39936\ns_waitcnt lgkmcnt(3)\n" \
    "v_smfmac_f32_16x16x64_bf16 v[20:23], v[28:31], v[32:39], v0\ns_waitcnt lgkmcnt(2)\nv_smfmac_f32_16x16x64_bf16 v[20:23], v[40:43], a[104:111], v0\ns_waitcnt lgkmcnt(1)\n" \
    "v_smfmac_f32_16x16x64_bf16 v[20:23], v[44:47], a[120:127], v0\ns_waitcnt lgkmcnt(0)\nv_smfmac_f32_16x16x64_bf16 v[20:23], v[48:51], a[152:159], v0\ns_nop 2\n" \
    "v_smfmac_f32_16x16x64_bf16 v[16:19], v[56:59], a[64:71], v0\ns_nop 2\nv_smfmac_f32_16x16x64_bf16 v[16:19], v[24:27], a[72:79], v0\nv_permlane32_swap_b32 v20, v22\n" \
    "v_smfmac_f32_16x16x64_bf16 v[16:19], v[28:31], a[88:95], v0\nv_permlane32_swap_b32 v21, v23\nv_smfmac_f32_16x16x64_bf16 v[16:19], v[40:43], a[96:103], v0\n" \
    "v_add_f32 v20, v20, v22\nv_smfmac_f32_16x16x64_bf16 v[16:19], v[44:47], a[112:119], v0\nv_add_f32 v21, v21, v23\n" \
    "v_smfmac_f32_16x16x64_bf16 v[16:19], v[48:51], a[144:151], v0\nv_cvt_pk_bf16_f32 v20, v20, v21\nds_write_b16 v4, v20 offset:8\nds_write_b16_d16_hi v4, v20 offset:24\n" \
    "v_mov_b32 v15, v15\nv_mov_b32 v15, v15\nv_mov_b32 v15, v15\nv_mov_b32 v15, v15\nv_mov_b32 v15, v15\nv_permlane32_swap_b32 v16, v18\nv_permlane32_swap_b32 v17, v19\n" \
    "v_pk_add_f32 v[16:17], v[16:17], v[18:19]\nv_cvt_pk_bf16_f32 v16, v16, v17\nds_write_b16 v4, v16 offset:0\nds_write_b16_d16_hi v4, v16 offset:16\nv_mov_b64 v[30:31], 0\n" \
    "v_mov_b64 v[28:29], 0\nv_mov_b64 v[26:27], 0\nv_mov_b64 v[24:25], 0\nv_mov_b64 v[70:71], 0\nv_mov_b64 v[68:69], 0\nv_mov_b64 v[66:67], 0\nv_mov_b64 v[64:65], 0\n" \
    "v_mov_b64 v[22:23], 0\nv_mov_b64 v[20:21], 0\nv_mov_b64 v[18:19], 0\nv_mov_b32 v15, v15\ns_waitcnt lgkmcnt(0)\ns_barrier\nv_mov_b64 v[16:17], 0\ns_branch .Lv7_%=\n" \
    ".Lv11_%=:\nds_read_b128 v[24:27], v3 offset:6144\nds_read_b128 v[28:31], v3 offset:7168\nds_read_b128 v[32:35], v3 offset:14336\nds_read_b128 v[36:39], v3 offset:15360\n" \
    "ds_read_b128 v[40:43], v3 offset:22528\nds_read_b128 v[44:47], v3 offset:23552\nds_read_b128 v[48:51], v3 offset:0\nds_read_b128 v[52:55], v3 offset:1024\n" \
    "ds_read_b128 v[56:59], v3 offset:2048\nds_read_b128 v[60:63], v3 offset:3072\nds_read_b128 v[64:67], v3 offset:4096\nds_read_b128 v[68:71], v3 offset:5120\n.Lv30_%=:\n" \
    "s_xor_b32 s90, s81, 0x800\n.Lv32_%=:\ns_memrealtime s[72:73]\ns_waitcnt lgkmcnt(0)\ns_sub_u32 s72, s72, s80\ns_cmp_lt_i32 s72, 0\ns_cbranch_scc1 .Lv32_%=\n" \
    "buffer_load_dwordx4 v[72:75], v2, s[64:67], s81 offen offset:0 sc1\nbuffer_load_dwordx4 v[76:79], v2, s[64:67], s81 offen offset:1024 sc1\n" \
    "buffer_load_dwordx4 v[80:83], v2, s[64:67], s90 offen offset:0 sc1\nbuffer_load_dwordx4 v[84:87], v2, s[64:67], s90 offen offset:1024 sc1\nv_mov_b64 v[16:17], 0\n" \
    "v_mov_b64 v[18:19], 0\nv_mov_b64 v[20:21], 0\nv_mov_b64 v[22:23], 0\ns_waitcnt vmcnt(3)\nds_write_b128 v1, v[72:75] offset:27648\n" \
    "v_smfmac_f32_16x16x64_bf16 v[20:23], v[72:75], v[24:31], v0\nv_max3_u32 v252, v72, v73, v74\nv_smfmac_f32_16x16x64_bf16 v[16:19], v[72:75], a[24:31], v0\n" \
    "s_waitcnt vmcnt(2)\nds_write_b128 v1, v[76:79] offset:31744\nv_max3_u32 v252, v252, v75, v76\nv_max3_u32 v253, v77, v78, v79\n" \
    "v_smfmac_f32_16x16x64_bf16 v[20:23], v[76:79], v[32:39], v0\ns_waitcnt vmcnt(1)\nds_write_b128 v1, v[80:83] offset:35840\nv_max3_u32 v252, v252, v80, v81\n" \
    "v_max3_u32 v253, v253, v82, v83\nv_smfmac_f32_16x16x64_bf16 v[16:19], v[76:79], a[56:63], v0\ns_waitcnt vmcnt(0)\nds_write_b128 v1, v[84:87] offset:39936\n" \
    "v_max3_u32 v252, v252, v84, v85\nv_max3_u32 v253, v253, v86, v87\nv_max_u32 v252, v252, v253\nv_cmp_eq_u32 vcc, -1, v252\ns_nop 0\ns_cbranch_vccnz .Lv31_%=\n" \
    "s_waitcnt lgkmcnt(0)\ns_barrier\nds_read_b128 v[88:91], v1 offset:24576\nds_read_b128 v[24:27], v1 offset:25600\nds_read_b128 v[28:31], v1 offset:26624\n" \
    "ds_read_b128 v[32:35], v1 offset:28672\nds_read_b128 v[72:75], v3 offset:8192\nds_read_b128 v[76:79], v3 offset:9216\n" \
    "v_smfmac_f32_16x16x64_bf16 v[20:23], v[80:83], v[40:47], v0\nds_read_b128 v[36:39], v1 offset:29696\nv_smfmac_f32_16x16x64_bf16 v[16:19], v[80:83], a[88:95], v0\n" \
    "ds_read_b128 v[40:43], v3 offset:10240\nv_smfmac_f32_16x16x64_bf16 v[20:23], v[84:87], a[152:159], v0\nds_read_b128 v[44:47], v3 offset:11264\n" \
    "v_smfmac_f32_16x16x64_bf16 v[16:19], v[84:87], a[144:151], v0\nds_read_b128 v[80:83], v1 offset:30720\ns_waitcnt lgkmcnt(9)\n" \
    "v_smfmac_f32_16x16x64_bf16 v[20:23], v[88:91], v[48:55], v0\nds_read_b128 v[48:51], v3 offset:12288\nv_smfmac_f32_16x16x64_bf16 v[16:19], v[88:91], a[0:7], v0\n" \
    "ds_read_b128 v[52:55], v3 offset:13312\ns_waitcnt lgkmcnt(10)\nv_smfmac_f32_16x16x64_bf16 v[20:23], v[24:27], v[56:63], v0\nds_read_b128 v[56:59], v1 offset:32768\n" \
    "v_smfmac_f32_16x16x64_bf16 v[16:19], v[24:27], a[8:15], v0\nds_read_b128 v[84:87], v3 offset:16384\ns_waitcnt lgkmcnt(11)\n" \
    "v_smfmac_f32_16x16x64_bf16 v[20:23], v[28:31], v[64:71], v0\nds_read_b128 v[88:91], v3 offset:17408\nv_smfmac_f32_16x16x64_bf16 v[16:19], v[28:31], a[16:23], v0\n" \
    "ds_read_b128 v[24:27], v1 offset:33792\ns_waitcnt lgkmcnt(10)\nv_smfmac_f32_16x16x64_bf16 v[20:23], v[32:35], v[72:79], v0\nds_read_b128 v[60:63], v3 offset:18432\n" \
    "v_smfmac_f32_16x16x64_bf16 v[16:19], v[32:35], a[32:39], v0\nds_read_b128 v[64:67], v3 offset:19456\ns_waitcnt lgkmcnt(9)\n" \
    "v_smfmac_f32_16x16x64_bf16 v[20:23], v[36:39], v[40:47], v0\nds_read_b128 v[28:31], v1 offset:34816\nv_smfmac_f32_16x16x64_bf16 v[16:19], v[36:39], a[40:47], v0\n" \
    "ds_read_b128 v[32:35], v3 offset:20480\ns_waitcnt lgkmcnt(8)\nv_smfmac_f32_16x16x64_bf16 v[20:23], v[80:83], v[48:55], v0\nds_read_b128 v[36:39], v3 offset:21504\n" \
    "v_smfmac_f32_16x16x64_bf16 v[16:19], v[80:83], a[48:55], v0\nds_read_b128 v[40:43], v1 offset:36864\ns_waitcnt lgkmcnt(7)\n" \
    "v_smfmac_f32_16x16x64_bf16 v[20:23], v[56:59], v[84:91], v0\nds_read_b128 v[44:47], v1 offset:37888\ns_waitcnt lgkmcnt(5)\n" \
    "v_smfmac_f32_16x16x64_bf16 v[20:23], v[24:27], v[60:67], v0\nds_read_b128 v[48:51], v1 offset:38912\ns_waitcnt lgkmcnt(3)\n" \
    "v_smfmac_f32_16x16x64_bf16 v[20:23], v[28:31], v[32:39], v0\ns_waitcnt lgkmcnt(2)\nv_smfmac_f32_16x16x64_bf16 v[20:23], v[40:43], a[104:111], v0\ns_waitcnt lgkmcnt(1)\n" \
    "v_smfmac_f32_16x16x64_bf16 v[20:23], v[44:47], a[120:127], v0\ns_waitcnt lgkmcnt(0)\nv_smfmac_f32_16x16x64_bf16 v[20:23], v[48:51], a[136:143], v0\ns_nop 2\n" \
    "v_smfmac_f32_16x16x64_bf16 v[16:19], v[56:59], a[64:71], v0\ns_nop 2\nv_smfmac_f32_16x16x64_bf16 v[16:19], v[24:27], a[72:79], v0\nv_permlane32_swap_b32 v20, v22\n" \
    "v_smfmac_f32_16x16x64_bf16 v[16:19], v[28:31], a[80:87], v0\nv_permlane32_swap_b32 v21, v23\nv_smfmac_f32_16x16x64_bf16 v[16:19], v[40:43], a[96:103], v0\n" \
    "v_add_f32 v20, v20, v22\nv_smfmac_f32_16x16x64_bf16 v[16:19], v[44:47], a[112:119], v0\nv_add_f32 v21, v21, v23\n" \
    "v_smfmac_f32_16x16x64_bf16 v[16:19], v[48:51], a[128:135], v0\nv_cvt_pk_bf16_f32 v20, v20, v21\nds_write_b16 v4, v20 offset:8\nds_write_b16_d16_hi v4, v20 offset:24\n" \
    "v_mov_b32 v15, v15\nv_mov_b32 v15, v15\nv_mov_b32 v15, v15\nv_mov_b32 v15, v15\nv_mov_b32 v15, v15\nv_permlane32_swap_b32 v16, v18\nv_permlane32_swap_b32 v17, v19\n" \
    "v_pk_add_f32 v[16:17], v[16:17], v[18:19]\nv_cvt_pk_bf16_f32 v16, v16, v17\nds_write_b16 v4, v16 offset:0\nds_write_b16_d16_hi v4, v16 offset:16\nv_mov_b64 v[30:31], 0\n" \
    "v_mov_b64 v[28:29], 0\nv_mov_b64 v[26:27], 0\nv_mov_b64 v[24:25], 0\nv_mov_b64 v[70:71], 0\nv_mov_b64 v[68:69], 0\nv_mov_b64 v[66:67], 0\nv_mov_b64 v[64:65], 0\n" \
    "v_mov_b64 v[22:23], 0\nv_mov_b64 v[20:21], 0\nv_mov_b64 v[18:19], 0\nv_mov_b32 v15, v15\ns_waitcnt lgkmcnt(0)\ns_barrier\nv_mov_b64 v[16:17], 0\n.Lv7_%=:\n" \
    "ds_read_b128 v[32:35], v5 offset:0\nv_mov_b64 v[48:49], 0\nds_read_b128 v[36:39], v5 offset:512\nv_mov_b64 v[50:51], 0\nds_read_b128 v[40:43], v5 offset:1024\n" \
    "v_mov_b64 v[52:53], 0\nds_read_b128 v[44:47], v5 offset:1536\nv_mov_b64 v[54:55], 0\ns_waitcnt lgkmcnt(3)\nv_smfmac_f32_16x16x64_bf16 v[16:19], v[32:35], a[160:167], v0\n" \
    "v_smfmac_f32_16x16x64_bf16 v[20:23], v[32:35], a[176:183], v0\ns_waitcnt lgkmcnt(2)\nv_smfmac_f32_16x16x64_bf16 v[16:19], v[36:39], a[168:175], v0\n" \
    "v_smfmac_f32_16x16x64_bf16 v[20:23], v[36:39], a[184:191], v0\ns_waitcnt lgkmcnt(1)\nv_smfmac_f32_16x16x64_bf16 v[16:19], v[40:43], a[192:199], v0\n" \
    "v_smfmac_f32_16x16x64_bf16 v[20:23], v[40:43], a[208:215], v0\ns_waitcnt lgkmcnt(0)\nv_smfmac_f32_16x16x64_bf16 v[16:19], v[44:47], a[200:207], v0\n" \
    "v_smfmac_f32_16x16x64_bf16 v[20:23], v[44:47], a[216:223], v0\ns_nop 2\nv_smfmac_f32_16x16x64_bf16 v[24:27], v[32:35], a[224:231], v0\ns_nop 2\n" \
    "v_cvt_pk_bf16_f32 v72, v16, v17\nv_smfmac_f32_16x16x64_bf16 v[28:31], v[32:35], a[240:247], v0\nv_cvt_pk_bf16_f32 v74, v18, v19\n" \
    "v_smfmac_f32_16x16x64_bf16 v[24:27], v[36:39], a[232:239], v0\nv_cvt_pk_bf16_f32 v73, v20, v21\nv_smfmac_f32_16x16x64_bf16 v[28:31], v[36:39], a[248:255], v0\n" \
    "v_cvt_pk_bf16_f32 v75, v22, v23\nv_smfmac_f32_16x16x64_bf16 v[24:27], v[40:43], v[92:99], v0\nv_permlane16_swap_b32 v72, v73\n" \
    "v_smfmac_f32_16x16x64_bf16 v[28:31], v[40:43], v[108:115], v0\nv_permlane16_swap_b32 v74, v75\nv_smfmac_f32_16x16x64_bf16 v[24:27], v[44:47], v[100:107], v0\n" \
    "buffer_store_dwordx4 v[72:75], v7, s[64:67], s86 offen offset:0 sc1\nv_smfmac_f32_16x16x64_bf16 v[28:31], v[44:47], v[116:123], v0\ns_nop 2\n" \
    "v_smfmac_f32_16x16x64_bf16 v[64:67], v[32:35], v[124:131], v0\ns_nop 1\nv_cvt_pk_bf16_f32 v76, v24, v25\nv_smfmac_f32_16x16x64_bf16 v[68:71], v[32:35], v[140:147], v0\n" \
    "v_cvt_pk_bf16_f32 v78, v26, v27\nv_smfmac_f32_16x16x64_bf16 v[64:67], v[36:39], v[132:139], v0\nv_cvt_pk_bf16_f32 v77, v28, v29\n" \
    "v_smfmac_f32_16x16x64_bf16 v[68:71], v[36:39], v[148:155], v0\nv_cvt_pk_bf16_f32 v79, v30, v31\nv_smfmac_f32_16x16x64_bf16 v[64:67], v[40:43], v[156:163], v0\n" \
    "v_permlane16_swap_b32 v76, v77\nv_smfmac_f32_16x16x64_bf16 v[68:71], v[40:43], v[172:179], v0\nv_permlane16_swap_b32 v78, v79\n" \
    "v_smfmac_f32_16x16x64_bf16 v[64:67], v[44:47], v[164:171], v0\nbuffer_store_dwordx4 v[76:79], v7, s[64:67], s87 offen offset:0 sc1\n" \
    "v_smfmac_f32_16x16x64_bf16 v[68:71], v[44:47], v[180:187], v0\ns_nop 2\nv_smfmac_f32_16x16x64_bf16 v[48:51], v[32:35], v[188:195], v0\ns_nop 1\n" \
    "v_cvt_pk_bf16_f32 v72, v64, v65\nv_smfmac_f32_16x16x64_bf16 v[52:55], v[32:35], v[204:211], v0\nv_cvt_pk_bf16_f32 v74, v66, v67\n" \
    "v_smfmac_f32_16x16x64_bf16 v[48:51], v[36:39], v[196:203], v0\nv_cvt_pk_bf16_f32 v73, v68, v69\nv_smfmac_f32_16x16x64_bf16 v[52:55], v[36:39], v[212:219], v0\n" \
    "v_cvt_pk_bf16_f32 v75, v70, v71\nv_smfmac_f32_16x16x64_bf16 v[48:51], v[40:43], v[220:227], v0\nv_permlane16_swap_b32 v72, v73\n" \
    "v_smfmac_f32_16x16x64_bf16 v[52:55], v[40:43], v[236:243], v0\nv_permlane16_swap_b32 v74, v75\nv_smfmac_f32_16x16x64_bf16 v[48:51], v[44:47], v[228:235], v0\n" \
    "buffer_store_dwordx4 v[72:75], v7, s[64:67], s88 offen offset:0 sc1\nv_smfmac_f32_16x16x64_bf16 v[52:55], v[44:47], v[244:251], v0\ns_nop 5\n" \
    "v_cvt_pk_bf16_f32 v76, v48, v49\nv_cvt_pk_bf16_f32 v78, v50, v51\nv_cvt_pk_bf16_f32 v77, v52, v53\nv_cvt_pk_bf16_f32 v79, v54, v55\ns_nop 0\n" \
    "v_permlane16_swap_b32 v76, v77\nv_permlane16_swap_b32 v78, v79\nbuffer_store_dwordx4 v[76:79], v7, s[64:67], s89 offen offset:0 sc1\ns_add_u32 s35, s35, 2\n" \
    "s_cmp_lt_u32 s35, 62\ns_cbranch_scc1 .Lv5_%=\ns_cmp_lt_u32 s35, 64\ns_cbranch_scc0 .Lv6_%=\nds_read_b128 v[56:59], v6 offset:0\nds_read_b128 v[60:63], v6 offset:1024\n" \
    "ds_read_b128 v[64:67], v6 offset:2048\ns_and_b32 s69, s50, 15\ns_lshl_b32 s69, s69, 21\ns_add_u32 s86, s39, s69\ns_lshr_b32 s69, s86, 8\ns_and_b32 s69, s69, 0x2000\n" \
    "s_xor_b32 s86, s86, s69\ns_add_u32 s87, s86, 0x40000\ns_add_u32 s88, s86, 0x80000\ns_add_u32 s89, s86, 0xc0000\ns_add_u32 s50, s50, 2\nv_mov_b64 v[32:33], 0\n" \
    "v_mov_b64 v[34:35], 0\nv_mov_b64 v[36:37], 0\nv_mov_b64 v[38:39], 0\nv_mov_b64 v[40:41], 0\nv_mov_b64 v[42:43], 0\nv_mov_b64 v[44:45], 0\nv_mov_b64 v[46:47], 0\n" \
    "s_waitcnt lgkmcnt(2)\nv_smfmac_f32_16x16x64_bf16 v[32:35], v[56:59], a[160:167], v0\nds_read_b128 v[68:71], v6 offset:3072\n" \
    "v_smfmac_f32_16x16x64_bf16 v[36:39], v[56:59], a[176:183], v0\ns_waitcnt lgkmcnt(2)\nv_smfmac_f32_16x16x64_bf16 v[32:35], v[60:63], a[168:175], v0\n" \
    "v_smfmac_f32_16x16x64_bf16 v[36:39], v[60:63], a[184:191], v0\ns_waitcnt lgkmcnt(1)\nv_smfmac_f32_16x16x64_bf16 v[32:35], v[64:67], a[192:199], v0\n" \
    "v_smfmac_f32_16x16x64_bf16 v[36:39], v[64:67], a[208:215], v0\ns_waitcnt lgkmcnt(0)\nv_smfmac_f32_16x16x64_bf16 v[32:35], v[68:71], a[200:207], v0\n" \
    "v_smfmac_f32_16x16x64_bf16 v[36:39], v[68:71], a[216:223], v0\ns_nop 2\nv_smfmac_f32_16x16x64_bf16 v[40:43], v[56:59], a[224:231], v0\ns_nop 2\n" \
    "v_cvt_pk_bf16_f32 v48, v32, v33\nv_cvt_pk_bf16_f32 v50, v34, v35\nv_smfmac_f32_16x16x64_bf16 v[44:47], v[56:59], a[240:247], v0\nv_cvt_pk_bf16_f32 v49, v36, v37\n" \
    "v_cvt_pk_bf16_f32 v51, v38, v39\nv_smfmac_f32_16x16x64_bf16 v[40:43], v[60:63], a[232:239], v0\nv_permlane16_swap_b32 v48, v49\n" \
    "v_smfmac_f32_16x16x64_bf16 v[44:47], v[60:63], a[248:255], v0\nv_permlane16_swap_b32 v50, v51\nv_smfmac_f32_16x16x64_bf16 v[40:43], v[64:67], v[92:99], v0\n" \
    "buffer_store_dwordx4 v[48:51], v8, s[64:67], s86 offen offset:0 sc1\nv_mov_b64 v[32:33], 0\nv_mov_b64 v[34:35], 0\n" \
    "v_smfmac_f32_16x16x64_bf16 v[44:47], v[64:67], v[108:115], v0\nv_mov_b64 v[36:37], 0\nv_mov_b64 v[38:39], 0\nv_smfmac_f32_16x16x64_bf16 v[40:43], v[68:71], v[100:107], v0\n" \
    "v_smfmac_f32_16x16x64_bf16 v[44:47], v[68:71], v[116:123], v0\ns_nop 2\nv_smfmac_f32_16x16x64_bf16 v[32:35], v[56:59], v[124:131], v0\ns_nop 2\n" \
    "v_cvt_pk_bf16_f32 v52, v40, v41\nv_cvt_pk_bf16_f32 v54, v42, v43\nv_smfmac_f32_16x16x64_bf16 v[36:39], v[56:59], v[140:147], v0\nv_cvt_pk_bf16_f32 v53, v44, v45\n" \
    "v_cvt_pk_bf16_f32 v55, v46, v47\nv_smfmac_f32_16x16x64_bf16 v[32:35], v[60:63], v[132:139], v0\nv_permlane16_swap_b32 v52, v53\n" \
    "v_smfmac_f32_16x16x64_bf16 v[36:39], v[60:63], v[148:155], v0\nv_permlane16_swap_b32 v54, v55\nv_smfmac_f32_16x16x64_bf16 v[32:35], v[64:67], v[156:163], v0\n" \
    "buffer_store_dwordx4 v[52:55], v8, s[64:67], s87 offen offset:0 sc1\nv_mov_b64 v[40:41], 0\nv_mov_b64 v[42:43], 0\n" \
    "v_smfmac_f32_16x16x64_bf16 v[36:39], v[64:67], v[172:179], v0\nv_mov_b64 v[44:45], 0\nv_mov_b64 v[46:47], 0\nv_smfmac_f32_16x16x64_bf16 v[32:35], v[68:71], v[164:171], v0\n" \
    "v_smfmac_f32_16x16x64_bf16 v[36:39], v[68:71], v[180:187], v0\ns_nop 2\nv_smfmac_f32_16x16x64_bf16 v[40:43], v[56:59], v[188:195], v0\ns_nop 2\n" \
    "v_cvt_pk_bf16_f32 v48, v32, v33\nv_cvt_pk_bf16_f32 v50, v34, v35\nv_smfmac_f32_16x16x64_bf16 v[44:47], v[56:59], v[204:211], v0\nv_cvt_pk_bf16_f32 v49, v36, v37\n" \
    "v_cvt_pk_bf16_f32 v51, v38, v39\nv_smfmac_f32_16x16x64_bf16 v[40:43], v[60:63], v[196:203], v0\nds_read_b128 v[56:59], v6 offset:4096\nv_permlane16_swap_b32 v48, v49\n" \
    "v_smfmac_f32_16x16x64_bf16 v[44:47], v[60:63], v[212:219], v0\nv_permlane16_swap_b32 v50, v51\nv_smfmac_f32_16x16x64_bf16 v[40:43], v[64:67], v[220:227], v0\n" \
    "ds_read_b128 v[60:63], v6 offset:5120\nbuffer_store_dwordx4 v[48:51], v8, s[64:67], s88 offen offset:0 sc1\nv_mov_b64 v[32:33], 0\nv_mov_b64 v[34:35], 0\n" \
    "v_smfmac_f32_16x16x64_bf16 v[44:47], v[64:67], v[236:243], v0\nv_mov_b64 v[36:37], 0\nv_mov_b64 v[38:39], 0\nv_smfmac_f32_16x16x64_bf16 v[40:43], v[68:71], v[228:235], v0\n" \
    "ds_read_b128 v[64:67], v6 offset:6144\nv_smfmac_f32_16x16x64_bf16 v[44:47], v[68:71], v[244:251], v0\ns_nop 2\ns_waitcnt lgkmcnt(2)\n" \
    "v_smfmac_f32_16x16x64_bf16 v[32:35], v[56:59], a[160:167], v0\nds_read_b128 v[68:71], v6 offset:7168\nv_cvt_pk_bf16_f32 v52, v40, v41\nv_cvt_pk_bf16_f32 v54, v42, v43\n" \
    "v_smfmac_f32_16x16x64_bf16 v[36:39], v[56:59], a[176:183], v0\ns_waitcnt lgkmcnt(2)\nv_cvt_pk_bf16_f32 v53, v44, v45\nv_cvt_pk_bf16_f32 v55, v46, v47\n" \
    "v_smfmac_f32_16x16x64_bf16 v[32:35], v[60:63], a[168:175], v0\nv_permlane16_swap_b32 v52, v53\nv_smfmac_f32_16x16x64_bf16 v[36:39], v[60:63], a[184:191], v0\n" \
    "s_waitcnt lgkmcnt(1)\nv_permlane16_swap_b32 v54, v55\nv_smfmac_f32_16x16x64_bf16 v[32:35], v[64:67], a[192:199], v0\n" \
    "buffer_store_dwordx4 v[52:55], v8, s[64:67], s89 offen offset:0 sc1\nv_mov_b64 v[40:41], 0\nv_mov_b64 v[42:43], 0\n" \
    "v_smfmac_f32_16x16x64_bf16 v[36:39], v[64:67], a[208:215], v0\ns_waitcnt lgkmcnt(0)\nv_mov_b64 v[44:45], 0\nv_mov_b64 v[46:47], 0\n" \
    "v_smfmac_f32_16x16x64_bf16 v[32:35], v[68:71], a[200:207], v0\nv_smfmac_f32_16x16x64_bf16 v[36:39], v[68:71], a[216:223], v0\ns_nop 2\n" \
    "v_smfmac_f32_16x16x64_bf16 v[40:43], v[56:59], a[224:231], v0\ns_nop 2\nv_cvt_pk_bf16_f32 v48, v32, v33\nv_cvt_pk_bf16_f32 v50, v34, v35\n" \
    "v_smfmac_f32_16x16x64_bf16 v[44:47], v[56:59], a[240:247], v0\nv_cvt_pk_bf16_f32 v49, v36, v37\nv_cvt_pk_bf16_f32 v51, v38, v39\n" \
    "v_smfmac_f32_16x16x64_bf16 v[40:43], v[60:63], a[232:239], v0\nv_permlane16_swap_b32 v48, v49\nv_smfmac_f32_16x16x64_bf16 v[44:47], v[60:63], a[248:255], v0\n" \
    "v_permlane16_swap_b32 v50, v51\nv_smfmac_f32_16x16x64_bf16 v[40:43], v[64:67], v[92:99], v0\nbuffer_store_dwordx4 v[48:51], v8, s[64:67], s86 offen offset:256 sc1\n" \
    "v_mov_b64 v[32:33], 0\nv_mov_b64 v[34:35], 0\nv_smfmac_f32_16x16x64_bf16 v[44:47], v[64:67], v[108:115], v0\nv_mov_b64 v[36:37], 0\nv_mov_b64 v[38:39], 0\n" \
    "v_smfmac_f32_16x16x64_bf16 v[40:43], v[68:71], v[100:107], v0\nv_smfmac_f32_16x16x64_bf16 v[44:47], v[68:71], v[116:123], v0\ns_nop 2\n" \
    "v_smfmac_f32_16x16x64_bf16 v[32:35], v[56:59], v[124:131], v0\ns_nop 2\nv_cvt_pk_bf16_f32 v52, v40, v41\nv_cvt_pk_bf16_f32 v54, v42, v43\n" \
    "v_smfmac_f32_16x16x64_bf16 v[36:39], v[56:59], v[140:147], v0\nv_cvt_pk_bf16_f32 v53, v44, v45\nv_cvt_pk_bf16_f32 v55, v46, v47\n" \
    "v_smfmac_f32_16x16x64_bf16 v[32:35], v[60:63], v[132:139], v0\nv_permlane16_swap_b32 v52, v53\nv_smfmac_f32_16x16x64_bf16 v[36:39], v[60:63], v[148:155], v0\n" \
    "v_permlane16_swap_b32 v54, v55\nv_smfmac_f32_16x16x64_bf16 v[32:35], v[64:67], v[156:163], v0\nbuffer_store_dwordx4 v[52:55], v8, s[64:67], s87 offen offset:256 sc1\n" \
    "v_mov_b64 v[40:41], 0\nv_mov_b64 v[42:43], 0\nv_smfmac_f32_16x16x64_bf16 v[36:39], v[64:67], v[172:179], v0\nv_mov_b64 v[44:45], 0\nv_mov_b64 v[46:47], 0\n" \
    "v_smfmac_f32_16x16x64_bf16 v[32:35], v[68:71], v[164:171], v0\nv_smfmac_f32_16x16x64_bf16 v[36:39], v[68:71], v[180:187], v0\ns_nop 2\n" \
    "v_smfmac_f32_16x16x64_bf16 v[40:43], v[56:59], v[188:195], v0\ns_nop 2\nv_cvt_pk_bf16_f32 v48, v32, v33\nv_cvt_pk_bf16_f32 v50, v34, v35\n" \
    "v_smfmac_f32_16x16x64_bf16 v[44:47], v[56:59], v[204:211], v0\nv_cvt_pk_bf16_f32 v49, v36, v37\nv_cvt_pk_bf16_f32 v51, v38, v39\n" \
    "v_smfmac_f32_16x16x64_bf16 v[40:43], v[60:63], v[196:203], v0\nds_read_b128 v[56:59], v6 offset:128\nv_permlane16_swap_b32 v48, v49\n" \
    "v_smfmac_f32_16x16x64_bf16 v[44:47], v[60:63], v[212:219], v0\nv_permlane16_swap_b32 v50, v51\nv_smfmac_f32_16x16x64_bf16 v[40:43], v[64:67], v[220:227], v0\n" \
    "ds_read_b128 v[60:63], v6 offset:1152\nbuffer_store_dwordx4 v[48:51], v8, s[64:67], s88 offen offset:256 sc1\nv_mov_b64 v[32:33], 0\nv_mov_b64 v[34:35], 0\n" \
    "v_smfmac_f32_16x16x64_bf16 v[44:47], v[64:67], v[236:243], v0\nv_mov_b64 v[36:37], 0\nv_mov_b64 v[38:39], 0\nv_smfmac_f32_16x16x64_bf16 v[40:43], v[68:71], v[228:235], v0\n" \
    "ds_read_b128 v[64:67], v6 offset:2176\nv_smfmac_f32_16x16x64_bf16 v[44:47], v[68:71], v[244:251], v0\ns_nop 2\ns_waitcnt lgkmcnt(2)\n" \
    "v_smfmac_f32_16x16x64_bf16 v[32:35], v[56:59], a[160:167], v0\nds_read_b128 v[68:71], v6 offset:3200\nv_cvt_pk_bf16_f32 v52, v40, v41\nv_cvt_pk_bf16_f32 v54, v42, v43\n" \
    "v_smfmac_f32_16x16x64_bf16 v[36:39], v[56:59], a[176:183], v0\ns_waitcnt lgkmcnt(2)\nv_cvt_pk_bf16_f32 v53, v44, v45\nv_cvt_pk_bf16_f32 v55, v46, v47\n" \
    "v_smfmac_f32_16x16x64_bf16 v[32:35], v[60:63], a[168:175], v0\nv_permlane16_swap_b32 v52, v53\nv_smfmac_f32_16x16x64_bf16 v[36:39], v[60:63], a[184:191], v0\n" \
    "s_waitcnt lgkmcnt(1)\nv_permlane16_swap_b32 v54, v55\nv_smfmac_f32_16x16x64_bf16 v[32:35], v[64:67], a[192:199], v0\n" \
    "buffer_store_dwordx4 v[52:55], v8, s[64:67], s89 offen offset:256 sc1\nv_mov_b64 v[40:41], 0\nv_mov_b64 v[42:43], 0\n" \
    "v_smfmac_f32_16x16x64_bf16 v[36:39], v[64:67], a[208:215], v0\ns_waitcnt lgkmcnt(0)\nv_mov_b64 v[44:45], 0\nv_mov_b64 v[46:47], 0\n" \
    "v_smfmac_f32_16x16x64_bf16 v[32:35], v[68:71], a[200:207], v0\nv_smfmac_f32_16x16x64_bf16 v[36:39], v[68:71], a[216:223], v0\ns_nop 2\n" \
    "v_smfmac_f32_16x16x64_bf16 v[40:43], v[56:59], a[224:231], v0\ns_nop 2\nv_cvt_pk_bf16_f32 v48, v32, v33\nv_cvt_pk_bf16_f32 v50, v34, v35\n" \
    "v_smfmac_f32_16x16x64_bf16 v[44:47], v[56:59], a[240:247], v0\nv_cvt_pk_bf16_f32 v49, v36, v37\nv_cvt_pk_bf16_f32 v51, v38, v39\n" \
    "v_smfmac_f32_16x16x64_bf16 v[40:43], v[60:63], a[232:239], v0\nv_permlane16_swap_b32 v48, v49\nv_smfmac_f32_16x16x64_bf16 v[44:47], v[60:63], a[248:255], v0\n" \
    "v_permlane16_swap_b32 v50, v51\nv_smfmac_f32_16x16x64_bf16 v[40:43], v[64:67], v[92:99], v0\nbuffer_store_dwordx4 v[48:51], v8, s[64:67], s86 offen offset:512 sc1\n" \
    "v_mov_b64 v[32:33], 0\nv_mov_b64 v[34:35], 0\nv_smfmac_f32_16x16x64_bf16 v[44:47], v[64:67], v[108:115], v0\nv_mov_b64 v[36:37], 0\nv_mov_b64 v[38:39], 0\n" \
    "v_smfmac_f32_16x16x64_bf16 v[40:43], v[68:71], v[100:107], v0\nv_smfmac_f32_16x16x64_bf16 v[44:47], v[68:71], v[116:123], v0\ns_nop 2\n" \
    "v_smfmac_f32_16x16x64_bf16 v[32:35], v[56:59], v[124:131], v0\ns_nop 2\nv_cvt_pk_bf16_f32 v52, v40, v41\nv_cvt_pk_bf16_f32 v54, v42, v43\n" \
    "v_smfmac_f32_16x16x64_bf16 v[36:39], v[56:59], v[140:147], v0\nv_cvt_pk_bf16_f32 v53, v44, v45\nv_cvt_pk_bf16_f32 v55, v46, v47\n" \
    "v_smfmac_f32_16x16x64_bf16 v[32:35], v[60:63], v[132:139], v0\nv_permlane16_swap_b32 v52, v53\nv_smfmac_f32_16x16x64_bf16 v[36:39], v[60:63], v[148:155], v0\n" \
    "v_permlane16_swap_b32 v54, v55\nv_smfmac_f32_16x16x64_bf16 v[32:35], v[64:67], v[156:163], v0\nbuffer_store_dwordx4 v[52:55], v8, s[64:67], s87 offen offset:512 sc1\n" \
    "v_mov_b64 v[40:41], 0\nv_mov_b64 v[42:43], 0\nv_smfmac_f32_16x16x64_bf16 v[36:39], v[64:67], v[172:179], v0\nv_mov_b64 v[44:45], 0\nv_mov_b64 v[46:47], 0\n" \
    "v_smfmac_f32_16x16x64_bf16 v[32:35], v[68:71], v[164:171], v0\nv_smfmac_f32_16x16x64_bf16 v[36:39], v[68:71], v[180:187], v0\ns_nop 2\n" \
    "v_smfmac_f32_16x16x64_bf16 v[40:43], v[56:59], v[188:195], v0\ns_nop 2\nv_cvt_pk_bf16_f32 v48, v32, v33\nv_cvt_pk_bf16_f32 v50, v34, v35\n" \
    "v_smfmac_f32_16x16x64_bf16 v[44:47], v[56:59], v[204:211], v0\nv_cvt_pk_bf16_f32 v49, v36, v37\nv_cvt_pk_bf16_f32 v51, v38, v39\n" \
    "v_smfmac_f32_16x16x64_bf16 v[40:43], v[60:63], v[196:203], v0\nds_read_b128 v[56:59], v6 offset:4224\nv_permlane16_swap_b32 v48, v49\n" \
    "v_smfmac_f32_16x16x64_bf16 v[44:47], v[60:63], v[212:219], v0\nv_permlane16_swap_b32 v50, v51\nv_smfmac_f32_16x16x64_bf16 v[40:43], v[64:67], v[220:227], v0\n" \
    "ds_read_b128 v[60:63], v6 offset:5248\nbuffer_store_dwordx4 v[48:51], v8, s[64:67], s88 offen offset:512 sc1\nv_mov_b64 v[32:33], 0\nv_mov_b64 v[34:35], 0\n" \
    "v_smfmac_f32_16x16x64_bf16 v[44:47], v[64:67], v[236:243], v0\nv_mov_b64 v[36:37], 0\nv_mov_b64 v[38:39], 0\nv_smfmac_f32_16x16x64_bf16 v[40:43], v[68:71], v[228:235], v0\n" \
    "ds_read_b128 v[64:67], v6 offset:6272\nv_smfmac_f32_16x16x64_bf16 v[44:47], v[68:71], v[244:251], v0\ns_nop 2\ns_waitcnt lgkmcnt(2)\n" \
    "v_smfmac_f32_16x16x64_bf16 v[32:35], v[56:59], a[160:167], v0\nds_read_b128 v[68:71], v6 offset:7296\nv_cvt_pk_bf16_f32 v52, v40, v41\nv_cvt_pk_bf16_f32 v54, v42, v43\n" \
    "v_smfmac_f32_16x16x64_bf16 v[36:39], v[56:59], a[176:183], v0\ns_waitcnt lgkmcnt(2)\nv_cvt_pk_bf16_f32 v53, v44, v45\nv_cvt_pk_bf16_f32 v55, v46, v47\n" \
    "v_smfmac_f32_16x16x64_bf16 v[32:35], v[60:63], a[168:175], v0\nv_permlane16_swap_b32 v52, v53\nv_smfmac_f32_16x16x64_bf16 v[36:39], v[60:63], a[184:191], v0\n" \
    "s_waitcnt lgkmcnt(1)\nv_permlane16_swap_b32 v54, v55\nv_smfmac_f32_16x16x64_bf16 v[32:35], v[64:67], a[192:199], v0\n" \
    "buffer_store_dwordx4 v[52:55], v8, s[64:67], s89 offen offset:512 sc1\nv_mov_b64 v[40:41], 0\nv_mov_b64 v[42:43], 0\n" \
    "v_smfmac_f32_16x16x64_bf16 v[36:39], v[64:67], a[208:215], v0\ns_waitcnt lgkmcnt(0)\nv_mov_b64 v[44:45], 0\nv_mov_b64 v[46:47], 0\n" \
    "v_smfmac_f32_16x16x64_bf16 v[32:35], v[68:71], a[200:207], v0\nv_smfmac_f32_16x16x64_bf16 v[36:39], v[68:71], a[216:223], v0\ns_nop 2\n" \
    "v_smfmac_f32_16x16x64_bf16 v[40:43], v[56:59], a[224:231], v0\ns_nop 2\nv_cvt_pk_bf16_f32 v48, v32, v33\nv_cvt_pk_bf16_f32 v50, v34, v35\n" \
    "v_smfmac_f32_16x16x64_bf16 v[44:47], v[56:59], a[240:247], v0\nv_cvt_pk_bf16_f32 v49, v36, v37\nv_cvt_pk_bf16_f32 v51, v38, v39\n" \
    "v_smfmac_f32_16x16x64_bf16 v[40:43], v[60:63], a[232:239], v0\nv_permlane16_swap_b32 v48, v49\nv_smfmac_f32_16x16x64_bf16 v[44:47], v[60:63], a[248:255], v0\n" \
    "v_permlane16_swap_b32 v50, v51\nv_smfmac_f32_16x16x64_bf16 v[40:43], v[64:67], v[92:99], v0\nbuffer_store_dwordx4 v[48:51], v8, s[64:67], s86 offen offset:768 sc1\n" \
    "v_mov_b64 v[32:33], 0\nv_mov_b64 v[34:35], 0\nv_smfmac_f32_16x16x64_bf16 v[44:47], v[64:67], v[108:115], v0\nv_mov_b64 v[36:37], 0\nv_mov_b64 v[38:39], 0\n" \
    "v_smfmac_f32_16x16x64_bf16 v[40:43], v[68:71], v[100:107], v0\nv_smfmac_f32_16x16x64_bf16 v[44:47], v[68:71], v[116:123], v0\ns_nop 2\n" \
    "v_smfmac_f32_16x16x64_bf16 v[32:35], v[56:59], v[124:131], v0\ns_nop 2\nv_cvt_pk_bf16_f32 v52, v40, v41\nv_cvt_pk_bf16_f32 v54, v42, v43\n" \
    "v_smfmac_f32_16x16x64_bf16 v[36:39], v[56:59], v[140:147], v0\nv_cvt_pk_bf16_f32 v53, v44, v45\nv_cvt_pk_bf16_f32 v55, v46, v47\n" \
    "v_smfmac_f32_16x16x64_bf16 v[32:35], v[60:63], v[132:139], v0\nv_permlane16_swap_b32 v52, v53\nv_smfmac_f32_16x16x64_bf16 v[36:39], v[60:63], v[148:155], v0\n" \
    "v_permlane16_swap_b32 v54, v55\nv_smfmac_f32_16x16x64_bf16 v[32:35], v[64:67], v[156:163], v0\nbuffer_store_dwordx4 v[52:55], v8, s[64:67], s87 offen offset:768 sc1\n" \
    "v_mov_b64 v[40:41], 0\nv_mov_b64 v[42:43], 0\nv_smfmac_f32_16x16x64_bf16 v[36:39], v[64:67], v[172:179], v0\nv_mov_b64 v[44:45], 0\nv_mov_b64 v[46:47], 0\n" \
    "v_smfmac_f32_16x16x64_bf16 v[32:35], v[68:71], v[164:171], v0\nv_smfmac_f32_16x16x64_bf16 v[36:39], v[68:71], v[180:187], v0\ns_nop 2\n" \
    "v_smfmac_f32_16x16x64_bf16 v[40:43], v[56:59], v[188:195], v0\ns_nop 2\nv_cvt_pk_bf16_f32 v48, v32, v33\nv_cvt_pk_bf16_f32 v50, v34, v35\n" \
    "v_smfmac_f32_16x16x64_bf16 v[44:47], v[56:59], v[204:211], v0\nv_cvt_pk_bf16_f32 v49, v36, v37\nv_cvt_pk_bf16_f32 v51, v38, v39\n" \
    "v_smfmac_f32_16x16x64_bf16 v[40:43], v[60:63], v[196:203], v0\nv_permlane16_swap_b32 v48, v49\nv_smfmac_f32_16x16x64_bf16 v[44:47], v[60:63], v[212:219], v0\n" \
    "v_permlane16_swap_b32 v50, v51\nv_smfmac_f32_16x16x64_bf16 v[40:43], v[64:67], v[220:227], v0\nbuffer_store_dwordx4 v[48:51], v8, s[64:67], s88 offen offset:768 sc1\n" \
    "v_smfmac_f32_16x16x64_bf16 v[44:47], v[64:67], v[236:243], v0\nv_smfmac_f32_16x16x64_bf16 v[40:43], v[68:71], v[228:235], v0\n" \
    "v_smfmac_f32_16x16x64_bf16 v[44:47], v[68:71], v[244:251], v0\ns_nop 6\nv_cvt_pk_bf16_f32 v52, v40, v41\nv_cvt_pk_bf16_f32 v54, v42, v43\nv_cvt_pk_bf16_f32 v53, v44, v45\n" \
    "v_cvt_pk_bf16_f32 v55, v46, v47\ns_nop 0\nv_permlane16_swap_b32 v52, v53\nv_permlane16_swap_b32 v54, v55\n" \
    "buffer_store_dwordx4 v[52:55], v8, s[64:67], s89 offen offset:768 sc1\nbuffer_load_dword v13, off, s[64:67], s42 sc1\ns_waitcnt vmcnt(0)\nv_mov_b32 v15, s50\n" \
    "s_mov_b64 exec, 1\nbuffer_store_dword v15, off, s[64:67], s48 sc1\ns_mov_b64 exec, -1\ns_branch .Lv4_%=\n.Lv6_%=:\ns_endpgm\n.Lv16_%=:\ns_mov_b32 s75, 33024\n" \
    "buffer_load_dword v253, off, s[64:67], s75 sc1\ns_waitcnt vmcnt(0)\nv_readfirstlane_b32 s74, v253\ns_nop 0\ns_cmp_ge_u32 s74, 32\ns_cbranch_scc1 .Lv17_%=\ns_sleep 8\n" \
    "s_add_u32 s68, s68, 1\ns_and_b32 s69, s68, 63\ns_cbranch_scc1 .Lv16_%=\ns_cmp_gt_u32 s68, 0x400000\ns_cbranch_scc1 .Lv1_%=\nbuffer_load_dword v253, off, s[64:67], 0 sc1\n" \
    "s_waitcnt vmcnt(0)\nv_readfirstlane_b32 s69, v253\ns_nop 0\ns_cmp_eq_u32 s69, 1\ns_cbranch_scc0 .Lv16_%=\ns_endpgm\n.Lv18_%=:\ns_memrealtime s[72:73]\n" \
    "s_waitcnt lgkmcnt(0)\ns_mov_b32 s80, s72\ns_branch .Lv14_%=\n.Lv12_%=:\nbuffer_load_dword v13, off, s[64:67], s42 sc1\ns_waitcnt vmcnt(0)\ns_branch .Lv19_%=\n.Lv15_%=:\n" \
    "s_sleep 6\ns_add_u32 s68, s68, 1\ns_and_b32 s69, s68, 63\ns_cbranch_scc1 .Lv12_%=\ns_cmp_gt_u32 s68, 0x400000\ns_cbranch_scc1 .Lv1_%=\n" \
    "buffer_load_dword v253, off, s[64:67], 0 sc1\ns_waitcnt vmcnt(0)\nv_readfirstlane_b32 s69, v253\ns_nop 0\ns_cmp_eq_u32 s69, 1\ns_cbranch_scc0 .Lv12_%=\ns_endpgm\n" \
    ".Lv22_%=:\ns_add_u32 s80, s80, s43\ns_xor_b32 s81, s81, 0x1000\ns_add_u32 s68, s68, 1\ns_and_b32 s69, s68, 63\ns_cbranch_scc1 .Lv21_%=\ns_cmp_gt_u32 s68, 0x400000\n" \
    "s_cbranch_scc1 .Lv1_%=\nbuffer_load_dword v253, off, s[64:67], 0 sc1\ns_waitcnt vmcnt(0)\nv_readfirstlane_b32 s69, v253\ns_nop 0\ns_cmp_eq_u32 s69, 1\n" \
    "s_cbranch_scc0 .Lv21_%=\ns_endpgm\n.Lv25_%=:\ns_add_u32 s80, s80, s43\ns_xor_b32 s81, s81, 0x1000\ns_add_u32 s68, s68, 1\ns_and_b32 s69, s68, 63\ns_cbranch_scc1 .Lv24_%=\n" \
    "s_cmp_gt_u32 s68, 0x400000\ns_cbranch_scc1 .Lv1_%=\nbuffer_load_dword v253, off, s[64:67], 0 sc1\ns_waitcnt vmcnt(0)\nv_readfirstlane_b32 s69, v253\ns_nop 0\n" \
    "s_cmp_eq_u32 s69, 1\ns_cbranch_scc0 .Lv24_%=\ns_endpgm\n.Lv28_%=:\ns_add_u32 s80, s80, s43\ns_xor_b32 s81, s81, 0x1000\ns_add_u32 s68, s68, 1\ns_and_b32 s69, s68, 63\n" \
    "s_cbranch_scc1 .Lv27_%=\ns_cmp_gt_u32 s68, 0x400000\ns_cbranch_scc1 .Lv1_%=\nbuffer_load_dword v253, off, s[64:67], 0 sc1\ns_waitcnt vmcnt(0)\n" \
    "v_readfirstlane_b32 s69, v253\ns_nop 0\ns_cmp_eq_u32 s69, 1\ns_cbranch_scc0 .Lv27_%=\ns_endpgm\n.Lv31_%=:\ns_add_u32 s80, s80, s43\ns_xor_b32 s81, s81, 0x1000\n" \
    "s_add_u32 s68, s68, 1\ns_and_b32 s69, s68, 63\ns_cbranch_scc1 .Lv30_%=\ns_cmp_gt_u32 s68, 0x400000\ns_cbranch_scc1 .Lv1_%=\nbuffer_load_dword v253, off, s[64:67], 0 sc1\n" \
    "s_waitcnt vmcnt(0)\nv_readfirstlane_b32 s69, v253\ns_nop 0\ns_cmp_eq_u32 s69, 1\ns_cbranch_scc0 .Lv30_%=\ns_endpgm\n.Lv1_%=:\nv_mov_b32 v253, 1\n" \
    "buffer_store_dword v253, off, s[64:67], 0 sc1\ns_endpgm\n"
#define ASM_R \
    ".Lr2_%=:\ns_and_b32 s69, s32, 15\ns_lshl_b32 s69, s69, 21\ns_add_u32 s82, s42, s69\ns_lshr_b32 s69, s82, 8\ns_and_b32 s69, s69, 0x2000\ns_xor_b32 s82, s82, s69\n" \
    "s_and_b32 s69, s32, 15\ns_lshl_b32 s69, s69, 19\ns_add_u32 s83, s43, s69\ns_lshr_b32 s69, s83, 8\ns_and_b32 s69, s69, 0x2000\ns_xor_b32 s83, s83, s69\n" \
    "s_and_b32 s69, s32, 15\ns_lshl_b32 s69, s69, 18\ns_add_u32 s84, s44, s69\ns_lshr_b32 s69, s84, 8\ns_and_b32 s69, s69, 0x2000\ns_xor_b32 s84, s84, s69\n" \
    "s_and_b32 s69, s32, 15\ns_lshl_b32 s69, s69, 18\ns_add_u32 s94, s45, s69\ns_lshr_b32 s69, s94, 8\ns_and_b32 s69, s69, 0x2000\ns_xor_b32 s94, s94, s69\n" \
    "s_add_u32 s69, s32, 1\ns_and_b32 s69, s69, 15\ns_lshl_b32 s69, s69, 17\ns_add_u32 s92, s46, s69\ns_and_b32 s69, s32, 63\ns_lshl_b32 s69, s69, 19\ns_add_u32 s93, s47, s69\n" \
    "s_mov_b32 s68, 0\ns_cmp_lt_u32 s32, 2\ns_cbranch_scc1 .Lr6_%=\ns_sub_u32 s77, s32, 1\n.Lr4_%=:\nds_read_b32 v253, v10\ns_waitcnt lgkmcnt(0)\nv_readfirstlane_b32 s74, v253\n" \
    "s_nop 0\ns_and_b32 s75, s74, 127\ns_cmp_lt_u32 s75, s77\ns_cbranch_scc1 .Lr5_%=\ns_cmp_gt_u32 s75, s77\ns_cbranch_scc1 .Lr6_%=\ns_memrealtime s[72:73]\n" \
    "s_waitcnt lgkmcnt(0)\ns_lshr_b32 s69, s74, 8\ns_sub_u32 s69, s69, s72\ns_bfe_i32 s69, s69, 0x180000\ns_add_u32 s76, s72, s69\ns_add_u32 s76, s76, s58\n.Lr21_%=:\n" \
    "s_memrealtime s[72:73]\ns_waitcnt lgkmcnt(0)\ns_sub_u32 s72, s72, s76\ns_cmp_lt_i32 s72, 0\ns_cbranch_scc1 .Lr21_%=\n.Lr6_%=:\n" \
    "buffer_load_dword v253, v8, s[64:67], 0 offen sc1\ns_waitcnt vmcnt(0)\nv_cmp_ge_u32 vcc, s32, v253\ns_nop 0\ns_cbranch_vccnz .Lr7_%=\ns_cmp_eq_u32 s32, 0\n" \
    "s_cbranch_scc1 .Lr15_%=\n.Lr13_%=:\nds_read_b32 v253, v10\ns_waitcnt lgkmcnt(0)\nv_readfirstlane_b32 s74, v253\ns_nop 0\ns_and_b32 s75, s74, 127\ns_cmp_lt_u32 s75, s32\n" \
    "s_cbranch_scc1 .Lr14_%=\ns_and_b32 s57, s74, 128\ns_memrealtime s[72:73]\ns_waitcnt lgkmcnt(0)\ns_lshr_b32 s69, s74, 8\ns_sub_u32 s69, s69, s72\n" \
    "s_bfe_i32 s69, s69, 0x180000\ns_add_u32 s56, s72, s69\ns_sub_u32 s76, s56, s40\n.Lr22_%=:\ns_memrealtime s[72:73]\ns_waitcnt lgkmcnt(0)\ns_sub_u32 s72, s72, s76\n" \
    "s_cmp_lt_i32 s72, 0\ns_cbranch_scc1 .Lr22_%=\n.Lr15_%=:\nbuffer_load_dwordx4 a[0:3], v1, s[64:67], s82 offen offset:0 sc1\n" \
    "buffer_load_dwordx4 a[4:7], v1, s[64:67], s82 offen offset:1024 sc1\nbuffer_load_dwordx4 a[8:11], v1, s[64:67], s82 offen offset:2048 sc1\n" \
    "buffer_load_dwordx4 a[12:15], v1, s[64:67], s82 offen offset:3072 sc1\ns_add_u32 s82, s82, 4096\nbuffer_load_dwordx4 a[16:19], v1, s[64:67], s82 offen offset:0 sc1\n" \
    "buffer_load_dwordx4 a[20:23], v1, s[64:67], s82 offen offset:1024 sc1\nbuffer_load_dwordx4 a[24:27], v1, s[64:67], s82 offen offset:2048 sc1\n" \
    "buffer_load_dwordx4 a[28:31], v1, s[64:67], s82 offen offset:3072 sc1\ns_add_u32 s82, s82, 12288\nbuffer_load_dwordx4 a[32:35], v1, s[64:67], s82 offen offset:0 sc1\n" \
    "buffer_load_dwordx4 a[36:39], v1, s[64:67], s82 offen offset:1024 sc1\nbuffer_load_dwordx4 a[40:43], v1, s[64:67], s82 offen offset:2048 sc1\n" \
    "buffer_load_dwordx4 a[44:47], v1, s[64:67], s82 offen offset:3072 sc1\ns_add_u32 s82, s82, 4096\nbuffer_load_dwordx4 a[48:51], v1, s[64:67], s82 offen offset:0 sc1\n" \
    "buffer_load_dwordx4 a[52:55], v1, s[64:67], s82 offen offset:1024 sc1\nbuffer_load_dwordx4 a[56:59], v1, s[64:67], s82 offen offset:2048 sc1\n" \
    "buffer_load_dwordx4 a[60:63], v1, s[64:67], s82 offen offset:3072 sc1\ns_add_u32 s82, s82, 12288\nbuffer_load_dwordx4 a[64:67], v1, s[64:67], s82 offen offset:0 sc1\n" \
    "buffer_load_dwordx4 a[68:71], v1, s[64:67], s82 offen offset:1024 sc1\nbuffer_load_dwordx4 a[72:75], v1, s[64:67], s82 offen offset:2048 sc1\n" \
    "buffer_load_dwordx4 a[76:79], v1, s[64:67], s82 offen offset:3072 sc1\ns_add_u32 s82, s82, 4096\nbuffer_load_dwordx4 a[80:83], v1, s[64:67], s82 offen offset:0 sc1\n" \
    "buffer_load_dwordx4 a[84:87], v1, s[64:67], s82 offen offset:1024 sc1\nbuffer_load_dwordx4 a[88:91], v1, s[64:67], s82 offen offset:2048 sc1\n" \
    "buffer_load_dwordx4 a[92:95], v1, s[64:67], s82 offen offset:3072 sc1\ns_add_u32 s82, s82, 12288\nbuffer_load_dwordx4 a[96:99], v1, s[64:67], s82 offen offset:0 sc1\n" \
    "buffer_load_dwordx4 a[100:103], v1, s[64:67], s82 offen offset:1024 sc1\nbuffer_load_dwordx4 a[104:107], v1, s[64:67], s82 offen offset:2048 sc1\n" \
    "buffer_load_dwordx4 a[108:111], v1, s[64:67], s82 offen offset:3072 sc1\ns_add_u32 s82, s82, 4096\nbuffer_load_dwordx4 a[112:115], v1, s[64:67], s82 offen offset:0 sc1\n" \
    "buffer_load_dwordx4 a[116:119], v1, s[64:67], s82 offen offset:1024 sc1\nbuffer_load_dwordx4 a[120:123], v1, s[64:67], s82 offen offset:2048 sc1\n" \
    "buffer_load_dwordx4 a[124:127], v1, s[64:67], s82 offen offset:3072 sc1\nglobal_load_dwordx4 v[224:227], v7, s[54:55]\ns_cmp_lg_u32 s32, 0\ns_cbranch_scc1 .Lr3_%=\n" \
    "s_memrealtime s[72:73]\ns_waitcnt lgkmcnt(0)\ns_add_u32 s72, s72, s41\n.Lr20_%=:\ns_add_u32 s56, s56, s38\ns_xor_b32 s57, s57, 128\ns_sub_u32 s69, s56, s72\n" \
    "s_cmp_lt_i32 s69, 0\ns_cbranch_scc1 .Lr20_%=\ns_add_u32 s76, s56, s35\ns_cmp_eq_u32 s48, 0\ns_cbranch_scc1 .Lr23_%=\ns_lshl_b32 s74, s76, 8\ns_add_u32 s74, s74, s57\n" \
    "s_add_u32 s74, s74, s32\ns_add_u32 s74, s74, 1\nv_mov_b32 v253, s74\ns_xor_b32 s69, s48, 512\ns_mov_b64 exec, 1\nbuffer_store_dword v253, off, s[64:67], s69 sc1\n" \
    "s_mov_b64 exec, -1\n.Lr23_%=:\n.Lr3_%=:\ns_waitcnt vmcnt(0)\ns_sub_u32 s76, s56, s59\n.Lr25_%=:\ns_memrealtime s[72:73]\ns_waitcnt lgkmcnt(0)\ns_sub_u32 s72, s72, s76\n" \
    "s_cmp_lt_i32 s72, 0\ns_cbranch_scc1 .Lr25_%=\nbuffer_load_dwordx4 v[24:27], off, s[64:67], s60 sc1\ns_sub_u32 s76, s56, s34\ns_mov_b32 s77, 0\ns_waitcnt vmcnt(0)\n" \
    "v_readfirstlane_b32 s74, v24\ns_nop 0\ns_and_b32 s75, s74, 127\ns_cmp_lg_u32 s75, s32\ns_cbranch_scc1 .Lr26_%=\ns_lshr_b32 s69, s74, 8\ns_sub_u32 s69, s69, s76\n" \
    "s_bfe_i32 s69, s69, 0x180000\ns_cmp_le_i32 s69, s77\ns_cbranch_scc1 .Lr26_%=\ns_mov_b32 s77, s69\ns_and_b32 s57, s74, 128\n.Lr26_%=:\nv_readfirstlane_b32 s74, v25\n" \
    "s_nop 0\ns_and_b32 s75, s74, 127\ns_cmp_lg_u32 s75, s32\ns_cbranch_scc1 .Lr27_%=\ns_lshr_b32 s69, s74, 8\ns_sub_u32 s69, s69, s76\ns_bfe_i32 s69, s69, 0x180000\n" \
    "s_cmp_le_i32 s69, s77\ns_cbranch_scc1 .Lr27_%=\ns_mov_b32 s77, s69\ns_and_b32 s57, s74, 128\n.Lr27_%=:\nv_readfirstlane_b32 s74, v26\ns_nop 0\ns_and_b32 s75, s74, 127\n" \
    "s_cmp_lg_u32 s75, s32\ns_cbranch_scc1 .Lr28_%=\ns_lshr_b32 s69, s74, 8\ns_sub_u32 s69, s69, s76\ns_bfe_i32 s69, s69, 0x180000\ns_cmp_le_i32 s69, s77\n" \
    "s_cbranch_scc1 .Lr28_%=\ns_mov_b32 s77, s69\ns_and_b32 s57, s74, 128\n.Lr28_%=:\nv_readfirstlane_b32 s74, v27\ns_nop 0\ns_and_b32 s75, s74, 127\ns_cmp_lg_u32 s75, s32\n" \
    "s_cbranch_scc1 .Lr29_%=\ns_lshr_b32 s69, s74, 8\ns_sub_u32 s69, s69, s76\ns_bfe_i32 s69, s69, 0x180000\ns_cmp_le_i32 s69, s77\ns_cbranch_scc1 .Lr29_%=\ns_mov_b32 s77, s69\n" \
    "s_and_b32 s57, s74, 128\n.Lr29_%=:\ns_cmp_eq_u32 s77, 0\ns_cbranch_scc1 .Lr24_%=\ns_cmp_eq_u32 s32, 0\ns_cbranch_scc1 .Lr24_%=\ns_add_u32 s56, s56, s77\n" \
    "s_add_u32 s76, s56, s35\ns_cmp_eq_u32 s48, 0\ns_cbranch_scc1 .Lr30_%=\ns_lshl_b32 s74, s76, 8\ns_add_u32 s74, s74, s57\ns_add_u32 s74, s74, s32\ns_add_u32 s74, s74, 1\n" \
    "v_mov_b32 v253, s74\ns_xor_b32 s69, s48, 512\ns_mov_b64 exec, 1\nbuffer_store_dword v253, off, s[64:67], s69 sc1\ns_mov_b64 exec, -1\n.Lr30_%=:\n.Lr24_%=:\n" \
    "s_lshl_b32 s69, s57, 7\ns_xor_b32 s84, s84, s69\ns_lshl_b32 s69, s57, 5\ns_xor_b32 s92, s92, s69\ns_add_u32 s75, s83, 4096\ns_mov_b32 s68, 0\n.Lr8_%=:\n" \
    "s_add_u32 s85, s84, 0x8000\ns_add_u32 s86, s84, 0x10000\ns_add_u32 s87, s84, 0x18000\ns_sub_u32 s76, s56, s36\n.Lr31_%=:\ns_memrealtime s[72:73]\ns_waitcnt lgkmcnt(0)\n" \
    "s_sub_u32 s72, s72, s76\ns_cmp_lt_i32 s72, 0\ns_cbranch_scc1 .Lr31_%=\nbuffer_load_dwordx4 v[32:35], v1, s[64:67], s83 offen offset:0 sc1\n" \
    "buffer_load_dwordx4 v[36:39], v1, s[64:67], s83 offen offset:1024 sc1\nbuffer_load_dwordx4 v[40:43], v1, s[64:67], s83 offen offset:2048 sc1\n" \
    "buffer_load_dwordx4 v[44:47], v1, s[64:67], s83 offen offset:3072 sc1\nbuffer_load_dwordx4 v[48:51], v1, s[64:67], s75 offen offset:0 sc1\n" \
    "buffer_load_dwordx4 v[52:55], v1, s[64:67], s75 offen offset:1024 sc1\nbuffer_load_dwordx4 v[56:59], v1, s[64:67], s75 offen offset:2048 sc1\n" \
    "buffer_load_dwordx4 v[60:63], v1, s[64:67], s75 offen offset:3072 sc1\ns_cmp_eq_u32 s32, 63\ns_cbranch_scc1 .Lr10_%=\n.Lr32_%=:\ns_memrealtime s[72:73]\n" \
    "s_waitcnt lgkmcnt(0)\ns_sub_u32 s72, s72, s56\ns_cmp_lt_i32 s72, 0\ns_cbranch_scc1 .Lr32_%=\nbuffer_load_dwordx4 v[64:67], v1, s[64:67], s84 offen offset:0 sc1\n" \
    "buffer_load_dwordx4 v[68:71], v1, s[64:67], s84 offen offset:1024 sc1\nbuffer_load_dwordx4 v[72:75], v1, s[64:67], s84 offen offset:2048 sc1\n" \
    "buffer_load_dwordx4 v[76:79], v1, s[64:67], s84 offen offset:3072 sc1\nbuffer_load_dwordx2 v[80:81], v2, s[64:67], s84 offen offset:0 sc1\n" \
    "buffer_load_dwordx2 v[82:83], v2, s[64:67], s84 offen offset:128 sc1\nbuffer_load_dwordx4 v[84:87], v1, s[64:67], s85 offen offset:0 sc1\n" \
    "buffer_load_dwordx4 v[88:91], v1, s[64:67], s85 offen offset:1024 sc1\nbuffer_load_dwordx4 v[92:95], v1, s[64:67], s85 offen offset:2048 sc1\n" \
    "buffer_load_dwordx4 v[96:99], v1, s[64:67], s85 offen offset:3072 sc1\nbuffer_load_dwordx2 v[100:101], v2, s[64:67], s85 offen offset:0 sc1\n" \
    "buffer_load_dwordx2 v[102:103], v2, s[64:67], s85 offen offset:128 sc1\nbuffer_load_dwordx4 v[104:107], v1, s[64:67], s86 offen offset:0 sc1\n" \
    "buffer_load_dwordx4 v[108:111], v1, s[64:67], s86 offen offset:1024 sc1\nbuffer_load_dwordx4 v[112:115], v1, s[64:67], s86 offen offset:2048 sc1\n" \
    "buffer_load_dwordx4 v[116:119], v1, s[64:67], s86 offen offset:3072 sc1\nbuffer_load_dwordx2 v[120:121], v2, s[64:67], s86 offen offset:0 sc1\n" \
    "buffer_load_dwordx2 v[122:123], v2, s[64:67], s86 offen offset:128 sc1\nbuffer_load_dwordx4 v[124:127], v1, s[64:67], s87 offen offset:0 sc1\n" \
    "buffer_load_dwordx4 v[128:131], v1, s[64:67], s87 offen offset:1024 sc1\nbuffer_load_dwordx4 v[132:135], v1, s[64:67], s87 offen offset:2048 sc1\n" \
    "buffer_load_dwordx4 v[136:139], v1, s[64:67], s87 offen offset:3072 sc1\nbuffer_load_dwordx2 v[140:141], v2, s[64:67], s87 offen offset:0 sc1\n" \
    "buffer_load_dwordx2 v[142:143], v2, s[64:67], s87 offen offset:128 sc1\nv_mov_b64 v[24:25], 0\nv_mov_b64 v[26:27], 0\ns_xor_b32 s77, s92, 0x1000\ns_waitcnt vmcnt(23)\n" \
    "v_smfmac_f32_16x16x64_bf16 v[24:27], v[64:67], a[0:7], v0\ns_waitcnt vmcnt(22)\nv_smfmac_f32_16x16x64_bf16 v[24:27], v[68:71], a[8:15], v0\ns_waitcnt vmcnt(21)\n" \
    "v_smfmac_f32_16x16x64_bf16 v[24:27], v[72:75], a[16:23], v0\ns_waitcnt vmcnt(20)\nv_smfmac_f32_16x16x64_bf16 v[24:27], v[76:79], a[24:31], v0\ns_waitcnt vmcnt(18)\n" \
    "v_smfmac_f32_16x16x64_bf16 v[24:27], v[80:83], v[32:39], v0\ns_waitcnt vmcnt(17)\nv_smfmac_f32_16x16x64_bf16 v[24:27], v[84:87], a[32:39], v0\ns_waitcnt vmcnt(16)\n" \
    "v_smfmac_f32_16x16x64_bf16 v[24:27], v[88:91], a[40:47], v0\ns_waitcnt vmcnt(15)\nv_smfmac_f32_16x16x64_bf16 v[24:27], v[92:95], a[48:55], v0\ns_waitcnt vmcnt(14)\n" \
    "v_smfmac_f32_16x16x64_bf16 v[24:27], v[96:99], a[56:63], v0\ns_waitcnt vmcnt(12)\nv_smfmac_f32_16x16x64_bf16 v[24:27], v[100:103], v[40:47], v0\ns_waitcnt vmcnt(11)\n" \
    "v_smfmac_f32_16x16x64_bf16 v[24:27], v[104:107], a[64:71], v0\ns_waitcnt vmcnt(10)\nv_smfmac_f32_16x16x64_bf16 v[24:27], v[108:111], a[72:79], v0\ns_waitcnt vmcnt(9)\n" \
    "v_smfmac_f32_16x16x64_bf16 v[24:27], v[112:115], a[80:87], v0\ns_waitcnt vmcnt(8)\nv_smfmac_f32_16x16x64_bf16 v[24:27], v[116:119], a[88:95], v0\ns_waitcnt vmcnt(6)\n" \
    "v_smfmac_f32_16x16x64_bf16 v[24:27], v[120:123], v[48:55], v0\ns_waitcnt vmcnt(5)\nv_smfmac_f32_16x16x64_bf16 v[24:27], v[124:127], a[96:103], v0\ns_waitcnt vmcnt(4)\n" \
    "v_smfmac_f32_16x16x64_bf16 v[24:27], v[128:131], a[104:111], v0\ns_waitcnt vmcnt(3)\nv_smfmac_f32_16x16x64_bf16 v[24:27], v[132:135], a[112:119], v0\ns_waitcnt vmcnt(2)\n" \
    "v_smfmac_f32_16x16x64_bf16 v[24:27], v[136:139], a[120:127], v0\ns_waitcnt vmcnt(0)\nv_smfmac_f32_16x16x64_bf16 v[24:27], v[140:143], v[56:63], v0\ns_nop 7\n" \
    "v_permlane32_swap_b32 v24, v26\nv_permlane32_swap_b32 v25, v27\nv_pk_add_f32 v[24:25], v[24:25], v[26:27]\nv_cvt_pk_bf16_f32 v26, v24, v25\nds_write_b32 v3, v26\n" \
    "ds_read_b64_tr_b16 v[28:29], v4\nds_read_b64_tr_b16 v[30:31], v4 offset:64\nv_cmp_u_f32 vcc, v24, v25\ns_nop 0\ns_cbranch_vccnz .Lr17_%=\n.Lr16_%=:\ns_waitcnt lgkmcnt(0)\n" \
    "buffer_store_dwordx4 v[28:31], v6, s[64:67], s92 offen sc1\nbuffer_store_dwordx4 v[28:31], v6, s[64:67], s77 offen sc1\n.Lr10_%=:\ns_memrealtime s[72:73]\n" \
    "s_waitcnt lgkmcnt(0)\ns_add_u32 s76, s56, s35\n.Lr34_%=:\ns_sub_u32 s69, s76, s72\ns_cmp_ge_i32 s69, s37\ns_cbranch_scc1 .Lr35_%=\ns_add_u32 s76, s76, s38\n" \
    "s_xor_b32 s57, s57, 128\ns_branch .Lr34_%=\n.Lr35_%=:\ns_add_u32 s76, s76, s34\ns_lshl_b32 s74, s76, 8\ns_add_u32 s74, s74, s57\ns_add_u32 s74, s74, s32\n" \
    "s_add_u32 s74, s74, 1\nv_mov_b32 v253, s74\nds_write_b32 v10, v253\ns_add_u32 s76, s76, s35\ns_cmp_eq_u32 s48, 0\ns_cbranch_scc1 .Lr36_%=\ns_lshl_b32 s74, s76, 8\n" \
    "s_add_u32 s74, s74, s57\ns_add_u32 s74, s74, s32\ns_add_u32 s74, s74, 2\nv_mov_b32 v253, s74\ns_xor_b32 s69, s48, 0\ns_mov_b64 exec, 1\n" \
    "buffer_store_dword v253, off, s[64:67], s69 sc1\ns_mov_b64 exec, -1\n.Lr36_%=:\ns_cmp_ge_u32 s32, 62\ns_cselect_b32 s76, s61, s39\ns_add_u32 s76, s76, s56\n" \
    "s_mov_b32 s68, 0\ns_mov_b32 s80, 0\n.Lr37_%=:\ns_memrealtime s[72:73]\ns_waitcnt lgkmcnt(0)\ns_sub_u32 s72, s72, s76\ns_cmp_lt_i32 s72, 0\ns_cbranch_scc1 .Lr37_%=\n" \
    ".Lr11_%=:\ns_add_u32 s76, s94, 0x20000\nbuffer_load_dword v250, v13, s[64:67], s94 offen sc1\nbuffer_load_dword v251, v13, s[64:67], s76 offen sc1\nv_mov_b64 v[24:25], 0\n" \
    "v_mov_b64 v[26:27], 0\nv_mov_b64 v[28:29], 0\nv_mov_b64 v[30:31], 0\nv_mov_b32 v252, 0\ns_xor_b32 s69, s49, 0x0\ns_add_u32 s84, s94, s69\ns_add_u32 s85, s84, 0x4000\n" \
    "s_add_u32 s86, s84, 0x8000\ns_add_u32 s87, s84, 0xc000\nbuffer_load_dwordx4 v[64:67], v1, s[64:67], s84 offen offset:0 sc1\n" \
    "buffer_load_dwordx4 v[68:71], v1, s[64:67], s84 offen offset:1024 sc1\nbuffer_load_dwordx4 v[72:75], v1, s[64:67], s84 offen offset:2048 sc1\n" \
    "buffer_load_dwordx4 v[76:79], v1, s[64:67], s84 offen offset:3072 sc1\nbuffer_load_dwordx2 v[80:81], v2, s[64:67], s84 offen offset:0 sc1\n" \
    "buffer_load_dwordx2 v[82:83], v2, s[64:67], s84 offen offset:128 sc1\nbuffer_load_dwordx4 v[84:87], v1, s[64:67], s85 offen offset:0 sc1\n" \
    "buffer_load_dwordx4 v[88:91], v1, s[64:67], s85 offen offset:1024 sc1\nbuffer_load_dwordx4 v[92:95], v1, s[64:67], s85 offen offset:2048 sc1\n" \
    "buffer_load_dwordx4 v[96:99], v1, s[64:67], s85 offen offset:3072 sc1\nbuffer_load_dwordx2 v[100:101], v2, s[64:67], s85 offen offset:0 sc1\n" \
    "buffer_load_dwordx2 v[102:103], v2, s[64:67], s85 offen offset:128 sc1\nbuffer_load_dwordx4 v[104:107], v1, s[64:67], s86 offen offset:0 sc1\n" \
    "buffer_load_dwordx4 v[108:111], v1, s[64:67], s86 offen offset:1024 sc1\nbuffer_load_dwordx4 v[112:115], v1, s[64:67], s86 offen offset:2048 sc1\n" \
    "buffer_load_dwordx4 v[116:119], v1, s[64:67], s86 offen offset:3072 sc1\nbuffer_load_dwordx2 v[120:121], v2, s[64:67], s86 offen offset:0 sc1\n" \
    "buffer_load_dwordx2 v[122:123], v2, s[64:67], s86 offen offset:128 sc1\nbuffer_load_dwordx4 v[124:127], v1, s[64:67], s87 offen offset:0 sc1\n" \
    "buffer_load_dwordx4 v[128:131], v1, s[64:67], s87 offen offset:1024 sc1\nbuffer_load_dwordx4 v[132:135], v1, s[64:67], s87 offen offset:2048 sc1\n" \
    "buffer_load_dwordx4 v[136:139], v1, s[64:67], s87 offen offset:3072 sc1\nbuffer_load_dwordx2 v[140:141], v2, s[64:67], s87 offen offset:0 sc1\n" \
    "buffer_load_dwordx2 v[142:143], v2, s[64:67], s87 offen offset:128 sc1\ns_xor_b32 s69, s49, 0x10000\ns_add_u32 s88, s94, s69\ns_add_u32 s89, s88, 0x4000\n" \
    "s_add_u32 s90, s88, 0x8000\ns_add_u32 s91, s88, 0xc000\nbuffer_load_dwordx4 v[144:147], v1, s[64:67], s88 offen offset:0 sc1\n" \
    "buffer_load_dwordx4 v[148:151], v1, s[64:67], s88 offen offset:1024 sc1\nbuffer_load_dwordx4 v[152:155], v1, s[64:67], s88 offen offset:2048 sc1\n" \
    "buffer_load_dwordx4 v[156:159], v1, s[64:67], s88 offen offset:3072 sc1\nbuffer_load_dwordx2 v[160:161], v2, s[64:67], s88 offen offset:0 sc1\n" \
    "buffer_load_dwordx2 v[162:163], v2, s[64:67], s88 offen offset:128 sc1\nbuffer_load_dwordx4 v[164:167], v1, s[64:67], s89 offen offset:0 sc1\n" \
    "buffer_load_dwordx4 v[168:171], v1, s[64:67], s89 offen offset:1024 sc1\nbuffer_load_dwordx4 v[172:175], v1, s[64:67], s89 offen offset:2048 sc1\n" \
    "buffer_load_dwordx4 v[176:179], v1, s[64:67], s89 offen offset:3072 sc1\nbuffer_load_dwordx2 v[180:181], v2, s[64:67], s89 offen offset:0 sc1\n" \
    "buffer_load_dwordx2 v[182:183], v2, s[64:67], s89 offen offset:128 sc1\nbuffer_load_dwordx4 v[184:187], v1, s[64:67], s90 offen offset:0 sc1\n" \
    "buffer_load_dwordx4 v[188:191], v1, s[64:67], s90 offen offset:1024 sc1\ns_waitcnt vmcnt(38)\nv_max_u32 v250, v250, v251\nv_cmp_eq_u32 vcc, -1, v250\ns_nop 0\n" \
    "s_cbranch_vccnz .Lr38_%=\ns_waitcnt vmcnt(37)\nv_smfmac_f32_16x16x64_bf16 v[24:27], v[64:67], a[0:7], v0\n" \
    "buffer_load_dwordx4 v[192:195], v1, s[64:67], s90 offen offset:2048 sc1\ns_waitcnt vmcnt(37)\nv_smfmac_f32_16x16x64_bf16 v[24:27], v[68:71], a[8:15], v0\n" \
    "buffer_load_dwordx4 v[196:199], v1, s[64:67], s90 offen offset:3072 sc1\ns_waitcnt vmcnt(37)\nv_smfmac_f32_16x16x64_bf16 v[24:27], v[72:75], a[16:23], v0\n" \
    "buffer_load_dwordx2 v[200:201], v2, s[64:67], s90 offen offset:0 sc1\nbuffer_load_dwordx2 v[202:203], v2, s[64:67], s90 offen offset:128 sc1\ns_waitcnt vmcnt(38)\n" \
    "v_smfmac_f32_16x16x64_bf16 v[24:27], v[76:79], a[24:31], v0\nbuffer_load_dwordx4 v[204:207], v1, s[64:67], s91 offen offset:0 sc1\ns_waitcnt vmcnt(37)\n" \
    "v_smfmac_f32_16x16x64_bf16 v[24:27], v[80:83], v[32:39], v0\nbuffer_load_dwordx4 v[208:211], v1, s[64:67], s91 offen offset:1024 sc1\ns_waitcnt vmcnt(37)\n" \
    "v_smfmac_f32_16x16x64_bf16 v[24:27], v[84:87], a[32:39], v0\nbuffer_load_dwordx4 v[212:215], v1, s[64:67], s91 offen offset:2048 sc1\ns_waitcnt vmcnt(37)\n" \
    "v_smfmac_f32_16x16x64_bf16 v[24:27], v[88:91], a[40:47], v0\nbuffer_load_dwordx4 v[216:219], v1, s[64:67], s91 offen offset:3072 sc1\ns_waitcnt vmcnt(37)\n" \
    "v_smfmac_f32_16x16x64_bf16 v[24:27], v[92:95], a[48:55], v0\nbuffer_load_dwordx2 v[220:221], v2, s[64:67], s91 offen offset:0 sc1\n" \
    "buffer_load_dwordx2 v[222:223], v2, s[64:67], s91 offen offset:128 sc1\ns_xor_b32 s69, s49, 0x20000\ns_waitcnt vmcnt(38)\n" \
    "v_smfmac_f32_16x16x64_bf16 v[24:27], v[96:99], a[56:63], v0\ns_add_u32 s84, s94, s69\ns_add_u32 s85, s84, 0x4000\ns_add_u32 s86, s84, 0x8000\ns_waitcnt vmcnt(36)\n" \
    "v_smfmac_f32_16x16x64_bf16 v[24:27], v[100:103], v[40:47], v0\ns_add_u32 s87, s84, 0xc000\nbuffer_load_dwordx4 v[64:67], v1, s[64:67], s84 offen offset:0 sc1\n" \
    "buffer_load_dwordx4 v[68:71], v1, s[64:67], s84 offen offset:1024 sc1\ns_waitcnt vmcnt(37)\nv_smfmac_f32_16x16x64_bf16 v[24:27], v[104:107], a[64:71], v0\n" \
    "buffer_load_dwordx4 v[72:75], v1, s[64:67], s84 offen offset:2048 sc1\ns_waitcnt vmcnt(37)\nv_smfmac_f32_16x16x64_bf16 v[24:27], v[108:111], a[72:79], v0\n" \
    "buffer_load_dwordx4 v[76:79], v1, s[64:67], s84 offen offset:3072 sc1\ns_waitcnt vmcnt(37)\nv_smfmac_f32_16x16x64_bf16 v[24:27], v[112:115], a[80:87], v0\n" \
    "buffer_load_dwordx2 v[80:81], v2, s[64:67], s84 offen offset:0 sc1\nbuffer_load_dwordx2 v[82:83], v2, s[64:67], s84 offen offset:128 sc1\ns_waitcnt vmcnt(38)\n" \
    "v_smfmac_f32_16x16x64_bf16 v[24:27], v[116:119], a[88:95], v0\nbuffer_load_dwordx4 v[84:87], v1, s[64:67], s85 offen offset:0 sc1\ns_waitcnt vmcnt(37)\n" \
    "v_smfmac_f32_16x16x64_bf16 v[24:27], v[120:123], v[48:55], v0\nbuffer_load_dwordx4 v[88:91], v1, s[64:67], s85 offen offset:1024 sc1\ns_waitcnt vmcnt(37)\n" \
    "v_smfmac_f32_16x16x64_bf16 v[24:27], v[124:127], a[96:103], v0\nbuffer_load_dwordx4 v[92:95], v1, s[64:67], s85 offen offset:2048 sc1\ns_waitcnt vmcnt(37)\n" \
    "v_smfmac_f32_16x16x64_bf16 v[24:27], v[128:131], a[104:111], v0\nbuffer_load_dwordx4 v[96:99], v1, s[64:67], s85 offen offset:3072 sc1\ns_waitcnt vmcnt(37)\n" \
    "v_smfmac_f32_16x16x64_bf16 v[24:27], v[132:135], a[112:119], v0\nbuffer_load_dwordx2 v[100:101], v2, s[64:67], s85 offen offset:0 sc1\n" \
    "buffer_load_dwordx2 v[102:103], v2, s[64:67], s85 offen offset:128 sc1\ns_waitcnt vmcnt(38)\nv_smfmac_f32_16x16x64_bf16 v[24:27], v[136:139], a[120:127], v0\n" \
    "buffer_load_dwordx4 v[104:107], v1, s[64:67], s86 offen offset:0 sc1\ns_waitcnt vmcnt(37)\nv_smfmac_f32_16x16x64_bf16 v[24:27], v[140:143], v[56:63], v0\n" \
    "buffer_load_dwordx4 v[108:111], v1, s[64:67], s86 offen offset:1024 sc1\ns_nop 1\ns_waitcnt vmcnt(37)\nv_smfmac_f32_16x16x64_bf16 v[28:31], v[144:147], a[0:7], v0\n" \
    "buffer_load_dwordx4 v[112:115], v1, s[64:67], s86 offen offset:2048 sc1\ns_nop 1\ns_waitcnt vmcnt(37)\nv_permlane32_swap_b32 v24, v26\n" \
    "v_smfmac_f32_16x16x64_bf16 v[28:31], v[148:151], a[8:15], v0\nbuffer_load_dwordx4 v[116:119], v1, s[64:67], s86 offen offset:3072 sc1\ns_waitcnt vmcnt(37)\n" \
    "v_permlane32_swap_b32 v25, v27\nv_smfmac_f32_16x16x64_bf16 v[28:31], v[152:155], a[16:23], v0\nbuffer_load_dwordx2 v[120:121], v2, s[64:67], s86 offen offset:0 sc1\n" \
    "buffer_load_dwordx2 v[122:123], v2, s[64:67], s86 offen offset:128 sc1\ns_waitcnt vmcnt(38)\nv_add_f32 v24, v24, v26\n" \
    "v_smfmac_f32_16x16x64_bf16 v[28:31], v[156:159], a[24:31], v0\nbuffer_load_dwordx4 v[124:127], v1, s[64:67], s87 offen offset:0 sc1\ns_waitcnt vmcnt(37)\n" \
    "v_add_f32 v25, v25, v27\nv_smfmac_f32_16x16x64_bf16 v[28:31], v[160:163], v[32:39], v0\nbuffer_load_dwordx4 v[128:131], v1, s[64:67], s87 offen offset:1024 sc1\n" \
    "s_waitcnt vmcnt(37)\nv_fma_f32 v252, v24, v25, v252\nv_smfmac_f32_16x16x64_bf16 v[28:31], v[164:167], a[32:39], v0\n" \
    "buffer_load_dwordx4 v[132:135], v1, s[64:67], s87 offen offset:2048 sc1\ns_waitcnt vmcnt(37)\nv_cvt_pk_bf16_f32 v24, v24, v25\n" \
    "v_smfmac_f32_16x16x64_bf16 v[28:31], v[168:171], a[40:47], v0\nbuffer_load_dwordx4 v[136:139], v1, s[64:67], s87 offen offset:3072 sc1\nds_write_b32 v3, v24 offset:0\n" \
    "s_waitcnt vmcnt(37)\nv_mov_b64 v[26:27], 0\nv_smfmac_f32_16x16x64_bf16 v[28:31], v[172:175], a[48:55], v0\n" \
    "buffer_load_dwordx2 v[140:141], v2, s[64:67], s87 offen offset:0 sc1\nbuffer_load_dwordx2 v[142:143], v2, s[64:67], s87 offen offset:128 sc1\ns_xor_b32 s69, s49, 0x30000\n" \
    "s_waitcnt vmcnt(38)\nv_mov_b64 v[24:25], 0\nv_smfmac_f32_16x16x64_bf16 v[28:31], v[176:179], a[56:63], v0\ns_add_u32 s88, s94, s69\ns_add_u32 s89, s88, 0x4000\n" \
    "s_add_u32 s90, s88, 0x8000\ns_waitcnt vmcnt(36)\nv_smfmac_f32_16x16x64_bf16 v[28:31], v[180:183], v[40:47], v0\ns_add_u32 s91, s88, 0xc000\n" \
    "buffer_load_dwordx4 v[144:147], v1, s[64:67], s88 offen offset:0 sc1\nbuffer_load_dwordx4 v[148:151], v1, s[64:67], s88 offen offset:1024 sc1\ns_waitcnt vmcnt(37)\n" \
    "v_smfmac_f32_16x16x64_bf16 v[28:31], v[184:187], a[64:71], v0\nbuffer_load_dwordx4 v[152:155], v1, s[64:67], s88 offen offset:2048 sc1\ns_waitcnt vmcnt(37)\n" \
    "v_smfmac_f32_16x16x64_bf16 v[28:31], v[188:191], a[72:79], v0\nbuffer_load_dwordx4 v[156:159], v1, s[64:67], s88 offen offset:3072 sc1\ns_waitcnt vmcnt(37)\n" \
    "v_smfmac_f32_16x16x64_bf16 v[28:31], v[192:195], a[80:87], v0\nbuffer_load_dwordx2 v[160:161], v2, s[64:67], s88 offen offset:0 sc1\n" \
    "buffer_load_dwordx2 v[162:163], v2, s[64:67], s88 offen offset:128 sc1\ns_waitcnt vmcnt(38)\nv_smfmac_f32_16x16x64_bf16 v[28:31], v[196:199], a[88:95], v0\n" \
    "buffer_load_dwordx4 v[164:167], v1, s[64:67], s89 offen offset:0 sc1\ns_waitcnt vmcnt(37)\nv_smfmac_f32_16x16x64_bf16 v[28:31], v[200:203], v[48:55], v0\n" \
    "buffer_load_dwordx4 v[168:171], v1, s[64:67], s89 offen offset:1024 sc1\ns_waitcnt vmcnt(37)\nv_smfmac_f32_16x16x64_bf16 v[28:31], v[204:207], a[96:103], v0\n" \
    "buffer_load_dwordx4 v[172:175], v1, s[64:67], s89 offen offset:2048 sc1\ns_waitcnt vmcnt(37)\nv_smfmac_f32_16x16x64_bf16 v[28:31], v[208:211], a[104:111], v0\n" \
    "buffer_load_dwordx4 v[176:179], v1, s[64:67], s89 offen offset:3072 sc1\ns_waitcnt vmcnt(37)\nv_smfmac_f32_16x16x64_bf16 v[28:31], v[212:215], a[112:119], v0\n" \
    "buffer_load_dwordx2 v[180:181], v2, s[64:67], s89 offen offset:0 sc1\nbuffer_load_dwordx2 v[182:183], v2, s[64:67], s89 offen offset:128 sc1\ns_waitcnt vmcnt(38)\n" \
    "v_smfmac_f32_16x16x64_bf16 v[28:31], v[216:219], a[120:127], v0\nbuffer_load_dwordx4 v[184:187], v1, s[64:67], s90 offen offset:0 sc1\ns_waitcnt vmcnt(37)\n" \
    "v_smfmac_f32_16x16x64_bf16 v[28:31], v[220:223], v[56:63], v0\nbuffer_load_dwordx4 v[188:191], v1, s[64:67], s90 offen offset:1024 sc1\ns_nop 1\ns_waitcnt vmcnt(37)\n" \
    "v_smfmac_f32_16x16x64_bf16 v[24:27], v[64:67], a[0:7], v0\nbuffer_load_dwordx4 v[192:195], v1, s[64:67], s90 offen offset:2048 sc1\ns_nop 1\ns_waitcnt vmcnt(37)\n" \
    "v_permlane32_swap_b32 v28, v30\nv_smfmac_f32_16x16x64_bf16 v[24:27], v[68:71], a[8:15], v0\nbuffer_load_dwordx4 v[196:199], v1, s[64:67], s90 offen offset:3072 sc1\n" \
    "s_waitcnt vmcnt(37)\nv_permlane32_swap_b32 v29, v31\nv_smfmac_f32_16x16x64_bf16 v[24:27], v[72:75], a[16:23], v0\n" \
    "buffer_load_dwordx2 v[200:201], v2, s[64:67], s90 offen offset:0 sc1\nbuffer_load_dwordx2 v[202:203], v2, s[64:67], s90 offen offset:128 sc1\ns_waitcnt vmcnt(38)\n" \
    "v_add_f32 v28, v28, v30\nv_smfmac_f32_16x16x64_bf16 v[24:27], v[76:79], a[24:31], v0\nbuffer_load_dwordx4 v[204:207], v1, s[64:67], s91 offen offset:0 sc1\n" \
    "s_waitcnt vmcnt(37)\nv_add_f32 v29, v29, v31\nv_smfmac_f32_16x16x64_bf16 v[24:27], v[80:83], v[32:39], v0\n" \
    "buffer_load_dwordx4 v[208:211], v1, s[64:67], s91 offen offset:1024 sc1\ns_waitcnt vmcnt(37)\nv_fma_f32 v252, v28, v29, v252\n" \
    "v_smfmac_f32_16x16x64_bf16 v[24:27], v[84:87], a[32:39], v0\nbuffer_load_dwordx4 v[212:215], v1, s[64:67], s91 offen offset:2048 sc1\ns_waitcnt vmcnt(37)\n" \
    "v_cvt_pk_bf16_f32 v28, v28, v29\nv_smfmac_f32_16x16x64_bf16 v[24:27], v[88:91], a[40:47], v0\nbuffer_load_dwordx4 v[216:219], v1, s[64:67], s91 offen offset:3072 sc1\n" \
    "ds_write_b32 v3, v28 offset:256\ns_waitcnt vmcnt(37)\nv_mov_b64 v[30:31], 0\nv_smfmac_f32_16x16x64_bf16 v[24:27], v[92:95], a[48:55], v0\n" \
    "buffer_load_dwordx2 v[220:221], v2, s[64:67], s91 offen offset:0 sc1\nbuffer_load_dwordx2 v[222:223], v2, s[64:67], s91 offen offset:128 sc1\ns_waitcnt vmcnt(38)\n" \
    "v_mov_b64 v[28:29], 0\nv_smfmac_f32_16x16x64_bf16 v[24:27], v[96:99], a[56:63], v0\ns_waitcnt vmcnt(36)\nv_smfmac_f32_16x16x64_bf16 v[24:27], v[100:103], v[40:47], v0\n" \
    "s_waitcnt vmcnt(35)\nv_smfmac_f32_16x16x64_bf16 v[24:27], v[104:107], a[64:71], v0\ns_waitcnt vmcnt(34)\nv_smfmac_f32_16x16x64_bf16 v[24:27], v[108:111], a[72:79], v0\n" \
    "s_waitcnt vmcnt(33)\nv_smfmac_f32_16x16x64_bf16 v[24:27], v[112:115], a[80:87], v0\ns_waitcnt vmcnt(32)\nv_smfmac_f32_16x16x64_bf16 v[24:27], v[116:119], a[88:95], v0\n" \
    "s_waitcnt vmcnt(30)\nv_smfmac_f32_16x16x64_bf16 v[24:27], v[120:123], v[48:55], v0\ns_waitcnt vmcnt(29)\nv_smfmac_f32_16x16x64_bf16 v[24:27], v[124:127], a[96:103], v0\n" \
    "s_waitcnt vmcnt(28)\nv_smfmac_f32_16x16x64_bf16 v[24:27], v[128:131], a[104:111], v0\ns_waitcnt vmcnt(27)\nv_smfmac_f32_16x16x64_bf16 v[24:27], v[132:135], a[112:119], v0\n" \
    "s_waitcnt vmcnt(26)\nv_smfmac_f32_16x16x64_bf16 v[24:27], v[136:139], a[120:127], v0\ns_waitcnt vmcnt(24)\nv_smfmac_f32_16x16x64_bf16 v[24:27], v[140:143], v[56:63], v0\n" \
    "s_nop 2\ns_waitcnt vmcnt(23)\nv_smfmac_f32_16x16x64_bf16 v[28:31], v[144:147], a[0:7], v0\ns_nop 2\ns_waitcnt vmcnt(22)\nv_permlane32_swap_b32 v24, v26\n" \
    "v_smfmac_f32_16x16x64_bf16 v[28:31], v[148:151], a[8:15], v0\ns_waitcnt vmcnt(21)\nv_permlane32_swap_b32 v25, v27\n" \
    "v_smfmac_f32_16x16x64_bf16 v[28:31], v[152:155], a[16:23], v0\ns_waitcnt vmcnt(20)\nv_add_f32 v24, v24, v26\nv_smfmac_f32_16x16x64_bf16 v[28:31], v[156:159], a[24:31], v0\n" \
    "s_waitcnt vmcnt(18)\nv_add_f32 v25, v25, v27\nv_smfmac_f32_16x16x64_bf16 v[28:31], v[160:163], v[32:39], v0\ns_waitcnt vmcnt(17)\nv_fma_f32 v252, v24, v25, v252\n" \
    "v_smfmac_f32_16x16x64_bf16 v[28:31], v[164:167], a[32:39], v0\ns_waitcnt vmcnt(16)\nv_cvt_pk_bf16_f32 v24, v24, v25\n" \
    "v_smfmac_f32_16x16x64_bf16 v[28:31], v[168:171], a[40:47], v0\nds_write_b32 v3, v24 offset:512\ns_waitcnt vmcnt(15)\nv_mov_b64 v[26:27], 0\n" \
    "v_smfmac_f32_16x16x64_bf16 v[28:31], v[172:175], a[48:55], v0\ns_waitcnt vmcnt(14)\nv_mov_b64 v[24:25], 0\nv_smfmac_f32_16x16x64_bf16 v[28:31], v[176:179], a[56:63], v0\n" \
    "s_waitcnt vmcnt(12)\nv_smfmac_f32_16x16x64_bf16 v[28:31], v[180:183], v[40:47], v0\ns_waitcnt vmcnt(11)\nv_smfmac_f32_16x16x64_bf16 v[28:31], v[184:187], a[64:71], v0\n" \
    "s_waitcnt vmcnt(10)\nv_smfmac_f32_16x16x64_bf16 v[28:31], v[188:191], a[72:79], v0\ns_waitcnt vmcnt(9)\nv_smfmac_f32_16x16x64_bf16 v[28:31], v[192:195], a[80:87], v0\n" \
    "s_waitcnt vmcnt(8)\nv_smfmac_f32_16x16x64_bf16 v[28:31], v[196:199], a[88:95], v0\ns_waitcnt vmcnt(6)\nv_smfmac_f32_16x16x64_bf16 v[28:31], v[200:203], v[48:55], v0\n" \
    "s_waitcnt vmcnt(5)\nv_smfmac_f32_16x16x64_bf16 v[28:31], v[204:207], a[96:103], v0\ns_waitcnt vmcnt(4)\nv_smfmac_f32_16x16x64_bf16 v[28:31], v[208:211], a[104:111], v0\n" \
    "s_waitcnt vmcnt(3)\nv_smfmac_f32_16x16x64_bf16 v[28:31], v[212:215], a[112:119], v0\ns_waitcnt vmcnt(2)\nv_smfmac_f32_16x16x64_bf16 v[28:31], v[216:219], a[120:127], v0\n" \
    "s_waitcnt vmcnt(0)\nv_smfmac_f32_16x16x64_bf16 v[28:31], v[220:223], v[56:63], v0\ns_nop 7\nv_permlane32_swap_b32 v28, v30\nv_permlane32_swap_b32 v29, v31\n" \
    "v_add_f32 v28, v28, v30\nv_add_f32 v29, v29, v31\nv_fma_f32 v252, v28, v29, v252\nv_cvt_pk_bf16_f32 v28, v28, v29\nv_cmp_u_f32 vcc, v252, v252\n" \
    "ds_write_b32 v3, v28 offset:768\nds_read_b64_tr_b16 v[24:25], v5\nds_read_b64_tr_b16 v[26:27], v5 offset:64\ns_cbranch_vccnz .Lr12_%=\n.Lr18_%=:\ns_cmp_lt_u32 s32, 4\n" \
    "s_cbranch_scc1 .Lr39_%=\n.Lr40_%=:\nv_lshlrev_b32 v72, 16, v224\nv_and_b32 v73, 0xffff0000, v224\nv_lshlrev_b32 v74, 16, v225\nv_and_b32 v75, 0xffff0000, v225\n" \
    "v_lshlrev_b32 v76, 16, v226\nv_and_b32 v77, 0xffff0000, v226\nv_lshlrev_b32 v78, 16, v227\nv_and_b32 v79, 0xffff0000, v227\ns_xor_b32 s77, s93, 0x2000\n" \
    "s_waitcnt lgkmcnt(0)\nv_lshlrev_b32 v64, 16, v24\nv_and_b32 v65, 0xffff0000, v24\nv_lshlrev_b32 v66, 16, v25\nv_and_b32 v67, 0xffff0000, v25\nv_lshlrev_b32 v68, 16, v26\n" \
    "v_and_b32 v69, 0xffff0000, v26\nv_lshlrev_b32 v70, 16, v27\nv_and_b32 v71, 0xffff0000, v27\nv_mul_f32 v64, v64, v16\nv_mul_f32 v65, v65, v17\nv_mul_f32 v66, v66, v18\n" \
    "v_mul_f32 v67, v67, v19\nv_mul_f32 v68, v68, v20\nv_mul_f32 v69, v69, v21\nv_mul_f32 v70, v70, v22\nv_mul_f32 v71, v71, v23\nv_cvt_pk_bf16_f32 v64, 0, v64\n" \
    "v_cvt_pk_bf16_f32 v65, 0, v65\nv_cvt_pk_bf16_f32 v66, 0, v66\nv_cvt_pk_bf16_f32 v67, 0, v67\nv_cvt_pk_bf16_f32 v68, 0, v68\nv_cvt_pk_bf16_f32 v69, 0, v69\n" \
    "v_cvt_pk_bf16_f32 v70, 0, v70\nv_cvt_pk_bf16_f32 v71, 0, v71\nv_add_f32 v64, v64, v72\nv_add_f32 v65, v65, v73\nv_add_f32 v66, v66, v74\nv_add_f32 v67, v67, v75\n" \
    "v_add_f32 v68, v68, v76\nv_add_f32 v69, v69, v77\nv_add_f32 v70, v70, v78\nv_add_f32 v71, v71, v79\nv_cvt_pk_bf16_f32 v24, v64, v65\nv_cvt_pk_bf16_f32 v25, v66, v67\n" \
    "v_cvt_pk_bf16_f32 v26, v68, v69\nv_cvt_pk_bf16_f32 v27, v70, v71\nv_min_u32 v28, -2, v24\nv_min_u32 v29, -2, v25\nv_min_u32 v30, -2, v26\nv_min_u32 v31, -2, v27\n" \
    "s_cmp_lt_u32 s32, 62\ns_cbranch_scc1 .Lr41_%=\nbuffer_store_dwordx4 v[28:31], v1, s[64:67], s93 offen sc1\nbuffer_store_dwordx4 v[28:31], v1, s[64:67], s77 offen sc1\n" \
    ".Lr41_%=:\nv_lshlrev_b32 v64, 16, v24\nv_and_b32 v65, 0xffff0000, v24\nv_lshlrev_b32 v66, 16, v25\nv_and_b32 v67, 0xffff0000, v25\nv_lshlrev_b32 v68, 16, v26\n" \
    "v_and_b32 v69, 0xffff0000, v26\nv_lshlrev_b32 v70, 16, v27\nv_and_b32 v71, 0xffff0000, v27\nv_add_f32 v72, v64, v65\nv_add_f32 v73, v66, v67\nv_add_f32 v74, v68, v69\n" \
    "v_add_f32 v75, v70, v71\nv_add_f32 v72, v72, v73\nv_add_f32 v74, v74, v75\nv_add_f32 v76, v72, v74\nv_add_f32 v77, v72, v74\ns_nop 1\nv_permlane32_swap_b32 v76, v77\n" \
    "v_add_f32 v76, v76, v77\nv_mul_f32 v76, 0x3d800000, v76\nv_sub_f32 v64, v64, v76\nv_sub_f32 v65, v65, v76\nv_sub_f32 v66, v66, v76\nv_sub_f32 v67, v67, v76\n" \
    "v_sub_f32 v68, v68, v76\nv_sub_f32 v69, v69, v76\nv_sub_f32 v70, v70, v76\nv_sub_f32 v71, v71, v76\nv_mul_f32 v72, v64, v64\nv_mul_f32 v73, v65, v65\n" \
    "v_fmac_f32 v72, v66, v66\nv_fmac_f32 v73, v67, v67\nv_fmac_f32 v72, v68, v68\nv_fmac_f32 v73, v69, v69\nv_fmac_f32 v72, v70, v70\nv_fmac_f32 v73, v71, v71\n" \
    "v_add_f32 v74, v72, v73\nv_add_f32 v75, v72, v73\ns_nop 1\nv_permlane32_swap_b32 v74, v75\nv_add_f32 v77, v74, v75\nv_min_u32 v76, -2, v76\nv_min_u32 v77, -2, v77\n" \
    "s_cmp_ge_u32 s32, 62\ns_cbranch_scc1 .Lr42_%=\ns_add_u32 s76, s32, 2\n.Lr44_%=:\nds_read_b32 v253, v10\ns_waitcnt lgkmcnt(0)\nv_readfirstlane_b32 s74, v253\ns_nop 0\n" \
    "s_and_b32 s75, s74, 127\ns_cmp_lt_u32 s75, s76\ns_cbranch_scc1 .Lr45_%=\nbuffer_store_dwordx4 v[28:31], v1, s[64:67], s93 offen sc1\n" \
    "buffer_store_dwordx4 v[28:31], v1, s[64:67], s77 offen sc1\n.Lr42_%=:\nbuffer_store_dwordx2 v[76:77], v9, s[64:67], s93 offen sc1\ns_cmp_ge_u32 s32, 56\n" \
    "s_cbranch_scc1 .Lr46_%=\nv_mov_b64 v[80:81], -1\nv_mov_b64 v[82:83], -1\ns_add_u32 s69, s32, 9\ns_and_b32 s69, s69, 15\ns_lshl_b32 s69, s69, 17\ns_add_u32 s92, s46, s69\n" \
    "s_xor_b32 s74, s92, 0x1000\nbuffer_store_dwordx4 v[80:83], v6, s[64:67], s92 offen sc1\nbuffer_store_dwordx4 v[80:83], v6, s[64:67], s74 offen sc1\n.Lr46_%=:\n" \
    "v_add_u32 v7, 0x40000, v7\ns_add_u32 s32, s32, 4\ns_cmp_lt_u32 s32, 64\ns_cbranch_scc1 .Lr2_%=\ns_endpgm\n.Lr5_%=:\ns_sleep 2\ns_add_u32 s68, s68, 1\n" \
    "s_and_b32 s69, s68, 63\ns_cbranch_scc1 .Lr4_%=\ns_cmp_gt_u32 s68, 0x400000\ns_cbranch_scc1 .Lr1_%=\nbuffer_load_dword v253, off, s[64:67], 0 sc1\ns_waitcnt vmcnt(0)\n" \
    "v_readfirstlane_b32 s69, v253\ns_nop 0\ns_cmp_eq_u32 s69, 1\ns_cbranch_scc0 .Lr4_%=\ns_endpgm\n.Lr7_%=:\ns_sleep 6\ns_add_u32 s68, s68, 1\ns_and_b32 s69, s68, 63\n" \
    "s_cbranch_scc1 .Lr6_%=\ns_cmp_gt_u32 s68, 0x400000\ns_cbranch_scc1 .Lr1_%=\nbuffer_load_dword v253, off, s[64:67], 0 sc1\ns_waitcnt vmcnt(0)\n" \
    "v_readfirstlane_b32 s69, v253\ns_nop 0\ns_cmp_eq_u32 s69, 1\ns_cbranch_scc0 .Lr6_%=\ns_endpgm\n.Lr14_%=:\ns_sleep 2\ns_add_u32 s68, s68, 1\ns_and_b32 s69, s68, 63\n" \
    "s_cbranch_scc1 .Lr13_%=\ns_cmp_gt_u32 s68, 0x400000\ns_cbranch_scc1 .Lr1_%=\nbuffer_load_dword v253, off, s[64:67], 0 sc1\ns_waitcnt vmcnt(0)\n" \
    "v_readfirstlane_b32 s69, v253\ns_nop 0\ns_cmp_eq_u32 s69, 1\ns_cbranch_scc0 .Lr13_%=\ns_endpgm\n.Lr17_%=:\ns_cmp_lt_u32 s68, 3\ns_cbranch_scc1 .Lr9_%=\n" \
    "v_max3_u32 v252, v32, v33, v34\nv_max3_u32 v253, v35, v36, v37\nv_max3_u32 v252, v252, v38, v39\nv_max3_u32 v253, v253, v40, v41\nv_max3_u32 v252, v252, v42, v43\n" \
    "v_max3_u32 v253, v253, v44, v45\nv_max3_u32 v252, v252, v46, v47\nv_max3_u32 v253, v253, v48, v49\nv_max3_u32 v252, v252, v50, v51\nv_max3_u32 v253, v253, v52, v53\n" \
    "v_max3_u32 v252, v252, v54, v55\nv_max3_u32 v253, v253, v56, v57\nv_max3_u32 v252, v252, v58, v59\nv_max3_u32 v253, v253, v60, v61\nv_max3_u32 v252, v252, v62, v63\n" \
    "v_max3_u32 v252, v252, v64, v65\nv_max3_u32 v253, v253, v66, v67\nv_max3_u32 v252, v252, v68, v69\nv_max3_u32 v253, v253, v70, v71\nv_max3_u32 v252, v252, v72, v73\n" \
    "v_max3_u32 v253, v253, v74, v75\nv_max3_u32 v252, v252, v76, v77\nv_max3_u32 v253, v253, v78, v79\nv_max3_u32 v252, v252, v80, v81\nv_max3_u32 v253, v253, v82, v83\n" \
    "v_max3_u32 v252, v252, v84, v85\nv_max3_u32 v253, v253, v86, v87\nv_max3_u32 v252, v252, v88, v89\nv_max3_u32 v253, v253, v90, v91\nv_max3_u32 v252, v252, v92, v93\n" \
    "v_max3_u32 v253, v253, v94, v95\nv_max3_u32 v252, v252, v96, v97\nv_max3_u32 v253, v253, v98, v99\nv_max3_u32 v252, v252, v100, v101\nv_max3_u32 v253, v253, v102, v103\n" \
    "v_max3_u32 v252, v252, v104, v105\nv_max3_u32 v253, v253, v106, v107\nv_max3_u32 v252, v252, v108, v109\nv_max3_u32 v253, v253, v110, v111\n" \
    "v_max3_u32 v252, v252, v112, v113\nv_max3_u32 v253, v253, v114, v115\nv_max3_u32 v252, v252, v116, v117\nv_max3_u32 v253, v253, v118, v119\n" \
    "v_max3_u32 v252, v252, v120, v121\nv_max3_u32 v253, v253, v122, v123\nv_max3_u32 v252, v252, v124, v125\nv_max3_u32 v253, v253, v126, v127\n" \
    "v_max3_u32 v252, v252, v128, v129\nv_max3_u32 v253, v253, v130, v131\nv_max3_u32 v252, v252, v132, v133\nv_max3_u32 v253, v253, v134, v135\n" \
    "v_max3_u32 v252, v252, v136, v137\nv_max3_u32 v253, v253, v138, v139\nv_max3_u32 v252, v252, v140, v141\nv_max3_u32 v253, v253, v142, v143\nv_max_u32 v252, v252, v253\n" \
    "v_cmp_eq_u32 vcc, -1, v252\ns_nop 0\ns_cbranch_vccz .Lr16_%=\n.Lr9_%=:\ns_add_u32 s56, s56, s38\ns_xor_b32 s57, s57, 128\ns_xor_b32 s84, s84, 0x4000\n" \
    "s_xor_b32 s92, s92, 0x1000\ns_add_u32 s76, s56, s35\ns_cmp_eq_u32 s48, 0\ns_cbranch_scc1 .Lr33_%=\ns_lshl_b32 s74, s76, 8\ns_add_u32 s74, s74, s57\n" \
    "s_add_u32 s74, s74, s32\ns_add_u32 s74, s74, 1\nv_mov_b32 v253, s74\ns_xor_b32 s69, s48, 512\ns_mov_b64 exec, 1\nbuffer_store_dword v253, off, s[64:67], s69 sc1\n" \
    "s_mov_b64 exec, -1\n.Lr33_%=:\ns_add_u32 s68, s68, 1\ns_and_b32 s69, s68, 63\ns_cbranch_scc1 .Lr8_%=\ns_cmp_gt_u32 s68, 0x400000\ns_cbranch_scc1 .Lr1_%=\n" \
    "buffer_load_dword v253, off, s[64:67], 0 sc1\ns_waitcnt vmcnt(0)\nv_readfirstlane_b32 s69, v253\ns_nop 0\ns_cmp_eq_u32 s69, 1\ns_cbranch_scc0 .Lr8_%=\ns_endpgm\n.Lr38_%=:\n" \
    "s_waitcnt vmcnt(0)\ns_add_u32 s68, s68, 1\ns_and_b32 s69, s68, 63\ns_cbranch_scc1 .Lr11_%=\ns_cmp_gt_u32 s68, 0x400000\ns_cbranch_scc1 .Lr1_%=\n" \
    "buffer_load_dword v253, off, s[64:67], 0 sc1\ns_waitcnt vmcnt(0)\nv_readfirstlane_b32 s69, v253\ns_nop 0\ns_cmp_eq_u32 s69, 1\ns_cbranch_scc0 .Lr11_%=\ns_endpgm\n" \
    ".Lr12_%=:\ns_cmp_lg_u32 s80, 0\ns_cbranch_scc1 .Lr18_%=\ns_sleep 4\nbuffer_load_dwordx4 v[32:35], v1, s[64:67], s83 offen offset:0 sc1\n" \
    "buffer_load_dwordx4 v[36:39], v1, s[64:67], s83 offen offset:1024 sc1\nbuffer_load_dwordx4 v[40:43], v1, s[64:67], s83 offen offset:2048 sc1\n" \
    "buffer_load_dwordx4 v[44:47], v1, s[64:67], s83 offen offset:3072 sc1\nbuffer_load_dwordx4 v[48:51], v1, s[64:67], s75 offen offset:0 sc1\n" \
    "buffer_load_dwordx4 v[52:55], v1, s[64:67], s75 offen offset:1024 sc1\nbuffer_load_dwordx4 v[56:59], v1, s[64:67], s75 offen offset:2048 sc1\n" \
    "buffer_load_dwordx4 v[60:63], v1, s[64:67], s75 offen offset:3072 sc1\ns_waitcnt vmcnt(0)\nv_max3_u32 v252, v32, v33, v34\nv_max3_u32 v253, v35, v36, v37\n" \
    "v_max3_u32 v252, v252, v38, v39\nv_max3_u32 v253, v253, v40, v41\nv_max3_u32 v252, v252, v42, v43\nv_max3_u32 v253, v253, v44, v45\nv_max3_u32 v252, v252, v46, v47\n" \
    "v_max3_u32 v253, v253, v48, v49\nv_max3_u32 v252, v252, v50, v51\nv_max3_u32 v253, v253, v52, v53\nv_max3_u32 v252, v252, v54, v55\nv_max3_u32 v253, v253, v56, v57\n" \
    "v_max3_u32 v252, v252, v58, v59\nv_max3_u32 v253, v253, v60, v61\nv_max3_u32 v252, v252, v62, v63\ns_mov_b32 s76, s94\n.Lr19_%=:\ns_add_u32 s84, s76, 0x0\n" \
    "s_add_u32 s85, s76, 0x4000\ns_add_u32 s86, s76, 0x8000\ns_add_u32 s87, s76, 0xc000\nbuffer_load_dwordx4 v[64:67], v1, s[64:67], s84 offen offset:0 sc1\n" \
    "buffer_load_dwordx4 v[68:71], v1, s[64:67], s84 offen offset:1024 sc1\nbuffer_load_dwordx4 v[72:75], v1, s[64:67], s84 offen offset:2048 sc1\n" \
    "buffer_load_dwordx4 v[76:79], v1, s[64:67], s84 offen offset:3072 sc1\nbuffer_load_dwordx2 v[80:81], v2, s[64:67], s84 offen offset:0 sc1\n" \
    "buffer_load_dwordx2 v[82:83], v2, s[64:67], s84 offen offset:128 sc1\nbuffer_load_dwordx4 v[84:87], v1, s[64:67], s85 offen offset:0 sc1\n" \
    "buffer_load_dwordx4 v[88:91], v1, s[64:67], s85 offen offset:1024 sc1\nbuffer_load_dwordx4 v[92:95], v1, s[64:67], s85 offen offset:2048 sc1\n" \
    "buffer_load_dwordx4 v[96:99], v1, s[64:67], s85 offen offset:3072 sc1\nbuffer_load_dwordx2 v[100:101], v2, s[64:67], s85 offen offset:0 sc1\n" \
    "buffer_load_dwordx2 v[102:103], v2, s[64:67], s85 offen offset:128 sc1\nbuffer_load_dwordx4 v[104:107], v1, s[64:67], s86 offen offset:0 sc1\n" \
    "buffer_load_dwordx4 v[108:111], v1, s[64:67], s86 offen offset:1024 sc1\nbuffer_load_dwordx4 v[112:115], v1, s[64:67], s86 offen offset:2048 sc1\n" \
    "buffer_load_dwordx4 v[116:119], v1, s[64:67], s86 offen offset:3072 sc1\nbuffer_load_dwordx2 v[120:121], v2, s[64:67], s86 offen offset:0 sc1\n" \
    "buffer_load_dwordx2 v[122:123], v2, s[64:67], s86 offen offset:128 sc1\nbuffer_load_dwordx4 v[124:127], v1, s[64:67], s87 offen offset:0 sc1\n" \
    "buffer_load_dwordx4 v[128:131], v1, s[64:67], s87 offen offset:1024 sc1\nbuffer_load_dwordx4 v[132:135], v1, s[64:67], s87 offen offset:2048 sc1\n" \
    "buffer_load_dwordx4 v[136:139], v1, s[64:67], s87 offen offset:3072 sc1\nbuffer_load_dwordx2 v[140:141], v2, s[64:67], s87 offen offset:0 sc1\n" \
    "buffer_load_dwordx2 v[142:143], v2, s[64:67], s87 offen offset:128 sc1\ns_waitcnt vmcnt(0)\nv_max3_u32 v252, v252, v64, v65\nv_max3_u32 v253, v253, v66, v67\n" \
    "v_max3_u32 v252, v252, v68, v69\nv_max3_u32 v253, v253, v70, v71\nv_max3_u32 v252, v252, v72, v73\nv_max3_u32 v253, v253, v74, v75\nv_max3_u32 v252, v252, v76, v77\n" \
    "v_max3_u32 v253, v253, v78, v79\nv_max3_u32 v252, v252, v80, v81\nv_max3_u32 v253, v253, v82, v83\nv_max3_u32 v252, v252, v84, v85\nv_max3_u32 v253, v253, v86, v87\n" \
    "v_max3_u32 v252, v252, v88, v89\nv_max3_u32 v253, v253, v90, v91\nv_max3_u32 v252, v252, v92, v93\nv_max3_u32 v253, v253, v94, v95\nv_max3_u32 v252, v252, v96, v97\n" \
    "v_max3_u32 v253, v253, v98, v99\nv_max3_u32 v252, v252, v100, v101\nv_max3_u32 v253, v253, v102, v103\nv_max3_u32 v252, v252, v104, v105\n" \
    "v_max3_u32 v253, v253, v106, v107\nv_max3_u32 v252, v252, v108, v109\nv_max3_u32 v253, v253, v110, v111\nv_max3_u32 v252, v252, v112, v113\n" \
    "v_max3_u32 v253, v253, v114, v115\nv_max3_u32 v252, v252, v116, v117\nv_max3_u32 v253, v253, v118, v119\nv_max3_u32 v252, v252, v120, v121\n" \
    "v_max3_u32 v253, v253, v122, v123\nv_max3_u32 v252, v252, v124, v125\nv_max3_u32 v253, v253, v126, v127\nv_max3_u32 v252, v252, v128, v129\n" \
    "v_max3_u32 v253, v253, v130, v131\nv_max3_u32 v252, v252, v132, v133\nv_max3_u32 v253, v253, v134, v135\nv_max3_u32 v252, v252, v136, v137\n" \
    "v_max3_u32 v253, v253, v138, v139\nv_max3_u32 v252, v252, v140, v141\nv_max3_u32 v253, v253, v142, v143\ns_add_u32 s76, s76, 0x10000\ns_sub_u32 s69, s76, s94\n" \
    "s_cmp_lt_u32 s69, 0x40000\ns_cbranch_scc1 .Lr19_%=\nv_max_u32 v252, v252, v253\nv_cmp_eq_u32 vcc, -1, v252\ns_nop 0\ns_cmp_eq_u64 vcc, 0\ns_cselect_b32 s80, 1, 0\n" \
    "s_add_u32 s68, s68, 1\ns_and_b32 s69, s68, 63\ns_cbranch_scc1 .Lr11_%=\ns_cmp_gt_u32 s68, 0x400000\ns_cbranch_scc1 .Lr1_%=\nbuffer_load_dword v253, off, s[64:67], 0 sc1\n" \
    "s_waitcnt vmcnt(0)\nv_readfirstlane_b32 s69, v253\ns_nop 0\ns_cmp_eq_u32 s69, 1\ns_cbranch_scc0 .Lr11_%=\ns_endpgm\n.Lr39_%=:\n" \
    "buffer_load_dwordx4 v[16:19], v12, s[64:67], 0 offen offset:0 sc1\nbuffer_load_dwordx4 v[20:23], v12, s[64:67], 0 offen offset:16 sc1\ns_waitcnt vmcnt(0)\n" \
    "v_max3_u32 v252, v16, v17, v18\nv_max3_u32 v253, v19, v20, v21\nv_max3_u32 v252, v252, v22, v23\nv_max_u32 v252, v252, v253\nv_cmp_eq_u32 vcc, -1, v252\ns_nop 0\n" \
    "s_cbranch_vccz .Lr40_%=\ns_sleep 8\ns_add_u32 s68, s68, 1\ns_and_b32 s69, s68, 63\ns_cbranch_scc1 .Lr39_%=\ns_cmp_gt_u32 s68, 0x400000\ns_cbranch_scc1 .Lr1_%=\n" \
    "buffer_load_dword v253, off, s[64:67], 0 sc1\ns_waitcnt vmcnt(0)\nv_readfirstlane_b32 s69, v253\ns_nop 0\ns_cmp_eq_u32 s69, 1\ns_cbranch_scc0 .Lr39_%=\ns_endpgm\n" \
    ".Lr45_%=:\ns_sleep 2\ns_add_u32 s68, s68, 1\ns_and_b32 s69, s68, 63\ns_cbranch_scc1 .Lr44_%=\ns_cmp_gt_u32 s68, 0x400000\ns_cbranch_scc1 .Lr1_%=\n" \
    "buffer_load_dword v253, off, s[64:67], 0 sc1\ns_waitcnt vmcnt(0)\nv_readfirstlane_b32 s69, v253\ns_nop 0\ns_cmp_eq_u32 s69, 1\ns_cbranch_scc0 .Lr44_%=\ns_endpgm\n.Lr1_%=:\n" \
    "v_mov_b32 v253, 1\nbuffer_store_dword v253, off, s[64:67], 0 sc1\ns_endpgm\n"
#define ASM_F1 \
    "s_mov_b32 s90, 0x7fff0000\ns_mov_b32 s91, 0x7fff0000\ns_mov_b32 s92, 0x7fff0000\ns_mov_b32 s93, 0x7fff0000\ns_mov_b32 s80, 0\ns_mov_b32 s81, 0\ns_mov_b32 s68, 0\n.Lf2_%=:\n" \
    "s_xor_b32 s85, s38, 512\ns_and_b32 s69, s32, 63\ns_lshl_b32 s69, s69, 19\ns_add_u32 s82, s36, s69\ns_lshr_b32 s69, s82, 8\ns_and_b32 s69, s69, 0x2000\n" \
    "s_xor_b32 s82, s82, s69\ns_add_u32 s69, s32, 1\ns_and_b32 s69, s69, 63\ns_lshl_b32 s69, s69, 19\ns_add_u32 s83, s36, s69\ns_lshr_b32 s69, s83, 8\n" \
    "s_and_b32 s69, s69, 0x2000\ns_xor_b32 s83, s83, s69\ns_add_u32 s84, s37, s80\ns_cmp_eq_u32 s81, 1\ns_cbranch_scc1 .Lf6_%=\ns_add_u32 s77, s32, 2\ns_cmp_eq_u32 s81, 2\n" \
    "s_cbranch_scc1 .Lf5_%=\n.Lf3_%=:\nbuffer_load_dword v150, off, s[64:67], s38 sc1\ns_waitcnt vmcnt(0)\nv_and_b32 v112, 127, v150\nv_cmp_gt_u32 vcc, s77, v112\ns_nop 0\n" \
    "s_cbranch_vccnz .Lf4_%=\n.Lf5_%=:\nv_and_b32 v112, 127, v150\n.Lf11_%=:\ns_sleep 1\ns_memrealtime s[72:73]\ns_waitcnt lgkmcnt(0)\ns_cmp_ge_u32 s77, 64\n" \
    "s_cselect_b32 s69, s60, s34\ns_sub_u32 s72, s72, s69\ns_lshl_b32 s72, s72, 8\nv_sub_u32 v113, s72, v150\nv_cmp_gt_i32 s[74:75], 0, v113\nv_cmp_eq_u32 vcc, s77, v112\n" \
    "s_nop 0\ns_and_b64 vcc, vcc, s[74:75]\ns_cbranch_scc1 .Lf11_%=\n.Lf12_%=:\nbuffer_load_dword v151, v2, s[64:67], s82 offen sc1\ns_add_u32 m0, s84, 0\n" \
    "s_add_u32 s69, s82, s48\nbuffer_load_dwordx4 v0, s[64:67], s69 offen offset:0 sc1 lds\nbuffer_load_dwordx4 v0, s[64:67], s69 offen offset:1024 sc1 lds\n" \
    "s_add_u32 m0, s84, 8192\ns_add_u32 s69, s82, s49\nbuffer_load_dwordx4 v0, s[64:67], s69 offen offset:0 sc1 lds\n" \
    "buffer_load_dwordx4 v0, s[64:67], s69 offen offset:1024 sc1 lds\ns_add_u32 m0, s84, 16384\ns_add_u32 s69, s82, s50\n" \
    "buffer_load_dwordx4 v0, s[64:67], s69 offen offset:0 sc1 lds\nbuffer_load_dwordx4 v0, s[64:67], s69 offen offset:1024 sc1 lds\n.Lf6_%=:\ns_waitcnt vmcnt(6)\n" \
    "v_cmp_eq_u32 vcc, -1, v151\ns_nop 0\ns_cbranch_vccnz .Lf13_%=\ns_cmp_lg_u32 s33, 0\ns_cbranch_scc1 .Lf14_%=\ns_memrealtime s[72:73]\ns_waitcnt lgkmcnt(0)\n" \
    "s_lshl_b32 s72, s72, 8\ns_add_u32 s72, s72, s32\ns_add_u32 s72, s72, 1\nv_mov_b32 v112, s72\ns_mov_b64 exec, 1\nbuffer_store_dword v112, off, s[64:67], s39 sc1\n" \
    "s_mov_b64 exec, -1\n.Lf14_%=:\ns_add_u32 s77, s32, 3\ns_add_u32 m0, s84, 24576\ns_add_u32 s69, s82, s51\nbuffer_load_dwordx4 v0, s[64:67], s69 offen offset:0 sc1 lds\n" \
    "buffer_load_dwordx4 v0, s[64:67], s69 offen offset:1024 sc1 lds\ns_waitcnt vmcnt(6)\ns_barrier\nds_read_b128 v[64:67], v1 offset:0\nds_read_b128 v[68:71], v1 offset:1024\n" \
    "ds_read_b128 v[72:75], v1 offset:2048\nds_read_b128 v[76:79], v1 offset:3072\ns_waitcnt lgkmcnt(3)\nv_mfma_f32_32x32x16_bf16 v[48:63], a[0:3], v[64:67], 0\n" \
    "ds_read_b128 v[80:83], v1 offset:4096\ns_add_u32 m0, s84, 32768\ns_add_u32 s69, s82, s52\nbuffer_load_dwordx4 v0, s[64:67], s69 offen offset:0 sc1 lds\n" \
    "s_waitcnt lgkmcnt(3)\nv_mfma_f32_32x32x16_bf16 v[48:63], a[4:7], v[68:71], v[48:63]\nds_read_b128 v[84:87], v1 offset:5120\ns_waitcnt lgkmcnt(3)\n" \
    "v_mfma_f32_32x32x16_bf16 v[48:63], a[8:11], v[72:75], v[48:63]\nds_read_b128 v[88:91], v1 offset:6144\nbuffer_load_dwordx4 v0, s[64:67], s69 offen offset:1024 sc1 lds\n" \
    "s_waitcnt lgkmcnt(3)\nv_mfma_f32_32x32x16_bf16 v[48:63], a[12:15], v[76:79], v[48:63]\nds_read_b128 v[92:95], v1 offset:7168\ns_waitcnt vmcnt(6) lgkmcnt(3)\ns_barrier\n" \
    "v_mfma_f32_32x32x16_bf16 v[48:63], a[16:19], v[80:83], v[48:63]\nds_read_b128 v[64:67], v1 offset:8192\ns_add_u32 s69, s82, s41\n" \
    "buffer_load_dwordx4 v[96:99], v0, s[64:67], s69 offen offset:0 sc1\ns_waitcnt lgkmcnt(3)\nv_mfma_f32_32x32x16_bf16 v[48:63], a[20:23], v[84:87], v[48:63]\n" \
    "ds_read_b128 v[68:71], v1 offset:9216\ns_waitcnt lgkmcnt(3)\nv_mfma_f32_32x32x16_bf16 v[48:63], a[24:27], v[88:91], v[48:63]\nds_read_b128 v[72:75], v1 offset:10240\n" \
    "buffer_load_dwordx4 v[100:103], v0, s[64:67], s69 offen offset:1024 sc1\ns_waitcnt lgkmcnt(3)\nv_mfma_f32_32x32x16_bf16 v[48:63], a[28:31], v[92:95], v[48:63]\n" \
    "ds_read_b128 v[76:79], v1 offset:11264\ns_waitcnt lgkmcnt(3)\nv_mfma_f32_32x32x16_bf16 v[48:63], a[32:35], v[64:67], v[48:63]\nds_read_b128 v[80:83], v1 offset:12288\n" \
    "buffer_load_dwordx4 v[104:107], v0, s[64:67], s69 offen offset:2048 sc1\ns_waitcnt lgkmcnt(3)\nv_mfma_f32_32x32x16_bf16 v[48:63], a[36:39], v[68:71], v[48:63]\n" \
    "ds_read_b128 v[84:87], v1 offset:13312\ns_waitcnt lgkmcnt(3)\nv_mfma_f32_32x32x16_bf16 v[48:63], a[40:43], v[72:75], v[48:63]\nds_read_b128 v[88:91], v1 offset:14336\n" \
    "buffer_load_dwordx4 v[108:111], v0, s[64:67], s69 offen offset:3072 sc1\ns_waitcnt lgkmcnt(3)\nv_mfma_f32_32x32x16_bf16 v[48:63], a[44:47], v[76:79], v[48:63]\n" \
    "ds_read_b128 v[92:95], v1 offset:15360\ns_waitcnt vmcnt(6) lgkmcnt(3)\ns_barrier\nv_mfma_f32_32x32x16_bf16 v[48:63], a[48:51], v[80:83], v[48:63]\n" \
    "ds_read_b128 v[64:67], v1 offset:16384\ns_add_u32 m0, s84, 40960\ns_add_u32 s69, s82, s53\nbuffer_load_dwordx4 v0, s[64:67], s69 offen offset:0 sc1 lds\n" \
    "s_waitcnt lgkmcnt(3)\nv_mfma_f32_32x32x16_bf16 v[48:63], a[52:55], v[84:87], v[48:63]\nds_read_b128 v[68:71], v1 offset:17408\ns_waitcnt lgkmcnt(3)\n" \
    "v_mfma_f32_32x32x16_bf16 v[48:63], a[56:59], v[88:91], v[48:63]\nds_read_b128 v[72:75], v1 offset:18432\nbuffer_load_dwordx4 v0, s[64:67], s69 offen offset:1024 sc1 lds\n" \
    "s_waitcnt lgkmcnt(3)\nv_mfma_f32_32x32x16_bf16 v[48:63], a[60:63], v[92:95], v[48:63]\nds_read_b128 v[76:79], v1 offset:19456\ns_waitcnt lgkmcnt(3)\n" \
    "v_mfma_f32_32x32x16_bf16 v[48:63], a[64:67], v[64:67], v[48:63]\nds_read_b128 v[80:83], v1 offset:20480\ns_add_u32 m0, s84, 49152\ns_add_u32 s69, s82, s54\n" \
    "buffer_load_dwordx4 v0, s[64:67], s69 offen offset:0 sc1 lds\ns_waitcnt lgkmcnt(3)\nv_mfma_f32_32x32x16_bf16 v[48:63], a[68:71], v[68:71], v[48:63]\n" \
    "ds_read_b128 v[84:87], v1 offset:21504\ns_waitcnt lgkmcnt(3)\nv_mfma_f32_32x32x16_bf16 v[48:63], a[72:75], v[72:75], v[48:63]\nds_read_b128 v[88:91], v1 offset:22528\n" \
    "buffer_load_dwordx4 v0, s[64:67], s69 offen offset:1024 sc1 lds\ns_waitcnt lgkmcnt(3)\nv_mfma_f32_32x32x16_bf16 v[48:63], a[76:79], v[76:79], v[48:63]\n" \
    "ds_read_b128 v[92:95], v1 offset:23552\ns_waitcnt lgkmcnt(3)\nv_mfma_f32_32x32x16_bf16 v[48:63], a[80:83], v[80:83], v[48:63]\nds_read_b128 v[64:67], v1 offset:24576\n" \
    "s_add_u32 m0, s84, 57344\ns_add_u32 s69, s82, s55\nbuffer_load_dwordx4 v0, s[64:67], s69 offen offset:0 sc1 lds\ns_waitcnt lgkmcnt(3)\n" \
    "v_mfma_f32_32x32x16_bf16 v[48:63], a[84:87], v[84:87], v[48:63]\nds_read_b128 v[68:71], v1 offset:25600\ns_waitcnt lgkmcnt(3)\n" \
    "v_mfma_f32_32x32x16_bf16 v[48:63], a[88:91], v[88:91], v[48:63]\nds_read_b128 v[72:75], v1 offset:26624\nbuffer_load_dwordx4 v0, s[64:67], s69 offen offset:1024 sc1 lds\n" \
    "s_waitcnt lgkmcnt(3)\nv_mfma_f32_32x32x16_bf16 v[48:63], a[92:95], v[92:95], v[48:63]\nds_read_b128 v[76:79], v1 offset:27648\ns_waitcnt lgkmcnt(3)\n" \
    "v_mfma_f32_32x32x16_bf16 v[48:63], a[96:99], v[64:67], v[48:63]\nds_read_b128 v[80:83], v1 offset:28672\ns_and_b32 s69, s32, 63\ns_lshl_b32 s69, s69, 19\n" \
    "s_add_u32 s82, s40, s69\ns_lshr_b32 s69, s82, 8\ns_and_b32 s69, s69, 0x2000\ns_waitcnt lgkmcnt(3)\nv_mfma_f32_32x32x16_bf16 v[48:63], a[100:103], v[68:71], v[48:63]\n" \
    "ds_read_b128 v[84:87], v1 offset:29696\ns_xor_b32 s82, s82, s69\ns_waitcnt lgkmcnt(3)\nv_mfma_f32_32x32x16_bf16 v[48:63], a[104:107], v[72:75], v[48:63]\n" \
    "ds_read_b128 v[88:91], v1 offset:30720\ns_waitcnt vmcnt(6)\ns_waitcnt lgkmcnt(3)\nv_max3_u32 v116, v96, v97, v98\nv_max3_u32 v116, v116, v99, v100\n" \
    "v_max3_u32 v116, v116, v101, v102\nv_max3_u32 v116, v116, v103, v104\nv_max3_u32 v116, v116, v105, v106\nv_mfma_f32_32x32x16_bf16 v[48:63], a[108:111], v[76:79], v[48:63]\n" \
    "ds_read_b128 v[92:95], v1 offset:31744\ns_waitcnt vmcnt(4) lgkmcnt(3)\ns_barrier\nv_max3_u32 v116, v116, v107, v108\nv_max3_u32 v116, v116, v109, v110\n" \
    "v_max_u32 v116, v116, v111\nv_cmp_eq_u32 s[70:71], -1, v116\nv_sub_f32 v114, v96, v100\nv_mfma_f32_32x32x16_bf16 v[48:63], a[112:115], v[80:83], v[48:63]\n" \
    "ds_read_b128 v[64:67], v1 offset:32768\ns_waitcnt lgkmcnt(3)\nv_add_f32 v96, v96, v100\nv_add_f32 v97, v97, v101\nv_mul_f32 v114, v114, v114\nv_mul_f32 v96, 0.5, v96\n" \
    "v_fmac_f32 v97, 0x41000000, v114\nv_mfma_f32_32x32x16_bf16 v[48:63], a[116:119], v[84:87], v[48:63]\nds_read_b128 v[68:71], v1 offset:33792\ns_waitcnt lgkmcnt(3)\n" \
    "v_sub_f32 v114, v104, v108\nv_add_f32 v104, v104, v108\nv_add_f32 v105, v105, v109\nv_mul_f32 v114, v114, v114\nv_mul_f32 v104, 0.5, v104\n" \
    "v_mfma_f32_32x32x16_bf16 v[48:63], a[120:123], v[88:91], v[48:63]\nds_read_b128 v[72:75], v1 offset:34816\ns_waitcnt lgkmcnt(3)\nv_fmac_f32 v105, 0x41000000, v114\n" \
    "v_sub_f32 v114, v96, v104\nv_add_f32 v96, v96, v104\nv_add_f32 v97, v97, v105\nv_mul_f32 v114, v114, v114\n" \
    "v_mfma_f32_32x32x16_bf16 v[48:63], a[124:127], v[92:95], v[48:63]\nds_read_b128 v[76:79], v1 offset:35840\ns_waitcnt lgkmcnt(3)\nv_mul_f32 v96, 0.5, v96\n" \
    "v_fmac_f32 v97, 0x41800000, v114\nv_sub_f32 v114, v98, v102\nv_add_f32 v98, v98, v102\nv_add_f32 v99, v99, v103\n" \
    "v_mfma_f32_32x32x16_bf16 v[48:63], a[128:131], v[64:67], v[48:63]\nds_read_b128 v[80:83], v1 offset:36864\ns_waitcnt lgkmcnt(3)\nv_mul_f32 v114, v114, v114\n" \
    "v_mul_f32 v98, 0.5, v98\nv_fmac_f32 v99, 0x41000000, v114\nv_sub_f32 v114, v106, v110\nv_add_f32 v106, v106, v110\n" \
    "v_mfma_f32_32x32x16_bf16 v[48:63], a[132:135], v[68:71], v[48:63]\nds_read_b128 v[84:87], v1 offset:37888\ns_waitcnt lgkmcnt(3)\nv_add_f32 v107, v107, v111\n" \
    "v_mul_f32 v114, v114, v114\nv_mul_f32 v106, 0.5, v106\nv_fmac_f32 v107, 0x41000000, v114\nv_sub_f32 v114, v98, v106\n" \
    "v_mfma_f32_32x32x16_bf16 v[48:63], a[136:139], v[72:75], v[48:63]\nds_read_b128 v[88:91], v1 offset:38912\ns_waitcnt lgkmcnt(3)\nv_add_f32 v98, v98, v106\n" \
    "v_add_f32 v99, v99, v107\nv_mul_f32 v114, v114, v114\nv_mul_f32 v98, 0.5, v98\nv_fmac_f32 v99, 0x41800000, v114\n" \
    "v_mfma_f32_32x32x16_bf16 v[48:63], a[140:143], v[76:79], v[48:63]\nds_read_b128 v[92:95], v1 offset:39936\ns_waitcnt lgkmcnt(3)\nv_mov_b64 v[100:101], v[96:97]\n" \
    "v_mov_b64 v[102:103], v[98:99]\nv_mfma_f32_32x32x16_bf16 v[48:63], a[144:147], v[80:83], v[48:63]\nds_read_b128 v[64:67], v1 offset:40960\ns_waitcnt lgkmcnt(3)\n" \
    "v_permlane16_swap_b32 v96, v100\nv_permlane16_swap_b32 v97, v101\nv_permlane16_swap_b32 v98, v102\nv_mfma_f32_32x32x16_bf16 v[48:63], a[148:151], v[84:87], v[48:63]\n" \
    "ds_read_b128 v[68:71], v1 offset:41984\ns_waitcnt lgkmcnt(3)\nv_permlane16_swap_b32 v99, v103\nv_sub_f32 v114, v96, v100\nv_add_f32 v96, v96, v100\n" \
    "v_add_f32 v97, v97, v101\nv_mfma_f32_32x32x16_bf16 v[48:63], a[152:155], v[88:91], v[48:63]\nds_read_b128 v[72:75], v1 offset:43008\ns_waitcnt lgkmcnt(3)\n" \
    "v_mul_f32 v114, v114, v114\nv_mul_f32 v96, 0.5, v96\nv_fmac_f32 v97, 0x42000000, v114\nv_sub_f32 v114, v98, v102\nv_add_f32 v98, v98, v102\n" \
    "v_mfma_f32_32x32x16_bf16 v[48:63], a[156:159], v[92:95], v[48:63]\nds_read_b128 v[76:79], v1 offset:44032\ns_waitcnt lgkmcnt(3)\nv_add_f32 v99, v99, v103\n" \
    "v_mul_f32 v114, v114, v114\nv_mul_f32 v98, 0.5, v98\nv_fmac_f32 v99, 0x42000000, v114\nv_mov_b64 v[100:101], v[96:97]\n" \
    "v_mfma_f32_32x32x16_bf16 v[48:63], a[160:163], v[64:67], v[48:63]\nds_read_b128 v[80:83], v1 offset:45056\nbuffer_load_dword v150, off, s[64:67], s85 sc1\n" \
    "s_waitcnt lgkmcnt(3)\nv_mov_b64 v[102:103], v[98:99]\nv_permlane32_swap_b32 v96, v100\nv_permlane32_swap_b32 v97, v101\n" \
    "v_mfma_f32_32x32x16_bf16 v[48:63], a[164:167], v[68:71], v[48:63]\nds_read_b128 v[84:87], v1 offset:46080\ns_waitcnt lgkmcnt(3)\nv_permlane32_swap_b32 v98, v102\n" \
    "v_permlane32_swap_b32 v99, v103\nv_sub_f32 v114, v96, v100\nv_mfma_f32_32x32x16_bf16 v[48:63], a[168:171], v[72:75], v[48:63]\nds_read_b128 v[88:91], v1 offset:47104\n" \
    "s_waitcnt lgkmcnt(3)\nv_add_f32 v96, v96, v100\nv_add_f32 v97, v97, v101\nv_mul_f32 v114, v114, v114\nv_mul_f32 v96, 0.5, v96\nv_fmac_f32 v97, 0x42800000, v114\n" \
    "v_mfma_f32_32x32x16_bf16 v[48:63], a[172:175], v[76:79], v[48:63]\nds_read_b128 v[92:95], v1 offset:48128\ns_waitcnt vmcnt(1) lgkmcnt(3)\ns_barrier\n" \
    "v_sub_f32 v114, v98, v102\nv_add_f32 v98, v98, v102\nv_add_f32 v99, v99, v103\nv_mul_f32 v114, v114, v114\nv_mul_f32 v98, 0.5, v98\n" \
    "v_mfma_f32_32x32x16_bf16 v[48:63], a[176:179], v[80:83], v[48:63]\nds_read_b128 v[64:67], v1 offset:49152\ns_waitcnt lgkmcnt(3)\nv_fmac_f32 v99, 0x42800000, v114\n" \
    "v_mfma_f32_32x32x16_bf16 v[48:63], a[180:183], v[84:87], v[48:63]\nds_read_b128 v[68:71], v1 offset:50176\ns_cmp_lg_u64 s[70:71], 0\ns_cselect_b32 s76, -1, 0\n" \
    "s_waitcnt lgkmcnt(3)\nv_min_u32 v96, -2, v96\nv_min_u32 v98, -2, v98\nv_or_b32 v96, s76, v96\nv_or_b32 v98, s76, v98\n" \
    "v_mfma_f32_32x32x16_bf16 v[48:63], a[184:187], v[88:91], v[48:63]\nds_read_b128 v[72:75], v1 offset:51200\nds_write_b128 v3, v[96:99]\ns_waitcnt lgkmcnt(4)\n" \
    "v_mfma_f32_32x32x16_bf16 v[48:63], a[188:191], v[92:95], v[48:63]\nds_read_b128 v[76:79], v1 offset:52224\ns_waitcnt lgkmcnt(4)\n" \
    "v_mfma_f32_32x32x16_bf16 v[48:63], a[192:195], v[64:67], v[48:63]\nds_read_b128 v[80:83], v1 offset:53248\ns_waitcnt lgkmcnt(2)\ns_barrier\n" \
    "ds_read_b64 v[96:97], v4 offset:0\nds_read_b64 v[98:99], v4 offset:256\nds_read_b64 v[100:101], v4 offset:512\n" \
    "v_mfma_f32_32x32x16_bf16 v[48:63], a[196:199], v[68:71], v[48:63]\nds_read_b128 v[84:87], v1 offset:54272\nds_read_b64 v[102:103], v4 offset:768\n" \
    "v_mfma_f32_32x32x16_bf16 v[48:63], a[200:203], v[72:75], v[48:63]\nds_read_b128 v[88:91], v1 offset:55296\ns_waitcnt lgkmcnt(7)\n" \
    "v_mfma_f32_32x32x16_bf16 v[48:63], a[204:207], v[76:79], v[48:63]\nds_read_b128 v[92:95], v1 offset:56320\ns_waitcnt lgkmcnt(2)\nv_max3_u32 v116, v96, v98, v100\n" \
    "v_max_u32 v116, v116, v102\nv_cmp_eq_u32 s[70:71], -1, v116\nv_sub_f32 v114, v96, v98\nv_add_f32 v96, v96, v98\n" \
    "v_mfma_f32_32x32x16_bf16 v[48:63], a[208:211], v[80:83], v[48:63]\nds_read_b128 v[64:67], v1 offset:57344\nv_add_f32 v97, v97, v99\nv_mul_f32 v114, v114, v114\n" \
    "v_mul_f32 v96, 0.5, v96\nv_fmac_f32 v97, 0x43000000, v114\nv_sub_f32 v114, v100, v102\nv_mfma_f32_32x32x16_bf16 v[48:63], a[212:215], v[84:87], v[48:63]\n" \
    "ds_read_b128 v[68:71], v1 offset:58368\ns_waitcnt lgkmcnt(3)\nv_add_f32 v100, v100, v102\nv_add_f32 v101, v101, v103\nv_mul_f32 v114, v114, v114\n" \
    "v_mul_f32 v100, 0.5, v100\nv_fmac_f32 v101, 0x43000000, v114\nv_mfma_f32_32x32x16_bf16 v[48:63], a[216:219], v[88:91], v[48:63]\nds_read_b128 v[72:75], v1 offset:59392\n" \
    "s_waitcnt lgkmcnt(3)\nv_sub_f32 v114, v96, v100\nv_add_f32 v96, v96, v100\nv_add_f32 v97, v97, v101\nv_mul_f32 v114, v114, v114\nv_mul_f32 v96, 0.5, v96\n" \
    "v_mfma_f32_32x32x16_bf16 v[48:63], a[220:223], v[92:95], v[48:63]\nds_read_b128 v[76:79], v1 offset:60416\ns_waitcnt lgkmcnt(3)\nv_fmac_f32 v97, 0x43800000, v114\n" \
    "v_mul_f32 v98, 0x3a800000, v97\nv_cvt_pk_bf16_f32 v96, 0, v96\nv_cvt_pk_bf16_f32 v98, 0, v98\nv_add_f32 v98, 0x3727c5ac, v98\n" \
    "v_mfma_f32_32x32x16_bf16 v[48:63], a[224:227], v[64:67], v[48:63]\nds_read_b128 v[80:83], v1 offset:61440\ns_waitcnt lgkmcnt(3)\nv_cvt_pk_bf16_f32 v98, 0, v98\n" \
    "v_sqrt_f32 v98, v98\nv_mfma_f32_32x32x16_bf16 v[48:63], a[228:231], v[68:71], v[48:63]\nds_read_b128 v[84:87], v1 offset:62464\ns_waitcnt lgkmcnt(3)\n" \
    "v_cvt_pk_bf16_f32 v98, 0, v98\nv_rcp_f32 v98, v98\nv_mfma_f32_32x32x16_bf16 v[48:63], a[232:235], v[72:75], v[48:63]\nds_read_b128 v[88:91], v1 offset:63488\n" \
    "s_waitcnt vmcnt(0)\ns_waitcnt lgkmcnt(3)\nv_and_b32 v112, 127, v150\nv_cmp_gt_u32 vcc, s77, v112\nv_mfma_f32_32x32x16_bf16 v[48:63], a[236:239], v[76:79], v[48:63]\n" \
    "ds_read_b128 v[92:95], v1 offset:64512\ns_cmp_eq_u64 vcc, 0\ns_cselect_b32 s81, 1, 0\ns_waitcnt lgkmcnt(3)\n" \
    "v_mfma_f32_32x32x16_bf16 v[48:63], a[240:243], v[80:83], v[48:63]\ns_waitcnt lgkmcnt(2)\nv_mfma_f32_32x32x16_bf16 v[48:63], a[244:247], v[84:87], v[48:63]\n" \
    "s_waitcnt lgkmcnt(0)\ns_memrealtime s[72:73]\nv_mfma_f32_32x32x16_bf16 v[48:63], a[248:251], v[88:91], v[48:63]\n" \
    "v_mfma_f32_32x32x16_bf16 v[48:63], a[252:255], v[92:95], v[48:63]\ns_waitcnt lgkmcnt(0)\ns_cmp_ge_u32 s77, 64\ns_cselect_b32 s69, s60, s34\ns_sub_u32 s72, s72, s69\n" \
    "s_lshl_b32 s72, s72, 8\nv_sub_u32 v113, s72, v150\nv_cmp_gt_i32 s[74:75], 0, v113\nv_cmp_eq_u32 vcc, s77, v112\ns_nop 0\ns_and_b64 vcc, vcc, s[74:75]\n" \
    "s_cselect_b32 s69, 1, 0\ns_lshl_b32 s81, s81, s69\ns_cmp_lg_u64 s[70:71], 0\ns_cbranch_scc1 .Lf9_%=\nv_cmp_u_f32 vcc, v48, v48\ns_nop 0\ns_cbranch_vccnz .Lf7_%=\n.Lf8_%=:\n" \
    "s_cmp_eq_u32 s81, 1\ns_cbranch_scc1 .Lf15_%=\nv_pk_fma_f32 v[48:49], v[16:17], v[96:97], v[48:49] op_sel_hi:[1,0,1] neg_lo:[0,1,0] neg_hi:[0,1,0]\n" \
    "v_pk_fma_f32 v[50:51], v[18:19], v[96:97], v[50:51] op_sel_hi:[1,0,1] neg_lo:[0,1,0] neg_hi:[0,1,0]\n" \
    "v_pk_fma_f32 v[52:53], v[20:21], v[96:97], v[52:53] op_sel_hi:[1,0,1] neg_lo:[0,1,0] neg_hi:[0,1,0]\n" \
    "v_pk_fma_f32 v[54:55], v[22:23], v[96:97], v[54:55] op_sel_hi:[1,0,1] neg_lo:[0,1,0] neg_hi:[0,1,0]\nv_pk_fma_f32 v[48:49], v[48:49], v[98:99], v[32:33] op_sel_hi:[1,0,1]\n" \
    "v_pk_fma_f32 v[50:51], v[50:51], v[98:99], v[34:35] op_sel_hi:[1,0,1]\nv_pk_fma_f32 v[52:53], v[52:53], v[98:99], v[36:37] op_sel_hi:[1,0,1]\n" \
    "v_pk_fma_f32 v[54:55], v[54:55], v[98:99], v[38:39] op_sel_hi:[1,0,1]\nv_pk_mul_f32 v[72:73], v[48:49], v[48:49]\nv_pk_mul_f32 v[74:75], v[50:51], v[50:51]\n" \
    "v_pk_mul_f32 v[76:77], v[52:53], v[52:53]\nv_pk_mul_f32 v[78:79], v[54:55], v[54:55]\nv_pk_fma_f32 v[72:73], v[72:73], v[6:7], v[8:9]\n" \
    "v_pk_fma_f32 v[74:75], v[74:75], v[6:7], v[8:9]\nv_pk_fma_f32 v[76:77], v[76:77], v[6:7], v[8:9]\nv_pk_fma_f32 v[78:79], v[78:79], v[6:7], v[8:9]\n" \
    "v_pk_mul_f32 v[72:73], v[72:73], v[48:49]\nv_pk_mul_f32 v[74:75], v[74:75], v[50:51]\nv_pk_mul_f32 v[76:77], v[76:77], v[52:53]\nv_pk_mul_f32 v[78:79], v[78:79], v[54:55]\n" \
    "v_exp_f32 v72, v72\nv_exp_f32 v73, v73\nv_exp_f32 v74, v74\nv_exp_f32 v75, v75\nv_exp_f32 v76, v76\nv_exp_f32 v77, v77\nv_exp_f32 v78, v78\nv_exp_f32 v79, v79\n" \
    "v_pk_add_f32 v[72:73], v[72:73], v[10:11]\nv_pk_add_f32 v[74:75], v[74:75], v[10:11]\nv_pk_add_f32 v[76:77], v[76:77], v[10:11]\nv_pk_add_f32 v[78:79], v[78:79], v[10:11]\n" \
    "v_rcp_f32 v72, v72\nv_rcp_f32 v73, v73\nv_rcp_f32 v74, v74\nv_rcp_f32 v75, v75\nv_rcp_f32 v76, v76\nv_rcp_f32 v77, v77\nv_rcp_f32 v78, v78\nv_rcp_f32 v79, v79\n" \
    "v_pk_mul_f32 v[72:73], v[72:73], v[48:49]\nv_pk_mul_f32 v[74:75], v[74:75], v[50:51]\nv_pk_mul_f32 v[76:77], v[76:77], v[52:53]\nv_pk_mul_f32 v[78:79], v[78:79], v[54:55]\n" \
    "v_cvt_pk_bf16_f32 v64, v72, v73\nv_cvt_pk_bf16_f32 v65, v74, v75\nv_cvt_pk_bf16_f32 v66, v76, v77\nv_cvt_pk_bf16_f32 v67, v78, v79\nv_min_u32 v64, -2, v64\n" \
    "v_min_u32 v65, -2, v65\nv_min_u32 v66, -2, v66\nv_min_u32 v67, -2, v67\nbuffer_store_dwordx4 v[64:67], v0, s[64:67], s82 offen offset:0 sc1\n" \
    "v_pk_fma_f32 v[56:57], v[24:25], v[96:97], v[56:57] op_sel_hi:[1,0,1] neg_lo:[0,1,0] neg_hi:[0,1,0]\n" \
    "v_pk_fma_f32 v[58:59], v[26:27], v[96:97], v[58:59] op_sel_hi:[1,0,1] neg_lo:[0,1,0] neg_hi:[0,1,0]\n" \
    "v_pk_fma_f32 v[60:61], v[28:29], v[96:97], v[60:61] op_sel_hi:[1,0,1] neg_lo:[0,1,0] neg_hi:[0,1,0]\n" \
    "v_pk_fma_f32 v[62:63], v[30:31], v[96:97], v[62:63] op_sel_hi:[1,0,1] neg_lo:[0,1,0] neg_hi:[0,1,0]\nv_pk_fma_f32 v[56:57], v[56:57], v[98:99], v[40:41] op_sel_hi:[1,0,1]\n" \
    "v_pk_fma_f32 v[58:59], v[58:59], v[98:99], v[42:43] op_sel_hi:[1,0,1]\nv_pk_fma_f32 v[60:61], v[60:61], v[98:99], v[44:45] op_sel_hi:[1,0,1]\n" \
    "v_pk_fma_f32 v[62:63], v[62:63], v[98:99], v[46:47] op_sel_hi:[1,0,1]\nv_pk_mul_f32 v[72:73], v[56:57], v[56:57]\nv_pk_mul_f32 v[74:75], v[58:59], v[58:59]\n" \
    "v_pk_mul_f32 v[76:77], v[60:61], v[60:61]\nv_pk_mul_f32 v[78:79], v[62:63], v[62:63]\nv_pk_fma_f32 v[72:73], v[72:73], v[6:7], v[8:9]\n" \
    "v_pk_fma_f32 v[74:75], v[74:75], v[6:7], v[8:9]\nv_pk_fma_f32 v[76:77], v[76:77], v[6:7], v[8:9]\nv_pk_fma_f32 v[78:79], v[78:79], v[6:7], v[8:9]\n" \
    "v_pk_mul_f32 v[72:73], v[72:73], v[56:57]\nv_pk_mul_f32 v[74:75], v[74:75], v[58:59]\nv_pk_mul_f32 v[76:77], v[76:77], v[60:61]\nv_pk_mul_f32 v[78:79], v[78:79], v[62:63]\n" \
    "v_exp_f32 v72, v72\nv_exp_f32 v73, v73\nv_exp_f32 v74, v74\nv_exp_f32 v75, v75\nv_exp_f32 v76, v76\nv_exp_f32 v77, v77\nv_exp_f32 v78, v78\nv_exp_f32 v79, v79\n" \
    "v_pk_add_f32 v[72:73], v[72:73], v[10:11]\nv_pk_add_f32 v[74:75], v[74:75], v[10:11]\nv_pk_add_f32 v[76:77], v[76:77], v[10:11]\nv_pk_add_f32 v[78:79], v[78:79], v[10:11]\n" \
    "v_rcp_f32 v72, v72\nv_rcp_f32 v73, v73\nv_rcp_f32 v74, v74\nv_rcp_f32 v75, v75\nv_rcp_f32 v76, v76\nv_rcp_f32 v77, v77\nv_rcp_f32 v78, v78\nv_rcp_f32 v79, v79\n" \
    "v_pk_mul_f32 v[72:73], v[72:73], v[56:57]\nv_pk_mul_f32 v[74:75], v[74:75], v[58:59]\nv_pk_mul_f32 v[76:77], v[76:77], v[60:61]\nv_pk_mul_f32 v[78:79], v[78:79], v[62:63]\n" \
    "v_cvt_pk_bf16_f32 v68, v72, v73\nv_cvt_pk_bf16_f32 v69, v74, v75\nv_cvt_pk_bf16_f32 v70, v76, v77\nv_cvt_pk_bf16_f32 v71, v78, v79\nv_min_u32 v68, -2, v68\n" \
    "v_min_u32 v69, -2, v69\nv_min_u32 v70, -2, v70\nv_min_u32 v71, -2, v71\nbuffer_store_dwordx4 v[68:71], v0, s[64:67], s82 offen offset:1024 sc1\n.Lf16_%=:\n" \
    "s_mov_b32 s38, s85\ns_xor_b32 s80, s80, 0x10000\nv_xor_b32 v1, 0x10000, v1\ns_mov_b32 s68, 0\ns_add_u32 s32, s32, 1\ns_cmp_lt_u32 s32, 64\ns_cbranch_scc1 .Lf2_%=\n" \
    "s_endpgm\n.Lf4_%=:\ns_sleep 8\ns_add_u32 s68, s68, 1\ns_and_b32 s69, s68, 63\ns_cbranch_scc1 .Lf3_%=\ns_cmp_gt_u32 s68, 0x400000\ns_cbranch_scc1 .Lf1_%=\n" \
    "buffer_load_dword v253, off, s[64:67], 0 sc1\ns_waitcnt vmcnt(0)\nv_readfirstlane_b32 s69, v253\ns_nop 0\ns_cmp_eq_u32 s69, 1\ns_cbranch_scc0 .Lf3_%=\ns_endpgm\n.Lf13_%=:\n" \
    "s_waitcnt vmcnt(0)\ns_add_u32 s68, s68, 1\ns_and_b32 s69, s68, 63\ns_cbranch_scc1 .Lf12_%=\ns_cmp_gt_u32 s68, 0x400000\ns_cbranch_scc1 .Lf1_%=\n" \
    "buffer_load_dword v253, off, s[64:67], 0 sc1\ns_waitcnt vmcnt(0)\nv_readfirstlane_b32 s69, v253\ns_nop 0\ns_cmp_eq_u32 s69, 1\ns_cbranch_scc0 .Lf12_%=\ns_endpgm\n" \
    ".Lf15_%=:\ns_xor_b32 s84, s84, 0x10000\nbuffer_load_dword v151, v2, s[64:67], s83 offen sc1\n" \
    "v_pk_fma_f32 v[48:49], v[16:17], v[96:97], v[48:49] op_sel_hi:[1,0,1] neg_lo:[0,1,0] neg_hi:[0,1,0]\n" \
    "v_pk_fma_f32 v[50:51], v[18:19], v[96:97], v[50:51] op_sel_hi:[1,0,1] neg_lo:[0,1,0] neg_hi:[0,1,0]\n" \
    "v_pk_fma_f32 v[52:53], v[20:21], v[96:97], v[52:53] op_sel_hi:[1,0,1] neg_lo:[0,1,0] neg_hi:[0,1,0]\n" \
    "v_pk_fma_f32 v[54:55], v[22:23], v[96:97], v[54:55] op_sel_hi:[1,0,1] neg_lo:[0,1,0] neg_hi:[0,1,0]\nv_pk_fma_f32 v[48:49], v[48:49], v[98:99], v[32:33] op_sel_hi:[1,0,1]\n" \
    "v_pk_fma_f32 v[50:51], v[50:51], v[98:99], v[34:35] op_sel_hi:[1,0,1]\nv_pk_fma_f32 v[52:53], v[52:53], v[98:99], v[36:37] op_sel_hi:[1,0,1]\n" \
    "v_pk_fma_f32 v[54:55], v[54:55], v[98:99], v[38:39] op_sel_hi:[1,0,1]\nv_pk_mul_f32 v[72:73], v[48:49], v[48:49]\nv_pk_mul_f32 v[74:75], v[50:51], v[50:51]\n" \
    "v_pk_mul_f32 v[76:77], v[52:53], v[52:53]\nv_pk_mul_f32 v[78:79], v[54:55], v[54:55]\ns_add_u32 m0, s84, 0\ns_add_u32 s69, s83, s48\n" \
    "buffer_load_dwordx4 v0, s[64:67], s69 offen offset:0 sc1 lds\nv_pk_fma_f32 v[72:73], v[72:73], v[6:7], v[8:9]\nv_pk_fma_f32 v[74:75], v[74:75], v[6:7], v[8:9]\n" \
    "v_pk_fma_f32 v[76:77], v[76:77], v[6:7], v[8:9]\nv_pk_fma_f32 v[78:79], v[78:79], v[6:7], v[8:9]\nv_pk_mul_f32 v[72:73], v[72:73], v[48:49]\n" \
    "v_pk_mul_f32 v[74:75], v[74:75], v[50:51]\nv_pk_mul_f32 v[76:77], v[76:77], v[52:53]\nv_pk_mul_f32 v[78:79], v[78:79], v[54:55]\nv_exp_f32 v72, v72\nv_exp_f32 v73, v73\n" \
    "v_exp_f32 v74, v74\nv_exp_f32 v75, v75\nbuffer_load_dwordx4 v0, s[64:67], s69 offen offset:1024 sc1 lds\nv_exp_f32 v76, v76\nv_exp_f32 v77, v77\nv_exp_f32 v78, v78\n" \
    "v_exp_f32 v79, v79\nv_pk_add_f32 v[72:73], v[72:73], v[10:11]\nv_pk_add_f32 v[74:75], v[74:75], v[10:11]\nv_pk_add_f32 v[76:77], v[76:77], v[10:11]\n" \
    "v_pk_add_f32 v[78:79], v[78:79], v[10:11]\nv_rcp_f32 v72, v72\nv_rcp_f32 v73, v73\nv_rcp_f32 v74, v74\nv_rcp_f32 v75, v75\ns_add_u32 m0, s84, 8192\n" \
    "s_add_u32 s69, s83, s49\nbuffer_load_dwordx4 v0, s[64:67], s69 offen offset:0 sc1 lds\nv_rcp_f32 v76, v76\nv_rcp_f32 v77, v77\nv_rcp_f32 v78, v78\nv_rcp_f32 v79, v79\n" \
    "v_pk_mul_f32 v[72:73], v[72:73], v[48:49]\nv_pk_mul_f32 v[74:75], v[74:75], v[50:51]\nv_pk_mul_f32 v[76:77], v[76:77], v[52:53]\nv_pk_mul_f32 v[78:79], v[78:79], v[54:55]\n" \
    "v_cvt_pk_bf16_f32 v64, v72, v73\nv_cvt_pk_bf16_f32 v65, v74, v75\nv_cvt_pk_bf16_f32 v66, v76, v77\nv_cvt_pk_bf16_f32 v67, v78, v79\n" \
    "buffer_load_dwordx4 v0, s[64:67], s69 offen offset:1024 sc1 lds\nv_min_u32 v64, -2, v64\nv_min_u32 v65, -2, v65\nv_min_u32 v66, -2, v66\nv_min_u32 v67, -2, v67\n" \
    "buffer_store_dwordx4 v[64:67], v0, s[64:67], s82 offen offset:0 sc1\nv_pk_fma_f32 v[56:57], v[24:25], v[96:97], v[56:57] op_sel_hi:[1,0,1] neg_lo:[0,1,0] neg_hi:[0,1,0]\n" \
    "v_pk_fma_f32 v[58:59], v[26:27], v[96:97], v[58:59] op_sel_hi:[1,0,1] neg_lo:[0,1,0] neg_hi:[0,1,0]\n" \
    "v_pk_fma_f32 v[60:61], v[28:29], v[96:97], v[60:61] op_sel_hi:[1,0,1] neg_lo:[0,1,0] neg_hi:[0,1,0]\n" \
    "v_pk_fma_f32 v[62:63], v[30:31], v[96:97], v[62:63] op_sel_hi:[1,0,1] neg_lo:[0,1,0] neg_hi:[0,1,0]\nv_pk_fma_f32 v[56:57], v[56:57], v[98:99], v[40:41] op_sel_hi:[1,0,1]\n" \
    "v_pk_fma_f32 v[58:59], v[58:59], v[98:99], v[42:43] op_sel_hi:[1,0,1]\nv_pk_fma_f32 v[60:61], v[60:61], v[98:99], v[44:45] op_sel_hi:[1,0,1]\ns_add_u32 m0, s84, 16384\n" \
    "s_add_u32 s69, s83, s50\nbuffer_load_dwordx4 v0, s[64:67], s69 offen offset:0 sc1 lds\nv_pk_fma_f32 v[62:63], v[62:63], v[98:99], v[46:47] op_sel_hi:[1,0,1]\n" \
    "v_pk_mul_f32 v[72:73], v[56:57], v[56:57]\nv_pk_mul_f32 v[74:75], v[58:59], v[58:59]\nv_pk_mul_f32 v[76:77], v[60:61], v[60:61]\nv_pk_mul_f32 v[78:79], v[62:63], v[62:63]\n" \
    "v_pk_fma_f32 v[72:73], v[72:73], v[6:7], v[8:9]\nv_pk_fma_f32 v[74:75], v[74:75], v[6:7], v[8:9]\nv_pk_fma_f32 v[76:77], v[76:77], v[6:7], v[8:9]\n" \
    "v_pk_fma_f32 v[78:79], v[78:79], v[6:7], v[8:9]\nv_pk_mul_f32 v[72:73], v[72:73], v[56:57]\nv_pk_mul_f32 v[74:75], v[74:75], v[58:59]\n" \
    "v_pk_mul_f32 v[76:77], v[76:77], v[60:61]\nbuffer_load_dwordx4 v0, s[64:67], s69 offen offset:1024 sc1 lds\nv_pk_mul_f32 v[78:79], v[78:79], v[62:63]\nv_exp_f32 v72, v72\n" \
    "v_exp_f32 v73, v73\nv_exp_f32 v74, v74\nv_exp_f32 v75, v75\nv_exp_f32 v76, v76\nv_exp_f32 v77, v77\nv_exp_f32 v78, v78\nv_exp_f32 v79, v79\n" \
    "v_pk_add_f32 v[72:73], v[72:73], v[10:11]\nv_pk_add_f32 v[74:75], v[74:75], v[10:11]\nv_pk_add_f32 v[76:77], v[76:77], v[10:11]\nv_pk_add_f32 v[78:79], v[78:79], v[10:11]\n" \
    "v_rcp_f32 v72, v72\nv_rcp_f32 v73, v73\nv_rcp_f32 v74, v74\nv_rcp_f32 v75, v75\nv_rcp_f32 v76, v76\nv_rcp_f32 v77, v77\nv_rcp_f32 v78, v78\nv_rcp_f32 v79, v79\n" \
    "v_pk_mul_f32 v[72:73], v[72:73], v[56:57]\nv_pk_mul_f32 v[74:75], v[74:75], v[58:59]\nv_pk_mul_f32 v[76:77], v[76:77], v[60:61]\nv_pk_mul_f32 v[78:79], v[78:79], v[62:63]\n" \
    "v_cvt_pk_bf16_f32 v68, v72, v73\nv_cvt_pk_bf16_f32 v69, v74, v75\nv_cvt_pk_bf16_f32 v70, v76, v77\nv_cvt_pk_bf16_f32 v71, v78, v79\nv_min_u32 v68, -2, v68\n" \
    "v_min_u32 v69, -2, v69\nv_min_u32 v70, -2, v70\nv_min_u32 v71, -2, v71\nbuffer_store_dwordx4 v[68:71], v0, s[64:67], s82 offen offset:1024 sc1\ns_branch .Lf16_%=\n" \
    ".Lf7_%=:\nv_mov_b32 v120, 0\nv_mov_b32 v121, v1\ns_mov_b32 s76, 16\n.Lf10_%=:\nds_read_b128 v[124:127], v121 offset:0\nds_read_b128 v[128:131], v121 offset:1024\n" \
    "ds_read_b128 v[132:135], v121 offset:2048\nds_read_b128 v[136:139], v121 offset:3072\nv_add_u32 v121, 0x1000, v121\ns_waitcnt lgkmcnt(0)\n" \
    "v_max3_u32 v120, v120, v124, v125\nv_max3_u32 v120, v120, v126, v127\nv_max3_u32 v120, v120, v128, v129\nv_max3_u32 v120, v120, v130, v131\n" \
    "v_max3_u32 v120, v120, v132, v133\nv_max3_u32 v120, v120, v134, v135\nv_max3_u32 v120, v120, v136, v137\nv_max3_u32 v120, v120, v138, v139\ns_sub_u32 s76, s76, 1\n" \
    "s_cmp_lg_u32 s76, 0\ns_cbranch_scc1 .Lf10_%=\nv_cmp_eq_u32 vcc, -1, v120\ns_nop 0\ns_cbranch_vccz .Lf8_%=\n.Lf9_%=:\ns_mov_b32 s81, 0\ns_barrier\ns_sleep 4\n" \
    "s_add_u32 s68, s68, 1\ns_and_b32 s69, s68, 63\ns_cbranch_scc1 .Lf2_%=\ns_cmp_gt_u32 s68, 0x400000\ns_cbranch_scc1 .Lf1_%=\nbuffer_load_dword v253, off, s[64:67], 0 sc1\n" \
    "s_waitcnt vmcnt(0)\nv_readfirstlane_b32 s69, v253\ns_nop 0\ns_cmp_eq_u32 s69, 1\ns_cbranch_scc0 .Lf2_%=\ns_endpgm\n.Lf1_%=:\nv_mov_b32 v253, 1\n" \
    "buffer_store_dword v253, off, s[64:67], 0 sc1\ns_endpgm\n"
#define ASM_F2 \
    "s_mov_b32 s90, 0x7fff0000\ns_mov_b32 s91, 0x7fff0000\ns_mov_b32 s92, 0x7fff0000\ns_mov_b32 s93, 0x7fff0000\ns_mov_b32 s80, 0\ns_mov_b32 s81, 0\ns_mov_b32 s68, 0\n.Lg2_%=:\n" \
    "s_xor_b32 s85, s38, 512\ns_and_b32 s69, s32, 63\ns_lshl_b32 s69, s69, 19\ns_add_u32 s82, s36, s69\ns_lshr_b32 s69, s82, 8\ns_and_b32 s69, s69, 0x2000\n" \
    "s_xor_b32 s82, s82, s69\ns_add_u32 s69, s32, 1\ns_and_b32 s69, s69, 63\ns_lshl_b32 s69, s69, 19\ns_add_u32 s83, s36, s69\ns_lshr_b32 s69, s83, 8\n" \
    "s_and_b32 s69, s69, 0x2000\ns_xor_b32 s83, s83, s69\ns_add_u32 s84, s37, s80\ns_cmp_eq_u32 s81, 1\ns_cbranch_scc1 .Lg6_%=\ns_add_u32 s77, s32, 1\ns_cmp_eq_u32 s81, 2\n" \
    "s_cbranch_scc1 .Lg5_%=\n.Lg3_%=:\nbuffer_load_dword v150, v3, s[64:67], 0 offen sc1\ns_waitcnt vmcnt(0)\nv_and_b32 v112, 127, v150\nv_cmp_gt_u32 vcc, s77, v112\ns_nop 0\n" \
    "s_cbranch_vccnz .Lg4_%=\n.Lg5_%=:\nv_and_b32 v112, 127, v150\n.Lg11_%=:\ns_sleep 1\ns_memrealtime s[72:73]\ns_waitcnt lgkmcnt(0)\ns_cmp_ge_u32 s77, 64\n" \
    "s_cselect_b32 s69, s60, s34\ns_sub_u32 s72, s72, s69\ns_lshl_b32 s72, s72, 8\nv_sub_u32 v113, s72, v150\nv_cmp_gt_i32 s[74:75], 0, v113\nv_cmp_eq_u32 vcc, s77, v112\n" \
    "s_nop 0\ns_and_b64 vcc, vcc, s[74:75]\ns_cbranch_scc1 .Lg11_%=\n.Lg12_%=:\nbuffer_load_dword v151, v2, s[64:67], s82 offen sc1\ns_add_u32 m0, s84, 0\n" \
    "s_add_u32 s69, s82, s48\nbuffer_load_dwordx4 v0, s[64:67], s69 offen offset:0 sc1 lds\nbuffer_load_dwordx4 v0, s[64:67], s69 offen offset:1024 sc1 lds\n" \
    "s_add_u32 m0, s84, 8192\ns_add_u32 s69, s82, s49\nbuffer_load_dwordx4 v0, s[64:67], s69 offen offset:0 sc1 lds\n" \
    "buffer_load_dwordx4 v0, s[64:67], s69 offen offset:1024 sc1 lds\ns_add_u32 m0, s84, 16384\ns_add_u32 s69, s82, s50\n" \
    "buffer_load_dwordx4 v0, s[64:67], s69 offen offset:0 sc1 lds\nbuffer_load_dwordx4 v0, s[64:67], s69 offen offset:1024 sc1 lds\n.Lg6_%=:\ns_waitcnt vmcnt(6)\n" \
    "v_cmp_eq_u32 vcc, -1, v151\ns_nop 0\ns_cbranch_vccnz .Lg13_%=\ns_add_u32 s77, s32, 2\ns_add_u32 m0, s84, 24576\ns_add_u32 s69, s82, s51\n" \
    "buffer_load_dwordx4 v0, s[64:67], s69 offen offset:0 sc1 lds\nbuffer_load_dwordx4 v0, s[64:67], s69 offen offset:1024 sc1 lds\ns_waitcnt vmcnt(6)\ns_barrier\n" \
    "ds_read_b128 v[64:67], v1 offset:0\nds_read_b128 v[68:71], v1 offset:1024\nds_read_b128 v[72:75], v1 offset:2048\nds_read_b128 v[76:79], v1 offset:3072\n" \
    "s_waitcnt lgkmcnt(3)\nv_mfma_f32_32x32x16_bf16 v[48:63], a[0:3], v[64:67], 0\nds_read_b128 v[80:83], v1 offset:4096\ns_add_u32 m0, s84, 32768\ns_add_u32 s69, s82, s52\n" \
    "buffer_load_dwordx4 v0, s[64:67], s69 offen offset:0 sc1 lds\ns_waitcnt lgkmcnt(3)\nv_mfma_f32_32x32x16_bf16 v[48:63], a[4:7], v[68:71], v[48:63]\n" \
    "ds_read_b128 v[84:87], v1 offset:5120\ns_waitcnt lgkmcnt(3)\nv_mfma_f32_32x32x16_bf16 v[48:63], a[8:11], v[72:75], v[48:63]\nds_read_b128 v[88:91], v1 offset:6144\n" \
    "buffer_load_dwordx4 v0, s[64:67], s69 offen offset:1024 sc1 lds\ns_waitcnt lgkmcnt(3)\nv_mfma_f32_32x32x16_bf16 v[48:63], a[12:15], v[76:79], v[48:63]\n" \
    "ds_read_b128 v[92:95], v1 offset:7168\ns_waitcnt vmcnt(6) lgkmcnt(3)\ns_barrier\nv_mfma_f32_32x32x16_bf16 v[48:63], a[16:19], v[80:83], v[48:63]\n" \
    "ds_read_b128 v[64:67], v1 offset:8192\ns_add_u32 m0, s84, 40960\ns_add_u32 s69, s82, s53\nbuffer_load_dwordx4 v0, s[64:67], s69 offen offset:0 sc1 lds\n" \
    "s_waitcnt lgkmcnt(3)\nv_mfma_f32_32x32x16_bf16 v[48:63], a[20:23], v[84:87], v[48:63]\nds_read_b128 v[68:71], v1 offset:9216\ns_waitcnt lgkmcnt(3)\n" \
    "v_mfma_f32_32x32x16_bf16 v[48:63], a[24:27], v[88:91], v[48:63]\nds_read_b128 v[72:75], v1 offset:10240\nbuffer_load_dwordx4 v0, s[64:67], s69 offen offset:1024 sc1 lds\n" \
    "s_waitcnt lgkmcnt(3)\nv_mfma_f32_32x32x16_bf16 v[48:63], a[28:31], v[92:95], v[48:63]\nds_read_b128 v[76:79], v1 offset:11264\ns_waitcnt lgkmcnt(3)\n" \
    "v_mfma_f32_32x32x16_bf16 v[48:63], a[32:35], v[64:67], v[48:63]\nds_read_b128 v[80:83], v1 offset:12288\ns_add_u32 m0, s84, 49152\ns_add_u32 s69, s82, s54\n" \
    "buffer_load_dwordx4 v0, s[64:67], s69 offen offset:0 sc1 lds\ns_waitcnt lgkmcnt(3)\nv_mfma_f32_32x32x16_bf16 v[48:63], a[36:39], v[68:71], v[48:63]\n" \
    "ds_read_b128 v[84:87], v1 offset:13312\ns_waitcnt lgkmcnt(3)\nv_mfma_f32_32x32x16_bf16 v[48:63], a[40:43], v[72:75], v[48:63]\nds_read_b128 v[88:91], v1 offset:14336\n" \
    "buffer_load_dwordx4 v0, s[64:67], s69 offen offset:1024 sc1 lds\ns_waitcnt lgkmcnt(3)\nv_mfma_f32_32x32x16_bf16 v[48:63], a[44:47], v[76:79], v[48:63]\n" \
    "ds_read_b128 v[92:95], v1 offset:15360\ns_waitcnt vmcnt(6) lgkmcnt(3)\ns_barrier\nv_mfma_f32_32x32x16_bf16 v[48:63], a[48:51], v[80:83], v[48:63]\n" \
    "ds_read_b128 v[64:67], v1 offset:16384\ns_and_b32 s69, s32, 3\ns_lshl_b32 s69, s69, 19\ns_add_u32 s86, s56, s69\ns_lshr_b32 s69, s86, 8\ns_and_b32 s69, s69, 0x2000\n" \
    "s_waitcnt lgkmcnt(3)\nv_mfma_f32_32x32x16_bf16 v[48:63], a[52:55], v[84:87], v[48:63]\nds_read_b128 v[68:71], v1 offset:17408\ns_xor_b32 s86, s86, s69\n" \
    "s_and_b32 s69, s32, 3\ns_lshl_b32 s69, s69, 19\ns_add_u32 s87, s57, s69\ns_lshr_b32 s69, s87, 8\ns_waitcnt lgkmcnt(3)\n" \
    "v_mfma_f32_32x32x16_bf16 v[48:63], a[56:59], v[88:91], v[48:63]\nds_read_b128 v[72:75], v1 offset:18432\ns_and_b32 s69, s69, 0x2000\ns_xor_b32 s87, s87, s69\n" \
    "s_and_b32 s69, s32, 3\ns_lshl_b32 s69, s69, 19\ns_add_u32 s88, s58, s69\ns_waitcnt lgkmcnt(3)\nv_mfma_f32_32x32x16_bf16 v[48:63], a[60:63], v[92:95], v[48:63]\n" \
    "ds_read_b128 v[76:79], v1 offset:19456\ns_lshr_b32 s69, s88, 8\ns_and_b32 s69, s69, 0x2000\ns_xor_b32 s88, s88, s69\ns_and_b32 s69, s32, 3\ns_lshl_b32 s69, s69, 19\n" \
    "s_waitcnt lgkmcnt(3)\nv_mfma_f32_32x32x16_bf16 v[48:63], a[64:67], v[64:67], v[48:63]\nds_read_b128 v[80:83], v1 offset:20480\ns_add_u32 s89, s59, s69\n" \
    "s_lshr_b32 s69, s89, 8\ns_and_b32 s69, s69, 0x2000\ns_xor_b32 s89, s89, s69\ns_bfe_u32 s78, s32, 0x10002\ns_waitcnt lgkmcnt(3)\n" \
    "v_mfma_f32_32x32x16_bf16 v[48:63], a[68:71], v[68:71], v[48:63]\nds_read_b128 v[84:87], v1 offset:21504\ns_waitcnt lgkmcnt(3)\n" \
    "v_mfma_f32_32x32x16_bf16 v[48:63], a[72:75], v[72:75], v[48:63]\nds_read_b128 v[88:91], v1 offset:22528\nbuffer_load_dwordx2 v[108:109], v4, s[64:67], s93 offen sc1\n" \
    "s_waitcnt lgkmcnt(3)\nv_mfma_f32_32x32x16_bf16 v[48:63], a[76:79], v[76:79], v[48:63]\nds_read_b128 v[92:95], v1 offset:23552\n" \
    "buffer_load_dwordx4 v[96:99], v0, s[64:67], s90 offen sc1\ns_waitcnt lgkmcnt(3)\nv_mfma_f32_32x32x16_bf16 v[48:63], a[80:83], v[80:83], v[48:63]\n" \
    "ds_read_b128 v[64:67], v1 offset:24576\nbuffer_load_dwordx4 v[100:103], v0, s[64:67], s91 offen sc1\ns_waitcnt lgkmcnt(3)\n" \
    "v_mfma_f32_32x32x16_bf16 v[48:63], a[84:87], v[84:87], v[48:63]\nds_read_b128 v[68:71], v1 offset:25600\nbuffer_load_dwordx4 v[104:107], v0, s[64:67], s92 offen sc1\n" \
    "s_waitcnt lgkmcnt(3)\nv_mfma_f32_32x32x16_bf16 v[48:63], a[88:91], v[88:91], v[48:63]\nds_read_b128 v[72:75], v1 offset:26624\ns_waitcnt lgkmcnt(3)\n" \
    "v_mfma_f32_32x32x16_bf16 v[48:63], a[92:95], v[92:95], v[48:63]\nds_read_b128 v[76:79], v1 offset:27648\ns_waitcnt lgkmcnt(3)\n" \
    "v_mfma_f32_32x32x16_bf16 v[48:63], a[96:99], v[64:67], v[48:63]\nds_read_b128 v[80:83], v1 offset:28672\ns_waitcnt lgkmcnt(3)\n" \
    "v_mfma_f32_32x32x16_bf16 v[48:63], a[100:103], v[68:71], v[48:63]\nds_read_b128 v[84:87], v1 offset:29696\ns_waitcnt lgkmcnt(3)\n" \
    "v_mfma_f32_32x32x16_bf16 v[48:63], a[104:107], v[72:75], v[48:63]\nds_read_b128 v[88:91], v1 offset:30720\ns_waitcnt lgkmcnt(3)\n" \
    "v_mfma_f32_32x32x16_bf16 v[48:63], a[108:111], v[76:79], v[48:63]\nds_read_b128 v[92:95], v1 offset:31744\ns_waitcnt vmcnt(6) lgkmcnt(3)\ns_barrier\n" \
    "v_mfma_f32_32x32x16_bf16 v[48:63], a[112:115], v[80:83], v[48:63]\nds_read_b128 v[64:67], v1 offset:32768\ns_waitcnt lgkmcnt(3)\n" \
    "v_mfma_f32_32x32x16_bf16 v[48:63], a[116:119], v[84:87], v[48:63]\nds_read_b128 v[68:71], v1 offset:33792\ns_waitcnt lgkmcnt(3)\n" \
    "v_mfma_f32_32x32x16_bf16 v[48:63], a[120:123], v[88:91], v[48:63]\nds_read_b128 v[72:75], v1 offset:34816\ns_waitcnt lgkmcnt(3)\n" \
    "v_mfma_f32_32x32x16_bf16 v[48:63], a[124:127], v[92:95], v[48:63]\nds_read_b128 v[76:79], v1 offset:35840\ns_waitcnt lgkmcnt(3)\n" \
    "v_mfma_f32_32x32x16_bf16 v[48:63], a[128:131], v[64:67], v[48:63]\nds_read_b128 v[80:83], v1 offset:36864\nbuffer_load_dword v150, v3, s[64:67], 0 offen sc1\n" \
    "s_waitcnt lgkmcnt(3)\nv_mfma_f32_32x32x16_bf16 v[48:63], a[132:135], v[68:71], v[48:63]\nds_read_b128 v[84:87], v1 offset:37888\ns_waitcnt lgkmcnt(3)\n" \
    "v_mfma_f32_32x32x16_bf16 v[48:63], a[136:139], v[72:75], v[48:63]\nds_read_b128 v[88:91], v1 offset:38912\ns_waitcnt lgkmcnt(3)\n" \
    "v_mfma_f32_32x32x16_bf16 v[48:63], a[140:143], v[76:79], v[48:63]\nds_read_b128 v[92:95], v1 offset:39936\ns_waitcnt lgkmcnt(3)\n" \
    "v_mfma_f32_32x32x16_bf16 v[48:63], a[144:147], v[80:83], v[48:63]\nds_read_b128 v[64:67], v1 offset:40960\ns_waitcnt lgkmcnt(3)\n" \
    "v_mfma_f32_32x32x16_bf16 v[48:63], a[148:151], v[84:87], v[48:63]\nds_read_b128 v[68:71], v1 offset:41984\ns_waitcnt vmcnt(1)\ns_waitcnt lgkmcnt(3)\n" \
    "v_max_u32 v116, v108, v109\nv_cmp_eq_u32 s[70:71], -1, v116\nv_mfma_f32_32x32x16_bf16 v[48:63], a[152:155], v[88:91], v[48:63]\nds_read_b128 v[72:75], v1 offset:43008\n" \
    "s_waitcnt lgkmcnt(3)\nv_cndmask_b32 v116, 0, 1, s[70:71]\nv_xor_b32 v96, s79, v96\nv_xor_b32 v97, s79, v97\nv_xor_b32 v98, s79, v98\nv_xor_b32 v99, s79, v99\n" \
    "v_mfma_f32_32x32x16_bf16 v[48:63], a[156:159], v[92:95], v[48:63]\nds_read_b128 v[76:79], v1 offset:44032\ns_waitcnt lgkmcnt(3)\nv_xor_b32 v100, s79, v100\n" \
    "v_xor_b32 v101, s79, v101\nv_xor_b32 v102, s79, v102\nv_xor_b32 v103, s79, v103\nv_xor_b32 v104, s79, v104\n" \
    "v_mfma_f32_32x32x16_bf16 v[48:63], a[160:163], v[64:67], v[48:63]\nds_read_b128 v[80:83], v1 offset:45056\ns_waitcnt lgkmcnt(3)\nv_xor_b32 v105, s79, v105\n" \
    "v_xor_b32 v106, s79, v106\nv_xor_b32 v107, s79, v107\nv_or3_b32 v116, v116, v96, v97\nv_or3_b32 v116, v116, v98, v99\n" \
    "v_mfma_f32_32x32x16_bf16 v[48:63], a[164:167], v[68:71], v[48:63]\nds_read_b128 v[84:87], v1 offset:46080\ns_waitcnt lgkmcnt(3)\nv_or3_b32 v116, v116, v100, v101\n" \
    "v_or3_b32 v116, v116, v102, v103\nv_or3_b32 v116, v116, v104, v105\nv_or3_b32 v116, v116, v106, v107\nv_and_b32 v116, 1, v116\n" \
    "v_mfma_f32_32x32x16_bf16 v[48:63], a[168:171], v[72:75], v[48:63]\nds_read_b128 v[88:91], v1 offset:47104\ns_waitcnt lgkmcnt(3)\nv_cmp_eq_u32 s[70:71], 1, v116\n" \
    "v_add_f32 v96, v96, v16\nv_add_f32 v97, v97, v17\nv_add_f32 v98, v98, v18\nv_add_f32 v99, v99, v19\nv_mfma_f32_32x32x16_bf16 v[48:63], a[172:175], v[76:79], v[48:63]\n" \
    "ds_read_b128 v[92:95], v1 offset:48128\ns_waitcnt lgkmcnt(3)\ns_barrier\nv_add_f32 v100, v100, v104\nv_add_f32 v101, v101, v105\nv_add_f32 v102, v102, v106\n" \
    "v_add_f32 v103, v103, v107\nv_add_f32 v96, v96, v100\nv_mfma_f32_32x32x16_bf16 v[48:63], a[176:179], v[80:83], v[48:63]\nds_read_b128 v[64:67], v1 offset:49152\n" \
    "s_waitcnt lgkmcnt(3)\nv_add_f32 v97, v97, v101\nv_add_f32 v98, v98, v102\nv_add_f32 v99, v99, v103\nv_cvt_pk_bf16_f32 v96, 0, v96\nv_cvt_pk_bf16_f32 v97, 0, v97\n" \
    "v_mfma_f32_32x32x16_bf16 v[48:63], a[180:183], v[84:87], v[48:63]\nds_read_b128 v[68:71], v1 offset:50176\ns_waitcnt lgkmcnt(3)\nv_cvt_pk_bf16_f32 v98, 0, v98\n" \
    "v_cvt_pk_bf16_f32 v99, 0, v99\nv_add_f32 v96, v96, v8\nv_add_f32 v97, v97, v9\nv_add_f32 v98, v98, v10\nv_mfma_f32_32x32x16_bf16 v[48:63], a[184:187], v[88:91], v[48:63]\n" \
    "ds_read_b128 v[72:75], v1 offset:51200\ns_waitcnt lgkmcnt(3)\nv_add_f32 v99, v99, v11\nv_cvt_pk_bf16_f32 v96, 0, v96\nv_cvt_pk_bf16_f32 v97, 0, v97\n" \
    "v_cvt_pk_bf16_f32 v98, 0, v98\nv_cvt_pk_bf16_f32 v99, 0, v99\nv_mfma_f32_32x32x16_bf16 v[48:63], a[188:191], v[92:95], v[48:63]\nds_read_b128 v[76:79], v1 offset:52224\n" \
    "s_waitcnt lgkmcnt(3)\nv_mul_f32 v96, v96, v12\nv_mul_f32 v97, v97, v13\nv_mul_f32 v98, v98, v14\nv_mul_f32 v99, v99, v15\nv_cvt_pk_bf16_f32 v96, 0, v96\n" \
    "v_mfma_f32_32x32x16_bf16 v[48:63], a[192:195], v[64:67], v[48:63]\nds_read_b128 v[80:83], v1 offset:53248\ns_waitcnt lgkmcnt(3)\nv_cvt_pk_bf16_f32 v97, 0, v97\n" \
    "v_cvt_pk_bf16_f32 v98, 0, v98\nv_cvt_pk_bf16_f32 v99, 0, v99\nv_lshlrev_b32 v100, 16, v108\nv_and_b32 v101, 0xffff0000, v108\n" \
    "v_mfma_f32_32x32x16_bf16 v[48:63], a[196:199], v[68:71], v[48:63]\nds_read_b128 v[84:87], v1 offset:54272\ns_waitcnt lgkmcnt(3)\nv_lshlrev_b32 v102, 16, v109\n" \
    "v_and_b32 v103, 0xffff0000, v109\nv_add_f32 v96, v96, v100\nv_add_f32 v97, v97, v101\nv_add_f32 v98, v98, v102\n" \
    "v_mfma_f32_32x32x16_bf16 v[48:63], a[200:203], v[72:75], v[48:63]\nds_read_b128 v[88:91], v1 offset:55296\ns_waitcnt vmcnt(0)\ns_waitcnt lgkmcnt(3)\n" \
    "v_and_b32 v112, 127, v150\nv_cmp_gt_u32 vcc, s77, v112\nv_add_f32 v99, v99, v103\nv_cvt_pk_bf16_f32 v116, v96, v97\nv_cvt_pk_bf16_f32 v117, v98, v99\n" \
    "v_mfma_f32_32x32x16_bf16 v[48:63], a[204:207], v[76:79], v[48:63]\nds_read_b128 v[92:95], v1 offset:56320\ns_cmp_eq_u64 vcc, 0\ns_cselect_b32 s81, 1, 0\n" \
    "s_waitcnt lgkmcnt(3)\nv_mfma_f32_32x32x16_bf16 v[48:63], a[208:211], v[80:83], v[48:63]\ns_waitcnt lgkmcnt(2)\n" \
    "v_mfma_f32_32x32x16_bf16 v[48:63], a[212:215], v[84:87], v[48:63]\ns_waitcnt lgkmcnt(0)\ns_memrealtime s[72:73]\n" \
    "v_mfma_f32_32x32x16_bf16 v[48:63], a[216:219], v[88:91], v[48:63]\nv_mfma_f32_32x32x16_bf16 v[48:63], a[220:223], v[92:95], v[48:63]\ns_waitcnt lgkmcnt(0)\n" \
    "s_cmp_ge_u32 s77, 64\ns_cselect_b32 s69, s60, s34\ns_sub_u32 s72, s72, s69\ns_lshl_b32 s72, s72, 8\nv_sub_u32 v113, s72, v150\nv_cmp_gt_i32 s[74:75], 0, v113\n" \
    "v_cmp_eq_u32 vcc, s77, v112\ns_nop 0\ns_and_b64 vcc, vcc, s[74:75]\ns_cselect_b32 s69, 1, 0\ns_lshl_b32 s81, s81, s69\ns_nop 7\ns_cmp_eq_u32 s90, 0x7fff0000\n" \
    "s_cbranch_scc1 .Lg15_%=\ns_cmp_lg_u64 s[70:71], 0\ns_cbranch_scc1 .Lg16_%=\n.Lg17_%=:\nglobal_store_dwordx2 v5, v[116:117], s[46:47]\nv_add_u32 v5, 0x10000, v5\n" \
    "s_mov_b32 s90, 0x7fff0000\ns_mov_b32 s91, 0x7fff0000\ns_mov_b32 s92, 0x7fff0000\ns_mov_b32 s93, 0x7fff0000\n.Lg15_%=:\ns_nop 1\nv_cmp_u_f32 vcc, v48, v48\ns_nop 0\n" \
    "s_cbranch_vccnz .Lg7_%=\n.Lg8_%=:\nv_and_or_b32 v52, v52, -2, s78\nv_and_or_b32 v53, v53, -2, s78\nv_and_or_b32 v54, v54, -2, s78\nv_and_or_b32 v55, v55, -2, s78\n" \
    "buffer_store_dwordx4 v[52:55], v0, s[64:67], s87 offen sc1\nv_and_or_b32 v56, v56, -2, s78\nv_and_or_b32 v57, v57, -2, s78\nv_and_or_b32 v58, v58, -2, s78\n" \
    "v_and_or_b32 v59, v59, -2, s78\nbuffer_store_dwordx4 v[56:59], v0, s[64:67], s88 offen sc1\nv_and_or_b32 v60, v60, -2, s78\nv_and_or_b32 v61, v61, -2, s78\n" \
    "v_and_or_b32 v62, v62, -2, s78\nv_and_or_b32 v63, v63, -2, s78\nbuffer_store_dwordx4 v[60:63], v0, s[64:67], s89 offen sc1\ns_cmp_lg_u32 s81, 1\ns_cbranch_scc1 .Lg20_%=\n" \
    "s_xor_b32 s84, s84, 0x10000\nbuffer_load_dword v151, v2, s[64:67], s83 offen sc1\ns_add_u32 m0, s84, 0\ns_add_u32 s69, s83, s48\n" \
    "buffer_load_dwordx4 v0, s[64:67], s69 offen offset:0 sc1 lds\nbuffer_load_dwordx4 v0, s[64:67], s69 offen offset:1024 sc1 lds\ns_add_u32 m0, s84, 8192\n" \
    "s_add_u32 s69, s83, s49\nbuffer_load_dwordx4 v0, s[64:67], s69 offen offset:0 sc1 lds\nbuffer_load_dwordx4 v0, s[64:67], s69 offen offset:1024 sc1 lds\n" \
    "s_add_u32 m0, s84, 16384\ns_add_u32 s69, s83, s50\nbuffer_load_dwordx4 v0, s[64:67], s69 offen offset:0 sc1 lds\n" \
    "buffer_load_dwordx4 v0, s[64:67], s69 offen offset:1024 sc1 lds\n.Lg20_%=:\nv_mov_b64 v[16:17], v[48:49]\nv_mov_b64 v[18:19], v[50:51]\ns_add_u32 s90, s86, 0x1000\n" \
    "s_add_u32 s91, s86, 0x4000\ns_add_u32 s92, s86, 0x5000\ns_mov_b32 s93, s82\ns_mov_b32 s79, s78\ns_cmp_lt_u32 s32, 63\ns_cbranch_scc1 .Lg14_%=\ns_memrealtime s[72:73]\n" \
    "s_waitcnt lgkmcnt(0)\ns_add_u32 s76, s72, s35\n.Lg21_%=:\ns_memrealtime s[72:73]\ns_waitcnt lgkmcnt(0)\ns_sub_u32 s72, s72, s76\ns_cmp_lt_i32 s72, 0\n" \
    "s_cbranch_scc1 .Lg21_%=\n.Lg18_%=:\nbuffer_load_dwordx2 v[108:109], v4, s[64:67], s93 offen sc1\nbuffer_load_dwordx4 v[96:99], v0, s[64:67], s90 offen sc1\n" \
    "buffer_load_dwordx4 v[100:103], v0, s[64:67], s91 offen sc1\nbuffer_load_dwordx4 v[104:107], v0, s[64:67], s92 offen sc1\ns_waitcnt vmcnt(0)\nv_max_u32 v116, v108, v109\n" \
    "v_cmp_eq_u32 s[70:71], -1, v116\ns_nop 1\nv_cndmask_b32 v116, 0, 1, s[70:71]\nv_xor_b32 v96, s79, v96\nv_xor_b32 v97, s79, v97\nv_xor_b32 v98, s79, v98\n" \
    "v_xor_b32 v99, s79, v99\nv_xor_b32 v100, s79, v100\nv_xor_b32 v101, s79, v101\nv_xor_b32 v102, s79, v102\nv_xor_b32 v103, s79, v103\nv_xor_b32 v104, s79, v104\n" \
    "v_xor_b32 v105, s79, v105\nv_xor_b32 v106, s79, v106\nv_xor_b32 v107, s79, v107\nv_or3_b32 v116, v116, v96, v97\nv_or3_b32 v116, v116, v98, v99\n" \
    "v_or3_b32 v116, v116, v100, v101\nv_or3_b32 v116, v116, v102, v103\nv_or3_b32 v116, v116, v104, v105\nv_or3_b32 v116, v116, v106, v107\nv_and_b32 v116, 1, v116\n" \
    "v_cmp_eq_u32 s[70:71], 1, v116\nv_pk_add_f32 v[96:97], v[96:97], v[16:17]\nv_pk_add_f32 v[98:99], v[98:99], v[18:19]\nv_pk_add_f32 v[100:101], v[100:101], v[104:105]\n" \
    "v_pk_add_f32 v[102:103], v[102:103], v[106:107]\nv_pk_add_f32 v[96:97], v[96:97], v[100:101]\nv_pk_add_f32 v[98:99], v[98:99], v[102:103]\nv_cvt_pk_bf16_f32 v96, 0, v96\n" \
    "v_cvt_pk_bf16_f32 v97, 0, v97\nv_cvt_pk_bf16_f32 v98, 0, v98\nv_cvt_pk_bf16_f32 v99, 0, v99\nv_pk_add_f32 v[96:97], v[96:97], v[8:9]\n" \
    "v_pk_add_f32 v[98:99], v[98:99], v[10:11]\nv_cvt_pk_bf16_f32 v96, 0, v96\nv_cvt_pk_bf16_f32 v97, 0, v97\nv_cvt_pk_bf16_f32 v98, 0, v98\nv_cvt_pk_bf16_f32 v99, 0, v99\n" \
    "v_pk_mul_f32 v[96:97], v[96:97], v[12:13]\nv_pk_mul_f32 v[98:99], v[98:99], v[14:15]\nv_cvt_pk_bf16_f32 v96, 0, v96\nv_cvt_pk_bf16_f32 v97, 0, v97\n" \
    "v_cvt_pk_bf16_f32 v98, 0, v98\nv_cvt_pk_bf16_f32 v99, 0, v99\nv_lshlrev_b32 v100, 16, v108\nv_and_b32 v101, 0xffff0000, v108\nv_lshlrev_b32 v102, 16, v109\n" \
    "v_and_b32 v103, 0xffff0000, v109\nv_pk_add_f32 v[96:97], v[96:97], v[100:101]\nv_pk_add_f32 v[98:99], v[98:99], v[102:103]\nv_cvt_pk_bf16_f32 v116, v96, v97\n" \
    "v_cvt_pk_bf16_f32 v117, v98, v99\ns_cmp_lg_u64 s[70:71], 0\ns_cbranch_scc1 .Lg19_%=\nglobal_store_dwordx2 v5, v[116:117], s[46:47]\nv_add_u32 v5, 0x10000, v5\n" \
    "s_mov_b32 s90, 0x7fff0000\ns_mov_b32 s91, 0x7fff0000\ns_mov_b32 s92, 0x7fff0000\ns_mov_b32 s93, 0x7fff0000\n.Lg14_%=:\ns_mov_b32 s38, s85\ns_xor_b32 s80, s80, 0x10000\n" \
    "v_xor_b32 v1, 0x10000, v1\ns_mov_b32 s68, 0\ns_add_u32 s32, s32, 1\ns_cmp_lt_u32 s32, 64\ns_cbranch_scc1 .Lg2_%=\ns_endpgm\n.Lg4_%=:\ns_sleep 8\ns_add_u32 s68, s68, 1\n" \
    "s_and_b32 s69, s68, 63\ns_cbranch_scc1 .Lg3_%=\ns_cmp_gt_u32 s68, 0x400000\ns_cbranch_scc1 .Lg1_%=\nbuffer_load_dword v253, off, s[64:67], 0 sc1\ns_waitcnt vmcnt(0)\n" \
    "v_readfirstlane_b32 s69, v253\ns_nop 0\ns_cmp_eq_u32 s69, 1\ns_cbranch_scc0 .Lg3_%=\ns_endpgm\n.Lg13_%=:\ns_waitcnt vmcnt(0)\ns_add_u32 s68, s68, 1\n" \
    "s_and_b32 s69, s68, 63\ns_cbranch_scc1 .Lg12_%=\ns_cmp_gt_u32 s68, 0x400000\ns_cbranch_scc1 .Lg1_%=\nbuffer_load_dword v253, off, s[64:67], 0 sc1\ns_waitcnt vmcnt(0)\n" \
    "v_readfirstlane_b32 s69, v253\ns_nop 0\ns_cmp_eq_u32 s69, 1\ns_cbranch_scc0 .Lg12_%=\ns_endpgm\n.Lg16_%=:\ns_sleep 4\n" \
    "buffer_load_dwordx2 v[108:109], v4, s[64:67], s93 offen sc1\nbuffer_load_dwordx4 v[96:99], v0, s[64:67], s90 offen sc1\n" \
    "buffer_load_dwordx4 v[100:103], v0, s[64:67], s91 offen sc1\nbuffer_load_dwordx4 v[104:107], v0, s[64:67], s92 offen sc1\ns_waitcnt vmcnt(0)\nv_max_u32 v116, v108, v109\n" \
    "v_cmp_eq_u32 s[70:71], -1, v116\ns_nop 1\nv_cndmask_b32 v116, 0, 1, s[70:71]\nv_xor_b32 v96, s79, v96\nv_xor_b32 v97, s79, v97\nv_xor_b32 v98, s79, v98\n" \
    "v_xor_b32 v99, s79, v99\nv_xor_b32 v100, s79, v100\nv_xor_b32 v101, s79, v101\nv_xor_b32 v102, s79, v102\nv_xor_b32 v103, s79, v103\nv_xor_b32 v104, s79, v104\n" \
    "v_xor_b32 v105, s79, v105\nv_xor_b32 v106, s79, v106\nv_xor_b32 v107, s79, v107\nv_or3_b32 v116, v116, v96, v97\nv_or3_b32 v116, v116, v98, v99\n" \
    "v_or3_b32 v116, v116, v100, v101\nv_or3_b32 v116, v116, v102, v103\nv_or3_b32 v116, v116, v104, v105\nv_or3_b32 v116, v116, v106, v107\nv_and_b32 v116, 1, v116\n" \
    "v_cmp_eq_u32 s[70:71], 1, v116\nv_pk_add_f32 v[96:97], v[96:97], v[16:17]\nv_pk_add_f32 v[98:99], v[98:99], v[18:19]\nv_pk_add_f32 v[100:101], v[100:101], v[104:105]\n" \
    "v_pk_add_f32 v[102:103], v[102:103], v[106:107]\nv_pk_add_f32 v[96:97], v[96:97], v[100:101]\nv_pk_add_f32 v[98:99], v[98:99], v[102:103]\nv_cvt_pk_bf16_f32 v96, 0, v96\n" \
    "v_cvt_pk_bf16_f32 v97, 0, v97\nv_cvt_pk_bf16_f32 v98, 0, v98\nv_cvt_pk_bf16_f32 v99, 0, v99\nv_pk_add_f32 v[96:97], v[96:97], v[8:9]\n" \
    "v_pk_add_f32 v[98:99], v[98:99], v[10:11]\nv_cvt_pk_bf16_f32 v96, 0, v96\nv_cvt_pk_bf16_f32 v97, 0, v97\nv_cvt_pk_bf16_f32 v98, 0, v98\nv_cvt_pk_bf16_f32 v99, 0, v99\n" \
    "v_pk_mul_f32 v[96:97], v[96:97], v[12:13]\nv_pk_mul_f32 v[98:99], v[98:99], v[14:15]\nv_cvt_pk_bf16_f32 v96, 0, v96\nv_cvt_pk_bf16_f32 v97, 0, v97\n" \
    "v_cvt_pk_bf16_f32 v98, 0, v98\nv_cvt_pk_bf16_f32 v99, 0, v99\nv_lshlrev_b32 v100, 16, v108\nv_and_b32 v101, 0xffff0000, v108\nv_lshlrev_b32 v102, 16, v109\n" \
    "v_and_b32 v103, 0xffff0000, v109\nv_pk_add_f32 v[96:97], v[96:97], v[100:101]\nv_pk_add_f32 v[98:99], v[98:99], v[102:103]\nv_cvt_pk_bf16_f32 v116, v96, v97\n" \
    "v_cvt_pk_bf16_f32 v117, v98, v99\ns_cmp_lg_u64 s[70:71], 0\ns_cbranch_scc0 .Lg17_%=\ns_add_u32 s68, s68, 1\ns_and_b32 s69, s68, 63\ns_cbranch_scc1 .Lg16_%=\n" \
    "s_cmp_gt_u32 s68, 0x400000\ns_cbranch_scc1 .Lg1_%=\nbuffer_load_dword v253, off, s[64:67], 0 sc1\ns_waitcnt vmcnt(0)\nv_readfirstlane_b32 s69, v253\ns_nop 0\n" \
    "s_cmp_eq_u32 s69, 1\ns_cbranch_scc0 .Lg16_%=\ns_endpgm\n.Lg19_%=:\ns_sleep 4\ns_add_u32 s68, s68, 1\ns_and_b32 s69, s68, 63\ns_cbranch_scc1 .Lg18_%=\n" \
    "s_cmp_gt_u32 s68, 0x400000\ns_cbranch_scc1 .Lg1_%=\nbuffer_load_dword v253, off, s[64:67], 0 sc1\ns_waitcnt vmcnt(0)\nv_readfirstlane_b32 s69, v253\ns_nop 0\n" \
    "s_cmp_eq_u32 s69, 1\ns_cbranch_scc0 .Lg18_%=\ns_endpgm\n.Lg7_%=:\nv_mov_b32 v120, 0\nv_mov_b32 v121, v1\ns_mov_b32 s76, 14\n.Lg10_%=:\n" \
    "ds_read_b128 v[124:127], v121 offset:0\nds_read_b128 v[128:131], v121 offset:1024\nds_read_b128 v[132:135], v121 offset:2048\nds_read_b128 v[136:139], v121 offset:3072\n" \
    "v_add_u32 v121, 0x1000, v121\ns_waitcnt lgkmcnt(0)\nv_max3_u32 v120, v120, v124, v125\nv_max3_u32 v120, v120, v126, v127\nv_max3_u32 v120, v120, v128, v129\n" \
    "v_max3_u32 v120, v120, v130, v131\nv_max3_u32 v120, v120, v132, v133\nv_max3_u32 v120, v120, v134, v135\nv_max3_u32 v120, v120, v136, v137\n" \
    "v_max3_u32 v120, v120, v138, v139\ns_sub_u32 s76, s76, 1\ns_cmp_lg_u32 s76, 0\ns_cbranch_scc1 .Lg10_%=\nv_cmp_eq_u32 vcc, -1, v120\ns_nop 0\ns_cbranch_vccz .Lg8_%=\n" \
    ".Lg9_%=:\ns_mov_b32 s81, 0\ns_barrier\ns_sleep 4\ns_add_u32 s68, s68, 1\ns_and_b32 s69, s68, 63\ns_cbranch_scc1 .Lg2_%=\ns_cmp_gt_u32 s68, 0x400000\n" \
    "s_cbranch_scc1 .Lg1_%=\nbuffer_load_dword v253, off, s[64:67], 0 sc1\ns_waitcnt vmcnt(0)\nv_readfirstlane_b32 s69, v253\ns_nop 0\ns_cmp_eq_u32 s69, 1\n" \
    "s_cbranch_scc0 .Lg2_%=\ns_endpgm\n.Lg1_%=:\nv_mov_b32 v253, 1\nbuffer_store_dword v253, off, s[64:67], 0 sc1\ns_endpgm\n"
typedef unsigned v32u __attribute__((ext_vector_type(32)));
typedef unsigned v16u __attribute__((ext_vector_type(16)));
#define ORD 1
template <bool isV>
__device__ __forceinline__ void s1_main(const Args& a, unsigned char* lds, const int xg, const int ii) {
    const int wave = a.pad0, head = xg * 4 + (ii >> 2), set = (ii >> 1) & 1;
    char* const ws = a.ws;
    unsigned* const ctl = (unsigned*)(ws + O_CTL);
    const unsigned pol = __builtin_amdgcn_readfirstlane(*(const unsigned*)(ws + O_POL));
    const unsigned pubb = isV ? O_UM + (wave & 1) * 262144 + head * 256 : O_EP + ((wave & 1) * 4 + (head >> 3)) * 32768;
    const unsigned psb = isV ? O_UX + (wave & 1) * MiB + (head >> 3) * 16384 + (head & 7) * 1024 : O_PS + (wave * 4 + (head >> 3)) * 16384;
    v16bf w[52], pend[12];
    v4u rX = mkrs(isV ? a.Wv : wave < 2 ? a.Wq : a.Wk, DM * HD * 2), rO = mkrs(a.Wo, DM * HD * 2);
    unsigned bo[4];
#pragma unroll
    for (int i = 0; i < 4; ++i) {
        const int lane = lane_id(), row = lane >> 3, ch = (lane & 7) ^ ((2 * (i & 1) + (row >> 2)) << 1);
        bo[i] = i < 2 ? (((isV ? 64 * (wave >> 1) : 0) + row) * HD + head * DH + 64 * (wave & 1) + 8 * ch) * 2 : ((head * DH + row) * DM + 256 * wave + 8 * ch) * 2;
    }
    asm volatile("s_nop 4" : "+s"(rX), "+s"(rO));
    const int kx = ORD * set;
    {
        const int lane = lane_id(), l15 = lane & 15, gb = lane >> 4;
        auto bufp = [&](int u, int wv) { const int b = (u + 4) % 5; return lds + (b < 3 ? L_W + wv * 24576 + b * 8192 : L_H + wv * 16384 + (b - 3) * 8192); };
        auto issue = [&](auto uu) {
            constexpr int u = decltype(uu)::v, c = u;
            constexpr bool wo = isV && c >= 8;
            const unsigned d = (unsigned)(size_t)(LAS unsigned char*)bufp(u, wave);
#pragma unroll
            for (int r8 = 0; r8 < 8; ++r8)
                bdma<(NTW & (u < 4 ? 1 : u < 8 ? 2 : 64))>(wo ? rO : rX, wo ? bo[2 + (r8 & 1)] : bo[r8 & 1], wo ? ((64 * (c & 1) + 8 * r8) * DM + 64 * ((c - 8) >> 1)) * 2 : (((isV ? 128 : 64) * c + 8 * r8) ^ (512 * kx)) * HD * 2, d + r8 * 1024);
        };
        const unsigned q = l15 >> 2, p = lane & 3;
        const unsigned row0 = 16 * (gb >> 1) + 4 * (gb & 1) + (q & 1) + 8 * (q >> 1);
        const unsigned sw = ((gb & 1) | ((q >> 1) << 1)) << 1;
        auto flush = [&](int j) {
            const v8u v = __builtin_bit_cast(v8u, pend[j]);
            unsigned char* d = lds + L_W + wave * 24576 + j * 2048 + lane * 16;
            *(LAS v4u*)d = (v4u){v[0], v[1], v[2], v[3]};
            *(LAS v4u*)(d + 1024) = (v4u){v[4], v[5], v[6], v[7]};
        };
        sfor<0, 5>(issue);
        sfor<0, 16>([&](auto cc) {
            constexpr int u = decltype(cc)::v, c = u;
            asm volatile("s_waitcnt vmcnt(%0)\n\ts_barrier" :: "n"(u == 0 ? 32 : u < 13 ? 24 : 8 * (15 - u)) : "memory");
            if constexpr (u > 0 && u + 4 < 16) issue(IC<u + 4>{});
            if (u >= 12 && u < 15) { flush(4 * u - 48); flush(4 * u - 47); flush(4 * u - 46); flush(4 * u - 45); }
            v16bf t[4];
#pragma unroll
            for (int n = 0; n < 4; ++n) {
                constexpr bool pk = isV && c >= 8;
                const unsigned char* buf = bufp(u, pk ? wave : (n & 2) + (wave >> 1));
                const unsigned o = pk ? (32 * (n & 1) + 16 * (gb & 1) + 4 * (gb >> 1) + (q & 1)) * 128 + (((4 * (n >> 1) + 2 * (q >> 1) + (p >> 1)) ^ (gb & 2)) << 4) + (p & 1) * 8
                                      : row0 * 128 + (((4 * (wave & 1) + 2 * (n & 1) + (p >> 1)) ^ sw) << 4) + (p & 1) * 8;
                const unsigned o2 = pk ? (o ^ 64) + 8 * 128 : o + 32 * 128;
                const v2u r0 = tr_rd(buf, o), r1 = tr_rd(buf, o + 2 * 128), r2 = tr_rd(buf, o2), r3 = tr_rd(buf, o2 + 2 * 128);
                const v8u v = {r0.x, r0.y, r1.x, r1.y, r2.x, r2.y, r3.x, r3.y};
                t[n] = __builtin_bit_cast(v16bf, v);
            }
            asm volatile("s_waitcnt lgkmcnt(0)" ::: "memory");
            sfor<0, 4>([&](auto nn) {
                constexpr int n = decltype(nn)::v, j = lsl(isV, c, n), r = ridx(isV, c, n);
                if constexpr (j >= 0) { pend[j] = t[n]; asm volatile("" : "+v"(pend[j])); }
                else {
                    w[r] = t[n];
                    if constexpr (r < 32) asm volatile("" : "+a"(w[r])); else asm volatile("" : "+v"(w[r]));
                }
            });
        });
    }
    RSTAMP(74, 0);
    if (!wave_wait<8>(ctl + C_A, 1, 1, NARM, ctl + C_ABORT)) asm volatile("s_endpgm");
    lds_barrier();
    RSTAMP(74, 1);
    const unsigned lb0 = (unsigned)(size_t)(LAS unsigned char*)lds;
    const unsigned lane = lane_id(), l15 = lane & 15, g = lane >> 4, q = l15 >> 2, p = lane & 3, l16 = lane * 16, to = g * 128 + (l15 & 7) * 16, xb = ((g & 1) << 1) | (g >> 1);
    const unsigned r = 8 * wave + (lane >> 3), ao = r * 2048 + 16 * ((lane & 7) ^ ((r >> 1) & 7)) + 1024, ra = lb0 + L_H + (l15 >> 3) * 2048 + (l15 & 7) * 128;
    const v16u c0 = {(lane & 8) ? 0xEEEEu : 0x4444u, lb0 + L_H + l16, l16, lb0 + L_W + wave * 24576 + l16, lb0 + L_T + wave * 512 + q * 128 + (g & 1) * 64 + (g >> 1) * 32 + p * 2,
                     lb0 + (isV ? L_T + to : L_QX + (wave >> 1) * 4096 + (wave & 1) * 128 + g * 128 + to), isV ? lb0 + L_QX + g * 128 + to : lb0 + L_T + to,
                     isV ? xb * 16384 + l15 * 16 : l15 < 8 ? (head & 7) * 512 + g * 128 + l15 * 16 : g ? 0x7FFF0000u : 4096 + (head & 7) * 128 + (l15 & 7) * 16, isV ? xb * 65536 + l15 * 16 : 0xFFFFFFFEu,
                     ra + 16 * (g ^ (l15 >> 1)), ra + 16 * ((4 + g) ^ (l15 >> 1)), ao, ao - 896,
                     0u, lb0 + (isV ? L_QX : L_KX) + wave * 1024 + l16, 0u};
    const unsigned sd = ((pol ^ (wave >> 1)) & 1) * 8192;
    PARS;
    const v16u s0 = {0x3e0293eeu, 0x3db504f3u, 0x3fb8aa3bu, (unsigned)set, (unsigned)wave, O_E1 + (((xg >> 1) & 1) * 4 + wave) * 16384 + ((pol ^ (xg >> 2)) & 1) * 8192 + 2048 * kx, pubb + sd, psb + (isV ? sd : 0),
                     O_H1 - 1024 + set * 65536, lb0 + L_H + wave * 2048, hw(pol, xg) + H_C + 512 * set, par(P_R), par(P_A) + par(P_B), par(P_X), lb0 + L_ST + (wave == STAMP_WAVE ? 0 : MiB), lb0 + L_ST};
    u64 cm[4];
#pragma unroll
    for (int i = 0; i < 4; ++i) cm[i] = __ballot(16 * (l15 >> 3) + 4 * g + i <= 8 * wave + (l15 & 7));
    const u64 dbg = DBG(a, blockIdx.x);
    const v16u s1 = {isV ? (unsigned)(C_F + 32 * (128 * set + 32 * wave + head)) * 4 : 0x00FFFFFFu, isV ? set : 0x00FF00FFu, isV ? set : (unsigned)cm[0], (unsigned)(cm[0] >> 32), (unsigned)cm[1], (unsigned)(cm[1] >> 32),
                     (unsigned)cm[2], (unsigned)(cm[2] >> 32), (unsigned)cm[3], (unsigned)(cm[3] >> 32), (unsigned)dbg, (unsigned)(dbg >> 32), (unsigned)(L_QX - L_T + wave * 2048), ii < 4 ? hw(pol, xg) + H_D + 4 * wave : 0u, 1024u * kx,
                     head < (isV ? 20 : 28) ? O_D + (isV ? D_X2 : 0) + head * 8192 + wave * 2048 : 0x70000000u};
    const v4u rws = mkrs(ws, WS_END);
    v32u ww[13];
#pragma unroll
    for (int i = 0; i < 13; ++i) {
        const v8u x0 = __builtin_bit_cast(v8u, w[4 * i]), x1 = __builtin_bit_cast(v8u, w[4 * i + 1]), x2 = __builtin_bit_cast(v8u, w[4 * i + 2]), x3 = __builtin_bit_cast(v8u, w[4 * i + 3]);
        ww[i] = __builtin_shufflevector(__builtin_shufflevector(x0, x1, 0, 1, 2, 3, 4, 5, 6, 7, 8, 9, 10, 11, 12, 13, 14, 15), __builtin_shufflevector(x2, x3, 0, 1, 2, 3, 4, 5, 6, 7, 8, 9, 10, 11, 12, 13, 14, 15),
                                        0, 1, 2, 3, 4, 5, 6, 7, 8, 9, 10, 11, 12, 13, 14, 15, 16, 17, 18, 19, 20, 21, 22, 23, 24, 25, 26, 27, 28, 29, 30, 31);
    }
#define S1_CALL(TXT) asm volatile("s_nop 4\n" TXT :: "{v[0:15]}"(c0), "{s[32:47]}"(s0), "{s[48:63]}"(s1), "{s[64:67]}"(rws), \
              "{a[0:31]}"(ww[0]), "{a[32:63]}"(ww[1]), "{a[64:95]}"(ww[2]), "{a[96:127]}"(ww[3]), "{a[128:159]}"(ww[4]), "{a[160:191]}"(ww[5]), "{a[192:223]}"(ww[6]), "{a[224:255]}"(ww[7]), \
              "{v[92:123]}"(ww[8]), "{v[124:155]}"(ww[9]), "{v[156:187]}"(ww[10]), "{v[188:219]}"(ww[11]), "{v[220:251]}"(ww[12]) : "memory", "s95", "v253")
    if constexpr (isV) S1_CALL(ASM_V); else S1_CALL(ASM_Q);
}

__device__ __forceinline__ float gelu_tanh(float v) {
    const float e = v * (-2.302208198f - 0.1029432397f * (v * v));
    return v * __builtin_amdgcn_rcpf(1.f + __builtin_amdgcn_exp2f(e));
}
__device__ __forceinline__ v4u unsent4(v4u v) { return (v4u){unsent(v.x), unsent(v.y), unsent(v.z), unsent(v.w)}; }
__device__ __forceinline__ unsigned rt_now() { return (unsigned)__builtin_amdgcn_s_memrealtime(); }
__device__ __forceinline__ void rt_delay(unsigned ticks  , unsigned t0 = rt_now()) {
    while ((unsigned)__builtin_amdgcn_s_memrealtime() - t0 < ticks) __builtin_amdgcn_s_sleep(2);
}
__device__ __forceinline__ float wave_sum(float x) {
    x += __uint_as_float(__builtin_amdgcn_update_dpp(0, __float_as_uint(x), 0xB1, 0xF, 0xF, false));
    x += __uint_as_float(__builtin_amdgcn_update_dpp(0, __float_as_uint(x), 0x4E, 0xF, 0xF, false));
    x += __uint_as_float(__builtin_amdgcn_update_dpp(0, __float_as_uint(x), 0x124, 0xF, 0xF, false));
    x += __uint_as_float(__builtin_amdgcn_update_dpp(0, __float_as_uint(x), 0x128, 0xF, 0xF, false));
    return red32_add(red16_add(x));
}
__device__ __forceinline__ void ln_row(const v4u (&xr)[2], const f32x4 (&c1)[4], const f32x4 (&sh)[4], v4u (&o)[2]) {
    f32x2 v[8];
    f32x2 s2 = {0.f, 0.f};
#pragma unroll
    for (int j = 0; j < 8; ++j) { v[j] = (f32x2){lo16f(xr[j >> 2][j & 3]), hi16f(xr[j >> 2][j & 3])}; s2 += v[j]; }
    const float mean = wave_sum(s2[0] + s2[1]) * (1.f / DM);
    f32x2 q2 = {0.f, 0.f};
#pragma unroll
    for (int j = 0; j < 8; ++j) { const f32x2 d = v[j] - mean; q2 += d * d; }
    const float vs = wave_sum(q2[0] + q2[1]);
    const float mb = rbf(mean), vb = rbf(vs * (1.f / DM));
    const float rsd = 1.f / rbf(sqrtf(rbf(vb + 1e-5f)));
    auto r2 = [](f32x2 z) { const unsigned u = pack2(z[0], z[1]); return (f32x2){lo16f(u), hi16f(u)}; };
#pragma unroll
    for (int j = 0; j < 8; ++j) {
        const f32x2 cc = {c1[j >> 1][2 * (j & 1)], c1[j >> 1][2 * (j & 1) + 1]}, ss = {sh[j >> 1][2 * (j & 1)], sh[j >> 1][2 * (j & 1) + 1]};
        const f32x2 y = r2(r2(r2(v[j] - mb) * rsd) * cc) + ss;
        o[j >> 2][j & 3] = pack2(y[0], y[1]);
    }
}
template <int N> __device__ __forceinline__ void ld_mod(rsrc_t rs, unsigned* ctl, const int (&col)[N], f32x4* m) {
    for (unsigned spins = 0;;) {
        unsigned mx = 0;
#pragma unroll
        for (int i = 0; i < N; ++i) { const v4u v = bld16(rs, O_MOD + col[i] * 4); mx = max(mx, umax4(v)); m[i] = __builtin_bit_cast(f32x4, v); }
        if (!__any(mx == SENT)) break;
        __builtin_amdgcn_s_sleep(6);
        if (spin_fail(spins, ctl + C_ABORT)) asm volatile("s_endpgm");
    }
}
__device__ __forceinline__ void set_m0(unsigned v) { asm volatile("s_mov_b32 m0, %0" :: "s"(v) : "m0"); }
__device__ __forceinline__ void mod8(const Args& a, int col, f32x4 (&m)[2]) {
    f32x4 p[8][2];
#pragma unroll
    for (int k = 0; k < 16; ++k) p[k >> 1][k & 1] = *(const f32x4*)(a.ws + O_MODP + ((k >> 1) * 2048 + col + 4 * (k & 1)) * 4);
#pragma unroll
    for (int h = 0; h < 2; ++h) {
        const f32x4 t = ((p[0][h] + p[1][h]) + (p[2][h] + p[3][h])) + ((p[4][h] + p[5][h]) + (p[6][h] + p[7][h]));
#pragma unroll
        for (int i = 0; i < 4; ++i) { float v = rbf(t[i]); if (col >= DM) v = rbf(1.f + v); m[h][i] = v; }
    }
}
__device__ __forceinline__ void mod_cols(const Args& a, rsrc_t rs, unsigned char* lds, int cg) {
    const int tid = otid();
    v4u wv[32];
#pragma unroll
    for (int i = 0; i < 32; ++i) wv[i] = __builtin_nontemporal_load((const v4u*)(a.w_mod + (size_t)((tid >> 3) + 32 * i) * 6144 + cg * 64 + 8 * (tid & 7)));
    {
        const v2u cr = *(const v2u*)(a.c + 4 * tid);
        f32x4 sv = {lo16f(cr.x), hi16f(cr.x), lo16f(cr.y), hi16f(cr.y)};
#pragma unroll
        for (int i = 0; i < 4; ++i) sv[i] = rbf(sv[i] / (1.f + __expf(-sv[i])));
        *(LAS f32x4*)(lds + 16384 + tid * 16) = sv;
    }
    lds_barrier();
    f32x2 acc[4] = {{0.f, 0.f}, {0.f, 0.f}, {0.f, 0.f}, {0.f, 0.f}};
#pragma unroll
    for (int i = 0; i < 32; ++i) {
        const float sv = *(const LAS float*)(lds + 16384 + ((tid >> 3) + 32 * i) * 4);
#pragma unroll
        for (int j = 0; j < 4; ++j) acc[j] += sv * (f32x2){lo16f(wv[i][j]), hi16f(wv[i][j])};
    }
    unsigned char* red = lds + 20480;
    *(LAS f32x4*)(red + (tid >> 3) * 272 + (tid & 7) * 32) = (f32x4){acc[0][0], acc[0][1], acc[1][0], acc[1][1]};
    *(LAS f32x4*)(red + (tid >> 3) * 272 + (tid & 7) * 32 + 16) = (f32x4){acc[2][0], acc[2][1], acc[3][0], acc[3][1]};
    lds_barrier();
    if (tid < 64) {
        f32x4 t = {0.f, 0.f, 0.f, 0.f};
#pragma unroll
        for (int i = 0; i < 8; ++i) t += *(const LAS f32x4*)(red + ((tid >> 4) * 8 + i) * 272 + (tid & 15) * 16);
#pragma unroll
        for (int i = 0; i < 4; ++i) { float v = rbf(red32_add(red16_add(t[i]))); if ((cg >> 4) == 4) v = rbf(1.f + v); t[i] = v; }
        if (tid < 16) bst16(rs, O_MOD + (cg * 64 + 4 * tid) * 4, unsent4(__builtin_bit_cast(v4u, t)));
    }
}
#define TD_A 300
#define TD_L 1150
constexpr int NARM0 = 5120;
__device__ __forceinline__ unsigned arm0(unsigned v) {
    if (v < 1280) return (v < 640 ? O_EP : O_PS - MiB) + v / 5 * 8192 + v % 5 * 1024;
    if (v < 2304) return O_E1 + 131072 + (v - 1280) * 1024;
    if (v < 3072) return O_D + (v - 2304) / 384 * D_SLOT + (v - 2304) % 384 * 1024;
    return O_SL + (v - 3072) * 1024;
}
__device__ __forceinline__ void f_start(const Args& a, rsrc_t rs, unsigned char* lds, int idx  , unsigned t0) {
    const int tid = otid(), lane = tid & 63, wave = a.pad0;
    unsigned* const ctl = (unsigned*)(a.ws + O_CTL);
    const int r0 = 4 * idx + wave;
    v4u xr[6][2];
    auto ldx = [&](int k, int row) { for (int c = 0; c < 2; ++c) xr[k][c] = *(const v4u*)(a.x + (size_t)row * DM + 512 * c + 8 * lane); };
    ldx(0, r0);
    {
        f32x4 m[2];
        mod8(a, 8 * tid, m);
        *(LAS f32x4*)(lds + 32 * tid) = m[0]; *(LAS f32x4*)(lds + 32 * tid + 16) = m[1];
    }
    lds_barrier();
    f32x4 c1[4], sh[4];
#pragma unroll
    for (int i = 0; i < 4; ++i) { const int col = 512 * (i >> 1) + 8 * lane + 4 * (i & 1); sh[i] = *(const LAS f32x4*)(lds + col * 4); c1[i] = *(const LAS f32x4*)(lds + (DM + col) * 4); }
    auto row = [&](int k, int r) {
        v4u o[2];
        ln_row(xr[k], c1, sh, o);
#pragma unroll
        for (int c = 0; c < 2; ++c) {
            const bool od = lane & 1;
            const unsigned y0 = __builtin_amdgcn_update_dpp(0, od ? o[c].x : o[c].z, 0xB1, 0xF, 0xF, false), y1 = __builtin_amdgcn_update_dpp(0, od ? o[c].y : o[c].w, 0xB1, 0xF, 0xF, false);
            bst16(rs, O_H1 + (unsigned)(r * DM + 512 * c + 8 * lane) * 2, od ? (v4u){y0, o[c].z, y1, o[c].w} : (v4u){o[c].x, y0, o[c].y, y1});
        }
    };
    row(0, r0);
    if (idx >= 64) {
        rt_delay(TD_A, t0);
        for (unsigned v = r0 - 256; v < NARM0; v += 256) bst16(rs, arm0(v) + lane * 16, sentv());
    }
    drained(ctl + C_A);
    if (idx >= 64) return;
    RSTAMP(75, 2);
    rt_delay(TD_L, t0);
    mod_cols(a, rs, lds, idx + 32);
    if (idx >= 32) return;
    for (int h = 0; h < 2; ++h) {
#pragma unroll
        for (int k = 0; k < 6; ++k) ldx(k, 512 + r0 + 128 * (6 * h + k));
#pragma unroll
        for (int k = 0; k < 6; ++k) row(k, 512 + r0 + 128 * (6 * h + k));
    }
    drained(ctl + C_H);
}
#define TD_W 1300
__device__ __forceinline__ unsigned r_ew(unsigned pol, unsigned lane) { return ((lane >> 4) & 1) * 65536 + (((O_E1 >> 21) ^ pol ^ (lane >> 5)) & 1) * 8192 + (lane & 15) * 16; }
__device__ __forceinline__ unsigned r_e1(int cb) { return O_E1 + ((cb >> 2) & 3) * 16384 + (cb >> 4) * 1024 + (cb & 3) * 256; }
__device__ __forceinline__ unsigned r_db(int cb) { return D_X2 + (cb >> 3) * 16384 + (cb & 7) * 1024; }
__device__ __forceinline__ unsigned r_st(int cb, unsigned lane) { return D_ST + (cb >> 5) * 16384 + (lane >> 5) * 8192 + (cb & 31) * 256 + (lane & 31) * 8; }
__device__ __forceinline__ void r_main(const Args& a, rsrc_t rs, unsigned char* lds, int xg, int i8, unsigned* ctl) {
    const unsigned lane = lane_id(), wave = a.pad0, l15 = lane & 15, g = lane >> 4, q = l15 >> 2, p = lane & 3, cb = 8 * xg + i8;
    const unsigned pol = __builtin_amdgcn_readfirstlane(*(const unsigned*)(a.ws + O_POL)), lb = (unsigned)(size_t)(LAS unsigned char*)lds;
    *(volatile LAS unsigned*)(lds + 4096) = 0u;
    if (!wave_wait<64>(ctl + C_A, 1, 1, NARM, ctl + C_ABORT)) asm volatile("s_endpgm");
    lds_barrier();
    RSTAMP(74, 0);
    const v16u c0 = {(lane & 8) ? 0xEEEEu : 0x4444u, lane * 16, 4096 + 2 * g * 128 + (l15 & 7) * 16 + (l15 >> 3) * 8, lb + wave * 1024 + l15 * 16 + (g & 1) * 8 + (g >> 1) * 4,
                     lb + wave * 1024 + (8 * (p >> 1) + q) * 16 + (p & 1) * 8, lb + wave * 1024 + ((2 * (g & 1) + (p >> 1)) ^ (cb & 3)) * 256 + (8 * (g >> 1) + q) * 16 + (p & 1) * 8,
                     r_ew(pol, lane), (wave * 32 + (lane & 31)) * 2048 + cb * 32 + (lane >> 5) * 16, (C_F + 32 * (128 * (wave & 1) + 32 * (cb >> 4) + (lane & 31))) * 4, r_st(cb, lane) - r_db(cb), lb + 4096, 0xFFFEFFFEu,
                     O_MOD + (2 * DM + 16 * cb + 8 * (lane >> 5)) * 4, (lane >> 3) * 16384 + 4096 + (lane & 7) * 128, 0u, 0u};
    const unsigned sd = ((pol ^ (xg >> 2)) & 1) * 8192;
    PARS;
    const v16u s0 = {wave, wave, par(P_A), par(P_B), par(P_EU), par(P_DMIN), par(P_R), par(P_TP), par(P_TU), par(P_T0),
                     O_UX + (cb & 31) * 65536 + sd, O_UM + (cb & 31) * 16384 + sd, O_EP + ((xg >> 1) & 1) * 131072 + sd, O_PS + sd, r_e1(cb), O_D + r_db(cb)};
    const u64 dbg = DBG(a, blockIdx.x);
    const v8u s1 = {i8 ? 0u : hw(pol, xg) + H_C + 512 * (wave & 1), (cb & 3) * 65536, lb + 8192 + wave * 32, lb + 8192, (unsigned)dbg, (unsigned)(dbg >> 32), (unsigned)(u64)a.x, (unsigned)((u64)a.x >> 32)};
    const v4u rws = mkrs(a.ws, WS_END);
    const unsigned n0 = __builtin_amdgcn_readfirstlane(rt_now() / par(P_R));
    const v8u s2 = {n0 * par(P_R), (n0 & 1) * 128, par(P_TF), par(P_X2), hw(pol, xg) + H_D, par(P_TT), 0u, 0u};
    asm volatile("s_nop 4\n" ASM_R :: "{v[0:15]}"(c0), "{s[32:47]}"(s0), "{s[48:55]}"(s1), "{s[56:63]}"(s2), "{s[64:67]}"(rws) : "memory", "s95", "v253", "a255");
}

typedef __attribute__((ext_vector_type(16))) float f32x16;
template <bool is2> __device__ __forceinline__ void f_main(const Args& a, rsrc_t rs, unsigned char* lds, int xg, int kq, int cg, unsigned* ctl) {
    constexpr int L_C = 131072, L_S = L_C + 8192, NG = is2 ? 7 : 8, LD = is2 ? DM : FF;
    const int tid = otid(), lane = tid & 63, wave = a.pad0, l15 = lane & 15, g = lane >> 4;
    const unsigned lb = (unsigned)(size_t)(LAS unsigned char*)lds, q = l15 >> 2, p = lane & 3, l16 = lane * 16;
    const unsigned pol = __builtin_amdgcn_readfirstlane(*(const unsigned*)(a.ws + O_POL));
    auto gr = [&](int G) { const int x = G + 2 * ((blockIdx.x >> 3) & 3); return x >= NG ? x - NG : x; };
    lds_barrier();
    v4u w[64];
    f32x16 u1 = {}, u2 = {};
    {
        v4u rW = mkrs(is2 ? a.w2 : a.w1, DM * FF * 2);
        unsigned bo[2];
#pragma unroll
        for (int i = 0; i < 2; ++i) bo[i] = (g * LD + 16 * ((l15 >> 1) ^ (g | i << 2)) + 8 * (lane & 1)) * 2;
        asm volatile("s_nop 4" : "+s"(rW));
        auto issue = [&](int G) {
            const unsigned r0 = 896 * kq + 128 * gr(G) + 32 * wave;
#pragma unroll
            for (int j = 0; j < 8; ++j) bdma<(NTW & 8)>(rW, bo[(j >> 1) & 1], ((r0 + 4 * j) * LD + 128 * cg) * 2, lb + (G & 3) * 32768 + wave * 8192 + j * 1024);
        };
        issue(0); issue(1); issue(2); issue(3);
        if (!is2) {
            f32x4 md[2];
            ld_mod<2>(rs, ctl, {4 * DM + 4 * tid, 3 * DM + 4 * tid}, md);
            *(LAS f32x4*)(lds + L_C + 16 * tid) = md[0]; *(LAS v2u*)(lds + L_C + 4096 + 8 * tid) = (v2u){pack2(md[1][0], md[1][1]), pack2(md[1][2], md[1][3])};
            asm volatile("s_waitcnt lgkmcnt(0)" ::: "memory");
        }
        const unsigned ox = is2 ? (4 * (g >> 1) + q) * 256 + 32 * ((2 * (kq ^ (2 * (g & 1) + (p >> 1))) + (wave >> 1)) ^ q) + 16 * (wave & 1) + 8 * (p & 1)
                                : (8 * (g >> 1) + q) * 256 + 32 * ((2 * wave + (g & 1)) ^ (q | (g >> 1) << 2)) + 8 * p;
        sfor<0, NG>([&](auto gg) {
            constexpr int G = decltype(gg)::v;
            asm volatile("s_waitcnt vmcnt(%0)\n\ts_barrier" :: "n"(G < NG - 3 ? 24 : 8 * (NG - 1 - G)) : "memory");
#pragma unroll
            for (int k = 0; k < 8; ++k) {
                const unsigned o = (G & 3) * 32768 + k * 4096;
                const v2u lo = tr_rd(lds, o + ox), hi = tr_rd(lds, o + (is2 ? (ox ^ 128) + 2048 : ox + 1024));
                w[8 * G + k] = (v4u){lo.x, lo.y, hi.x, hi.y};
                if constexpr (!is2) {
                    const unsigned k0 = 128 * gr(G) + 16 * k + 8 * (g >> 1);
                    const f32x4 ca = *(const LAS f32x4*)(lds + L_C + 4 * k0), cb = *(const LAS f32x4*)(lds + L_C + 4 * k0 + 16);
                    u2 = __builtin_amdgcn_mfma_f32_32x32x16_bf16(__builtin_bit_cast(v8bf, w[8 * G + k]), __builtin_bit_cast(v8bf, lds16(lds + L_C + 4096 + 2 * k0)), u2, 0, 0, 0);
                    w[8 * G + k] = (v4u){pack2(lo16f(lo.x) * ca[0], hi16f(lo.x) * ca[1]), pack2(lo16f(lo.y) * ca[2], hi16f(lo.y) * ca[3]), pack2(lo16f(hi.x) * cb[0], hi16f(hi.x) * cb[1]), pack2(lo16f(hi.y) * cb[2], hi16f(hi.y) * cb[3])};
                    u1 = __builtin_amdgcn_mfma_f32_32x32x16_bf16(__builtin_bit_cast(v8bf, w[8 * G + k]), __builtin_bit_cast(v8bf, (v4u){0x3F803F80u, 0x3F803F80u, 0x3F803F80u, 0x3F803F80u}), u1, 0, 0, 0);
                }
            }
            if constexpr (!is2) asm volatile("" : "+v"(u1), "+v"(u2));
#pragma unroll
            for (int k = 8 * G; k < 8 * G + 8; ++k) asm volatile("" : "+a"(w[k]));
            lds_barrier();
            if (G + 4 < NG) issue(G + 4);
        });
    }
    const unsigned sd = ((pol ^ (xg >> 2)) & 1) * 8192;
    v16u c0 = {l16, lb + l16, is2 ? min(lane, 6) * 16384 + wave * 2048 + 2044 : D_ST - D_X2 + (l15 >> 3) * 16384 + ((l15 & 6) * 4 + 2 * wave + (lane & 1)) * 256, lb + L_S + wave * 256 + l15 * 16, lb + L_S + (lane & 31) * 8, 0u, 0xbdd2d3e8u, 0xbdd2d3e8u, 0xc0135761u, 0xc0135761u, 0x3f800000u, 0x3f800000u, 0u, 0u, 0u, 0u};
    if (is2) {
        const int col = 128 * cg + 32 * kq + 8 * wave + 4 * (lane >> 5);
        const v2u br = *(const v2u*)(a.b2 + col);
        f32x4 gv;
        ld_mod<1>(rs, ctl, {5 * DM + col}, &gv);
        const int f1 = 7 * (kq & 1) + min(lane, 6);
        c0[3] = hw(pol, (xg & 4) + (f1 >> 2)) + H_P + 256 * (f1 & 3); c0[4] = r_db(col >> 4) - (kq & 1) * 7 * 16384 + (wave & 1) * 512 + (lane & 31) * 16 + (lane >> 5) * 8; c0[5] = ((lane & 31) * DM + col) * 2;
        c0[8] = br.x << 16; c0[9] = br.x & 0xFFFF0000u; c0[10] = br.y << 16; c0[11] = br.y & 0xFFFF0000u;
#pragma unroll
        for (int i = 0; i < 4; ++i) c0[12 + i] = __float_as_uint(gv[i]);
    } else {
#pragma unroll
        for (int h = 0; h < 4; ++h) {
            const v2u br = *(const v2u*)(a.b1 + 128 * cg + 32 * wave + 8 * h + 4 * (g >> 1));
            u2[4 * h] += lo16f(br.x); u2[4 * h + 1] += hi16f(br.x); u2[4 * h + 2] += lo16f(br.y); u2[4 * h + 3] += hi16f(br.y);
        }
    }
    if (!wave_wait<8>(ctl + C_A, 1, 1, NARM, ctl + C_ABORT)) asm volatile("s_endpgm");
    RSTAMP(74, 0);
    const u64 dbg = DBG(a, blockIdx.x);
    PARS;
    const v16u s0 = {0u, (unsigned)wave, is2 ? par(P_T1) : -par(P_TZ), par(P_TS), O_D + (is2 ? (kq & 1) * 7 * 16384 : D_X2) + sd, lb + wave * 2048, hw(pol, xg) + H_C, hw(pol, xg) + H_P + 256 * ((blockIdx.x >> 3) & 3),
                     O_D + (cg >= 14 ? cg - 14 : cg) * 16384 + wave * 2048 + sd, D_ST - D_X2 + (wave >> 1) * 16384 + (wave & 1) * 4096, lb + L_S + 2048 + (wave == STAMP_WAVE ? 0 : MiB), lb + L_S + 2048,
                     (unsigned)dbg, (unsigned)(dbg >> 32), (unsigned)(u64)a.out, (unsigned)((u64)a.out >> 32)};
    v16u s1;
#pragma unroll
    for (int i = 0; i < 16; ++i) {
        const int o = kq ^ (i & 3);
        s1[i] = i < 8 ? gr(i) * 16384 + wave * 2048 : O_SL + ((cg * 2 + (o & 1)) * 2 + ((i >> 1) & 1)) * 16384 + ((pol ^ (o >> 1)) & 1) * 8192 + (i & 1) * 4096 + wave * 1024;
    }
    s1[12] = is2 ? par(P_T2) : -par(P_TY);
    const v4u rws = mkrs(a.ws, WS_END);
    v32u ww[8];
#pragma unroll
    for (int i = 0; i < NG; ++i) {
        const v16u x0 = __builtin_shufflevector(__builtin_shufflevector(w[8 * i], w[8 * i + 1], 0, 1, 2, 3, 4, 5, 6, 7), __builtin_shufflevector(w[8 * i + 2], w[8 * i + 3], 0, 1, 2, 3, 4, 5, 6, 7), 0, 1, 2, 3, 4, 5, 6, 7, 8, 9, 10, 11, 12, 13, 14, 15);
        const v16u x1 = __builtin_shufflevector(__builtin_shufflevector(w[8 * i + 4], w[8 * i + 5], 0, 1, 2, 3, 4, 5, 6, 7), __builtin_shufflevector(w[8 * i + 6], w[8 * i + 7], 0, 1, 2, 3, 4, 5, 6, 7), 0, 1, 2, 3, 4, 5, 6, 7, 8, 9, 10, 11, 12, 13, 14, 15);
        ww[i] = __builtin_shufflevector(x0, x1, 0, 1, 2, 3, 4, 5, 6, 7, 8, 9, 10, 11, 12, 13, 14, 15, 16, 17, 18, 19, 20, 21, 22, 23, 24, 25, 26, 27, 28, 29, 30, 31);
    }
#define F_IN "{v[0:15]}"(c0), "{s[32:47]}"(s0), "{s[48:63]}"(s1), "{s[64:67]}"(rws), "{a[0:31]}"(ww[0]), "{a[32:63]}"(ww[1]), "{a[64:95]}"(ww[2]), "{a[96:127]}"(ww[3]), "{a[128:159]}"(ww[4]), "{a[160:191]}"(ww[5]), "{a[192:223]}"(ww[6])
    if constexpr (is2) asm volatile("s_nop 4\n" ASM_F2 :: F_IN : "memory", "s95");
    else asm volatile("s_nop 7\ns_nop 7\n" ASM_F1 :: F_IN, "{a[224:255]}"(ww[7]), "{v[16:31]}"(u1), "{v[32:47]}"(u2) : "memory", "s95");
}

__global__ void __launch_bounds__(256, 1) k_main(Args) {
    extern __shared__ __attribute__((aligned(16))) unsigned char lds[];
    const int xg = blockIdx.x & 7, ii = blockIdx.x >> 3;
    int wv = __builtin_amdgcn_readfirstlane(threadIdx.x >> 6); asm volatile("" : "+s"(wv));
    if (blockIdx.x >= 224) code_touch(lds + LDS_BYTES - 1024, ((blockIdx.x >> 3) - 28) * 4 + wv);
    auto args = [&]() { PARS; Args r = *(const Args*)pp_; r.pad0 = wv; r.mode = blockIdx.x; return r; };
    const Args a = args();
    RSTAMP(75, 0);
    if (ii < 16) {
        if (ii & 1) s1_main<true>(args(), lds, xg, ii); else s1_main<false>(args(), lds, xg, ii);
        return;
    }
    const rsrc_t rs = __builtin_amdgcn_make_buffer_rsrc((void*)a.ws, (short)0, (int)WS_END, 0x00020000);
    unsigned* const ctl = (unsigned*)(a.ws + O_CTL);
    const int i4 = ii & 3, cg = 4 * (xg & 3) + i4;
    const unsigned t0 = rt_now();
    f_start(args(), rs, lds, (blockIdx.x - 64) & 127, t0);
    if (ii < 24) {
        rt_delay(TD_W, t0);
        r_main(args(), rs, lds, xg, ii - 16, ctl);
        return;
    }
    RSTAMP(75, 1);
    if (ii >= 28 && cg >= 14) return;
    if (ii < 28) f_main<true>(args(), rs, lds, xg, xg >> 1, 4 * (xg & 1) + i4, ctl);
    else f_main<false>(args(), rs, lds, xg, 0, 14 * (xg >> 2) + cg, ctl);
}

extern "C" void kernel_launch(void* const* d_in, const int* in_sizes, int n_in,
                              void* d_out, int out_size, void* d_ws, size_t ws_size,
                              hipStream_t stream) {
    Args a;
    a.x = (const u16*)d_in[0]; a.Wq = (const u16*)d_in[2]; a.Wk = (const u16*)d_in[3];
    a.Wv = (const u16*)d_in[4]; a.Wo = (const u16*)d_in[5];
    a.w1 = (const u16*)d_in[8]; a.b1 = (const u16*)d_in[9]; a.w2 = (const u16*)d_in[10]; a.b2 = (const u16*)d_in[11];
    a.out = (u16*)d_out; a.ws = (char*)d_ws; a.mode = 0; a.pad0 = 0;
    a.c = (const u16*)d_in[12]; a.w_mod = (const u16*)d_in[13];
    k_pre<<<280, 256, 0, stream>>>(a.c, a.w_mod, (const u16*)d_in[1], a.ws);
    hipFuncSetAttribute((const void*)k_main, hipFuncAttributeMaxDynamicSharedMemorySize, LDS_BYTES);
    k_main<<<256, 256, LDS_BYTES, stream>>>(a);
}
```

```cpp
#include <hip/hip_runtime.h>
#include <hip/hip_bf16.h>
#include <stdint.h>

typedef unsigned short u16;
typedef unsigned long long u64;
typedef __attribute__((ext_vector_type(8))) short bf16x8;
typedef __attribute__((ext_vector_type(4))) short bf16x4;
typedef __attribute__((ext_vector_type(4))) __bf16 bf16x4_t;
typedef __attribute__((ext_vector_type(8))) __bf16 v8bf;
typedef __attribute__((ext_vector_type(16))) __bf16 v16bf;
typedef __attribute__((ext_vector_type(4))) float f32x4;
typedef __attribute__((ext_vector_type(2))) float f32x2;
typedef unsigned v8u __attribute__((ext_vector_type(8)));
typedef unsigned v4u __attribute__((ext_vector_type(4)));
typedef unsigned v2u __attribute__((ext_vector_type(2)));
#define GAS __attribute__((address_space(1)))
#define LAS __attribute__((address_space(3)))
typedef GAS unsigned gu32;
#define RLX_AGENT __ATOMIC_RELAXED, __HIP_MEMORY_SCOPE_AGENT

constexpr int S_TOK = 2048, DM = 1024, NH = 32, DH = 128, MEMR = 8, SEG = 32, NSEG = 64;
constexpr int HD = NH * DH;
constexpr int QKN = 2 * HD;
constexpr int FF = 3584;

#define NTW 79
constexpr int RG = 16;
#define RGX 16
constexpr unsigned MiB = 1u << 20;
constexpr unsigned O_CTL = 0;
constexpr unsigned O_MOD = 65536;
constexpr unsigned O_POL = O_MOD + 32768;
constexpr unsigned O_MODP = 131072;
constexpr unsigned O_EP = 2 * MiB;
constexpr unsigned O_PS = O_EP + RG * 262144;
constexpr unsigned O_UM = O_PS + RG * 262144;
constexpr unsigned O_E1 = O_UM + RG * 524288;
constexpr unsigned O_H1 = O_E1 + RG * 131072;
constexpr unsigned O_UX = O_H1 + 4 * MiB;
constexpr unsigned O_D = O_UX + RGX * 2 * MiB, D_SLOT = 524288, D_X2 = 14 * 16384, D_ST = 22 * 16384;
constexpr unsigned O_SL = O_D + 32 * MiB;
constexpr unsigned WS_END = O_SL + 2 * MiB;
static_assert(WS_END <= 256 * MiB && O_UX % (2 * MiB) == 0 && O_E1 % (2 * MiB) == 0, "ws");
__device__ __forceinline__ unsigned nearg(unsigned pol, unsigned off, unsigned h) { return off + (((off >> 21) ^ pol ^ h) & 1) * 8192; }
constexpr int C_ABORT = 0;
constexpr int C_A = 32, NARM = 128;
constexpr int C_F = 64;
constexpr int C_H = C_F + 8192;
constexpr unsigned O_HW = 49152;
constexpr int H_C = 0;
constexpr int H_D = 256;
constexpr int H_P = 1024;
__device__ __forceinline__ unsigned hw(unsigned pol, int x) { return nearg(pol, O_HW, x >> 2) + (x & 3) * 2048; }

__device__ __forceinline__ float bf2f(u16 v) { return __uint_as_float(((unsigned)v) << 16); }
__device__ __forceinline__ u16 f2bf(float f) {
    __hip_bfloat16 h = __float2bfloat16(f);
    return __builtin_bit_cast(u16, h);
}
__device__ __forceinline__ float rbf(float f) { return bf2f(f2bf(f)); }

#define CODE_KB 100
#define NPR 6
#define CTC 3
__device__ __forceinline__ void code_touch(unsigned char* lds, int part) {
    u64 pc; const unsigned sz = CODE_KB * 1024;
    asm volatile("s_getpc_b64 %0" : "=s"(pc));
    const v4u r = {(unsigned)pc, (unsigned)(pc >> 32) & 0xFFFFu, sz, 0x00020000u};
    asm volatile("s_mov_b32 m0, %2\n\ts_nop 4\n\tbuffer_load_dword %0, %1, 0 offen lds" :: "v"(((threadIdx.x & 63) + 64 * part) * 128), "s"(r), "s"((unsigned)(size_t)(LAS unsigned char*)lds) : "m0");
}
__global__ __launch_bounds__(256) void k_pre(const u16* __restrict__ c, const u16* __restrict__ w_mod, const u16* __restrict__ mem0, char* __restrict__ ws) {
    __shared__ float red[32][65];
    const int tid = threadIdx.x;
    const unsigned bid = blockIdx.x;
    if (bid >= 24) {
        const int cg = (bid - 24) & 31, kq = (bid - 24) >> 5, c8 = tid & 7, kr = 128 * kq + (tid >> 3);
        v4u wv[4];
#pragma unroll
        for (int i = 0; i < 4; ++i) wv[i] = NTW & 4 ? __builtin_nontemporal_load((const v4u*)(w_mod + (size_t)(kr + 32 * i) * 6144 + cg * 64 + 8 * c8)) : *(const v4u*)(w_mod + (size_t)(kr + 32 * i) * 6144 + cg * 64 + 8 * c8);
        float acc[8] = {0.f, 0.f, 0.f, 0.f, 0.f, 0.f, 0.f, 0.f};
#pragma unroll
        for (int i = 0; i < 4; ++i) {
            const float v = bf2f(c[kr + 32 * i]), sv = rbf(v / (1.f + __expf(-v)));
#pragma unroll
            for (int j = 0; j < 4; ++j) { acc[2 * j] += sv * __uint_as_float(wv[i][j] << 16); acc[2 * j + 1] += sv * __uint_as_float(wv[i][j] & 0xFFFF0000u); }
        }
#pragma unroll
        for (int j = 0; j < 8; ++j) red[tid >> 3][8 * c8 + j] = acc[j];
        __syncthreads();
        if (tid < 64) {
            float v = 0.f;
#pragma unroll
            for (int i = 0; i < 32; ++i) v += red[i][tid];
            ((float*)(ws + O_MODP))[kq * 2048 + cg * 64 + tid] = v;
        }
        return;
    }
    if (bid >= 16) {
        if (bid < 22) *(uint4*)(ws + O_MOD + (bid - 16) * 4096 + tid * 16) = make_uint4(~0u, ~0u, ~0u, ~0u);
        if (bid == 23) { for (int i = 0; i < 4; ++i) code_touch((unsigned char*)red, i * 4 + (tid >> 6)); asm volatile("s_waitcnt vmcnt(0)"); }
        if (bid != 22 || tid) return;
        unsigned best[2] = {~0u, ~0u};
        for (int r = 0; r < NPR; ++r) {
            const u64 t0 = __builtin_readcyclecounter();
            asm volatile("global_store_dword %0, %1, %2 sc1\n\ts_waitcnt vmcnt(0)" :: "v"(O_POL + 128 + (r & 1) * 8192 + (r & 2) * 2048), "v"(0u), "s"(ws) : "memory");
            best[r & 1] = min(best[r & 1], (unsigned)(__builtin_readcyclecounter() - t0));
        }
        const unsigned xcc = __builtin_amdgcn_s_getreg((3 << 11) | 20);
        *(unsigned*)(ws + O_POL) = ((best[1] < best[0]) ^ (xcc >> 2)) & 1;
        return;
    }
    ((uint4*)(ws + O_CTL))[bid * 256 + tid] = make_uint4(0u, 0u, 0u, 0u);
    const unsigned u = (tid >> 6) * 256 + ((bid >> 1) & 3) * 64 + (tid & 63);
    uint4 m = *(const uint4*)(mem0 + (u & 7) * DM + (u >> 4) * 16 + ((u >> 3) & 1) * 8);
    m.x = min(m.x, 0xFFFFFFFEu); m.y = min(m.y, 0xFFFFFFFEu); m.z = min(m.z, 0xFFFFFFFEu); m.w = min(m.w, 0xFFFFFFFEu);
    *(uint4*)(ws + O_E1 + bid * 8192 + tid * 16) = m; *(uint4*)(ws + O_E1 + bid * 8192 + 4096 + tid * 16) = m;
}

enum { P_A, P_B, P_EU, P_DMIN, P_R, P_TP, P_TU, P_T0, P_X, P_TZ, P_TF, P_X2, P_T1, P_TS, P_TT, P_TY, P_T2, P_17, NPAR };
#define CH_PAR 152, 72, 8, 4, 60, 108, 300, 90, 36, -30u, 10, 72, 190, 30, 24, 136, 140, 0
struct Args {
    const u16* Wq; const u16* Wk; const u16* Wv; const u16* Wo;
    const u16* c; const u16* w_mod;
    const u16* x; const u16* w1; const u16* b1; const u16* w2; const u16* b2;
    u16* out; char* ws;
    unsigned mode, pad0;
};
#define PARS auto pp_ = (__attribute__((address_space(4))) const Args*)__builtin_amdgcn_kernarg_segment_ptr(); asm volatile("" : "+s"(pp_))
constexpr unsigned PARV[NPAR] = {CH_PAR};
#define par(i) PARV[i]
#define RSTAMP(sg, i) do { } while (0)
#define STAMP_WAVE 0
#define DBG(a, i) 0ull
constexpr unsigned SPIN_CAP = 1u << 22;
constexpr unsigned SENT = 0xFFFFFFFFu;

__device__ __forceinline__ void lds_barrier() { asm volatile("s_waitcnt lgkmcnt(0)\n\ts_barrier" ::: "memory"); }
__device__ __forceinline__ void wait_vm0() { asm volatile("s_waitcnt vmcnt(0)" ::: "memory"); }
typedef __bf16 bf16x2_t __attribute__((ext_vector_type(2)));
__device__ __forceinline__ unsigned pack2(float a, float b) { const f32x2 v = {a, b}; return __builtin_bit_cast(unsigned, __builtin_convertvector(v, bf16x2_t)); }
__device__ __forceinline__ float lo16f(unsigned u) { return __uint_as_float(u << 16); }
__device__ __forceinline__ float hi16f(unsigned u) { return __uint_as_float(u & 0xFFFF0000u); }
__device__ __forceinline__ unsigned umax4(v4u x) { return max(max(x.x, x.y), max(x.z, x.w)); }
__device__ __forceinline__ unsigned unsent(unsigned x) { return min(x, 0xFFFFFFFEu); }

__device__ __forceinline__ unsigned ldg4_sc1(const void* sb, unsigned vo) {
    unsigned r;
    asm volatile("global_load_dword %0, %1, %2 sc1\n\ts_waitcnt vmcnt(0)" : "=&v"(r) : "v"(vo), "s"(sb) : "memory");
    return r;
}
__device__ __forceinline__ bool spin_fail(unsigned& spins, unsigned* abortw) {
    if (++spins > SPIN_CAP) { __hip_atomic_store((gu32*)abortw, 1u, RLX_AGENT); return true; }
    if ((spins & 63u) == 0 && __builtin_amdgcn_readfirstlane(__hip_atomic_load((gu32*)abortw, RLX_AGENT)) == 1u) return true;
    return false;
}
__device__ __forceinline__ float red16_add(float x) { auto r = __builtin_amdgcn_permlane16_swap(__float_as_uint(x), __float_as_uint(x), false, false); return __uint_as_float(r[0]) + __uint_as_float(r[1]); }
__device__ __forceinline__ float red32_add(float x) { auto r = __builtin_amdgcn_permlane32_swap(__float_as_uint(x), __float_as_uint(x), false, false); return __uint_as_float(r[0]) + __uint_as_float(r[1]); }
__device__ __forceinline__ v2u tr_rd(const unsigned char* lds_base, unsigned off) {
    return __builtin_bit_cast(v2u, __builtin_amdgcn_ds_read_tr16_b64_v4bf16((LAS bf16x4_t*)(lds_base + off)));
}
__device__ __forceinline__ v4u lds16(const unsigned char* p) { return *(const LAS v4u*)p; }
__device__ __forceinline__ void dma16(const void* src, unsigned char* dst, int aux) {
    if (aux) __builtin_amdgcn_global_load_lds((const unsigned*)src, (unsigned*)dst, 16, 0, 2);
    else __builtin_amdgcn_global_load_lds((const unsigned*)src, (unsigned*)dst, 16, 0, 0);
}

__device__ __forceinline__ v4u mkrs(const void* p, unsigned bytes) { return (v4u){(unsigned)(u64)p, (unsigned)((u64)p >> 32) & 0xFFFFu, bytes, 0x00020000u}; }
template <int AUX> __device__ __forceinline__ void bdma(v4u r, unsigned vo, unsigned so, unsigned ldsaddr) {
    if (AUX == 16) asm volatile("s_mov_b32 m0, %0\n\ts_nop 0\n\tbuffer_load_dwordx4 %1, %2, %3 offen sc1 lds" :: "s"(ldsaddr), "v"(vo), "s"(r), "s"(so) : "m0");
    else if (AUX) asm volatile("s_mov_b32 m0, %0\n\ts_nop 0\n\tbuffer_load_dwordx4 %1, %2, %3 offen nt lds" :: "s"(ldsaddr), "v"(vo), "s"(r), "s"(so) : "m0");
    else asm volatile("s_mov_b32 m0, %0\n\ts_nop 0\n\tbuffer_load_dwordx4 %1, %2, %3 offen lds" :: "s"(ldsaddr), "v"(vo), "s"(r), "s"(so) : "m0");
}

template <int I> struct IC { static constexpr int v = I; };
template <int B, int E, class F> __device__ __forceinline__ void sfor(F&& f) { if constexpr (B < E) { f(IC<B>{}); sfor<B + 1, E>(f); } }
#define SB() __builtin_amdgcn_sched_barrier(0)

__device__ __forceinline__ int lane_id() { int l; asm volatile("v_mbcnt_lo_u32_b32 %0, -1, 0\n\tv_mbcnt_hi_u32_b32 %0, -1, %0" : "=v"(l)); return l; }
constexpr int LDS_BYTES = 163840;
typedef __amdgpu_buffer_rsrc_t rsrc_t;
__device__ __forceinline__ v4u bld16(rsrc_t r, unsigned off) { return __builtin_bit_cast(v4u, __builtin_amdgcn_raw_buffer_load_b128(r, off, 0, 16)); }
__device__ __forceinline__ v2u bld8(rsrc_t r, unsigned off) { return __builtin_bit_cast(v2u, __builtin_amdgcn_raw_buffer_load_b64(r, off, 0, 16)); }
__device__ __forceinline__ void bst16(rsrc_t r, unsigned off, v4u v) { __builtin_amdgcn_raw_buffer_store_b128(__builtin_bit_cast(__attribute__((ext_vector_type(4))) unsigned, v), r, off, 0, 16); }
__device__ __forceinline__ void bst8(rsrc_t r, unsigned off, v2u v) { __builtin_amdgcn_raw_buffer_store_b64(__builtin_bit_cast(__attribute__((ext_vector_type(2))) unsigned, v), r, off, 0, 16); }

constexpr int L_FLAG = LDS_BYTES - 64;
#define otid() (a.pad0 * 64 + lane_id())
__device__ __forceinline__ v4u sld16(rsrc_t r, unsigned vo, unsigned so) { return __builtin_bit_cast(v4u, __builtin_amdgcn_raw_buffer_load_b128(r, vo, so, 16)); }
__device__ __forceinline__ v4u sentv() { unsigned x = SENT; asm volatile("" : "+v"(x)); return (v4u){x, x, x, x}; }
#define BW_SLEEP 16
template <int SLP = BW_SLEEP> __device__ __forceinline__ bool wave_wait(const unsigned* p, int stride, int n, unsigned target, unsigned* abortw, unsigned char* lds = nullptr) {
    const int lane = lane_id();
    for (unsigned spins = 0;;) {
        const unsigned v = lane < n ? __hip_atomic_load((gu32*)(p + lane * stride), RLX_AGENT) : target;
        if (!__any(v < target)) { if (lds && lane == 0) *(volatile LAS unsigned*)(lds + L_FLAG + 32) = v; return true; }
        __builtin_amdgcn_s_sleep(SLP);
        if (++spins > (SPIN_CAP << 1)) { __hip_atomic_store((gu32*)abortw, 1u, RLX_AGENT); return false; }
        if ((spins & 63u) == 0 && __builtin_amdgcn_readfirstlane(__hip_atomic_load((gu32*)abortw, RLX_AGENT)) == 1u) return false;
    }
}
__device__ __forceinline__ void drained(unsigned* cnt) {
    asm volatile("s_waitcnt vmcnt(0)\n\ts_barrier" ::: "memory");
    if (threadIdx.x == 0) __hip_atomic_fetch_add((gu32*)cnt, 1u, RLX_AGENT);
}

constexpr int L_W = 0;
constexpr int L_H = 98304;
constexpr int L_KX = L_H + 40960;
constexpr int L_QX = L_KX + 8192;
constexpr int L_T = L_QX + 8192;
constexpr int L_ST = L_T + 4096;
constexpr int lsl(bool isV, int c, int n) { return isV ? (c < 6 && (n & 1) ? 2 * c + (n >> 1) : -1) : (c < 12 && n == 1 ? c : -1); }
constexpr int ridx(bool isV, int c, int n) { int r = 0; for (int i = 0; i < 4 * c + n; ++i) r += lsl(isV, i >> 2, i & 3) < 0; return r; }
#define ASM_Q \
    "s_add_u32 m0, s41, 0\ns_xor_b32 s85, s62, 0\ns_add_u32 s85, s85, s40\nbuffer_load_dwordx4 v11, s[64:67], s85 offen sc1 lds\n" \
    "buffer_load_dwordx4 v12, s[64:67], s85 offen offset:1024 sc1 lds\ns_add_u32 m0, s41, 8192\ns_xor_b32 s85, s62, 256\ns_add_u32 s85, s85, s40\n" \
    "buffer_load_dwordx4 v11, s[64:67], s85 offen sc1 lds\nbuffer_load_dwordx4 v12, s[64:67], s85 offen offset:1024 sc1 lds\ns_add_u32 m0, s41, 16384\ns_xor_b32 s85, s62, 512\n" \
    "s_add_u32 s85, s85, s40\nbuffer_load_dwordx4 v11, s[64:67], s85 offen sc1 lds\nbuffer_load_dwordx4 v12, s[64:67], s85 offen offset:1024 sc1 lds\ns_memrealtime s[72:73]\n" \
    "s_waitcnt lgkmcnt(0)\ns_mov_b32 s80, s72\ns_mov_b32 s83, 0x7fff0000\ns_branch .Lq8_%=\n.Lq9_%=:\ns_waitcnt vmcnt(0)\ns_barrier\n.Lq2_%=:\n" \
    "ds_read_b128 v[64:67], v3 offset:0\nds_read_b128 v[68:71], v9 offset:0\nds_read_b128 v[72:75], v9 offset:4096\n" \
    "v_mov_b32_dpp v16, v20 quad_perm:[0,1,2,3] row_mask:0xf bank_mask:0xc\nv_mov_b32_dpp v17, v21 quad_perm:[0,1,2,3] row_mask:0xf bank_mask:0xc\n" \
    "v_mov_b32_dpp v18, v22 quad_perm:[0,1,2,3] row_mask:0xf bank_mask:0xc\nv_mov_b32_dpp v19, v23 quad_perm:[0,1,2,3] row_mask:0xf bank_mask:0xc\nv_mov_b32 v254, 0xff800000\n" \
    "v_cvt_pk_bf16_f32 v16, 0, v16\nv_cvt_pk_bf16_f32 v17, 0, v17\nv_cvt_pk_bf16_f32 v18, 0, v18\nv_cvt_pk_bf16_f32 v19, 0, v19\nv_mul_f32 v16, s33, v16\n" \
    "v_mul_f32 v17, s33, v17\nv_mul_f32 v18, s33, v18\ns_waitcnt lgkmcnt(0)\nv_mfma_f32_16x16x32_bf16 v[48:51], a[8:11], v[68:71], 0\ns_add_u32 m0, s41, 24576\n" \
    "v_mul_f32 v19, s33, v19\nv_cvt_pk_bf16_f32 v16, 0, v16\nv_mfma_f32_16x16x32_bf16 v[52:55], a[8:11], v[72:75], 0\nds_read_b128 v[76:79], v3 offset:1024\n" \
    "v_cvt_pk_bf16_f32 v17, 0, v17\nv_cvt_pk_bf16_f32 v18, 0, v18\nv_mfma_f32_16x16x32_bf16 v[56:59], a[16:19], v[68:71], 0\nds_read_b128 v[80:83], v10 offset:0\n" \
    "v_cvt_pk_bf16_f32 v19, 0, v19\nv_cndmask_b32 v16, v254, v16, s[50:51]\nv_mfma_f32_16x16x32_bf16 v[60:63], a[16:19], v[72:75], 0\nds_read_b128 v[84:87], v10 offset:4096\n" \
    "v_cndmask_b32 v17, v254, v17, s[52:53]\nv_cndmask_b32 v18, v254, v18, s[54:55]\nv_mfma_f32_16x16x32_bf16 v[32:35], a[0:3], v[68:71], 0\ns_xor_b32 s85, s62, 768\n" \
    "v_cndmask_b32 v19, v254, v19, s[56:57]\nv_max3_f32 v254, v16, v17, v18\nv_mfma_f32_16x16x32_bf16 v[36:39], a[0:3], v[72:75], 0\ns_add_u32 s85, s85, s40\n" \
    "v_max_f32 v254, v254, v19\nv_mfma_f32_16x16x32_bf16 v[40:43], v[64:67], v[68:71], 0\nbuffer_load_dwordx4 v11, s[64:67], s85 offen sc1 lds\n" \
    "v_max_f32_dpp v252, v254, v254 row_ror:8 row_mask:0xf bank_mask:0xf\nv_max_f32_dpp v253, v254, v254 row_ror:8 row_mask:0xf bank_mask:0xf\n" \
    "v_mfma_f32_16x16x32_bf16 v[44:47], v[64:67], v[72:75], 0\ns_nop 0\ns_waitcnt lgkmcnt(0)\nv_permlane32_swap_b32 v252, v253\n" \
    "v_mfma_f32_16x16x32_bf16 v[48:51], a[12:15], v[80:83], v[48:51]\nv_max_f32 v252, v252, v253\nv_cvt_pk_bf16_f32 v24, 0, v24\n" \
    "v_mfma_f32_16x16x32_bf16 v[52:55], a[12:15], v[84:87], v[52:55]\nds_read_b128 v[88:91], v3 offset:2048\nv_cvt_pk_bf16_f32 v25, 0, v25\nv_cvt_pk_bf16_f32 v26, 0, v26\n" \
    "v_mfma_f32_16x16x32_bf16 v[56:59], a[20:23], v[80:83], v[56:59]\nds_read_b128 v[64:67], v9 offset:1024\nv_cvt_pk_bf16_f32 v27, 0, v27\nv_mul_f32 v24, s33, v24\n" \
    "v_mfma_f32_16x16x32_bf16 v[60:63], a[20:23], v[84:87], v[60:63]\nds_read_b128 v[68:71], v9 offset:5120\nv_mul_f32 v25, s33, v25\nv_mul_f32 v26, s33, v26\n" \
    "v_mfma_f32_16x16x32_bf16 v[32:35], a[4:7], v[80:83], v[32:35]\nbuffer_load_dwordx4 v12, s[64:67], s85 offen offset:1024 sc1 lds\nv_mul_f32 v27, s33, v27\n" \
    "v_cvt_pk_bf16_f32 v24, 0, v24\nv_mfma_f32_16x16x32_bf16 v[36:39], a[4:7], v[84:87], v[36:39]\ns_add_u32 m0, s41, 32768\nv_cvt_pk_bf16_f32 v25, 0, v25\n" \
    "v_cvt_pk_bf16_f32 v26, 0, v26\nv_mfma_f32_16x16x32_bf16 v[40:43], v[76:79], v[80:83], v[40:43]\ns_xor_b32 s85, s62, 1024\nv_cvt_pk_bf16_f32 v27, 0, v27\n" \
    "v_max3_f32 v254, v252, v24, v25\nv_mfma_f32_16x16x32_bf16 v[44:47], v[76:79], v[84:87], v[44:47]\ns_add_u32 s85, s85, s40\ns_waitcnt lgkmcnt(0)\n" \
    "v_max3_f32 v252, v254, v26, v27\nv_mfma_f32_16x16x32_bf16 v[48:51], a[32:35], v[64:67], v[48:51]\nbuffer_load_dwordx4 v11, s[64:67], s85 offen sc1 lds\n" \
    "v_max3_f32 v253, v254, v26, v27\nv_mfma_f32_16x16x32_bf16 v[52:55], a[32:35], v[68:71], v[52:55]\nds_read_b128 v[72:75], v3 offset:3072\nv_permlane16_swap_b32 v252, v253\n" \
    "v_mfma_f32_16x16x32_bf16 v[56:59], a[40:43], v[64:67], v[56:59]\nds_read_b128 v[76:79], v10 offset:1024\nv_max_f32 v252, v252, v253\nv_sub_f32 v16, v16, v252\n" \
    "v_mfma_f32_16x16x32_bf16 v[60:63], a[40:43], v[68:71], v[60:63]\nds_read_b128 v[80:83], v10 offset:5120\nv_sub_f32 v17, v17, v252\nv_sub_f32 v18, v18, v252\n" \
    "v_mfma_f32_16x16x32_bf16 v[32:35], a[24:27], v[64:67], v[32:35]\nbuffer_load_dwordx4 v12, s[64:67], s85 offen offset:1024 sc1 lds\nv_sub_f32 v19, v19, v252\n" \
    "v_sub_f32 v24, v24, v252\nv_mfma_f32_16x16x32_bf16 v[36:39], a[24:27], v[68:71], v[36:39]\ns_add_u32 s84, s40, 0x20000\nv_sub_f32 v25, v25, v252\nv_sub_f32 v26, v26, v252\n" \
    "v_mfma_f32_16x16x32_bf16 v[40:43], v[88:91], v[64:67], v[40:43]\ns_cmp_lt_u32 s35, 62\ns_cselect_b32 s84, s84, s40\nv_sub_f32 v27, v27, v252\n" \
    "v_mfma_f32_16x16x32_bf16 v[44:47], v[88:91], v[68:71], v[44:47]\ns_waitcnt lgkmcnt(0)\nv_mul_f32 v16, s34, v16\nv_mul_f32 v17, s34, v17\n" \
    "v_mfma_f32_16x16x32_bf16 v[48:51], a[36:39], v[76:79], v[48:51]\nv_mul_f32 v18, s34, v18\nv_mul_f32 v19, s34, v19\n" \
    "v_mfma_f32_16x16x32_bf16 v[52:55], a[36:39], v[80:83], v[52:55]\nds_read_b128 v[64:67], v3 offset:4096\nv_mul_f32 v24, s34, v24\nv_mul_f32 v25, s34, v25\n" \
    "v_mfma_f32_16x16x32_bf16 v[56:59], a[44:47], v[76:79], v[56:59]\nds_read_b128 v[68:71], v9 offset:8192\nv_mul_f32 v26, s34, v26\nv_mul_f32 v27, s34, v27\n" \
    "v_mfma_f32_16x16x32_bf16 v[60:63], a[44:47], v[80:83], v[60:63]\nds_read_b128 v[84:87], v9 offset:12288\nv_exp_f32 v16, v16\n" \
    "v_mfma_f32_16x16x32_bf16 v[32:35], a[28:31], v[76:79], v[32:35]\nv_exp_f32 v17, v17\nv_mfma_f32_16x16x32_bf16 v[36:39], a[28:31], v[80:83], v[36:39]\nv_exp_f32 v18, v18\n" \
    "v_mfma_f32_16x16x32_bf16 v[40:43], v[72:75], v[76:79], v[40:43]\nv_exp_f32 v19, v19\nv_mfma_f32_16x16x32_bf16 v[44:47], v[72:75], v[80:83], v[44:47]\ns_waitcnt lgkmcnt(0)\n" \
    "v_exp_f32 v24, v24\nv_mfma_f32_16x16x32_bf16 v[48:51], a[56:59], v[68:71], v[48:51]\nv_add_f32 v254, v16, v18\nv_add_f32 v255, v17, v19\n" \
    "v_mfma_f32_16x16x32_bf16 v[52:55], a[56:59], v[84:87], v[52:55]\nds_read_b128 v[88:91], v3 offset:5120\nv_exp_f32 v25, v25\n" \
    "v_mfma_f32_16x16x32_bf16 v[56:59], a[64:67], v[68:71], v[56:59]\nds_read_b128 v[72:75], v10 offset:8192\nv_add_f32 v254, v254, v255\nv_exp_f32 v26, v26\n" \
    "v_mfma_f32_16x16x32_bf16 v[60:63], a[64:67], v[84:87], v[60:63]\nds_read_b128 v[76:79], v10 offset:12288\nv_exp_f32 v27, v27\n" \
    "v_mfma_f32_16x16x32_bf16 v[32:35], a[48:51], v[68:71], v[32:35]\nv_add_f32_dpp v252, v254, v254 row_ror:8 row_mask:0xf bank_mask:0xf\n" \
    "v_add_f32_dpp v253, v254, v254 row_ror:8 row_mask:0xf bank_mask:0xf\nv_mfma_f32_16x16x32_bf16 v[36:39], a[48:51], v[84:87], v[36:39]\nv_add_f32 v254, v24, v26\n" \
    "v_add_f32 v255, v25, v27\nv_mfma_f32_16x16x32_bf16 v[40:43], v[64:67], v[68:71], v[40:43]\nv_permlane32_swap_b32 v252, v253\n" \
    "v_mfma_f32_16x16x32_bf16 v[44:47], v[64:67], v[84:87], v[44:47]\ns_waitcnt lgkmcnt(0)\nv_add_f32 v254, v254, v252\nv_add_f32 v255, v255, v253\n" \
    "v_mfma_f32_16x16x32_bf16 v[48:51], a[60:63], v[72:75], v[48:51]\nv_add_f32 v252, v254, v255\nv_add_f32 v253, v255, v254\n" \
    "v_mfma_f32_16x16x32_bf16 v[52:55], a[60:63], v[76:79], v[52:55]\nds_read_b128 v[64:67], v3 offset:6144\nv_permlane16_swap_b32 v252, v253\n" \
    "v_mfma_f32_16x16x32_bf16 v[56:59], a[68:71], v[72:75], v[56:59]\nds_read_b128 v[68:71], v9 offset:9216\nv_add_f32 v252, v252, v253\nv_rcp_f32 v252, v252\n" \
    "v_mfma_f32_16x16x32_bf16 v[60:63], a[68:71], v[76:79], v[60:63]\nds_read_b128 v[80:83], v9 offset:13312\nv_mul_f32 v16, v16, v252\nv_mul_f32 v17, v17, v252\n" \
    "v_mfma_f32_16x16x32_bf16 v[32:35], a[52:55], v[72:75], v[32:35]\nv_mul_f32 v24, v24, v252\nv_mul_f32 v25, v25, v252\n" \
    "v_mfma_f32_16x16x32_bf16 v[36:39], a[52:55], v[76:79], v[36:39]\nv_cvt_pk_bf16_f32 v20, v16, v17\nv_cvt_pk_bf16_f32 v22, v24, v25\n" \
    "v_mfma_f32_16x16x32_bf16 v[40:43], v[88:91], v[72:75], v[40:43]\nv_mul_f32 v18, v18, v252\nv_mul_f32 v19, v19, v252\n" \
    "v_mfma_f32_16x16x32_bf16 v[44:47], v[88:91], v[76:79], v[44:47]\ns_waitcnt lgkmcnt(0)\nv_mul_f32 v26, v26, v252\nv_mul_f32 v27, v27, v252\n" \
    "v_mfma_f32_16x16x32_bf16 v[48:51], a[80:83], v[68:71], v[48:51]\nv_cvt_pk_bf16_f32 v21, v18, v19\nv_cvt_pk_bf16_f32 v23, v26, v27\n" \
    "v_mfma_f32_16x16x32_bf16 v[52:55], a[80:83], v[80:83], v[52:55]\nds_read_b128 v[72:75], v3 offset:7168\nv_permlane32_swap_b32 v20, v22\n" \
    "v_mfma_f32_16x16x32_bf16 v[56:59], a[88:91], v[68:71], v[56:59]\nds_read_b128 v[76:79], v10 offset:9216\nv_permlane32_swap_b32 v21, v23\n" \
    "v_mfma_f32_16x16x32_bf16 v[60:63], a[88:91], v[80:83], v[60:63]\nds_read_b128 v[84:87], v10 offset:13312\ns_xor_b32 s74, s83, 0x2000\n" \
    "v_mfma_f32_16x16x32_bf16 v[32:35], a[72:75], v[68:71], v[32:35]\nbuffer_store_dwordx4 v[20:23], v7, s[64:67], s83 offen sc1\n" \
    "v_mfma_f32_16x16x32_bf16 v[36:39], a[72:75], v[80:83], v[36:39]\nbuffer_store_dwordx4 v[20:23], v7, s[64:67], s74 offen sc1\nv_mov_b64 v[28:29], -1\n" \
    "v_mov_b64 v[30:31], -1\nv_mfma_f32_16x16x32_bf16 v[40:43], v[64:67], v[68:71], v[40:43]\ns_add_u32 s69, s35, 2\ns_and_b32 s69, s69, 15\n" \
    "v_mfma_f32_16x16x32_bf16 v[44:47], v[64:67], v[80:83], v[44:47]\ns_lshl_b32 s69, s69, 18\ns_add_u32 s82, s38, s69\ns_waitcnt lgkmcnt(0)\n" \
    "v_mfma_f32_16x16x32_bf16 v[48:51], a[84:87], v[76:79], v[48:51]\ns_lshr_b32 s69, s82, 8\ns_and_b32 s69, s69, 0x2000\n" \
    "v_mfma_f32_16x16x32_bf16 v[52:55], a[84:87], v[84:87], v[52:55]\nds_read_b128 v[80:83], v3 offset:8192\ns_xor_b32 s82, s82, s69\n" \
    "v_mfma_f32_16x16x32_bf16 v[56:59], a[92:95], v[76:79], v[56:59]\nds_read_b128 v[88:91], v9 offset:16384\ns_add_u32 s69, s35, 2\n" \
    "v_mfma_f32_16x16x32_bf16 v[60:63], a[92:95], v[84:87], v[60:63]\nds_read_b128 v[64:67], v9 offset:20480\ns_and_b32 s69, s69, 15\n" \
    "v_mfma_f32_16x16x32_bf16 v[32:35], a[76:79], v[76:79], v[32:35]\ns_lshl_b32 s69, s69, 18\ns_add_u32 s83, s39, s69\n" \
    "v_mfma_f32_16x16x32_bf16 v[36:39], a[76:79], v[84:87], v[36:39]\ns_xor_b32 s74, s82, 0x4000\ns_xor_b32 s75, s83, 0x2000\n" \
    "v_mfma_f32_16x16x32_bf16 v[40:43], v[72:75], v[76:79], v[40:43]\nv_mfma_f32_16x16x32_bf16 v[44:47], v[72:75], v[84:87], v[44:47]\ns_waitcnt lgkmcnt(0)\n" \
    "v_mfma_f32_16x16x32_bf16 v[48:51], a[104:107], v[88:91], v[48:51]\nv_mfma_f32_16x16x32_bf16 v[52:55], a[104:107], v[64:67], v[52:55]\n" \
    "ds_read_b128 v[84:87], v3 offset:9216\nv_mfma_f32_16x16x32_bf16 v[56:59], a[112:115], v[88:91], v[56:59]\nds_read_b128 v[68:71], v10 offset:16384\n" \
    "v_mfma_f32_16x16x32_bf16 v[60:63], a[112:115], v[64:67], v[60:63]\nds_read_b128 v[72:75], v10 offset:20480\nbuffer_store_dwordx4 v[28:31], v7, s[64:67], s82 offen sc1\n" \
    "v_mfma_f32_16x16x32_bf16 v[32:35], a[96:99], v[88:91], v[32:35]\nv_mfma_f32_16x16x32_bf16 v[36:39], a[96:99], v[64:67], v[36:39]\n" \
    "v_mfma_f32_16x16x32_bf16 v[40:43], v[80:83], v[88:91], v[40:43]\nv_mfma_f32_16x16x32_bf16 v[44:47], v[80:83], v[64:67], v[44:47]\ns_waitcnt lgkmcnt(0)\n" \
    "v_mfma_f32_16x16x32_bf16 v[48:51], a[108:111], v[68:71], v[48:51]\nv_mfma_f32_16x16x32_bf16 v[52:55], a[108:111], v[72:75], v[52:55]\n" \
    "ds_read_b128 v[64:67], v3 offset:10240\nbuffer_store_dwordx4 v[28:31], v7, s[64:67], s74 offen sc1\nv_mfma_f32_16x16x32_bf16 v[56:59], a[116:119], v[68:71], v[56:59]\n" \
    "ds_read_b128 v[88:91], v9 offset:17408\nv_mfma_f32_16x16x32_bf16 v[60:63], a[116:119], v[72:75], v[60:63]\nds_read_b128 v[76:79], v9 offset:21504\n" \
    "v_mfma_f32_16x16x32_bf16 v[32:35], a[100:103], v[68:71], v[32:35]\nv_mfma_f32_16x16x32_bf16 v[36:39], a[100:103], v[72:75], v[36:39]\n" \
    "v_mfma_f32_16x16x32_bf16 v[40:43], v[84:87], v[68:71], v[40:43]\nv_mfma_f32_16x16x32_bf16 v[44:47], v[84:87], v[72:75], v[44:47]\n" \
    "buffer_store_dwordx4 v[28:31], v7, s[64:67], s83 offen sc1\ns_waitcnt lgkmcnt(0)\nv_mfma_f32_16x16x32_bf16 v[48:51], a[128:131], v[88:91], v[48:51]\n" \
    "v_mfma_f32_16x16x32_bf16 v[52:55], a[128:131], v[76:79], v[52:55]\nds_read_b128 v[68:71], v3 offset:11264\n" \
    "v_mfma_f32_16x16x32_bf16 v[56:59], a[136:139], v[88:91], v[56:59]\nds_read_b128 v[72:75], v10 offset:17408\n" \
    "v_mfma_f32_16x16x32_bf16 v[60:63], a[136:139], v[76:79], v[60:63]\nds_read_b128 v[80:83], v10 offset:21504\n" \
    "v_mfma_f32_16x16x32_bf16 v[32:35], a[120:123], v[88:91], v[32:35]\nv_mfma_f32_16x16x32_bf16 v[36:39], a[120:123], v[76:79], v[36:39]\n" \
    "buffer_store_dwordx4 v[28:31], v7, s[64:67], s75 offen sc1\nv_lshrrev_b32 v16, 1, v2\nv_add_u32 v16, s60, v16\n" \
    "v_mfma_f32_16x16x32_bf16 v[40:43], v[64:67], v[88:91], v[40:43]\nds_write_b64 v16, v[28:29]\nv_mfma_f32_16x16x32_bf16 v[44:47], v[64:67], v[76:79], v[44:47]\n" \
    "ds_write_b64 v16, v[28:29] offset:2048\ns_add_u32 s69, s35, 2\ns_waitcnt lgkmcnt(2)\nv_mfma_f32_16x16x32_bf16 v[48:51], a[132:135], v[72:75], v[48:51]\n" \
    "s_and_b32 s69, s69, 255\ns_lshl_b32 s69, s69, 19\nv_mfma_f32_16x16x32_bf16 v[52:55], a[132:135], v[80:83], v[52:55]\nds_read_b128 v[64:67], v3 offset:12288\n" \
    "s_add_u32 s74, s63, s69\ns_waitcnt vmcnt(6)\ns_barrier\nv_mfma_f32_16x16x32_bf16 v[56:59], a[140:143], v[72:75], v[56:59]\nds_read_b128 v[76:79], v9 offset:24576\n" \
    "s_cmp_lt_u32 s35, 62\ns_cselect_b32 s74, s74, 0x7fff0000\nv_mfma_f32_16x16x32_bf16 v[60:63], a[140:143], v[80:83], v[60:63]\nds_read_b128 v[84:87], v9 offset:28672\n" \
    "s_add_u32 m0, s41, 0\nv_mfma_f32_16x16x32_bf16 v[32:35], a[124:127], v[72:75], v[32:35]\ns_xor_b32 s85, s62, 1280\ns_add_u32 s85, s85, s40\n" \
    "v_mfma_f32_16x16x32_bf16 v[36:39], a[124:127], v[80:83], v[36:39]\nbuffer_load_dwordx4 v11, s[64:67], s85 offen sc1 lds\n" \
    "v_mfma_f32_16x16x32_bf16 v[40:43], v[68:71], v[72:75], v[40:43]\nv_mfma_f32_16x16x32_bf16 v[44:47], v[68:71], v[80:83], v[44:47]\ns_waitcnt lgkmcnt(0)\n" \
    "v_mfma_f32_16x16x32_bf16 v[48:51], a[152:155], v[76:79], v[48:51]\nbuffer_load_dwordx4 v12, s[64:67], s85 offen offset:1024 sc1 lds\ns_add_u32 m0, s41, 8192\n" \
    "v_mfma_f32_16x16x32_bf16 v[52:55], a[152:155], v[84:87], v[52:55]\nds_read_b128 v[80:83], v3 offset:13312\ns_xor_b32 s85, s62, 1536\n" \
    "v_mfma_f32_16x16x32_bf16 v[56:59], a[160:163], v[76:79], v[56:59]\nds_read_b128 v[88:91], v10 offset:24576\ns_add_u32 s85, s85, s40\n" \
    "v_mfma_f32_16x16x32_bf16 v[60:63], a[160:163], v[84:87], v[60:63]\nds_read_b128 v[68:71], v10 offset:28672\nbuffer_load_dwordx4 v11, s[64:67], s85 offen sc1 lds\n" \
    "v_mfma_f32_16x16x32_bf16 v[32:35], a[144:147], v[76:79], v[32:35]\nbuffer_store_dwordx4 v[28:31], v2, s[64:67], s74 offen offset:0 sc1\n" \
    "v_mfma_f32_16x16x32_bf16 v[36:39], a[144:147], v[84:87], v[36:39]\nv_mfma_f32_16x16x32_bf16 v[40:43], v[64:67], v[76:79], v[40:43]\n" \
    "buffer_load_dwordx4 v12, s[64:67], s85 offen offset:1024 sc1 lds\nv_mfma_f32_16x16x32_bf16 v[44:47], v[64:67], v[84:87], v[44:47]\ns_waitcnt lgkmcnt(0)\n" \
    "v_mfma_f32_16x16x32_bf16 v[48:51], a[156:159], v[88:91], v[48:51]\nv_mfma_f32_16x16x32_bf16 v[52:55], a[156:159], v[68:71], v[52:55]\n" \
    "ds_read_b128 v[64:67], v3 offset:14336\nv_mfma_f32_16x16x32_bf16 v[56:59], a[164:167], v[88:91], v[56:59]\nds_read_b128 v[84:87], v9 offset:25600\n" \
    "buffer_store_dwordx4 v[28:31], v2, s[64:67], s74 offen offset:1024 sc1\nv_mfma_f32_16x16x32_bf16 v[60:63], a[164:167], v[68:71], v[60:63]\n" \
    "ds_read_b128 v[72:75], v9 offset:29696\ns_and_b32 s69, s35, 15\nv_mfma_f32_16x16x32_bf16 v[32:35], a[148:151], v[88:91], v[32:35]\ns_lshl_b32 s69, s69, 18\n" \
    "s_add_u32 s82, s38, s69\nv_mfma_f32_16x16x32_bf16 v[36:39], a[148:151], v[68:71], v[36:39]\ns_lshr_b32 s69, s82, 8\ns_and_b32 s69, s69, 0x2000\n" \
    "v_mfma_f32_16x16x32_bf16 v[40:43], v[80:83], v[88:91], v[40:43]\ns_xor_b32 s82, s82, s69\ns_and_b32 s69, s35, 15\n" \
    "v_mfma_f32_16x16x32_bf16 v[44:47], v[80:83], v[68:71], v[44:47]\ns_lshl_b32 s69, s69, 18\ns_add_u32 s83, s39, s69\ns_waitcnt lgkmcnt(0)\n" \
    "v_mfma_f32_16x16x32_bf16 v[48:51], a[176:179], v[84:87], v[48:51]\nv_mfma_f32_16x16x32_bf16 v[52:55], a[176:179], v[72:75], v[52:55]\n" \
    "ds_read_b128 v[68:71], v3 offset:15360\nv_mfma_f32_16x16x32_bf16 v[56:59], a[184:187], v[84:87], v[56:59]\nds_read_b128 v[88:91], v10 offset:25600\n" \
    "v_mfma_f32_16x16x32_bf16 v[60:63], a[184:187], v[72:75], v[60:63]\nds_read_b128 v[76:79], v10 offset:29696\n" \
    "v_mfma_f32_16x16x32_bf16 v[32:35], a[168:171], v[84:87], v[32:35]\nv_mfma_f32_16x16x32_bf16 v[36:39], a[168:171], v[72:75], v[36:39]\n" \
    "v_mfma_f32_16x16x32_bf16 v[40:43], v[64:67], v[84:87], v[40:43]\nv_mfma_f32_16x16x32_bf16 v[44:47], v[64:67], v[72:75], v[44:47]\ns_waitcnt lgkmcnt(0)\n" \
    "v_mfma_f32_16x16x32_bf16 v[48:51], a[180:183], v[88:91], v[48:51]\nv_mfma_f32_16x16x32_bf16 v[52:55], a[180:183], v[76:79], v[52:55]\n" \
    "ds_read_b128 v[64:67], v3 offset:16384\nv_mfma_f32_16x16x32_bf16 v[56:59], a[188:191], v[88:91], v[56:59]\nds_read_b128 v[72:75], v9 offset:32768\n" \
    "v_mfma_f32_16x16x32_bf16 v[60:63], a[188:191], v[76:79], v[60:63]\nds_read_b128 v[80:83], v9 offset:36864\n" \
    "v_mfma_f32_16x16x32_bf16 v[32:35], a[172:175], v[88:91], v[32:35]\nv_mfma_f32_16x16x32_bf16 v[36:39], a[172:175], v[76:79], v[36:39]\n" \
    "v_mfma_f32_16x16x32_bf16 v[40:43], v[68:71], v[88:91], v[40:43]\nv_mfma_f32_16x16x32_bf16 v[44:47], v[68:71], v[76:79], v[44:47]\ns_waitcnt lgkmcnt(0)\n" \
    "v_mfma_f32_16x16x32_bf16 v[48:51], a[200:203], v[72:75], v[48:51]\nv_mfma_f32_16x16x32_bf16 v[52:55], a[200:203], v[80:83], v[52:55]\n" \
    "ds_read_b128 v[68:71], v3 offset:17408\nv_mfma_f32_16x16x32_bf16 v[56:59], a[208:211], v[72:75], v[56:59]\nds_read_b128 v[76:79], v10 offset:32768\n" \
    "v_mfma_f32_16x16x32_bf16 v[60:63], a[208:211], v[80:83], v[60:63]\nds_read_b128 v[84:87], v10 offset:36864\n" \
    "v_mfma_f32_16x16x32_bf16 v[32:35], a[192:195], v[72:75], v[32:35]\nv_mfma_f32_16x16x32_bf16 v[36:39], a[192:195], v[80:83], v[36:39]\n" \
    "v_mfma_f32_16x16x32_bf16 v[40:43], v[64:67], v[72:75], v[40:43]\nv_mfma_f32_16x16x32_bf16 v[44:47], v[64:67], v[80:83], v[44:47]\ns_waitcnt lgkmcnt(0)\n" \
    "v_mfma_f32_16x16x32_bf16 v[48:51], a[204:207], v[76:79], v[48:51]\nv_mfma_f32_16x16x32_bf16 v[52:55], a[204:207], v[84:87], v[52:55]\n" \
    "ds_read_b128 v[64:67], v3 offset:18432\nv_mfma_f32_16x16x32_bf16 v[56:59], a[212:215], v[76:79], v[56:59]\nds_read_b128 v[72:75], v9 offset:33792\n" \
    "v_mfma_f32_16x16x32_bf16 v[60:63], a[212:215], v[84:87], v[60:63]\nds_read_b128 v[80:83], v9 offset:37888\n" \
    "v_mfma_f32_16x16x32_bf16 v[32:35], a[196:199], v[76:79], v[32:35]\nv_mfma_f32_16x16x32_bf16 v[36:39], a[196:199], v[84:87], v[36:39]\n" \
    "v_mfma_f32_16x16x32_bf16 v[40:43], v[68:71], v[76:79], v[40:43]\nv_mfma_f32_16x16x32_bf16 v[44:47], v[68:71], v[84:87], v[44:47]\ns_waitcnt lgkmcnt(0)\n" \
    "v_mfma_f32_16x16x32_bf16 v[48:51], a[224:227], v[72:75], v[48:51]\nv_mfma_f32_16x16x32_bf16 v[52:55], a[224:227], v[80:83], v[52:55]\n" \
    "ds_read_b128 v[68:71], v3 offset:19456\nv_mfma_f32_16x16x32_bf16 v[56:59], a[232:235], v[72:75], v[56:59]\nds_read_b128 v[76:79], v10 offset:33792\n" \
    "v_mfma_f32_16x16x32_bf16 v[60:63], a[232:235], v[80:83], v[60:63]\nds_read_b128 v[84:87], v10 offset:37888\n" \
    "v_mfma_f32_16x16x32_bf16 v[32:35], a[216:219], v[72:75], v[32:35]\nv_mfma_f32_16x16x32_bf16 v[36:39], a[216:219], v[80:83], v[36:39]\n" \
    "v_mfma_f32_16x16x32_bf16 v[40:43], v[64:67], v[72:75], v[40:43]\nv_mfma_f32_16x16x32_bf16 v[44:47], v[64:67], v[80:83], v[44:47]\ns_waitcnt lgkmcnt(0)\n" \
    "v_mfma_f32_16x16x32_bf16 v[48:51], a[228:231], v[76:79], v[48:51]\nv_mfma_f32_16x16x32_bf16 v[52:55], a[228:231], v[84:87], v[52:55]\n" \
    "ds_read_b128 v[64:67], v3 offset:20480\ns_waitcnt vmcnt(1)\ns_barrier\nv_mfma_f32_16x16x32_bf16 v[56:59], a[236:239], v[76:79], v[56:59]\n" \
    "ds_read_b128 v[72:75], v9 offset:0\nv_mfma_f32_16x16x32_bf16 v[60:63], a[236:239], v[84:87], v[60:63]\nds_read_b128 v[80:83], v9 offset:4096\ns_add_u32 m0, s41, 16384\n" \
    "v_mfma_f32_16x16x32_bf16 v[32:35], a[220:223], v[76:79], v[32:35]\ns_xor_b32 s85, s62, 1792\ns_add_u32 s85, s85, s40\n" \
    "v_mfma_f32_16x16x32_bf16 v[36:39], a[220:223], v[84:87], v[36:39]\nbuffer_load_dwordx4 v11, s[64:67], s85 offen sc1 lds\n" \
    "v_mfma_f32_16x16x32_bf16 v[40:43], v[68:71], v[76:79], v[40:43]\nv_mfma_f32_16x16x32_bf16 v[44:47], v[68:71], v[84:87], v[44:47]\ns_waitcnt lgkmcnt(0)\n" \
    "v_mfma_f32_16x16x32_bf16 v[48:51], a[248:251], v[72:75], v[48:51]\nbuffer_load_dwordx4 v12, s[64:67], s85 offen offset:1024 sc1 lds\n" \
    "v_mfma_f32_16x16x32_bf16 v[52:55], a[248:251], v[80:83], v[52:55]\nds_read_b128 v[68:71], v3 offset:21504\nv_mfma_f32_16x16x32_bf16 v[56:59], v[92:95], v[72:75], v[56:59]\n" \
    "ds_read_b128 v[76:79], v10 offset:0\nv_mfma_f32_16x16x32_bf16 v[60:63], v[92:95], v[80:83], v[60:63]\nds_read_b128 v[84:87], v10 offset:4096\n" \
    "v_mfma_f32_16x16x32_bf16 v[32:35], a[240:243], v[72:75], v[32:35]\nv_mfma_f32_16x16x32_bf16 v[36:39], a[240:243], v[80:83], v[36:39]\n" \
    "v_mfma_f32_16x16x32_bf16 v[40:43], v[64:67], v[72:75], v[40:43]\nv_mfma_f32_16x16x32_bf16 v[44:47], v[64:67], v[80:83], v[44:47]\ns_waitcnt lgkmcnt(0)\n" \
    "v_mfma_f32_16x16x32_bf16 v[48:51], a[252:255], v[76:79], v[48:51]\nv_mfma_f32_16x16x32_bf16 v[52:55], a[252:255], v[84:87], v[52:55]\n" \
    "ds_read_b128 v[64:67], v3 offset:22528\nv_mfma_f32_16x16x32_bf16 v[56:59], v[96:99], v[76:79], v[56:59]\nds_read_b128 v[72:75], v9 offset:1024\n" \
    "v_mfma_f32_16x16x32_bf16 v[60:63], v[96:99], v[84:87], v[60:63]\nds_read_b128 v[80:83], v9 offset:5120\nv_mfma_f32_16x16x32_bf16 v[32:35], a[244:247], v[76:79], v[32:35]\n" \
    "v_mfma_f32_16x16x32_bf16 v[36:39], a[244:247], v[84:87], v[36:39]\nv_mfma_f32_16x16x32_bf16 v[40:43], v[68:71], v[76:79], v[40:43]\n" \
    "v_mfma_f32_16x16x32_bf16 v[44:47], v[68:71], v[84:87], v[44:47]\ns_waitcnt lgkmcnt(0)\nv_mfma_f32_16x16x32_bf16 v[48:51], v[108:111], v[72:75], v[48:51]\n" \
    "v_mfma_f32_16x16x32_bf16 v[52:55], v[108:111], v[80:83], v[52:55]\nds_read_b128 v[68:71], v3 offset:23552\n" \
    "v_mfma_f32_16x16x32_bf16 v[56:59], v[116:119], v[72:75], v[56:59]\nds_read_b128 v[76:79], v10 offset:1024\n" \
    "v_mfma_f32_16x16x32_bf16 v[60:63], v[116:119], v[80:83], v[60:63]\nds_read_b128 v[84:87], v10 offset:5120\n" \
    "v_mfma_f32_16x16x32_bf16 v[32:35], v[100:103], v[72:75], v[32:35]\nv_mfma_f32_16x16x32_bf16 v[36:39], v[100:103], v[80:83], v[36:39]\n" \
    "v_mfma_f32_16x16x32_bf16 v[40:43], v[64:67], v[72:75], v[40:43]\nv_mfma_f32_16x16x32_bf16 v[44:47], v[64:67], v[80:83], v[44:47]\ns_waitcnt lgkmcnt(0)\n" \
    "v_mfma_f32_16x16x32_bf16 v[48:51], v[112:115], v[76:79], v[48:51]\nv_mfma_f32_16x16x32_bf16 v[52:55], v[112:115], v[84:87], v[52:55]\n" \
    "v_mfma_f32_16x16x32_bf16 v[56:59], v[120:123], v[76:79], v[56:59]\nds_read_b128 v[64:67], v9 offset:8192\n" \
    "v_mfma_f32_16x16x32_bf16 v[60:63], v[120:123], v[84:87], v[60:63]\nds_read_b128 v[72:75], v9 offset:12288\n" \
    "v_mfma_f32_16x16x32_bf16 v[32:35], v[104:107], v[76:79], v[32:35]\nv_mfma_f32_16x16x32_bf16 v[36:39], v[104:107], v[84:87], v[36:39]\n" \
    "v_mfma_f32_16x16x32_bf16 v[40:43], v[68:71], v[76:79], v[40:43]\nv_mfma_f32_16x16x32_bf16 v[44:47], v[68:71], v[84:87], v[44:47]\ns_waitcnt lgkmcnt(0)\n" \
    "v_mfma_f32_16x16x32_bf16 v[48:51], v[140:143], v[64:67], v[48:51]\nv_mfma_f32_16x16x32_bf16 v[52:55], v[140:143], v[72:75], v[52:55]\n" \
    "v_mfma_f32_16x16x32_bf16 v[56:59], v[148:151], v[64:67], v[56:59]\nds_read_b128 v[68:71], v10 offset:8192\n" \
    "v_mfma_f32_16x16x32_bf16 v[60:63], v[148:151], v[72:75], v[60:63]\nds_read_b128 v[76:79], v10 offset:12288\n" \
    "v_mfma_f32_16x16x32_bf16 v[32:35], v[124:127], v[64:67], v[32:35]\nv_mfma_f32_16x16x32_bf16 v[36:39], v[124:127], v[72:75], v[36:39]\n" \
    "v_mfma_f32_16x16x32_bf16 v[40:43], v[132:135], v[64:67], v[40:43]\nv_mfma_f32_16x16x32_bf16 v[44:47], v[132:135], v[72:75], v[44:47]\ns_waitcnt lgkmcnt(0)\n" \
    "v_mfma_f32_16x16x32_bf16 v[48:51], v[144:147], v[68:71], v[48:51]\nbuffer_load_dword v13, off, s[64:67], s42 sc1\ns_and_b32 s69, s35, 15\n" \
    "v_mfma_f32_16x16x32_bf16 v[52:55], v[144:147], v[76:79], v[52:55]\ns_lshl_b32 s69, s69, 17\ns_add_u32 s81, s37, s69\n" \
    "v_mfma_f32_16x16x32_bf16 v[56:59], v[152:155], v[68:71], v[56:59]\nds_read_b128 v[64:67], v9 offset:9216\ns_lshr_b32 s69, s81, 8\n" \
    "v_mfma_f32_16x16x32_bf16 v[60:63], v[152:155], v[76:79], v[60:63]\nds_read_b128 v[72:75], v9 offset:13312\ns_and_b32 s69, s69, 0x2000\n" \
    "v_mfma_f32_16x16x32_bf16 v[32:35], v[128:131], v[68:71], v[32:35]\ns_xor_b32 s81, s81, s69\ns_mov_b32 s68, 0\n" \
    "v_mfma_f32_16x16x32_bf16 v[36:39], v[128:131], v[76:79], v[36:39]\nv_mfma_f32_16x16x32_bf16 v[40:43], v[136:139], v[68:71], v[40:43]\n" \
    "v_mfma_f32_16x16x32_bf16 v[44:47], v[136:139], v[76:79], v[44:47]\ns_waitcnt lgkmcnt(0)\nv_mfma_f32_16x16x32_bf16 v[48:51], v[172:175], v[64:67], v[48:51]\n" \
    "v_mfma_f32_16x16x32_bf16 v[52:55], v[172:175], v[72:75], v[52:55]\nv_mfma_f32_16x16x32_bf16 v[56:59], v[180:183], v[64:67], v[56:59]\n" \
    "ds_read_b128 v[68:71], v10 offset:9216\nv_mfma_f32_16x16x32_bf16 v[60:63], v[180:183], v[72:75], v[60:63]\nds_read_b128 v[76:79], v10 offset:13312\n" \
    "v_mfma_f32_16x16x32_bf16 v[32:35], v[156:159], v[64:67], v[32:35]\nv_mfma_f32_16x16x32_bf16 v[36:39], v[156:159], v[72:75], v[36:39]\n" \
    "v_mfma_f32_16x16x32_bf16 v[40:43], v[164:167], v[64:67], v[40:43]\nv_mfma_f32_16x16x32_bf16 v[44:47], v[164:167], v[72:75], v[44:47]\ns_waitcnt lgkmcnt(0)\n" \
    "v_mfma_f32_16x16x32_bf16 v[48:51], v[176:179], v[68:71], v[48:51]\nv_mfma_f32_16x16x32_bf16 v[52:55], v[176:179], v[76:79], v[52:55]\ns_waitcnt vmcnt(1)\ns_barrier\n" \
    "v_mfma_f32_16x16x32_bf16 v[56:59], v[184:187], v[68:71], v[56:59]\nds_read_b128 v[64:67], v9 offset:16384\n" \
    "v_mfma_f32_16x16x32_bf16 v[60:63], v[184:187], v[76:79], v[60:63]\nds_read_b128 v[72:75], v9 offset:20480\ns_add_u32 m0, s41, 0\n" \
    "v_mfma_f32_16x16x32_bf16 v[32:35], v[160:163], v[68:71], v[32:35]\ns_xor_b32 s85, s62, 0\ns_add_u32 s85, s85, s84\n" \
    "v_mfma_f32_16x16x32_bf16 v[36:39], v[160:163], v[76:79], v[36:39]\nbuffer_load_dwordx4 v11, s[64:67], s85 offen sc1 lds\n" \
    "v_mfma_f32_16x16x32_bf16 v[40:43], v[168:171], v[68:71], v[40:43]\nv_mfma_f32_16x16x32_bf16 v[44:47], v[168:171], v[76:79], v[44:47]\ns_waitcnt lgkmcnt(0)\n" \
    "v_mfma_f32_16x16x32_bf16 v[48:51], v[204:207], v[64:67], v[48:51]\nbuffer_load_dwordx4 v12, s[64:67], s85 offen offset:1024 sc1 lds\n" \
    "v_mfma_f32_16x16x32_bf16 v[52:55], v[204:207], v[72:75], v[52:55]\nv_mfma_f32_16x16x32_bf16 v[56:59], v[212:215], v[64:67], v[56:59]\n" \
    "ds_read_b128 v[68:71], v10 offset:16384\nv_mfma_f32_16x16x32_bf16 v[60:63], v[212:215], v[72:75], v[60:63]\nds_read_b128 v[76:79], v10 offset:20480\n" \
    "v_mfma_f32_16x16x32_bf16 v[32:35], v[188:191], v[64:67], v[32:35]\ns_waitcnt vmcnt(2)\nv_readfirstlane_b32 s74, v13\n" \
    "v_mfma_f32_16x16x32_bf16 v[36:39], v[188:191], v[72:75], v[36:39]\ns_and_b32 s75, s74, 127\ns_lshr_b32 s69, s74, 8\n" \
    "v_mfma_f32_16x16x32_bf16 v[40:43], v[196:199], v[64:67], v[40:43]\ns_sub_u32 s69, s69, s80\ns_bfe_i32 s69, s69, 0x180000\n" \
    "v_mfma_f32_16x16x32_bf16 v[44:47], v[196:199], v[72:75], v[44:47]\ns_cmp_eq_u32 s75, 0\ns_cselect_b32 s69, 0, s69\ns_waitcnt lgkmcnt(0)\n" \
    "v_mfma_f32_16x16x32_bf16 v[48:51], v[208:211], v[68:71], v[48:51]\ns_add_u32 s80, s80, s69\ns_lshl_b32 s74, s74, 5\n" \
    "v_mfma_f32_16x16x32_bf16 v[52:55], v[208:211], v[76:79], v[52:55]\ns_and_b32 s74, s74, 0x1000\ns_andn2_b32 s81, s81, 0x1000\n" \
    "v_mfma_f32_16x16x32_bf16 v[56:59], v[216:219], v[68:71], v[56:59]\nds_read_b128 v[64:67], v9 offset:17408\ns_or_b32 s81, s81, s74\n" \
    "v_mfma_f32_16x16x32_bf16 v[60:63], v[216:219], v[76:79], v[60:63]\nds_read_b128 v[72:75], v9 offset:21504\ns_lshl_b32 s74, s74, 2\n" \
    "v_mfma_f32_16x16x32_bf16 v[32:35], v[192:195], v[68:71], v[32:35]\ns_andn2_b32 s82, s82, 0x4000\ns_or_b32 s82, s82, s74\n" \
    "v_mfma_f32_16x16x32_bf16 v[36:39], v[192:195], v[76:79], v[36:39]\nv_mfma_f32_16x16x32_bf16 v[40:43], v[200:203], v[68:71], v[40:43]\n" \
    "ds_read_b128 v[68:71], v10 offset:17408\nv_mfma_f32_16x16x32_bf16 v[44:47], v[200:203], v[76:79], v[44:47]\nds_read_b128 v[76:79], v10 offset:21504\ns_waitcnt lgkmcnt(2)\n" \
    "v_mfma_f32_16x16x32_bf16 v[48:51], v[236:239], v[64:67], v[48:51]\nv_mfma_f32_16x16x32_bf16 v[52:55], v[236:239], v[72:75], v[52:55]\n" \
    "v_mfma_f32_16x16x32_bf16 v[56:59], v[244:247], v[64:67], v[56:59]\nv_mfma_f32_16x16x32_bf16 v[60:63], v[244:247], v[72:75], v[60:63]\ns_waitcnt lgkmcnt(0)\ns_barrier\n" \
    "v_mfma_f32_16x16x32_bf16 v[48:51], v[240:243], v[68:71], v[48:51]\ns_add_u32 m0, s41, 8192\ns_xor_b32 s85, s62, 256\n" \
    "v_mfma_f32_16x16x32_bf16 v[52:55], v[240:243], v[76:79], v[52:55]\ns_add_u32 s85, s85, s84\nbuffer_load_dwordx4 v11, s[64:67], s85 offen sc1 lds\n" \
    "v_mfma_f32_16x16x32_bf16 v[56:59], v[248:251], v[68:71], v[56:59]\nv_mfma_f32_16x16x32_bf16 v[60:63], v[248:251], v[76:79], v[60:63]\ns_nop 0\n" \
    "v_cvt_pk_bf16_f32 v16, v48, v49\nv_cvt_pk_bf16_f32 v17, v50, v51\nv_mfma_f32_16x16x32_bf16 v[32:35], v[220:223], v[64:67], v[32:35]\n" \
    "buffer_load_dwordx4 v12, s[64:67], s85 offen offset:1024 sc1 lds\ns_add_u32 m0, s41, 16384\nv_mfma_f32_16x16x32_bf16 v[36:39], v[220:223], v[72:75], v[36:39]\n" \
    "s_xor_b32 s85, s62, 512\ns_add_u32 s85, s85, s84\nv_cvt_pk_bf16_f32 v18, v56, v57\nv_mfma_f32_16x16x32_bf16 v[40:43], v[228:231], v[64:67], v[40:43]\n" \
    "buffer_load_dwordx4 v11, s[64:67], s85 offen sc1 lds\nv_cvt_pk_bf16_f32 v19, v58, v59\nv_mfma_f32_16x16x32_bf16 v[44:47], v[228:231], v[72:75], v[44:47]\n" \
    "ds_write_b128 v14, v[16:19] offset:0\nv_cvt_pk_bf16_f32 v20, v52, v53\nv_cvt_pk_bf16_f32 v21, v54, v55\nv_mfma_f32_16x16x32_bf16 v[32:35], v[224:227], v[68:71], v[32:35]\n" \
    "v_cvt_pk_bf16_f32 v22, v60, v61\nv_cvt_pk_bf16_f32 v23, v62, v63\nv_mfma_f32_16x16x32_bf16 v[36:39], v[224:227], v[76:79], v[36:39]\n" \
    "buffer_load_dwordx4 v12, s[64:67], s85 offen offset:1024 sc1 lds\nds_write_b128 v14, v[20:23] offset:4096\n" \
    "v_mfma_f32_16x16x32_bf16 v[40:43], v[232:235], v[68:71], v[40:43]\nv_mfma_f32_16x16x32_bf16 v[44:47], v[232:235], v[76:79], v[44:47]\ns_nop 0\n" \
    "v_cvt_pk_bf16_f32 v24, v32, v33\nv_cvt_pk_bf16_f32 v25, v34, v35\ns_nop 3\nv_cvt_pk_bf16_f32 v26, v40, v41\nv_cvt_pk_bf16_f32 v27, v42, v43\n" \
    "ds_write_b128 v14, v[24:27] offset:8192\nv_cvt_pk_bf16_f32 v28, v36, v37\nv_cvt_pk_bf16_f32 v29, v38, v39\nv_cvt_pk_bf16_f32 v30, v44, v45\n" \
    "v_cvt_pk_bf16_f32 v31, v46, v47\nds_write_b128 v14, v[28:31] offset:12288\ns_lshr_b32 s69, s35, 1\ns_cmp_eq_u32 s69, 5\ns_cbranch_scc1 .Lq10_%=\n.Lq11_%=:\n" \
    "s_cmp_lt_u32 s75, s35\ns_cbranch_scc1 .Lq12_%=\ns_cmp_eq_u32 s36, 1\ns_cbranch_scc1 .Lq5_%=\ns_cmp_eq_u32 s36, 2\ns_cbranch_scc1 .Lq6_%=\ns_cmp_eq_u32 s36, 3\n" \
    "s_cbranch_scc1 .Lq7_%=\n.Lq4_%=:\nds_read_b128 v[32:35], v3 offset:0\nds_read_b128 v[36:39], v3 offset:1024\nds_read_b128 v[40:43], v3 offset:8192\n" \
    "ds_read_b128 v[44:47], v3 offset:9216\nds_read_b128 v[48:51], v3 offset:16384\nds_read_b128 v[52:55], v3 offset:17408\nds_read_b128 v[56:59], v3 offset:2048\n" \
    "ds_read_b128 v[60:63], v3 offset:3072\nds_read_b128 v[64:67], v3 offset:4096\nds_read_b128 v[68:71], v3 offset:5120\n.Lq15_%=:\ns_memrealtime s[72:73]\n" \
    "s_waitcnt lgkmcnt(0)\ns_mov_b32 s68, 0\ns_sub_u32 s76, s80, s45\ns_sub_u32 s69, s72, s76\ns_cmp_lt_i32 s69, -8\ns_cbranch_scc1 .Lq18_%=\n.Lq16_%=:\n" \
    "s_xor_b32 s90, s81, 0x800\n.Lq20_%=:\ns_memrealtime s[72:73]\ns_waitcnt lgkmcnt(0)\ns_sub_u32 s72, s72, s80\ns_cmp_lt_i32 s72, 0\ns_cbranch_scc1 .Lq20_%=\n" \
    "buffer_load_dwordx4 v[72:75], v2, s[64:67], s81 offen offset:0 sc1\ns_nop 7\nbuffer_load_dwordx4 v[76:79], v2, s[64:67], s81 offen offset:1024 sc1\ns_nop 7\n" \
    "buffer_load_dwordx4 v[80:83], v2, s[64:67], s90 offen offset:0 sc1\ns_nop 7\nbuffer_load_dwordx4 v[84:87], v2, s[64:67], s90 offen offset:1024 sc1\nv_mov_b64 v[16:17], 0\n" \
    "v_mov_b64 v[18:19], 0\nv_mov_b64 v[20:21], 0\nv_mov_b64 v[22:23], 0\nv_mov_b64 v[24:25], 0\nv_mov_b64 v[26:27], 0\nv_mov_b64 v[28:29], 0\nv_mov_b64 v[30:31], 0\n" \
    "s_waitcnt vmcnt(3)\nv_smfmac_f32_16x16x64_bf16 v[20:23], v[72:75], v[32:39], v0\nds_write_b128 v1, v[72:75] offset:24576\nv_max3_u32 v252, v72, v73, v74\n" \
    "v_smfmac_f32_16x16x64_bf16 v[16:19], v[72:75], a[0:7], v0\nv_smfmac_f32_16x16x64_bf16 v[24:27], v[72:75], a[8:15], v0\n" \
    "v_smfmac_f32_16x16x64_bf16 v[28:31], v[72:75], a[16:23], v0\ns_waitcnt vmcnt(2)\nds_write_b128 v1, v[76:79] offset:28672\nv_max3_u32 v252, v252, v75, v76\n" \
    "v_smfmac_f32_16x16x64_bf16 v[20:23], v[76:79], v[40:47], v0\nv_max3_u32 v252, v252, v77, v78\nv_smfmac_f32_16x16x64_bf16 v[16:19], v[76:79], a[96:103], v0\n" \
    "v_cmp_u_f32 vcc, v252, v79\nv_smfmac_f32_16x16x64_bf16 v[24:27], v[76:79], a[104:111], v0\ns_cbranch_vccnz .Lq26_%=\n.Lq22_%=:\n" \
    "v_smfmac_f32_16x16x64_bf16 v[28:31], v[76:79], a[112:119], v0\nds_read_b128 v[36:39], v3 offset:7168\ns_waitcnt vmcnt(1)\nv_max3_u32 v252, v80, v81, v82\n" \
    "v_cmp_u_f32 vcc, v252, v83\ns_nop 0\ns_cbranch_vccnz .Lq27_%=\n.Lq23_%=:\nv_smfmac_f32_16x16x64_bf16 v[20:23], v[80:83], v[48:55], v0\n" \
    "ds_write_b128 v1, v[80:83] offset:32768\nv_smfmac_f32_16x16x64_bf16 v[16:19], v[80:83], a[192:199], v0\nds_read_b128 v[32:35], v3 offset:6144\ns_waitcnt lgkmcnt(3)\n" \
    "s_barrier\nds_read_b128 v[88:91], v1 offset:25600\nv_smfmac_f32_16x16x64_bf16 v[24:27], v[80:83], a[200:207], v0\nds_read_b128 v[40:43], v3 offset:10240\n" \
    "v_smfmac_f32_16x16x64_bf16 v[28:31], v[80:83], a[208:215], v0\nds_read_b128 v[48:51], v1 offset:26624\ns_waitcnt vmcnt(0)\nv_max3_u32 v252, v84, v85, v86\n" \
    "v_cmp_u_f32 vcc, v252, v87\ns_nop 0\ns_cbranch_vccnz .Lq28_%=\n.Lq24_%=:\nv_smfmac_f32_16x16x64_bf16 v[20:23], v[84:87], v[132:139], v0\n" \
    "ds_write_b128 v1, v[84:87] offset:36864\nv_smfmac_f32_16x16x64_bf16 v[16:19], v[84:87], v[124:131], v0\nds_read_b128 v[52:55], v1 offset:27648\n" \
    "v_smfmac_f32_16x16x64_bf16 v[24:27], v[84:87], v[140:147], v0\nds_read_b128 v[72:75], v1 offset:29696\nv_smfmac_f32_16x16x64_bf16 v[28:31], v[84:87], v[148:155], v0\n" \
    "ds_read_b128 v[44:47], v3 offset:11264\ns_waitcnt lgkmcnt(6)\nv_smfmac_f32_16x16x64_bf16 v[20:23], v[88:91], v[56:63], v0\nds_read_b128 v[56:59], v1 offset:30720\n" \
    "v_smfmac_f32_16x16x64_bf16 v[16:19], v[88:91], a[24:31], v0\nds_read_b128 v[76:79], v3 offset:12288\nv_smfmac_f32_16x16x64_bf16 v[24:27], v[88:91], a[32:39], v0\n" \
    "ds_read_b128 v[80:83], v3 offset:13312\nv_smfmac_f32_16x16x64_bf16 v[28:31], v[88:91], a[40:47], v0\nds_read_b128 v[60:63], v1 offset:31744\ns_waitcnt lgkmcnt(7)\n" \
    "s_barrier\nv_smfmac_f32_16x16x64_bf16 v[20:23], v[48:51], v[64:71], v0\nds_read_b128 v[64:67], v3 offset:14336\n" \
    "v_smfmac_f32_16x16x64_bf16 v[16:19], v[48:51], a[48:55], v0\nds_read_b128 v[68:71], v3 offset:15360\ns_waitcnt lgkmcnt(8)\n" \
    "v_smfmac_f32_16x16x64_bf16 v[20:23], v[52:55], v[32:39], v0\nds_read_b128 v[32:35], v1 offset:33792\nv_smfmac_f32_16x16x64_bf16 v[16:19], v[52:55], a[72:79], v0\n" \
    "ds_read_b128 v[84:87], v3 offset:18432\ns_waitcnt lgkmcnt(8)\nv_smfmac_f32_16x16x64_bf16 v[20:23], v[72:75], v[40:47], v0\nds_read_b128 v[88:91], v3 offset:19456\n" \
    "v_smfmac_f32_16x16x64_bf16 v[16:19], v[72:75], a[120:127], v0\nds_read_b128 v[36:39], v1 offset:34816\ns_waitcnt lgkmcnt(7)\n" \
    "v_smfmac_f32_16x16x64_bf16 v[20:23], v[56:59], v[76:83], v0\nds_read_b128 v[40:43], v3 offset:20480\nv_smfmac_f32_16x16x64_bf16 v[16:19], v[56:59], a[144:151], v0\n" \
    "ds_read_b128 v[44:47], v3 offset:21504\ns_waitcnt lgkmcnt(6)\nv_smfmac_f32_16x16x64_bf16 v[20:23], v[60:63], v[64:71], v0\nds_read_b128 v[64:67], v1 offset:35840\n" \
    "v_smfmac_f32_16x16x64_bf16 v[16:19], v[60:63], a[168:175], v0\nds_read_b128 v[76:79], v3 offset:22528\ns_waitcnt lgkmcnt(5)\n" \
    "v_smfmac_f32_16x16x64_bf16 v[20:23], v[32:35], v[84:91], v0\nds_read_b128 v[80:83], v3 offset:23552\nv_smfmac_f32_16x16x64_bf16 v[16:19], v[32:35], a[216:223], v0\n" \
    "ds_read_b128 v[68:71], v1 offset:37888\ns_waitcnt lgkmcnt(4)\nv_smfmac_f32_16x16x64_bf16 v[20:23], v[36:39], v[40:47], v0\nds_read_b128 v[40:43], v1 offset:38912\n" \
    "v_smfmac_f32_16x16x64_bf16 v[16:19], v[36:39], a[240:247], v0\nds_read_b128 v[44:47], v1 offset:39936\ns_waitcnt lgkmcnt(3)\n" \
    "v_smfmac_f32_16x16x64_bf16 v[20:23], v[64:67], v[76:83], v0\nv_smfmac_f32_16x16x64_bf16 v[16:19], v[64:67], v[100:107], v0\ns_waitcnt lgkmcnt(2)\n" \
    "v_smfmac_f32_16x16x64_bf16 v[20:23], v[68:71], v[164:171], v0\ns_waitcnt lgkmcnt(1)\nv_smfmac_f32_16x16x64_bf16 v[20:23], v[40:43], v[196:203], v0\ns_waitcnt lgkmcnt(0)\n" \
    "v_smfmac_f32_16x16x64_bf16 v[20:23], v[44:47], v[228:235], v0\ns_nop 2\nv_smfmac_f32_16x16x64_bf16 v[16:19], v[68:71], v[156:163], v0\ns_nop 2\n" \
    "v_smfmac_f32_16x16x64_bf16 v[16:19], v[40:43], v[188:195], v0\nv_permlane32_swap_b32 v20, v22\nv_smfmac_f32_16x16x64_bf16 v[16:19], v[44:47], v[220:227], v0\ns_nop 1\n" \
    "v_permlane32_swap_b32 v21, v23\nv_smfmac_f32_16x16x64_bf16 v[24:27], v[48:51], a[56:63], v0\ns_nop 1\nv_add_f32 v20, v20, v22\n" \
    "v_smfmac_f32_16x16x64_bf16 v[28:31], v[48:51], a[64:71], v0\nv_add_f32 v21, v21, v23\nv_smfmac_f32_16x16x64_bf16 v[24:27], v[52:55], a[80:87], v0\nv_mul_f32 v20, s32, v20\n" \
    "v_smfmac_f32_16x16x64_bf16 v[28:31], v[52:55], a[88:95], v0\nv_mul_f32 v21, s32, v21\nv_smfmac_f32_16x16x64_bf16 v[24:27], v[72:75], a[128:135], v0\n" \
    "v_cvt_pk_bf16_f32 v20, v20, v21\nv_smfmac_f32_16x16x64_bf16 v[28:31], v[72:75], a[136:143], v0\nds_write_b16 v4, v20 offset:8\nv_permlane32_swap_b32 v16, v18\n" \
    "v_smfmac_f32_16x16x64_bf16 v[24:27], v[56:59], a[152:159], v0\nds_write_b16_d16_hi v4, v20 offset:24\nv_permlane32_swap_b32 v17, v19\n" \
    "v_smfmac_f32_16x16x64_bf16 v[24:27], v[60:63], a[176:183], v0\nds_read_b128 v[48:51], v1 offset:40960\nv_add_f32 v16, v16, v18\n" \
    "v_smfmac_f32_16x16x64_bf16 v[24:27], v[32:35], a[224:231], v0\nds_read_b128 v[52:55], v1 offset:45056\nv_add_f32 v17, v17, v19\n" \
    "v_smfmac_f32_16x16x64_bf16 v[24:27], v[36:39], a[248:255], v0\nds_read_b128 v[72:75], v1 offset:41984\nv_mul_f32 v16, s32, v16\n" \
    "v_smfmac_f32_16x16x64_bf16 v[24:27], v[64:67], v[108:115], v0\nds_read_b128 v[76:79], v1 offset:46080\nv_mul_f32 v17, s32, v17\n" \
    "v_smfmac_f32_16x16x64_bf16 v[24:27], v[68:71], v[172:179], v0\nds_read_b128 v[80:83], v1 offset:43008\nv_cvt_pk_bf16_f32 v16, v16, v17\n" \
    "v_smfmac_f32_16x16x64_bf16 v[24:27], v[40:43], v[204:211], v0\nds_write_b16 v4, v16 offset:0\nv_smfmac_f32_16x16x64_bf16 v[24:27], v[44:47], v[236:243], v0\n" \
    "ds_write_b16_d16_hi v4, v16 offset:16\ns_nop 1\nv_smfmac_f32_16x16x64_bf16 v[28:31], v[56:59], a[160:167], v0\nds_read_b128 v[56:59], v1 offset:47104\ns_nop 1\n" \
    "v_smfmac_f32_16x16x64_bf16 v[28:31], v[60:63], a[184:191], v0\nds_read_b128 v[60:63], v1 offset:44032\nv_permlane32_swap_b32 v24, v26\n" \
    "v_smfmac_f32_16x16x64_bf16 v[28:31], v[32:35], a[232:239], v0\nds_read_b128 v[32:35], v1 offset:48128\nv_permlane32_swap_b32 v25, v27\n" \
    "v_smfmac_f32_16x16x64_bf16 v[28:31], v[36:39], v[92:99], v0\nds_read_b128 v[36:39], v6 offset:0\nv_add_f32 v24, v24, v26\n" \
    "v_smfmac_f32_16x16x64_bf16 v[28:31], v[64:67], v[116:123], v0\nds_read_b128 v[64:67], v6 offset:512\nv_add_f32 v25, v25, v27\n" \
    "v_smfmac_f32_16x16x64_bf16 v[28:31], v[68:71], v[180:187], v0\nds_read_b128 v[68:71], v6 offset:1024\nv_cvt_pk_bf16_f32 v24, v24, v25\n" \
    "v_smfmac_f32_16x16x64_bf16 v[28:31], v[40:43], v[212:219], v0\nds_read_b128 v[40:43], v6 offset:1536\nv_smfmac_f32_16x16x64_bf16 v[28:31], v[44:47], v[244:251], v0\n" \
    "ds_write_b16 v4, v24 offset:2048\nds_write_b16_d16_hi v4, v24 offset:2064\ns_waitcnt lgkmcnt(5)\nv_mfma_f32_16x16x32_bf16 v[16:19], v[48:51], v[36:39], 0\ns_nop 2\n" \
    "v_mfma_f32_16x16x32_bf16 v[20:23], v[52:55], v[36:39], 0\nv_permlane32_swap_b32 v28, v30\nv_permlane32_swap_b32 v29, v31\nv_add_f32 v28, v28, v30\nv_add_f32 v29, v29, v31\n" \
    "v_cvt_pk_bf16_f32 v28, v28, v29\nds_write_b16 v4, v28 offset:2056\nds_write_b16_d16_hi v4, v28 offset:2072\ns_waitcnt lgkmcnt(6)\n" \
    "v_mfma_f32_16x16x32_bf16 v[16:19], v[72:75], v[64:67], v[16:19]\nv_mfma_f32_16x16x32_bf16 v[20:23], v[76:79], v[64:67], v[20:23]\ns_waitcnt lgkmcnt(5)\n" \
    "v_mfma_f32_16x16x32_bf16 v[16:19], v[80:83], v[68:71], v[16:19]\nds_read_b128 v[44:47], v8 offset:0\nds_read_b128 v[84:87], v8 offset:512\n" \
    "v_mfma_f32_16x16x32_bf16 v[20:23], v[56:59], v[68:71], v[20:23]\nds_read_b128 v[88:91], v8 offset:1024\nds_read_b128 v[28:31], v8 offset:1536\ns_waitcnt lgkmcnt(8)\n" \
    "v_mfma_f32_16x16x32_bf16 v[16:19], v[60:63], v[40:43], v[16:19]\nv_mfma_f32_16x16x32_bf16 v[20:23], v[32:35], v[40:43], v[20:23]\nv_mov_b32 v15, v15\nv_mov_b32 v15, v15\n" \
    "s_waitcnt lgkmcnt(3)\nv_mfma_f32_16x16x32_bf16 v[24:27], v[44:47], v[36:39], 0\ns_nop 2\ns_waitcnt lgkmcnt(2)\n" \
    "v_mov_b32_dpp v16, v20 quad_perm:[0,1,2,3] row_mask:0xf bank_mask:0xc\nv_mov_b32_dpp v17, v21 quad_perm:[0,1,2,3] row_mask:0xf bank_mask:0xc\n" \
    "v_mfma_f32_16x16x32_bf16 v[24:27], v[84:87], v[64:67], v[24:27]\ns_waitcnt lgkmcnt(1)\nv_mov_b32_dpp v18, v22 quad_perm:[0,1,2,3] row_mask:0xf bank_mask:0xc\n" \
    "v_mov_b32_dpp v19, v23 quad_perm:[0,1,2,3] row_mask:0xf bank_mask:0xc\nv_mfma_f32_16x16x32_bf16 v[24:27], v[88:91], v[68:71], v[24:27]\ns_waitcnt lgkmcnt(0)\n" \
    "v_max3_f32 v254, v16, v17, v18\nv_max_f32 v254, v254, v19\nv_mfma_f32_16x16x32_bf16 v[24:27], v[28:31], v[40:43], v[24:27]\ns_nop 0\n" \
    "v_max_f32_dpp v252, v254, v254 row_ror:8 row_mask:0xf bank_mask:0xf\nv_max_f32_dpp v253, v254, v254 row_ror:8 row_mask:0xf bank_mask:0xf\ns_nop 1\n" \
    "v_permlane32_swap_b32 v252, v253\nv_max_f32 v252, v252, v253\ns_xor_b32 s74, s82, 0x4000\nv_max3_f32 v254, v252, v24, v25\nv_max3_f32 v252, v254, v26, v27\n" \
    "v_max3_f32 v253, v254, v26, v27\ns_nop 1\nv_permlane16_swap_b32 v252, v253\nv_max_f32 v252, v252, v253\n" \
    "v_pk_add_f32 v[16:17], v[16:17], v[252:253] op_sel_hi:[1,0] neg_lo:[0,1] neg_hi:[0,1]\n" \
    "v_pk_add_f32 v[18:19], v[18:19], v[252:253] op_sel_hi:[1,0] neg_lo:[0,1] neg_hi:[0,1]\n" \
    "v_pk_add_f32 v[24:25], v[24:25], v[252:253] op_sel_hi:[1,0] neg_lo:[0,1] neg_hi:[0,1]\n" \
    "v_pk_add_f32 v[26:27], v[26:27], v[252:253] op_sel_hi:[1,0] neg_lo:[0,1] neg_hi:[0,1]\nv_exp_f32 v16, v16\nv_exp_f32 v17, v17\nv_exp_f32 v18, v18\nv_exp_f32 v19, v19\n" \
    "v_exp_f32 v24, v24\nv_pk_add_f32 v[254:255], v[16:17], v[18:19]\nv_exp_f32 v25, v25\nv_add_f32 v254, v254, v255\nv_exp_f32 v26, v26\nv_exp_f32 v27, v27\n" \
    "v_add_f32_dpp v252, v254, v254 row_ror:8 row_mask:0xf bank_mask:0xf\nv_add_f32_dpp v253, v254, v254 row_ror:8 row_mask:0xf bank_mask:0xf\n" \
    "v_pk_add_f32 v[254:255], v[24:25], v[26:27]\ns_nop 0\nv_permlane32_swap_b32 v252, v253\nv_pk_add_f32 v[254:255], v[254:255], v[252:253]\n" \
    "v_pk_add_f32 v[252:253], v[254:255], v[254:255] op_sel:[0,1] op_sel_hi:[1,0]\ns_nop 1\nv_permlane16_swap_b32 v252, v253\nv_add_f32 v252, v252, v253\n" \
    "v_cmp_u_f32 vcc, v252, v252\nv_rcp_f32 v252, v252\ns_cbranch_vccnz .Lq14_%=\nv_pk_mul_f32 v[16:17], v[16:17], v[252:253] op_sel_hi:[1,0]\n" \
    "v_pk_mul_f32 v[24:25], v[24:25], v[252:253] op_sel_hi:[1,0]\nv_cvt_pk_bf16_f32 v20, v16, v17\nv_cvt_pk_bf16_f32 v22, v24, v25\n" \
    "v_pk_mul_f32 v[18:19], v[18:19], v[252:253] op_sel_hi:[1,0]\nv_pk_mul_f32 v[26:27], v[26:27], v[252:253] op_sel_hi:[1,0]\nv_cvt_pk_bf16_f32 v21, v18, v19\n" \
    "v_cvt_pk_bf16_f32 v23, v26, v27\nv_permlane32_swap_b32 v20, v22\ns_nop 0\nv_permlane32_swap_b32 v21, v23\nbuffer_store_dwordx4 v[20:23], v7, s[64:67], s82 offen sc1\n" \
    "buffer_store_dwordx4 v[20:23], v7, s[64:67], s74 offen sc1\ns_branch .Lq3_%=\n.Lq5_%=:\nds_read_b128 v[32:35], v3 offset:2048\nds_read_b128 v[36:39], v3 offset:3072\n" \
    "ds_read_b128 v[40:43], v3 offset:10240\nds_read_b128 v[44:47], v3 offset:11264\nds_read_b128 v[48:51], v3 offset:18432\nds_read_b128 v[52:55], v3 offset:19456\n" \
    "ds_read_b128 v[56:59], v3 offset:0\nds_read_b128 v[60:63], v3 offset:1024\nds_read_b128 v[64:67], v3 offset:4096\nds_read_b128 v[68:71], v3 offset:5120\n.Lq34_%=:\n" \
    "s_memrealtime s[72:73]\ns_waitcnt lgkmcnt(0)\ns_mov_b32 s68, 0\ns_sub_u32 s76, s80, s45\ns_sub_u32 s69, s72, s76\ns_cmp_lt_i32 s69, -8\ns_cbranch_scc1 .Lq37_%=\n.Lq35_%=:\n" \
    "s_xor_b32 s90, s81, 0x800\n.Lq39_%=:\ns_memrealtime s[72:73]\ns_waitcnt lgkmcnt(0)\ns_sub_u32 s72, s72, s80\ns_cmp_lt_i32 s72, 0\ns_cbranch_scc1 .Lq39_%=\n" \
    "buffer_load_dwordx4 v[72:75], v2, s[64:67], s81 offen offset:0 sc1\ns_nop 7\nbuffer_load_dwordx4 v[76:79], v2, s[64:67], s81 offen offset:1024 sc1\ns_nop 7\n" \
    "buffer_load_dwordx4 v[80:83], v2, s[64:67], s90 offen offset:0 sc1\ns_nop 7\nbuffer_load_dwordx4 v[84:87], v2, s[64:67], s90 offen offset:1024 sc1\nv_mov_b64 v[16:17], 0\n" \
    "v_mov_b64 v[18:19], 0\nv_mov_b64 v[20:21], 0\nv_mov_b64 v[22:23], 0\nv_mov_b64 v[24:25], 0\nv_mov_b64 v[26:27], 0\nv_mov_b64 v[28:29], 0\nv_mov_b64 v[30:31], 0\n" \
    "s_waitcnt vmcnt(3)\nv_smfmac_f32_16x16x64_bf16 v[20:23], v[72:75], v[32:39], v0\nds_write_b128 v1, v[72:75] offset:25600\nv_max3_u32 v252, v72, v73, v74\n" \
    "v_smfmac_f32_16x16x64_bf16 v[16:19], v[72:75], a[24:31], v0\nv_smfmac_f32_16x16x64_bf16 v[24:27], v[72:75], a[32:39], v0\n" \
    "v_smfmac_f32_16x16x64_bf16 v[28:31], v[72:75], a[40:47], v0\ns_waitcnt vmcnt(2)\nds_write_b128 v1, v[76:79] offset:29696\nv_max3_u32 v252, v252, v75, v76\n" \
    "v_smfmac_f32_16x16x64_bf16 v[20:23], v[76:79], v[40:47], v0\nv_max3_u32 v252, v252, v77, v78\nv_smfmac_f32_16x16x64_bf16 v[16:19], v[76:79], a[120:127], v0\n" \
    "v_cmp_u_f32 vcc, v252, v79\nv_smfmac_f32_16x16x64_bf16 v[24:27], v[76:79], a[128:135], v0\ns_cbranch_vccnz .Lq45_%=\n.Lq41_%=:\n" \
    "v_smfmac_f32_16x16x64_bf16 v[28:31], v[76:79], a[136:143], v0\nds_read_b128 v[36:39], v3 offset:7168\ns_waitcnt vmcnt(1)\nv_max3_u32 v252, v80, v81, v82\n" \
    "v_cmp_u_f32 vcc, v252, v83\ns_nop 0\ns_cbranch_vccnz .Lq46_%=\n.Lq42_%=:\nv_smfmac_f32_16x16x64_bf16 v[20:23], v[80:83], v[48:55], v0\n" \
    "ds_write_b128 v1, v[80:83] offset:33792\nv_smfmac_f32_16x16x64_bf16 v[16:19], v[80:83], a[216:223], v0\nds_read_b128 v[32:35], v3 offset:6144\ns_waitcnt lgkmcnt(3)\n" \
    "s_barrier\nds_read_b128 v[88:91], v1 offset:24576\nv_smfmac_f32_16x16x64_bf16 v[24:27], v[80:83], a[224:231], v0\nds_read_b128 v[40:43], v3 offset:8192\n" \
    "v_smfmac_f32_16x16x64_bf16 v[28:31], v[80:83], a[232:239], v0\nds_read_b128 v[48:51], v1 offset:26624\ns_waitcnt vmcnt(0)\nv_max3_u32 v252, v84, v85, v86\n" \
    "v_cmp_u_f32 vcc, v252, v87\ns_nop 0\ns_cbranch_vccnz .Lq47_%=\n.Lq43_%=:\nv_smfmac_f32_16x16x64_bf16 v[20:23], v[84:87], v[164:171], v0\n" \
    "ds_write_b128 v1, v[84:87] offset:37888\nv_smfmac_f32_16x16x64_bf16 v[16:19], v[84:87], v[156:163], v0\nds_read_b128 v[52:55], v1 offset:27648\n" \
    "v_smfmac_f32_16x16x64_bf16 v[24:27], v[84:87], v[172:179], v0\nds_read_b128 v[72:75], v1 offset:28672\nv_smfmac_f32_16x16x64_bf16 v[28:31], v[84:87], v[180:187], v0\n" \
    "ds_read_b128 v[44:47], v3 offset:9216\ns_waitcnt lgkmcnt(6)\nv_smfmac_f32_16x16x64_bf16 v[20:23], v[88:91], v[56:63], v0\nds_read_b128 v[56:59], v1 offset:30720\n" \
    "v_smfmac_f32_16x16x64_bf16 v[16:19], v[88:91], a[0:7], v0\nds_read_b128 v[76:79], v3 offset:12288\nv_smfmac_f32_16x16x64_bf16 v[24:27], v[88:91], a[8:15], v0\n" \
    "ds_read_b128 v[80:83], v3 offset:13312\nv_smfmac_f32_16x16x64_bf16 v[28:31], v[88:91], a[16:23], v0\nds_read_b128 v[60:63], v1 offset:31744\ns_waitcnt lgkmcnt(7)\n" \
    "s_barrier\nv_smfmac_f32_16x16x64_bf16 v[20:23], v[48:51], v[64:71], v0\nds_read_b128 v[64:67], v3 offset:14336\n" \
    "v_smfmac_f32_16x16x64_bf16 v[16:19], v[48:51], a[48:55], v0\nds_read_b128 v[68:71], v3 offset:15360\ns_waitcnt lgkmcnt(8)\n" \
    "v_smfmac_f32_16x16x64_bf16 v[20:23], v[52:55], v[32:39], v0\nds_read_b128 v[32:35], v1 offset:32768\nv_smfmac_f32_16x16x64_bf16 v[16:19], v[52:55], a[72:79], v0\n" \
    "ds_read_b128 v[84:87], v3 offset:16384\ns_waitcnt lgkmcnt(8)\nv_smfmac_f32_16x16x64_bf16 v[20:23], v[72:75], v[40:47], v0\nds_read_b128 v[88:91], v3 offset:17408\n" \
    "v_smfmac_f32_16x16x64_bf16 v[16:19], v[72:75], a[96:103], v0\nds_read_b128 v[36:39], v1 offset:34816\ns_waitcnt lgkmcnt(7)\n" \
    "v_smfmac_f32_16x16x64_bf16 v[20:23], v[56:59], v[76:83], v0\nds_read_b128 v[40:43], v3 offset:20480\nv_smfmac_f32_16x16x64_bf16 v[16:19], v[56:59], a[144:151], v0\n" \
    "ds_read_b128 v[44:47], v3 offset:21504\ns_waitcnt lgkmcnt(6)\nv_smfmac_f32_16x16x64_bf16 v[20:23], v[60:63], v[64:71], v0\nds_read_b128 v[64:67], v1 offset:35840\n" \
    "v_smfmac_f32_16x16x64_bf16 v[16:19], v[60:63], a[168:175], v0\nds_read_b128 v[76:79], v3 offset:22528\ns_waitcnt lgkmcnt(5)\n" \
    "v_smfmac_f32_16x16x64_bf16 v[20:23], v[32:35], v[84:91], v0\nds_read_b128 v[80:83], v3 offset:23552\nv_smfmac_f32_16x16x64_bf16 v[16:19], v[32:35], a[192:199], v0\n" \
    "ds_read_b128 v[68:71], v1 offset:36864\ns_waitcnt lgkmcnt(4)\nv_smfmac_f32_16x16x64_bf16 v[20:23], v[36:39], v[40:47], v0\nds_read_b128 v[40:43], v1 offset:38912\n" \
    "v_smfmac_f32_16x16x64_bf16 v[16:19], v[36:39], a[240:247], v0\nds_read_b128 v[44:47], v1 offset:39936\ns_waitcnt lgkmcnt(3)\n" \
    "v_smfmac_f32_16x16x64_bf16 v[20:23], v[64:67], v[76:83], v0\nv_smfmac_f32_16x16x64_bf16 v[16:19], v[64:67], v[100:107], v0\ns_waitcnt lgkmcnt(2)\n" \
    "v_smfmac_f32_16x16x64_bf16 v[20:23], v[68:71], v[132:139], v0\ns_waitcnt lgkmcnt(1)\nv_smfmac_f32_16x16x64_bf16 v[20:23], v[40:43], v[196:203], v0\ns_waitcnt lgkmcnt(0)\n" \
    "v_smfmac_f32_16x16x64_bf16 v[20:23], v[44:47], v[228:235], v0\ns_nop 2\nv_smfmac_f32_16x16x64_bf16 v[16:19], v[68:71], v[124:131], v0\ns_nop 2\n" \
    "v_smfmac_f32_16x16x64_bf16 v[16:19], v[40:43], v[188:195], v0\nv_permlane32_swap_b32 v20, v22\nv_smfmac_f32_16x16x64_bf16 v[16:19], v[44:47], v[220:227], v0\ns_nop 1\n" \
    "v_permlane32_swap_b32 v21, v23\nv_smfmac_f32_16x16x64_bf16 v[24:27], v[48:51], a[56:63], v0\ns_nop 1\nv_add_f32 v20, v20, v22\n" \
    "v_smfmac_f32_16x16x64_bf16 v[28:31], v[48:51], a[64:71], v0\nv_add_f32 v21, v21, v23\nv_smfmac_f32_16x16x64_bf16 v[24:27], v[52:55], a[80:87], v0\nv_mul_f32 v20, s32, v20\n" \
    "v_smfmac_f32_16x16x64_bf16 v[28:31], v[52:55], a[88:95], v0\nv_mul_f32 v21, s32, v21\nv_smfmac_f32_16x16x64_bf16 v[24:27], v[72:75], a[104:111], v0\n" \
    "v_cvt_pk_bf16_f32 v20, v20, v21\nv_smfmac_f32_16x16x64_bf16 v[28:31], v[72:75], a[112:119], v0\nds_write_b16 v4, v20 offset:8\nv_permlane32_swap_b32 v16, v18\n" \
    "v_smfmac_f32_16x16x64_bf16 v[24:27], v[56:59], a[152:159], v0\nds_write_b16_d16_hi v4, v20 offset:24\nv_permlane32_swap_b32 v17, v19\n" \
    "v_smfmac_f32_16x16x64_bf16 v[24:27], v[60:63], a[176:183], v0\nds_read_b128 v[48:51], v1 offset:40960\nv_add_f32 v16, v16, v18\n" \
    "v_smfmac_f32_16x16x64_bf16 v[24:27], v[32:35], a[200:207], v0\nds_read_b128 v[52:55], v1 offset:45056\nv_add_f32 v17, v17, v19\n" \
    "v_smfmac_f32_16x16x64_bf16 v[24:27], v[36:39], a[248:255], v0\nds_read_b128 v[72:75], v1 offset:41984\nv_mul_f32 v16, s32, v16\n" \
    "v_smfmac_f32_16x16x64_bf16 v[24:27], v[64:67], v[108:115], v0\nds_read_b128 v[76:79], v1 offset:46080\nv_mul_f32 v17, s32, v17\n" \
    "v_smfmac_f32_16x16x64_bf16 v[24:27], v[68:71], v[140:147], v0\nds_read_b128 v[80:83], v1 offset:43008\nv_cvt_pk_bf16_f32 v16, v16, v17\n" \
    "v_smfmac_f32_16x16x64_bf16 v[24:27], v[40:43], v[204:211], v0\nds_write_b16 v4, v16 offset:0\nv_smfmac_f32_16x16x64_bf16 v[24:27], v[44:47], v[236:243], v0\n" \
    "ds_write_b16_d16_hi v4, v16 offset:16\ns_nop 1\nv_smfmac_f32_16x16x64_bf16 v[28:31], v[56:59], a[160:167], v0\nds_read_b128 v[56:59], v1 offset:47104\ns_nop 1\n" \
    "v_smfmac_f32_16x16x64_bf16 v[28:31], v[60:63], a[184:191], v0\nds_read_b128 v[60:63], v1 offset:44032\nv_permlane32_swap_b32 v24, v26\n" \
    "v_smfmac_f32_16x16x64_bf16 v[28:31], v[32:35], a[208:215], v0\nds_read_b128 v[32:35], v1 offset:48128\nv_permlane32_swap_b32 v25, v27\n" \
    "v_smfmac_f32_16x16x64_bf16 v[28:31], v[36:39], v[92:99], v0\nds_read_b128 v[36:39], v6 offset:0\nv_add_f32 v24, v24, v26\n" \
    "v_smfmac_f32_16x16x64_bf16 v[28:31], v[64:67], v[116:123], v0\nds_read_b128 v[64:67], v6 offset:512\nv_add_f32 v25, v25, v27\n" \
    "v_smfmac_f32_16x16x64_bf16 v[28:31], v[68:71], v[148:155], v0\nds_read_b128 v[68:71], v6 offset:1024\nv_cvt_pk_bf16_f32 v24, v24, v25\n" \
    "v_smfmac_f32_16x16x64_bf16 v[28:31], v[40:43], v[212:219], v0\nds_read_b128 v[40:43], v6 offset:1536\nv_smfmac_f32_16x16x64_bf16 v[28:31], v[44:47], v[244:251], v0\n" \
    "ds_write_b16 v4, v24 offset:2048\nds_write_b16_d16_hi v4, v24 offset:2064\ns_waitcnt lgkmcnt(5)\nv_mfma_f32_16x16x32_bf16 v[16:19], v[48:51], v[36:39], 0\ns_nop 2\n" \
    "v_mfma_f32_16x16x32_bf16 v[20:23], v[52:55], v[36:39], 0\nv_permlane32_swap_b32 v28, v30\nv_permlane32_swap_b32 v29, v31\nv_add_f32 v28, v28, v30\nv_add_f32 v29, v29, v31\n" \
    "v_cvt_pk_bf16_f32 v28, v28, v29\nds_write_b16 v4, v28 offset:2056\nds_write_b16_d16_hi v4, v28 offset:2072\ns_waitcnt lgkmcnt(6)\n" \
    "v_mfma_f32_16x16x32_bf16 v[16:19], v[72:75], v[64:67], v[16:19]\nv_mfma_f32_16x16x32_bf16 v[20:23], v[76:79], v[64:67], v[20:23]\ns_waitcnt lgkmcnt(5)\n" \
    "v_mfma_f32_16x16x32_bf16 v[16:19], v[80:83], v[68:71], v[16:19]\nds_read_b128 v[44:47], v8 offset:0\nds_read_b128 v[84:87], v8 offset:512\n" \
    "v_mfma_f32_16x16x32_bf16 v[20:23], v[56:59], v[68:71], v[20:23]\nds_read_b128 v[88:91], v8 offset:1024\nds_read_b128 v[28:31], v8 offset:1536\ns_waitcnt lgkmcnt(8)\n" \
    "v_mfma_f32_16x16x32_bf16 v[16:19], v[60:63], v[40:43], v[16:19]\nv_mfma_f32_16x16x32_bf16 v[20:23], v[32:35], v[40:43], v[20:23]\nv_mov_b32 v15, v15\nv_mov_b32 v15, v15\n" \
    "s_waitcnt lgkmcnt(3)\nv_mfma_f32_16x16x32_bf16 v[24:27], v[44:47], v[36:39], 0\ns_nop 2\ns_waitcnt lgkmcnt(2)\n" \
    "v_mov_b32_dpp v16, v20 quad_perm:[0,1,2,3] row_mask:0xf bank_mask:0xc\nv_mov_b32_dpp v17, v21 quad_perm:[0,1,2,3] row_mask:0xf bank_mask:0xc\n" \
    "v_mfma_f32_16x16x32_bf16 v[24:27], v[84:87], v[64:67], v[24:27]\ns_waitcnt lgkmcnt(1)\nv_mov_b32_dpp v18, v22 quad_perm:[0,1,2,3] row_mask:0xf bank_mask:0xc\n" \
    "v_mov_b32_dpp v19, v23 quad_perm:[0,1,2,3] row_mask:0xf bank_mask:0xc\nv_mfma_f32_16x16x32_bf16 v[24:27], v[88:91], v[68:71], v[24:27]\ns_waitcnt lgkmcnt(0)\n" \
    "v_max3_f32 v254, v16, v17, v18\nv_max_f32 v254, v254, v19\nv_mfma_f32_16x16x32_bf16 v[24:27], v[28:31], v[40:43], v[24:27]\ns_nop 0\n" \
    "v_max_f32_dpp v252, v254, v254 row_ror:8 row_mask:0xf bank_mask:0xf\nv_max_f32_dpp v253, v254, v254 row_ror:8 row_mask:0xf bank_mask:0xf\ns_nop 1\n" \
    "v_permlane32_swap_b32 v252, v253\nv_max_f32 v252, v252, v253\ns_xor_b32 s74, s82, 0x4000\nv_max3_f32 v254, v252, v24, v25\nv_max3_f32 v252, v254, v26, v27\n" \
    "v_max3_f32 v253, v254, v26, v27\ns_nop 1\nv_permlane16_swap_b32 v252, v253\nv_max_f32 v252, v252, v253\n" \
    "v_pk_add_f32 v[16:17], v[16:17], v[252:253] op_sel_hi:[1,0] neg_lo:[0,1] neg_hi:[0,1]\n" \
    "v_pk_add_f32 v[18:19], v[18:19], v[252:253] op_sel_hi:[1,0] neg_lo:[0,1] neg_hi:[0,1]\n" \
    "v_pk_add_f32 v[24:25], v[24:25], v[252:253] op_sel_hi:[1,0] neg_lo:[0,1] neg_hi:[0,1]\n" \
    "v_pk_add_f32 v[26:27], v[26:27], v[252:253] op_sel_hi:[1,0] neg_lo:[0,1] neg_hi:[0,1]\nv_exp_f32 v16, v16\nv_exp_f32 v17, v17\nv_exp_f32 v18, v18\nv_exp_f32 v19, v19\n" \
    "v_exp_f32 v24, v24\nv_pk_add_f32 v[254:255], v[16:17], v[18:19]\nv_exp_f32 v25, v25\nv_add_f32 v254, v254, v255\nv_exp_f32 v26, v26\nv_exp_f32 v27, v27\n" \
    "v_add_f32_dpp v252, v254, v254 row_ror:8 row_mask:0xf bank_mask:0xf\nv_add_f32_dpp v253, v254, v254 row_ror:8 row_mask:0xf bank_mask:0xf\n" \
    "v_pk_add_f32 v[254:255], v[24:25], v[26:27]\ns_nop 0\nv_permlane32_swap_b32 v252, v253\nv_pk_add_f32 v[254:255], v[254:255], v[252:253]\n" \
    "v_pk_add_f32 v[252:253], v[254:255], v[254:255] op_sel:[0,1] op_sel_hi:[1,0]\ns_nop 1\nv_permlane16_swap_b32 v252, v253\nv_add_f32 v252, v252, v253\n" \
    "v_cmp_u_f32 vcc, v252, v252\nv_rcp_f32 v252, v252\ns_cbranch_vccnz .Lq14_%=\nv_pk_mul_f32 v[16:17], v[16:17], v[252:253] op_sel_hi:[1,0]\n" \
    "v_pk_mul_f32 v[24:25], v[24:25], v[252:253] op_sel_hi:[1,0]\nv_cvt_pk_bf16_f32 v20, v16, v17\nv_cvt_pk_bf16_f32 v22, v24, v25\n" \
    "v_pk_mul_f32 v[18:19], v[18:19], v[252:253] op_sel_hi:[1,0]\nv_pk_mul_f32 v[26:27], v[26:27], v[252:253] op_sel_hi:[1,0]\nv_cvt_pk_bf16_f32 v21, v18, v19\n" \
    "v_cvt_pk_bf16_f32 v23, v26, v27\nv_permlane32_swap_b32 v20, v22\ns_nop 0\nv_permlane32_swap_b32 v21, v23\nbuffer_store_dwordx4 v[20:23], v7, s[64:67], s82 offen sc1\n" \
    "buffer_store_dwordx4 v[20:23], v7, s[64:67], s74 offen sc1\ns_branch .Lq3_%=\n.Lq6_%=:\nds_read_b128 v[32:35], v3 offset:4096\nds_read_b128 v[36:39], v3 offset:5120\n" \
    "ds_read_b128 v[40:43], v3 offset:12288\nds_read_b128 v[44:47], v3 offset:13312\nds_read_b128 v[48:51], v3 offset:20480\nds_read_b128 v[52:55], v3 offset:21504\n" \
    "ds_read_b128 v[56:59], v3 offset:0\nds_read_b128 v[60:63], v3 offset:1024\nds_read_b128 v[64:67], v3 offset:2048\nds_read_b128 v[68:71], v3 offset:3072\n.Lq53_%=:\n" \
    "s_memrealtime s[72:73]\ns_waitcnt lgkmcnt(0)\ns_mov_b32 s68, 0\ns_sub_u32 s76, s80, s45\ns_sub_u32 s69, s72, s76\ns_cmp_lt_i32 s69, -8\ns_cbranch_scc1 .Lq56_%=\n.Lq54_%=:\n" \
    "s_xor_b32 s90, s81, 0x800\n.Lq58_%=:\ns_memrealtime s[72:73]\ns_waitcnt lgkmcnt(0)\ns_sub_u32 s72, s72, s80\ns_cmp_lt_i32 s72, 0\ns_cbranch_scc1 .Lq58_%=\n" \
    "buffer_load_dwordx4 v[72:75], v2, s[64:67], s81 offen offset:0 sc1\ns_nop 7\nbuffer_load_dwordx4 v[76:79], v2, s[64:67], s81 offen offset:1024 sc1\ns_nop 7\n" \
    "buffer_load_dwordx4 v[80:83], v2, s[64:67], s90 offen offset:0 sc1\ns_nop 7\nbuffer_load_dwordx4 v[84:87], v2, s[64:67], s90 offen offset:1024 sc1\nv_mov_b64 v[16:17], 0\n" \
    "v_mov_b64 v[18:19], 0\nv_mov_b64 v[20:21], 0\nv_mov_b64 v[22:23], 0\nv_mov_b64 v[24:25], 0\nv_mov_b64 v[26:27], 0\nv_mov_b64 v[28:29], 0\nv_mov_b64 v[30:31], 0\n" \
    "s_waitcnt vmcnt(3)\nv_smfmac_f32_16x16x64_bf16 v[20:23], v[72:75], v[32:39], v0\nds_write_b128 v1, v[72:75] offset:26624\nv_max3_u32 v252, v72, v73, v74\n" \
    "v_smfmac_f32_16x16x64_bf16 v[16:19], v[72:75], a[48:55], v0\nv_smfmac_f32_16x16x64_bf16 v[24:27], v[72:75], a[56:63], v0\n" \
    "v_smfmac_f32_16x16x64_bf16 v[28:31], v[72:75], a[64:71], v0\ns_waitcnt vmcnt(2)\nds_write_b128 v1, v[76:79] offset:30720\nv_max3_u32 v252, v252, v75, v76\n" \
    "v_smfmac_f32_16x16x64_bf16 v[20:23], v[76:79], v[40:47], v0\nv_max3_u32 v252, v252, v77, v78\nv_smfmac_f32_16x16x64_bf16 v[16:19], v[76:79], a[144:151], v0\n" \
    "v_cmp_u_f32 vcc, v252, v79\nv_smfmac_f32_16x16x64_bf16 v[24:27], v[76:79], a[152:159], v0\ns_cbranch_vccnz .Lq64_%=\n.Lq60_%=:\n" \
    "v_smfmac_f32_16x16x64_bf16 v[28:31], v[76:79], a[160:167], v0\nds_read_b128 v[36:39], v3 offset:7168\ns_waitcnt vmcnt(1)\nv_max3_u32 v252, v80, v81, v82\n" \
    "v_cmp_u_f32 vcc, v252, v83\ns_nop 0\ns_cbranch_vccnz .Lq65_%=\n.Lq61_%=:\nv_smfmac_f32_16x16x64_bf16 v[20:23], v[80:83], v[48:55], v0\n" \
    "ds_write_b128 v1, v[80:83] offset:34816\nv_smfmac_f32_16x16x64_bf16 v[16:19], v[80:83], a[240:247], v0\nds_read_b128 v[32:35], v3 offset:6144\ns_waitcnt lgkmcnt(3)\n" \
    "s_barrier\nds_read_b128 v[88:91], v1 offset:24576\nv_smfmac_f32_16x16x64_bf16 v[24:27], v[80:83], a[248:255], v0\nds_read_b128 v[40:43], v3 offset:8192\n" \
    "v_smfmac_f32_16x16x64_bf16 v[28:31], v[80:83], v[92:99], v0\nds_read_b128 v[48:51], v1 offset:25600\ns_waitcnt vmcnt(0)\nv_max3_u32 v252, v84, v85, v86\n" \
    "v_cmp_u_f32 vcc, v252, v87\ns_nop 0\ns_cbranch_vccnz .Lq66_%=\n.Lq62_%=:\nv_smfmac_f32_16x16x64_bf16 v[20:23], v[84:87], v[196:203], v0\n" \
    "ds_write_b128 v1, v[84:87] offset:38912\nv_smfmac_f32_16x16x64_bf16 v[16:19], v[84:87], v[188:195], v0\nds_read_b128 v[52:55], v1 offset:27648\n" \
    "v_smfmac_f32_16x16x64_bf16 v[24:27], v[84:87], v[204:211], v0\nds_read_b128 v[72:75], v1 offset:28672\nv_smfmac_f32_16x16x64_bf16 v[28:31], v[84:87], v[212:219], v0\n" \
    "ds_read_b128 v[44:47], v3 offset:9216\ns_waitcnt lgkmcnt(6)\nv_smfmac_f32_16x16x64_bf16 v[20:23], v[88:91], v[56:63], v0\nds_read_b128 v[56:59], v1 offset:29696\n" \
    "v_smfmac_f32_16x16x64_bf16 v[16:19], v[88:91], a[0:7], v0\nds_read_b128 v[76:79], v3 offset:10240\nv_smfmac_f32_16x16x64_bf16 v[24:27], v[88:91], a[8:15], v0\n" \
    "ds_read_b128 v[80:83], v3 offset:11264\nv_smfmac_f32_16x16x64_bf16 v[28:31], v[88:91], a[16:23], v0\nds_read_b128 v[60:63], v1 offset:31744\ns_waitcnt lgkmcnt(7)\n" \
    "s_barrier\nv_smfmac_f32_16x16x64_bf16 v[20:23], v[48:51], v[64:71], v0\nds_read_b128 v[64:67], v3 offset:14336\n" \
    "v_smfmac_f32_16x16x64_bf16 v[16:19], v[48:51], a[24:31], v0\nds_read_b128 v[68:71], v3 offset:15360\ns_waitcnt lgkmcnt(8)\n" \
    "v_smfmac_f32_16x16x64_bf16 v[20:23], v[52:55], v[32:39], v0\nds_read_b128 v[32:35], v1 offset:32768\nv_smfmac_f32_16x16x64_bf16 v[16:19], v[52:55], a[72:79], v0\n" \
    "ds_read_b128 v[84:87], v3 offset:16384\ns_waitcnt lgkmcnt(8)\nv_smfmac_f32_16x16x64_bf16 v[20:23], v[72:75], v[40:47], v0\nds_read_b128 v[88:91], v3 offset:17408\n" \
    "v_smfmac_f32_16x16x64_bf16 v[16:19], v[72:75], a[96:103], v0\nds_read_b128 v[36:39], v1 offset:33792\ns_waitcnt lgkmcnt(7)\n" \
    "v_smfmac_f32_16x16x64_bf16 v[20:23], v[56:59], v[76:83], v0\nds_read_b128 v[40:43], v3 offset:18432\nv_smfmac_f32_16x16x64_bf16 v[16:19], v[56:59], a[120:127], v0\n" \
    "ds_read_b128 v[44:47], v3 offset:19456\ns_waitcnt lgkmcnt(6)\nv_smfmac_f32_16x16x64_bf16 v[20:23], v[60:63], v[64:71], v0\nds_read_b128 v[64:67], v1 offset:35840\n" \
    "v_smfmac_f32_16x16x64_bf16 v[16:19], v[60:63], a[168:175], v0\nds_read_b128 v[76:79], v3 offset:22528\ns_waitcnt lgkmcnt(5)\n" \
    "v_smfmac_f32_16x16x64_bf16 v[20:23], v[32:35], v[84:91], v0\nds_read_b128 v[80:83], v3 offset:23552\nv_smfmac_f32_16x16x64_bf16 v[16:19], v[32:35], a[192:199], v0\n" \
    "ds_read_b128 v[68:71], v1 offset:36864\ns_waitcnt lgkmcnt(4)\nv_smfmac_f32_16x16x64_bf16 v[20:23], v[36:39], v[40:47], v0\nds_read_b128 v[40:43], v1 offset:37888\n" \
    "v_smfmac_f32_16x16x64_bf16 v[16:19], v[36:39], a[216:223], v0\nds_read_b128 v[44:47], v1 offset:39936\ns_waitcnt lgkmcnt(3)\n" \
    "v_smfmac_f32_16x16x64_bf16 v[20:23], v[64:67], v[76:83], v0\nv_smfmac_f32_16x16x64_bf16 v[16:19], v[64:67], v[100:107], v0\ns_waitcnt lgkmcnt(2)\n" \
    "v_smfmac_f32_16x16x64_bf16 v[20:23], v[68:71], v[132:139], v0\ns_waitcnt lgkmcnt(1)\nv_smfmac_f32_16x16x64_bf16 v[20:23], v[40:43], v[164:171], v0\ns_waitcnt lgkmcnt(0)\n" \
    "v_smfmac_f32_16x16x64_bf16 v[20:23], v[44:47], v[228:235], v0\ns_nop 2\nv_smfmac_f32_16x16x64_bf16 v[16:19], v[68:71], v[124:131], v0\ns_nop 2\n" \
    "v_smfmac_f32_16x16x64_bf16 v[16:19], v[40:43], v[156:163], v0\nv_permlane32_swap_b32 v20, v22\nv_smfmac_f32_16x16x64_bf16 v[16:19], v[44:47], v[220:227], v0\ns_nop 1\n" \
    "v_permlane32_swap_b32 v21, v23\nv_smfmac_f32_16x16x64_bf16 v[24:27], v[48:51], a[32:39], v0\ns_nop 1\nv_add_f32 v20, v20, v22\n" \
    "v_smfmac_f32_16x16x64_bf16 v[28:31], v[48:51], a[40:47], v0\nv_add_f32 v21, v21, v23\nv_smfmac_f32_16x16x64_bf16 v[24:27], v[52:55], a[80:87], v0\nv_mul_f32 v20, s32, v20\n" \
    "v_smfmac_f32_16x16x64_bf16 v[28:31], v[52:55], a[88:95], v0\nv_mul_f32 v21, s32, v21\nv_smfmac_f32_16x16x64_bf16 v[24:27], v[72:75], a[104:111], v0\n" \
    "v_cvt_pk_bf16_f32 v20, v20, v21\nv_smfmac_f32_16x16x64_bf16 v[28:31], v[72:75], a[112:119], v0\nds_write_b16 v4, v20 offset:8\nv_permlane32_swap_b32 v16, v18\n" \
    "v_smfmac_f32_16x16x64_bf16 v[24:27], v[56:59], a[128:135], v0\nds_write_b16_d16_hi v4, v20 offset:24\nv_permlane32_swap_b32 v17, v19\n" \
    "v_smfmac_f32_16x16x64_bf16 v[24:27], v[60:63], a[176:183], v0\nds_read_b128 v[48:51], v1 offset:40960\nv_add_f32 v16, v16, v18\n" \
    "v_smfmac_f32_16x16x64_bf16 v[24:27], v[32:35], a[200:207], v0\nds_read_b128 v[52:55], v1 offset:45056\nv_add_f32 v17, v17, v19\n" \
    "v_smfmac_f32_16x16x64_bf16 v[24:27], v[36:39], a[224:231], v0\nds_read_b128 v[72:75], v1 offset:41984\nv_mul_f32 v16, s32, v16\n" \
    "v_smfmac_f32_16x16x64_bf16 v[24:27], v[64:67], v[108:115], v0\nds_read_b128 v[76:79], v1 offset:46080\nv_mul_f32 v17, s32, v17\n" \
    "v_smfmac_f32_16x16x64_bf16 v[24:27], v[68:71], v[140:147], v0\nds_read_b128 v[80:83], v1 offset:43008\nv_cvt_pk_bf16_f32 v16, v16, v17\n" \
    "v_smfmac_f32_16x16x64_bf16 v[24:27], v[40:43], v[172:179], v0\nds_write_b16 v4, v16 offset:0\nv_smfmac_f32_16x16x64_bf16 v[24:27], v[44:47], v[236:243], v0\n" \
    "ds_write_b16_d16_hi v4, v16 offset:16\ns_nop 1\nv_smfmac_f32_16x16x64_bf16 v[28:31], v[56:59], a[136:143], v0\nds_read_b128 v[56:59], v1 offset:47104\ns_nop 1\n" \
    "v_smfmac_f32_16x16x64_bf16 v[28:31], v[60:63], a[184:191], v0\nds_read_b128 v[60:63], v1 offset:44032\nv_permlane32_swap_b32 v24, v26\n" \
    "v_smfmac_f32_16x16x64_bf16 v[28:31], v[32:35], a[208:215], v0\nds_read_b128 v[32:35], v1 offset:48128\nv_permlane32_swap_b32 v25, v27\n" \
    "v_smfmac_f32_16x16x64_bf16 v[28:31], v[36:39], a[232:239], v0\nds_read_b128 v[36:39], v6 offset:0\nv_add_f32 v24, v24, v26\n" \
    "v_smfmac_f32_16x16x64_bf16 v[28:31], v[64:67], v[116:123], v0\nds_read_b128 v[64:67], v6 offset:512\nv_add_f32 v25, v25, v27\n" \
    "v_smfmac_f32_16x16x64_bf16 v[28:31], v[68:71], v[148:155], v0\nds_read_b128 v[68:71], v6 offset:1024\nv_cvt_pk_bf16_f32 v24, v24, v25\n" \
    "v_smfmac_f32_16x16x64_bf16 v[28:31], v[40:43], v[180:187], v0\nds_read_b128 v[40:43], v6 offset:1536\nv_smfmac_f32_16x16x64_bf16 v[28:31], v[44:47], v[244:251], v0\n" \
    "ds_write_b16 v4, v24 offset:2048\nds_write_b16_d16_hi v4, v24 offset:2064\ns_waitcnt lgkmcnt(5)\nv_mfma_f32_16x16x32_bf16 v[16:19], v[48:51], v[36:39], 0\ns_nop 2\n" \
    "v_mfma_f32_16x16x32_bf16 v[20:23], v[52:55], v[36:39], 0\nv_permlane32_swap_b32 v28, v30\nv_permlane32_swap_b32 v29, v31\nv_add_f32 v28, v28, v30\nv_add_f32 v29, v29, v31\n" \
    "v_cvt_pk_bf16_f32 v28, v28, v29\nds_write_b16 v4, v28 offset:2056\nds_write_b16_d16_hi v4, v28 offset:2072\ns_waitcnt lgkmcnt(6)\n" \
    "v_mfma_f32_16x16x32_bf16 v[16:19], v[72:75], v[64:67], v[16:19]\nv_mfma_f32_16x16x32_bf16 v[20:23], v[76:79], v[64:67], v[20:23]\ns_waitcnt lgkmcnt(5)\n" \
    "v_mfma_f32_16x16x32_bf16 v[16:19], v[80:83], v[68:71], v[16:19]\nds_read_b128 v[44:47], v8 offset:0\nds_read_b128 v[84:87], v8 offset:512\n" \
    "v_mfma_f32_16x16x32_bf16 v[20:23], v[56:59], v[68:71], v[20:23]\nds_read_b128 v[88:91], v8 offset:1024\nds_read_b128 v[28:31], v8 offset:1536\ns_waitcnt lgkmcnt(8)\n" \
    "v_mfma_f32_16x16x32_bf16 v[16:19], v[60:63], v[40:43], v[16:19]\nv_mfma_f32_16x16x32_bf16 v[20:23], v[32:35], v[40:43], v[20:23]\nv_mov_b32 v15, v15\nv_mov_b32 v15, v15\n" \
    "s_waitcnt lgkmcnt(3)\nv_mfma_f32_16x16x32_bf16 v[24:27], v[44:47], v[36:39], 0\ns_nop 2\ns_waitcnt lgkmcnt(2)\n" \
    "v_mov_b32_dpp v16, v20 quad_perm:[0,1,2,3] row_mask:0xf bank_mask:0xc\nv_mov_b32_dpp v17, v21 quad_perm:[0,1,2,3] row_mask:0xf bank_mask:0xc\n" \
    "v_mfma_f32_16x16x32_bf16 v[24:27], v[84:87], v[64:67], v[24:27]\ns_waitcnt lgkmcnt(1)\nv_mov_b32_dpp v18, v22 quad_perm:[0,1,2,3] row_mask:0xf bank_mask:0xc\n" \
    "v_mov_b32_dpp v19, v23 quad_perm:[0,1,2,3] row_mask:0xf bank_mask:0xc\nv_mfma_f32_16x16x32_bf16 v[24:27], v[88:91], v[68:71], v[24:27]\ns_waitcnt lgkmcnt(0)\n" \
    "v_max3_f32 v254, v16, v17, v18\nv_max_f32 v254, v254, v19\nv_mfma_f32_16x16x32_bf16 v[24:27], v[28:31], v[40:43], v[24:27]\ns_nop 0\n" \
    "v_max_f32_dpp v252, v254, v254 row_ror:8 row_mask:0xf bank_mask:0xf\nv_max_f32_dpp v253, v254, v254 row_ror:8 row_mask:0xf bank_mask:0xf\ns_nop 1\n" \
    "v_permlane32_swap_b32 v252, v253\nv_max_f32 v252, v252, v253\ns_xor_b32 s74, s82, 0x4000\nv_max3_f32 v254, v252, v24, v25\nv_max3_f32 v252, v254, v26, v27\n" \
    "v_max3_f32 v253, v254, v26, v27\ns_nop 1\nv_permlane16_swap_b32 v252, v253\nv_max_f32 v252, v252, v253\n" \
    "v_pk_add_f32 v[16:17], v[16:17], v[252:253] op_sel_hi:[1,0] neg_lo:[0,1] neg_hi:[0,1]\n" \
    "v_pk_add_f32 v[18:19], v[18:19], v[252:253] op_sel_hi:[1,0] neg_lo:[0,1] neg_hi:[0,1]\n" \
    "v_pk_add_f32 v[24:25], v[24:25], v[252:253] op_sel_hi:[1,0] neg_lo:[0,1] neg_hi:[0,1]\n" \
    "v_pk_add_f32 v[26:27], v[26:27], v[252:253] op_sel_hi:[1,0] neg_lo:[0,1] neg_hi:[0,1]\nv_exp_f32 v16, v16\nv_exp_f32 v17, v17\nv_exp_f32 v18, v18\nv_exp_f32 v19, v19\n" \
    "v_exp_f32 v24, v24\nv_pk_add_f32 v[254:255], v[16:17], v[18:19]\nv_exp_f32 v25, v25\nv_add_f32 v254, v254, v255\nv_exp_f32 v26, v26\nv_exp_f32 v27, v27\n" \
    "v_add_f32_dpp v252, v254, v254 row_ror:8 row_mask:0xf bank_mask:0xf\nv_add_f32_dpp v253, v254, v254 row_ror:8 row_mask:0xf bank_mask:0xf\n" \
    "v_pk_add_f32 v[254:255], v[24:25], v[26:27]\ns_nop 0\nv_permlane32_swap_b32 v252, v253\nv_pk_add_f32 v[254:255], v[254:255], v[252:253]\n" \
    "v_pk_add_f32 v[252:253], v[254:255], v[254:255] op_sel:[0,1] op_sel_hi:[1,0]\ns_nop 1\nv_permlane16_swap_b32 v252, v253\nv_add_f32 v252, v252, v253\n" \
    "v_cmp_u_f32 vcc, v252, v252\nv_rcp_f32 v252, v252\ns_cbranch_vccnz .Lq14_%=\nv_pk_mul_f32 v[16:17], v[16:17], v[252:253] op_sel_hi:[1,0]\n" \
    "v_pk_mul_f32 v[24:25], v[24:25], v[252:253] op_sel_hi:[1,0]\nv_cvt_pk_bf16_f32 v20, v16, v17\nv_cvt_pk_bf16_f32 v22, v24, v25\n" \
    "v_pk_mul_f32 v[18:19], v[18:19], v[252:253] op_sel_hi:[1,0]\nv_pk_mul_f32 v[26:27], v[26:27], v[252:253] op_sel_hi:[1,0]\nv_cvt_pk_bf16_f32 v21, v18, v19\n" \
    "v_cvt_pk_bf16_f32 v23, v26, v27\nv_permlane32_swap_b32 v20, v22\ns_nop 0\nv_permlane32_swap_b32 v21, v23\nbuffer_store_dwordx4 v[20:23], v7, s[64:67], s82 offen sc1\n" \
    "buffer_store_dwordx4 v[20:23], v7, s[64:67], s74 offen sc1\ns_branch .Lq3_%=\n.Lq7_%=:\nds_read_b128 v[32:35], v3 offset:6144\nds_read_b128 v[36:39], v3 offset:7168\n" \
    "ds_read_b128 v[40:43], v3 offset:14336\nds_read_b128 v[44:47], v3 offset:15360\nds_read_b128 v[48:51], v3 offset:22528\nds_read_b128 v[52:55], v3 offset:23552\n" \
    "ds_read_b128 v[56:59], v3 offset:0\nds_read_b128 v[60:63], v3 offset:1024\nds_read_b128 v[64:67], v3 offset:2048\nds_read_b128 v[68:71], v3 offset:3072\n.Lq72_%=:\n" \
    "s_memrealtime s[72:73]\ns_waitcnt lgkmcnt(0)\ns_mov_b32 s68, 0\ns_sub_u32 s76, s80, s45\ns_sub_u32 s69, s72, s76\ns_cmp_lt_i32 s69, -8\ns_cbranch_scc1 .Lq75_%=\n.Lq73_%=:\n" \
    "s_xor_b32 s90, s81, 0x800\n.Lq77_%=:\ns_memrealtime s[72:73]\ns_waitcnt lgkmcnt(0)\ns_sub_u32 s72, s72, s80\ns_cmp_lt_i32 s72, 0\ns_cbranch_scc1 .Lq77_%=\n" \
    "buffer_load_dwordx4 v[72:75], v2, s[64:67], s81 offen offset:0 sc1\ns_nop 7\nbuffer_load_dwordx4 v[76:79], v2, s[64:67], s81 offen offset:1024 sc1\ns_nop 7\n" \
    "buffer_load_dwordx4 v[80:83], v2, s[64:67], s90 offen offset:0 sc1\ns_nop 7\nbuffer_load_dwordx4 v[84:87], v2, s[64:67], s90 offen offset:1024 sc1\nv_mov_b64 v[16:17], 0\n" \
    "v_mov_b64 v[18:19], 0\nv_mov_b64 v[20:21], 0\nv_mov_b64 v[22:23], 0\nv_mov_b64 v[24:25], 0\nv_mov_b64 v[26:27], 0\nv_mov_b64 v[28:29], 0\nv_mov_b64 v[30:31], 0\n" \
    "s_waitcnt vmcnt(3)\nv_smfmac_f32_16x16x64_bf16 v[20:23], v[72:75], v[32:39], v0\nds_write_b128 v1, v[72:75] offset:27648\nv_max3_u32 v252, v72, v73, v74\n" \
    "v_smfmac_f32_16x16x64_bf16 v[16:19], v[72:75], a[72:79], v0\nv_smfmac_f32_16x16x64_bf16 v[24:27], v[72:75], a[80:87], v0\n" \
    "v_smfmac_f32_16x16x64_bf16 v[28:31], v[72:75], a[88:95], v0\ns_waitcnt vmcnt(2)\nds_write_b128 v1, v[76:79] offset:31744\nv_max3_u32 v252, v252, v75, v76\n" \
    "v_smfmac_f32_16x16x64_bf16 v[20:23], v[76:79], v[40:47], v0\nv_max3_u32 v252, v252, v77, v78\nv_smfmac_f32_16x16x64_bf16 v[16:19], v[76:79], a[168:175], v0\n" \
    "v_cmp_u_f32 vcc, v252, v79\nv_smfmac_f32_16x16x64_bf16 v[24:27], v[76:79], a[176:183], v0\ns_cbranch_vccnz .Lq83_%=\n.Lq79_%=:\n" \
    "v_smfmac_f32_16x16x64_bf16 v[28:31], v[76:79], a[184:191], v0\nds_read_b128 v[36:39], v3 offset:5120\ns_waitcnt vmcnt(1)\nv_max3_u32 v252, v80, v81, v82\n" \
    "v_cmp_u_f32 vcc, v252, v83\ns_nop 0\ns_cbranch_vccnz .Lq84_%=\n.Lq80_%=:\nv_smfmac_f32_16x16x64_bf16 v[20:23], v[80:83], v[48:55], v0\n" \
    "ds_write_b128 v1, v[80:83] offset:35840\nv_smfmac_f32_16x16x64_bf16 v[16:19], v[80:83], v[100:107], v0\nds_read_b128 v[32:35], v3 offset:4096\ns_waitcnt lgkmcnt(3)\n" \
    "s_barrier\nds_read_b128 v[88:91], v1 offset:24576\nv_smfmac_f32_16x16x64_bf16 v[24:27], v[80:83], v[108:115], v0\nds_read_b128 v[40:43], v3 offset:8192\n" \
    "v_smfmac_f32_16x16x64_bf16 v[28:31], v[80:83], v[116:123], v0\nds_read_b128 v[48:51], v1 offset:25600\ns_waitcnt vmcnt(0)\nv_max3_u32 v252, v84, v85, v86\n" \
    "v_cmp_u_f32 vcc, v252, v87\ns_nop 0\ns_cbranch_vccnz .Lq85_%=\n.Lq81_%=:\nv_smfmac_f32_16x16x64_bf16 v[20:23], v[84:87], v[228:235], v0\n" \
    "ds_write_b128 v1, v[84:87] offset:39936\nv_smfmac_f32_16x16x64_bf16 v[16:19], v[84:87], v[220:227], v0\nds_read_b128 v[52:55], v1 offset:26624\n" \
    "v_smfmac_f32_16x16x64_bf16 v[24:27], v[84:87], v[236:243], v0\nds_read_b128 v[72:75], v1 offset:28672\nv_smfmac_f32_16x16x64_bf16 v[28:31], v[84:87], v[244:251], v0\n" \
    "ds_read_b128 v[44:47], v3 offset:9216\ns_waitcnt lgkmcnt(6)\nv_smfmac_f32_16x16x64_bf16 v[20:23], v[88:91], v[56:63], v0\nds_read_b128 v[56:59], v1 offset:29696\n" \
    "v_smfmac_f32_16x16x64_bf16 v[16:19], v[88:91], a[0:7], v0\nds_read_b128 v[76:79], v3 offset:10240\nv_smfmac_f32_16x16x64_bf16 v[24:27], v[88:91], a[8:15], v0\n" \
    "ds_read_b128 v[80:83], v3 offset:11264\nv_smfmac_f32_16x16x64_bf16 v[28:31], v[88:91], a[16:23], v0\nds_read_b128 v[60:63], v1 offset:30720\ns_waitcnt lgkmcnt(7)\n" \
    "s_barrier\nv_smfmac_f32_16x16x64_bf16 v[20:23], v[48:51], v[64:71], v0\nds_read_b128 v[64:67], v3 offset:12288\n" \
    "v_smfmac_f32_16x16x64_bf16 v[16:19], v[48:51], a[24:31], v0\nds_read_b128 v[68:71], v3 offset:13312\ns_waitcnt lgkmcnt(8)\n" \
    "v_smfmac_f32_16x16x64_bf16 v[20:23], v[52:55], v[32:39], v0\nds_read_b128 v[32:35], v1 offset:32768\nv_smfmac_f32_16x16x64_bf16 v[16:19], v[52:55], a[48:55], v0\n" \
    "ds_read_b128 v[84:87], v3 offset:16384\ns_waitcnt lgkmcnt(8)\nv_smfmac_f32_16x16x64_bf16 v[20:23], v[72:75], v[40:47], v0\nds_read_b128 v[88:91], v3 offset:17408\n" \
    "v_smfmac_f32_16x16x64_bf16 v[16:19], v[72:75], a[96:103], v0\nds_read_b128 v[36:39], v1 offset:33792\ns_waitcnt lgkmcnt(7)\n" \
    "v_smfmac_f32_16x16x64_bf16 v[20:23], v[56:59], v[76:83], v0\nds_read_b128 v[40:43], v3 offset:18432\nv_smfmac_f32_16x16x64_bf16 v[16:19], v[56:59], a[120:127], v0\n" \
    "ds_read_b128 v[44:47], v3 offset:19456\ns_waitcnt lgkmcnt(6)\nv_smfmac_f32_16x16x64_bf16 v[20:23], v[60:63], v[64:71], v0\nds_read_b128 v[64:67], v1 offset:34816\n" \
    "v_smfmac_f32_16x16x64_bf16 v[16:19], v[60:63], a[144:151], v0\nds_read_b128 v[76:79], v3 offset:20480\ns_waitcnt lgkmcnt(5)\n" \
    "v_smfmac_f32_16x16x64_bf16 v[20:23], v[32:35], v[84:91], v0\nds_read_b128 v[80:83], v3 offset:21504\nv_smfmac_f32_16x16x64_bf16 v[16:19], v[32:35], a[192:199], v0\n" \
    "ds_read_b128 v[68:71], v1 offset:36864\ns_waitcnt lgkmcnt(4)\nv_smfmac_f32_16x16x64_bf16 v[20:23], v[36:39], v[40:47], v0\nds_read_b128 v[40:43], v1 offset:37888\n" \
    "v_smfmac_f32_16x16x64_bf16 v[16:19], v[36:39], a[216:223], v0\nds_read_b128 v[44:47], v1 offset:38912\ns_waitcnt lgkmcnt(3)\n" \
    "v_smfmac_f32_16x16x64_bf16 v[20:23], v[64:67], v[76:83], v0\nv_smfmac_f32_16x16x64_bf16 v[16:19], v[64:67], a[240:247], v0\ns_waitcnt lgkmcnt(2)\n" \
    "v_smfmac_f32_16x16x64_bf16 v[20:23], v[68:71], v[132:139], v0\ns_waitcnt lgkmcnt(1)\nv_smfmac_f32_16x16x64_bf16 v[20:23], v[40:43], v[164:171], v0\ns_waitcnt lgkmcnt(0)\n" \
    "v_smfmac_f32_16x16x64_bf16 v[20:23], v[44:47], v[196:203], v0\ns_nop 2\nv_smfmac_f32_16x16x64_bf16 v[16:19], v[68:71], v[124:131], v0\ns_nop 2\n" \
    "v_smfmac_f32_16x16x64_bf16 v[16:19], v[40:43], v[156:163], v0\nv_permlane32_swap_b32 v20, v22\nv_smfmac_f32_16x16x64_bf16 v[16:19], v[44:47], v[188:195], v0\ns_nop 1\n" \
    "v_permlane32_swap_b32 v21, v23\nv_smfmac_f32_16x16x64_bf16 v[24:27], v[48:51], a[32:39], v0\ns_nop 1\nv_add_f32 v20, v20, v22\n" \
    "v_smfmac_f32_16x16x64_bf16 v[28:31], v[48:51], a[40:47], v0\nv_add_f32 v21, v21, v23\nv_smfmac_f32_16x16x64_bf16 v[24:27], v[52:55], a[56:63], v0\nv_mul_f32 v20, s32, v20\n" \
    "v_smfmac_f32_16x16x64_bf16 v[28:31], v[52:55], a[64:71], v0\nv_mul_f32 v21, s32, v21\nv_smfmac_f32_16x16x64_bf16 v[24:27], v[72:75], a[104:111], v0\n" \
    "v_cvt_pk_bf16_f32 v20, v20, v21\nv_smfmac_f32_16x16x64_bf16 v[28:31], v[72:75], a[112:119], v0\nds_write_b16 v4, v20 offset:8\nv_permlane32_swap_b32 v16, v18\n" \
    "v_smfmac_f32_16x16x64_bf16 v[24:27], v[56:59], a[128:135], v0\nds_write_b16_d16_hi v4, v20 offset:24\nv_permlane32_swap_b32 v17, v19\n" \
    "v_smfmac_f32_16x16x64_bf16 v[24:27], v[60:63], a[152:159], v0\nds_read_b128 v[48:51], v1 offset:40960\nv_add_f32 v16, v16, v18\n" \
    "v_smfmac_f32_16x16x64_bf16 v[24:27], v[32:35], a[200:207], v0\nds_read_b128 v[52:55], v1 offset:45056\nv_add_f32 v17, v17, v19\n" \
    "v_smfmac_f32_16x16x64_bf16 v[24:27], v[36:39], a[224:231], v0\nds_read_b128 v[72:75], v1 offset:41984\nv_mul_f32 v16, s32, v16\n" \
    "v_smfmac_f32_16x16x64_bf16 v[24:27], v[64:67], a[248:255], v0\nds_read_b128 v[76:79], v1 offset:46080\nv_mul_f32 v17, s32, v17\n" \
    "v_smfmac_f32_16x16x64_bf16 v[24:27], v[68:71], v[140:147], v0\nds_read_b128 v[80:83], v1 offset:43008\nv_cvt_pk_bf16_f32 v16, v16, v17\n" \
    "v_smfmac_f32_16x16x64_bf16 v[24:27], v[40:43], v[172:179], v0\nds_write_b16 v4, v16 offset:0\nv_smfmac_f32_16x16x64_bf16 v[24:27], v[44:47], v[204:211], v0\n" \
    "ds_write_b16_d16_hi v4, v16 offset:16\ns_nop 1\nv_smfmac_f32_16x16x64_bf16 v[28:31], v[56:59], a[136:143], v0\nds_read_b128 v[56:59], v1 offset:47104\ns_nop 1\n" \
    "v_smfmac_f32_16x16x64_bf16 v[28:31], v[60:63], a[160:167], v0\nds_read_b128 v[60:63], v1 offset:44032\nv_permlane32_swap_b32 v24, v26\n" \
    "v_smfmac_f32_16x16x64_bf16 v[28:31], v[32:35], a[208:215], v0\nds_read_b128 v[32:35], v1 offset:48128\nv_permlane32_swap_b32 v25, v27\n" \
    "v_smfmac_f32_16x16x64_bf16 v[28:31], v[36:39], a[232:239], v0\nds_read_b128 v[36:39], v6 offset:0\nv_add_f32 v24, v24, v26\n" \
    "v_smfmac_f32_16x16x64_bf16 v[28:31], v[64:67], v[92:99], v0\nds_read_b128 v[64:67], v6 offset:512\nv_add_f32 v25, v25, v27\n" \
    "v_smfmac_f32_16x16x64_bf16 v[28:31], v[68:71], v[148:155], v0\nds_read_b128 v[68:71], v6 offset:1024\nv_cvt_pk_bf16_f32 v24, v24, v25\n" \
    "v_smfmac_f32_16x16x64_bf16 v[28:31], v[40:43], v[180:187], v0\nds_read_b128 v[40:43], v6 offset:1536\nv_smfmac_f32_16x16x64_bf16 v[28:31], v[44:47], v[212:219], v0\n" \
    "ds_write_b16 v4, v24 offset:2048\nds_write_b16_d16_hi v4, v24 offset:2064\ns_waitcnt lgkmcnt(5)\nv_mfma_f32_16x16x32_bf16 v[16:19], v[48:51], v[36:39], 0\ns_nop 2\n" \
    "v_mfma_f32_16x16x32_bf16 v[20:23], v[52:55], v[36:39], 0\nv_permlane32_swap_b32 v28, v30\nv_permlane32_swap_b32 v29, v31\nv_add_f32 v28, v28, v30\nv_add_f32 v29, v29, v31\n" \
    "v_cvt_pk_bf16_f32 v28, v28, v29\nds_write_b16 v4, v28 offset:2056\nds_write_b16_d16_hi v4, v28 offset:2072\ns_waitcnt lgkmcnt(6)\n" \
    "v_mfma_f32_16x16x32_bf16 v[16:19], v[72:75], v[64:67], v[16:19]\nv_mfma_f32_16x16x32_bf16 v[20:23], v[76:79], v[64:67], v[20:23]\ns_waitcnt lgkmcnt(5)\n" \
    "v_mfma_f32_16x16x32_bf16 v[16:19], v[80:83], v[68:71], v[16:19]\nds_read_b128 v[44:47], v8 offset:0\nds_read_b128 v[84:87], v8 offset:512\n" \
    "v_mfma_f32_16x16x32_bf16 v[20:23], v[56:59], v[68:71], v[20:23]\nds_read_b128 v[88:91], v8 offset:1024\nds_read_b128 v[28:31], v8 offset:1536\ns_waitcnt lgkmcnt(8)\n" \
    "v_mfma_f32_16x16x32_bf16 v[16:19], v[60:63], v[40:43], v[16:19]\nv_mfma_f32_16x16x32_bf16 v[20:23], v[32:35], v[40:43], v[20:23]\nv_mov_b32 v15, v15\nv_mov_b32 v15, v15\n" \
    "s_waitcnt lgkmcnt(3)\nv_mfma_f32_16x16x32_bf16 v[24:27], v[44:47], v[36:39], 0\ns_nop 2\ns_waitcnt lgkmcnt(2)\n" \
    "v_mov_b32_dpp v16, v20 quad_perm:[0,1,2,3] row_mask:0xf bank_mask:0xc\nv_mov_b32_dpp v17, v21 quad_perm:[0,1,2,3] row_mask:0xf bank_mask:0xc\n" \
    "v_mfma_f32_16x16x32_bf16 v[24:27], v[84:87], v[64:67], v[24:27]\ns_waitcnt lgkmcnt(1)\nv_mov_b32_dpp v18, v22 quad_perm:[0,1,2,3] row_mask:0xf bank_mask:0xc\n" \
    "v_mov_b32_dpp v19, v23 quad_perm:[0,1,2,3] row_mask:0xf bank_mask:0xc\nv_mfma_f32_16x16x32_bf16 v[24:27], v[88:91], v[68:71], v[24:27]\ns_waitcnt lgkmcnt(0)\n" \
    "v_max3_f32 v254, v16, v17, v18\nv_max_f32 v254, v254, v19\nv_mfma_f32_16x16x32_bf16 v[24:27], v[28:31], v[40:43], v[24:27]\ns_nop 0\n" \
    "v_max_f32_dpp v252, v254, v254 row_ror:8 row_mask:0xf bank_mask:0xf\nv_max_f32_dpp v253, v254, v254 row_ror:8 row_mask:0xf bank_mask:0xf\ns_nop 1\n" \
    "v_permlane32_swap_b32 v252, v253\nv_max_f32 v252, v252, v253\ns_xor_b32 s74, s82, 0x4000\nv_max3_f32 v254, v252, v24, v25\nv_max3_f32 v252, v254, v26, v27\n" \
    "v_max3_f32 v253, v254, v26, v27\ns_nop 1\nv_permlane16_swap_b32 v252, v253\nv_max_f32 v252, v252, v253\n" \
    "v_pk_add_f32 v[16:17], v[16:17], v[252:253] op_sel_hi:[1,0] neg_lo:[0,1] neg_hi:[0,1]\n" \
    "v_pk_add_f32 v[18:19], v[18:19], v[252:253] op_sel_hi:[1,0] neg_lo:[0,1] neg_hi:[0,1]\n" \
    "v_pk_add_f32 v[24:25], v[24:25], v[252:253] op_sel_hi:[1,0] neg_lo:[0,1] neg_hi:[0,1]\n" \
    "v_pk_add_f32 v[26:27], v[26:27], v[252:253] op_sel_hi:[1,0] neg_lo:[0,1] neg_hi:[0,1]\nv_exp_f32 v16, v16\nv_exp_f32 v17, v17\nv_exp_f32 v18, v18\nv_exp_f32 v19, v19\n" \
    "v_exp_f32 v24, v24\nv_pk_add_f32 v[254:255], v[16:17], v[18:19]\nv_exp_f32 v25, v25\nv_add_f32 v254, v254, v255\nv_exp_f32 v26, v26\nv_exp_f32 v27, v27\n" \
    "v_add_f32_dpp v252, v254, v254 row_ror:8 row_mask:0xf bank_mask:0xf\nv_add_f32_dpp v253, v254, v254 row_ror:8 row_mask:0xf bank_mask:0xf\n" \
    "v_pk_add_f32 v[254:255], v[24:25], v[26:27]\ns_nop 0\nv_permlane32_swap_b32 v252, v253\nv_pk_add_f32 v[254:255], v[254:255], v[252:253]\n" \
    "v_pk_add_f32 v[252:253], v[254:255], v[254:255] op_sel:[0,1] op_sel_hi:[1,0]\ns_nop 1\nv_permlane16_swap_b32 v252, v253\nv_add_f32 v252, v252, v253\n" \
    "v_cmp_u_f32 vcc, v252, v252\nv_rcp_f32 v252, v252\ns_cbranch_vccnz .Lq14_%=\nv_pk_mul_f32 v[16:17], v[16:17], v[252:253] op_sel_hi:[1,0]\n" \
    "v_pk_mul_f32 v[24:25], v[24:25], v[252:253] op_sel_hi:[1,0]\nv_cvt_pk_bf16_f32 v20, v16, v17\nv_cvt_pk_bf16_f32 v22, v24, v25\n" \
    "v_pk_mul_f32 v[18:19], v[18:19], v[252:253] op_sel_hi:[1,0]\nv_pk_mul_f32 v[26:27], v[26:27], v[252:253] op_sel_hi:[1,0]\nv_cvt_pk_bf16_f32 v21, v18, v19\n" \
    "v_cvt_pk_bf16_f32 v23, v26, v27\nv_permlane32_swap_b32 v20, v22\ns_nop 0\nv_permlane32_swap_b32 v21, v23\nbuffer_store_dwordx4 v[20:23], v7, s[64:67], s82 offen sc1\n" \
    "buffer_store_dwordx4 v[20:23], v7, s[64:67], s74 offen sc1\n.Lq3_%=:\nds_read_b128 v[36:39], v5 offset:0\nds_read_b128 v[64:67], v5 offset:1024\n" \
    "ds_read_b128 v[68:71], v5 offset:2048\nds_read_b128 v[40:43], v5 offset:3072\ns_mov_b32 s40, s84\ns_add_u32 s35, s35, 2\ns_barrier\ns_waitcnt lgkmcnt(3)\n" \
    "v_mfma_f32_16x16x32_bf16 v[16:19], v[48:51], v[36:39], 0\nv_mfma_f32_16x16x32_bf16 v[20:23], v[52:55], v[36:39], 0\ns_waitcnt lgkmcnt(2)\n" \
    "v_mfma_f32_16x16x32_bf16 v[16:19], v[72:75], v[64:67], v[16:19]\nv_mfma_f32_16x16x32_bf16 v[20:23], v[76:79], v[64:67], v[20:23]\ns_waitcnt lgkmcnt(1)\n" \
    "v_mfma_f32_16x16x32_bf16 v[16:19], v[80:83], v[68:71], v[16:19]\nv_mfma_f32_16x16x32_bf16 v[20:23], v[56:59], v[68:71], v[20:23]\ns_waitcnt lgkmcnt(0)\n" \
    "v_mfma_f32_16x16x32_bf16 v[16:19], v[60:63], v[40:43], v[16:19]\nv_mfma_f32_16x16x32_bf16 v[20:23], v[32:35], v[40:43], v[20:23]\n" \
    "v_mfma_f32_16x16x32_bf16 v[24:27], v[44:47], v[36:39], 0\nv_mfma_f32_16x16x32_bf16 v[24:27], v[84:87], v[64:67], v[24:27]\n" \
    "v_mfma_f32_16x16x32_bf16 v[24:27], v[88:91], v[68:71], v[24:27]\nv_mfma_f32_16x16x32_bf16 v[24:27], v[28:31], v[40:43], v[24:27]\ns_cmp_lt_u32 s35, 64\n" \
    "s_cbranch_scc1 .Lq2_%=\n.Lq8_%=:\ns_nop 1\nv_mov_b32_dpp v16, v20 quad_perm:[0,1,2,3] row_mask:0xf bank_mask:0xc\n" \
    "v_mov_b32_dpp v17, v21 quad_perm:[0,1,2,3] row_mask:0xf bank_mask:0xc\nv_mov_b32_dpp v18, v22 quad_perm:[0,1,2,3] row_mask:0xf bank_mask:0xc\n" \
    "v_mov_b32_dpp v19, v23 quad_perm:[0,1,2,3] row_mask:0xf bank_mask:0xc\nv_mov_b32 v254, 0xff800000\nv_cvt_pk_bf16_f32 v16, 0, v16\nv_cvt_pk_bf16_f32 v17, 0, v17\n" \
    "v_cvt_pk_bf16_f32 v18, 0, v18\nv_cvt_pk_bf16_f32 v19, 0, v19\nv_mul_f32 v16, s33, v16\nv_mul_f32 v17, s33, v17\nv_mul_f32 v18, s33, v18\nv_mul_f32 v19, s33, v19\n" \
    "v_cvt_pk_bf16_f32 v16, 0, v16\nv_cvt_pk_bf16_f32 v17, 0, v17\nv_cvt_pk_bf16_f32 v18, 0, v18\nv_cvt_pk_bf16_f32 v19, 0, v19\nv_cndmask_b32 v16, v254, v16, s[50:51]\n" \
    "v_cndmask_b32 v17, v254, v17, s[52:53]\nv_cndmask_b32 v18, v254, v18, s[54:55]\nv_cndmask_b32 v19, v254, v19, s[56:57]\nv_max3_f32 v254, v16, v17, v18\n" \
    "v_max_f32 v254, v254, v19\ns_nop 1\nv_max_f32_dpp v252, v254, v254 row_ror:8 row_mask:0xf bank_mask:0xf\n" \
    "v_max_f32_dpp v253, v254, v254 row_ror:8 row_mask:0xf bank_mask:0xf\ns_nop 1\nv_permlane32_swap_b32 v252, v253\nv_max_f32 v252, v252, v253\nv_cvt_pk_bf16_f32 v24, 0, v24\n" \
    "v_cvt_pk_bf16_f32 v25, 0, v25\nv_cvt_pk_bf16_f32 v26, 0, v26\nv_cvt_pk_bf16_f32 v27, 0, v27\nv_mul_f32 v24, s33, v24\nv_mul_f32 v25, s33, v25\nv_mul_f32 v26, s33, v26\n" \
    "v_mul_f32 v27, s33, v27\nv_cvt_pk_bf16_f32 v24, 0, v24\nv_cvt_pk_bf16_f32 v25, 0, v25\nv_cvt_pk_bf16_f32 v26, 0, v26\nv_cvt_pk_bf16_f32 v27, 0, v27\n" \
    "v_max3_f32 v254, v252, v24, v25\nv_max3_f32 v252, v254, v26, v27\nv_max3_f32 v253, v254, v26, v27\ns_nop 1\nv_permlane16_swap_b32 v252, v253\nv_max_f32 v252, v252, v253\n" \
    "v_pk_add_f32 v[16:17], v[16:17], v[252:253] op_sel_hi:[1,0] neg_lo:[0,1] neg_hi:[0,1]\n" \
    "v_pk_add_f32 v[18:19], v[18:19], v[252:253] op_sel_hi:[1,0] neg_lo:[0,1] neg_hi:[0,1]\n" \
    "v_pk_add_f32 v[24:25], v[24:25], v[252:253] op_sel_hi:[1,0] neg_lo:[0,1] neg_hi:[0,1]\n" \
    "v_pk_add_f32 v[26:27], v[26:27], v[252:253] op_sel_hi:[1,0] neg_lo:[0,1] neg_hi:[0,1]\nv_mul_f32 v16, s34, v16\nv_mul_f32 v17, s34, v17\nv_mul_f32 v18, s34, v18\n" \
    "v_mul_f32 v19, s34, v19\nv_mul_f32 v24, s34, v24\nv_mul_f32 v25, s34, v25\nv_mul_f32 v26, s34, v26\nv_mul_f32 v27, s34, v27\nv_exp_f32 v16, v16\nv_exp_f32 v17, v17\n" \
    "v_exp_f32 v18, v18\nv_exp_f32 v19, v19\nv_exp_f32 v24, v24\nv_pk_add_f32 v[254:255], v[16:17], v[18:19]\nv_exp_f32 v25, v25\nv_add_f32 v254, v254, v255\n" \
    "v_exp_f32 v26, v26\nv_exp_f32 v27, v27\nv_add_f32_dpp v252, v254, v254 row_ror:8 row_mask:0xf bank_mask:0xf\n" \
    "v_add_f32_dpp v253, v254, v254 row_ror:8 row_mask:0xf bank_mask:0xf\nv_pk_add_f32 v[254:255], v[24:25], v[26:27]\ns_nop 0\nv_permlane32_swap_b32 v252, v253\n" \
    "v_pk_add_f32 v[254:255], v[254:255], v[252:253]\nv_pk_add_f32 v[252:253], v[254:255], v[254:255] op_sel:[0,1] op_sel_hi:[1,0]\ns_nop 1\nv_permlane16_swap_b32 v252, v253\n" \
    "v_add_f32 v252, v252, v253\nv_rcp_f32 v252, v252\ns_nop 0\nv_pk_mul_f32 v[16:17], v[16:17], v[252:253] op_sel_hi:[1,0]\n" \
    "v_pk_mul_f32 v[24:25], v[24:25], v[252:253] op_sel_hi:[1,0]\nv_cvt_pk_bf16_f32 v20, v16, v17\nv_cvt_pk_bf16_f32 v22, v24, v25\n" \
    "v_pk_mul_f32 v[18:19], v[18:19], v[252:253] op_sel_hi:[1,0]\nv_pk_mul_f32 v[26:27], v[26:27], v[252:253] op_sel_hi:[1,0]\nv_cvt_pk_bf16_f32 v21, v18, v19\n" \
    "v_cvt_pk_bf16_f32 v23, v26, v27\nv_permlane32_swap_b32 v20, v22\ns_nop 0\nv_permlane32_swap_b32 v21, v23\ns_xor_b32 s74, s83, 0x2000\n" \
    "buffer_store_dwordx4 v[20:23], v7, s[64:67], s83 offen sc1\nbuffer_store_dwordx4 v[20:23], v7, s[64:67], s74 offen sc1\ns_cmp_lt_u32 s35, 64\ns_cbranch_scc1 .Lq9_%=\n" \
    "s_endpgm\n.Lq10_%=:\ns_mov_b32 s76, 33024\nbuffer_load_dword v253, off, s[64:67], s76 sc1\ns_waitcnt vmcnt(0)\nv_readfirstlane_b32 s74, v253\ns_nop 0\n" \
    "s_cmp_ge_u32 s74, 32\ns_cbranch_scc1 .Lq11_%=\ns_sleep 8\ns_add_u32 s68, s68, 1\ns_and_b32 s69, s68, 63\ns_cbranch_scc1 .Lq10_%=\ns_cmp_gt_u32 s68, 0x400000\n" \
    "s_cbranch_scc1 .Lq1_%=\nbuffer_load_dword v253, off, s[64:67], 0 sc1\ns_waitcnt vmcnt(0)\nv_readfirstlane_b32 s69, v253\ns_nop 0\ns_cmp_eq_u32 s69, 1\n" \
    "s_cbranch_scc0 .Lq10_%=\ns_endpgm\n.Lq12_%=:\ns_sleep 6\nbuffer_load_dword v13, off, s[64:67], s42 sc1\ns_waitcnt vmcnt(0)\nv_readfirstlane_b32 s74, v13\ns_nop 0\n" \
    "s_and_b32 s75, s74, 127\ns_lshr_b32 s69, s74, 8\ns_sub_u32 s69, s69, s80\ns_bfe_i32 s69, s69, 0x180000\ns_cmp_eq_u32 s75, 0\ns_cselect_b32 s69, 0, s69\n" \
    "s_add_u32 s80, s80, s69\ns_lshl_b32 s74, s74, 5\ns_and_b32 s74, s74, 0x1000\ns_andn2_b32 s81, s81, 0x1000\ns_or_b32 s81, s81, s74\ns_lshl_b32 s74, s74, 2\n" \
    "s_andn2_b32 s82, s82, 0x4000\ns_or_b32 s82, s82, s74\ns_add_u32 s68, s68, 1\ns_and_b32 s69, s68, 63\ns_cbranch_scc1 .Lq11_%=\ns_cmp_gt_u32 s68, 0x400000\n" \
    "s_cbranch_scc1 .Lq1_%=\nbuffer_load_dword v253, off, s[64:67], 0 sc1\ns_waitcnt vmcnt(0)\nv_readfirstlane_b32 s69, v253\ns_nop 0\ns_cmp_eq_u32 s69, 1\n" \
    "s_cbranch_scc0 .Lq11_%=\ns_endpgm\n.Lq18_%=:\ns_memrealtime s[72:73]\ns_waitcnt lgkmcnt(0)\ns_sub_u32 s72, s72, s76\ns_cmp_lt_i32 s72, 0\ns_cbranch_scc1 .Lq18_%=\n" \
    "buffer_load_dword v13, off, s[64:67], s42 sc1\ns_waitcnt vmcnt(0)\nv_readfirstlane_b32 s74, v13\ns_nop 0\ns_and_b32 s75, s74, 127\ns_lshr_b32 s69, s74, 8\n" \
    "s_sub_u32 s69, s69, s80\ns_bfe_i32 s69, s69, 0x180000\ns_cmp_eq_u32 s75, 0\ns_cselect_b32 s69, 0, s69\ns_add_u32 s80, s80, s69\ns_lshl_b32 s74, s74, 5\n" \
    "s_and_b32 s74, s74, 0x1000\ns_andn2_b32 s81, s81, 0x1000\ns_or_b32 s81, s81, s74\ns_lshl_b32 s74, s74, 2\ns_andn2_b32 s82, s82, 0x4000\ns_or_b32 s82, s82, s74\n" \
    "s_branch .Lq15_%=\n.Lq26_%=:\nv_mov_b32 v252, 0\nv_max3_u32 v252, v252, v72, v73\nv_max3_u32 v252, v252, v74, v75\nv_max3_u32 v252, v252, v76, v77\n" \
    "v_max3_u32 v252, v252, v78, v79\nv_cmp_eq_u32 vcc, -1, v252\ns_nop 0\ns_cbranch_vccz .Lq22_%=\ns_add_u32 s80, s80, s43\ns_xor_b32 s81, s81, 0x1000\n" \
    "s_xor_b32 s90, s90, 0x1000\ns_xor_b32 s82, s82, 0x4000\ns_cmp_eq_u32 s61, 0\ns_cbranch_scc1 .Lq29_%=\ns_lshr_b32 s69, s81, 5\ns_and_b32 s69, s69, 128\n" \
    "s_lshl_b32 s74, s80, 8\ns_add_u32 s74, s74, s69\ns_add_u32 s74, s74, s35\nv_mov_b32 v253, s74\ns_mov_b64 exec, 1\nbuffer_store_dword v253, off, s[64:67], s61 sc1\n" \
    "s_mov_b64 exec, -1\n.Lq29_%=:\ns_add_u32 s68, s68, 1\ns_and_b32 s69, s68, 63\ns_cbranch_scc1 .Lq16_%=\ns_cmp_gt_u32 s68, 0x400000\ns_cbranch_scc1 .Lq1_%=\n" \
    "buffer_load_dword v253, off, s[64:67], 0 sc1\ns_waitcnt vmcnt(0)\nv_readfirstlane_b32 s69, v253\ns_nop 0\ns_cmp_eq_u32 s69, 1\ns_cbranch_scc0 .Lq16_%=\ns_endpgm\n" \
    ".Lq27_%=:\nv_mov_b32 v252, 0\nv_max3_u32 v252, v252, v80, v81\nv_max3_u32 v252, v252, v82, v83\nv_cmp_eq_u32 vcc, -1, v252\ns_nop 0\ns_cbranch_vccz .Lq23_%=\n" \
    "s_add_u32 s80, s80, s43\ns_xor_b32 s81, s81, 0x1000\ns_xor_b32 s90, s90, 0x1000\ns_xor_b32 s82, s82, 0x4000\ns_cmp_eq_u32 s61, 0\ns_cbranch_scc1 .Lq31_%=\n" \
    "s_lshr_b32 s69, s81, 5\ns_and_b32 s69, s69, 128\ns_lshl_b32 s74, s80, 8\ns_add_u32 s74, s74, s69\ns_add_u32 s74, s74, s35\nv_mov_b32 v253, s74\ns_mov_b64 exec, 1\n" \
    "buffer_store_dword v253, off, s[64:67], s61 sc1\ns_mov_b64 exec, -1\n.Lq31_%=:\n.Lq30_%=:\ns_memrealtime s[72:73]\ns_waitcnt vmcnt(0) lgkmcnt(0)\ns_sub_u32 s72, s72, s80\n" \
    "s_cmp_lt_i32 s72, 0\ns_cbranch_scc1 .Lq30_%=\nbuffer_load_dwordx4 v[80:83], v2, s[64:67], s90 offen offset:0 sc1\n" \
    "buffer_load_dwordx4 v[84:87], v2, s[64:67], s90 offen offset:1024 sc1\ns_waitcnt vmcnt(0)\ns_add_u32 s68, s68, 1\ns_and_b32 s69, s68, 63\ns_cbranch_scc1 .Lq27_%=\n" \
    "s_cmp_gt_u32 s68, 0x400000\ns_cbranch_scc1 .Lq1_%=\nbuffer_load_dword v253, off, s[64:67], 0 sc1\ns_waitcnt vmcnt(0)\nv_readfirstlane_b32 s69, v253\ns_nop 0\n" \
    "s_cmp_eq_u32 s69, 1\ns_cbranch_scc0 .Lq27_%=\ns_endpgm\n.Lq28_%=:\nv_mov_b32 v252, 0\nv_max3_u32 v252, v252, v84, v85\nv_max3_u32 v252, v252, v86, v87\n" \
    "v_cmp_eq_u32 vcc, -1, v252\ns_nop 0\ns_cbranch_vccz .Lq24_%=\ns_add_u32 s80, s80, s43\ns_xor_b32 s81, s81, 0x1000\ns_xor_b32 s90, s90, 0x1000\ns_xor_b32 s82, s82, 0x4000\n" \
    "s_cmp_eq_u32 s61, 0\ns_cbranch_scc1 .Lq33_%=\ns_lshr_b32 s69, s81, 5\ns_and_b32 s69, s69, 128\ns_lshl_b32 s74, s80, 8\ns_add_u32 s74, s74, s69\ns_add_u32 s74, s74, s35\n" \
    "v_mov_b32 v253, s74\ns_mov_b64 exec, 1\nbuffer_store_dword v253, off, s[64:67], s61 sc1\ns_mov_b64 exec, -1\n.Lq33_%=:\n.Lq32_%=:\ns_memrealtime s[72:73]\n" \
    "s_waitcnt vmcnt(0) lgkmcnt(0)\ns_sub_u32 s72, s72, s80\ns_cmp_lt_i32 s72, 0\ns_cbranch_scc1 .Lq32_%=\n" \
    "buffer_load_dwordx4 v[84:87], v2, s[64:67], s90 offen offset:1024 sc1\ns_waitcnt vmcnt(0)\ns_add_u32 s68, s68, 1\ns_and_b32 s69, s68, 63\ns_cbranch_scc1 .Lq28_%=\n" \
    "s_cmp_gt_u32 s68, 0x400000\ns_cbranch_scc1 .Lq1_%=\nbuffer_load_dword v253, off, s[64:67], 0 sc1\ns_waitcnt vmcnt(0)\nv_readfirstlane_b32 s69, v253\ns_nop 0\n" \
    "s_cmp_eq_u32 s69, 1\ns_cbranch_scc0 .Lq28_%=\ns_endpgm\n.Lq37_%=:\ns_memrealtime s[72:73]\ns_waitcnt lgkmcnt(0)\ns_sub_u32 s72, s72, s76\ns_cmp_lt_i32 s72, 0\n" \
    "s_cbranch_scc1 .Lq37_%=\nbuffer_load_dword v13, off, s[64:67], s42 sc1\ns_waitcnt vmcnt(0)\nv_readfirstlane_b32 s74, v13\ns_nop 0\ns_and_b32 s75, s74, 127\n" \
    "s_lshr_b32 s69, s74, 8\ns_sub_u32 s69, s69, s80\ns_bfe_i32 s69, s69, 0x180000\ns_cmp_eq_u32 s75, 0\ns_cselect_b32 s69, 0, s69\ns_add_u32 s80, s80, s69\n" \
    "s_lshl_b32 s74, s74, 5\ns_and_b32 s74, s74, 0x1000\ns_andn2_b32 s81, s81, 0x1000\ns_or_b32 s81, s81, s74\ns_lshl_b32 s74, s74, 2\ns_andn2_b32 s82, s82, 0x4000\n" \
    "s_or_b32 s82, s82, s74\ns_branch .Lq34_%=\n.Lq45_%=:\nv_mov_b32 v252, 0\nv_max3_u32 v252, v252, v72, v73\nv_max3_u32 v252, v252, v74, v75\nv_max3_u32 v252, v252, v76, v77\n" \
    "v_max3_u32 v252, v252, v78, v79\nv_cmp_eq_u32 vcc, -1, v252\ns_nop 0\ns_cbranch_vccz .Lq41_%=\ns_add_u32 s80, s80, s43\ns_xor_b32 s81, s81, 0x1000\n" \
    "s_xor_b32 s90, s90, 0x1000\ns_xor_b32 s82, s82, 0x4000\ns_cmp_eq_u32 s61, 0\ns_cbranch_scc1 .Lq48_%=\ns_lshr_b32 s69, s81, 5\ns_and_b32 s69, s69, 128\n" \
    "s_lshl_b32 s74, s80, 8\ns_add_u32 s74, s74, s69\ns_add_u32 s74, s74, s35\nv_mov_b32 v253, s74\ns_mov_b64 exec, 1\nbuffer_store_dword v253, off, s[64:67], s61 sc1\n" \
    "s_mov_b64 exec, -1\n.Lq48_%=:\ns_add_u32 s68, s68, 1\ns_and_b32 s69, s68, 63\ns_cbranch_scc1 .Lq35_%=\ns_cmp_gt_u32 s68, 0x400000\ns_cbranch_scc1 .Lq1_%=\n" \
    "buffer_load_dword v253, off, s[64:67], 0 sc1\ns_waitcnt vmcnt(0)\nv_readfirstlane_b32 s69, v253\ns_nop 0\ns_cmp_eq_u32 s69, 1\ns_cbranch_scc0 .Lq35_%=\ns_endpgm\n" \
    ".Lq46_%=:\nv_mov_b32 v252, 0\nv_max3_u32 v252, v252, v80, v81\nv_max3_u32 v252, v252, v82, v83\nv_cmp_eq_u32 vcc, -1, v252\ns_nop 0\ns_cbranch_vccz .Lq42_%=\n" \
    "s_add_u32 s80, s80, s43\ns_xor_b32 s81, s81, 0x1000\ns_xor_b32 s90, s90, 0x1000\ns_xor_b32 s82, s82, 0x4000\ns_cmp_eq_u32 s61, 0\ns_cbranch_scc1 .Lq50_%=\n" \
    "s_lshr_b32 s69, s81, 5\ns_and_b32 s69, s69, 128\ns_lshl_b32 s74, s80, 8\ns_add_u32 s74, s74, s69\ns_add_u32 s74, s74, s35\nv_mov_b32 v253, s74\ns_mov_b64 exec, 1\n" \
    "buffer_store_dword v253, off, s[64:67], s61 sc1\ns_mov_b64 exec, -1\n.Lq50_%=:\n.Lq49_%=:\ns_memrealtime s[72:73]\ns_waitcnt vmcnt(0) lgkmcnt(0)\ns_sub_u32 s72, s72, s80\n" \
    "s_cmp_lt_i32 s72, 0\ns_cbranch_scc1 .Lq49_%=\nbuffer_load_dwordx4 v[80:83], v2, s[64:67], s90 offen offset:0 sc1\n" \
    "buffer_load_dwordx4 v[84:87], v2, s[64:67], s90 offen offset:1024 sc1\ns_waitcnt vmcnt(0)\ns_add_u32 s68, s68, 1\ns_and_b32 s69, s68, 63\ns_cbranch_scc1 .Lq46_%=\n" \
    "s_cmp_gt_u32 s68, 0x400000\ns_cbranch_scc1 .Lq1_%=\nbuffer_load_dword v253, off, s[64:67], 0 sc1\ns_waitcnt vmcnt(0)\nv_readfirstlane_b32 s69, v253\ns_nop 0\n" \
    "s_cmp_eq_u32 s69, 1\ns_cbranch_scc0 .Lq46_%=\ns_endpgm\n.Lq47_%=:\nv_mov_b32 v252, 0\nv_max3_u32 v252, v252, v84, v85\nv_max3_u32 v252, v252, v86, v87\n" \
    "v_cmp_eq_u32 vcc, -1, v252\ns_nop 0\ns_cbranch_vccz .Lq43_%=\ns_add_u32 s80, s80, s43\ns_xor_b32 s81, s81, 0x1000\ns_xor_b32 s90, s90, 0x1000\ns_xor_b32 s82, s82, 0x4000\n" \
    "s_cmp_eq_u32 s61, 0\ns_cbranch_scc1 .Lq52_%=\ns_lshr_b32 s69, s81, 5\ns_and_b32 s69, s69, 128\ns_lshl_b32 s74, s80, 8\ns_add_u32 s74, s74, s69\ns_add_u32 s74, s74, s35\n" \
    "v_mov_b32 v253, s74\ns_mov_b64 exec, 1\nbuffer_store_dword v253, off, s[64:67], s61 sc1\ns_mov_b64 exec, -1\n.Lq52_%=:\n.Lq51_%=:\ns_memrealtime s[72:73]\n" \
    "s_waitcnt vmcnt(0) lgkmcnt(0)\ns_sub_u32 s72, s72, s80\ns_cmp_lt_i32 s72, 0\ns_cbranch_scc1 .Lq51_%=\n" \
    "buffer_load_dwordx4 v[84:87], v2, s[64:67], s90 offen offset:1024 sc1\ns_waitcnt vmcnt(0)\ns_add_u32 s68, s68, 1\ns_and_b32 s69, s68, 63\ns_cbranch_scc1 .Lq47_%=\n" \
    "s_cmp_gt_u32 s68, 0x400000\ns_cbranch_scc1 .Lq1_%=\nbuffer_load_dword v253, off, s[64:67], 0 sc1\ns_waitcnt vmcnt(0)\nv_readfirstlane_b32 s69, v253\ns_nop 0\n" \
    "s_cmp_eq_u32 s69, 1\ns_cbranch_scc0 .Lq47_%=\ns_endpgm\n.Lq56_%=:\ns_memrealtime s[72:73]\ns_waitcnt lgkmcnt(0)\ns_sub_u32 s72, s72, s76\ns_cmp_lt_i32 s72, 0\n" \
    "s_cbranch_scc1 .Lq56_%=\nbuffer_load_dword v13, off, s[64:67], s42 sc1\ns_waitcnt vmcnt(0)\nv_readfirstlane_b32 s74, v13\ns_nop 0\ns_and_b32 s75, s74, 127\n" \
    "s_lshr_b32 s69, s74, 8\ns_sub_u32 s69, s69, s80\ns_bfe_i32 s69, s69, 0x180000\ns_cmp_eq_u32 s75, 0\ns_cselect_b32 s69, 0, s69\ns_add_u32 s80, s80, s69\n" \
    "s_lshl_b32 s74, s74, 5\ns_and_b32 s74, s74, 0x1000\ns_andn2_b32 s81, s81, 0x1000\ns_or_b32 s81, s81, s74\ns_lshl_b32 s74, s74, 2\ns_andn2_b32 s82, s82, 0x4000\n" \
    "s_or_b32 s82, s82, s74\ns_branch .Lq53_%=\n.Lq64_%=:\nv_mov_b32 v252, 0\nv_max3_u32 v252, v252, v72, v73\nv_max3_u32 v252, v252, v74, v75\nv_max3_u32 v252, v252, v76, v77\n" \
    "v_max3_u32 v252, v252, v78, v79\nv_cmp_eq_u32 vcc, -1, v252\ns_nop 0\ns_cbranch_vccz .Lq60_%=\ns_add_u32 s80, s80, s43\ns_xor_b32 s81, s81, 0x1000\n" \
    "s_xor_b32 s90, s90, 0x1000\ns_xor_b32 s82, s82, 0x4000\ns_cmp_eq_u32 s61, 0\ns_cbranch_scc1 .Lq67_%=\ns_lshr_b32 s69, s81, 5\ns_and_b32 s69, s69, 128\n" \
    "s_lshl_b32 s74, s80, 8\ns_add_u32 s74, s74, s69\ns_add_u32 s74, s74, s35\nv_mov_b32 v253, s74\ns_mov_b64 exec, 1\nbuffer_store_dword v253, off, s[64:67], s61 sc1\n" \
    "s_mov_b64 exec, -1\n.Lq67_%=:\ns_add_u32 s68, s68, 1\ns_and_b32 s69, s68, 63\ns_cbranch_scc1 .Lq54_%=\ns_cmp_gt_u32 s68, 0x400000\ns_cbranch_scc1 .Lq1_%=\n" \
    "buffer_load_dword v253, off, s[64:67], 0 sc1\ns_waitcnt vmcnt(0)\nv_readfirstlane_b32 s69, v253\ns_nop 0\ns_cmp_eq_u32 s69, 1\ns_cbranch_scc0 .Lq54_%=\ns_endpgm\n" \
    ".Lq65_%=:\nv_mov_b32 v252, 0\nv_max3_u32 v252, v252, v80, v81\nv_max3_u32 v252, v252, v82, v83\nv_cmp_eq_u32 vcc, -1, v252\ns_nop 0\ns_cbranch_vccz .Lq61_%=\n" \
    "s_add_u32 s80, s80, s43\ns_xor_b32 s81, s81, 0x1000\ns_xor_b32 s90, s90, 0x1000\ns_xor_b32 s82, s82, 0x4000\ns_cmp_eq_u32 s61, 0\ns_cbranch_scc1 .Lq69_%=\n" \
    "s_lshr_b32 s69, s81, 5\ns_and_b32 s69, s69, 128\ns_lshl_b32 s74, s80, 8\ns_add_u32 s74, s74, s69\ns_add_u32 s74, s74, s35\nv_mov_b32 v253, s74\ns_mov_b64 exec, 1\n" \
    "buffer_store_dword v253, off, s[64:67], s61 sc1\ns_mov_b64 exec, -1\n.Lq69_%=:\n.Lq68_%=:\ns_memrealtime s[72:73]\ns_waitcnt vmcnt(0) lgkmcnt(0)\ns_sub_u32 s72, s72, s80\n" \
    "s_cmp_lt_i32 s72, 0\ns_cbranch_scc1 .Lq68_%=\nbuffer_load_dwordx4 v[80:83], v2, s[64:67], s90 offen offset:0 sc1\n" \
    "buffer_load_dwordx4 v[84:87], v2, s[64:67], s90 offen offset:1024 sc1\ns_waitcnt vmcnt(0)\ns_add_u32 s68, s68, 1\ns_and_b32 s69, s68, 63\ns_cbranch_scc1 .Lq65_%=\n" \
    "s_cmp_gt_u32 s68, 0x400000\ns_cbranch_scc1 .Lq1_%=\nbuffer_load_dword v253, off, s[64:67], 0 sc1\ns_waitcnt vmcnt(0)\nv_readfirstlane_b32 s69, v253\ns_nop 0\n" \
    "s_cmp_eq_u32 s69, 1\ns_cbranch_scc0 .Lq65_%=\ns_endpgm\n.Lq66_%=:\nv_mov_b32 v252, 0\nv_max3_u32 v252, v252, v84, v85\nv_max3_u32 v252, v252, v86, v87\n" \
    "v_cmp_eq_u32 vcc, -1, v252\ns_nop 0\ns_cbranch_vccz .Lq62_%=\ns_add_u32 s80, s80, s43\ns_xor_b32 s81, s81, 0x1000\ns_xor_b32 s90, s90, 0x1000\ns_xor_b32 s82, s82, 0x4000\n" \
    "s_cmp_eq_u32 s61, 0\ns_cbranch_scc1 .Lq71_%=\ns_lshr_b32 s69, s81, 5\ns_and_b32 s69, s69, 128\ns_lshl_b32 s74, s80, 8\ns_add_u32 s74, s74, s69\ns_add_u32 s74, s74, s35\n" \
    "v_mov_b32 v253, s74\ns_mov_b64 exec, 1\nbuffer_store_dword v253, off, s[64:67], s61 sc1\ns_mov_b64 exec, -1\n.Lq71_%=:\n.Lq70_%=:\ns_memrealtime s[72:73]\n" \
    "s_waitcnt vmcnt(0) lgkmcnt(0)\ns_sub_u32 s72, s72, s80\ns_cmp_lt_i32 s72, 0\ns_cbranch_scc1 .Lq70_%=\n" \
    "buffer_load_dwordx4 v[84:87], v2, s[64:67], s90 offen offset:1024 sc1\ns_waitcnt vmcnt(0)\ns_add_u32 s68, s68, 1\ns_and_b32 s69, s68, 63\ns_cbranch_scc1 .Lq66_%=\n" \
    "s_cmp_gt_u32 s68, 0x400000\ns_cbranch_scc1 .Lq1_%=\nbuffer_load_dword v253, off, s[64:67], 0 sc1\ns_waitcnt vmcnt(0)\nv_readfirstlane_b32 s69, v253\ns_nop 0\n" \
    "s_cmp_eq_u32 s69, 1\ns_cbranch_scc0 .Lq66_%=\ns_endpgm\n.Lq75_%=:\ns_memrealtime s[72:73]\ns_waitcnt lgkmcnt(0)\ns_sub_u32 s72, s72, s76\ns_cmp_lt_i32 s72, 0\n" \
    "s_cbranch_scc1 .Lq75_%=\nbuffer_load_dword v13, off, s[64:67], s42 sc1\ns_waitcnt vmcnt(0)\nv_readfirstlane_b32 s74, v13\ns_nop 0\ns_and_b32 s75, s74, 127\n" \
    "s_lshr_b32 s69, s74, 8\ns_sub_u32 s69, s69, s80\ns_bfe_i32 s69, s69, 0x180000\ns_cmp_eq_u32 s75, 0\ns_cselect_b32 s69, 0, s69\ns_add_u32 s80, s80, s69\n" \
    "s_lshl_b32 s74, s74, 5\ns_and_b32 s74, s74, 0x1000\ns_andn2_b32 s81, s81, 0x1000\ns_or_b32 s81, s81, s74\ns_lshl_b32 s74, s74, 2\ns_andn2_b32 s82, s82, 0x4000\n" \
    "s_or_b32 s82, s82, s74\ns_branch .Lq72_%=\n.Lq83_%=:\nv_mov_b32 v252, 0\nv_max3_u32 v252, v252, v72, v73\nv_max3_u32 v252, v252, v74, v75\nv_max3_u32 v252, v252, v76, v77\n" \
    "v_max3_u32 v252, v252, v78, v79\nv_cmp_eq_u32 vcc, -1, v252\ns_nop 0\ns_cbranch_vccz .Lq79_%=\ns_add_u32 s80, s80, s43\ns_xor_b32 s81, s81, 0x1000\n" \
    "s_xor_b32 s90, s90, 0x1000\ns_xor_b32 s82, s82, 0x4000\ns_cmp_eq_u32 s61, 0\ns_cbranch_scc1 .Lq86_%=\ns_lshr_b32 s69, s81, 5\ns_and_b32 s69, s69, 128\n" \
    "s_lshl_b32 s74, s80, 8\ns_add_u32 s74, s74, s69\ns_add_u32 s74, s74, s35\nv_mov_b32 v253, s74\ns_mov_b64 exec, 1\nbuffer_store_dword v253, off, s[64:67], s61 sc1\n" \
    "s_mov_b64 exec, -1\n.Lq86_%=:\ns_add_u32 s68, s68, 1\ns_and_b32 s69, s68, 63\ns_cbranch_scc1 .Lq73_%=\ns_cmp_gt_u32 s68, 0x400000\ns_cbranch_scc1 .Lq1_%=\n" \
    "buffer_load_dword v253, off, s[64:67], 0 sc1\ns_waitcnt vmcnt(0)\nv_readfirstlane_b32 s69, v253\ns_nop 0\ns_cmp_eq_u32 s69, 1\ns_cbranch_scc0 .Lq73_%=\ns_endpgm\n" \
    ".Lq84_%=:\nv_mov_b32 v252, 0\nv_max3_u32 v252, v252, v80, v81\nv_max3_u32 v252, v252, v82, v83\nv_cmp_eq_u32 vcc, -1, v252\ns_nop 0\ns_cbranch_vccz .Lq80_%=\n" \
    "s_add_u32 s80, s80, s43\ns_xor_b32 s81, s81, 0x1000\ns_xor_b32 s90, s90, 0x1000\ns_xor_b32 s82, s82, 0x4000\ns_cmp_eq_u32 s61, 0\ns_cbranch_scc1 .Lq88_%=\n" \
    "s_lshr_b32 s69, s81, 5\ns_and_b32 s69, s69, 128\ns_lshl_b32 s74, s80, 8\ns_add_u32 s74, s74, s69\ns_add_u32 s74, s74, s35\nv_mov_b32 v253, s74\ns_mov_b64 exec, 1\n" \
    "buffer_store_dword v253, off, s[64:67], s61 sc1\ns_mov_b64 exec, -1\n.Lq88_%=:\n.Lq87_%=:\ns_memrealtime s[72:73]\ns_waitcnt vmcnt(0) lgkmcnt(0)\ns_sub_u32 s72, s72, s80\n" \
    "s_cmp_lt_i32 s72, 0\ns_cbranch_scc1 .Lq87_%=\nbuffer_load_dwordx4 v[80:83], v2, s[64:67], s90 offen offset:0 sc1\n" \
    "buffer_load_dwordx4 v[84:87], v2, s[64:67], s90 offen offset:1024 sc1\ns_waitcnt vmcnt(0)\ns_add_u32 s68, s68, 1\ns_and_b32 s69, s68, 63\ns_cbranch_scc1 .Lq84_%=\n" \
    "s_cmp_gt_u32 s68, 0x400000\ns_cbranch_scc1 .Lq1_%=\nbuffer_load_dword v253, off, s[64:67], 0 sc1\ns_waitcnt vmcnt(0)\nv_readfirstlane_b32 s69, v253\ns_nop 0\n" \
    "s_cmp_eq_u32 s69, 1\ns_cbranch_scc0 .Lq84_%=\ns_endpgm\n.Lq85_%=:\nv_mov_b32 v252, 0\nv_max3_u32 v252, v252, v84, v85\nv_max3_u32 v252, v252, v86, v87\n" \
    "v_cmp_eq_u32 vcc, -1, v252\ns_nop 0\ns_cbranch_vccz .Lq81_%=\ns_add_u32 s80, s80, s43\ns_xor_b32 s81, s81, 0x1000\ns_xor_b32 s90, s90, 0x1000\ns_xor_b32 s82, s82, 0x4000\n" \
    "s_cmp_eq_u32 s61, 0\ns_cbranch_scc1 .Lq90_%=\ns_lshr_b32 s69, s81, 5\ns_and_b32 s69, s69, 128\ns_lshl_b32 s74, s80, 8\ns_add_u32 s74, s74, s69\ns_add_u32 s74, s74, s35\n" \
    "v_mov_b32 v253, s74\ns_mov_b64 exec, 1\nbuffer_store_dword v253, off, s[64:67], s61 sc1\ns_mov_b64 exec, -1\n.Lq90_%=:\n.Lq89_%=:\ns_memrealtime s[72:73]\n" \
    "s_waitcnt vmcnt(0) lgkmcnt(0)\ns_sub_u32 s72, s72, s80\ns_cmp_lt_i32 s72, 0\ns_cbranch_scc1 .Lq89_%=\n" \
    "buffer_load_dwordx4 v[84:87], v2, s[64:67], s90 offen offset:1024 sc1\ns_waitcnt vmcnt(0)\ns_add_u32 s68, s68, 1\ns_and_b32 s69, s68, 63\ns_cbranch_scc1 .Lq85_%=\n" \
    "s_cmp_gt_u32 s68, 0x400000\ns_cbranch_scc1 .Lq1_%=\nbuffer_load_dword v253, off, s[64:67], 0 sc1\ns_waitcnt vmcnt(0)\nv_readfirstlane_b32 s69, v253\ns_nop 0\n" \
    "s_cmp_eq_u32 s69, 1\ns_cbranch_scc0 .Lq85_%=\ns_endpgm\n.Lq14_%=:\ns_mov_b32 s68, 0\n.Lq91_%=:\nds_read_b128 v[36:39], v6 offset:0\nds_read_b128 v[44:47], v8 offset:0\n" \
    "ds_read_b128 v[64:67], v6 offset:512\nds_read_b128 v[84:87], v8 offset:512\nds_read_b128 v[68:71], v6 offset:1024\nds_read_b128 v[88:91], v8 offset:1024\n" \
    "ds_read_b128 v[40:43], v6 offset:1536\nds_read_b128 v[28:31], v8 offset:1536\nv_mov_b32 v252, 0\ns_waitcnt lgkmcnt(0)\nv_pk_max_u16 v252, v252, v36\n" \
    "v_pk_max_u16 v252, v252, v37\nv_pk_max_u16 v252, v252, v38\nv_pk_max_u16 v252, v252, v39\nv_pk_max_u16 v252, v252, v64\nv_pk_max_u16 v252, v252, v65\n" \
    "v_pk_max_u16 v252, v252, v66\nv_pk_max_u16 v252, v252, v67\nv_pk_max_u16 v252, v252, v68\nv_pk_max_u16 v252, v252, v69\nv_pk_max_u16 v252, v252, v70\n" \
    "v_pk_max_u16 v252, v252, v71\nv_pk_max_u16 v252, v252, v40\nv_pk_max_u16 v252, v252, v41\nv_pk_max_u16 v252, v252, v42\nv_pk_max_u16 v252, v252, v43\n" \
    "v_pk_max_u16 v252, v252, v44\nv_pk_max_u16 v252, v252, v45\nv_pk_max_u16 v252, v252, v46\nv_pk_max_u16 v252, v252, v47\nv_pk_max_u16 v252, v252, v84\n" \
    "v_pk_max_u16 v252, v252, v85\nv_pk_max_u16 v252, v252, v86\nv_pk_max_u16 v252, v252, v87\nv_pk_max_u16 v252, v252, v88\nv_pk_max_u16 v252, v252, v89\n" \
    "v_pk_max_u16 v252, v252, v90\nv_pk_max_u16 v252, v252, v91\nv_pk_max_u16 v252, v252, v28\nv_pk_max_u16 v252, v252, v29\nv_pk_max_u16 v252, v252, v30\n" \
    "v_pk_max_u16 v252, v252, v31\nv_pk_max_u16 v252, v252, v252 op_sel:[0,1] op_sel_hi:[1,0]\nv_cmp_eq_u32 vcc, -1, v252\ns_nop 0\ns_cbranch_vccnz .Lq92_%=\n" \
    "v_mfma_f32_16x16x32_bf16 v[16:19], v[48:51], v[36:39], 0\nv_mfma_f32_16x16x32_bf16 v[20:23], v[52:55], v[36:39], 0\n" \
    "v_mfma_f32_16x16x32_bf16 v[16:19], v[72:75], v[64:67], v[16:19]\nv_mfma_f32_16x16x32_bf16 v[20:23], v[76:79], v[64:67], v[20:23]\n" \
    "v_mfma_f32_16x16x32_bf16 v[16:19], v[80:83], v[68:71], v[16:19]\nv_mfma_f32_16x16x32_bf16 v[20:23], v[56:59], v[68:71], v[20:23]\n" \
    "v_mfma_f32_16x16x32_bf16 v[16:19], v[60:63], v[40:43], v[16:19]\nv_mfma_f32_16x16x32_bf16 v[20:23], v[32:35], v[40:43], v[20:23]\n" \
    "v_mfma_f32_16x16x32_bf16 v[24:27], v[44:47], v[36:39], 0\nv_mfma_f32_16x16x32_bf16 v[24:27], v[84:87], v[64:67], v[24:27]\n" \
    "v_mfma_f32_16x16x32_bf16 v[24:27], v[88:91], v[68:71], v[24:27]\nv_mfma_f32_16x16x32_bf16 v[24:27], v[28:31], v[40:43], v[24:27]\ns_nop 3\n" \
    "v_mov_b32_dpp v16, v20 quad_perm:[0,1,2,3] row_mask:0xf bank_mask:0xc\nv_mov_b32_dpp v17, v21 quad_perm:[0,1,2,3] row_mask:0xf bank_mask:0xc\n" \
    "v_mov_b32_dpp v18, v22 quad_perm:[0,1,2,3] row_mask:0xf bank_mask:0xc\nv_mov_b32_dpp v19, v23 quad_perm:[0,1,2,3] row_mask:0xf bank_mask:0xc\n" \
    "v_max3_f32 v254, v16, v17, v18\nv_max_f32 v254, v254, v19\ns_nop 1\nv_max_f32_dpp v252, v254, v254 row_ror:8 row_mask:0xf bank_mask:0xf\n" \
    "v_max_f32_dpp v253, v254, v254 row_ror:8 row_mask:0xf bank_mask:0xf\ns_nop 1\nv_permlane32_swap_b32 v252, v253\nv_max_f32 v252, v252, v253\n" \
    "v_max3_f32 v254, v252, v24, v25\nv_max3_f32 v252, v254, v26, v27\nv_max3_f32 v253, v254, v26, v27\ns_nop 1\nv_permlane16_swap_b32 v252, v253\nv_max_f32 v252, v252, v253\n" \
    "v_pk_add_f32 v[16:17], v[16:17], v[252:253] op_sel_hi:[1,0] neg_lo:[0,1] neg_hi:[0,1]\n" \
    "v_pk_add_f32 v[18:19], v[18:19], v[252:253] op_sel_hi:[1,0] neg_lo:[0,1] neg_hi:[0,1]\n" \
    "v_pk_add_f32 v[24:25], v[24:25], v[252:253] op_sel_hi:[1,0] neg_lo:[0,1] neg_hi:[0,1]\n" \
    "v_pk_add_f32 v[26:27], v[26:27], v[252:253] op_sel_hi:[1,0] neg_lo:[0,1] neg_hi:[0,1]\nv_exp_f32 v16, v16\nv_exp_f32 v17, v17\nv_exp_f32 v18, v18\nv_exp_f32 v19, v19\n" \
    "v_exp_f32 v24, v24\nv_pk_add_f32 v[254:255], v[16:17], v[18:19]\nv_exp_f32 v25, v25\nv_add_f32 v254, v254, v255\nv_exp_f32 v26, v26\nv_exp_f32 v27, v27\n" \
    "v_add_f32_dpp v252, v254, v254 row_ror:8 row_mask:0xf bank_mask:0xf\nv_add_f32_dpp v253, v254, v254 row_ror:8 row_mask:0xf bank_mask:0xf\n" \
    "v_pk_add_f32 v[254:255], v[24:25], v[26:27]\ns_nop 0\nv_permlane32_swap_b32 v252, v253\nv_pk_add_f32 v[254:255], v[254:255], v[252:253]\n" \
    "v_pk_add_f32 v[252:253], v[254:255], v[254:255] op_sel:[0,1] op_sel_hi:[1,0]\ns_nop 1\nv_permlane16_swap_b32 v252, v253\nv_add_f32 v252, v252, v253\nv_rcp_f32 v252, v252\n" \
    "s_nop 0\nv_pk_mul_f32 v[16:17], v[16:17], v[252:253] op_sel_hi:[1,0]\nv_pk_mul_f32 v[24:25], v[24:25], v[252:253] op_sel_hi:[1,0]\nv_cvt_pk_bf16_f32 v20, v16, v17\n" \
    "v_cvt_pk_bf16_f32 v22, v24, v25\nv_pk_mul_f32 v[18:19], v[18:19], v[252:253] op_sel_hi:[1,0]\nv_pk_mul_f32 v[26:27], v[26:27], v[252:253] op_sel_hi:[1,0]\n" \
    "v_cvt_pk_bf16_f32 v21, v18, v19\nv_cvt_pk_bf16_f32 v23, v26, v27\nv_permlane32_swap_b32 v20, v22\ns_nop 0\nv_permlane32_swap_b32 v21, v23\n" \
    "buffer_store_dwordx4 v[20:23], v7, s[64:67], s82 offen sc1\nbuffer_store_dwordx4 v[20:23], v7, s[64:67], s74 offen sc1\ns_branch .Lq3_%=\n.Lq92_%=:\ns_add_u32 s68, s68, 1\n" \
    "s_and_b32 s69, s68, 63\ns_cbranch_scc1 .Lq91_%=\ns_cmp_gt_u32 s68, 0x400000\ns_cbranch_scc1 .Lq1_%=\nbuffer_load_dword v253, off, s[64:67], 0 sc1\ns_waitcnt vmcnt(0)\n" \
    "v_readfirstlane_b32 s69, v253\ns_nop 0\ns_cmp_eq_u32 s69, 1\ns_cbranch_scc0 .Lq91_%=\ns_endpgm\n.Lq1_%=:\nv_mov_b32 v253, 1\nbuffer_store_dword v253, off, s[64:67], 0 sc1\n" \
    "s_endpgm\n"
#define ASM_V \
    "s_add_u32 m0, s41, 0\ns_xor_b32 s85, s62, 0\ns_add_u32 s85, s85, s40\nbuffer_load_dwordx4 v11, s[64:67], s85 offen sc1 lds\n" \
    "buffer_load_dwordx4 v12, s[64:67], s85 offen offset:1024 sc1 lds\ns_add_u32 m0, s41, 8192\ns_xor_b32 s85, s62, 256\ns_add_u32 s85, s85, s40\n" \
    "buffer_load_dwordx4 v11, s[64:67], s85 offen sc1 lds\nbuffer_load_dwordx4 v12, s[64:67], s85 offen offset:1024 sc1 lds\ns_add_u32 m0, s41, 16384\ns_xor_b32 s85, s62, 512\n" \
    "s_add_u32 s85, s85, s40\nbuffer_load_dwordx4 v11, s[64:67], s85 offen sc1 lds\nbuffer_load_dwordx4 v12, s[64:67], s85 offen offset:1024 sc1 lds\ns_add_u32 m0, s41, 40960\n" \
    "s_xor_b32 s85, s62, 768\ns_add_u32 s85, s85, s40\nbuffer_load_dwordx4 v11, s[64:67], s85 offen sc1 lds\nbuffer_load_dwordx4 v12, s[64:67], s85 offen offset:1024 sc1 lds\n" \
    "s_memrealtime s[72:73]\ns_waitcnt lgkmcnt(0)\ns_mov_b32 s80, s72\ns_waitcnt vmcnt(0)\ns_barrier\nds_read_b128 v[56:59], v3 offset:0\nds_read_b128 v[60:63], v9 offset:0\n" \
    "ds_read_b128 v[64:67], v9 offset:4096\nds_read_b128 v[68:71], v3 offset:1024\nds_read_b128 v[72:75], v10 offset:0\nds_read_b128 v[76:79], v10 offset:4096\n" \
    "s_waitcnt lgkmcnt(3)\nv_mfma_f32_16x16x32_bf16 v[16:19], a[0:3], v[60:63], 0\ns_add_u32 m0, s41, 24576\ns_xor_b32 s85, s62, 1024\n" \
    "v_mfma_f32_16x16x32_bf16 v[24:27], v[56:59], v[60:63], 0\nds_read_b128 v[60:63], v3 offset:2048\ns_add_u32 s85, s85, s40\n" \
    "v_mfma_f32_16x16x32_bf16 v[20:23], a[0:3], v[64:67], 0\nds_read_b128 v[80:83], v9 offset:1024\nbuffer_load_dwordx4 v11, s[64:67], s85 offen sc1 lds\n" \
    "v_mfma_f32_16x16x32_bf16 v[28:31], v[56:59], v[64:67], 0\nds_read_b128 v[56:59], v9 offset:5120\ns_waitcnt lgkmcnt(3)\n" \
    "v_mfma_f32_16x16x32_bf16 v[16:19], a[4:7], v[72:75], v[16:19]\nv_mfma_f32_16x16x32_bf16 v[24:27], v[68:71], v[72:75], v[24:27]\nds_read_b128 v[64:67], v3 offset:3072\n" \
    "buffer_load_dwordx4 v12, s[64:67], s85 offen offset:1024 sc1 lds\nv_mfma_f32_16x16x32_bf16 v[20:23], a[4:7], v[76:79], v[20:23]\nds_read_b128 v[72:75], v10 offset:1024\n" \
    "s_add_u32 m0, s41, 32768\nv_mfma_f32_16x16x32_bf16 v[28:31], v[68:71], v[76:79], v[28:31]\nds_read_b128 v[68:71], v10 offset:5120\ns_xor_b32 s85, s62, 1280\n" \
    "s_waitcnt lgkmcnt(3)\nv_mfma_f32_16x16x32_bf16 v[16:19], a[8:11], v[80:83], v[16:19]\ns_add_u32 s85, s85, s40\nbuffer_load_dwordx4 v11, s[64:67], s85 offen sc1 lds\n" \
    "v_mfma_f32_16x16x32_bf16 v[24:27], v[60:63], v[80:83], v[24:27]\nds_read_b128 v[76:79], v3 offset:4096\nv_mfma_f32_16x16x32_bf16 v[20:23], a[8:11], v[56:59], v[20:23]\n" \
    "ds_read_b128 v[80:83], v9 offset:8192\nv_mfma_f32_16x16x32_bf16 v[28:31], v[60:63], v[56:59], v[28:31]\nds_read_b128 v[56:59], v9 offset:12288\n" \
    "buffer_load_dwordx4 v12, s[64:67], s85 offen offset:1024 sc1 lds\ns_waitcnt lgkmcnt(3)\nv_mfma_f32_16x16x32_bf16 v[16:19], a[12:15], v[72:75], v[16:19]\n" \
    "s_add_u32 s84, s40, 0x20000\ns_cmp_lt_u32 s49, 62\ns_cselect_b32 s84, s84, s40\nv_mfma_f32_16x16x32_bf16 v[24:27], v[64:67], v[72:75], v[24:27]\n" \
    "ds_read_b128 v[60:63], v3 offset:5120\nv_mov_b64 v[252:253], -1\nv_mov_b64 v[254:255], -1\nv_mfma_f32_16x16x32_bf16 v[20:23], a[12:15], v[68:71], v[20:23]\n" \
    "ds_read_b128 v[72:75], v10 offset:8192\ns_and_b32 s69, s49, 15\nv_mfma_f32_16x16x32_bf16 v[28:31], v[64:67], v[68:71], v[28:31]\nds_read_b128 v[64:67], v10 offset:12288\n" \
    "s_lshl_b32 s69, s69, 19\ns_waitcnt lgkmcnt(3)\nv_mfma_f32_16x16x32_bf16 v[16:19], a[16:19], v[80:83], v[16:19]\ns_add_u32 s82, s38, s69\ns_lshr_b32 s69, s82, 8\n" \
    "v_mfma_f32_16x16x32_bf16 v[24:27], v[76:79], v[80:83], v[24:27]\nds_read_b128 v[68:71], v3 offset:6144\ns_and_b32 s69, s69, 0x2000\n" \
    "v_mfma_f32_16x16x32_bf16 v[20:23], a[16:19], v[56:59], v[20:23]\nds_read_b128 v[80:83], v9 offset:9216\ns_xor_b32 s82, s82, s69\n" \
    "v_mfma_f32_16x16x32_bf16 v[28:31], v[76:79], v[56:59], v[28:31]\nds_read_b128 v[56:59], v9 offset:13312\ns_and_b32 s69, s35, 15\ns_waitcnt lgkmcnt(3)\n" \
    "v_mfma_f32_16x16x32_bf16 v[16:19], a[20:23], v[72:75], v[16:19]\ns_lshl_b32 s69, s69, 19\ns_add_u32 s0, s38, s69\n" \
    "v_mfma_f32_16x16x32_bf16 v[24:27], v[60:63], v[72:75], v[24:27]\nds_read_b128 v[72:75], v3 offset:7168\ns_lshr_b32 s69, s0, 8\n" \
    "v_mfma_f32_16x16x32_bf16 v[20:23], a[20:23], v[64:67], v[20:23]\nds_read_b128 v[76:79], v10 offset:9216\ns_and_b32 s69, s69, 0x2000\n" \
    "v_mfma_f32_16x16x32_bf16 v[28:31], v[60:63], v[64:67], v[28:31]\nds_read_b128 v[60:63], v10 offset:13312\ns_xor_b32 s0, s0, s69\ns_waitcnt lgkmcnt(3)\n" \
    "v_mfma_f32_16x16x32_bf16 v[16:19], a[24:27], v[80:83], v[16:19]\ns_add_u32 s1, s0, 0x10000\ns_add_u32 s2, s0, 0x20000\n" \
    "v_mfma_f32_16x16x32_bf16 v[24:27], v[68:71], v[80:83], v[24:27]\nds_read_b128 v[64:67], v3 offset:8192\ns_add_u32 s3, s0, 0x30000\nv_lshrrev_b32 v15, 1, v2\n" \
    "v_mfma_f32_16x16x32_bf16 v[20:23], a[24:27], v[56:59], v[20:23]\nds_read_b128 v[80:83], v9 offset:16384\nv_add_u32 v15, s60, v15\n" \
    "v_mfma_f32_16x16x32_bf16 v[28:31], v[68:71], v[56:59], v[28:31]\nds_read_b128 v[56:59], v9 offset:20480\ns_waitcnt lgkmcnt(3)\n" \
    "v_mfma_f32_16x16x32_bf16 v[16:19], a[28:31], v[76:79], v[16:19]\nds_write_b64 v15, v[252:253]\nv_mfma_f32_16x16x32_bf16 v[24:27], v[72:75], v[76:79], v[24:27]\n" \
    "ds_read_b128 v[68:71], v3 offset:9216\nv_mfma_f32_16x16x32_bf16 v[20:23], a[28:31], v[60:63], v[20:23]\nds_read_b128 v[76:79], v10 offset:16384\n" \
    "v_mfma_f32_16x16x32_bf16 v[28:31], v[72:75], v[60:63], v[28:31]\nds_read_b128 v[60:63], v10 offset:20480\ns_waitcnt lgkmcnt(4)\n" \
    "v_mfma_f32_16x16x32_bf16 v[16:19], a[32:35], v[80:83], v[16:19]\nv_mfma_f32_16x16x32_bf16 v[24:27], v[64:67], v[80:83], v[24:27]\nds_read_b128 v[72:75], v3 offset:10240\n" \
    "v_mfma_f32_16x16x32_bf16 v[20:23], a[32:35], v[56:59], v[20:23]\nds_read_b128 v[80:83], v9 offset:17408\nbuffer_load_dword v13, off, s[64:67], s42 sc1\n" \
    "v_mfma_f32_16x16x32_bf16 v[28:31], v[64:67], v[56:59], v[28:31]\nds_read_b128 v[56:59], v9 offset:21504\ns_and_b32 s69, s49, 255\ns_waitcnt lgkmcnt(3)\n" \
    "v_mfma_f32_16x16x32_bf16 v[16:19], a[36:39], v[76:79], v[16:19]\ns_lshl_b32 s69, s69, 19\ns_add_u32 s74, s63, s69\n" \
    "v_mfma_f32_16x16x32_bf16 v[24:27], v[68:71], v[76:79], v[24:27]\nds_read_b128 v[64:67], v3 offset:11264\ns_cmp_lt_u32 s49, 64\ns_cselect_b32 s74, s74, 0x7fff0000\n" \
    "v_mfma_f32_16x16x32_bf16 v[20:23], a[36:39], v[60:63], v[20:23]\nds_read_b128 v[76:79], v10 offset:17408\nv_mfma_f32_16x16x32_bf16 v[28:31], v[68:71], v[60:63], v[28:31]\n" \
    "ds_read_b128 v[60:63], v10 offset:21504\ns_waitcnt lgkmcnt(3)\nv_mfma_f32_16x16x32_bf16 v[16:19], a[40:43], v[80:83], v[16:19]\n" \
    "v_mfma_f32_16x16x32_bf16 v[24:27], v[72:75], v[80:83], v[24:27]\nds_read_b128 v[68:71], v3 offset:12288\ns_barrier\n" \
    "v_mfma_f32_16x16x32_bf16 v[20:23], a[40:43], v[56:59], v[20:23]\nds_read_b128 v[80:83], v9 offset:40960\nv_mfma_f32_16x16x32_bf16 v[28:31], v[72:75], v[56:59], v[28:31]\n" \
    "ds_read_b128 v[56:59], v9 offset:45056\ns_add_u32 m0, s41, 0\ns_waitcnt lgkmcnt(3)\nv_mfma_f32_16x16x32_bf16 v[16:19], a[44:47], v[76:79], v[16:19]\n" \
    "s_xor_b32 s85, s62, 1536\ns_add_u32 s85, s85, s40\nv_mfma_f32_16x16x32_bf16 v[24:27], v[64:67], v[76:79], v[24:27]\nds_read_b128 v[72:75], v3 offset:13312\n" \
    "buffer_load_dwordx4 v11, s[64:67], s85 offen sc1 lds\nv_mfma_f32_16x16x32_bf16 v[20:23], a[44:47], v[60:63], v[20:23]\nds_read_b128 v[76:79], v10 offset:40960\n" \
    "buffer_store_dwordx4 v[252:255], v2, s[64:67], s74 offen offset:0 sc1\nv_mfma_f32_16x16x32_bf16 v[28:31], v[64:67], v[60:63], v[28:31]\n" \
    "ds_read_b128 v[60:63], v10 offset:45056\ns_waitcnt lgkmcnt(3)\nv_mfma_f32_16x16x32_bf16 v[16:19], a[48:51], v[80:83], v[16:19]\n" \
    "buffer_load_dwordx4 v12, s[64:67], s85 offen offset:1024 sc1 lds\ns_add_u32 m0, s41, 8192\nv_mfma_f32_16x16x32_bf16 v[24:27], v[68:71], v[80:83], v[24:27]\n" \
    "ds_read_b128 v[64:67], v3 offset:14336\ns_xor_b32 s85, s62, 1792\nv_mfma_f32_16x16x32_bf16 v[20:23], a[48:51], v[56:59], v[20:23]\nds_read_b128 v[80:83], v9 offset:41984\n" \
    "s_add_u32 s85, s85, s40\nv_mfma_f32_16x16x32_bf16 v[28:31], v[68:71], v[56:59], v[28:31]\nds_read_b128 v[56:59], v9 offset:46080\n" \
    "buffer_load_dwordx4 v11, s[64:67], s85 offen sc1 lds\ns_waitcnt lgkmcnt(3)\nv_mfma_f32_16x16x32_bf16 v[16:19], a[52:55], v[76:79], v[16:19]\n" \
    "buffer_store_dwordx4 v[252:255], v2, s[64:67], s74 offen offset:1024 sc1\nv_mfma_f32_16x16x32_bf16 v[24:27], v[72:75], v[76:79], v[24:27]\n" \
    "ds_read_b128 v[68:71], v3 offset:15360\nv_mfma_f32_16x16x32_bf16 v[20:23], a[52:55], v[60:63], v[20:23]\nds_read_b128 v[76:79], v10 offset:41984\n" \
    "buffer_load_dwordx4 v12, s[64:67], s85 offen offset:1024 sc1 lds\nv_mfma_f32_16x16x32_bf16 v[28:31], v[72:75], v[60:63], v[28:31]\nds_read_b128 v[60:63], v10 offset:46080\n" \
    "s_waitcnt lgkmcnt(3)\nv_mfma_f32_16x16x32_bf16 v[16:19], a[56:59], v[80:83], v[16:19]\nbuffer_store_dwordx4 v[252:255], v7, s[64:67], s82 offen sc1\n" \
    "s_add_u32 s82, s82, 0x10000\nv_mfma_f32_16x16x32_bf16 v[24:27], v[64:67], v[80:83], v[24:27]\nds_read_b128 v[72:75], v3 offset:16384\ns_waitcnt vmcnt(8)\ns_barrier\n" \
    "v_mfma_f32_16x16x32_bf16 v[20:23], a[56:59], v[56:59], v[20:23]\nds_read_b128 v[80:83], v9 offset:24576\nv_mfma_f32_16x16x32_bf16 v[28:31], v[64:67], v[56:59], v[28:31]\n" \
    "ds_read_b128 v[56:59], v9 offset:28672\ns_waitcnt lgkmcnt(3)\nv_mfma_f32_16x16x32_bf16 v[16:19], a[60:63], v[76:79], v[16:19]\n" \
    "buffer_store_dwordx4 v[252:255], v7, s[64:67], s82 offen sc1\ns_add_u32 s82, s82, 0x10000\nv_mfma_f32_16x16x32_bf16 v[24:27], v[68:71], v[76:79], v[24:27]\n" \
    "ds_read_b128 v[64:67], v3 offset:17408\nv_mfma_f32_16x16x32_bf16 v[20:23], a[60:63], v[60:63], v[20:23]\nds_read_b128 v[76:79], v10 offset:24576\n" \
    "v_mfma_f32_16x16x32_bf16 v[28:31], v[68:71], v[60:63], v[28:31]\nds_read_b128 v[60:63], v10 offset:28672\ns_waitcnt lgkmcnt(3)\n" \
    "v_mfma_f32_16x16x32_bf16 v[16:19], a[64:67], v[80:83], v[16:19]\nbuffer_store_dwordx4 v[252:255], v7, s[64:67], s82 offen sc1\ns_add_u32 s82, s82, 0x10000\n" \
    "v_mfma_f32_16x16x32_bf16 v[24:27], v[72:75], v[80:83], v[24:27]\nds_read_b128 v[68:71], v3 offset:18432\nv_mfma_f32_16x16x32_bf16 v[20:23], a[64:67], v[56:59], v[20:23]\n" \
    "ds_read_b128 v[80:83], v9 offset:25600\nv_mfma_f32_16x16x32_bf16 v[28:31], v[72:75], v[56:59], v[28:31]\nds_read_b128 v[56:59], v9 offset:29696\ns_waitcnt lgkmcnt(3)\n" \
    "v_mfma_f32_16x16x32_bf16 v[16:19], a[68:71], v[76:79], v[16:19]\nbuffer_store_dwordx4 v[252:255], v7, s[64:67], s82 offen sc1\ns_add_u32 s82, s82, 0x10000\n" \
    "v_mfma_f32_16x16x32_bf16 v[24:27], v[64:67], v[76:79], v[24:27]\nds_read_b128 v[72:75], v3 offset:19456\ns_add_u32 s49, s49, 2\n" \
    "v_mfma_f32_16x16x32_bf16 v[20:23], a[68:71], v[60:63], v[20:23]\nds_read_b128 v[76:79], v10 offset:25600\ns_and_b32 s69, s35, 15\n" \
    "v_mfma_f32_16x16x32_bf16 v[28:31], v[64:67], v[60:63], v[28:31]\nds_read_b128 v[60:63], v10 offset:29696\ns_lshl_b32 s69, s69, 17\ns_waitcnt lgkmcnt(3)\n" \
    "v_mfma_f32_16x16x32_bf16 v[16:19], a[72:75], v[80:83], v[16:19]\ns_add_u32 s81, s37, s69\ns_lshr_b32 s69, s81, 8\n" \
    "v_mfma_f32_16x16x32_bf16 v[24:27], v[68:71], v[80:83], v[24:27]\nds_read_b128 v[64:67], v3 offset:20480\ns_and_b32 s69, s69, 0x2000\n" \
    "v_mfma_f32_16x16x32_bf16 v[20:23], a[72:75], v[56:59], v[20:23]\nds_read_b128 v[80:83], v9 offset:32768\ns_xor_b32 s81, s81, s69\n" \
    "v_mfma_f32_16x16x32_bf16 v[28:31], v[68:71], v[56:59], v[28:31]\nds_read_b128 v[56:59], v9 offset:36864\ns_mov_b32 s68, 0\ns_waitcnt lgkmcnt(3)\n" \
    "v_mfma_f32_16x16x32_bf16 v[16:19], a[76:79], v[76:79], v[16:19]\nv_mfma_f32_16x16x32_bf16 v[24:27], v[72:75], v[76:79], v[24:27]\nds_read_b128 v[68:71], v3 offset:21504\n" \
    "v_mfma_f32_16x16x32_bf16 v[20:23], a[76:79], v[60:63], v[20:23]\nds_read_b128 v[76:79], v10 offset:32768\nv_mfma_f32_16x16x32_bf16 v[28:31], v[72:75], v[60:63], v[28:31]\n" \
    "ds_read_b128 v[60:63], v10 offset:36864\ns_waitcnt lgkmcnt(3)\nv_mfma_f32_16x16x32_bf16 v[16:19], a[80:83], v[80:83], v[16:19]\n" \
    "v_mfma_f32_16x16x32_bf16 v[24:27], v[64:67], v[80:83], v[24:27]\nds_read_b128 v[72:75], v3 offset:22528\nv_mfma_f32_16x16x32_bf16 v[20:23], a[80:83], v[56:59], v[20:23]\n" \
    "ds_read_b128 v[80:83], v9 offset:33792\nv_mfma_f32_16x16x32_bf16 v[28:31], v[64:67], v[56:59], v[28:31]\nds_read_b128 v[56:59], v9 offset:37888\ns_waitcnt lgkmcnt(3)\n" \
    "v_mfma_f32_16x16x32_bf16 v[16:19], a[84:87], v[76:79], v[16:19]\nv_mfma_f32_16x16x32_bf16 v[24:27], v[68:71], v[76:79], v[24:27]\nds_read_b128 v[64:67], v3 offset:23552\n" \
    "v_mfma_f32_16x16x32_bf16 v[20:23], a[84:87], v[60:63], v[20:23]\nds_read_b128 v[76:79], v10 offset:33792\nv_mfma_f32_16x16x32_bf16 v[28:31], v[68:71], v[60:63], v[28:31]\n" \
    "ds_read_b128 v[60:63], v10 offset:37888\ns_waitcnt lgkmcnt(3)\nv_mfma_f32_16x16x32_bf16 v[16:19], a[88:91], v[80:83], v[16:19]\n" \
    "v_mfma_f32_16x16x32_bf16 v[24:27], v[72:75], v[80:83], v[24:27]\ns_waitcnt vmcnt(4)\ns_barrier\nv_mfma_f32_16x16x32_bf16 v[20:23], a[88:91], v[56:59], v[20:23]\n" \
    "ds_read_b128 v[68:71], v9 offset:0\nv_mfma_f32_16x16x32_bf16 v[28:31], v[72:75], v[56:59], v[28:31]\nds_read_b128 v[56:59], v9 offset:4096\ns_add_u32 m0, s41, 16384\n" \
    "s_waitcnt lgkmcnt(2)\nv_mfma_f32_16x16x32_bf16 v[16:19], a[92:95], v[76:79], v[16:19]\ns_xor_b32 s85, s62, 512\ns_add_u32 s85, s85, s84\n" \
    "v_mfma_f32_16x16x32_bf16 v[24:27], v[64:67], v[76:79], v[24:27]\nbuffer_load_dwordx4 v11, s[64:67], s85 offen sc1 lds\n" \
    "v_mfma_f32_16x16x32_bf16 v[20:23], a[92:95], v[60:63], v[20:23]\nds_read_b128 v[72:75], v10 offset:0\nv_mfma_f32_16x16x32_bf16 v[28:31], v[64:67], v[60:63], v[28:31]\n" \
    "ds_read_b128 v[60:63], v10 offset:4096\ns_waitcnt lgkmcnt(2)\nv_mfma_f32_16x16x32_bf16 v[16:19], a[96:99], v[68:71], v[16:19]\n" \
    "buffer_load_dwordx4 v12, s[64:67], s85 offen offset:1024 sc1 lds\ns_add_u32 m0, s41, 40960\nv_mfma_f32_16x16x32_bf16 v[24:27], a[104:107], v[68:71], v[24:27]\n" \
    "s_xor_b32 s85, s62, 768\ns_add_u32 s85, s85, s84\nv_mfma_f32_16x16x32_bf16 v[20:23], a[96:99], v[56:59], v[20:23]\nds_read_b128 v[64:67], v9 offset:1024\n" \
    "buffer_load_dwordx4 v11, s[64:67], s85 offen sc1 lds\nv_mfma_f32_16x16x32_bf16 v[28:31], a[104:107], v[56:59], v[28:31]\nds_read_b128 v[56:59], v9 offset:5120\n" \
    "s_waitcnt lgkmcnt(2)\nv_mfma_f32_16x16x32_bf16 v[16:19], a[100:103], v[72:75], v[16:19]\nv_mfma_f32_16x16x32_bf16 v[24:27], a[108:111], v[72:75], v[24:27]\n" \
    "buffer_load_dwordx4 v12, s[64:67], s85 offen offset:1024 sc1 lds\nv_mfma_f32_16x16x32_bf16 v[20:23], a[100:103], v[60:63], v[20:23]\n" \
    "ds_read_b128 v[68:71], v10 offset:1024\nv_mfma_f32_16x16x32_bf16 v[28:31], a[108:111], v[60:63], v[28:31]\nds_read_b128 v[60:63], v10 offset:5120\ns_waitcnt lgkmcnt(2)\n" \
    "v_mfma_f32_16x16x32_bf16 v[16:19], a[112:115], v[64:67], v[16:19]\nv_mfma_f32_16x16x32_bf16 v[24:27], a[120:123], v[64:67], v[24:27]\n" \
    "v_mfma_f32_16x16x32_bf16 v[20:23], a[112:115], v[56:59], v[20:23]\nds_read_b128 v[64:67], v9 offset:8192\n" \
    "v_mfma_f32_16x16x32_bf16 v[28:31], a[120:123], v[56:59], v[28:31]\nds_read_b128 v[56:59], v9 offset:12288\ns_waitcnt lgkmcnt(2)\n" \
    "v_mfma_f32_16x16x32_bf16 v[16:19], a[116:119], v[68:71], v[16:19]\nv_mfma_f32_16x16x32_bf16 v[24:27], a[124:127], v[68:71], v[24:27]\n" \
    "v_mfma_f32_16x16x32_bf16 v[20:23], a[116:119], v[60:63], v[20:23]\nds_read_b128 v[68:71], v10 offset:8192\n" \
    "v_mfma_f32_16x16x32_bf16 v[28:31], a[124:127], v[60:63], v[28:31]\nds_read_b128 v[60:63], v10 offset:12288\ns_waitcnt lgkmcnt(2)\n" \
    "v_mfma_f32_16x16x32_bf16 v[16:19], a[128:131], v[64:67], v[16:19]\nv_readfirstlane_b32 s74, v13\nv_mfma_f32_16x16x32_bf16 v[24:27], a[136:139], v[64:67], v[24:27]\n" \
    "s_and_b32 s75, s74, 127\ns_lshr_b32 s69, s74, 8\nv_mfma_f32_16x16x32_bf16 v[20:23], a[128:131], v[56:59], v[20:23]\nds_read_b128 v[64:67], v9 offset:9216\n" \
    "s_sub_u32 s69, s69, s80\nv_mfma_f32_16x16x32_bf16 v[28:31], a[136:139], v[56:59], v[28:31]\nds_read_b128 v[56:59], v9 offset:13312\ns_bfe_i32 s69, s69, 0x180000\n" \
    "s_waitcnt lgkmcnt(2)\nv_mfma_f32_16x16x32_bf16 v[16:19], a[132:135], v[68:71], v[16:19]\nds_read_b128 v[72:75], v10 offset:9216\ns_cmp_eq_u32 s75, 0\n" \
    "s_cselect_b32 s69, 0, s69\nv_mfma_f32_16x16x32_bf16 v[24:27], a[140:143], v[68:71], v[24:27]\nds_read_b128 v[68:71], v10 offset:13312\ns_add_u32 s80, s80, s69\n" \
    "v_mfma_f32_16x16x32_bf16 v[20:23], a[132:135], v[60:63], v[20:23]\ns_lshl_b32 s74, s74, 5\ns_and_b32 s74, s74, 0x1000\n" \
    "v_mfma_f32_16x16x32_bf16 v[28:31], a[140:143], v[60:63], v[28:31]\ns_andn2_b32 s81, s81, 0x1000\ns_or_b32 s81, s81, s74\ns_waitcnt lgkmcnt(2)\n" \
    "v_mfma_f32_16x16x32_bf16 v[16:19], a[144:147], v[64:67], v[16:19]\nv_mfma_f32_16x16x32_bf16 v[24:27], a[152:155], v[64:67], v[24:27]\ns_waitcnt lgkmcnt(0)\ns_barrier\n" \
    "v_mfma_f32_16x16x32_bf16 v[16:19], a[148:151], v[72:75], v[16:19]\ns_add_u32 m0, s41, 0\ns_xor_b32 s85, s62, 0\n" \
    "v_mfma_f32_16x16x32_bf16 v[24:27], a[156:159], v[72:75], v[24:27]\ns_add_u32 s85, s85, s84\nbuffer_load_dwordx4 v11, s[64:67], s85 offen sc1 lds\n" \
    "v_mfma_f32_16x16x32_bf16 v[20:23], a[144:147], v[56:59], v[20:23]\ns_nop 1\nv_cvt_pk_bf16_f32 v32, v16, v17\nv_cvt_pk_bf16_f32 v33, v18, v19\n" \
    "v_mfma_f32_16x16x32_bf16 v[28:31], a[152:155], v[56:59], v[28:31]\nv_cvt_pk_bf16_f32 v34, v24, v25\nv_cvt_pk_bf16_f32 v35, v26, v27\n" \
    "v_mfma_f32_16x16x32_bf16 v[20:23], a[148:151], v[68:71], v[20:23]\nbuffer_load_dwordx4 v12, s[64:67], s85 offen offset:1024 sc1 lds\ns_add_u32 m0, s41, 8192\n" \
    "v_mfma_f32_16x16x32_bf16 v[28:31], a[156:159], v[68:71], v[28:31]\ns_xor_b32 s85, s62, 256\ns_add_u32 s85, s85, s84\nds_write_b128 v14, v[32:35] offset:0\n" \
    "buffer_load_dwordx4 v11, s[64:67], s85 offen sc1 lds\nbuffer_load_dwordx4 v12, s[64:67], s85 offen offset:1024 sc1 lds\nv_cvt_pk_bf16_f32 v36, v20, v21\n" \
    "v_cvt_pk_bf16_f32 v37, v22, v23\ns_mov_b32 s40, s84\nv_cvt_pk_bf16_f32 v38, v28, v29\nv_cvt_pk_bf16_f32 v39, v30, v31\nds_write_b128 v14, v[36:39] offset:4096\n" \
    "s_waitcnt vmcnt(0) lgkmcnt(0)\ns_barrier\n.Lv5_%=:\nds_read_b128 v[56:59], v6 offset:0\nds_read_b128 v[60:63], v6 offset:1024\nds_read_b128 v[64:67], v6 offset:2048\n" \
    "v_mov_b64 v[32:33], 0\nv_mov_b64 v[34:35], 0\nv_mov_b64 v[36:37], 0\nv_mov_b64 v[38:39], 0\ns_and_b32 s69, s50, 15\ns_lshl_b32 s69, s69, 21\ns_add_u32 s86, s39, s69\n" \
    "s_lshr_b32 s69, s86, 8\ns_and_b32 s69, s69, 0x2000\ns_xor_b32 s86, s86, s69\ns_waitcnt lgkmcnt(2)\nv_smfmac_f32_16x16x64_bf16 v[32:35], v[56:59], a[160:167], v0\n" \
    "ds_read_b128 v[68:71], v6 offset:3072\nv_mov_b64 v[40:41], 0\nv_smfmac_f32_16x16x64_bf16 v[36:39], v[56:59], a[176:183], v0\nds_read_b128 v[72:75], v3 offset:0\n" \
    "s_waitcnt lgkmcnt(3)\nv_mov_b64 v[42:43], 0\nv_smfmac_f32_16x16x64_bf16 v[32:35], v[60:63], a[168:175], v0\nds_read_b128 v[76:79], v9 offset:0\nv_mov_b64 v[44:45], 0\n" \
    "v_smfmac_f32_16x16x64_bf16 v[36:39], v[60:63], a[184:191], v0\nds_read_b128 v[80:83], v9 offset:4096\ns_waitcnt lgkmcnt(4)\nv_mov_b64 v[46:47], 0\n" \
    "v_smfmac_f32_16x16x64_bf16 v[32:35], v[64:67], a[192:199], v0\ns_add_u32 s87, s86, 0x40000\ns_add_u32 s88, s86, 0x80000\n" \
    "v_smfmac_f32_16x16x64_bf16 v[36:39], v[64:67], a[208:215], v0\ns_add_u32 s89, s86, 0xc0000\ns_add_u32 s50, s50, 2\ns_waitcnt lgkmcnt(3)\n" \
    "v_smfmac_f32_16x16x64_bf16 v[32:35], v[68:71], a[200:207], v0\ns_add_u32 m0, s41, 24576\ns_xor_b32 s85, s62, 1024\n" \
    "v_smfmac_f32_16x16x64_bf16 v[36:39], v[68:71], a[216:223], v0\ns_add_u32 s85, s85, s40\nbuffer_load_dwordx4 v11, s[64:67], s85 offen sc1 lds\ns_waitcnt lgkmcnt(0)\n" \
    "v_mfma_f32_16x16x32_bf16 v[16:19], a[0:3], v[76:79], 0\ns_nop 0\nv_cvt_pk_bf16_f32 v48, v32, v33\nv_cvt_pk_bf16_f32 v50, v34, v35\n" \
    "v_mfma_f32_16x16x32_bf16 v[24:27], v[72:75], v[76:79], 0\nv_cvt_pk_bf16_f32 v49, v36, v37\nv_cvt_pk_bf16_f32 v51, v38, v39\n" \
    "v_smfmac_f32_16x16x64_bf16 v[40:43], v[56:59], a[224:231], v0\nbuffer_load_dwordx4 v12, s[64:67], s85 offen offset:1024 sc1 lds\nv_permlane16_swap_b32 v48, v49\n" \
    "v_smfmac_f32_16x16x64_bf16 v[44:47], v[56:59], a[240:247], v0\ns_add_u32 m0, s41, 32768\nv_permlane16_swap_b32 v50, v51\n" \
    "v_smfmac_f32_16x16x64_bf16 v[40:43], v[60:63], a[232:239], v0\nbuffer_store_dwordx4 v[48:51], v8, s[64:67], s86 offen offset:0 sc1\nv_mov_b64 v[32:33], 0\n" \
    "v_smfmac_f32_16x16x64_bf16 v[44:47], v[60:63], a[248:255], v0\ns_xor_b32 s85, s62, 1280\nv_mov_b64 v[34:35], 0\n" \
    "v_smfmac_f32_16x16x64_bf16 v[40:43], v[64:67], v[92:99], v0\ns_add_u32 s85, s85, s40\nv_mov_b64 v[36:37], 0\nv_smfmac_f32_16x16x64_bf16 v[44:47], v[64:67], v[108:115], v0\n" \
    "buffer_load_dwordx4 v11, s[64:67], s85 offen sc1 lds\nv_mov_b64 v[38:39], 0\nv_smfmac_f32_16x16x64_bf16 v[40:43], v[68:71], v[100:107], v0\n" \
    "v_smfmac_f32_16x16x64_bf16 v[44:47], v[68:71], v[116:123], v0\ns_nop 1\nv_mfma_f32_16x16x32_bf16 v[20:23], a[0:3], v[80:83], 0\n" \
    "buffer_load_dwordx4 v12, s[64:67], s85 offen offset:1024 sc1 lds\ns_add_u32 s84, s40, 0x20000\nv_mfma_f32_16x16x32_bf16 v[28:31], v[72:75], v[80:83], 0\n" \
    "s_cmp_lt_u32 s49, 62\ns_cselect_b32 s84, s84, s40\nv_cvt_pk_bf16_f32 v52, v40, v41\nv_smfmac_f32_16x16x64_bf16 v[32:35], v[56:59], v[124:131], v0\n" \
    "v_cvt_pk_bf16_f32 v54, v42, v43\nv_smfmac_f32_16x16x64_bf16 v[36:39], v[56:59], v[140:147], v0\nds_read_b128 v[72:75], v3 offset:1024\nv_cvt_pk_bf16_f32 v53, v44, v45\n" \
    "v_smfmac_f32_16x16x64_bf16 v[32:35], v[60:63], v[132:139], v0\nds_read_b128 v[76:79], v10 offset:0\nv_cvt_pk_bf16_f32 v55, v46, v47\n" \
    "v_smfmac_f32_16x16x64_bf16 v[36:39], v[60:63], v[148:155], v0\nds_read_b128 v[80:83], v10 offset:4096\nv_permlane16_swap_b32 v52, v53\n" \
    "v_smfmac_f32_16x16x64_bf16 v[32:35], v[64:67], v[156:163], v0\nv_permlane16_swap_b32 v54, v55\nv_smfmac_f32_16x16x64_bf16 v[36:39], v[64:67], v[172:179], v0\n" \
    "buffer_store_dwordx4 v[52:55], v8, s[64:67], s87 offen offset:0 sc1\nv_mov_b64 v[40:41], 0\nv_smfmac_f32_16x16x64_bf16 v[32:35], v[68:71], v[164:171], v0\n" \
    "v_mov_b64 v[42:43], 0\nv_smfmac_f32_16x16x64_bf16 v[36:39], v[68:71], v[180:187], v0\ns_waitcnt lgkmcnt(0)\nv_mov_b64 v[44:45], 0\n" \
    "v_mfma_f32_16x16x32_bf16 v[16:19], a[4:7], v[76:79], v[16:19]\nv_mov_b64 v[46:47], 0\nv_mov_b64 v[252:253], -1\n" \
    "v_mfma_f32_16x16x32_bf16 v[24:27], v[72:75], v[76:79], v[24:27]\nv_cvt_pk_bf16_f32 v48, v32, v33\nv_cvt_pk_bf16_f32 v50, v34, v35\n" \
    "v_smfmac_f32_16x16x64_bf16 v[40:43], v[56:59], v[188:195], v0\nv_cvt_pk_bf16_f32 v49, v36, v37\nv_smfmac_f32_16x16x64_bf16 v[44:47], v[56:59], v[204:211], v0\n" \
    "v_cvt_pk_bf16_f32 v51, v38, v39\nv_smfmac_f32_16x16x64_bf16 v[40:43], v[60:63], v[196:203], v0\nv_permlane16_swap_b32 v48, v49\n" \
    "v_smfmac_f32_16x16x64_bf16 v[44:47], v[60:63], v[212:219], v0\nv_permlane16_swap_b32 v50, v51\nv_smfmac_f32_16x16x64_bf16 v[40:43], v[64:67], v[220:227], v0\n" \
    "buffer_store_dwordx4 v[48:51], v8, s[64:67], s88 offen offset:0 sc1\nv_mov_b64 v[32:33], 0\nv_smfmac_f32_16x16x64_bf16 v[44:47], v[64:67], v[236:243], v0\n" \
    "ds_read_b128 v[76:79], v6 offset:128\nv_mov_b64 v[34:35], 0\nv_smfmac_f32_16x16x64_bf16 v[40:43], v[68:71], v[228:235], v0\nv_mov_b64 v[36:37], 0\n" \
    "v_smfmac_f32_16x16x64_bf16 v[44:47], v[68:71], v[244:251], v0\nds_read_b128 v[56:59], v6 offset:1152\nv_mov_b64 v[38:39], 0\n" \
    "v_mfma_f32_16x16x32_bf16 v[20:23], a[4:7], v[80:83], v[20:23]\ns_nop 0\nv_mov_b64 v[254:255], -1\nv_mfma_f32_16x16x32_bf16 v[28:31], v[72:75], v[80:83], v[28:31]\n" \
    "ds_read_b128 v[60:63], v6 offset:2176\ns_waitcnt lgkmcnt(2)\nv_cvt_pk_bf16_f32 v52, v40, v41\nv_smfmac_f32_16x16x64_bf16 v[32:35], v[76:79], a[160:167], v0\n" \
    "ds_read_b128 v[64:67], v6 offset:3200\nv_cvt_pk_bf16_f32 v54, v42, v43\nv_smfmac_f32_16x16x64_bf16 v[36:39], v[76:79], a[176:183], v0\n" \
    "ds_read_b128 v[68:71], v3 offset:2048\ns_waitcnt lgkmcnt(3)\nv_cvt_pk_bf16_f32 v53, v44, v45\nv_smfmac_f32_16x16x64_bf16 v[32:35], v[56:59], a[168:175], v0\n" \
    "ds_read_b128 v[72:75], v9 offset:1024\nv_cvt_pk_bf16_f32 v55, v46, v47\nv_smfmac_f32_16x16x64_bf16 v[36:39], v[56:59], a[184:191], v0\n" \
    "ds_read_b128 v[80:83], v9 offset:5120\ns_waitcnt lgkmcnt(4)\nv_permlane16_swap_b32 v52, v53\nv_smfmac_f32_16x16x64_bf16 v[32:35], v[60:63], a[192:199], v0\n" \
    "s_and_b32 s69, s49, 15\nv_permlane16_swap_b32 v54, v55\nv_smfmac_f32_16x16x64_bf16 v[36:39], v[60:63], a[208:215], v0\n" \
    "buffer_store_dwordx4 v[52:55], v8, s[64:67], s89 offen offset:0 sc1\ns_waitcnt lgkmcnt(3)\nv_mov_b64 v[40:41], 0\n" \
    "v_smfmac_f32_16x16x64_bf16 v[32:35], v[64:67], a[200:207], v0\ns_lshl_b32 s69, s69, 19\nv_mov_b64 v[42:43], 0\n" \
    "v_smfmac_f32_16x16x64_bf16 v[36:39], v[64:67], a[216:223], v0\ns_add_u32 s82, s38, s69\ns_waitcnt lgkmcnt(0)\nv_mov_b64 v[44:45], 0\n" \
    "v_mfma_f32_16x16x32_bf16 v[16:19], a[8:11], v[72:75], v[16:19]\ns_lshr_b32 s69, s82, 8\nv_mov_b64 v[46:47], 0\nv_cvt_pk_bf16_f32 v48, v32, v33\n" \
    "v_mfma_f32_16x16x32_bf16 v[24:27], v[68:71], v[72:75], v[24:27]\ns_and_b32 s69, s69, 0x2000\nv_cvt_pk_bf16_f32 v50, v34, v35\nv_cvt_pk_bf16_f32 v49, v36, v37\n" \
    "v_smfmac_f32_16x16x64_bf16 v[40:43], v[76:79], a[224:231], v0\ns_xor_b32 s82, s82, s69\nv_cvt_pk_bf16_f32 v51, v38, v39\n" \
    "v_smfmac_f32_16x16x64_bf16 v[44:47], v[76:79], a[240:247], v0\ns_and_b32 s69, s35, 15\nv_permlane16_swap_b32 v48, v49\n" \
    "v_smfmac_f32_16x16x64_bf16 v[40:43], v[56:59], a[232:239], v0\ns_lshl_b32 s69, s69, 19\nv_permlane16_swap_b32 v50, v51\n" \
    "v_smfmac_f32_16x16x64_bf16 v[44:47], v[56:59], a[248:255], v0\nbuffer_store_dwordx4 v[48:51], v8, s[64:67], s86 offen offset:256 sc1\nv_mov_b64 v[32:33], 0\n" \
    "v_smfmac_f32_16x16x64_bf16 v[40:43], v[60:63], v[92:99], v0\ns_add_u32 s0, s38, s69\nv_mov_b64 v[34:35], 0\nv_smfmac_f32_16x16x64_bf16 v[44:47], v[60:63], v[108:115], v0\n" \
    "s_lshr_b32 s69, s0, 8\nv_mov_b64 v[36:37], 0\nv_smfmac_f32_16x16x64_bf16 v[40:43], v[64:67], v[100:107], v0\ns_and_b32 s69, s69, 0x2000\nv_mov_b64 v[38:39], 0\n" \
    "v_smfmac_f32_16x16x64_bf16 v[44:47], v[64:67], v[116:123], v0\ns_xor_b32 s0, s0, s69\ns_add_u32 s1, s0, 0x10000\n" \
    "v_mfma_f32_16x16x32_bf16 v[20:23], a[8:11], v[80:83], v[20:23]\ns_add_u32 s2, s0, 0x20000\ns_add_u32 s3, s0, 0x30000\nv_cvt_pk_bf16_f32 v52, v40, v41\n" \
    "v_mfma_f32_16x16x32_bf16 v[28:31], v[68:71], v[80:83], v[28:31]\nv_cvt_pk_bf16_f32 v54, v42, v43\nv_cvt_pk_bf16_f32 v53, v44, v45\n" \
    "v_smfmac_f32_16x16x64_bf16 v[32:35], v[76:79], v[124:131], v0\nv_cvt_pk_bf16_f32 v55, v46, v47\nv_smfmac_f32_16x16x64_bf16 v[36:39], v[76:79], v[140:147], v0\n" \
    "ds_read_b128 v[68:71], v3 offset:3072\nv_permlane16_swap_b32 v52, v53\nv_smfmac_f32_16x16x64_bf16 v[32:35], v[56:59], v[132:139], v0\n" \
    "ds_read_b128 v[72:75], v10 offset:1024\nv_permlane16_swap_b32 v54, v55\nv_smfmac_f32_16x16x64_bf16 v[36:39], v[56:59], v[148:155], v0\n" \
    "ds_read_b128 v[80:83], v10 offset:5120\nbuffer_store_dwordx4 v[52:55], v8, s[64:67], s87 offen offset:256 sc1\nv_mov_b64 v[40:41], 0\n" \
    "v_smfmac_f32_16x16x64_bf16 v[32:35], v[60:63], v[156:163], v0\nv_mov_b64 v[42:43], 0\nv_smfmac_f32_16x16x64_bf16 v[36:39], v[60:63], v[172:179], v0\nv_mov_b64 v[44:45], 0\n" \
    "v_smfmac_f32_16x16x64_bf16 v[32:35], v[64:67], v[164:171], v0\nv_mov_b64 v[46:47], 0\nv_smfmac_f32_16x16x64_bf16 v[36:39], v[64:67], v[180:187], v0\ns_nop 0\n" \
    "s_waitcnt lgkmcnt(0)\nv_lshrrev_b32 v15, 1, v2\nv_mfma_f32_16x16x32_bf16 v[16:19], a[12:15], v[72:75], v[16:19]\ns_nop 0\nv_add_u32 v15, s60, v15\n" \
    "v_cvt_pk_bf16_f32 v48, v32, v33\nv_mfma_f32_16x16x32_bf16 v[24:27], v[68:71], v[72:75], v[24:27]\nds_write_b64 v15, v[252:253]\nv_cvt_pk_bf16_f32 v50, v34, v35\n" \
    "v_cvt_pk_bf16_f32 v49, v36, v37\nv_smfmac_f32_16x16x64_bf16 v[40:43], v[76:79], v[188:195], v0\nv_cvt_pk_bf16_f32 v51, v38, v39\n" \
    "v_smfmac_f32_16x16x64_bf16 v[44:47], v[76:79], v[204:211], v0\nv_permlane16_swap_b32 v48, v49\nv_smfmac_f32_16x16x64_bf16 v[40:43], v[56:59], v[196:203], v0\n" \
    "v_permlane16_swap_b32 v50, v51\nv_smfmac_f32_16x16x64_bf16 v[44:47], v[56:59], v[212:219], v0\nbuffer_store_dwordx4 v[48:51], v8, s[64:67], s88 offen offset:256 sc1\n" \
    "v_mov_b64 v[32:33], 0\nv_smfmac_f32_16x16x64_bf16 v[40:43], v[60:63], v[220:227], v0\nv_mov_b64 v[34:35], 0\nv_smfmac_f32_16x16x64_bf16 v[44:47], v[60:63], v[236:243], v0\n" \
    "ds_read_b128 v[56:59], v6 offset:4096\nv_mov_b64 v[36:37], 0\nv_smfmac_f32_16x16x64_bf16 v[40:43], v[64:67], v[228:235], v0\nv_mov_b64 v[38:39], 0\n" \
    "v_smfmac_f32_16x16x64_bf16 v[44:47], v[64:67], v[244:251], v0\nds_read_b128 v[60:63], v6 offset:5120\ns_nop 0\n" \
    "v_mfma_f32_16x16x32_bf16 v[20:23], a[12:15], v[80:83], v[20:23]\ns_nop 1\nv_mfma_f32_16x16x32_bf16 v[28:31], v[68:71], v[80:83], v[28:31]\n" \
    "ds_read_b128 v[64:67], v6 offset:6144\ns_waitcnt lgkmcnt(2)\nv_cvt_pk_bf16_f32 v52, v40, v41\nv_smfmac_f32_16x16x64_bf16 v[32:35], v[56:59], a[160:167], v0\n" \
    "ds_read_b128 v[68:71], v6 offset:7168\nv_cvt_pk_bf16_f32 v54, v42, v43\nv_smfmac_f32_16x16x64_bf16 v[36:39], v[56:59], a[176:183], v0\n" \
    "ds_read_b128 v[72:75], v3 offset:4096\ns_waitcnt lgkmcnt(3)\nv_cvt_pk_bf16_f32 v53, v44, v45\nv_smfmac_f32_16x16x64_bf16 v[32:35], v[60:63], a[168:175], v0\n" \
    "ds_read_b128 v[76:79], v9 offset:8192\nv_cvt_pk_bf16_f32 v55, v46, v47\nv_smfmac_f32_16x16x64_bf16 v[36:39], v[60:63], a[184:191], v0\n" \
    "ds_read_b128 v[80:83], v9 offset:12288\ns_waitcnt lgkmcnt(4)\nv_permlane16_swap_b32 v52, v53\nv_smfmac_f32_16x16x64_bf16 v[32:35], v[64:67], a[192:199], v0\n" \
    "v_permlane16_swap_b32 v54, v55\nv_smfmac_f32_16x16x64_bf16 v[36:39], v[64:67], a[208:215], v0\nbuffer_store_dwordx4 v[52:55], v8, s[64:67], s89 offen offset:256 sc1\n" \
    "s_waitcnt lgkmcnt(3)\nv_mov_b64 v[40:41], 0\nv_smfmac_f32_16x16x64_bf16 v[32:35], v[68:71], a[200:207], v0\nv_mov_b64 v[42:43], 0\n" \
    "v_smfmac_f32_16x16x64_bf16 v[36:39], v[68:71], a[216:223], v0\ns_waitcnt lgkmcnt(0)\nv_mov_b64 v[44:45], 0\n" \
    "v_mfma_f32_16x16x32_bf16 v[16:19], a[16:19], v[76:79], v[16:19]\nv_mov_b64 v[46:47], 0\nv_mfma_f32_16x16x32_bf16 v[24:27], v[72:75], v[76:79], v[24:27]\ns_nop 0\n" \
    "v_cvt_pk_bf16_f32 v48, v32, v33\nv_cvt_pk_bf16_f32 v50, v34, v35\nv_smfmac_f32_16x16x64_bf16 v[40:43], v[56:59], a[224:231], v0\nv_cvt_pk_bf16_f32 v49, v36, v37\n" \
    "v_smfmac_f32_16x16x64_bf16 v[44:47], v[56:59], a[240:247], v0\nv_cvt_pk_bf16_f32 v51, v38, v39\nv_smfmac_f32_16x16x64_bf16 v[40:43], v[60:63], a[232:239], v0\n" \
    "v_permlane16_swap_b32 v48, v49\nv_smfmac_f32_16x16x64_bf16 v[44:47], v[60:63], a[248:255], v0\nv_permlane16_swap_b32 v50, v51\n" \
    "v_smfmac_f32_16x16x64_bf16 v[40:43], v[64:67], v[92:99], v0\nbuffer_store_dwordx4 v[48:51], v8, s[64:67], s86 offen offset:512 sc1\nv_mov_b64 v[32:33], 0\n" \
    "v_smfmac_f32_16x16x64_bf16 v[44:47], v[64:67], v[108:115], v0\nv_mov_b64 v[34:35], 0\nv_smfmac_f32_16x16x64_bf16 v[40:43], v[68:71], v[100:107], v0\nv_mov_b64 v[36:37], 0\n" \
    "v_smfmac_f32_16x16x64_bf16 v[44:47], v[68:71], v[116:123], v0\nv_mov_b64 v[38:39], 0\nv_mfma_f32_16x16x32_bf16 v[20:23], a[16:19], v[80:83], v[20:23]\ns_nop 1\n" \
    "v_mfma_f32_16x16x32_bf16 v[28:31], v[72:75], v[80:83], v[28:31]\ns_nop 0\nv_cvt_pk_bf16_f32 v52, v40, v41\nv_cvt_pk_bf16_f32 v54, v42, v43\n" \
    "v_smfmac_f32_16x16x64_bf16 v[32:35], v[56:59], v[124:131], v0\nv_cvt_pk_bf16_f32 v53, v44, v45\nv_smfmac_f32_16x16x64_bf16 v[36:39], v[56:59], v[140:147], v0\n" \
    "ds_read_b128 v[72:75], v3 offset:5120\nv_cvt_pk_bf16_f32 v55, v46, v47\nv_smfmac_f32_16x16x64_bf16 v[32:35], v[60:63], v[132:139], v0\n" \
    "ds_read_b128 v[76:79], v10 offset:8192\nv_permlane16_swap_b32 v52, v53\nv_smfmac_f32_16x16x64_bf16 v[36:39], v[60:63], v[148:155], v0\n" \
    "ds_read_b128 v[80:83], v10 offset:12288\nv_permlane16_swap_b32 v54, v55\nv_smfmac_f32_16x16x64_bf16 v[32:35], v[64:67], v[156:163], v0\n" \
    "buffer_store_dwordx4 v[52:55], v8, s[64:67], s87 offen offset:512 sc1\nv_mov_b64 v[40:41], 0\nv_smfmac_f32_16x16x64_bf16 v[36:39], v[64:67], v[172:179], v0\n" \
    "v_mov_b64 v[42:43], 0\nv_smfmac_f32_16x16x64_bf16 v[32:35], v[68:71], v[164:171], v0\nv_mov_b64 v[44:45], 0\nv_smfmac_f32_16x16x64_bf16 v[36:39], v[68:71], v[180:187], v0\n" \
    "s_waitcnt lgkmcnt(0)\nv_mov_b64 v[46:47], 0\nv_mfma_f32_16x16x32_bf16 v[16:19], a[20:23], v[76:79], v[16:19]\ns_nop 1\n" \
    "v_mfma_f32_16x16x32_bf16 v[24:27], v[72:75], v[76:79], v[24:27]\nv_cvt_pk_bf16_f32 v48, v32, v33\nv_cvt_pk_bf16_f32 v50, v34, v35\n" \
    "v_smfmac_f32_16x16x64_bf16 v[40:43], v[56:59], v[188:195], v0\nv_cvt_pk_bf16_f32 v49, v36, v37\nv_smfmac_f32_16x16x64_bf16 v[44:47], v[56:59], v[204:211], v0\n" \
    "v_cvt_pk_bf16_f32 v51, v38, v39\nv_smfmac_f32_16x16x64_bf16 v[40:43], v[60:63], v[196:203], v0\nv_permlane16_swap_b32 v48, v49\n" \
    "v_smfmac_f32_16x16x64_bf16 v[44:47], v[60:63], v[212:219], v0\nv_permlane16_swap_b32 v50, v51\nv_smfmac_f32_16x16x64_bf16 v[40:43], v[64:67], v[220:227], v0\n" \
    "buffer_store_dwordx4 v[48:51], v8, s[64:67], s88 offen offset:512 sc1\nv_mov_b64 v[32:33], 0\nv_smfmac_f32_16x16x64_bf16 v[44:47], v[64:67], v[236:243], v0\n" \
    "ds_read_b128 v[76:79], v6 offset:4224\nv_mov_b64 v[34:35], 0\nv_smfmac_f32_16x16x64_bf16 v[40:43], v[68:71], v[228:235], v0\nv_mov_b64 v[36:37], 0\n" \
    "v_smfmac_f32_16x16x64_bf16 v[44:47], v[68:71], v[244:251], v0\nds_read_b128 v[56:59], v6 offset:5248\nv_mov_b64 v[38:39], 0\n" \
    "v_mfma_f32_16x16x32_bf16 v[20:23], a[20:23], v[80:83], v[20:23]\ns_nop 1\nv_mfma_f32_16x16x32_bf16 v[28:31], v[72:75], v[80:83], v[28:31]\n" \
    "ds_read_b128 v[60:63], v6 offset:6272\ns_waitcnt lgkmcnt(2)\nv_cvt_pk_bf16_f32 v52, v40, v41\nv_smfmac_f32_16x16x64_bf16 v[32:35], v[76:79], a[160:167], v0\n" \
    "ds_read_b128 v[64:67], v6 offset:7296\nv_cvt_pk_bf16_f32 v54, v42, v43\nv_smfmac_f32_16x16x64_bf16 v[36:39], v[76:79], a[176:183], v0\n" \
    "ds_read_b128 v[68:71], v3 offset:6144\ns_waitcnt lgkmcnt(3)\nv_cvt_pk_bf16_f32 v53, v44, v45\nv_smfmac_f32_16x16x64_bf16 v[32:35], v[56:59], a[168:175], v0\n" \
    "ds_read_b128 v[72:75], v9 offset:9216\nv_cvt_pk_bf16_f32 v55, v46, v47\nv_smfmac_f32_16x16x64_bf16 v[36:39], v[56:59], a[184:191], v0\n" \
    "ds_read_b128 v[80:83], v9 offset:13312\ns_waitcnt lgkmcnt(4)\nv_permlane16_swap_b32 v52, v53\nv_smfmac_f32_16x16x64_bf16 v[32:35], v[60:63], a[192:199], v0\n" \
    "v_permlane16_swap_b32 v54, v55\nv_smfmac_f32_16x16x64_bf16 v[36:39], v[60:63], a[208:215], v0\nbuffer_store_dwordx4 v[52:55], v8, s[64:67], s89 offen offset:512 sc1\n" \
    "s_waitcnt lgkmcnt(3)\nv_mov_b64 v[40:41], 0\nv_smfmac_f32_16x16x64_bf16 v[32:35], v[64:67], a[200:207], v0\nv_mov_b64 v[42:43], 0\n" \
    "v_smfmac_f32_16x16x64_bf16 v[36:39], v[64:67], a[216:223], v0\ns_waitcnt lgkmcnt(0)\nv_mov_b64 v[44:45], 0\n" \
    "v_mfma_f32_16x16x32_bf16 v[16:19], a[24:27], v[72:75], v[16:19]\nv_mov_b64 v[46:47], 0\nv_mfma_f32_16x16x32_bf16 v[24:27], v[68:71], v[72:75], v[24:27]\ns_nop 0\n" \
    "v_cvt_pk_bf16_f32 v48, v32, v33\nv_cvt_pk_bf16_f32 v50, v34, v35\nv_smfmac_f32_16x16x64_bf16 v[40:43], v[76:79], a[224:231], v0\nv_cvt_pk_bf16_f32 v49, v36, v37\n" \
    "v_smfmac_f32_16x16x64_bf16 v[44:47], v[76:79], a[240:247], v0\nv_cvt_pk_bf16_f32 v51, v38, v39\nv_smfmac_f32_16x16x64_bf16 v[40:43], v[56:59], a[232:239], v0\n" \
    "v_permlane16_swap_b32 v48, v49\nv_smfmac_f32_16x16x64_bf16 v[44:47], v[56:59], a[248:255], v0\nv_permlane16_swap_b32 v50, v51\n" \
    "v_smfmac_f32_16x16x64_bf16 v[40:43], v[60:63], v[92:99], v0\nbuffer_store_dwordx4 v[48:51], v8, s[64:67], s86 offen offset:768 sc1\nv_mov_b64 v[32:33], 0\n" \
    "v_smfmac_f32_16x16x64_bf16 v[44:47], v[60:63], v[108:115], v0\nv_mov_b64 v[34:35], 0\nv_smfmac_f32_16x16x64_bf16 v[40:43], v[64:67], v[100:107], v0\nv_mov_b64 v[36:37], 0\n" \
    "v_smfmac_f32_16x16x64_bf16 v[44:47], v[64:67], v[116:123], v0\nv_mov_b64 v[38:39], 0\nv_mfma_f32_16x16x32_bf16 v[20:23], a[24:27], v[80:83], v[20:23]\ns_nop 1\n" \
    "v_mfma_f32_16x16x32_bf16 v[28:31], v[68:71], v[80:83], v[28:31]\ns_nop 0\nv_cvt_pk_bf16_f32 v52, v40, v41\nv_cvt_pk_bf16_f32 v54, v42, v43\n" \
    "v_smfmac_f32_16x16x64_bf16 v[32:35], v[76:79], v[124:131], v0\nv_cvt_pk_bf16_f32 v53, v44, v45\nv_smfmac_f32_16x16x64_bf16 v[36:39], v[76:79], v[140:147], v0\n" \
    "ds_read_b128 v[68:71], v3 offset:7168\nv_cvt_pk_bf16_f32 v55, v46, v47\nv_smfmac_f32_16x16x64_bf16 v[32:35], v[56:59], v[132:139], v0\n" \
    "ds_read_b128 v[72:75], v10 offset:9216\nv_permlane16_swap_b32 v52, v53\nv_smfmac_f32_16x16x64_bf16 v[36:39], v[56:59], v[148:155], v0\n" \
    "ds_read_b128 v[80:83], v10 offset:13312\nv_permlane16_swap_b32 v54, v55\nv_smfmac_f32_16x16x64_bf16 v[32:35], v[60:63], v[156:163], v0\n" \
    "buffer_store_dwordx4 v[52:55], v8, s[64:67], s87 offen offset:768 sc1\nv_mov_b64 v[40:41], 0\nv_smfmac_f32_16x16x64_bf16 v[36:39], v[60:63], v[172:179], v0\n" \
    "v_mov_b64 v[42:43], 0\nv_smfmac_f32_16x16x64_bf16 v[32:35], v[64:67], v[164:171], v0\nv_mov_b64 v[44:45], 0\nv_smfmac_f32_16x16x64_bf16 v[36:39], v[64:67], v[180:187], v0\n" \
    "s_waitcnt lgkmcnt(0)\nv_mov_b64 v[46:47], 0\nv_mfma_f32_16x16x32_bf16 v[16:19], a[28:31], v[72:75], v[16:19]\ns_nop 1\n" \
    "v_mfma_f32_16x16x32_bf16 v[24:27], v[68:71], v[72:75], v[24:27]\nv_cvt_pk_bf16_f32 v48, v32, v33\nv_cvt_pk_bf16_f32 v50, v34, v35\n" \
    "v_smfmac_f32_16x16x64_bf16 v[40:43], v[76:79], v[188:195], v0\nv_cvt_pk_bf16_f32 v49, v36, v37\nv_smfmac_f32_16x16x64_bf16 v[44:47], v[76:79], v[204:211], v0\n" \
    "v_cvt_pk_bf16_f32 v51, v38, v39\nv_smfmac_f32_16x16x64_bf16 v[40:43], v[56:59], v[196:203], v0\nv_permlane16_swap_b32 v48, v49\n" \
    "v_smfmac_f32_16x16x64_bf16 v[44:47], v[56:59], v[212:219], v0\nds_read_b128 v[56:59], v3 offset:8192\nv_permlane16_swap_b32 v50, v51\n" \
    "v_smfmac_f32_16x16x64_bf16 v[40:43], v[60:63], v[220:227], v0\nds_read_b128 v[72:75], v9 offset:16384\n" \
    "buffer_store_dwordx4 v[48:51], v8, s[64:67], s88 offen offset:768 sc1\nv_smfmac_f32_16x16x64_bf16 v[44:47], v[60:63], v[236:243], v0\n" \
    "ds_read_b128 v[60:63], v9 offset:20480\nv_smfmac_f32_16x16x64_bf16 v[40:43], v[64:67], v[228:235], v0\nv_smfmac_f32_16x16x64_bf16 v[44:47], v[64:67], v[244:251], v0\n" \
    "ds_read_b128 v[64:67], v3 offset:9216\ns_nop 0\nv_mfma_f32_16x16x32_bf16 v[20:23], a[28:31], v[80:83], v[20:23]\nds_read_b128 v[76:79], v10 offset:16384\ns_nop 0\n" \
    "v_mfma_f32_16x16x32_bf16 v[28:31], v[68:71], v[80:83], v[28:31]\nds_read_b128 v[68:71], v10 offset:20480\ns_waitcnt lgkmcnt(3)\nv_cvt_pk_bf16_f32 v52, v40, v41\n" \
    "v_mfma_f32_16x16x32_bf16 v[16:19], a[32:35], v[72:75], v[16:19]\nv_cvt_pk_bf16_f32 v54, v42, v43\nv_cvt_pk_bf16_f32 v53, v44, v45\n" \
    "v_mfma_f32_16x16x32_bf16 v[24:27], v[56:59], v[72:75], v[24:27]\nds_read_b128 v[72:75], v3 offset:10240\nv_cvt_pk_bf16_f32 v55, v46, v47\nv_permlane16_swap_b32 v52, v53\n" \
    "v_mfma_f32_16x16x32_bf16 v[20:23], a[32:35], v[60:63], v[20:23]\nds_read_b128 v[80:83], v9 offset:17408\nv_permlane16_swap_b32 v54, v55\n" \
    "v_mfma_f32_16x16x32_bf16 v[28:31], v[56:59], v[60:63], v[28:31]\nds_read_b128 v[56:59], v9 offset:21504\n" \
    "buffer_store_dwordx4 v[52:55], v8, s[64:67], s89 offen offset:768 sc1\ns_waitcnt lgkmcnt(3)\nv_mfma_f32_16x16x32_bf16 v[16:19], a[36:39], v[76:79], v[16:19]\n" \
    "buffer_load_dword v13, off, s[64:67], s42 sc1\ns_and_b32 s69, s49, 255\nv_mfma_f32_16x16x32_bf16 v[24:27], v[64:67], v[76:79], v[24:27]\n" \
    "ds_read_b128 v[60:63], v3 offset:11264\ns_lshl_b32 s69, s69, 19\nv_mfma_f32_16x16x32_bf16 v[20:23], a[36:39], v[68:71], v[20:23]\nds_read_b128 v[76:79], v10 offset:17408\n" \
    "s_add_u32 s74, s63, s69\nv_mfma_f32_16x16x32_bf16 v[28:31], v[64:67], v[68:71], v[28:31]\nds_read_b128 v[64:67], v10 offset:21504\ns_cmp_lt_u32 s49, 64\n" \
    "s_cselect_b32 s74, s74, 0x7fff0000\ns_waitcnt lgkmcnt(3)\nv_mfma_f32_16x16x32_bf16 v[16:19], a[40:43], v[80:83], v[16:19]\n" \
    "v_mfma_f32_16x16x32_bf16 v[24:27], v[72:75], v[80:83], v[24:27]\nds_read_b128 v[68:71], v3 offset:12288\ns_waitcnt vmcnt(16)\ns_barrier\n" \
    "v_mfma_f32_16x16x32_bf16 v[20:23], a[40:43], v[56:59], v[20:23]\nds_read_b128 v[80:83], v9 offset:40960\nv_mfma_f32_16x16x32_bf16 v[28:31], v[72:75], v[56:59], v[28:31]\n" \
    "ds_read_b128 v[56:59], v9 offset:45056\ns_add_u32 m0, s41, 0\ns_waitcnt lgkmcnt(3)\nv_mfma_f32_16x16x32_bf16 v[16:19], a[44:47], v[76:79], v[16:19]\n" \
    "s_xor_b32 s85, s62, 1536\ns_add_u32 s85, s85, s40\nv_mfma_f32_16x16x32_bf16 v[24:27], v[60:63], v[76:79], v[24:27]\nds_read_b128 v[72:75], v3 offset:13312\n" \
    "buffer_load_dwordx4 v11, s[64:67], s85 offen sc1 lds\nv_mfma_f32_16x16x32_bf16 v[20:23], a[44:47], v[64:67], v[20:23]\nds_read_b128 v[76:79], v10 offset:40960\n" \
    "buffer_store_dwordx4 v[252:255], v2, s[64:67], s74 offen offset:0 sc1\nv_mfma_f32_16x16x32_bf16 v[28:31], v[60:63], v[64:67], v[28:31]\n" \
    "ds_read_b128 v[60:63], v10 offset:45056\ns_waitcnt lgkmcnt(3)\nv_mfma_f32_16x16x32_bf16 v[16:19], a[48:51], v[80:83], v[16:19]\n" \
    "buffer_load_dwordx4 v12, s[64:67], s85 offen offset:1024 sc1 lds\ns_add_u32 m0, s41, 8192\nv_mfma_f32_16x16x32_bf16 v[24:27], v[68:71], v[80:83], v[24:27]\n" \
    "ds_read_b128 v[64:67], v3 offset:14336\ns_xor_b32 s85, s62, 1792\nv_mfma_f32_16x16x32_bf16 v[20:23], a[48:51], v[56:59], v[20:23]\nds_read_b128 v[80:83], v9 offset:41984\n" \
    "s_add_u32 s85, s85, s40\nv_mfma_f32_16x16x32_bf16 v[28:31], v[68:71], v[56:59], v[28:31]\nds_read_b128 v[56:59], v9 offset:46080\n" \
    "buffer_load_dwordx4 v11, s[64:67], s85 offen sc1 lds\ns_waitcnt lgkmcnt(3)\nv_mfma_f32_16x16x32_bf16 v[16:19], a[52:55], v[76:79], v[16:19]\n" \
    "buffer_store_dwordx4 v[252:255], v2, s[64:67], s74 offen offset:1024 sc1\nv_mfma_f32_16x16x32_bf16 v[24:27], v[72:75], v[76:79], v[24:27]\n" \
    "ds_read_b128 v[68:71], v3 offset:15360\nv_mfma_f32_16x16x32_bf16 v[20:23], a[52:55], v[60:63], v[20:23]\nds_read_b128 v[76:79], v10 offset:41984\n" \
    "buffer_load_dwordx4 v12, s[64:67], s85 offen offset:1024 sc1 lds\nv_mfma_f32_16x16x32_bf16 v[28:31], v[72:75], v[60:63], v[28:31]\nds_read_b128 v[60:63], v10 offset:46080\n" \
    "s_waitcnt lgkmcnt(3)\nv_mfma_f32_16x16x32_bf16 v[16:19], a[56:59], v[80:83], v[16:19]\nbuffer_store_dwordx4 v[252:255], v7, s[64:67], s82 offen sc1\n" \
    "s_add_u32 s82, s82, 0x10000\nv_mfma_f32_16x16x32_bf16 v[24:27], v[64:67], v[80:83], v[24:27]\nds_read_b128 v[72:75], v3 offset:16384\n" \
    "v_mfma_f32_16x16x32_bf16 v[20:23], a[56:59], v[56:59], v[20:23]\nds_read_b128 v[80:83], v9 offset:24576\nv_mfma_f32_16x16x32_bf16 v[28:31], v[64:67], v[56:59], v[28:31]\n" \
    "ds_read_b128 v[56:59], v9 offset:28672\ns_waitcnt lgkmcnt(3)\nv_mfma_f32_16x16x32_bf16 v[16:19], a[60:63], v[76:79], v[16:19]\n" \
    "buffer_store_dwordx4 v[252:255], v7, s[64:67], s82 offen sc1\ns_add_u32 s82, s82, 0x10000\nv_mfma_f32_16x16x32_bf16 v[24:27], v[68:71], v[76:79], v[24:27]\n" \
    "ds_read_b128 v[64:67], v3 offset:17408\nv_mfma_f32_16x16x32_bf16 v[20:23], a[60:63], v[60:63], v[20:23]\nds_read_b128 v[76:79], v10 offset:24576\n" \
    "v_mfma_f32_16x16x32_bf16 v[28:31], v[68:71], v[60:63], v[28:31]\nds_read_b128 v[60:63], v10 offset:28672\ns_waitcnt lgkmcnt(3)\n" \
    "v_mfma_f32_16x16x32_bf16 v[16:19], a[64:67], v[80:83], v[16:19]\nbuffer_store_dwordx4 v[252:255], v7, s[64:67], s82 offen sc1\ns_add_u32 s82, s82, 0x10000\n" \
    "v_mfma_f32_16x16x32_bf16 v[24:27], v[72:75], v[80:83], v[24:27]\nds_read_b128 v[68:71], v3 offset:18432\nv_mfma_f32_16x16x32_bf16 v[20:23], a[64:67], v[56:59], v[20:23]\n" \
    "ds_read_b128 v[80:83], v9 offset:25600\nv_mfma_f32_16x16x32_bf16 v[28:31], v[72:75], v[56:59], v[28:31]\nds_read_b128 v[56:59], v9 offset:29696\ns_waitcnt lgkmcnt(3)\n" \
    "v_mfma_f32_16x16x32_bf16 v[16:19], a[68:71], v[76:79], v[16:19]\nbuffer_store_dwordx4 v[252:255], v7, s[64:67], s82 offen sc1\ns_add_u32 s82, s82, 0x10000\n" \
    "v_mfma_f32_16x16x32_bf16 v[24:27], v[64:67], v[76:79], v[24:27]\nds_read_b128 v[72:75], v3 offset:19456\ns_add_u32 s49, s49, 2\n" \
    "v_mfma_f32_16x16x32_bf16 v[20:23], a[68:71], v[60:63], v[20:23]\nds_read_b128 v[76:79], v10 offset:25600\ns_and_b32 s69, s35, 15\n" \
    "v_mfma_f32_16x16x32_bf16 v[28:31], v[64:67], v[60:63], v[28:31]\nds_read_b128 v[60:63], v10 offset:29696\ns_lshl_b32 s69, s69, 17\ns_waitcnt lgkmcnt(3)\n" \
    "v_mfma_f32_16x16x32_bf16 v[16:19], a[72:75], v[80:83], v[16:19]\ns_add_u32 s81, s37, s69\ns_lshr_b32 s69, s81, 8\n" \
    "v_mfma_f32_16x16x32_bf16 v[24:27], v[68:71], v[80:83], v[24:27]\nds_read_b128 v[64:67], v3 offset:20480\ns_and_b32 s69, s69, 0x2000\n" \
    "v_mfma_f32_16x16x32_bf16 v[20:23], a[72:75], v[56:59], v[20:23]\nds_read_b128 v[80:83], v9 offset:32768\ns_xor_b32 s81, s81, s69\n" \
    "v_mfma_f32_16x16x32_bf16 v[28:31], v[68:71], v[56:59], v[28:31]\nds_read_b128 v[56:59], v9 offset:36864\ns_mov_b32 s68, 0\ns_waitcnt lgkmcnt(3)\n" \
    "v_mfma_f32_16x16x32_bf16 v[16:19], a[76:79], v[76:79], v[16:19]\ns_waitcnt vmcnt(11)\nv_mov_b32 v15, s50\ns_mov_b64 exec, 1\n" \
    "buffer_store_dword v15, off, s[64:67], s48 sc1\ns_mov_b64 exec, -1\nv_mfma_f32_16x16x32_bf16 v[24:27], v[72:75], v[76:79], v[24:27]\n" \
    "ds_read_b128 v[68:71], v3 offset:21504\nv_mfma_f32_16x16x32_bf16 v[20:23], a[76:79], v[60:63], v[20:23]\nds_read_b128 v[76:79], v10 offset:32768\n" \
    "v_mfma_f32_16x16x32_bf16 v[28:31], v[72:75], v[60:63], v[28:31]\nds_read_b128 v[60:63], v10 offset:36864\ns_waitcnt lgkmcnt(3)\n" \
    "v_mfma_f32_16x16x32_bf16 v[16:19], a[80:83], v[80:83], v[16:19]\nv_mfma_f32_16x16x32_bf16 v[24:27], v[64:67], v[80:83], v[24:27]\nds_read_b128 v[72:75], v3 offset:22528\n" \
    "v_mfma_f32_16x16x32_bf16 v[20:23], a[80:83], v[56:59], v[20:23]\nds_read_b128 v[80:83], v9 offset:33792\nv_mfma_f32_16x16x32_bf16 v[28:31], v[64:67], v[56:59], v[28:31]\n" \
    "ds_read_b128 v[56:59], v9 offset:37888\ns_waitcnt lgkmcnt(3)\nv_mfma_f32_16x16x32_bf16 v[16:19], a[84:87], v[76:79], v[16:19]\n" \
    "v_mfma_f32_16x16x32_bf16 v[24:27], v[68:71], v[76:79], v[24:27]\nds_read_b128 v[64:67], v3 offset:23552\nv_mfma_f32_16x16x32_bf16 v[20:23], a[84:87], v[60:63], v[20:23]\n" \
    "ds_read_b128 v[76:79], v10 offset:33792\nv_mfma_f32_16x16x32_bf16 v[28:31], v[68:71], v[60:63], v[28:31]\nds_read_b128 v[60:63], v10 offset:37888\ns_waitcnt lgkmcnt(3)\n" \
    "v_mfma_f32_16x16x32_bf16 v[16:19], a[88:91], v[80:83], v[16:19]\nv_mfma_f32_16x16x32_bf16 v[24:27], v[72:75], v[80:83], v[24:27]\ns_waitcnt vmcnt(5)\ns_barrier\n" \
    "v_mfma_f32_16x16x32_bf16 v[20:23], a[88:91], v[56:59], v[20:23]\nds_read_b128 v[68:71], v9 offset:0\nv_mfma_f32_16x16x32_bf16 v[28:31], v[72:75], v[56:59], v[28:31]\n" \
    "ds_read_b128 v[56:59], v9 offset:4096\ns_add_u32 m0, s41, 16384\ns_waitcnt lgkmcnt(2)\nv_mfma_f32_16x16x32_bf16 v[16:19], a[92:95], v[76:79], v[16:19]\n" \
    "s_xor_b32 s85, s62, 512\ns_add_u32 s85, s85, s84\nv_mfma_f32_16x16x32_bf16 v[24:27], v[64:67], v[76:79], v[24:27]\nbuffer_load_dwordx4 v11, s[64:67], s85 offen sc1 lds\n" \
    "v_mfma_f32_16x16x32_bf16 v[20:23], a[92:95], v[60:63], v[20:23]\nds_read_b128 v[72:75], v10 offset:0\nv_mfma_f32_16x16x32_bf16 v[28:31], v[64:67], v[60:63], v[28:31]\n" \
    "ds_read_b128 v[60:63], v10 offset:4096\ns_waitcnt lgkmcnt(2)\nv_mfma_f32_16x16x32_bf16 v[16:19], a[96:99], v[68:71], v[16:19]\n" \
    "buffer_load_dwordx4 v12, s[64:67], s85 offen offset:1024 sc1 lds\ns_add_u32 m0, s41, 40960\nv_mfma_f32_16x16x32_bf16 v[24:27], a[104:107], v[68:71], v[24:27]\n" \
    "s_xor_b32 s85, s62, 768\ns_add_u32 s85, s85, s84\nv_mfma_f32_16x16x32_bf16 v[20:23], a[96:99], v[56:59], v[20:23]\nds_read_b128 v[64:67], v9 offset:1024\n" \
    "buffer_load_dwordx4 v11, s[64:67], s85 offen sc1 lds\nv_mfma_f32_16x16x32_bf16 v[28:31], a[104:107], v[56:59], v[28:31]\nds_read_b128 v[56:59], v9 offset:5120\n" \
    "s_waitcnt lgkmcnt(2)\nv_mfma_f32_16x16x32_bf16 v[16:19], a[100:103], v[72:75], v[16:19]\nv_mfma_f32_16x16x32_bf16 v[24:27], a[108:111], v[72:75], v[24:27]\n" \
    "buffer_load_dwordx4 v12, s[64:67], s85 offen offset:1024 sc1 lds\nv_mfma_f32_16x16x32_bf16 v[20:23], a[100:103], v[60:63], v[20:23]\n" \
    "ds_read_b128 v[68:71], v10 offset:1024\nv_mfma_f32_16x16x32_bf16 v[28:31], a[108:111], v[60:63], v[28:31]\nds_read_b128 v[60:63], v10 offset:5120\ns_waitcnt lgkmcnt(2)\n" \
    "v_mfma_f32_16x16x32_bf16 v[16:19], a[112:115], v[64:67], v[16:19]\nv_mfma_f32_16x16x32_bf16 v[24:27], a[120:123], v[64:67], v[24:27]\n" \
    "v_mfma_f32_16x16x32_bf16 v[20:23], a[112:115], v[56:59], v[20:23]\nds_read_b128 v[64:67], v9 offset:8192\n" \
    "v_mfma_f32_16x16x32_bf16 v[28:31], a[120:123], v[56:59], v[28:31]\nds_read_b128 v[56:59], v9 offset:12288\ns_waitcnt lgkmcnt(2)\n" \
    "v_mfma_f32_16x16x32_bf16 v[16:19], a[116:119], v[68:71], v[16:19]\nv_mfma_f32_16x16x32_bf16 v[24:27], a[124:127], v[68:71], v[24:27]\n" \
    "v_mfma_f32_16x16x32_bf16 v[20:23], a[116:119], v[60:63], v[20:23]\nds_read_b128 v[68:71], v10 offset:8192\n" \
    "v_mfma_f32_16x16x32_bf16 v[28:31], a[124:127], v[60:63], v[28:31]\nds_read_b128 v[60:63], v10 offset:12288\ns_waitcnt lgkmcnt(2)\n" \
    "v_mfma_f32_16x16x32_bf16 v[16:19], a[128:131], v[64:67], v[16:19]\nv_readfirstlane_b32 s74, v13\nv_mfma_f32_16x16x32_bf16 v[24:27], a[136:139], v[64:67], v[24:27]\n" \
    "s_and_b32 s75, s74, 127\ns_lshr_b32 s69, s74, 8\nv_mfma_f32_16x16x32_bf16 v[20:23], a[128:131], v[56:59], v[20:23]\nds_read_b128 v[64:67], v9 offset:9216\n" \
    "s_sub_u32 s69, s69, s80\nv_mfma_f32_16x16x32_bf16 v[28:31], a[136:139], v[56:59], v[28:31]\nds_read_b128 v[56:59], v9 offset:13312\ns_bfe_i32 s69, s69, 0x180000\n" \
    "s_waitcnt lgkmcnt(2)\nv_mfma_f32_16x16x32_bf16 v[16:19], a[132:135], v[68:71], v[16:19]\nds_read_b128 v[72:75], v10 offset:9216\ns_cmp_eq_u32 s75, 0\n" \
    "s_cselect_b32 s69, 0, s69\nv_mfma_f32_16x16x32_bf16 v[24:27], a[140:143], v[68:71], v[24:27]\nds_read_b128 v[68:71], v10 offset:13312\ns_add_u32 s80, s80, s69\n" \
    "v_mfma_f32_16x16x32_bf16 v[20:23], a[132:135], v[60:63], v[20:23]\ns_lshl_b32 s74, s74, 5\ns_and_b32 s74, s74, 0x1000\n" \
    "v_mfma_f32_16x16x32_bf16 v[28:31], a[140:143], v[60:63], v[28:31]\ns_andn2_b32 s81, s81, 0x1000\ns_or_b32 s81, s81, s74\ns_waitcnt lgkmcnt(2)\n" \
    "v_mfma_f32_16x16x32_bf16 v[16:19], a[144:147], v[64:67], v[16:19]\nv_mfma_f32_16x16x32_bf16 v[24:27], a[152:155], v[64:67], v[24:27]\ns_waitcnt lgkmcnt(0)\ns_barrier\n" \
    "v_mfma_f32_16x16x32_bf16 v[16:19], a[148:151], v[72:75], v[16:19]\ns_add_u32 m0, s41, 0\ns_xor_b32 s85, s62, 0\n" \
    "v_mfma_f32_16x16x32_bf16 v[24:27], a[156:159], v[72:75], v[24:27]\ns_add_u32 s85, s85, s84\nbuffer_load_dwordx4 v11, s[64:67], s85 offen sc1 lds\n" \
    "v_mfma_f32_16x16x32_bf16 v[20:23], a[144:147], v[56:59], v[20:23]\ns_nop 1\nv_cvt_pk_bf16_f32 v32, v16, v17\nv_cvt_pk_bf16_f32 v33, v18, v19\n" \
    "v_mfma_f32_16x16x32_bf16 v[28:31], a[152:155], v[56:59], v[28:31]\nv_cvt_pk_bf16_f32 v34, v24, v25\nv_cvt_pk_bf16_f32 v35, v26, v27\n" \
    "v_mfma_f32_16x16x32_bf16 v[20:23], a[148:151], v[68:71], v[20:23]\nbuffer_load_dwordx4 v12, s[64:67], s85 offen offset:1024 sc1 lds\ns_add_u32 m0, s41, 8192\n" \
    "v_mfma_f32_16x16x32_bf16 v[28:31], a[156:159], v[68:71], v[28:31]\ns_xor_b32 s85, s62, 256\ns_add_u32 s85, s85, s84\nds_write_b128 v14, v[32:35] offset:0\n" \
    "buffer_load_dwordx4 v11, s[64:67], s85 offen sc1 lds\nbuffer_load_dwordx4 v12, s[64:67], s85 offen offset:1024 sc1 lds\nv_cvt_pk_bf16_f32 v36, v20, v21\n" \
    "v_cvt_pk_bf16_f32 v37, v22, v23\ns_mov_b32 s40, s84\nv_cvt_pk_bf16_f32 v38, v28, v29\nv_cvt_pk_bf16_f32 v39, v30, v31\nds_write_b128 v14, v[36:39] offset:4096\n.Lv4_%=:\n" \
    "s_lshr_b32 s69, s35, 1\ns_cmp_eq_u32 s69, 5\ns_cbranch_scc1 .Lv12_%=\n.Lv13_%=:\ns_cmp_lt_u32 s75, s35\ns_cbranch_scc1 .Lv14_%=\ns_cmp_eq_u32 s36, 1\n" \
    "s_cbranch_scc1 .Lv9_%=\ns_cmp_eq_u32 s36, 2\ns_cbranch_scc1 .Lv10_%=\ns_cmp_eq_u32 s36, 3\ns_cbranch_scc1 .Lv11_%=\n.Lv8_%=:\nds_read_b128 v[24:27], v3 offset:0\n" \
    "ds_read_b128 v[28:31], v3 offset:1024\nds_read_b128 v[32:35], v3 offset:8192\nds_read_b128 v[36:39], v3 offset:9216\nds_read_b128 v[40:43], v3 offset:16384\n" \
    "ds_read_b128 v[44:47], v3 offset:17408\nds_read_b128 v[48:51], v3 offset:2048\nds_read_b128 v[52:55], v3 offset:3072\nds_read_b128 v[56:59], v3 offset:4096\n" \
    "ds_read_b128 v[60:63], v3 offset:5120\nds_read_b128 v[64:67], v3 offset:6144\nds_read_b128 v[68:71], v3 offset:7168\n.Lv16_%=:\ns_memrealtime s[72:73]\n" \
    "s_waitcnt lgkmcnt(0)\ns_mov_b32 s68, 0\ns_sub_u32 s76, s80, s45\ns_sub_u32 s69, s72, s76\ns_cmp_lt_i32 s69, -8\ns_cbranch_scc1 .Lv19_%=\n.Lv17_%=:\n" \
    "s_xor_b32 s90, s81, 0x800\n.Lv21_%=:\ns_memrealtime s[72:73]\ns_waitcnt lgkmcnt(0)\ns_sub_u32 s72, s72, s80\ns_cmp_lt_i32 s72, 0\ns_cbranch_scc1 .Lv21_%=\n" \
    "buffer_load_dwordx4 v[72:75], v2, s[64:67], s81 offen offset:0 sc1\ns_nop 7\nbuffer_load_dwordx4 v[76:79], v2, s[64:67], s81 offen offset:1024 sc1\ns_nop 7\n" \
    "buffer_load_dwordx4 v[80:83], v2, s[64:67], s90 offen offset:0 sc1\ns_nop 7\nbuffer_load_dwordx4 v[84:87], v2, s[64:67], s90 offen offset:1024 sc1\nv_mov_b64 v[16:17], 0\n" \
    "v_mov_b64 v[18:19], 0\nv_mov_b64 v[20:21], 0\nv_mov_b64 v[22:23], 0\ns_waitcnt vmcnt(3)\ns_nop 0\nv_smfmac_f32_16x16x64_bf16 v[20:23], v[72:75], v[24:31], v0\n" \
    "ds_write_b128 v1, v[72:75] offset:24576\nv_max3_u32 v252, v72, v73, v74\nv_smfmac_f32_16x16x64_bf16 v[16:19], v[72:75], a[0:7], v0\ns_waitcnt vmcnt(2)\n" \
    "ds_write_b128 v1, v[76:79] offset:28672\nv_max3_u32 v252, v252, v75, v76\nv_max3_u32 v252, v252, v77, v78\nv_cmp_u_f32 vcc, v252, v79\n" \
    "v_smfmac_f32_16x16x64_bf16 v[20:23], v[76:79], v[32:39], v0\ns_cbranch_vccnz .Lv27_%=\n.Lv23_%=:\nv_smfmac_f32_16x16x64_bf16 v[16:19], v[76:79], a[32:39], v0\n" \
    "ds_read_b128 v[24:27], v3 offset:10240\nds_read_b128 v[28:31], v3 offset:11264\ns_waitcnt vmcnt(1)\nv_max3_u32 v252, v80, v81, v82\nv_cmp_u_f32 vcc, v252, v83\ns_nop 0\n" \
    "s_cbranch_vccnz .Lv28_%=\n.Lv24_%=:\nv_smfmac_f32_16x16x64_bf16 v[20:23], v[80:83], v[40:47], v0\nds_write_b128 v1, v[80:83] offset:32768\n" \
    "ds_read_b128 v[36:39], v3 offset:13312\ns_waitcnt lgkmcnt(4)\ns_barrier\nds_read_b128 v[88:91], v1 offset:25600\nds_read_b128 v[40:43], v1 offset:26624\n" \
    "v_smfmac_f32_16x16x64_bf16 v[16:19], v[80:83], a[64:71], v0\nds_read_b128 v[32:35], v3 offset:12288\nds_read_b128 v[44:47], v1 offset:27648\ns_waitcnt vmcnt(0)\n" \
    "v_max3_u32 v252, v84, v85, v86\nv_cmp_u_f32 vcc, v252, v87\ns_nop 0\ns_cbranch_vccnz .Lv29_%=\n.Lv25_%=:\nv_smfmac_f32_16x16x64_bf16 v[20:23], v[84:87], a[104:111], v0\n" \
    "ds_write_b128 v1, v[84:87] offset:36864\nv_smfmac_f32_16x16x64_bf16 v[16:19], v[84:87], a[96:103], v0\nds_read_b128 v[72:75], v1 offset:29696\ns_waitcnt lgkmcnt(5)\n" \
    "v_smfmac_f32_16x16x64_bf16 v[20:23], v[88:91], v[48:55], v0\nds_read_b128 v[48:51], v1 offset:30720\nv_smfmac_f32_16x16x64_bf16 v[16:19], v[88:91], a[8:15], v0\n" \
    "ds_read_b128 v[52:55], v1 offset:31744\ns_waitcnt lgkmcnt(6)\nv_smfmac_f32_16x16x64_bf16 v[20:23], v[40:43], v[56:63], v0\nds_read_b128 v[56:59], v3 offset:14336\n" \
    "s_waitcnt lgkmcnt(4)\ns_barrier\nv_smfmac_f32_16x16x64_bf16 v[16:19], v[40:43], a[16:23], v0\nds_read_b128 v[60:63], v3 offset:15360\n" \
    "v_smfmac_f32_16x16x64_bf16 v[20:23], v[44:47], v[64:71], v0\nds_read_b128 v[40:43], v1 offset:33792\nv_smfmac_f32_16x16x64_bf16 v[16:19], v[44:47], a[24:31], v0\n" \
    "ds_read_b128 v[64:67], v3 offset:18432\ns_waitcnt lgkmcnt(6)\nv_smfmac_f32_16x16x64_bf16 v[20:23], v[72:75], v[24:31], v0\nds_read_b128 v[68:71], v3 offset:19456\n" \
    "v_smfmac_f32_16x16x64_bf16 v[16:19], v[72:75], a[40:47], v0\nds_read_b128 v[44:47], v1 offset:34816\ns_waitcnt lgkmcnt(7)\n" \
    "v_smfmac_f32_16x16x64_bf16 v[20:23], v[48:51], v[32:39], v0\nds_read_b128 v[24:27], v3 offset:20480\nv_smfmac_f32_16x16x64_bf16 v[16:19], v[48:51], a[48:55], v0\n" \
    "ds_read_b128 v[28:31], v3 offset:21504\ns_waitcnt lgkmcnt(6)\nv_smfmac_f32_16x16x64_bf16 v[20:23], v[52:55], v[56:63], v0\nds_read_b128 v[48:51], v1 offset:35840\n" \
    "v_smfmac_f32_16x16x64_bf16 v[16:19], v[52:55], a[56:63], v0\nds_read_b128 v[32:35], v3 offset:22528\ns_waitcnt lgkmcnt(5)\n" \
    "v_smfmac_f32_16x16x64_bf16 v[20:23], v[40:43], v[64:71], v0\nds_read_b128 v[36:39], v3 offset:23552\nv_smfmac_f32_16x16x64_bf16 v[16:19], v[40:43], a[72:79], v0\n" \
    "ds_read_b128 v[40:43], v1 offset:37888\ns_waitcnt lgkmcnt(4)\nv_smfmac_f32_16x16x64_bf16 v[20:23], v[44:47], v[24:31], v0\nds_read_b128 v[24:27], v1 offset:38912\n" \
    "v_smfmac_f32_16x16x64_bf16 v[16:19], v[44:47], a[80:87], v0\nds_read_b128 v[28:31], v1 offset:39936\ns_waitcnt lgkmcnt(3)\n" \
    "v_smfmac_f32_16x16x64_bf16 v[20:23], v[48:51], v[32:39], v0\ns_waitcnt lgkmcnt(2)\nv_smfmac_f32_16x16x64_bf16 v[20:23], v[40:43], a[120:127], v0\ns_waitcnt lgkmcnt(1)\n" \
    "v_smfmac_f32_16x16x64_bf16 v[20:23], v[24:27], a[136:143], v0\ns_waitcnt lgkmcnt(0)\nv_smfmac_f32_16x16x64_bf16 v[20:23], v[28:31], a[152:159], v0\ns_nop 2\n" \
    "v_smfmac_f32_16x16x64_bf16 v[16:19], v[48:51], a[88:95], v0\ns_nop 2\nv_smfmac_f32_16x16x64_bf16 v[16:19], v[40:43], a[112:119], v0\nv_permlane32_swap_b32 v20, v22\n" \
    "v_smfmac_f32_16x16x64_bf16 v[16:19], v[24:27], a[128:135], v0\nv_permlane32_swap_b32 v21, v23\nv_smfmac_f32_16x16x64_bf16 v[16:19], v[28:31], a[144:151], v0\n" \
    "v_add_f32 v20, v20, v22\nv_add_f32 v21, v21, v23\nv_cvt_pk_bf16_f32 v20, v20, v21\nds_write_b16 v4, v20 offset:8\nds_write_b16_d16_hi v4, v20 offset:24\n" \
    "v_mov_b32 v15, v15\nv_mov_b32 v15, v15\nv_mov_b32 v15, v15\nv_permlane32_swap_b32 v16, v18\nv_permlane32_swap_b32 v17, v19\nv_pk_add_f32 v[16:17], v[16:17], v[18:19]\n" \
    "v_cvt_pk_bf16_f32 v16, v16, v17\nds_write_b16 v4, v16 offset:0\nds_write_b16_d16_hi v4, v16 offset:16\ns_branch .Lv7_%=\n.Lv9_%=:\nds_read_b128 v[24:27], v3 offset:2048\n" \
    "ds_read_b128 v[28:31], v3 offset:3072\nds_read_b128 v[32:35], v3 offset:10240\nds_read_b128 v[36:39], v3 offset:11264\nds_read_b128 v[40:43], v3 offset:18432\n" \
    "ds_read_b128 v[44:47], v3 offset:19456\nds_read_b128 v[48:51], v3 offset:0\nds_read_b128 v[52:55], v3 offset:1024\nds_read_b128 v[56:59], v3 offset:4096\n" \
    "ds_read_b128 v[60:63], v3 offset:5120\nds_read_b128 v[64:67], v3 offset:6144\nds_read_b128 v[68:71], v3 offset:7168\n.Lv32_%=:\ns_memrealtime s[72:73]\n" \
    "s_waitcnt lgkmcnt(0)\ns_mov_b32 s68, 0\ns_sub_u32 s76, s80, s45\ns_sub_u32 s69, s72, s76\ns_cmp_lt_i32 s69, -8\ns_cbranch_scc1 .Lv35_%=\n.Lv33_%=:\n" \
    "s_xor_b32 s90, s81, 0x800\n.Lv37_%=:\ns_memrealtime s[72:73]\ns_waitcnt lgkmcnt(0)\ns_sub_u32 s72, s72, s80\ns_cmp_lt_i32 s72, 0\ns_cbranch_scc1 .Lv37_%=\n" \
    "buffer_load_dwordx4 v[72:75], v2, s[64:67], s81 offen offset:0 sc1\ns_nop 7\nbuffer_load_dwordx4 v[76:79], v2, s[64:67], s81 offen offset:1024 sc1\ns_nop 7\n" \
    "buffer_load_dwordx4 v[80:83], v2, s[64:67], s90 offen offset:0 sc1\ns_nop 7\nbuffer_load_dwordx4 v[84:87], v2, s[64:67], s90 offen offset:1024 sc1\nv_mov_b64 v[16:17], 0\n" \
    "v_mov_b64 v[18:19], 0\nv_mov_b64 v[20:21], 0\nv_mov_b64 v[22:23], 0\ns_waitcnt vmcnt(3)\ns_nop 0\nv_smfmac_f32_16x16x64_bf16 v[20:23], v[72:75], v[24:31], v0\n" \
    "ds_write_b128 v1, v[72:75] offset:25600\nv_max3_u32 v252, v72, v73, v74\nv_smfmac_f32_16x16x64_bf16 v[16:19], v[72:75], a[8:15], v0\ns_waitcnt vmcnt(2)\n" \
    "ds_write_b128 v1, v[76:79] offset:29696\nv_max3_u32 v252, v252, v75, v76\nv_max3_u32 v252, v252, v77, v78\nv_cmp_u_f32 vcc, v252, v79\n" \
    "v_smfmac_f32_16x16x64_bf16 v[20:23], v[76:79], v[32:39], v0\ns_cbranch_vccnz .Lv43_%=\n.Lv39_%=:\nv_smfmac_f32_16x16x64_bf16 v[16:19], v[76:79], a[40:47], v0\n" \
    "ds_read_b128 v[24:27], v3 offset:8192\nds_read_b128 v[28:31], v3 offset:9216\ns_waitcnt vmcnt(1)\nv_max3_u32 v252, v80, v81, v82\nv_cmp_u_f32 vcc, v252, v83\ns_nop 0\n" \
    "s_cbranch_vccnz .Lv44_%=\n.Lv40_%=:\nv_smfmac_f32_16x16x64_bf16 v[20:23], v[80:83], v[40:47], v0\nds_write_b128 v1, v[80:83] offset:33792\n" \
    "ds_read_b128 v[36:39], v3 offset:13312\ns_waitcnt lgkmcnt(4)\ns_barrier\nds_read_b128 v[88:91], v1 offset:24576\nds_read_b128 v[40:43], v1 offset:26624\n" \
    "v_smfmac_f32_16x16x64_bf16 v[16:19], v[80:83], a[72:79], v0\nds_read_b128 v[32:35], v3 offset:12288\nds_read_b128 v[44:47], v1 offset:27648\ns_waitcnt vmcnt(0)\n" \
    "v_max3_u32 v252, v84, v85, v86\nv_cmp_u_f32 vcc, v252, v87\ns_nop 0\ns_cbranch_vccnz .Lv45_%=\n.Lv41_%=:\nv_smfmac_f32_16x16x64_bf16 v[20:23], v[84:87], a[120:127], v0\n" \
    "ds_write_b128 v1, v[84:87] offset:37888\nv_smfmac_f32_16x16x64_bf16 v[16:19], v[84:87], a[112:119], v0\nds_read_b128 v[72:75], v1 offset:28672\ns_waitcnt lgkmcnt(5)\n" \
    "v_smfmac_f32_16x16x64_bf16 v[20:23], v[88:91], v[48:55], v0\nds_read_b128 v[48:51], v1 offset:30720\nv_smfmac_f32_16x16x64_bf16 v[16:19], v[88:91], a[0:7], v0\n" \
    "ds_read_b128 v[52:55], v1 offset:31744\ns_waitcnt lgkmcnt(6)\nv_smfmac_f32_16x16x64_bf16 v[20:23], v[40:43], v[56:63], v0\nds_read_b128 v[56:59], v3 offset:14336\n" \
    "s_waitcnt lgkmcnt(4)\ns_barrier\nv_smfmac_f32_16x16x64_bf16 v[16:19], v[40:43], a[16:23], v0\nds_read_b128 v[60:63], v3 offset:15360\n" \
    "v_smfmac_f32_16x16x64_bf16 v[20:23], v[44:47], v[64:71], v0\nds_read_b128 v[40:43], v1 offset:32768\nv_smfmac_f32_16x16x64_bf16 v[16:19], v[44:47], a[24:31], v0\n" \
    "ds_read_b128 v[64:67], v3 offset:16384\ns_waitcnt lgkmcnt(6)\nv_smfmac_f32_16x16x64_bf16 v[20:23], v[72:75], v[24:31], v0\nds_read_b128 v[68:71], v3 offset:17408\n" \
    "v_smfmac_f32_16x16x64_bf16 v[16:19], v[72:75], a[32:39], v0\nds_read_b128 v[44:47], v1 offset:34816\ns_waitcnt lgkmcnt(7)\n" \
    "v_smfmac_f32_16x16x64_bf16 v[20:23], v[48:51], v[32:39], v0\nds_read_b128 v[24:27], v3 offset:20480\nv_smfmac_f32_16x16x64_bf16 v[16:19], v[48:51], a[48:55], v0\n" \
    "ds_read_b128 v[28:31], v3 offset:21504\ns_waitcnt lgkmcnt(6)\nv_smfmac_f32_16x16x64_bf16 v[20:23], v[52:55], v[56:63], v0\nds_read_b128 v[48:51], v1 offset:35840\n" \
    "v_smfmac_f32_16x16x64_bf16 v[16:19], v[52:55], a[56:63], v0\nds_read_b128 v[32:35], v3 offset:22528\ns_waitcnt lgkmcnt(5)\n" \
    "v_smfmac_f32_16x16x64_bf16 v[20:23], v[40:43], v[64:71], v0\nds_read_b128 v[36:39], v3 offset:23552\nv_smfmac_f32_16x16x64_bf16 v[16:19], v[40:43], a[64:71], v0\n" \
    "ds_read_b128 v[40:43], v1 offset:36864\ns_waitcnt lgkmcnt(4)\nv_smfmac_f32_16x16x64_bf16 v[20:23], v[44:47], v[24:31], v0\nds_read_b128 v[24:27], v1 offset:38912\n" \
    "v_smfmac_f32_16x16x64_bf16 v[16:19], v[44:47], a[80:87], v0\nds_read_b128 v[28:31], v1 offset:39936\ns_waitcnt lgkmcnt(3)\n" \
    "v_smfmac_f32_16x16x64_bf16 v[20:23], v[48:51], v[32:39], v0\ns_waitcnt lgkmcnt(2)\nv_smfmac_f32_16x16x64_bf16 v[20:23], v[40:43], a[104:111], v0\ns_waitcnt lgkmcnt(1)\n" \
    "v_smfmac_f32_16x16x64_bf16 v[20:23], v[24:27], a[136:143], v0\ns_waitcnt lgkmcnt(0)\nv_smfmac_f32_16x16x64_bf16 v[20:23], v[28:31], a[152:159], v0\ns_nop 2\n" \
    "v_smfmac_f32_16x16x64_bf16 v[16:19], v[48:51], a[88:95], v0\ns_nop 2\nv_smfmac_f32_16x16x64_bf16 v[16:19], v[40:43], a[96:103], v0\nv_permlane32_swap_b32 v20, v22\n" \
    "v_smfmac_f32_16x16x64_bf16 v[16:19], v[24:27], a[128:135], v0\nv_permlane32_swap_b32 v21, v23\nv_smfmac_f32_16x16x64_bf16 v[16:19], v[28:31], a[144:151], v0\n" \
    "v_add_f32 v20, v20, v22\nv_add_f32 v21, v21, v23\nv_cvt_pk_bf16_f32 v20, v20, v21\nds_write_b16 v4, v20 offset:8\nds_write_b16_d16_hi v4, v20 offset:24\n" \
    "v_mov_b32 v15, v15\nv_mov_b32 v15, v15\nv_mov_b32 v15, v15\nv_permlane32_swap_b32 v16, v18\nv_permlane32_swap_b32 v17, v19\nv_pk_add_f32 v[16:17], v[16:17], v[18:19]\n" \
    "v_cvt_pk_bf16_f32 v16, v16, v17\nds_write_b16 v4, v16 offset:0\nds_write_b16_d16_hi v4, v16 offset:16\ns_branch .Lv7_%=\n.Lv10_%=:\nds_read_b128 v[24:27], v3 offset:4096\n" \
    "ds_read_b128 v[28:31], v3 offset:5120\nds_read_b128 v[32:35], v3 offset:12288\nds_read_b128 v[36:39], v3 offset:13312\nds_read_b128 v[40:43], v3 offset:20480\n" \
    "ds_read_b128 v[44:47], v3 offset:21504\nds_read_b128 v[48:51], v3 offset:0\nds_read_b128 v[52:55], v3 offset:1024\nds_read_b128 v[56:59], v3 offset:2048\n" \
    "ds_read_b128 v[60:63], v3 offset:3072\nds_read_b128 v[64:67], v3 offset:6144\nds_read_b128 v[68:71], v3 offset:7168\n.Lv48_%=:\ns_memrealtime s[72:73]\n" \
    "s_waitcnt lgkmcnt(0)\ns_mov_b32 s68, 0\ns_sub_u32 s76, s80, s45\ns_sub_u32 s69, s72, s76\ns_cmp_lt_i32 s69, -8\ns_cbranch_scc1 .Lv51_%=\n.Lv49_%=:\n" \
    "s_xor_b32 s90, s81, 0x800\n.Lv53_%=:\ns_memrealtime s[72:73]\ns_waitcnt lgkmcnt(0)\ns_sub_u32 s72, s72, s80\ns_cmp_lt_i32 s72, 0\ns_cbranch_scc1 .Lv53_%=\n" \
    "buffer_load_dwordx4 v[72:75], v2, s[64:67], s81 offen offset:0 sc1\ns_nop 7\nbuffer_load_dwordx4 v[76:79], v2, s[64:67], s81 offen offset:1024 sc1\ns_nop 7\n" \
    "buffer_load_dwordx4 v[80:83], v2, s[64:67], s90 offen offset:0 sc1\ns_nop 7\nbuffer_load_dwordx4 v[84:87], v2, s[64:67], s90 offen offset:1024 sc1\nv_mov_b64 v[16:17], 0\n" \
    "v_mov_b64 v[18:19], 0\nv_mov_b64 v[20:21], 0\nv_mov_b64 v[22:23], 0\ns_waitcnt vmcnt(3)\ns_nop 0\nv_smfmac_f32_16x16x64_bf16 v[20:23], v[72:75], v[24:31], v0\n" \
    "ds_write_b128 v1, v[72:75] offset:26624\nv_max3_u32 v252, v72, v73, v74\nv_smfmac_f32_16x16x64_bf16 v[16:19], v[72:75], a[16:23], v0\ns_waitcnt vmcnt(2)\n" \
    "ds_write_b128 v1, v[76:79] offset:30720\nv_max3_u32 v252, v252, v75, v76\nv_max3_u32 v252, v252, v77, v78\nv_cmp_u_f32 vcc, v252, v79\n" \
    "v_smfmac_f32_16x16x64_bf16 v[20:23], v[76:79], v[32:39], v0\ns_cbranch_vccnz .Lv59_%=\n.Lv55_%=:\nv_smfmac_f32_16x16x64_bf16 v[16:19], v[76:79], a[48:55], v0\n" \
    "ds_read_b128 v[24:27], v3 offset:8192\nds_read_b128 v[28:31], v3 offset:9216\ns_waitcnt vmcnt(1)\nv_max3_u32 v252, v80, v81, v82\nv_cmp_u_f32 vcc, v252, v83\ns_nop 0\n" \
    "s_cbranch_vccnz .Lv60_%=\n.Lv56_%=:\nv_smfmac_f32_16x16x64_bf16 v[20:23], v[80:83], v[40:47], v0\nds_write_b128 v1, v[80:83] offset:34816\n" \
    "ds_read_b128 v[36:39], v3 offset:11264\ns_waitcnt lgkmcnt(4)\ns_barrier\nds_read_b128 v[88:91], v1 offset:24576\nds_read_b128 v[40:43], v1 offset:25600\n" \
    "v_smfmac_f32_16x16x64_bf16 v[16:19], v[80:83], a[80:87], v0\nds_read_b128 v[32:35], v3 offset:10240\nds_read_b128 v[44:47], v1 offset:27648\ns_waitcnt vmcnt(0)\n" \
    "v_max3_u32 v252, v84, v85, v86\nv_cmp_u_f32 vcc, v252, v87\ns_nop 0\ns_cbranch_vccnz .Lv61_%=\n.Lv57_%=:\nv_smfmac_f32_16x16x64_bf16 v[20:23], v[84:87], a[136:143], v0\n" \
    "ds_write_b128 v1, v[84:87] offset:38912\nv_smfmac_f32_16x16x64_bf16 v[16:19], v[84:87], a[128:135], v0\nds_read_b128 v[72:75], v1 offset:28672\ns_waitcnt lgkmcnt(5)\n" \
    "v_smfmac_f32_16x16x64_bf16 v[20:23], v[88:91], v[48:55], v0\nds_read_b128 v[48:51], v1 offset:29696\nv_smfmac_f32_16x16x64_bf16 v[16:19], v[88:91], a[0:7], v0\n" \
    "ds_read_b128 v[52:55], v1 offset:31744\ns_waitcnt lgkmcnt(6)\nv_smfmac_f32_16x16x64_bf16 v[20:23], v[40:43], v[56:63], v0\nds_read_b128 v[56:59], v3 offset:14336\n" \
    "s_waitcnt lgkmcnt(4)\ns_barrier\nv_smfmac_f32_16x16x64_bf16 v[16:19], v[40:43], a[8:15], v0\nds_read_b128 v[60:63], v3 offset:15360\n" \
    "v_smfmac_f32_16x16x64_bf16 v[20:23], v[44:47], v[64:71], v0\nds_read_b128 v[40:43], v1 offset:32768\nv_smfmac_f32_16x16x64_bf16 v[16:19], v[44:47], a[24:31], v0\n" \
    "ds_read_b128 v[64:67], v3 offset:16384\ns_waitcnt lgkmcnt(6)\nv_smfmac_f32_16x16x64_bf16 v[20:23], v[72:75], v[24:31], v0\nds_read_b128 v[68:71], v3 offset:17408\n" \
    "v_smfmac_f32_16x16x64_bf16 v[16:19], v[72:75], a[32:39], v0\nds_read_b128 v[44:47], v1 offset:33792\ns_waitcnt lgkmcnt(7)\n" \
    "v_smfmac_f32_16x16x64_bf16 v[20:23], v[48:51], v[32:39], v0\nds_read_b128 v[24:27], v3 offset:18432\nv_smfmac_f32_16x16x64_bf16 v[16:19], v[48:51], a[40:47], v0\n" \
    "ds_read_b128 v[28:31], v3 offset:19456\ns_waitcnt lgkmcnt(6)\nv_smfmac_f32_16x16x64_bf16 v[20:23], v[52:55], v[56:63], v0\nds_read_b128 v[48:51], v1 offset:35840\n" \
    "v_smfmac_f32_16x16x64_bf16 v[16:19], v[52:55], a[56:63], v0\nds_read_b128 v[32:35], v3 offset:22528\ns_waitcnt lgkmcnt(5)\n" \
    "v_smfmac_f32_16x16x64_bf16 v[20:23], v[40:43], v[64:71], v0\nds_read_b128 v[36:39], v3 offset:23552\nv_smfmac_f32_16x16x64_bf16 v[16:19], v[40:43], a[64:71], v0\n" \
    "ds_read_b128 v[40:43], v1 offset:36864\ns_waitcnt lgkmcnt(4)\nv_smfmac_f32_16x16x64_bf16 v[20:23], v[44:47], v[24:31], v0\nds_read_b128 v[24:27], v1 offset:37888\n" \
    "v_smfmac_f32_16x16x64_bf16 v[16:19], v[44:47], a[72:79], v0\nds_read_b128 v[28:31], v1 offset:39936\ns_waitcnt lgkmcnt(3)\n" \
    "v_smfmac_f32_16x16x64_bf16 v[20:23], v[48:51], v[32:39], v0\ns_waitcnt lgkmcnt(2)\nv_smfmac_f32_16x16x64_bf16 v[20:23], v[40:43], a[104:111], v0\ns_waitcnt lgkmcnt(1)\n" \
    "v_smfmac_f32_16x16x64_bf16 v[20:23], v[24:27], a[120:127], v0\ns_waitcnt lgkmcnt(0)\nv_smfmac_f32_16x16x64_bf16 v[20:23], v[28:31], a[152:159], v0\ns_nop 2\n" \
    "v_smfmac_f32_16x16x64_bf16 v[16:19], v[48:51], a[88:95], v0\ns_nop 2\nv_smfmac_f32_16x16x64_bf16 v[16:19], v[40:43], a[96:103], v0\nv_permlane32_swap_b32 v20, v22\n" \
    "v_smfmac_f32_16x16x64_bf16 v[16:19], v[24:27], a[112:119], v0\nv_permlane32_swap_b32 v21, v23\nv_smfmac_f32_16x16x64_bf16 v[16:19], v[28:31], a[144:151], v0\n" \
    "v_add_f32 v20, v20, v22\nv_add_f32 v21, v21, v23\nv_cvt_pk_bf16_f32 v20, v20, v21\nds_write_b16 v4, v20 offset:8\nds_write_b16_d16_hi v4, v20 offset:24\n" \
    "v_mov_b32 v15, v15\nv_mov_b32 v15, v15\nv_mov_b32 v15, v15\nv_permlane32_swap_b32 v16, v18\nv_permlane32_swap_b32 v17, v19\nv_pk_add_f32 v[16:17], v[16:17], v[18:19]\n" \
    "v_cvt_pk_bf16_f32 v16, v16, v17\nds_write_b16 v4, v16 offset:0\nds_write_b16_d16_hi v4, v16 offset:16\ns_branch .Lv7_%=\n.Lv11_%=:\nds_read_b128 v[24:27], v3 offset:6144\n" \
    "ds_read_b128 v[28:31], v3 offset:7168\nds_read_b128 v[32:35], v3 offset:14336\nds_read_b128 v[36:39], v3 offset:15360\nds_read_b128 v[40:43], v3 offset:22528\n" \
    "ds_read_b128 v[44:47], v3 offset:23552\nds_read_b128 v[48:51], v3 offset:0\nds_read_b128 v[52:55], v3 offset:1024\nds_read_b128 v[56:59], v3 offset:2048\n" \
    "ds_read_b128 v[60:63], v3 offset:3072\nds_read_b128 v[64:67], v3 offset:4096\nds_read_b128 v[68:71], v3 offset:5120\n.Lv64_%=:\ns_memrealtime s[72:73]\n" \
    "s_waitcnt lgkmcnt(0)\ns_mov_b32 s68, 0\ns_sub_u32 s76, s80, s45\ns_sub_u32 s69, s72, s76\ns_cmp_lt_i32 s69, -8\ns_cbranch_scc1 .Lv67_%=\n.Lv65_%=:\n" \
    "s_xor_b32 s90, s81, 0x800\n.Lv69_%=:\ns_memrealtime s[72:73]\ns_waitcnt lgkmcnt(0)\ns_sub_u32 s72, s72, s80\ns_cmp_lt_i32 s72, 0\ns_cbranch_scc1 .Lv69_%=\n" \
    "buffer_load_dwordx4 v[72:75], v2, s[64:67], s81 offen offset:0 sc1\ns_nop 7\nbuffer_load_dwordx4 v[76:79], v2, s[64:67], s81 offen offset:1024 sc1\ns_nop 7\n" \
    "buffer_load_dwordx4 v[80:83], v2, s[64:67], s90 offen offset:0 sc1\ns_nop 7\nbuffer_load_dwordx4 v[84:87], v2, s[64:67], s90 offen offset:1024 sc1\nv_mov_b64 v[16:17], 0\n" \
    "v_mov_b64 v[18:19], 0\nv_mov_b64 v[20:21], 0\nv_mov_b64 v[22:23], 0\ns_waitcnt vmcnt(3)\ns_nop 0\nv_smfmac_f32_16x16x64_bf16 v[20:23], v[72:75], v[24:31], v0\n" \
    "ds_write_b128 v1, v[72:75] offset:27648\nv_max3_u32 v252, v72, v73, v74\nv_smfmac_f32_16x16x64_bf16 v[16:19], v[72:75], a[24:31], v0\ns_waitcnt vmcnt(2)\n" \
    "ds_write_b128 v1, v[76:79] offset:31744\nv_max3_u32 v252, v252, v75, v76\nv_max3_u32 v252, v252, v77, v78\nv_cmp_u_f32 vcc, v252, v79\n" \
    "v_smfmac_f32_16x16x64_bf16 v[20:23], v[76:79], v[32:39], v0\ns_cbranch_vccnz .Lv75_%=\n.Lv71_%=:\nv_smfmac_f32_16x16x64_bf16 v[16:19], v[76:79], a[56:63], v0\n" \
    "ds_read_b128 v[24:27], v3 offset:8192\nds_read_b128 v[28:31], v3 offset:9216\ns_waitcnt vmcnt(1)\nv_max3_u32 v252, v80, v81, v82\nv_cmp_u_f32 vcc, v252, v83\ns_nop 0\n" \
    "s_cbranch_vccnz .Lv76_%=\n.Lv72_%=:\nv_smfmac_f32_16x16x64_bf16 v[20:23], v[80:83], v[40:47], v0\nds_write_b128 v1, v[80:83] offset:35840\n" \
    "ds_read_b128 v[36:39], v3 offset:11264\ns_waitcnt lgkmcnt(4)\ns_barrier\nds_read_b128 v[88:91], v1 offset:24576\nds_read_b128 v[40:43], v1 offset:25600\n" \
    "v_smfmac_f32_16x16x64_bf16 v[16:19], v[80:83], a[88:95], v0\nds_read_b128 v[32:35], v3 offset:10240\nds_read_b128 v[44:47], v1 offset:26624\ns_waitcnt vmcnt(0)\n" \
    "v_max3_u32 v252, v84, v85, v86\nv_cmp_u_f32 vcc, v252, v87\ns_nop 0\ns_cbranch_vccnz .Lv77_%=\n.Lv73_%=:\nv_smfmac_f32_16x16x64_bf16 v[20:23], v[84:87], a[152:159], v0\n" \
    "ds_write_b128 v1, v[84:87] offset:39936\nv_smfmac_f32_16x16x64_bf16 v[16:19], v[84:87], a[144:151], v0\nds_read_b128 v[72:75], v1 offset:28672\ns_waitcnt lgkmcnt(5)\n" \
    "v_smfmac_f32_16x16x64_bf16 v[20:23], v[88:91], v[48:55], v0\nds_read_b128 v[48:51], v1 offset:29696\nv_smfmac_f32_16x16x64_bf16 v[16:19], v[88:91], a[0:7], v0\n" \
    "ds_read_b128 v[52:55], v1 offset:30720\ns_waitcnt lgkmcnt(6)\nv_smfmac_f32_16x16x64_bf16 v[20:23], v[40:43], v[56:63], v0\nds_read_b128 v[56:59], v3 offset:12288\n" \
    "s_waitcnt lgkmcnt(4)\ns_barrier\nv_smfmac_f32_16x16x64_bf16 v[16:19], v[40:43], a[8:15], v0\nds_read_b128 v[60:63], v3 offset:13312\n" \
    "v_smfmac_f32_16x16x64_bf16 v[20:23], v[44:47], v[64:71], v0\nds_read_b128 v[40:43], v1 offset:32768\nv_smfmac_f32_16x16x64_bf16 v[16:19], v[44:47], a[16:23], v0\n" \
    "ds_read_b128 v[64:67], v3 offset:16384\ns_waitcnt lgkmcnt(6)\nv_smfmac_f32_16x16x64_bf16 v[20:23], v[72:75], v[24:31], v0\nds_read_b128 v[68:71], v3 offset:17408\n" \
    "v_smfmac_f32_16x16x64_bf16 v[16:19], v[72:75], a[32:39], v0\nds_read_b128 v[44:47], v1 offset:33792\ns_waitcnt lgkmcnt(7)\n" \
    "v_smfmac_f32_16x16x64_bf16 v[20:23], v[48:51], v[32:39], v0\nds_read_b128 v[24:27], v3 offset:18432\nv_smfmac_f32_16x16x64_bf16 v[16:19], v[48:51], a[40:47], v0\n" \
    "ds_read_b128 v[28:31], v3 offset:19456\ns_waitcnt lgkmcnt(6)\nv_smfmac_f32_16x16x64_bf16 v[20:23], v[52:55], v[56:63], v0\nds_read_b128 v[48:51], v1 offset:34816\n" \
    "v_smfmac_f32_16x16x64_bf16 v[16:19], v[52:55], a[48:55], v0\nds_read_b128 v[32:35], v3 offset:20480\ns_waitcnt lgkmcnt(5)\n" \
    "v_smfmac_f32_16x16x64_bf16 v[20:23], v[40:43], v[64:71], v0\nds_read_b128 v[36:39], v3 offset:21504\nv_smfmac_f32_16x16x64_bf16 v[16:19], v[40:43], a[64:71], v0\n" \
    "ds_read_b128 v[40:43], v1 offset:36864\ns_waitcnt lgkmcnt(4)\nv_smfmac_f32_16x16x64_bf16 v[20:23], v[44:47], v[24:31], v0\nds_read_b128 v[24:27], v1 offset:37888\n" \
    "v_smfmac_f32_16x16x64_bf16 v[16:19], v[44:47], a[72:79], v0\nds_read_b128 v[28:31], v1 offset:38912\ns_waitcnt lgkmcnt(3)\n" \
    "v_smfmac_f32_16x16x64_bf16 v[20:23], v[48:51], v[32:39], v0\ns_waitcnt lgkmcnt(2)\nv_smfmac_f32_16x16x64_bf16 v[20:23], v[40:43], a[104:111], v0\ns_waitcnt lgkmcnt(1)\n" \
    "v_smfmac_f32_16x16x64_bf16 v[20:23], v[24:27], a[120:127], v0\ns_waitcnt lgkmcnt(0)\nv_smfmac_f32_16x16x64_bf16 v[20:23], v[28:31], a[136:143], v0\ns_nop 2\n" \
    "v_smfmac_f32_16x16x64_bf16 v[16:19], v[48:51], a[80:87], v0\ns_nop 2\nv_smfmac_f32_16x16x64_bf16 v[16:19], v[40:43], a[96:103], v0\nv_permlane32_swap_b32 v20, v22\n" \
    "v_smfmac_f32_16x16x64_bf16 v[16:19], v[24:27], a[112:119], v0\nv_permlane32_swap_b32 v21, v23\nv_smfmac_f32_16x16x64_bf16 v[16:19], v[28:31], a[128:135], v0\n" \
    "v_add_f32 v20, v20, v22\nv_add_f32 v21, v21, v23\nv_cvt_pk_bf16_f32 v20, v20, v21\nds_write_b16 v4, v20 offset:8\nds_write_b16_d16_hi v4, v20 offset:24\n" \
    "v_mov_b32 v15, v15\nv_mov_b32 v15, v15\nv_mov_b32 v15, v15\nv_permlane32_swap_b32 v16, v18\nv_permlane32_swap_b32 v17, v19\nv_pk_add_f32 v[16:17], v[16:17], v[18:19]\n" \
    "v_cvt_pk_bf16_f32 v16, v16, v17\nds_write_b16 v4, v16 offset:0\nds_write_b16_d16_hi v4, v16 offset:16\nv_mov_b32 v15, v15\nv_mov_b32 v15, v15\n.Lv7_%=:\n.Lv80_%=:\n" \
    "v_mov_b64 v[54:55], 0\nv_mov_b64 v[52:53], 0\nv_mov_b64 v[50:51], 0\nv_mov_b64 v[48:49], 0\nv_mov_b64 v[70:71], 0\nv_mov_b64 v[68:69], 0\nv_mov_b64 v[66:67], 0\n" \
    "v_mov_b64 v[64:65], 0\nds_read_b128 v[32:35], v5 offset:0\nds_read_b128 v[36:39], v5 offset:512\nds_read_b128 v[40:43], v5 offset:1024\n" \
    "ds_read_b128 v[44:47], v5 offset:1536\nv_mov_b64 v[30:31], 0\nv_mov_b64 v[28:29], 0\nv_mov_b64 v[26:27], 0\nv_mov_b64 v[24:25], 0\nv_mov_b64 v[22:23], 0\n" \
    "v_mov_b64 v[20:21], 0\nv_mov_b64 v[18:19], 0\nv_mov_b64 v[16:17], 0\ns_waitcnt lgkmcnt(3)\ns_nop 0\nv_smfmac_f32_16x16x64_bf16 v[16:19], v[32:35], a[160:167], v0\n" \
    "v_smfmac_f32_16x16x64_bf16 v[20:23], v[32:35], a[176:183], v0\ns_waitcnt lgkmcnt(2)\nv_smfmac_f32_16x16x64_bf16 v[16:19], v[36:39], a[168:175], v0\n" \
    "v_smfmac_f32_16x16x64_bf16 v[20:23], v[36:39], a[184:191], v0\ns_waitcnt lgkmcnt(1)\nv_smfmac_f32_16x16x64_bf16 v[16:19], v[40:43], a[192:199], v0\n" \
    "v_smfmac_f32_16x16x64_bf16 v[20:23], v[40:43], a[208:215], v0\ns_waitcnt lgkmcnt(0)\nv_smfmac_f32_16x16x64_bf16 v[16:19], v[44:47], a[200:207], v0\n" \
    "v_smfmac_f32_16x16x64_bf16 v[20:23], v[44:47], a[216:223], v0\ns_nop 2\nv_smfmac_f32_16x16x64_bf16 v[24:27], v[32:35], a[224:231], v0\ns_nop 2\n" \
    "v_cmp_u_f32 s[4:5], v16, v17\nv_smfmac_f32_16x16x64_bf16 v[28:31], v[32:35], a[240:247], v0\nv_cmp_u_f32 vcc, v18, v19\n" \
    "v_smfmac_f32_16x16x64_bf16 v[24:27], v[36:39], a[232:239], v0\nv_cvt_pk_bf16_f32 v72, v16, v17\nv_smfmac_f32_16x16x64_bf16 v[28:31], v[36:39], a[248:255], v0\n" \
    "v_cvt_pk_bf16_f32 v74, v18, v19\nv_smfmac_f32_16x16x64_bf16 v[24:27], v[40:43], v[92:99], v0\ns_or_b64 vcc, vcc, s[4:5]\ns_cbranch_scc1 .Lv81_%=\n.Lv82_%=:\n" \
    "v_cvt_pk_bf16_f32 v73, v20, v21\nv_smfmac_f32_16x16x64_bf16 v[28:31], v[40:43], v[108:115], v0\nv_cvt_pk_bf16_f32 v75, v22, v23\n" \
    "v_smfmac_f32_16x16x64_bf16 v[24:27], v[44:47], v[100:107], v0\nv_permlane16_swap_b32 v72, v73\nv_smfmac_f32_16x16x64_bf16 v[28:31], v[44:47], v[116:123], v0\n" \
    "v_permlane16_swap_b32 v74, v75\nv_smfmac_f32_16x16x64_bf16 v[64:67], v[32:35], v[124:131], v0\nbuffer_store_dwordx4 v[72:75], v7, s[64:67], s0 offen offset:0 sc1\ns_nop 1\n" \
    "v_smfmac_f32_16x16x64_bf16 v[68:71], v[32:35], v[140:147], v0\nv_cvt_pk_bf16_f32 v76, v24, v25\nv_smfmac_f32_16x16x64_bf16 v[64:67], v[36:39], v[132:139], v0\n" \
    "v_cvt_pk_bf16_f32 v78, v26, v27\nv_smfmac_f32_16x16x64_bf16 v[68:71], v[36:39], v[148:155], v0\nv_cvt_pk_bf16_f32 v77, v28, v29\n" \
    "v_smfmac_f32_16x16x64_bf16 v[64:67], v[40:43], v[156:163], v0\nv_cvt_pk_bf16_f32 v79, v30, v31\nv_smfmac_f32_16x16x64_bf16 v[68:71], v[40:43], v[172:179], v0\n" \
    "v_permlane16_swap_b32 v76, v77\nv_smfmac_f32_16x16x64_bf16 v[64:67], v[44:47], v[164:171], v0\nv_permlane16_swap_b32 v78, v79\n" \
    "v_smfmac_f32_16x16x64_bf16 v[68:71], v[44:47], v[180:187], v0\nbuffer_store_dwordx4 v[76:79], v7, s[64:67], s1 offen offset:0 sc1\ns_nop 1\n" \
    "v_smfmac_f32_16x16x64_bf16 v[48:51], v[32:35], v[188:195], v0\ns_nop 1\nv_cvt_pk_bf16_f32 v72, v64, v65\nv_smfmac_f32_16x16x64_bf16 v[52:55], v[32:35], v[204:211], v0\n" \
    "v_cvt_pk_bf16_f32 v74, v66, v67\nv_smfmac_f32_16x16x64_bf16 v[48:51], v[36:39], v[196:203], v0\nv_cvt_pk_bf16_f32 v73, v68, v69\n" \
    "v_smfmac_f32_16x16x64_bf16 v[52:55], v[36:39], v[212:219], v0\nv_cvt_pk_bf16_f32 v75, v70, v71\nv_smfmac_f32_16x16x64_bf16 v[48:51], v[40:43], v[220:227], v0\n" \
    "v_permlane16_swap_b32 v72, v73\nv_smfmac_f32_16x16x64_bf16 v[52:55], v[40:43], v[236:243], v0\nv_permlane16_swap_b32 v74, v75\n" \
    "v_smfmac_f32_16x16x64_bf16 v[48:51], v[44:47], v[228:235], v0\nbuffer_store_dwordx4 v[72:75], v7, s[64:67], s2 offen offset:0 sc1\n" \
    "v_smfmac_f32_16x16x64_bf16 v[52:55], v[44:47], v[244:251], v0\ns_nop 5\nv_cvt_pk_bf16_f32 v76, v48, v49\nv_cvt_pk_bf16_f32 v78, v50, v51\nv_cvt_pk_bf16_f32 v77, v52, v53\n" \
    "v_cvt_pk_bf16_f32 v79, v54, v55\ns_nop 0\nv_permlane16_swap_b32 v76, v77\nv_permlane16_swap_b32 v78, v79\n" \
    "buffer_store_dwordx4 v[76:79], v7, s[64:67], s3 offen offset:0 sc1\ns_barrier\ns_add_u32 s35, s35, 2\ns_cmp_lt_u32 s35, 62\ns_cbranch_scc1 .Lv5_%=\ns_cmp_lt_u32 s35, 64\n" \
    "s_cbranch_scc0 .Lv6_%=\nds_read_b128 v[56:59], v6 offset:0\nds_read_b128 v[60:63], v6 offset:1024\nds_read_b128 v[64:67], v6 offset:2048\nv_mov_b64 v[32:33], 0\n" \
    "v_mov_b64 v[34:35], 0\nv_mov_b64 v[36:37], 0\nv_mov_b64 v[38:39], 0\ns_and_b32 s69, s50, 15\ns_lshl_b32 s69, s69, 21\ns_add_u32 s86, s39, s69\ns_lshr_b32 s69, s86, 8\n" \
    "s_and_b32 s69, s69, 0x2000\ns_xor_b32 s86, s86, s69\ns_waitcnt lgkmcnt(2)\nv_smfmac_f32_16x16x64_bf16 v[32:35], v[56:59], a[160:167], v0\ns_add_u32 s87, s86, 0x40000\n" \
    "s_add_u32 s88, s86, 0x80000\nv_mov_b64 v[40:41], 0\nv_mov_b64 v[42:43], 0\nv_smfmac_f32_16x16x64_bf16 v[36:39], v[56:59], a[176:183], v0\n" \
    "ds_read_b128 v[68:71], v6 offset:3072\ns_add_u32 s89, s86, 0xc0000\ns_waitcnt lgkmcnt(2)\nv_mov_b64 v[44:45], 0\nv_mov_b64 v[46:47], 0\n" \
    "v_smfmac_f32_16x16x64_bf16 v[32:35], v[60:63], a[168:175], v0\ns_add_u32 s50, s50, 2\nv_mov_b64 v[252:253], -1\n" \
    "v_smfmac_f32_16x16x64_bf16 v[36:39], v[60:63], a[184:191], v0\ns_and_b32 s69, s35, 15\ns_lshl_b32 s69, s69, 19\ns_add_u32 s0, s38, s69\ns_waitcnt lgkmcnt(1)\n" \
    "v_smfmac_f32_16x16x64_bf16 v[32:35], v[64:67], a[192:199], v0\ns_lshr_b32 s69, s0, 8\ns_and_b32 s69, s69, 0x2000\ns_xor_b32 s0, s0, s69\n" \
    "v_smfmac_f32_16x16x64_bf16 v[36:39], v[64:67], a[208:215], v0\ns_add_u32 s1, s0, 0x10000\ns_add_u32 s2, s0, 0x20000\ns_add_u32 s3, s0, 0x30000\ns_waitcnt lgkmcnt(0)\n" \
    "v_smfmac_f32_16x16x64_bf16 v[32:35], v[68:71], a[200:207], v0\nv_smfmac_f32_16x16x64_bf16 v[36:39], v[68:71], a[216:223], v0\ns_nop 2\n" \
    "v_smfmac_f32_16x16x64_bf16 v[40:43], v[56:59], a[224:231], v0\ns_nop 2\nv_cvt_pk_bf16_f32 v48, v32, v33\nv_cvt_pk_bf16_f32 v50, v34, v35\n" \
    "v_smfmac_f32_16x16x64_bf16 v[44:47], v[56:59], a[240:247], v0\nv_cvt_pk_bf16_f32 v49, v36, v37\nv_cvt_pk_bf16_f32 v51, v38, v39\n" \
    "v_smfmac_f32_16x16x64_bf16 v[40:43], v[60:63], a[232:239], v0\nv_permlane16_swap_b32 v48, v49\nv_smfmac_f32_16x16x64_bf16 v[44:47], v[60:63], a[248:255], v0\n" \
    "v_permlane16_swap_b32 v50, v51\nv_smfmac_f32_16x16x64_bf16 v[40:43], v[64:67], v[92:99], v0\nbuffer_store_dwordx4 v[48:51], v8, s[64:67], s86 offen offset:0 sc1\n" \
    "v_mov_b64 v[32:33], 0\nv_mov_b64 v[34:35], 0\nv_smfmac_f32_16x16x64_bf16 v[44:47], v[64:67], v[108:115], v0\nv_mov_b64 v[36:37], 0\nv_mov_b64 v[38:39], 0\n" \
    "v_smfmac_f32_16x16x64_bf16 v[40:43], v[68:71], v[100:107], v0\nv_smfmac_f32_16x16x64_bf16 v[44:47], v[68:71], v[116:123], v0\ns_nop 2\n" \
    "v_smfmac_f32_16x16x64_bf16 v[32:35], v[56:59], v[124:131], v0\ns_nop 2\nv_cvt_pk_bf16_f32 v52, v40, v41\nv_cvt_pk_bf16_f32 v54, v42, v43\n" \
    "v_smfmac_f32_16x16x64_bf16 v[36:39], v[56:59], v[140:147], v0\nv_cvt_pk_bf16_f32 v53, v44, v45\nv_cvt_pk_bf16_f32 v55, v46, v47\n" \
    "v_smfmac_f32_16x16x64_bf16 v[32:35], v[60:63], v[132:139], v0\nv_permlane16_swap_b32 v52, v53\nv_smfmac_f32_16x16x64_bf16 v[36:39], v[60:63], v[148:155], v0\n" \
    "v_permlane16_swap_b32 v54, v55\nv_smfmac_f32_16x16x64_bf16 v[32:35], v[64:67], v[156:163], v0\nbuffer_store_dwordx4 v[52:55], v8, s[64:67], s87 offen offset:0 sc1\n" \
    "v_mov_b64 v[40:41], 0\nv_mov_b64 v[42:43], 0\nv_smfmac_f32_16x16x64_bf16 v[36:39], v[64:67], v[172:179], v0\nv_mov_b64 v[44:45], 0\nv_mov_b64 v[46:47], 0\n" \
    "v_smfmac_f32_16x16x64_bf16 v[32:35], v[68:71], v[164:171], v0\nv_smfmac_f32_16x16x64_bf16 v[36:39], v[68:71], v[180:187], v0\ns_nop 2\n" \
    "v_smfmac_f32_16x16x64_bf16 v[40:43], v[56:59], v[188:195], v0\ns_nop 2\nv_cvt_pk_bf16_f32 v48, v32, v33\nv_cvt_pk_bf16_f32 v50, v34, v35\n" \
    "v_smfmac_f32_16x16x64_bf16 v[44:47], v[56:59], v[204:211], v0\nv_cvt_pk_bf16_f32 v49, v36, v37\nv_cvt_pk_bf16_f32 v51, v38, v39\n" \
    "v_smfmac_f32_16x16x64_bf16 v[40:43], v[60:63], v[196:203], v0\nv_permlane16_swap_b32 v48, v49\nv_smfmac_f32_16x16x64_bf16 v[44:47], v[60:63], v[212:219], v0\n" \
    "ds_read_b128 v[56:59], v6 offset:128\nv_permlane16_swap_b32 v50, v51\nv_smfmac_f32_16x16x64_bf16 v[40:43], v[64:67], v[220:227], v0\n" \
    "buffer_store_dwordx4 v[48:51], v8, s[64:67], s88 offen offset:0 sc1\nv_mov_b64 v[32:33], 0\nv_mov_b64 v[34:35], 0\n" \
    "v_smfmac_f32_16x16x64_bf16 v[44:47], v[64:67], v[236:243], v0\nds_read_b128 v[60:63], v6 offset:1152\nv_mov_b64 v[36:37], 0\nv_mov_b64 v[38:39], 0\n" \
    "v_smfmac_f32_16x16x64_bf16 v[40:43], v[68:71], v[228:235], v0\nv_smfmac_f32_16x16x64_bf16 v[44:47], v[68:71], v[244:251], v0\nds_read_b128 v[64:67], v6 offset:2176\n" \
    "s_nop 1\ns_waitcnt lgkmcnt(2)\nv_smfmac_f32_16x16x64_bf16 v[32:35], v[56:59], a[160:167], v0\ns_nop 1\nv_cvt_pk_bf16_f32 v52, v40, v41\nv_cvt_pk_bf16_f32 v54, v42, v43\n" \
    "v_smfmac_f32_16x16x64_bf16 v[36:39], v[56:59], a[176:183], v0\nds_read_b128 v[68:71], v6 offset:3200\ns_waitcnt lgkmcnt(2)\nv_cvt_pk_bf16_f32 v53, v44, v45\n" \
    "v_cvt_pk_bf16_f32 v55, v46, v47\nv_smfmac_f32_16x16x64_bf16 v[32:35], v[60:63], a[168:175], v0\nv_permlane16_swap_b32 v52, v53\n" \
    "v_smfmac_f32_16x16x64_bf16 v[36:39], v[60:63], a[184:191], v0\ns_waitcnt lgkmcnt(1)\nv_permlane16_swap_b32 v54, v55\n" \
    "v_smfmac_f32_16x16x64_bf16 v[32:35], v[64:67], a[192:199], v0\nbuffer_store_dwordx4 v[52:55], v8, s[64:67], s89 offen offset:0 sc1\nv_mov_b64 v[40:41], 0\n" \
    "v_mov_b64 v[42:43], 0\nv_smfmac_f32_16x16x64_bf16 v[36:39], v[64:67], a[208:215], v0\ns_waitcnt lgkmcnt(0)\nv_mov_b64 v[44:45], 0\nv_mov_b64 v[46:47], 0\n" \
    "v_smfmac_f32_16x16x64_bf16 v[32:35], v[68:71], a[200:207], v0\nv_smfmac_f32_16x16x64_bf16 v[36:39], v[68:71], a[216:223], v0\ns_nop 2\n" \
    "v_smfmac_f32_16x16x64_bf16 v[40:43], v[56:59], a[224:231], v0\ns_nop 2\nv_cvt_pk_bf16_f32 v48, v32, v33\nv_cvt_pk_bf16_f32 v50, v34, v35\n" \
    "v_smfmac_f32_16x16x64_bf16 v[44:47], v[56:59], a[240:247], v0\nv_cvt_pk_bf16_f32 v49, v36, v37\nv_cvt_pk_bf16_f32 v51, v38, v39\n" \
    "v_smfmac_f32_16x16x64_bf16 v[40:43], v[60:63], a[232:239], v0\nv_permlane16_swap_b32 v48, v49\nv_smfmac_f32_16x16x64_bf16 v[44:47], v[60:63], a[248:255], v0\n" \
    "v_permlane16_swap_b32 v50, v51\nv_smfmac_f32_16x16x64_bf16 v[40:43], v[64:67], v[92:99], v0\nbuffer_store_dwordx4 v[48:51], v8, s[64:67], s86 offen offset:256 sc1\n" \
    "v_mov_b64 v[32:33], 0\nv_mov_b64 v[34:35], 0\nv_smfmac_f32_16x16x64_bf16 v[44:47], v[64:67], v[108:115], v0\nv_mov_b64 v[36:37], 0\nv_mov_b64 v[38:39], 0\n" \
    "v_smfmac_f32_16x16x64_bf16 v[40:43], v[68:71], v[100:107], v0\nv_smfmac_f32_16x16x64_bf16 v[44:47], v[68:71], v[116:123], v0\ns_nop 2\n" \
    "v_smfmac_f32_16x16x64_bf16 v[32:35], v[56:59], v[124:131], v0\ns_nop 2\nv_cvt_pk_bf16_f32 v52, v40, v41\nv_cvt_pk_bf16_f32 v54, v42, v43\n" \
    "v_smfmac_f32_16x16x64_bf16 v[36:39], v[56:59], v[140:147], v0\nv_cvt_pk_bf16_f32 v53, v44, v45\nv_cvt_pk_bf16_f32 v55, v46, v47\n" \
    "v_smfmac_f32_16x16x64_bf16 v[32:35], v[60:63], v[132:139], v0\nv_permlane16_swap_b32 v52, v53\nv_smfmac_f32_16x16x64_bf16 v[36:39], v[60:63], v[148:155], v0\n" \
    "v_permlane16_swap_b32 v54, v55\nv_smfmac_f32_16x16x64_bf16 v[32:35], v[64:67], v[156:163], v0\nbuffer_store_dwordx4 v[52:55], v8, s[64:67], s87 offen offset:256 sc1\n" \
    "v_mov_b64 v[40:41], 0\nv_mov_b64 v[42:43], 0\nv_smfmac_f32_16x16x64_bf16 v[36:39], v[64:67], v[172:179], v0\nv_mov_b64 v[44:45], 0\nv_mov_b64 v[46:47], 0\n" \
    "v_smfmac_f32_16x16x64_bf16 v[32:35], v[68:71], v[164:171], v0\nv_smfmac_f32_16x16x64_bf16 v[36:39], v[68:71], v[180:187], v0\ns_nop 2\n" \
    "v_smfmac_f32_16x16x64_bf16 v[40:43], v[56:59], v[188:195], v0\ns_nop 2\nv_cvt_pk_bf16_f32 v48, v32, v33\nv_cvt_pk_bf16_f32 v50, v34, v35\n" \
    "v_smfmac_f32_16x16x64_bf16 v[44:47], v[56:59], v[204:211], v0\nv_cvt_pk_bf16_f32 v49, v36, v37\nv_cvt_pk_bf16_f32 v51, v38, v39\n" \
    "v_smfmac_f32_16x16x64_bf16 v[40:43], v[60:63], v[196:203], v0\nv_permlane16_swap_b32 v48, v49\nv_smfmac_f32_16x16x64_bf16 v[44:47], v[60:63], v[212:219], v0\n" \
    "ds_read_b128 v[56:59], v6 offset:4096\nv_permlane16_swap_b32 v50, v51\nv_smfmac_f32_16x16x64_bf16 v[40:43], v[64:67], v[220:227], v0\n" \
    "buffer_store_dwordx4 v[48:51], v8, s[64:67], s88 offen offset:256 sc1\nv_mov_b64 v[32:33], 0\nv_mov_b64 v[34:35], 0\n" \
    "v_smfmac_f32_16x16x64_bf16 v[44:47], v[64:67], v[236:243], v0\nds_read_b128 v[60:63], v6 offset:5120\nv_mov_b64 v[36:37], 0\nv_mov_b64 v[38:39], 0\n" \
    "v_smfmac_f32_16x16x64_bf16 v[40:43], v[68:71], v[228:235], v0\nv_smfmac_f32_16x16x64_bf16 v[44:47], v[68:71], v[244:251], v0\nds_read_b128 v[64:67], v6 offset:6144\n" \
    "s_nop 1\ns_waitcnt lgkmcnt(2)\nv_smfmac_f32_16x16x64_bf16 v[32:35], v[56:59], a[160:167], v0\ns_nop 1\nv_cvt_pk_bf16_f32 v52, v40, v41\nv_cvt_pk_bf16_f32 v54, v42, v43\n" \
    "v_smfmac_f32_16x16x64_bf16 v[36:39], v[56:59], a[176:183], v0\nds_read_b128 v[68:71], v6 offset:7168\ns_waitcnt lgkmcnt(2)\nv_cvt_pk_bf16_f32 v53, v44, v45\n" \
    "v_cvt_pk_bf16_f32 v55, v46, v47\nv_smfmac_f32_16x16x64_bf16 v[32:35], v[60:63], a[168:175], v0\nv_permlane16_swap_b32 v52, v53\n" \
    "v_smfmac_f32_16x16x64_bf16 v[36:39], v[60:63], a[184:191], v0\ns_waitcnt lgkmcnt(1)\nv_permlane16_swap_b32 v54, v55\n" \
    "v_smfmac_f32_16x16x64_bf16 v[32:35], v[64:67], a[192:199], v0\nbuffer_store_dwordx4 v[52:55], v8, s[64:67], s89 offen offset:256 sc1\nv_mov_b64 v[40:41], 0\n" \
    "v_mov_b64 v[42:43], 0\nv_smfmac_f32_16x16x64_bf16 v[36:39], v[64:67], a[208:215], v0\ns_waitcnt lgkmcnt(0)\nv_mov_b64 v[44:45], 0\nv_mov_b64 v[46:47], 0\n" \
    "v_smfmac_f32_16x16x64_bf16 v[32:35], v[68:71], a[200:207], v0\nv_smfmac_f32_16x16x64_bf16 v[36:39], v[68:71], a[216:223], v0\ns_nop 2\n" \
    "v_smfmac_f32_16x16x64_bf16 v[40:43], v[56:59], a[224:231], v0\ns_nop 2\nv_cvt_pk_bf16_f32 v48, v32, v33\nv_cvt_pk_bf16_f32 v50, v34, v35\n" \
    "v_smfmac_f32_16x16x64_bf16 v[44:47], v[56:59], a[240:247], v0\nv_cvt_pk_bf16_f32 v49, v36, v37\nv_cvt_pk_bf16_f32 v51, v38, v39\n" \
    "v_smfmac_f32_16x16x64_bf16 v[40:43], v[60:63], a[232:239], v0\nv_permlane16_swap_b32 v48, v49\nv_smfmac_f32_16x16x64_bf16 v[44:47], v[60:63], a[248:255], v0\n" \
    "v_permlane16_swap_b32 v50, v51\nv_smfmac_f32_16x16x64_bf16 v[40:43], v[64:67], v[92:99], v0\nbuffer_store_dwordx4 v[48:51], v8, s[64:67], s86 offen offset:512 sc1\n" \
    "v_mov_b64 v[32:33], 0\nv_mov_b64 v[34:35], 0\nv_smfmac_f32_16x16x64_bf16 v[44:47], v[64:67], v[108:115], v0\nv_mov_b64 v[36:37], 0\nv_mov_b64 v[38:39], 0\n" \
    "v_smfmac_f32_16x16x64_bf16 v[40:43], v[68:71], v[100:107], v0\nv_smfmac_f32_16x16x64_bf16 v[44:47], v[68:71], v[116:123], v0\ns_nop 2\n" \
    "v_smfmac_f32_16x16x64_bf16 v[32:35], v[56:59], v[124:131], v0\ns_nop 2\nv_cvt_pk_bf16_f32 v52, v40, v41\nv_cvt_pk_bf16_f32 v54, v42, v43\n" \
    "v_smfmac_f32_16x16x64_bf16 v[36:39], v[56:59], v[140:147], v0\nv_cvt_pk_bf16_f32 v53, v44, v45\nv_cvt_pk_bf16_f32 v55, v46, v47\n" \
    "v_smfmac_f32_16x16x64_bf16 v[32:35], v[60:63], v[132:139], v0\nv_permlane16_swap_b32 v52, v53\nv_smfmac_f32_16x16x64_bf16 v[36:39], v[60:63], v[148:155], v0\n" \
    "v_permlane16_swap_b32 v54, v55\nv_smfmac_f32_16x16x64_bf16 v[32:35], v[64:67], v[156:163], v0\nbuffer_store_dwordx4 v[52:55], v8, s[64:67], s87 offen offset:512 sc1\n" \
    "buffer_load_dword v13, off, s[64:67], s42 sc1\nv_mov_b64 v[40:41], 0\nv_mov_b64 v[42:43], 0\nv_smfmac_f32_16x16x64_bf16 v[36:39], v[64:67], v[172:179], v0\n" \
    "s_and_b32 s69, s35, 15\ns_lshl_b32 s69, s69, 17\nv_mov_b64 v[44:45], 0\nv_mov_b64 v[46:47], 0\nv_smfmac_f32_16x16x64_bf16 v[32:35], v[68:71], v[164:171], v0\n" \
    "s_add_u32 s81, s37, s69\ns_lshr_b32 s69, s81, 8\ns_and_b32 s69, s69, 0x2000\nv_smfmac_f32_16x16x64_bf16 v[36:39], v[68:71], v[180:187], v0\ns_xor_b32 s81, s81, s69\n" \
    "s_mov_b32 s68, 0\ns_nop 0\nv_smfmac_f32_16x16x64_bf16 v[40:43], v[56:59], v[188:195], v0\nv_cvt_pk_bf16_f32 v48, v32, v33\nv_cvt_pk_bf16_f32 v50, v34, v35\n" \
    "v_smfmac_f32_16x16x64_bf16 v[44:47], v[56:59], v[204:211], v0\ns_nop 0\nv_cvt_pk_bf16_f32 v49, v36, v37\nv_cvt_pk_bf16_f32 v51, v38, v39\n" \
    "v_smfmac_f32_16x16x64_bf16 v[40:43], v[60:63], v[196:203], v0\nv_permlane16_swap_b32 v48, v49\nv_smfmac_f32_16x16x64_bf16 v[44:47], v[60:63], v[212:219], v0\n" \
    "ds_read_b128 v[56:59], v6 offset:4224\nv_permlane16_swap_b32 v50, v51\nv_smfmac_f32_16x16x64_bf16 v[40:43], v[64:67], v[220:227], v0\n" \
    "buffer_store_dwordx4 v[48:51], v8, s[64:67], s88 offen offset:512 sc1\nv_mov_b64 v[32:33], 0\nv_mov_b64 v[34:35], 0\n" \
    "v_smfmac_f32_16x16x64_bf16 v[44:47], v[64:67], v[236:243], v0\nds_read_b128 v[60:63], v6 offset:5248\nv_mov_b64 v[36:37], 0\nv_mov_b64 v[38:39], 0\n" \
    "v_smfmac_f32_16x16x64_bf16 v[40:43], v[68:71], v[228:235], v0\nv_smfmac_f32_16x16x64_bf16 v[44:47], v[68:71], v[244:251], v0\nds_read_b128 v[64:67], v6 offset:6272\n" \
    "s_nop 1\ns_waitcnt lgkmcnt(2)\nv_smfmac_f32_16x16x64_bf16 v[32:35], v[56:59], a[160:167], v0\ns_nop 1\nv_cvt_pk_bf16_f32 v52, v40, v41\nv_cvt_pk_bf16_f32 v54, v42, v43\n" \
    "v_smfmac_f32_16x16x64_bf16 v[36:39], v[56:59], a[176:183], v0\nds_read_b128 v[68:71], v6 offset:7296\ns_waitcnt lgkmcnt(2)\nv_cvt_pk_bf16_f32 v53, v44, v45\n" \
    "v_cvt_pk_bf16_f32 v55, v46, v47\nv_smfmac_f32_16x16x64_bf16 v[32:35], v[60:63], a[168:175], v0\nv_permlane16_swap_b32 v52, v53\n" \
    "v_smfmac_f32_16x16x64_bf16 v[36:39], v[60:63], a[184:191], v0\ns_waitcnt lgkmcnt(1)\nv_permlane16_swap_b32 v54, v55\n" \
    "v_smfmac_f32_16x16x64_bf16 v[32:35], v[64:67], a[192:199], v0\nbuffer_store_dwordx4 v[52:55], v8, s[64:67], s89 offen offset:512 sc1\nv_mov_b64 v[40:41], 0\n" \
    "v_mov_b64 v[42:43], 0\nv_smfmac_f32_16x16x64_bf16 v[36:39], v[64:67], a[208:215], v0\ns_waitcnt lgkmcnt(0)\nv_mov_b64 v[44:45], 0\nv_mov_b64 v[46:47], 0\n" \
    "v_smfmac_f32_16x16x64_bf16 v[32:35], v[68:71], a[200:207], v0\nv_smfmac_f32_16x16x64_bf16 v[36:39], v[68:71], a[216:223], v0\ns_nop 2\n" \
    "v_smfmac_f32_16x16x64_bf16 v[40:43], v[56:59], a[224:231], v0\ns_nop 2\nv_cvt_pk_bf16_f32 v48, v32, v33\nv_cvt_pk_bf16_f32 v50, v34, v35\n" \
    "v_smfmac_f32_16x16x64_bf16 v[44:47], v[56:59], a[240:247], v0\nv_cvt_pk_bf16_f32 v49, v36, v37\nv_cvt_pk_bf16_f32 v51, v38, v39\n" \
    "v_smfmac_f32_16x16x64_bf16 v[40:43], v[60:63], a[232:239], v0\nv_permlane16_swap_b32 v48, v49\nv_smfmac_f32_16x16x64_bf16 v[44:47], v[60:63], a[248:255], v0\n" \
    "v_permlane16_swap_b32 v50, v51\nv_smfmac_f32_16x16x64_bf16 v[40:43], v[64:67], v[92:99], v0\nbuffer_store_dwordx4 v[48:51], v8, s[64:67], s86 offen offset:768 sc1\n" \
    "v_mov_b64 v[32:33], 0\nv_mov_b64 v[34:35], 0\nv_smfmac_f32_16x16x64_bf16 v[44:47], v[64:67], v[108:115], v0\nv_mov_b64 v[36:37], 0\nv_mov_b64 v[38:39], 0\n" \
    "v_smfmac_f32_16x16x64_bf16 v[40:43], v[68:71], v[100:107], v0\nv_smfmac_f32_16x16x64_bf16 v[44:47], v[68:71], v[116:123], v0\ns_nop 2\n" \
    "v_smfmac_f32_16x16x64_bf16 v[32:35], v[56:59], v[124:131], v0\ns_waitcnt vmcnt(3)\ns_nop 0\nv_readfirstlane_b32 s74, v13\nv_cvt_pk_bf16_f32 v52, v40, v41\n" \
    "v_smfmac_f32_16x16x64_bf16 v[36:39], v[56:59], v[140:147], v0\ns_and_b32 s75, s74, 127\ns_lshr_b32 s69, s74, 8\nv_cvt_pk_bf16_f32 v54, v42, v43\n" \
    "v_cvt_pk_bf16_f32 v53, v44, v45\nv_smfmac_f32_16x16x64_bf16 v[32:35], v[60:63], v[132:139], v0\ns_sub_u32 s69, s69, s80\ns_bfe_i32 s69, s69, 0x180000\n" \
    "v_cvt_pk_bf16_f32 v55, v46, v47\nv_permlane16_swap_b32 v52, v53\nv_smfmac_f32_16x16x64_bf16 v[36:39], v[60:63], v[148:155], v0\ns_cmp_eq_u32 s75, 0\n" \
    "s_cselect_b32 s69, 0, s69\nv_permlane16_swap_b32 v54, v55\nv_smfmac_f32_16x16x64_bf16 v[32:35], v[64:67], v[156:163], v0\n" \
    "buffer_store_dwordx4 v[52:55], v8, s[64:67], s87 offen offset:768 sc1\ns_add_u32 s80, s80, s69\nv_mov_b64 v[40:41], 0\nv_mov_b64 v[42:43], 0\n" \
    "v_smfmac_f32_16x16x64_bf16 v[36:39], v[64:67], v[172:179], v0\ns_lshl_b32 s74, s74, 5\ns_and_b32 s74, s74, 0x1000\nv_mov_b64 v[44:45], 0\nv_mov_b64 v[46:47], 0\n" \
    "v_smfmac_f32_16x16x64_bf16 v[32:35], v[68:71], v[164:171], v0\ns_andn2_b32 s81, s81, 0x1000\ns_or_b32 s81, s81, s74\nv_lshrrev_b32 v15, 1, v2\nv_add_u32 v15, s60, v15\n" \
    "v_smfmac_f32_16x16x64_bf16 v[36:39], v[68:71], v[180:187], v0\nds_write_b64 v15, v[252:253]\ns_nop 1\nv_cvt_pk_bf16_f32 v48, v32, v33\nv_cvt_pk_bf16_f32 v50, v34, v35\n" \
    "v_smfmac_f32_16x16x64_bf16 v[40:43], v[56:59], v[188:195], v0\ns_nop 1\nv_cvt_pk_bf16_f32 v49, v36, v37\nv_cvt_pk_bf16_f32 v51, v38, v39\n" \
    "v_smfmac_f32_16x16x64_bf16 v[44:47], v[56:59], v[204:211], v0\nv_permlane16_swap_b32 v48, v49\nv_smfmac_f32_16x16x64_bf16 v[40:43], v[60:63], v[196:203], v0\n" \
    "v_permlane16_swap_b32 v50, v51\nv_smfmac_f32_16x16x64_bf16 v[44:47], v[60:63], v[212:219], v0\nbuffer_store_dwordx4 v[48:51], v8, s[64:67], s88 offen offset:768 sc1\n" \
    "v_smfmac_f32_16x16x64_bf16 v[40:43], v[64:67], v[220:227], v0\nv_smfmac_f32_16x16x64_bf16 v[44:47], v[64:67], v[236:243], v0\n" \
    "v_smfmac_f32_16x16x64_bf16 v[40:43], v[68:71], v[228:235], v0\nv_smfmac_f32_16x16x64_bf16 v[44:47], v[68:71], v[244:251], v0\ns_nop 6\nv_cvt_pk_bf16_f32 v52, v40, v41\n" \
    "v_cvt_pk_bf16_f32 v54, v42, v43\nv_cvt_pk_bf16_f32 v53, v44, v45\nv_cvt_pk_bf16_f32 v55, v46, v47\ns_nop 0\nv_permlane16_swap_b32 v52, v53\nv_permlane16_swap_b32 v54, v55\n" \
    "buffer_store_dwordx4 v[52:55], v8, s[64:67], s89 offen offset:768 sc1\ns_waitcnt lgkmcnt(0)\ns_waitcnt vmcnt(0)\nv_mov_b32 v15, s50\ns_mov_b64 exec, 1\n" \
    "buffer_store_dword v15, off, s[64:67], s48 sc1\ns_mov_b64 exec, -1\ns_branch .Lv4_%=\n.Lv6_%=:\ns_endpgm\n.Lv12_%=:\ns_mov_b32 s76, 33024\n" \
    "buffer_load_dword v253, off, s[64:67], s76 sc1\ns_waitcnt vmcnt(0)\nv_readfirstlane_b32 s74, v253\ns_nop 0\ns_cmp_ge_u32 s74, 32\ns_cbranch_scc1 .Lv13_%=\ns_sleep 8\n" \
    "s_add_u32 s68, s68, 1\ns_and_b32 s69, s68, 63\ns_cbranch_scc1 .Lv12_%=\ns_cmp_gt_u32 s68, 0x400000\ns_cbranch_scc1 .Lv1_%=\nbuffer_load_dword v253, off, s[64:67], 0 sc1\n" \
    "s_waitcnt vmcnt(0)\nv_readfirstlane_b32 s69, v253\ns_nop 0\ns_cmp_eq_u32 s69, 1\ns_cbranch_scc0 .Lv12_%=\ns_endpgm\n.Lv14_%=:\ns_sleep 6\n" \
    "buffer_load_dword v13, off, s[64:67], s42 sc1\ns_waitcnt vmcnt(0)\nv_readfirstlane_b32 s74, v13\ns_nop 0\ns_and_b32 s75, s74, 127\ns_lshr_b32 s69, s74, 8\n" \
    "s_sub_u32 s69, s69, s80\ns_bfe_i32 s69, s69, 0x180000\ns_cmp_eq_u32 s75, 0\ns_cselect_b32 s69, 0, s69\ns_add_u32 s80, s80, s69\ns_lshl_b32 s74, s74, 5\n" \
    "s_and_b32 s74, s74, 0x1000\ns_andn2_b32 s81, s81, 0x1000\ns_or_b32 s81, s81, s74\ns_add_u32 s68, s68, 1\ns_and_b32 s69, s68, 63\ns_cbranch_scc1 .Lv13_%=\n" \
    "s_cmp_gt_u32 s68, 0x400000\ns_cbranch_scc1 .Lv1_%=\nbuffer_load_dword v253, off, s[64:67], 0 sc1\ns_waitcnt vmcnt(0)\nv_readfirstlane_b32 s69, v253\ns_nop 0\n" \
    "s_cmp_eq_u32 s69, 1\ns_cbranch_scc0 .Lv13_%=\ns_endpgm\n.Lv19_%=:\ns_memrealtime s[72:73]\ns_waitcnt lgkmcnt(0)\ns_sub_u32 s72, s72, s76\ns_cmp_lt_i32 s72, 0\n" \
    "s_cbranch_scc1 .Lv19_%=\nbuffer_load_dword v13, off, s[64:67], s42 sc1\ns_waitcnt vmcnt(0)\nv_readfirstlane_b32 s74, v13\ns_nop 0\ns_and_b32 s75, s74, 127\n" \
    "s_lshr_b32 s69, s74, 8\ns_sub_u32 s69, s69, s80\ns_bfe_i32 s69, s69, 0x180000\ns_cmp_eq_u32 s75, 0\ns_cselect_b32 s69, 0, s69\ns_add_u32 s80, s80, s69\n" \
    "s_lshl_b32 s74, s74, 5\ns_and_b32 s74, s74, 0x1000\ns_andn2_b32 s81, s81, 0x1000\ns_or_b32 s81, s81, s74\ns_branch .Lv16_%=\n.Lv27_%=:\nv_mov_b32 v252, 0\n" \
    "v_max3_u32 v252, v252, v72, v73\nv_max3_u32 v252, v252, v74, v75\nv_max3_u32 v252, v252, v76, v77\nv_max3_u32 v252, v252, v78, v79\nv_cmp_eq_u32 vcc, -1, v252\ns_nop 0\n" \
    "s_cbranch_vccz .Lv23_%=\ns_add_u32 s80, s80, s43\ns_xor_b32 s81, s81, 0x1000\ns_xor_b32 s90, s90, 0x1000\ns_add_u32 s68, s68, 1\ns_and_b32 s69, s68, 63\n" \
    "s_cbranch_scc1 .Lv17_%=\ns_cmp_gt_u32 s68, 0x400000\ns_cbranch_scc1 .Lv1_%=\nbuffer_load_dword v253, off, s[64:67], 0 sc1\ns_waitcnt vmcnt(0)\n" \
    "v_readfirstlane_b32 s69, v253\ns_nop 0\ns_cmp_eq_u32 s69, 1\ns_cbranch_scc0 .Lv17_%=\ns_endpgm\n.Lv28_%=:\nv_mov_b32 v252, 0\nv_max3_u32 v252, v252, v80, v81\n" \
    "v_max3_u32 v252, v252, v82, v83\nv_cmp_eq_u32 vcc, -1, v252\ns_nop 0\ns_cbranch_vccz .Lv24_%=\ns_add_u32 s80, s80, s43\ns_xor_b32 s81, s81, 0x1000\n" \
    "s_xor_b32 s90, s90, 0x1000\n.Lv30_%=:\ns_memrealtime s[72:73]\ns_waitcnt vmcnt(0) lgkmcnt(0)\ns_sub_u32 s72, s72, s80\ns_cmp_lt_i32 s72, 0\ns_cbranch_scc1 .Lv30_%=\n" \
    "buffer_load_dwordx4 v[80:83], v2, s[64:67], s90 offen offset:0 sc1\nbuffer_load_dwordx4 v[84:87], v2, s[64:67], s90 offen offset:1024 sc1\ns_waitcnt vmcnt(0)\n" \
    "s_add_u32 s68, s68, 1\ns_and_b32 s69, s68, 63\ns_cbranch_scc1 .Lv28_%=\ns_cmp_gt_u32 s68, 0x400000\ns_cbranch_scc1 .Lv1_%=\nbuffer_load_dword v253, off, s[64:67], 0 sc1\n" \
    "s_waitcnt vmcnt(0)\nv_readfirstlane_b32 s69, v253\ns_nop 0\ns_cmp_eq_u32 s69, 1\ns_cbranch_scc0 .Lv28_%=\ns_endpgm\n.Lv29_%=:\nv_mov_b32 v252, 0\n" \
    "v_max3_u32 v252, v252, v84, v85\nv_max3_u32 v252, v252, v86, v87\nv_cmp_eq_u32 vcc, -1, v252\ns_nop 0\ns_cbranch_vccz .Lv25_%=\ns_add_u32 s80, s80, s43\n" \
    "s_xor_b32 s81, s81, 0x1000\ns_xor_b32 s90, s90, 0x1000\n.Lv31_%=:\ns_memrealtime s[72:73]\ns_waitcnt vmcnt(0) lgkmcnt(0)\ns_sub_u32 s72, s72, s80\ns_cmp_lt_i32 s72, 0\n" \
    "s_cbranch_scc1 .Lv31_%=\nbuffer_load_dwordx4 v[84:87], v2, s[64:67], s90 offen offset:1024 sc1\ns_waitcnt vmcnt(0)\ns_add_u32 s68, s68, 1\ns_and_b32 s69, s68, 63\n" \
    "s_cbranch_scc1 .Lv29_%=\ns_cmp_gt_u32 s68, 0x400000\ns_cbranch_scc1 .Lv1_%=\nbuffer_load_dword v253, off, s[64:67], 0 sc1\ns_waitcnt vmcnt(0)\n" \
    "v_readfirstlane_b32 s69, v253\ns_nop 0\ns_cmp_eq_u32 s69, 1\ns_cbranch_scc0 .Lv29_%=\ns_endpgm\n.Lv35_%=:\ns_memrealtime s[72:73]\ns_waitcnt lgkmcnt(0)\n" \
    "s_sub_u32 s72, s72, s76\ns_cmp_lt_i32 s72, 0\ns_cbranch_scc1 .Lv35_%=\nbuffer_load_dword v13, off, s[64:67], s42 sc1\ns_waitcnt vmcnt(0)\nv_readfirstlane_b32 s74, v13\n" \
    "s_nop 0\ns_and_b32 s75, s74, 127\ns_lshr_b32 s69, s74, 8\ns_sub_u32 s69, s69, s80\ns_bfe_i32 s69, s69, 0x180000\ns_cmp_eq_u32 s75, 0\ns_cselect_b32 s69, 0, s69\n" \
    "s_add_u32 s80, s80, s69\ns_lshl_b32 s74, s74, 5\ns_and_b32 s74, s74, 0x1000\ns_andn2_b32 s81, s81, 0x1000\ns_or_b32 s81, s81, s74\ns_branch .Lv32_%=\n.Lv43_%=:\n" \
    "v_mov_b32 v252, 0\nv_max3_u32 v252, v252, v72, v73\nv_max3_u32 v252, v252, v74, v75\nv_max3_u32 v252, v252, v76, v77\nv_max3_u32 v252, v252, v78, v79\n" \
    "v_cmp_eq_u32 vcc, -1, v252\ns_nop 0\ns_cbranch_vccz .Lv39_%=\ns_add_u32 s80, s80, s43\ns_xor_b32 s81, s81, 0x1000\ns_xor_b32 s90, s90, 0x1000\ns_add_u32 s68, s68, 1\n" \
    "s_and_b32 s69, s68, 63\ns_cbranch_scc1 .Lv33_%=\ns_cmp_gt_u32 s68, 0x400000\ns_cbranch_scc1 .Lv1_%=\nbuffer_load_dword v253, off, s[64:67], 0 sc1\ns_waitcnt vmcnt(0)\n" \
    "v_readfirstlane_b32 s69, v253\ns_nop 0\ns_cmp_eq_u32 s69, 1\ns_cbranch_scc0 .Lv33_%=\ns_endpgm\n.Lv44_%=:\nv_mov_b32 v252, 0\nv_max3_u32 v252, v252, v80, v81\n" \
    "v_max3_u32 v252, v252, v82, v83\nv_cmp_eq_u32 vcc, -1, v252\ns_nop 0\ns_cbranch_vccz .Lv40_%=\ns_add_u32 s80, s80, s43\ns_xor_b32 s81, s81, 0x1000\n" \
    "s_xor_b32 s90, s90, 0x1000\n.Lv46_%=:\ns_memrealtime s[72:73]\ns_waitcnt vmcnt(0) lgkmcnt(0)\ns_sub_u32 s72, s72, s80\ns_cmp_lt_i32 s72, 0\ns_cbranch_scc1 .Lv46_%=\n" \
    "buffer_load_dwordx4 v[80:83], v2, s[64:67], s90 offen offset:0 sc1\nbuffer_load_dwordx4 v[84:87], v2, s[64:67], s90 offen offset:1024 sc1\ns_waitcnt vmcnt(0)\n" \
    "s_add_u32 s68, s68, 1\ns_and_b32 s69, s68, 63\ns_cbranch_scc1 .Lv44_%=\ns_cmp_gt_u32 s68, 0x400000\ns_cbranch_scc1 .Lv1_%=\nbuffer_load_dword v253, off, s[64:67], 0 sc1\n" \
    "s_waitcnt vmcnt(0)\nv_readfirstlane_b32 s69, v253\ns_nop 0\ns_cmp_eq_u32 s69, 1\ns_cbranch_scc0 .Lv44_%=\ns_endpgm\n.Lv45_%=:\nv_mov_b32 v252, 0\n" \
    "v_max3_u32 v252, v252, v84, v85\nv_max3_u32 v252, v252, v86, v87\nv_cmp_eq_u32 vcc, -1, v252\ns_nop 0\ns_cbranch_vccz .Lv41_%=\ns_add_u32 s80, s80, s43\n" \
    "s_xor_b32 s81, s81, 0x1000\ns_xor_b32 s90, s90, 0x1000\n.Lv47_%=:\ns_memrealtime s[72:73]\ns_waitcnt vmcnt(0) lgkmcnt(0)\ns_sub_u32 s72, s72, s80\ns_cmp_lt_i32 s72, 0\n" \
    "s_cbranch_scc1 .Lv47_%=\nbuffer_load_dwordx4 v[84:87], v2, s[64:67], s90 offen offset:1024 sc1\ns_waitcnt vmcnt(0)\ns_add_u32 s68, s68, 1\ns_and_b32 s69, s68, 63\n" \
    "s_cbranch_scc1 .Lv45_%=\ns_cmp_gt_u32 s68, 0x400000\ns_cbranch_scc1 .Lv1_%=\nbuffer_load_dword v253, off, s[64:67], 0 sc1\ns_waitcnt vmcnt(0)\n" \
    "v_readfirstlane_b32 s69, v253\ns_nop 0\ns_cmp_eq_u32 s69, 1\ns_cbranch_scc0 .Lv45_%=\ns_endpgm\n.Lv51_%=:\ns_memrealtime s[72:73]\ns_waitcnt lgkmcnt(0)\n" \
    "s_sub_u32 s72, s72, s76\ns_cmp_lt_i32 s72, 0\ns_cbranch_scc1 .Lv51_%=\nbuffer_load_dword v13, off, s[64:67], s42 sc1\ns_waitcnt vmcnt(0)\nv_readfirstlane_b32 s74, v13\n" \
    "s_nop 0\ns_and_b32 s75, s74, 127\ns_lshr_b32 s69, s74, 8\ns_sub_u32 s69, s69, s80\ns_bfe_i32 s69, s69, 0x180000\ns_cmp_eq_u32 s75, 0\ns_cselect_b32 s69, 0, s69\n" \
    "s_add_u32 s80, s80, s69\ns_lshl_b32 s74, s74, 5\ns_and_b32 s74, s74, 0x1000\ns_andn2_b32 s81, s81, 0x1000\ns_or_b32 s81, s81, s74\ns_branch .Lv48_%=\n.Lv59_%=:\n" \
    "v_mov_b32 v252, 0\nv_max3_u32 v252, v252, v72, v73\nv_max3_u32 v252, v252, v74, v75\nv_max3_u32 v252, v252, v76, v77\nv_max3_u32 v252, v252, v78, v79\n" \
    "v_cmp_eq_u32 vcc, -1, v252\ns_nop 0\ns_cbranch_vccz .Lv55_%=\ns_add_u32 s80, s80, s43\ns_xor_b32 s81, s81, 0x1000\ns_xor_b32 s90, s90, 0x1000\ns_add_u32 s68, s68, 1\n" \
    "s_and_b32 s69, s68, 63\ns_cbranch_scc1 .Lv49_%=\ns_cmp_gt_u32 s68, 0x400000\ns_cbranch_scc1 .Lv1_%=\nbuffer_load_dword v253, off, s[64:67], 0 sc1\ns_waitcnt vmcnt(0)\n" \
    "v_readfirstlane_b32 s69, v253\ns_nop 0\ns_cmp_eq_u32 s69, 1\ns_cbranch_scc0 .Lv49_%=\ns_endpgm\n.Lv60_%=:\nv_mov_b32 v252, 0\nv_max3_u32 v252, v252, v80, v81\n" \
    "v_max3_u32 v252, v252, v82, v83\nv_cmp_eq_u32 vcc, -1, v252\ns_nop 0\ns_cbranch_vccz .Lv56_%=\ns_add_u32 s80, s80, s43\ns_xor_b32 s81, s81, 0x1000\n" \
    "s_xor_b32 s90, s90, 0x1000\n.Lv62_%=:\ns_memrealtime s[72:73]\ns_waitcnt vmcnt(0) lgkmcnt(0)\ns_sub_u32 s72, s72, s80\ns_cmp_lt_i32 s72, 0\ns_cbranch_scc1 .Lv62_%=\n" \
    "buffer_load_dwordx4 v[80:83], v2, s[64:67], s90 offen offset:0 sc1\nbuffer_load_dwordx4 v[84:87], v2, s[64:67], s90 offen offset:1024 sc1\ns_waitcnt vmcnt(0)\n" \
    "s_add_u32 s68, s68, 1\ns_and_b32 s69, s68, 63\ns_cbranch_scc1 .Lv60_%=\ns_cmp_gt_u32 s68, 0x400000\ns_cbranch_scc1 .Lv1_%=\nbuffer_load_dword v253, off, s[64:67], 0 sc1\n" \
    "s_waitcnt vmcnt(0)\nv_readfirstlane_b32 s69, v253\ns_nop 0\ns_cmp_eq_u32 s69, 1\ns_cbranch_scc0 .Lv60_%=\ns_endpgm\n.Lv61_%=:\nv_mov_b32 v252, 0\n" \
    "v_max3_u32 v252, v252, v84, v85\nv_max3_u32 v252, v252, v86, v87\nv_cmp_eq_u32 vcc, -1, v252\ns_nop 0\ns_cbranch_vccz .Lv57_%=\ns_add_u32 s80, s80, s43\n" \
    "s_xor_b32 s81, s81, 0x1000\ns_xor_b32 s90, s90, 0x1000\n.Lv63_%=:\ns_memrealtime s[72:73]\ns_waitcnt vmcnt(0) lgkmcnt(0)\ns_sub_u32 s72, s72, s80\ns_cmp_lt_i32 s72, 0\n" \
    "s_cbranch_scc1 .Lv63_%=\nbuffer_load_dwordx4 v[84:87], v2, s[64:67], s90 offen offset:1024 sc1\ns_waitcnt vmcnt(0)\ns_add_u32 s68, s68, 1\ns_and_b32 s69, s68, 63\n" \
    "s_cbranch_scc1 .Lv61_%=\ns_cmp_gt_u32 s68, 0x400000\ns_cbranch_scc1 .Lv1_%=\nbuffer_load_dword v253, off, s[64:67], 0 sc1\ns_waitcnt vmcnt(0)\n" \
    "v_readfirstlane_b32 s69, v253\ns_nop 0\ns_cmp_eq_u32 s69, 1\ns_cbranch_scc0 .Lv61_%=\ns_endpgm\n.Lv67_%=:\ns_memrealtime s[72:73]\ns_waitcnt lgkmcnt(0)\n" \
    "s_sub_u32 s72, s72, s76\ns_cmp_lt_i32 s72, 0\ns_cbranch_scc1 .Lv67_%=\nbuffer_load_dword v13, off, s[64:67], s42 sc1\ns_waitcnt vmcnt(0)\nv_readfirstlane_b32 s74, v13\n" \
    "s_nop 0\ns_and_b32 s75, s74, 127\ns_lshr_b32 s69, s74, 8\ns_sub_u32 s69, s69, s80\ns_bfe_i32 s69, s69, 0x180000\ns_cmp_eq_u32 s75, 0\ns_cselect_b32 s69, 0, s69\n" \
    "s_add_u32 s80, s80, s69\ns_lshl_b32 s74, s74, 5\ns_and_b32 s74, s74, 0x1000\ns_andn2_b32 s81, s81, 0x1000\ns_or_b32 s81, s81, s74\ns_branch .Lv64_%=\n.Lv75_%=:\n" \
    "v_mov_b32 v252, 0\nv_max3_u32 v252, v252, v72, v73\nv_max3_u32 v252, v252, v74, v75\nv_max3_u32 v252, v252, v76, v77\nv_max3_u32 v252, v252, v78, v79\n" \
    "v_cmp_eq_u32 vcc, -1, v252\ns_nop 0\ns_cbranch_vccz .Lv71_%=\ns_add_u32 s80, s80, s43\ns_xor_b32 s81, s81, 0x1000\ns_xor_b32 s90, s90, 0x1000\ns_add_u32 s68, s68, 1\n" \
    "s_and_b32 s69, s68, 63\ns_cbranch_scc1 .Lv65_%=\ns_cmp_gt_u32 s68, 0x400000\ns_cbranch_scc1 .Lv1_%=\nbuffer_load_dword v253, off, s[64:67], 0 sc1\ns_waitcnt vmcnt(0)\n" \
    "v_readfirstlane_b32 s69, v253\ns_nop 0\ns_cmp_eq_u32 s69, 1\ns_cbranch_scc0 .Lv65_%=\ns_endpgm\n.Lv76_%=:\nv_mov_b32 v252, 0\nv_max3_u32 v252, v252, v80, v81\n" \
    "v_max3_u32 v252, v252, v82, v83\nv_cmp_eq_u32 vcc, -1, v252\ns_nop 0\ns_cbranch_vccz .Lv72_%=\ns_add_u32 s80, s80, s43\ns_xor_b32 s81, s81, 0x1000\n" \
    "s_xor_b32 s90, s90, 0x1000\n.Lv78_%=:\ns_memrealtime s[72:73]\ns_waitcnt vmcnt(0) lgkmcnt(0)\ns_sub_u32 s72, s72, s80\ns_cmp_lt_i32 s72, 0\ns_cbranch_scc1 .Lv78_%=\n" \
    "buffer_load_dwordx4 v[80:83], v2, s[64:67], s90 offen offset:0 sc1\nbuffer_load_dwordx4 v[84:87], v2, s[64:67], s90 offen offset:1024 sc1\ns_waitcnt vmcnt(0)\n" \
    "s_add_u32 s68, s68, 1\ns_and_b32 s69, s68, 63\ns_cbranch_scc1 .Lv76_%=\ns_cmp_gt_u32 s68, 0x400000\ns_cbranch_scc1 .Lv1_%=\nbuffer_load_dword v253, off, s[64:67], 0 sc1\n" \
    "s_waitcnt vmcnt(0)\nv_readfirstlane_b32 s69, v253\ns_nop 0\ns_cmp_eq_u32 s69, 1\ns_cbranch_scc0 .Lv76_%=\ns_endpgm\n.Lv77_%=:\nv_mov_b32 v252, 0\n" \
    "v_max3_u32 v252, v252, v84, v85\nv_max3_u32 v252, v252, v86, v87\nv_cmp_eq_u32 vcc, -1, v252\ns_nop 0\ns_cbranch_vccz .Lv73_%=\ns_add_u32 s80, s80, s43\n" \
    "s_xor_b32 s81, s81, 0x1000\ns_xor_b32 s90, s90, 0x1000\n.Lv79_%=:\ns_memrealtime s[72:73]\ns_waitcnt vmcnt(0) lgkmcnt(0)\ns_sub_u32 s72, s72, s80\ns_cmp_lt_i32 s72, 0\n" \
    "s_cbranch_scc1 .Lv79_%=\nbuffer_load_dwordx4 v[84:87], v2, s[64:67], s90 offen offset:1024 sc1\ns_waitcnt vmcnt(0)\ns_add_u32 s68, s68, 1\ns_and_b32 s69, s68, 63\n" \
    "s_cbranch_scc1 .Lv77_%=\ns_cmp_gt_u32 s68, 0x400000\ns_cbranch_scc1 .Lv1_%=\nbuffer_load_dword v253, off, s[64:67], 0 sc1\ns_waitcnt vmcnt(0)\n" \
    "v_readfirstlane_b32 s69, v253\ns_nop 0\ns_cmp_eq_u32 s69, 1\ns_cbranch_scc0 .Lv77_%=\ns_endpgm\n.Lv81_%=:\nv_mov_b32 v252, 0\nv_pk_max_u16 v252, v252, v32\n" \
    "v_pk_max_u16 v252, v252, v33\nv_pk_max_u16 v252, v252, v34\nv_pk_max_u16 v252, v252, v35\nv_pk_max_u16 v252, v252, v36\nv_pk_max_u16 v252, v252, v37\n" \
    "v_pk_max_u16 v252, v252, v38\nv_pk_max_u16 v252, v252, v39\nv_pk_max_u16 v252, v252, v40\nv_pk_max_u16 v252, v252, v41\nv_pk_max_u16 v252, v252, v42\n" \
    "v_pk_max_u16 v252, v252, v43\nv_pk_max_u16 v252, v252, v44\nv_pk_max_u16 v252, v252, v45\nv_pk_max_u16 v252, v252, v46\nv_pk_max_u16 v252, v252, v47\n" \
    "v_pk_max_u16 v252, v252, v252 op_sel:[0,1] op_sel_hi:[1,0]\nv_cmp_eq_u32 vcc, -1, v252\ns_nop 0\ns_cbranch_vccz .Lv82_%=\ns_add_u32 s68, s68, 1\ns_and_b32 s69, s68, 63\n" \
    "s_cbranch_scc1 .Lv80_%=\ns_cmp_gt_u32 s68, 0x400000\ns_cbranch_scc1 .Lv1_%=\nbuffer_load_dword v253, off, s[64:67], 0 sc1\ns_waitcnt vmcnt(0)\n" \
    "v_readfirstlane_b32 s69, v253\ns_nop 0\ns_cmp_eq_u32 s69, 1\ns_cbranch_scc0 .Lv80_%=\ns_endpgm\n.Lv1_%=:\nv_mov_b32 v253, 1\nbuffer_store_dword v253, off, s[64:67], 0 sc1\n" \
    "s_endpgm\n"
#define ASM_R \
    ".Lr2_%=:\ns_and_b32 s69, s32, 15\ns_lshl_b32 s69, s69, 21\ns_add_u32 s82, s42, s69\ns_lshr_b32 s69, s82, 8\ns_and_b32 s69, s69, 0x2000\ns_xor_b32 s82, s82, s69\n" \
    "s_and_b32 s69, s32, 15\ns_lshl_b32 s69, s69, 19\ns_add_u32 s83, s43, s69\ns_lshr_b32 s69, s83, 8\ns_and_b32 s69, s69, 0x2000\ns_xor_b32 s83, s83, s69\n" \
    "s_and_b32 s69, s32, 15\ns_lshl_b32 s69, s69, 18\ns_add_u32 s84, s44, s69\ns_lshr_b32 s69, s84, 8\ns_and_b32 s69, s69, 0x2000\ns_xor_b32 s84, s84, s69\n" \
    "s_and_b32 s69, s32, 15\ns_lshl_b32 s69, s69, 18\ns_add_u32 s94, s45, s69\ns_lshr_b32 s69, s94, 8\ns_and_b32 s69, s69, 0x2000\ns_xor_b32 s94, s94, s69\n" \
    "s_add_u32 s69, s32, 1\ns_and_b32 s69, s69, 15\ns_lshl_b32 s69, s69, 17\ns_add_u32 s92, s46, s69\ns_and_b32 s69, s32, 63\ns_lshl_b32 s69, s69, 19\ns_add_u32 s93, s47, s69\n" \
    "s_mov_b32 s68, 0\ns_cmp_lt_u32 s32, 2\ns_cbranch_scc1 .Lr6_%=\ns_sub_u32 s77, s32, 1\n.Lr4_%=:\nds_read_b32 v253, v10\ns_waitcnt lgkmcnt(0)\nv_readfirstlane_b32 s74, v253\n" \
    "s_nop 0\ns_and_b32 s75, s74, 127\ns_cmp_lt_u32 s75, s77\ns_cbranch_scc1 .Lr5_%=\ns_cmp_gt_u32 s75, s77\ns_cbranch_scc1 .Lr6_%=\ns_memrealtime s[72:73]\n" \
    "s_waitcnt lgkmcnt(0)\ns_lshr_b32 s69, s74, 8\ns_sub_u32 s69, s69, s72\ns_bfe_i32 s69, s69, 0x180000\ns_add_u32 s76, s72, s69\ns_add_u32 s76, s76, s58\n.Lr21_%=:\n" \
    "s_memrealtime s[72:73]\ns_waitcnt lgkmcnt(0)\ns_sub_u32 s72, s72, s76\ns_cmp_lt_i32 s72, 0\ns_cbranch_scc1 .Lr21_%=\n.Lr6_%=:\n" \
    "buffer_load_dword v253, v8, s[64:67], 0 offen sc1\ns_waitcnt vmcnt(0)\nv_cmp_ge_u32 vcc, s32, v253\ns_nop 0\ns_cbranch_vccnz .Lr7_%=\ns_cmp_eq_u32 s32, 0\n" \
    "s_cbranch_scc1 .Lr15_%=\n.Lr13_%=:\nds_read_b32 v253, v10\ns_waitcnt lgkmcnt(0)\nv_readfirstlane_b32 s74, v253\ns_nop 0\ns_and_b32 s75, s74, 127\ns_cmp_lt_u32 s75, s32\n" \
    "s_cbranch_scc1 .Lr14_%=\ns_and_b32 s57, s74, 128\ns_memrealtime s[72:73]\ns_waitcnt lgkmcnt(0)\ns_lshr_b32 s69, s74, 8\ns_sub_u32 s69, s69, s72\n" \
    "s_bfe_i32 s69, s69, 0x180000\ns_add_u32 s56, s72, s69\ns_sub_u32 s76, s56, s40\n.Lr22_%=:\ns_memrealtime s[72:73]\ns_waitcnt lgkmcnt(0)\ns_sub_u32 s72, s72, s76\n" \
    "s_cmp_lt_i32 s72, 0\ns_cbranch_scc1 .Lr22_%=\n.Lr15_%=:\nbuffer_load_dwordx4 a[0:3], v11, s[64:67], s82 offen offset:0 sc1\n" \
    "buffer_load_dwordx4 a[16:19], v11, s[64:67], s82 offen offset:512 sc1\nbuffer_load_dwordx4 a[4:7], v11, s[64:67], s82 offen offset:2048 sc1\n" \
    "buffer_load_dwordx4 a[20:23], v11, s[64:67], s82 offen offset:2560 sc1\ns_add_u32 s82, s82, 4096\nbuffer_load_dwordx4 a[8:11], v11, s[64:67], s82 offen offset:0 sc1\n" \
    "buffer_load_dwordx4 a[24:27], v11, s[64:67], s82 offen offset:512 sc1\nbuffer_load_dwordx4 a[12:15], v11, s[64:67], s82 offen offset:2048 sc1\n" \
    "buffer_load_dwordx4 a[28:31], v11, s[64:67], s82 offen offset:2560 sc1\ns_add_u32 s82, s82, 12288\nbuffer_load_dwordx4 a[32:35], v11, s[64:67], s82 offen offset:0 sc1\n" \
    "buffer_load_dwordx4 a[48:51], v11, s[64:67], s82 offen offset:512 sc1\nbuffer_load_dwordx4 a[36:39], v11, s[64:67], s82 offen offset:2048 sc1\n" \
    "buffer_load_dwordx4 a[52:55], v11, s[64:67], s82 offen offset:2560 sc1\ns_add_u32 s82, s82, 4096\nbuffer_load_dwordx4 a[40:43], v11, s[64:67], s82 offen offset:0 sc1\n" \
    "buffer_load_dwordx4 a[56:59], v11, s[64:67], s82 offen offset:512 sc1\nbuffer_load_dwordx4 a[44:47], v11, s[64:67], s82 offen offset:2048 sc1\n" \
    "buffer_load_dwordx4 a[60:63], v11, s[64:67], s82 offen offset:2560 sc1\ns_add_u32 s82, s82, 12288\nbuffer_load_dwordx4 a[64:67], v11, s[64:67], s82 offen offset:0 sc1\n" \
    "buffer_load_dwordx4 a[80:83], v11, s[64:67], s82 offen offset:512 sc1\nbuffer_load_dwordx4 a[68:71], v11, s[64:67], s82 offen offset:2048 sc1\n" \
    "buffer_load_dwordx4 a[84:87], v11, s[64:67], s82 offen offset:2560 sc1\ns_add_u32 s82, s82, 4096\nbuffer_load_dwordx4 a[72:75], v11, s[64:67], s82 offen offset:0 sc1\n" \
    "buffer_load_dwordx4 a[88:91], v11, s[64:67], s82 offen offset:512 sc1\nbuffer_load_dwordx4 a[76:79], v11, s[64:67], s82 offen offset:2048 sc1\n" \
    "buffer_load_dwordx4 a[92:95], v11, s[64:67], s82 offen offset:2560 sc1\ns_add_u32 s82, s82, 12288\nbuffer_load_dwordx4 a[96:99], v11, s[64:67], s82 offen offset:0 sc1\n" \
    "buffer_load_dwordx4 a[112:115], v11, s[64:67], s82 offen offset:512 sc1\nbuffer_load_dwordx4 a[100:103], v11, s[64:67], s82 offen offset:2048 sc1\n" \
    "buffer_load_dwordx4 a[116:119], v11, s[64:67], s82 offen offset:2560 sc1\ns_add_u32 s82, s82, 4096\nbuffer_load_dwordx4 a[104:107], v11, s[64:67], s82 offen offset:0 sc1\n" \
    "buffer_load_dwordx4 a[120:123], v11, s[64:67], s82 offen offset:512 sc1\nbuffer_load_dwordx4 a[108:111], v11, s[64:67], s82 offen offset:2048 sc1\n" \
    "buffer_load_dwordx4 a[124:127], v11, s[64:67], s82 offen offset:2560 sc1\nglobal_load_dwordx4 v[224:227], v7, s[54:55]\ns_cmp_lg_u32 s32, 0\ns_cbranch_scc1 .Lr3_%=\n" \
    "s_memrealtime s[72:73]\ns_waitcnt lgkmcnt(0)\ns_add_u32 s72, s72, s41\n.Lr20_%=:\ns_add_u32 s56, s56, s38\ns_xor_b32 s57, s57, 128\ns_sub_u32 s69, s56, s72\n" \
    "s_cmp_lt_i32 s69, 0\ns_cbranch_scc1 .Lr20_%=\ns_add_u32 s76, s56, s35\ns_cmp_eq_u32 s48, 0\ns_cbranch_scc1 .Lr23_%=\ns_lshl_b32 s74, s76, 8\ns_add_u32 s74, s74, s57\n" \
    "s_add_u32 s74, s74, s32\ns_add_u32 s74, s74, 1\nv_mov_b32 v253, s74\ns_xor_b32 s69, s48, 512\ns_mov_b64 exec, 1\nbuffer_store_dword v253, off, s[64:67], s69 sc1\n" \
    "s_mov_b64 exec, -1\n.Lr23_%=:\n.Lr3_%=:\ns_waitcnt vmcnt(0)\ns_sub_u32 s76, s56, s59\n.Lr25_%=:\ns_memrealtime s[72:73]\ns_waitcnt lgkmcnt(0)\ns_sub_u32 s72, s72, s76\n" \
    "s_cmp_lt_i32 s72, 0\ns_cbranch_scc1 .Lr25_%=\nbuffer_load_dwordx4 v[24:27], off, s[64:67], s60 sc1\ns_sub_u32 s76, s56, s34\ns_mov_b32 s77, 0\ns_waitcnt vmcnt(0)\n" \
    "v_readfirstlane_b32 s74, v24\ns_nop 0\ns_and_b32 s75, s74, 127\ns_cmp_lg_u32 s75, s32\ns_cbranch_scc1 .Lr26_%=\ns_lshr_b32 s69, s74, 8\ns_sub_u32 s69, s69, s76\n" \
    "s_bfe_i32 s69, s69, 0x180000\ns_cmp_le_i32 s69, s77\ns_cbranch_scc1 .Lr26_%=\ns_mov_b32 s77, s69\ns_and_b32 s57, s74, 128\n.Lr26_%=:\nv_readfirstlane_b32 s74, v25\n" \
    "s_nop 0\ns_and_b32 s75, s74, 127\ns_cmp_lg_u32 s75, s32\ns_cbranch_scc1 .Lr27_%=\ns_lshr_b32 s69, s74, 8\ns_sub_u32 s69, s69, s76\ns_bfe_i32 s69, s69, 0x180000\n" \
    "s_cmp_le_i32 s69, s77\ns_cbranch_scc1 .Lr27_%=\ns_mov_b32 s77, s69\ns_and_b32 s57, s74, 128\n.Lr27_%=:\nv_readfirstlane_b32 s74, v26\ns_nop 0\ns_and_b32 s75, s74, 127\n" \
    "s_cmp_lg_u32 s75, s32\ns_cbranch_scc1 .Lr28_%=\ns_lshr_b32 s69, s74, 8\ns_sub_u32 s69, s69, s76\ns_bfe_i32 s69, s69, 0x180000\ns_cmp_le_i32 s69, s77\n" \
    "s_cbranch_scc1 .Lr28_%=\ns_mov_b32 s77, s69\ns_and_b32 s57, s74, 128\n.Lr28_%=:\nv_readfirstlane_b32 s74, v27\ns_nop 0\ns_and_b32 s75, s74, 127\ns_cmp_lg_u32 s75, s32\n" \
    "s_cbranch_scc1 .Lr29_%=\ns_lshr_b32 s69, s74, 8\ns_sub_u32 s69, s69, s76\ns_bfe_i32 s69, s69, 0x180000\ns_cmp_le_i32 s69, s77\ns_cbranch_scc1 .Lr29_%=\ns_mov_b32 s77, s69\n" \
    "s_and_b32 s57, s74, 128\n.Lr29_%=:\ns_cmp_eq_u32 s77, 0\ns_cbranch_scc1 .Lr24_%=\ns_cmp_eq_u32 s32, 0\ns_cbranch_scc1 .Lr24_%=\ns_add_u32 s56, s56, s77\n" \
    "s_add_u32 s76, s56, s35\ns_cmp_eq_u32 s48, 0\ns_cbranch_scc1 .Lr30_%=\ns_lshl_b32 s74, s76, 8\ns_add_u32 s74, s74, s57\ns_add_u32 s74, s74, s32\ns_add_u32 s74, s74, 1\n" \
    "v_mov_b32 v253, s74\ns_xor_b32 s69, s48, 512\ns_mov_b64 exec, 1\nbuffer_store_dword v253, off, s[64:67], s69 sc1\ns_mov_b64 exec, -1\n.Lr30_%=:\n.Lr24_%=:\n" \
    "s_lshl_b32 s69, s57, 7\ns_xor_b32 s84, s84, s69\ns_lshl_b32 s69, s57, 5\ns_xor_b32 s92, s92, s69\ns_add_u32 s75, s83, 4096\ns_mov_b32 s68, 0\n.Lr8_%=:\n" \
    "s_add_u32 s85, s84, 0x8000\ns_add_u32 s86, s84, 0x10000\ns_add_u32 s87, s84, 0x18000\ns_sub_u32 s76, s56, s36\n.Lr31_%=:\ns_memrealtime s[72:73]\ns_waitcnt lgkmcnt(0)\n" \
    "s_sub_u32 s72, s72, s76\ns_cmp_lt_i32 s72, 0\ns_cbranch_scc1 .Lr31_%=\nbuffer_load_dwordx4 v[32:35], v1, s[64:67], s83 offen offset:0 sc1\n" \
    "buffer_load_dwordx4 v[36:39], v1, s[64:67], s83 offen offset:1024 sc1\nbuffer_load_dwordx4 v[40:43], v1, s[64:67], s83 offen offset:2048 sc1\n" \
    "buffer_load_dwordx4 v[44:47], v1, s[64:67], s83 offen offset:3072 sc1\nbuffer_load_dwordx4 v[48:51], v1, s[64:67], s75 offen offset:0 sc1\n" \
    "buffer_load_dwordx4 v[52:55], v1, s[64:67], s75 offen offset:1024 sc1\nbuffer_load_dwordx4 v[56:59], v1, s[64:67], s75 offen offset:2048 sc1\n" \
    "buffer_load_dwordx4 v[60:63], v1, s[64:67], s75 offen offset:3072 sc1\ns_cmp_eq_u32 s32, 63\ns_cbranch_scc1 .Lr32_%=\n.Lr33_%=:\ns_memrealtime s[72:73]\n" \
    "s_waitcnt lgkmcnt(0)\ns_sub_u32 s72, s72, s56\ns_cmp_lt_i32 s72, 0\ns_cbranch_scc1 .Lr33_%=\nbuffer_load_dwordx4 v[68:71], v1, s[64:67], s84 offen offset:0 sc1\n" \
    "buffer_load_dwordx4 v[72:75], v1, s[64:67], s84 offen offset:1024 sc1\nbuffer_load_dwordx4 v[76:79], v1, s[64:67], s84 offen offset:2048 sc1\n" \
    "buffer_load_dwordx4 v[80:83], v1, s[64:67], s84 offen offset:3072 sc1\nbuffer_load_dwordx4 v[88:91], v1, s[64:67], s85 offen offset:0 sc1\n" \
    "buffer_load_dwordx4 v[92:95], v1, s[64:67], s85 offen offset:1024 sc1\nbuffer_load_dwordx4 v[96:99], v1, s[64:67], s85 offen offset:2048 sc1\n" \
    "buffer_load_dwordx4 v[100:103], v1, s[64:67], s85 offen offset:3072 sc1\nbuffer_load_dwordx4 v[108:111], v1, s[64:67], s86 offen offset:0 sc1\n" \
    "buffer_load_dwordx4 v[112:115], v1, s[64:67], s86 offen offset:1024 sc1\nbuffer_load_dwordx4 v[116:119], v1, s[64:67], s86 offen offset:2048 sc1\n" \
    "buffer_load_dwordx4 v[120:123], v1, s[64:67], s86 offen offset:3072 sc1\nbuffer_load_dwordx4 v[128:131], v1, s[64:67], s87 offen offset:0 sc1\n" \
    "buffer_load_dwordx4 v[132:135], v1, s[64:67], s87 offen offset:1024 sc1\nbuffer_load_dwordx4 v[136:139], v1, s[64:67], s87 offen offset:2048 sc1\n" \
    "buffer_load_dwordx4 v[140:143], v1, s[64:67], s87 offen offset:3072 sc1\nbuffer_load_dwordx4 v[64:67], v2, s[64:67], s84 offen offset:0 sc1\n" \
    "buffer_load_dwordx4 v[84:87], v2, s[64:67], s85 offen offset:0 sc1\nbuffer_load_dwordx4 v[104:107], v2, s[64:67], s86 offen offset:0 sc1\n" \
    "buffer_load_dwordx4 v[124:127], v2, s[64:67], s87 offen offset:0 sc1\nv_mov_b64 v[24:25], 0\nv_mov_b64 v[26:27], 0\ns_xor_b32 s77, s92, 0x1000\ns_waitcnt vmcnt(26)\n" \
    "v_permlane16_swap_b32 v32, v33\nv_permlane16_swap_b32 v34, v35\nv_permlane16_swap_b32 v36, v37\nv_permlane16_swap_b32 v38, v39\ns_waitcnt vmcnt(24)\n" \
    "v_permlane16_swap_b32 v40, v41\nv_permlane16_swap_b32 v42, v43\nv_permlane16_swap_b32 v44, v45\nv_permlane16_swap_b32 v46, v47\ns_waitcnt vmcnt(22)\n" \
    "v_permlane16_swap_b32 v48, v49\nv_permlane16_swap_b32 v50, v51\nv_permlane16_swap_b32 v52, v53\nv_permlane16_swap_b32 v54, v55\ns_waitcnt vmcnt(20)\n" \
    "v_permlane16_swap_b32 v56, v57\nv_permlane16_swap_b32 v58, v59\nv_permlane16_swap_b32 v60, v61\nv_permlane16_swap_b32 v62, v63\ns_waitcnt vmcnt(19)\n" \
    "v_smfmac_f32_16x16x64_bf16 v[24:27], v[68:71], a[0:7], v0\ns_waitcnt vmcnt(18)\nv_smfmac_f32_16x16x64_bf16 v[24:27], v[72:75], a[8:15], v0\ns_waitcnt vmcnt(17)\n" \
    "v_smfmac_f32_16x16x64_bf16 v[24:27], v[76:79], a[16:23], v0\ns_waitcnt vmcnt(16)\nv_smfmac_f32_16x16x64_bf16 v[24:27], v[80:83], a[24:31], v0\ns_waitcnt vmcnt(15)\n" \
    "v_smfmac_f32_16x16x64_bf16 v[24:27], v[88:91], a[32:39], v0\ns_waitcnt vmcnt(14)\nv_smfmac_f32_16x16x64_bf16 v[24:27], v[92:95], a[40:47], v0\ns_waitcnt vmcnt(13)\n" \
    "v_smfmac_f32_16x16x64_bf16 v[24:27], v[96:99], a[48:55], v0\ns_waitcnt vmcnt(12)\nv_smfmac_f32_16x16x64_bf16 v[24:27], v[100:103], a[56:63], v0\ns_waitcnt vmcnt(11)\n" \
    "v_smfmac_f32_16x16x64_bf16 v[24:27], v[108:111], a[64:71], v0\ns_waitcnt vmcnt(10)\nv_smfmac_f32_16x16x64_bf16 v[24:27], v[112:115], a[72:79], v0\ns_waitcnt vmcnt(9)\n" \
    "v_smfmac_f32_16x16x64_bf16 v[24:27], v[116:119], a[80:87], v0\ns_waitcnt vmcnt(8)\nv_smfmac_f32_16x16x64_bf16 v[24:27], v[120:123], a[88:95], v0\ns_waitcnt vmcnt(7)\n" \
    "v_smfmac_f32_16x16x64_bf16 v[24:27], v[128:131], a[96:103], v0\ns_waitcnt vmcnt(6)\nv_smfmac_f32_16x16x64_bf16 v[24:27], v[132:135], a[104:111], v0\ns_waitcnt vmcnt(5)\n" \
    "v_smfmac_f32_16x16x64_bf16 v[24:27], v[136:139], a[112:119], v0\ns_waitcnt vmcnt(4)\nv_smfmac_f32_16x16x64_bf16 v[24:27], v[140:143], a[120:127], v0\ns_waitcnt vmcnt(3)\n" \
    "v_smfmac_f32_16x16x64_bf16 v[24:27], v[64:67], v[32:39], v0\ns_waitcnt vmcnt(2)\nv_smfmac_f32_16x16x64_bf16 v[24:27], v[84:87], v[40:47], v0\ns_waitcnt vmcnt(1)\n" \
    "v_smfmac_f32_16x16x64_bf16 v[24:27], v[104:107], v[48:55], v0\ns_waitcnt vmcnt(0)\nv_smfmac_f32_16x16x64_bf16 v[24:27], v[124:127], v[56:63], v0\ns_nop 7\n" \
    "v_permlane32_swap_b32 v24, v26\nv_permlane32_swap_b32 v25, v27\nv_pk_add_f32 v[24:25], v[24:25], v[26:27]\nv_cvt_pk_bf16_f32 v26, v24, v25\nds_write_b32 v3, v26\n" \
    "ds_read_b64_tr_b16 v[28:29], v4\nds_read_b64_tr_b16 v[30:31], v4 offset:64\nv_cmp_u_f32 vcc, v24, v25\ns_nop 0\ns_cbranch_vccnz .Lr17_%=\n.Lr16_%=:\ns_waitcnt lgkmcnt(0)\n" \
    "buffer_store_dwordx4 v[28:31], v6, s[64:67], s92 offen sc1\nbuffer_store_dwordx4 v[28:31], v6, s[64:67], s77 offen sc1\n.Lr10_%=:\ns_memrealtime s[72:73]\n" \
    "s_waitcnt lgkmcnt(0)\ns_add_u32 s76, s56, s35\n.Lr35_%=:\ns_sub_u32 s69, s76, s72\ns_cmp_ge_i32 s69, s37\ns_cbranch_scc1 .Lr36_%=\ns_add_u32 s76, s76, s38\n" \
    "s_xor_b32 s57, s57, 128\ns_branch .Lr35_%=\n.Lr36_%=:\ns_add_u32 s76, s76, s34\ns_lshl_b32 s74, s76, 8\ns_add_u32 s74, s74, s57\ns_add_u32 s74, s74, s32\n" \
    "s_add_u32 s74, s74, 1\nv_mov_b32 v253, s74\nds_write_b32 v10, v253\ns_add_u32 s76, s76, s35\ns_cmp_eq_u32 s48, 0\ns_cbranch_scc1 .Lr37_%=\ns_lshl_b32 s74, s76, 8\n" \
    "s_add_u32 s74, s74, s57\ns_add_u32 s74, s74, s32\ns_add_u32 s74, s74, 2\nv_mov_b32 v253, s74\ns_xor_b32 s69, s48, 0\ns_mov_b64 exec, 1\n" \
    "buffer_store_dword v253, off, s[64:67], s69 sc1\ns_mov_b64 exec, -1\n.Lr37_%=:\ns_xor_b32 s69, s49, 0x20000\ns_add_u32 s0, s94, s69\ns_add_u32 s1, s0, 0x4000\n" \
    "s_add_u32 s2, s0, 0x8000\ns_add_u32 s3, s0, 0xc000\ns_xor_b32 s69, s49, 0x30000\ns_add_u32 s4, s94, s69\ns_add_u32 s5, s4, 0x4000\ns_add_u32 s6, s4, 0x8000\n" \
    "s_add_u32 s7, s4, 0xc000\ns_xor_b32 s69, s49, 0x0\ns_add_u32 s8, s94, s69\ns_add_u32 s9, s8, 0x4000\ns_add_u32 s10, s8, 0x8000\ns_add_u32 s11, s8, 0xc000\n" \
    "s_xor_b32 s69, s49, 0x10000\ns_add_u32 s12, s94, s69\ns_add_u32 s13, s12, 0x4000\ns_add_u32 s14, s12, 0x8000\ns_add_u32 s15, s12, 0xc000\ns_cmp_ge_u32 s32, 62\n" \
    "s_cselect_b32 s76, s61, s39\ns_add_u32 s76, s76, s56\ns_mov_b32 s68, 0\ns_mov_b32 s80, 0\n.Lr38_%=:\ns_memrealtime s[72:73]\ns_waitcnt lgkmcnt(0)\ns_sub_u32 s72, s72, s76\n" \
    "s_cmp_lt_i32 s72, 0\ns_cbranch_scc1 .Lr38_%=\n.Lr11_%=:\nv_mov_b64 v[24:25], 0\nv_mov_b64 v[26:27], 0\nv_mov_b64 v[28:29], 0\nv_mov_b64 v[30:31], 0\n" \
    "v_mov_b64 v[228:229], 0\nv_mov_b64 v[230:231], 0\nv_mov_b64 v[232:233], 0\nv_mov_b64 v[234:235], 0\nv_mov_b32 v252, 0\nv_mov_b32 v250, 0\n" \
    "buffer_load_dwordx4 v[64:67], v2, s[64:67], s0 offen offset:0 sc1\nbuffer_load_dwordx4 v[84:87], v2, s[64:67], s1 offen offset:0 sc1\n" \
    "buffer_load_dwordx4 v[104:107], v2, s[64:67], s2 offen offset:0 sc1\nbuffer_load_dwordx4 v[124:127], v2, s[64:67], s3 offen offset:0 sc1\n" \
    "buffer_load_dwordx4 v[144:147], v2, s[64:67], s4 offen offset:0 sc1\nbuffer_load_dwordx4 v[148:151], v2, s[64:67], s5 offen offset:0 sc1\n" \
    "buffer_load_dwordx4 v[152:155], v2, s[64:67], s6 offen offset:0 sc1\nbuffer_load_dwordx4 v[156:159], v2, s[64:67], s7 offen offset:0 sc1\n" \
    "buffer_load_dwordx4 v[160:163], v2, s[64:67], s8 offen offset:0 sc1\nbuffer_load_dwordx4 v[164:167], v2, s[64:67], s9 offen offset:0 sc1\n" \
    "buffer_load_dwordx4 v[168:171], v2, s[64:67], s10 offen offset:0 sc1\nbuffer_load_dwordx4 v[172:175], v2, s[64:67], s11 offen offset:0 sc1\n" \
    "buffer_load_dwordx4 v[176:179], v2, s[64:67], s12 offen offset:0 sc1\nbuffer_load_dwordx4 v[180:183], v2, s[64:67], s13 offen offset:0 sc1\n" \
    "buffer_load_dwordx4 v[184:187], v2, s[64:67], s14 offen offset:0 sc1\nbuffer_load_dwordx4 v[188:191], v2, s[64:67], s15 offen offset:0 sc1\n" \
    "buffer_load_dwordx4 v[68:71], v1, s[64:67], s0 offen offset:0 sc1\nbuffer_load_dwordx4 v[72:75], v1, s[64:67], s0 offen offset:1024 sc1\n" \
    "buffer_load_dwordx4 v[76:79], v1, s[64:67], s0 offen offset:2048 sc1\nbuffer_load_dwordx4 v[80:83], v1, s[64:67], s0 offen offset:3072 sc1\n" \
    "buffer_load_dwordx4 v[88:91], v1, s[64:67], s1 offen offset:0 sc1\nbuffer_load_dwordx4 v[92:95], v1, s[64:67], s1 offen offset:1024 sc1\n" \
    "buffer_load_dwordx4 v[96:99], v1, s[64:67], s1 offen offset:2048 sc1\nbuffer_load_dwordx4 v[100:103], v1, s[64:67], s1 offen offset:3072 sc1\n" \
    "buffer_load_dwordx4 v[108:111], v1, s[64:67], s2 offen offset:0 sc1\nbuffer_load_dwordx4 v[112:115], v1, s[64:67], s2 offen offset:1024 sc1\n" \
    "buffer_load_dwordx4 v[116:119], v1, s[64:67], s2 offen offset:2048 sc1\nbuffer_load_dwordx4 v[120:123], v1, s[64:67], s2 offen offset:3072 sc1\n" \
    "buffer_load_dwordx4 v[128:131], v1, s[64:67], s3 offen offset:0 sc1\nbuffer_load_dwordx4 v[132:135], v1, s[64:67], s3 offen offset:1024 sc1\n" \
    "buffer_load_dwordx4 v[136:139], v1, s[64:67], s3 offen offset:2048 sc1\nbuffer_load_dwordx4 v[140:143], v1, s[64:67], s3 offen offset:3072 sc1\ns_waitcnt vmcnt(30)\n" \
    "v_smfmac_f32_16x16x64_bf16 v[24:27], v[64:67], v[32:39], v0\nbuffer_load_dwordx4 a[128:131], v1, s[64:67], s4 offen offset:0 sc1\n" \
    "v_smfmac_f32_16x16x64_bf16 v[24:27], v[84:87], v[40:47], v0\nbuffer_load_dwordx4 a[132:135], v1, s[64:67], s4 offen offset:1024 sc1\ns_waitcnt vmcnt(30)\n" \
    "v_smfmac_f32_16x16x64_bf16 v[24:27], v[104:107], v[48:55], v0\nbuffer_load_dwordx4 a[136:139], v1, s[64:67], s4 offen offset:2048 sc1\nv_max3_u32 v250, v250, v64, v84\n" \
    "v_smfmac_f32_16x16x64_bf16 v[24:27], v[124:127], v[56:63], v0\nbuffer_load_dwordx4 a[140:143], v1, s[64:67], s4 offen offset:3072 sc1\ns_waitcnt vmcnt(30)\n" \
    "v_max3_u32 v250, v250, v104, v124\nv_smfmac_f32_16x16x64_bf16 v[28:31], v[144:147], v[32:39], v0\nbuffer_load_dwordx4 a[144:147], v1, s[64:67], s5 offen offset:0 sc1\n" \
    "v_smfmac_f32_16x16x64_bf16 v[28:31], v[148:151], v[40:47], v0\nbuffer_load_dwordx4 a[148:151], v1, s[64:67], s5 offen offset:1024 sc1\ns_waitcnt vmcnt(30)\n" \
    "v_smfmac_f32_16x16x64_bf16 v[28:31], v[152:155], v[48:55], v0\nbuffer_load_dwordx4 a[152:155], v1, s[64:67], s5 offen offset:2048 sc1\nv_max3_u32 v250, v250, v144, v148\n" \
    "v_smfmac_f32_16x16x64_bf16 v[28:31], v[156:159], v[56:63], v0\nbuffer_load_dwordx4 a[156:159], v1, s[64:67], s5 offen offset:3072 sc1\ns_waitcnt vmcnt(30)\n" \
    "v_max3_u32 v250, v250, v152, v156\nv_smfmac_f32_16x16x64_bf16 v[228:231], v[160:163], v[32:39], v0\nbuffer_load_dwordx4 a[160:163], v1, s[64:67], s6 offen offset:0 sc1\n" \
    "v_smfmac_f32_16x16x64_bf16 v[228:231], v[164:167], v[40:47], v0\nbuffer_load_dwordx4 a[164:167], v1, s[64:67], s6 offen offset:1024 sc1\ns_waitcnt vmcnt(30)\n" \
    "v_smfmac_f32_16x16x64_bf16 v[228:231], v[168:171], v[48:55], v0\nbuffer_load_dwordx4 a[168:171], v1, s[64:67], s6 offen offset:2048 sc1\nv_max3_u32 v250, v250, v160, v164\n" \
    "v_smfmac_f32_16x16x64_bf16 v[228:231], v[172:175], v[56:63], v0\nbuffer_load_dwordx4 a[172:175], v1, s[64:67], s6 offen offset:3072 sc1\ns_waitcnt vmcnt(30)\n" \
    "v_max3_u32 v250, v250, v168, v172\nv_smfmac_f32_16x16x64_bf16 v[232:235], v[176:179], v[32:39], v0\nbuffer_load_dwordx4 a[176:179], v1, s[64:67], s7 offen offset:0 sc1\n" \
    "v_smfmac_f32_16x16x64_bf16 v[232:235], v[180:183], v[40:47], v0\nbuffer_load_dwordx4 a[180:183], v1, s[64:67], s7 offen offset:1024 sc1\ns_waitcnt vmcnt(30)\n" \
    "v_smfmac_f32_16x16x64_bf16 v[232:235], v[184:187], v[48:55], v0\nbuffer_load_dwordx4 a[184:187], v1, s[64:67], s7 offen offset:2048 sc1\nv_max3_u32 v250, v250, v176, v180\n" \
    "v_smfmac_f32_16x16x64_bf16 v[232:235], v[188:191], v[56:63], v0\nbuffer_load_dwordx4 a[188:191], v1, s[64:67], s7 offen offset:3072 sc1\ns_waitcnt vmcnt(30)\n" \
    "v_max3_u32 v250, v250, v184, v188\nv_smfmac_f32_16x16x64_bf16 v[24:27], v[68:71], a[0:7], v0\nbuffer_load_dwordx4 a[192:195], v1, s[64:67], s8 offen offset:0 sc1\n" \
    "v_cmp_eq_u32 vcc, -1, v250\nv_smfmac_f32_16x16x64_bf16 v[24:27], v[72:75], a[8:15], v0\nbuffer_load_dwordx4 a[196:199], v1, s[64:67], s8 offen offset:1024 sc1\n" \
    "s_waitcnt vmcnt(30)\nv_smfmac_f32_16x16x64_bf16 v[24:27], v[76:79], a[16:23], v0\nbuffer_load_dwordx4 a[200:203], v1, s[64:67], s9 offen offset:0 sc1\n" \
    "s_cbranch_vccnz .Lr39_%=\nv_smfmac_f32_16x16x64_bf16 v[24:27], v[80:83], a[24:31], v0\nbuffer_load_dwordx4 a[204:207], v1, s[64:67], s9 offen offset:1024 sc1\n" \
    "s_waitcnt vmcnt(30)\nv_smfmac_f32_16x16x64_bf16 v[24:27], v[88:91], a[32:39], v0\nbuffer_load_dwordx4 a[208:211], v1, s[64:67], s10 offen offset:0 sc1\n" \
    "v_smfmac_f32_16x16x64_bf16 v[24:27], v[92:95], a[40:47], v0\nbuffer_load_dwordx4 a[212:215], v1, s[64:67], s10 offen offset:1024 sc1\ns_waitcnt vmcnt(30)\n" \
    "v_smfmac_f32_16x16x64_bf16 v[24:27], v[96:99], a[48:55], v0\nbuffer_load_dwordx4 a[216:219], v1, s[64:67], s11 offen offset:0 sc1\n" \
    "v_smfmac_f32_16x16x64_bf16 v[24:27], v[100:103], a[56:63], v0\nbuffer_load_dwordx4 a[220:223], v1, s[64:67], s11 offen offset:1024 sc1\ns_waitcnt vmcnt(30)\n" \
    "v_smfmac_f32_16x16x64_bf16 v[24:27], v[108:111], a[64:71], v0\nbuffer_load_dwordx4 a[224:227], v1, s[64:67], s12 offen offset:0 sc1\n" \
    "v_smfmac_f32_16x16x64_bf16 v[24:27], v[112:115], a[72:79], v0\nbuffer_load_dwordx4 a[228:231], v1, s[64:67], s12 offen offset:1024 sc1\ns_waitcnt vmcnt(30)\n" \
    "v_smfmac_f32_16x16x64_bf16 v[24:27], v[116:119], a[80:87], v0\nbuffer_load_dwordx4 a[232:235], v1, s[64:67], s13 offen offset:0 sc1\n" \
    "v_smfmac_f32_16x16x64_bf16 v[24:27], v[120:123], a[88:95], v0\nbuffer_load_dwordx4 a[236:239], v1, s[64:67], s13 offen offset:1024 sc1\ns_waitcnt vmcnt(30)\n" \
    "v_smfmac_f32_16x16x64_bf16 v[24:27], v[128:131], a[96:103], v0\nbuffer_load_dwordx4 a[240:243], v1, s[64:67], s14 offen offset:0 sc1\n" \
    "v_smfmac_f32_16x16x64_bf16 v[24:27], v[132:135], a[104:111], v0\nbuffer_load_dwordx4 a[244:247], v1, s[64:67], s14 offen offset:1024 sc1\ns_waitcnt vmcnt(30)\n" \
    "v_smfmac_f32_16x16x64_bf16 v[24:27], v[136:139], a[112:119], v0\nbuffer_load_dwordx4 a[248:251], v1, s[64:67], s15 offen offset:0 sc1\n" \
    "v_smfmac_f32_16x16x64_bf16 v[24:27], v[140:143], a[120:127], v0\nbuffer_load_dwordx4 a[252:255], v1, s[64:67], s15 offen offset:1024 sc1\ns_nop 1\ns_waitcnt vmcnt(30)\n" \
    "v_smfmac_f32_16x16x64_bf16 v[28:31], a[128:131], a[0:7], v0\ns_nop 2\nv_permlane32_swap_b32 v24, v26\nv_smfmac_f32_16x16x64_bf16 v[28:31], a[132:135], a[8:15], v0\n" \
    "s_waitcnt vmcnt(28)\nv_permlane32_swap_b32 v25, v27\nv_smfmac_f32_16x16x64_bf16 v[28:31], a[136:139], a[16:23], v0\nv_add_f32 v24, v24, v26\n" \
    "v_smfmac_f32_16x16x64_bf16 v[28:31], a[140:143], a[24:31], v0\ns_waitcnt vmcnt(26)\nv_add_f32 v25, v25, v27\nv_smfmac_f32_16x16x64_bf16 v[28:31], a[144:147], a[32:39], v0\n" \
    "v_fma_f32 v252, v24, v25, v252\nv_smfmac_f32_16x16x64_bf16 v[28:31], a[148:151], a[40:47], v0\ns_waitcnt vmcnt(24)\nv_cvt_pk_bf16_f32 v24, v24, v25\n" \
    "v_smfmac_f32_16x16x64_bf16 v[28:31], a[152:155], a[48:55], v0\nds_write_b32 v3, v24 offset:512\nv_smfmac_f32_16x16x64_bf16 v[28:31], a[156:159], a[56:63], v0\n" \
    "s_waitcnt vmcnt(22)\nv_smfmac_f32_16x16x64_bf16 v[28:31], a[160:163], a[64:71], v0\nv_smfmac_f32_16x16x64_bf16 v[28:31], a[164:167], a[72:79], v0\ns_waitcnt vmcnt(20)\n" \
    "v_smfmac_f32_16x16x64_bf16 v[28:31], a[168:171], a[80:87], v0\nv_smfmac_f32_16x16x64_bf16 v[28:31], a[172:175], a[88:95], v0\ns_waitcnt vmcnt(18)\n" \
    "v_smfmac_f32_16x16x64_bf16 v[28:31], a[176:179], a[96:103], v0\nv_smfmac_f32_16x16x64_bf16 v[28:31], a[180:183], a[104:111], v0\ns_waitcnt vmcnt(16)\n" \
    "v_smfmac_f32_16x16x64_bf16 v[28:31], a[184:187], a[112:119], v0\nv_smfmac_f32_16x16x64_bf16 v[28:31], a[188:191], a[120:127], v0\ns_nop 2\ns_waitcnt vmcnt(14)\n" \
    "v_smfmac_f32_16x16x64_bf16 v[228:231], a[192:195], a[0:7], v0\ns_nop 2\nv_permlane32_swap_b32 v28, v30\nv_smfmac_f32_16x16x64_bf16 v[228:231], a[196:199], a[8:15], v0\n" \
    "s_waitcnt vmcnt(12)\nv_permlane32_swap_b32 v29, v31\nv_smfmac_f32_16x16x64_bf16 v[228:231], a[200:203], a[32:39], v0\nv_add_f32 v28, v28, v30\n" \
    "v_smfmac_f32_16x16x64_bf16 v[228:231], a[204:207], a[40:47], v0\ns_waitcnt vmcnt(10)\nv_add_f32 v29, v29, v31\n" \
    "v_smfmac_f32_16x16x64_bf16 v[228:231], a[208:211], a[64:71], v0\nv_fma_f32 v252, v28, v29, v252\nv_smfmac_f32_16x16x64_bf16 v[228:231], a[212:215], a[72:79], v0\n" \
    "s_waitcnt vmcnt(8)\nv_cvt_pk_bf16_f32 v28, v28, v29\nv_smfmac_f32_16x16x64_bf16 v[228:231], a[216:219], a[96:103], v0\nds_write_b32 v3, v28 offset:768\n" \
    "v_smfmac_f32_16x16x64_bf16 v[228:231], a[220:223], a[104:111], v0\ns_nop 2\ns_waitcnt vmcnt(6)\nv_smfmac_f32_16x16x64_bf16 v[232:235], a[224:227], a[0:7], v0\ns_nop 2\n" \
    "v_permlane32_swap_b32 v228, v230\nv_smfmac_f32_16x16x64_bf16 v[232:235], a[228:231], a[8:15], v0\ns_waitcnt vmcnt(4)\nv_permlane32_swap_b32 v229, v231\n" \
    "v_smfmac_f32_16x16x64_bf16 v[232:235], a[232:235], a[32:39], v0\nv_add_f32 v228, v228, v230\nv_smfmac_f32_16x16x64_bf16 v[232:235], a[236:239], a[40:47], v0\n" \
    "s_waitcnt vmcnt(2)\nv_add_f32 v229, v229, v231\nv_smfmac_f32_16x16x64_bf16 v[232:235], a[240:243], a[64:71], v0\nv_fma_f32 v252, v228, v229, v252\n" \
    "v_smfmac_f32_16x16x64_bf16 v[232:235], a[244:247], a[72:79], v0\ns_waitcnt vmcnt(0)\nv_cvt_pk_bf16_f32 v228, v228, v229\n" \
    "v_smfmac_f32_16x16x64_bf16 v[232:235], a[248:251], a[96:103], v0\nds_write_b32 v3, v228 offset:0\nv_smfmac_f32_16x16x64_bf16 v[232:235], a[252:255], a[104:111], v0\n" \
    "s_nop 7\nv_permlane32_swap_b32 v232, v234\nv_permlane32_swap_b32 v233, v235\nv_add_f32 v232, v232, v234\nv_add_f32 v233, v233, v235\nv_fma_f32 v252, v232, v233, v252\n" \
    "v_cvt_pk_bf16_f32 v232, v232, v233\nv_cmp_u_f32 vcc, v252, v252\nds_write_b32 v3, v232 offset:256\nds_read_b64_tr_b16 v[24:25], v5\n" \
    "ds_read_b64_tr_b16 v[26:27], v5 offset:64\ns_cbranch_vccnz .Lr12_%=\n.Lr18_%=:\ns_cmp_lt_u32 s32, 4\ns_cbranch_scc1 .Lr40_%=\n.Lr41_%=:\nv_lshlrev_b32 v72, 16, v224\n" \
    "v_and_b32 v73, 0xffff0000, v224\nv_lshlrev_b32 v74, 16, v225\nv_and_b32 v75, 0xffff0000, v225\nv_lshlrev_b32 v76, 16, v226\nv_and_b32 v77, 0xffff0000, v226\n" \
    "v_lshlrev_b32 v78, 16, v227\nv_and_b32 v79, 0xffff0000, v227\ns_xor_b32 s77, s93, 0x2000\ns_waitcnt lgkmcnt(0)\nv_lshlrev_b32 v64, 16, v24\nv_and_b32 v65, 0xffff0000, v24\n" \
    "v_lshlrev_b32 v66, 16, v25\nv_and_b32 v67, 0xffff0000, v25\nv_lshlrev_b32 v68, 16, v26\nv_and_b32 v69, 0xffff0000, v26\nv_lshlrev_b32 v70, 16, v27\n" \
    "v_and_b32 v71, 0xffff0000, v27\nv_mul_f32 v64, v64, v16\nv_mul_f32 v65, v65, v17\nv_mul_f32 v66, v66, v18\nv_mul_f32 v67, v67, v19\nv_mul_f32 v68, v68, v20\n" \
    "v_mul_f32 v69, v69, v21\nv_mul_f32 v70, v70, v22\nv_mul_f32 v71, v71, v23\nv_cvt_pk_bf16_f32 v64, 0, v64\nv_cvt_pk_bf16_f32 v65, 0, v65\nv_cvt_pk_bf16_f32 v66, 0, v66\n" \
    "v_cvt_pk_bf16_f32 v67, 0, v67\nv_cvt_pk_bf16_f32 v68, 0, v68\nv_cvt_pk_bf16_f32 v69, 0, v69\nv_cvt_pk_bf16_f32 v70, 0, v70\nv_cvt_pk_bf16_f32 v71, 0, v71\n" \
    "v_add_f32 v64, v64, v72\nv_add_f32 v65, v65, v73\nv_add_f32 v66, v66, v74\nv_add_f32 v67, v67, v75\nv_add_f32 v68, v68, v76\nv_add_f32 v69, v69, v77\n" \
    "v_add_f32 v70, v70, v78\nv_add_f32 v71, v71, v79\nv_cvt_pk_bf16_f32 v24, v64, v65\nv_cvt_pk_bf16_f32 v25, v66, v67\nv_cvt_pk_bf16_f32 v26, v68, v69\n" \
    "v_cvt_pk_bf16_f32 v27, v70, v71\nv_min_u32 v28, -2, v24\nv_min_u32 v29, -2, v25\nv_min_u32 v30, -2, v26\nv_min_u32 v31, -2, v27\ns_cmp_lt_u32 s32, 62\n" \
    "s_cbranch_scc1 .Lr42_%=\nbuffer_store_dwordx4 v[28:31], v1, s[64:67], s93 offen sc1\nbuffer_store_dwordx4 v[28:31], v1, s[64:67], s77 offen sc1\n.Lr42_%=:\n" \
    "v_lshlrev_b32 v64, 16, v24\nv_and_b32 v65, 0xffff0000, v24\nv_lshlrev_b32 v66, 16, v25\nv_and_b32 v67, 0xffff0000, v25\nv_lshlrev_b32 v68, 16, v26\n" \
    "v_and_b32 v69, 0xffff0000, v26\nv_lshlrev_b32 v70, 16, v27\nv_and_b32 v71, 0xffff0000, v27\nv_add_f32 v72, v64, v65\nv_add_f32 v73, v66, v67\nv_add_f32 v74, v68, v69\n" \
    "v_add_f32 v75, v70, v71\nv_add_f32 v72, v72, v73\nv_add_f32 v74, v74, v75\nv_add_f32 v76, v72, v74\nv_add_f32 v77, v72, v74\ns_nop 1\nv_permlane32_swap_b32 v76, v77\n" \
    "v_add_f32 v76, v76, v77\nv_mul_f32 v76, 0x3d800000, v76\nv_sub_f32 v64, v64, v76\nv_sub_f32 v65, v65, v76\nv_sub_f32 v66, v66, v76\nv_sub_f32 v67, v67, v76\n" \
    "v_sub_f32 v68, v68, v76\nv_sub_f32 v69, v69, v76\nv_sub_f32 v70, v70, v76\nv_sub_f32 v71, v71, v76\nv_mul_f32 v72, v64, v64\nv_mul_f32 v73, v65, v65\n" \
    "v_fmac_f32 v72, v66, v66\nv_fmac_f32 v73, v67, v67\nv_fmac_f32 v72, v68, v68\nv_fmac_f32 v73, v69, v69\nv_fmac_f32 v72, v70, v70\nv_fmac_f32 v73, v71, v71\n" \
    "v_add_f32 v74, v72, v73\nv_add_f32 v75, v72, v73\ns_nop 1\nv_permlane32_swap_b32 v74, v75\nv_add_f32 v77, v74, v75\nv_min_u32 v76, -2, v76\nv_min_u32 v77, -2, v77\n" \
    "s_cmp_ge_u32 s32, 62\ns_cbranch_scc1 .Lr43_%=\ns_add_u32 s76, s32, 2\n.Lr45_%=:\nds_read_b32 v253, v10\ns_waitcnt lgkmcnt(0)\nv_readfirstlane_b32 s74, v253\ns_nop 0\n" \
    "s_and_b32 s75, s74, 127\ns_cmp_lt_u32 s75, s76\ns_cbranch_scc1 .Lr46_%=\nbuffer_store_dwordx4 v[28:31], v1, s[64:67], s93 offen sc1\n" \
    "buffer_store_dwordx4 v[28:31], v1, s[64:67], s77 offen sc1\n.Lr43_%=:\nbuffer_store_dwordx2 v[76:77], v9, s[64:67], s93 offen sc1\ns_cmp_ge_u32 s32, 56\n" \
    "s_cbranch_scc1 .Lr47_%=\nv_mov_b64 v[80:81], -1\nv_mov_b64 v[82:83], -1\ns_add_u32 s69, s32, 9\ns_and_b32 s69, s69, 15\ns_lshl_b32 s69, s69, 17\ns_add_u32 s92, s46, s69\n" \
    "s_xor_b32 s74, s92, 0x1000\nbuffer_store_dwordx4 v[80:83], v6, s[64:67], s92 offen sc1\nbuffer_store_dwordx4 v[80:83], v6, s[64:67], s74 offen sc1\n.Lr47_%=:\n" \
    "v_add_u32 v7, 0x40000, v7\ns_add_u32 s32, s32, 4\ns_cmp_lt_u32 s32, 64\ns_cbranch_scc1 .Lr2_%=\ns_endpgm\n.Lr5_%=:\ns_sleep 2\ns_add_u32 s68, s68, 1\n" \
    "s_and_b32 s69, s68, 63\ns_cbranch_scc1 .Lr4_%=\ns_cmp_gt_u32 s68, 0x400000\ns_cbranch_scc1 .Lr1_%=\nbuffer_load_dword v253, off, s[64:67], 0 sc1\ns_waitcnt vmcnt(0)\n" \
    "v_readfirstlane_b32 s69, v253\ns_nop 0\ns_cmp_eq_u32 s69, 1\ns_cbranch_scc0 .Lr4_%=\ns_endpgm\n.Lr7_%=:\ns_sleep 6\ns_add_u32 s68, s68, 1\ns_and_b32 s69, s68, 63\n" \
    "s_cbranch_scc1 .Lr6_%=\ns_cmp_gt_u32 s68, 0x400000\ns_cbranch_scc1 .Lr1_%=\nbuffer_load_dword v253, off, s[64:67], 0 sc1\ns_waitcnt vmcnt(0)\n" \
    "v_readfirstlane_b32 s69, v253\ns_nop 0\ns_cmp_eq_u32 s69, 1\ns_cbranch_scc0 .Lr6_%=\ns_endpgm\n.Lr14_%=:\ns_sleep 2\ns_add_u32 s68, s68, 1\ns_and_b32 s69, s68, 63\n" \
    "s_cbranch_scc1 .Lr13_%=\ns_cmp_gt_u32 s68, 0x400000\ns_cbranch_scc1 .Lr1_%=\nbuffer_load_dword v253, off, s[64:67], 0 sc1\ns_waitcnt vmcnt(0)\n" \
    "v_readfirstlane_b32 s69, v253\ns_nop 0\ns_cmp_eq_u32 s69, 1\ns_cbranch_scc0 .Lr13_%=\ns_endpgm\n.Lr32_%=:\ns_waitcnt vmcnt(0)\nv_permlane16_swap_b32 v32, v33\n" \
    "v_permlane16_swap_b32 v34, v35\nv_permlane16_swap_b32 v36, v37\nv_permlane16_swap_b32 v38, v39\nv_permlane16_swap_b32 v40, v41\nv_permlane16_swap_b32 v42, v43\n" \
    "v_permlane16_swap_b32 v44, v45\nv_permlane16_swap_b32 v46, v47\nv_permlane16_swap_b32 v48, v49\nv_permlane16_swap_b32 v50, v51\nv_permlane16_swap_b32 v52, v53\n" \
    "v_permlane16_swap_b32 v54, v55\nv_permlane16_swap_b32 v56, v57\nv_permlane16_swap_b32 v58, v59\nv_permlane16_swap_b32 v60, v61\nv_permlane16_swap_b32 v62, v63\n" \
    "s_branch .Lr10_%=\n.Lr17_%=:\ns_cmp_lt_u32 s68, 3\ns_cbranch_scc1 .Lr9_%=\nv_max3_u32 v252, v32, v33, v34\nv_max3_u32 v253, v35, v36, v37\nv_max3_u32 v252, v252, v38, v39\n" \
    "v_max3_u32 v253, v253, v40, v41\nv_max3_u32 v252, v252, v42, v43\nv_max3_u32 v253, v253, v44, v45\nv_max3_u32 v252, v252, v46, v47\nv_max3_u32 v253, v253, v48, v49\n" \
    "v_max3_u32 v252, v252, v50, v51\nv_max3_u32 v253, v253, v52, v53\nv_max3_u32 v252, v252, v54, v55\nv_max3_u32 v253, v253, v56, v57\nv_max3_u32 v252, v252, v58, v59\n" \
    "v_max3_u32 v253, v253, v60, v61\nv_max3_u32 v252, v252, v62, v63\nv_max3_u32 v252, v252, v64, v65\nv_max3_u32 v253, v253, v66, v67\nv_max3_u32 v252, v252, v68, v69\n" \
    "v_max3_u32 v253, v253, v70, v71\nv_max3_u32 v252, v252, v72, v73\nv_max3_u32 v253, v253, v74, v75\nv_max3_u32 v252, v252, v76, v77\nv_max3_u32 v253, v253, v78, v79\n" \
    "v_max3_u32 v252, v252, v80, v81\nv_max3_u32 v253, v253, v82, v83\nv_max3_u32 v252, v252, v84, v85\nv_max3_u32 v253, v253, v86, v87\nv_max3_u32 v252, v252, v88, v89\n" \
    "v_max3_u32 v253, v253, v90, v91\nv_max3_u32 v252, v252, v92, v93\nv_max3_u32 v253, v253, v94, v95\nv_max3_u32 v252, v252, v96, v97\nv_max3_u32 v253, v253, v98, v99\n" \
    "v_max3_u32 v252, v252, v100, v101\nv_max3_u32 v253, v253, v102, v103\nv_max3_u32 v252, v252, v104, v105\nv_max3_u32 v253, v253, v106, v107\n" \
    "v_max3_u32 v252, v252, v108, v109\nv_max3_u32 v253, v253, v110, v111\nv_max3_u32 v252, v252, v112, v113\nv_max3_u32 v253, v253, v114, v115\n" \
    "v_max3_u32 v252, v252, v116, v117\nv_max3_u32 v253, v253, v118, v119\nv_max3_u32 v252, v252, v120, v121\nv_max3_u32 v253, v253, v122, v123\n" \
    "v_max3_u32 v252, v252, v124, v125\nv_max3_u32 v253, v253, v126, v127\nv_max3_u32 v252, v252, v128, v129\nv_max3_u32 v253, v253, v130, v131\n" \
    "v_max3_u32 v252, v252, v132, v133\nv_max3_u32 v253, v253, v134, v135\nv_max3_u32 v252, v252, v136, v137\nv_max3_u32 v253, v253, v138, v139\n" \
    "v_max3_u32 v252, v252, v140, v141\nv_max3_u32 v253, v253, v142, v143\nv_max_u32 v252, v252, v253\nv_cmp_eq_u32 vcc, -1, v252\ns_nop 0\ns_cbranch_vccz .Lr16_%=\n.Lr9_%=:\n" \
    "s_add_u32 s56, s56, s38\ns_xor_b32 s57, s57, 128\ns_xor_b32 s84, s84, 0x4000\ns_xor_b32 s92, s92, 0x1000\ns_add_u32 s76, s56, s35\ns_cmp_eq_u32 s48, 0\n" \
    "s_cbranch_scc1 .Lr34_%=\ns_lshl_b32 s74, s76, 8\ns_add_u32 s74, s74, s57\ns_add_u32 s74, s74, s32\ns_add_u32 s74, s74, 1\nv_mov_b32 v253, s74\ns_xor_b32 s69, s48, 512\n" \
    "s_mov_b64 exec, 1\nbuffer_store_dword v253, off, s[64:67], s69 sc1\ns_mov_b64 exec, -1\n.Lr34_%=:\ns_add_u32 s68, s68, 1\ns_and_b32 s69, s68, 63\ns_cbranch_scc1 .Lr8_%=\n" \
    "s_cmp_gt_u32 s68, 0x400000\ns_cbranch_scc1 .Lr1_%=\nbuffer_load_dword v253, off, s[64:67], 0 sc1\ns_waitcnt vmcnt(0)\nv_readfirstlane_b32 s69, v253\ns_nop 0\n" \
    "s_cmp_eq_u32 s69, 1\ns_cbranch_scc0 .Lr8_%=\ns_endpgm\n.Lr39_%=:\ns_waitcnt vmcnt(0)\ns_add_u32 s68, s68, 1\ns_and_b32 s69, s68, 63\ns_cbranch_scc1 .Lr11_%=\n" \
    "s_cmp_gt_u32 s68, 0x400000\ns_cbranch_scc1 .Lr1_%=\nbuffer_load_dword v253, off, s[64:67], 0 sc1\ns_waitcnt vmcnt(0)\nv_readfirstlane_b32 s69, v253\ns_nop 0\n" \
    "s_cmp_eq_u32 s69, 1\ns_cbranch_scc0 .Lr11_%=\ns_endpgm\n.Lr12_%=:\ns_cmp_lg_u32 s80, 0\ns_cbranch_scc1 .Lr18_%=\ns_sleep 4\n" \
    "buffer_load_dwordx4 v[32:35], v1, s[64:67], s83 offen offset:0 sc1\nbuffer_load_dwordx4 v[36:39], v1, s[64:67], s83 offen offset:1024 sc1\n" \
    "buffer_load_dwordx4 v[40:43], v1, s[64:67], s83 offen offset:2048 sc1\nbuffer_load_dwordx4 v[44:47], v1, s[64:67], s83 offen offset:3072 sc1\n" \
    "buffer_load_dwordx4 v[48:51], v1, s[64:67], s75 offen offset:0 sc1\nbuffer_load_dwordx4 v[52:55], v1, s[64:67], s75 offen offset:1024 sc1\n" \
    "buffer_load_dwordx4 v[56:59], v1, s[64:67], s75 offen offset:2048 sc1\nbuffer_load_dwordx4 v[60:63], v1, s[64:67], s75 offen offset:3072 sc1\ns_waitcnt vmcnt(0)\n" \
    "v_permlane16_swap_b32 v32, v33\nv_permlane16_swap_b32 v34, v35\nv_permlane16_swap_b32 v36, v37\nv_permlane16_swap_b32 v38, v39\nv_permlane16_swap_b32 v40, v41\n" \
    "v_permlane16_swap_b32 v42, v43\nv_permlane16_swap_b32 v44, v45\nv_permlane16_swap_b32 v46, v47\nv_permlane16_swap_b32 v48, v49\nv_permlane16_swap_b32 v50, v51\n" \
    "v_permlane16_swap_b32 v52, v53\nv_permlane16_swap_b32 v54, v55\nv_permlane16_swap_b32 v56, v57\nv_permlane16_swap_b32 v58, v59\nv_permlane16_swap_b32 v60, v61\n" \
    "v_permlane16_swap_b32 v62, v63\nv_max3_u32 v252, v32, v33, v34\nv_max3_u32 v253, v35, v36, v37\nv_max3_u32 v252, v252, v38, v39\nv_max3_u32 v253, v253, v40, v41\n" \
    "v_max3_u32 v252, v252, v42, v43\nv_max3_u32 v253, v253, v44, v45\nv_max3_u32 v252, v252, v46, v47\nv_max3_u32 v253, v253, v48, v49\nv_max3_u32 v252, v252, v50, v51\n" \
    "v_max3_u32 v253, v253, v52, v53\nv_max3_u32 v252, v252, v54, v55\nv_max3_u32 v253, v253, v56, v57\nv_max3_u32 v252, v252, v58, v59\nv_max3_u32 v253, v253, v60, v61\n" \
    "v_max3_u32 v252, v252, v62, v63\ns_mov_b32 s76, s94\n.Lr19_%=:\ns_add_u32 s84, s76, 0x0\ns_add_u32 s85, s76, 0x4000\ns_add_u32 s86, s76, 0x8000\n" \
    "s_add_u32 s87, s76, 0xc000\nbuffer_load_dwordx4 v[64:67], v2, s[64:67], s84 offen offset:0 sc1\nbuffer_load_dwordx4 v[68:71], v1, s[64:67], s84 offen offset:0 sc1\n" \
    "buffer_load_dwordx4 v[72:75], v1, s[64:67], s84 offen offset:1024 sc1\nbuffer_load_dwordx4 v[76:79], v1, s[64:67], s84 offen offset:2048 sc1\n" \
    "buffer_load_dwordx4 v[80:83], v1, s[64:67], s84 offen offset:3072 sc1\nbuffer_load_dwordx4 v[84:87], v2, s[64:67], s85 offen offset:0 sc1\n" \
    "buffer_load_dwordx4 v[88:91], v1, s[64:67], s85 offen offset:0 sc1\nbuffer_load_dwordx4 v[92:95], v1, s[64:67], s85 offen offset:1024 sc1\n" \
    "buffer_load_dwordx4 v[96:99], v1, s[64:67], s85 offen offset:2048 sc1\nbuffer_load_dwordx4 v[100:103], v1, s[64:67], s85 offen offset:3072 sc1\n" \
    "buffer_load_dwordx4 v[104:107], v2, s[64:67], s86 offen offset:0 sc1\nbuffer_load_dwordx4 v[108:111], v1, s[64:67], s86 offen offset:0 sc1\n" \
    "buffer_load_dwordx4 v[112:115], v1, s[64:67], s86 offen offset:1024 sc1\nbuffer_load_dwordx4 v[116:119], v1, s[64:67], s86 offen offset:2048 sc1\n" \
    "buffer_load_dwordx4 v[120:123], v1, s[64:67], s86 offen offset:3072 sc1\nbuffer_load_dwordx4 v[124:127], v2, s[64:67], s87 offen offset:0 sc1\n" \
    "buffer_load_dwordx4 v[128:131], v1, s[64:67], s87 offen offset:0 sc1\nbuffer_load_dwordx4 v[132:135], v1, s[64:67], s87 offen offset:1024 sc1\n" \
    "buffer_load_dwordx4 v[136:139], v1, s[64:67], s87 offen offset:2048 sc1\nbuffer_load_dwordx4 v[140:143], v1, s[64:67], s87 offen offset:3072 sc1\ns_waitcnt vmcnt(0)\n" \
    "v_max3_u32 v252, v252, v64, v65\nv_max3_u32 v253, v253, v66, v67\nv_max3_u32 v252, v252, v68, v69\nv_max3_u32 v253, v253, v70, v71\nv_max3_u32 v252, v252, v72, v73\n" \
    "v_max3_u32 v253, v253, v74, v75\nv_max3_u32 v252, v252, v76, v77\nv_max3_u32 v253, v253, v78, v79\nv_max3_u32 v252, v252, v80, v81\nv_max3_u32 v253, v253, v82, v83\n" \
    "v_max3_u32 v252, v252, v84, v85\nv_max3_u32 v253, v253, v86, v87\nv_max3_u32 v252, v252, v88, v89\nv_max3_u32 v253, v253, v90, v91\nv_max3_u32 v252, v252, v92, v93\n" \
    "v_max3_u32 v253, v253, v94, v95\nv_max3_u32 v252, v252, v96, v97\nv_max3_u32 v253, v253, v98, v99\nv_max3_u32 v252, v252, v100, v101\nv_max3_u32 v253, v253, v102, v103\n" \
    "v_max3_u32 v252, v252, v104, v105\nv_max3_u32 v253, v253, v106, v107\nv_max3_u32 v252, v252, v108, v109\nv_max3_u32 v253, v253, v110, v111\n" \
    "v_max3_u32 v252, v252, v112, v113\nv_max3_u32 v253, v253, v114, v115\nv_max3_u32 v252, v252, v116, v117\nv_max3_u32 v253, v253, v118, v119\n" \
    "v_max3_u32 v252, v252, v120, v121\nv_max3_u32 v253, v253, v122, v123\nv_max3_u32 v252, v252, v124, v125\nv_max3_u32 v253, v253, v126, v127\n" \
    "v_max3_u32 v252, v252, v128, v129\nv_max3_u32 v253, v253, v130, v131\nv_max3_u32 v252, v252, v132, v133\nv_max3_u32 v253, v253, v134, v135\n" \
    "v_max3_u32 v252, v252, v136, v137\nv_max3_u32 v253, v253, v138, v139\nv_max3_u32 v252, v252, v140, v141\nv_max3_u32 v253, v253, v142, v143\ns_add_u32 s76, s76, 0x10000\n" \
    "s_sub_u32 s69, s76, s94\ns_cmp_lt_u32 s69, 0x40000\ns_cbranch_scc1 .Lr19_%=\nv_max_u32 v252, v252, v253\nv_cmp_eq_u32 vcc, -1, v252\ns_nop 0\ns_cmp_eq_u64 vcc, 0\n" \
    "s_cselect_b32 s80, 1, 0\ns_add_u32 s68, s68, 1\ns_and_b32 s69, s68, 63\ns_cbranch_scc1 .Lr11_%=\ns_cmp_gt_u32 s68, 0x400000\ns_cbranch_scc1 .Lr1_%=\n" \
    "buffer_load_dword v253, off, s[64:67], 0 sc1\ns_waitcnt vmcnt(0)\nv_readfirstlane_b32 s69, v253\ns_nop 0\ns_cmp_eq_u32 s69, 1\ns_cbranch_scc0 .Lr11_%=\ns_endpgm\n" \
    ".Lr40_%=:\nbuffer_load_dwordx4 v[16:19], v12, s[64:67], 0 offen offset:0 sc1\nbuffer_load_dwordx4 v[20:23], v12, s[64:67], 0 offen offset:16 sc1\ns_waitcnt vmcnt(0)\n" \
    "v_max3_u32 v252, v16, v17, v18\nv_max3_u32 v253, v19, v20, v21\nv_max3_u32 v252, v252, v22, v23\nv_max_u32 v252, v252, v253\nv_cmp_eq_u32 vcc, -1, v252\ns_nop 0\n" \
    "s_cbranch_vccz .Lr41_%=\ns_sleep 8\ns_add_u32 s68, s68, 1\ns_and_b32 s69, s68, 63\ns_cbranch_scc1 .Lr40_%=\ns_cmp_gt_u32 s68, 0x400000\ns_cbranch_scc1 .Lr1_%=\n" \
    "buffer_load_dword v253, off, s[64:67], 0 sc1\ns_waitcnt vmcnt(0)\nv_readfirstlane_b32 s69, v253\ns_nop 0\ns_cmp_eq_u32 s69, 1\ns_cbranch_scc0 .Lr40_%=\ns_endpgm\n" \
    ".Lr46_%=:\ns_sleep 2\ns_add_u32 s68, s68, 1\ns_and_b32 s69, s68, 63\ns_cbranch_scc1 .Lr45_%=\ns_cmp_gt_u32 s68, 0x400000\ns_cbranch_scc1 .Lr1_%=\n" \
    "buffer_load_dword v253, off, s[64:67], 0 sc1\ns_waitcnt vmcnt(0)\nv_readfirstlane_b32 s69, v253\ns_nop 0\ns_cmp_eq_u32 s69, 1\ns_cbranch_scc0 .Lr45_%=\ns_endpgm\n.Lr1_%=:\n" \
    "v_mov_b32 v253, 1\nbuffer_store_dword v253, off, s[64:67], 0 sc1\ns_endpgm\n"
#define ASM_F1 \
    "s_mov_b32 s90, 0x7fff0000\ns_mov_b32 s91, 0x7fff0000\ns_mov_b32 s92, 0x7fff0000\ns_mov_b32 s93, 0x7fff0000\ns_mov_b32 s80, 0\ns_mov_b32 s81, 0\ns_mov_b32 s68, 0\n.Lf2_%=:\n" \
    "s_xor_b32 s85, s38, 512\ns_and_b32 s69, s32, 63\ns_lshl_b32 s69, s69, 19\ns_add_u32 s82, s36, s69\ns_lshr_b32 s69, s82, 8\ns_and_b32 s69, s69, 0x2000\n" \
    "s_xor_b32 s82, s82, s69\ns_add_u32 s69, s32, 1\ns_and_b32 s69, s69, 63\ns_lshl_b32 s69, s69, 19\ns_add_u32 s83, s36, s69\ns_lshr_b32 s69, s83, 8\n" \
    "s_and_b32 s69, s69, 0x2000\ns_xor_b32 s83, s83, s69\ns_add_u32 s84, s37, s80\ns_cmp_eq_u32 s81, 1\ns_cbranch_scc1 .Lf6_%=\ns_add_u32 s77, s32, 2\ns_cmp_eq_u32 s81, 2\n" \
    "s_cbranch_scc1 .Lf5_%=\n.Lf3_%=:\nbuffer_load_dword v150, off, s[64:67], s38 sc1\ns_waitcnt vmcnt(0)\nv_and_b32 v112, 127, v150\nv_cmp_gt_u32 vcc, s77, v112\ns_nop 0\n" \
    "s_cbranch_vccnz .Lf4_%=\n.Lf5_%=:\nv_and_b32 v112, 127, v150\n.Lf11_%=:\ns_sleep 1\ns_memrealtime s[72:73]\ns_waitcnt lgkmcnt(0)\ns_cmp_ge_u32 s77, 64\n" \
    "s_cselect_b32 s69, s60, s34\ns_sub_u32 s72, s72, s69\ns_lshl_b32 s72, s72, 8\nv_sub_u32 v113, s72, v150\nv_cmp_gt_i32 s[74:75], 0, v113\nv_cmp_eq_u32 vcc, s77, v112\n" \
    "s_nop 0\ns_and_b64 vcc, vcc, s[74:75]\ns_cbranch_scc1 .Lf11_%=\n.Lf12_%=:\nbuffer_load_dword v151, v2, s[64:67], s82 offen sc1\ns_add_u32 m0, s84, 0\n" \
    "s_add_u32 s69, s82, s48\nbuffer_load_dwordx4 v0, s[64:67], s69 offen offset:0 sc1 lds\nbuffer_load_dwordx4 v0, s[64:67], s69 offen offset:1024 sc1 lds\n" \
    "s_add_u32 m0, s84, 8192\ns_add_u32 s69, s82, s49\nbuffer_load_dwordx4 v0, s[64:67], s69 offen offset:0 sc1 lds\n" \
    "buffer_load_dwordx4 v0, s[64:67], s69 offen offset:1024 sc1 lds\ns_add_u32 m0, s84, 16384\ns_add_u32 s69, s82, s50\n" \
    "buffer_load_dwordx4 v0, s[64:67], s69 offen offset:0 sc1 lds\nbuffer_load_dwordx4 v0, s[64:67], s69 offen offset:1024 sc1 lds\n.Lf6_%=:\ns_waitcnt vmcnt(6)\n" \
    "v_cmp_eq_u32 vcc, -1, v151\ns_nop 0\ns_cbranch_vccnz .Lf13_%=\ns_cmp_lg_u32 s33, 0\ns_cbranch_scc1 .Lf14_%=\ns_memrealtime s[72:73]\ns_waitcnt lgkmcnt(0)\n" \
    "s_lshl_b32 s72, s72, 8\ns_add_u32 s72, s72, s32\ns_add_u32 s72, s72, 1\nv_mov_b32 v112, s72\ns_mov_b64 exec, 1\nbuffer_store_dword v112, off, s[64:67], s39 sc1\n" \
    "s_mov_b64 exec, -1\n.Lf14_%=:\ns_add_u32 s77, s32, 3\ns_add_u32 m0, s84, 24576\ns_add_u32 s69, s82, s51\nbuffer_load_dwordx4 v0, s[64:67], s69 offen offset:0 sc1 lds\n" \
    "buffer_load_dwordx4 v0, s[64:67], s69 offen offset:1024 sc1 lds\ns_waitcnt vmcnt(6)\ns_barrier\nds_read_b128 v[64:67], v1 offset:0\nds_read_b128 v[68:71], v1 offset:1024\n" \
    "ds_read_b128 v[72:75], v1 offset:2048\nds_read_b128 v[76:79], v1 offset:3072\ns_waitcnt lgkmcnt(3)\nv_mfma_f32_32x32x16_bf16 v[48:63], a[0:3], v[64:67], 0\n" \
    "ds_read_b128 v[80:83], v1 offset:4096\ns_add_u32 m0, s84, 32768\ns_add_u32 s69, s82, s52\nbuffer_load_dwordx4 v0, s[64:67], s69 offen offset:0 sc1 lds\n" \
    "s_waitcnt lgkmcnt(3)\nv_mfma_f32_32x32x16_bf16 v[48:63], a[4:7], v[68:71], v[48:63]\nds_read_b128 v[84:87], v1 offset:5120\ns_waitcnt lgkmcnt(3)\n" \
    "v_mfma_f32_32x32x16_bf16 v[48:63], a[8:11], v[72:75], v[48:63]\nds_read_b128 v[88:91], v1 offset:6144\nbuffer_load_dwordx4 v0, s[64:67], s69 offen offset:1024 sc1 lds\n" \
    "s_waitcnt lgkmcnt(3)\nv_mfma_f32_32x32x16_bf16 v[48:63], a[12:15], v[76:79], v[48:63]\nds_read_b128 v[92:95], v1 offset:7168\ns_waitcnt vmcnt(6) lgkmcnt(3)\ns_barrier\n" \
    "v_mfma_f32_32x32x16_bf16 v[48:63], a[16:19], v[80:83], v[48:63]\nds_read_b128 v[64:67], v1 offset:8192\ns_add_u32 s69, s82, s41\n" \
    "buffer_load_dwordx4 v[96:99], v0, s[64:67], s69 offen offset:0 sc1\ns_waitcnt lgkmcnt(3)\nv_mfma_f32_32x32x16_bf16 v[48:63], a[20:23], v[84:87], v[48:63]\n" \
    "ds_read_b128 v[68:71], v1 offset:9216\ns_waitcnt lgkmcnt(3)\nv_mfma_f32_32x32x16_bf16 v[48:63], a[24:27], v[88:91], v[48:63]\nds_read_b128 v[72:75], v1 offset:10240\n" \
    "buffer_load_dwordx4 v[100:103], v0, s[64:67], s69 offen offset:1024 sc1\ns_waitcnt lgkmcnt(3)\nv_mfma_f32_32x32x16_bf16 v[48:63], a[28:31], v[92:95], v[48:63]\n" \
    "ds_read_b128 v[76:79], v1 offset:11264\ns_waitcnt lgkmcnt(3)\nv_mfma_f32_32x32x16_bf16 v[48:63], a[32:35], v[64:67], v[48:63]\nds_read_b128 v[80:83], v1 offset:12288\n" \
    "buffer_load_dwordx4 v[104:107], v0, s[64:67], s69 offen offset:2048 sc1\ns_waitcnt lgkmcnt(3)\nv_mfma_f32_32x32x16_bf16 v[48:63], a[36:39], v[68:71], v[48:63]\n" \
    "ds_read_b128 v[84:87], v1 offset:13312\ns_waitcnt lgkmcnt(3)\nv_mfma_f32_32x32x16_bf16 v[48:63], a[40:43], v[72:75], v[48:63]\nds_read_b128 v[88:91], v1 offset:14336\n" \
    "buffer_load_dwordx4 v[108:111], v0, s[64:67], s69 offen offset:3072 sc1\ns_waitcnt lgkmcnt(3)\nv_mfma_f32_32x32x16_bf16 v[48:63], a[44:47], v[76:79], v[48:63]\n" \
    "ds_read_b128 v[92:95], v1 offset:15360\ns_waitcnt vmcnt(6) lgkmcnt(3)\ns_barrier\nv_mfma_f32_32x32x16_bf16 v[48:63], a[48:51], v[80:83], v[48:63]\n" \
    "ds_read_b128 v[64:67], v1 offset:16384\ns_add_u32 m0, s84, 40960\ns_add_u32 s69, s82, s53\nbuffer_load_dwordx4 v0, s[64:67], s69 offen offset:0 sc1 lds\n" \
    "s_waitcnt lgkmcnt(3)\nv_mfma_f32_32x32x16_bf16 v[48:63], a[52:55], v[84:87], v[48:63]\nds_read_b128 v[68:71], v1 offset:17408\ns_waitcnt lgkmcnt(3)\n" \
    "v_mfma_f32_32x32x16_bf16 v[48:63], a[56:59], v[88:91], v[48:63]\nds_read_b128 v[72:75], v1 offset:18432\nbuffer_load_dwordx4 v0, s[64:67], s69 offen offset:1024 sc1 lds\n" \
    "s_waitcnt lgkmcnt(3)\nv_mfma_f32_32x32x16_bf16 v[48:63], a[60:63], v[92:95], v[48:63]\nds_read_b128 v[76:79], v1 offset:19456\ns_waitcnt lgkmcnt(3)\n" \
    "v_mfma_f32_32x32x16_bf16 v[48:63], a[64:67], v[64:67], v[48:63]\nds_read_b128 v[80:83], v1 offset:20480\ns_add_u32 m0, s84, 49152\ns_add_u32 s69, s82, s54\n" \
    "buffer_load_dwordx4 v0, s[64:67], s69 offen offset:0 sc1 lds\ns_waitcnt lgkmcnt(3)\nv_mfma_f32_32x32x16_bf16 v[48:63], a[68:71], v[68:71], v[48:63]\n" \
    "ds_read_b128 v[84:87], v1 offset:21504\ns_waitcnt lgkmcnt(3)\nv_mfma_f32_32x32x16_bf16 v[48:63], a[72:75], v[72:75], v[48:63]\nds_read_b128 v[88:91], v1 offset:22528\n" \
    "buffer_load_dwordx4 v0, s[64:67], s69 offen offset:1024 sc1 lds\ns_waitcnt lgkmcnt(3)\nv_mfma_f32_32x32x16_bf16 v[48:63], a[76:79], v[76:79], v[48:63]\n" \
    "ds_read_b128 v[92:95], v1 offset:23552\ns_waitcnt lgkmcnt(3)\nv_mfma_f32_32x32x16_bf16 v[48:63], a[80:83], v[80:83], v[48:63]\nds_read_b128 v[64:67], v1 offset:24576\n" \
    "s_add_u32 m0, s84, 57344\ns_add_u32 s69, s82, s55\nbuffer_load_dwordx4 v0, s[64:67], s69 offen offset:0 sc1 lds\ns_waitcnt lgkmcnt(3)\n" \
    "v_mfma_f32_32x32x16_bf16 v[48:63], a[84:87], v[84:87], v[48:63]\nds_read_b128 v[68:71], v1 offset:25600\ns_waitcnt lgkmcnt(3)\n" \
    "v_mfma_f32_32x32x16_bf16 v[48:63], a[88:91], v[88:91], v[48:63]\nds_read_b128 v[72:75], v1 offset:26624\nbuffer_load_dwordx4 v0, s[64:67], s69 offen offset:1024 sc1 lds\n" \
    "s_waitcnt lgkmcnt(3)\nv_mfma_f32_32x32x16_bf16 v[48:63], a[92:95], v[92:95], v[48:63]\nds_read_b128 v[76:79], v1 offset:27648\ns_waitcnt lgkmcnt(3)\n" \
    "v_mfma_f32_32x32x16_bf16 v[48:63], a[96:99], v[64:67], v[48:63]\nds_read_b128 v[80:83], v1 offset:28672\ns_and_b32 s69, s32, 63\ns_lshl_b32 s69, s69, 19\n" \
    "s_add_u32 s82, s40, s69\ns_lshr_b32 s69, s82, 8\ns_and_b32 s69, s69, 0x2000\ns_waitcnt lgkmcnt(3)\nv_mfma_f32_32x32x16_bf16 v[48:63], a[100:103], v[68:71], v[48:63]\n" \
    "ds_read_b128 v[84:87], v1 offset:29696\ns_xor_b32 s82, s82, s69\ns_waitcnt lgkmcnt(3)\nv_mfma_f32_32x32x16_bf16 v[48:63], a[104:107], v[72:75], v[48:63]\n" \
    "ds_read_b128 v[88:91], v1 offset:30720\ns_waitcnt vmcnt(6)\ns_waitcnt lgkmcnt(3)\nv_max3_u32 v116, v96, v97, v98\nv_max3_u32 v116, v116, v99, v100\n" \
    "v_max3_u32 v116, v116, v101, v102\nv_max3_u32 v116, v116, v103, v104\nv_max3_u32 v116, v116, v105, v106\nv_mfma_f32_32x32x16_bf16 v[48:63], a[108:111], v[76:79], v[48:63]\n" \
    "ds_read_b128 v[92:95], v1 offset:31744\ns_waitcnt vmcnt(4) lgkmcnt(3)\ns_barrier\nv_max3_u32 v116, v116, v107, v108\nv_max3_u32 v116, v116, v109, v110\n" \
    "v_max_u32 v116, v116, v111\nv_cmp_eq_u32 s[70:71], -1, v116\nv_sub_f32 v114, v96, v100\nv_mfma_f32_32x32x16_bf16 v[48:63], a[112:115], v[80:83], v[48:63]\n" \
    "ds_read_b128 v[64:67], v1 offset:32768\ns_waitcnt lgkmcnt(3)\nv_add_f32 v96, v96, v100\nv_add_f32 v97, v97, v101\nv_mul_f32 v114, v114, v114\nv_mul_f32 v96, 0.5, v96\n" \
    "v_fmac_f32 v97, 0x41000000, v114\nv_mfma_f32_32x32x16_bf16 v[48:63], a[116:119], v[84:87], v[48:63]\nds_read_b128 v[68:71], v1 offset:33792\ns_waitcnt lgkmcnt(3)\n" \
    "v_sub_f32 v114, v104, v108\nv_add_f32 v104, v104, v108\nv_add_f32 v105, v105, v109\nv_mul_f32 v114, v114, v114\nv_mul_f32 v104, 0.5, v104\n" \
    "v_mfma_f32_32x32x16_bf16 v[48:63], a[120:123], v[88:91], v[48:63]\nds_read_b128 v[72:75], v1 offset:34816\ns_waitcnt lgkmcnt(3)\nv_fmac_f32 v105, 0x41000000, v114\n" \
    "v_sub_f32 v114, v96, v104\nv_add_f32 v96, v96, v104\nv_add_f32 v97, v97, v105\nv_mul_f32 v114, v114, v114\n" \
    "v_mfma_f32_32x32x16_bf16 v[48:63], a[124:127], v[92:95], v[48:63]\nds_read_b128 v[76:79], v1 offset:35840\ns_waitcnt lgkmcnt(3)\nv_mul_f32 v96, 0.5, v96\n" \
    "v_fmac_f32 v97, 0x41800000, v114\nv_sub_f32 v114, v98, v102\nv_add_f32 v98, v98, v102\nv_add_f32 v99, v99, v103\n" \
    "v_mfma_f32_32x32x16_bf16 v[48:63], a[128:131], v[64:67], v[48:63]\nds_read_b128 v[80:83], v1 offset:36864\ns_waitcnt lgkmcnt(3)\nv_mul_f32 v114, v114, v114\n" \
    "v_mul_f32 v98, 0.5, v98\nv_fmac_f32 v99, 0x41000000, v114\nv_sub_f32 v114, v106, v110\nv_add_f32 v106, v106, v110\n" \
    "v_mfma_f32_32x32x16_bf16 v[48:63], a[132:135], v[68:71], v[48:63]\nds_read_b128 v[84:87], v1 offset:37888\ns_waitcnt lgkmcnt(3)\nv_add_f32 v107, v107, v111\n" \
    "v_mul_f32 v114, v114, v114\nv_mul_f32 v106, 0.5, v106\nv_fmac_f32 v107, 0x41000000, v114\nv_sub_f32 v114, v98, v106\n" \
    "v_mfma_f32_32x32x16_bf16 v[48:63], a[136:139], v[72:75], v[48:63]\nds_read_b128 v[88:91], v1 offset:38912\ns_waitcnt lgkmcnt(3)\nv_add_f32 v98, v98, v106\n" \
    "v_add_f32 v99, v99, v107\nv_mul_f32 v114, v114, v114\nv_mul_f32 v98, 0.5, v98\nv_fmac_f32 v99, 0x41800000, v114\n" \
    "v_mfma_f32_32x32x16_bf16 v[48:63], a[140:143], v[76:79], v[48:63]\nds_read_b128 v[92:95], v1 offset:39936\ns_waitcnt lgkmcnt(3)\nv_mov_b64 v[100:101], v[96:97]\n" \
    "v_mov_b64 v[102:103], v[98:99]\nv_mfma_f32_32x32x16_bf16 v[48:63], a[144:147], v[80:83], v[48:63]\nds_read_b128 v[64:67], v1 offset:40960\ns_waitcnt lgkmcnt(3)\n" \
    "v_permlane16_swap_b32 v96, v100\nv_permlane16_swap_b32 v97, v101\nv_permlane16_swap_b32 v98, v102\nv_mfma_f32_32x32x16_bf16 v[48:63], a[148:151], v[84:87], v[48:63]\n" \
    "ds_read_b128 v[68:71], v1 offset:41984\ns_waitcnt lgkmcnt(3)\nv_permlane16_swap_b32 v99, v103\nv_sub_f32 v114, v96, v100\nv_add_f32 v96, v96, v100\n" \
    "v_add_f32 v97, v97, v101\nv_mfma_f32_32x32x16_bf16 v[48:63], a[152:155], v[88:91], v[48:63]\nds_read_b128 v[72:75], v1 offset:43008\ns_waitcnt lgkmcnt(3)\n" \
    "v_mul_f32 v114, v114, v114\nv_mul_f32 v96, 0.5, v96\nv_fmac_f32 v97, 0x42000000, v114\nv_sub_f32 v114, v98, v102\nv_add_f32 v98, v98, v102\n" \
    "v_mfma_f32_32x32x16_bf16 v[48:63], a[156:159], v[92:95], v[48:63]\nds_read_b128 v[76:79], v1 offset:44032\ns_waitcnt lgkmcnt(3)\nv_add_f32 v99, v99, v103\n" \
    "v_mul_f32 v114, v114, v114\nv_mul_f32 v98, 0.5, v98\nv_fmac_f32 v99, 0x42000000, v114\nv_mov_b64 v[100:101], v[96:97]\n" \
    "v_mfma_f32_32x32x16_bf16 v[48:63], a[160:163], v[64:67], v[48:63]\nds_read_b128 v[80:83], v1 offset:45056\nbuffer_load_dword v150, off, s[64:67], s85 sc1\n" \
    "s_waitcnt lgkmcnt(3)\nv_mov_b64 v[102:103], v[98:99]\nv_permlane32_swap_b32 v96, v100\nv_permlane32_swap_b32 v97, v101\n" \
    "v_mfma_f32_32x32x16_bf16 v[48:63], a[164:167], v[68:71], v[48:63]\nds_read_b128 v[84:87], v1 offset:46080\ns_waitcnt lgkmcnt(3)\nv_permlane32_swap_b32 v98, v102\n" \
    "v_permlane32_swap_b32 v99, v103\nv_sub_f32 v114, v96, v100\nv_mfma_f32_32x32x16_bf16 v[48:63], a[168:171], v[72:75], v[48:63]\nds_read_b128 v[88:91], v1 offset:47104\n" \
    "s_waitcnt lgkmcnt(3)\nv_add_f32 v96, v96, v100\nv_add_f32 v97, v97, v101\nv_mul_f32 v114, v114, v114\nv_mul_f32 v96, 0.5, v96\nv_fmac_f32 v97, 0x42800000, v114\n" \
    "v_mfma_f32_32x32x16_bf16 v[48:63], a[172:175], v[76:79], v[48:63]\nds_read_b128 v[92:95], v1 offset:48128\ns_waitcnt vmcnt(1) lgkmcnt(3)\ns_barrier\n" \
    "v_sub_f32 v114, v98, v102\nv_add_f32 v98, v98, v102\nv_add_f32 v99, v99, v103\nv_mul_f32 v114, v114, v114\nv_mul_f32 v98, 0.5, v98\n" \
    "v_mfma_f32_32x32x16_bf16 v[48:63], a[176:179], v[80:83], v[48:63]\nds_read_b128 v[64:67], v1 offset:49152\ns_waitcnt lgkmcnt(3)\nv_fmac_f32 v99, 0x42800000, v114\n" \
    "v_mfma_f32_32x32x16_bf16 v[48:63], a[180:183], v[84:87], v[48:63]\nds_read_b128 v[68:71], v1 offset:50176\ns_cmp_lg_u64 s[70:71], 0\ns_cselect_b32 s76, -1, 0\n" \
    "s_waitcnt lgkmcnt(3)\nv_min_u32 v96, -2, v96\nv_min_u32 v98, -2, v98\nv_or_b32 v96, s76, v96\nv_or_b32 v98, s76, v98\n" \
    "v_mfma_f32_32x32x16_bf16 v[48:63], a[184:187], v[88:91], v[48:63]\nds_read_b128 v[72:75], v1 offset:51200\nds_write_b128 v3, v[96:99]\ns_waitcnt lgkmcnt(4)\n" \
    "v_mfma_f32_32x32x16_bf16 v[48:63], a[188:191], v[92:95], v[48:63]\nds_read_b128 v[76:79], v1 offset:52224\ns_waitcnt lgkmcnt(4)\n" \
    "v_mfma_f32_32x32x16_bf16 v[48:63], a[192:195], v[64:67], v[48:63]\nds_read_b128 v[80:83], v1 offset:53248\ns_waitcnt lgkmcnt(2)\ns_barrier\n" \
    "ds_read_b64 v[96:97], v4 offset:0\nds_read_b64 v[98:99], v4 offset:256\nds_read_b64 v[100:101], v4 offset:512\n" \
    "v_mfma_f32_32x32x16_bf16 v[48:63], a[196:199], v[68:71], v[48:63]\nds_read_b128 v[84:87], v1 offset:54272\nds_read_b64 v[102:103], v4 offset:768\n" \
    "v_mfma_f32_32x32x16_bf16 v[48:63], a[200:203], v[72:75], v[48:63]\nds_read_b128 v[88:91], v1 offset:55296\ns_waitcnt lgkmcnt(7)\n" \
    "v_mfma_f32_32x32x16_bf16 v[48:63], a[204:207], v[76:79], v[48:63]\nds_read_b128 v[92:95], v1 offset:56320\ns_waitcnt lgkmcnt(2)\nv_max3_u32 v116, v96, v98, v100\n" \
    "v_max_u32 v116, v116, v102\nv_cmp_eq_u32 s[70:71], -1, v116\nv_sub_f32 v114, v96, v98\nv_add_f32 v96, v96, v98\n" \
    "v_mfma_f32_32x32x16_bf16 v[48:63], a[208:211], v[80:83], v[48:63]\nds_read_b128 v[64:67], v1 offset:57344\nv_add_f32 v97, v97, v99\nv_mul_f32 v114, v114, v114\n" \
    "v_mul_f32 v96, 0.5, v96\nv_fmac_f32 v97, 0x43000000, v114\nv_sub_f32 v114, v100, v102\nv_mfma_f32_32x32x16_bf16 v[48:63], a[212:215], v[84:87], v[48:63]\n" \
    "ds_read_b128 v[68:71], v1 offset:58368\ns_waitcnt lgkmcnt(3)\nv_add_f32 v100, v100, v102\nv_add_f32 v101, v101, v103\nv_mul_f32 v114, v114, v114\n" \
    "v_mul_f32 v100, 0.5, v100\nv_fmac_f32 v101, 0x43000000, v114\nv_mfma_f32_32x32x16_bf16 v[48:63], a[216:219], v[88:91], v[48:63]\nds_read_b128 v[72:75], v1 offset:59392\n" \
    "s_waitcnt lgkmcnt(3)\nv_sub_f32 v114, v96, v100\nv_add_f32 v96, v96, v100\nv_add_f32 v97, v97, v101\nv_mul_f32 v114, v114, v114\nv_mul_f32 v96, 0.5, v96\n" \
    "v_mfma_f32_32x32x16_bf16 v[48:63], a[220:223], v[92:95], v[48:63]\nds_read_b128 v[76:79], v1 offset:60416\ns_waitcnt lgkmcnt(3)\nv_fmac_f32 v97, 0x43800000, v114\n" \
    "v_mul_f32 v98, 0x3a800000, v97\nv_cvt_pk_bf16_f32 v96, 0, v96\nv_cvt_pk_bf16_f32 v98, 0, v98\nv_add_f32 v98, 0x3727c5ac, v98\n" \
    "v_mfma_f32_32x32x16_bf16 v[48:63], a[224:227], v[64:67], v[48:63]\nds_read_b128 v[80:83], v1 offset:61440\ns_waitcnt lgkmcnt(3)\nv_cvt_pk_bf16_f32 v98, 0, v98\n" \
    "v_sqrt_f32 v98, v98\nv_mfma_f32_32x32x16_bf16 v[48:63], a[228:231], v[68:71], v[48:63]\nds_read_b128 v[84:87], v1 offset:62464\ns_waitcnt lgkmcnt(3)\n" \
    "v_cvt_pk_bf16_f32 v98, 0, v98\nv_rcp_f32 v98, v98\nv_mfma_f32_32x32x16_bf16 v[48:63], a[232:235], v[72:75], v[48:63]\nds_read_b128 v[88:91], v1 offset:63488\n" \
    "s_waitcnt vmcnt(0)\ns_waitcnt lgkmcnt(3)\nv_and_b32 v112, 127, v150\nv_cmp_gt_u32 vcc, s77, v112\nv_mfma_f32_32x32x16_bf16 v[48:63], a[236:239], v[76:79], v[48:63]\n" \
    "ds_read_b128 v[92:95], v1 offset:64512\ns_cmp_eq_u64 vcc, 0\ns_cselect_b32 s81, 1, 0\ns_waitcnt lgkmcnt(3)\n" \
    "v_mfma_f32_32x32x16_bf16 v[48:63], a[240:243], v[80:83], v[48:63]\ns_waitcnt lgkmcnt(2)\nv_mfma_f32_32x32x16_bf16 v[48:63], a[244:247], v[84:87], v[48:63]\n" \
    "s_waitcnt lgkmcnt(0)\ns_memrealtime s[72:73]\nv_mfma_f32_32x32x16_bf16 v[48:63], a[248:251], v[88:91], v[48:63]\n" \
    "v_mfma_f32_32x32x16_bf16 v[48:63], a[252:255], v[92:95], v[48:63]\ns_waitcnt lgkmcnt(0)\ns_cmp_ge_u32 s77, 64\ns_cselect_b32 s69, s60, s34\ns_sub_u32 s72, s72, s69\n" \
    "s_lshl_b32 s72, s72, 8\nv_sub_u32 v113, s72, v150\nv_cmp_gt_i32 s[74:75], 0, v113\nv_cmp_eq_u32 vcc, s77, v112\ns_nop 0\ns_and_b64 vcc, vcc, s[74:75]\n" \
    "s_cselect_b32 s69, 1, 0\ns_lshl_b32 s81, s81, s69\ns_cmp_lg_u64 s[70:71], 0\ns_cbranch_scc1 .Lf9_%=\nv_cmp_u_f32 vcc, v48, v48\ns_nop 0\ns_cbranch_vccnz .Lf7_%=\n.Lf8_%=:\n" \
    "s_cmp_eq_u32 s81, 1\ns_cbranch_scc1 .Lf15_%=\nv_pk_fma_f32 v[48:49], v[16:17], v[96:97], v[48:49] op_sel_hi:[1,0,1] neg_lo:[0,1,0] neg_hi:[0,1,0]\n" \
    "v_pk_fma_f32 v[50:51], v[18:19], v[96:97], v[50:51] op_sel_hi:[1,0,1] neg_lo:[0,1,0] neg_hi:[0,1,0]\n" \
    "v_pk_fma_f32 v[52:53], v[20:21], v[96:97], v[52:53] op_sel_hi:[1,0,1] neg_lo:[0,1,0] neg_hi:[0,1,0]\n" \
    "v_pk_fma_f32 v[54:55], v[22:23], v[96:97], v[54:55] op_sel_hi:[1,0,1] neg_lo:[0,1,0] neg_hi:[0,1,0]\nv_pk_fma_f32 v[48:49], v[48:49], v[98:99], v[32:33] op_sel_hi:[1,0,1]\n" \
    "v_pk_fma_f32 v[50:51], v[50:51], v[98:99], v[34:35] op_sel_hi:[1,0,1]\nv_pk_fma_f32 v[52:53], v[52:53], v[98:99], v[36:37] op_sel_hi:[1,0,1]\n" \
    "v_pk_fma_f32 v[54:55], v[54:55], v[98:99], v[38:39] op_sel_hi:[1,0,1]\nv_pk_mul_f32 v[72:73], v[48:49], v[48:49]\nv_pk_mul_f32 v[74:75], v[50:51], v[50:51]\n" \
    "v_pk_mul_f32 v[76:77], v[52:53], v[52:53]\nv_pk_mul_f32 v[78:79], v[54:55], v[54:55]\nv_pk_fma_f32 v[72:73], v[72:73], v[6:7], v[8:9]\n" \
    "v_pk_fma_f32 v[74:75], v[74:75], v[6:7], v[8:9]\nv_pk_fma_f32 v[76:77], v[76:77], v[6:7], v[8:9]\nv_pk_fma_f32 v[78:79], v[78:79], v[6:7], v[8:9]\n" \
    "v_pk_mul_f32 v[72:73], v[72:73], v[48:49]\nv_pk_mul_f32 v[74:75], v[74:75], v[50:51]\nv_pk_mul_f32 v[76:77], v[76:77], v[52:53]\nv_pk_mul_f32 v[78:79], v[78:79], v[54:55]\n" \
    "v_exp_f32 v72, v72\nv_exp_f32 v73, v73\nv_exp_f32 v74, v74\nv_exp_f32 v75, v75\nv_exp_f32 v76, v76\nv_exp_f32 v77, v77\nv_exp_f32 v78, v78\nv_exp_f32 v79, v79\n" \
    "v_pk_add_f32 v[72:73], v[72:73], v[10:11]\nv_pk_add_f32 v[74:75], v[74:75], v[10:11]\nv_pk_add_f32 v[76:77], v[76:77], v[10:11]\nv_pk_add_f32 v[78:79], v[78:79], v[10:11]\n" \
    "v_rcp_f32 v72, v72\nv_rcp_f32 v73, v73\nv_rcp_f32 v74, v74\nv_rcp_f32 v75, v75\nv_rcp_f32 v76, v76\nv_rcp_f32 v77, v77\nv_rcp_f32 v78, v78\nv_rcp_f32 v79, v79\n" \
    "v_pk_mul_f32 v[72:73], v[72:73], v[48:49]\nv_pk_mul_f32 v[74:75], v[74:75], v[50:51]\nv_pk_mul_f32 v[76:77], v[76:77], v[52:53]\nv_pk_mul_f32 v[78:79], v[78:79], v[54:55]\n" \
    "v_cvt_pk_bf16_f32 v64, v72, v73\nv_cvt_pk_bf16_f32 v65, v74, v75\nv_cvt_pk_bf16_f32 v66, v76, v77\nv_cvt_pk_bf16_f32 v67, v78, v79\nv_min_u32 v64, -2, v64\n" \
    "v_min_u32 v65, -2, v65\nv_min_u32 v66, -2, v66\nv_min_u32 v67, -2, v67\nbuffer_store_dwordx4 v[64:67], v0, s[64:67], s82 offen offset:0 sc1\n" \
    "v_pk_fma_f32 v[56:57], v[24:25], v[96:97], v[56:57] op_sel_hi:[1,0,1] neg_lo:[0,1,0] neg_hi:[0,1,0]\n" \
    "v_pk_fma_f32 v[58:59], v[26:27], v[96:97], v[58:59] op_sel_hi:[1,0,1] neg_lo:[0,1,0] neg_hi:[0,1,0]\n" \
    "v_pk_fma_f32 v[60:61], v[28:29], v[96:97], v[60:61] op_sel_hi:[1,0,1] neg_lo:[0,1,0] neg_hi:[0,1,0]\n" \
    "v_pk_fma_f32 v[62:63], v[30:31], v[96:97], v[62:63] op_sel_hi:[1,0,1] neg_lo:[0,1,0] neg_hi:[0,1,0]\nv_pk_fma_f32 v[56:57], v[56:57], v[98:99], v[40:41] op_sel_hi:[1,0,1]\n" \
    "v_pk_fma_f32 v[58:59], v[58:59], v[98:99], v[42:43] op_sel_hi:[1,0,1]\nv_pk_fma_f32 v[60:61], v[60:61], v[98:99], v[44:45] op_sel_hi:[1,0,1]\n" \
    "v_pk_fma_f32 v[62:63], v[62:63], v[98:99], v[46:47] op_sel_hi:[1,0,1]\nv_pk_mul_f32 v[72:73], v[56:57], v[56:57]\nv_pk_mul_f32 v[74:75], v[58:59], v[58:59]\n" \
    "v_pk_mul_f32 v[76:77], v[60:61], v[60:61]\nv_pk_mul_f32 v[78:79], v[62:63], v[62:63]\nv_pk_fma_f32 v[72:73], v[72:73], v[6:7], v[8:9]\n" \
    "v_pk_fma_f32 v[74:75], v[74:75], v[6:7], v[8:9]\nv_pk_fma_f32 v[76:77], v[76:77], v[6:7], v[8:9]\nv_pk_fma_f32 v[78:79], v[78:79], v[6:7], v[8:9]\n" \
    "v_pk_mul_f32 v[72:73], v[72:73], v[56:57]\nv_pk_mul_f32 v[74:75], v[74:75], v[58:59]\nv_pk_mul_f32 v[76:77], v[76:77], v[60:61]\nv_pk_mul_f32 v[78:79], v[78:79], v[62:63]\n" \
    "v_exp_f32 v72, v72\nv_exp_f32 v73, v73\nv_exp_f32 v74, v74\nv_exp_f32 v75, v75\nv_exp_f32 v76, v76\nv_exp_f32 v77, v77\nv_exp_f32 v78, v78\nv_exp_f32 v79, v79\n" \
    "v_pk_add_f32 v[72:73], v[72:73], v[10:11]\nv_pk_add_f32 v[74:75], v[74:75], v[10:11]\nv_pk_add_f32 v[76:77], v[76:77], v[10:11]\nv_pk_add_f32 v[78:79], v[78:79], v[10:11]\n" \
    "v_rcp_f32 v72, v72\nv_rcp_f32 v73, v73\nv_rcp_f32 v74, v74\nv_rcp_f32 v75, v75\nv_rcp_f32 v76, v76\nv_rcp_f32 v77, v77\nv_rcp_f32 v78, v78\nv_rcp_f32 v79, v79\n" \
    "v_pk_mul_f32 v[72:73], v[72:73], v[56:57]\nv_pk_mul_f32 v[74:75], v[74:75], v[58:59]\nv_pk_mul_f32 v[76:77], v[76:77], v[60:61]\nv_pk_mul_f32 v[78:79], v[78:79], v[62:63]\n" \
    "v_cvt_pk_bf16_f32 v68, v72, v73\nv_cvt_pk_bf16_f32 v69, v74, v75\nv_cvt_pk_bf16_f32 v70, v76, v77\nv_cvt_pk_bf16_f32 v71, v78, v79\nv_min_u32 v68, -2, v68\n" \
    "v_min_u32 v69, -2, v69\nv_min_u32 v70, -2, v70\nv_min_u32 v71, -2, v71\nbuffer_store_dwordx4 v[68:71], v0, s[64:67], s82 offen offset:1024 sc1\n.Lf16_%=:\n" \
    "s_mov_b32 s38, s85\ns_xor_b32 s80, s80, 0x10000\nv_xor_b32 v1, 0x10000, v1\ns_mov_b32 s68, 0\ns_add_u32 s32, s32, 1\ns_cmp_lt_u32 s32, 64\ns_cbranch_scc1 .Lf2_%=\n" \
    "s_endpgm\n.Lf4_%=:\ns_sleep 8\ns_add_u32 s68, s68, 1\ns_and_b32 s69, s68, 63\ns_cbranch_scc1 .Lf3_%=\ns_cmp_gt_u32 s68, 0x400000\ns_cbranch_scc1 .Lf1_%=\n" \
    "buffer_load_dword v253, off, s[64:67], 0 sc1\ns_waitcnt vmcnt(0)\nv_readfirstlane_b32 s69, v253\ns_nop 0\ns_cmp_eq_u32 s69, 1\ns_cbranch_scc0 .Lf3_%=\ns_endpgm\n.Lf13_%=:\n" \
    "s_waitcnt vmcnt(0)\ns_add_u32 s68, s68, 1\ns_and_b32 s69, s68, 63\ns_cbranch_scc1 .Lf12_%=\ns_cmp_gt_u32 s68, 0x400000\ns_cbranch_scc1 .Lf1_%=\n" \
    "buffer_load_dword v253, off, s[64:67], 0 sc1\ns_waitcnt vmcnt(0)\nv_readfirstlane_b32 s69, v253\ns_nop 0\ns_cmp_eq_u32 s69, 1\ns_cbranch_scc0 .Lf12_%=\ns_endpgm\n" \
    ".Lf15_%=:\ns_xor_b32 s84, s84, 0x10000\nbuffer_load_dword v151, v2, s[64:67], s83 offen sc1\n" \
    "v_pk_fma_f32 v[48:49], v[16:17], v[96:97], v[48:49] op_sel_hi:[1,0,1] neg_lo:[0,1,0] neg_hi:[0,1,0]\n" \
    "v_pk_fma_f32 v[50:51], v[18:19], v[96:97], v[50:51] op_sel_hi:[1,0,1] neg_lo:[0,1,0] neg_hi:[0,1,0]\n" \
    "v_pk_fma_f32 v[52:53], v[20:21], v[96:97], v[52:53] op_sel_hi:[1,0,1] neg_lo:[0,1,0] neg_hi:[0,1,0]\n" \
    "v_pk_fma_f32 v[54:55], v[22:23], v[96:97], v[54:55] op_sel_hi:[1,0,1] neg_lo:[0,1,0] neg_hi:[0,1,0]\nv_pk_fma_f32 v[48:49], v[48:49], v[98:99], v[32:33] op_sel_hi:[1,0,1]\n" \
    "v_pk_fma_f32 v[50:51], v[50:51], v[98:99], v[34:35] op_sel_hi:[1,0,1]\nv_pk_fma_f32 v[52:53], v[52:53], v[98:99], v[36:37] op_sel_hi:[1,0,1]\n" \
    "v_pk_fma_f32 v[54:55], v[54:55], v[98:99], v[38:39] op_sel_hi:[1,0,1]\nv_pk_mul_f32 v[72:73], v[48:49], v[48:49]\nv_pk_mul_f32 v[74:75], v[50:51], v[50:51]\n" \
    "v_pk_mul_f32 v[76:77], v[52:53], v[52:53]\nv_pk_mul_f32 v[78:79], v[54:55], v[54:55]\ns_add_u32 m0, s84, 0\ns_add_u32 s69, s83, s48\n" \
    "buffer_load_dwordx4 v0, s[64:67], s69 offen offset:0 sc1 lds\nv_pk_fma_f32 v[72:73], v[72:73], v[6:7], v[8:9]\nv_pk_fma_f32 v[74:75], v[74:75], v[6:7], v[8:9]\n" \
    "v_pk_fma_f32 v[76:77], v[76:77], v[6:7], v[8:9]\nv_pk_fma_f32 v[78:79], v[78:79], v[6:7], v[8:9]\nv_pk_mul_f32 v[72:73], v[72:73], v[48:49]\n" \
    "v_pk_mul_f32 v[74:75], v[74:75], v[50:51]\nv_pk_mul_f32 v[76:77], v[76:77], v[52:53]\nv_pk_mul_f32 v[78:79], v[78:79], v[54:55]\nv_exp_f32 v72, v72\nv_exp_f32 v73, v73\n" \
    "v_exp_f32 v74, v74\nv_exp_f32 v75, v75\nbuffer_load_dwordx4 v0, s[64:67], s69 offen offset:1024 sc1 lds\nv_exp_f32 v76, v76\nv_exp_f32 v77, v77\nv_exp_f32 v78, v78\n" \
    "v_exp_f32 v79, v79\nv_pk_add_f32 v[72:73], v[72:73], v[10:11]\nv_pk_add_f32 v[74:75], v[74:75], v[10:11]\nv_pk_add_f32 v[76:77], v[76:77], v[10:11]\n" \
    "v_pk_add_f32 v[78:79], v[78:79], v[10:11]\nv_rcp_f32 v72, v72\nv_rcp_f32 v73, v73\nv_rcp_f32 v74, v74\nv_rcp_f32 v75, v75\ns_add_u32 m0, s84, 8192\n" \
    "s_add_u32 s69, s83, s49\nbuffer_load_dwordx4 v0, s[64:67], s69 offen offset:0 sc1 lds\nv_rcp_f32 v76, v76\nv_rcp_f32 v77, v77\nv_rcp_f32 v78, v78\nv_rcp_f32 v79, v79\n" \
    "v_pk_mul_f32 v[72:73], v[72:73], v[48:49]\nv_pk_mul_f32 v[74:75], v[74:75], v[50:51]\nv_pk_mul_f32 v[76:77], v[76:77], v[52:53]\nv_pk_mul_f32 v[78:79], v[78:79], v[54:55]\n" \
    "v_cvt_pk_bf16_f32 v64, v72, v73\nv_cvt_pk_bf16_f32 v65, v74, v75\nv_cvt_pk_bf16_f32 v66, v76, v77\nv_cvt_pk_bf16_f32 v67, v78, v79\n" \
    "buffer_load_dwordx4 v0, s[64:67], s69 offen offset:1024 sc1 lds\nv_min_u32 v64, -2, v64\nv_min_u32 v65, -2, v65\nv_min_u32 v66, -2, v66\nv_min_u32 v67, -2, v67\n" \
    "buffer_store_dwordx4 v[64:67], v0, s[64:67], s82 offen offset:0 sc1\nv_pk_fma_f32 v[56:57], v[24:25], v[96:97], v[56:57] op_sel_hi:[1,0,1] neg_lo:[0,1,0] neg_hi:[0,1,0]\n" \
    "v_pk_fma_f32 v[58:59], v[26:27], v[96:97], v[58:59] op_sel_hi:[1,0,1] neg_lo:[0,1,0] neg_hi:[0,1,0]\n" \
    "v_pk_fma_f32 v[60:61], v[28:29], v[96:97], v[60:61] op_sel_hi:[1,0,1] neg_lo:[0,1,0] neg_hi:[0,1,0]\n" \
    "v_pk_fma_f32 v[62:63], v[30:31], v[96:97], v[62:63] op_sel_hi:[1,0,1] neg_lo:[0,1,0] neg_hi:[0,1,0]\nv_pk_fma_f32 v[56:57], v[56:57], v[98:99], v[40:41] op_sel_hi:[1,0,1]\n" \
    "v_pk_fma_f32 v[58:59], v[58:59], v[98:99], v[42:43] op_sel_hi:[1,0,1]\nv_pk_fma_f32 v[60:61], v[60:61], v[98:99], v[44:45] op_sel_hi:[1,0,1]\ns_add_u32 m0, s84, 16384\n" \
    "s_add_u32 s69, s83, s50\nbuffer_load_dwordx4 v0, s[64:67], s69 offen offset:0 sc1 lds\nv_pk_fma_f32 v[62:63], v[62:63], v[98:99], v[46:47] op_sel_hi:[1,0,1]\n" \
    "v_pk_mul_f32 v[72:73], v[56:57], v[56:57]\nv_pk_mul_f32 v[74:75], v[58:59], v[58:59]\nv_pk_mul_f32 v[76:77], v[60:61], v[60:61]\nv_pk_mul_f32 v[78:79], v[62:63], v[62:63]\n" \
    "v_pk_fma_f32 v[72:73], v[72:73], v[6:7], v[8:9]\nv_pk_fma_f32 v[74:75], v[74:75], v[6:7], v[8:9]\nv_pk_fma_f32 v[76:77], v[76:77], v[6:7], v[8:9]\n" \
    "v_pk_fma_f32 v[78:79], v[78:79], v[6:7], v[8:9]\nv_pk_mul_f32 v[72:73], v[72:73], v[56:57]\nv_pk_mul_f32 v[74:75], v[74:75], v[58:59]\n" \
    "v_pk_mul_f32 v[76:77], v[76:77], v[60:61]\nbuffer_load_dwordx4 v0, s[64:67], s69 offen offset:1024 sc1 lds\nv_pk_mul_f32 v[78:79], v[78:79], v[62:63]\nv_exp_f32 v72, v72\n" \
    "v_exp_f32 v73, v73\nv_exp_f32 v74, v74\nv_exp_f32 v75, v75\nv_exp_f32 v76, v76\nv_exp_f32 v77, v77\nv_exp_f32 v78, v78\nv_exp_f32 v79, v79\n" \
    "v_pk_add_f32 v[72:73], v[72:73], v[10:11]\nv_pk_add_f32 v[74:75], v[74:75], v[10:11]\nv_pk_add_f32 v[76:77], v[76:77], v[10:11]\nv_pk_add_f32 v[78:79], v[78:79], v[10:11]\n" \
    "v_rcp_f32 v72, v72\nv_rcp_f32 v73, v73\nv_rcp_f32 v74, v74\nv_rcp_f32 v75, v75\nv_rcp_f32 v76, v76\nv_rcp_f32 v77, v77\nv_rcp_f32 v78, v78\nv_rcp_f32 v79, v79\n" \
    "v_pk_mul_f32 v[72:73], v[72:73], v[56:57]\nv_pk_mul_f32 v[74:75], v[74:75], v[58:59]\nv_pk_mul_f32 v[76:77], v[76:77], v[60:61]\nv_pk_mul_f32 v[78:79], v[78:79], v[62:63]\n" \
    "v_cvt_pk_bf16_f32 v68, v72, v73\nv_cvt_pk_bf16_f32 v69, v74, v75\nv_cvt_pk_bf16_f32 v70, v76, v77\nv_cvt_pk_bf16_f32 v71, v78, v79\nv_min_u32 v68, -2, v68\n" \
    "v_min_u32 v69, -2, v69\nv_min_u32 v70, -2, v70\nv_min_u32 v71, -2, v71\nbuffer_store_dwordx4 v[68:71], v0, s[64:67], s82 offen offset:1024 sc1\ns_branch .Lf16_%=\n" \
    ".Lf7_%=:\nv_mov_b32 v120, 0\nv_mov_b32 v121, v1\ns_mov_b32 s76, 16\n.Lf10_%=:\nds_read_b128 v[124:127], v121 offset:0\nds_read_b128 v[128:131], v121 offset:1024\n" \
    "ds_read_b128 v[132:135], v121 offset:2048\nds_read_b128 v[136:139], v121 offset:3072\nv_add_u32 v121, 0x1000, v121\ns_waitcnt lgkmcnt(0)\n" \
    "v_max3_u32 v120, v120, v124, v125\nv_max3_u32 v120, v120, v126, v127\nv_max3_u32 v120, v120, v128, v129\nv_max3_u32 v120, v120, v130, v131\n" \
    "v_max3_u32 v120, v120, v132, v133\nv_max3_u32 v120, v120, v134, v135\nv_max3_u32 v120, v120, v136, v137\nv_max3_u32 v120, v120, v138, v139\ns_sub_u32 s76, s76, 1\n" \
    "s_cmp_lg_u32 s76, 0\ns_cbranch_scc1 .Lf10_%=\nv_cmp_eq_u32 vcc, -1, v120\ns_nop 0\ns_cbranch_vccz .Lf8_%=\n.Lf9_%=:\ns_mov_b32 s81, 0\ns_barrier\ns_sleep 4\n" \
    "s_add_u32 s68, s68, 1\ns_and_b32 s69, s68, 63\ns_cbranch_scc1 .Lf2_%=\ns_cmp_gt_u32 s68, 0x400000\ns_cbranch_scc1 .Lf1_%=\nbuffer_load_dword v253, off, s[64:67], 0 sc1\n" \
    "s_waitcnt vmcnt(0)\nv_readfirstlane_b32 s69, v253\ns_nop 0\ns_cmp_eq_u32 s69, 1\ns_cbranch_scc0 .Lf2_%=\ns_endpgm\n.Lf1_%=:\nv_mov_b32 v253, 1\n" \
    "buffer_store_dword v253, off, s[64:67], 0 sc1\ns_endpgm\n"
#define ASM_F2 \
    "s_mov_b32 s90, 0x7fff0000\ns_mov_b32 s91, 0x7fff0000\ns_mov_b32 s92, 0x7fff0000\ns_mov_b32 s93, 0x7fff0000\ns_mov_b32 s80, 0\ns_mov_b32 s81, 0\ns_mov_b32 s68, 0\n.Lg2_%=:\n" \
    "s_xor_b32 s85, s38, 512\ns_and_b32 s69, s32, 63\ns_lshl_b32 s69, s69, 19\ns_add_u32 s82, s36, s69\ns_lshr_b32 s69, s82, 8\ns_and_b32 s69, s69, 0x2000\n" \
    "s_xor_b32 s82, s82, s69\ns_add_u32 s69, s32, 1\ns_and_b32 s69, s69, 63\ns_lshl_b32 s69, s69, 19\ns_add_u32 s83, s36, s69\ns_lshr_b32 s69, s83, 8\n" \
    "s_and_b32 s69, s69, 0x2000\ns_xor_b32 s83, s83, s69\ns_add_u32 s84, s37, s80\ns_cmp_eq_u32 s81, 1\ns_cbranch_scc1 .Lg6_%=\ns_add_u32 s77, s32, 1\ns_cmp_eq_u32 s81, 2\n" \
    "s_cbranch_scc1 .Lg5_%=\n.Lg3_%=:\nbuffer_load_dword v150, v3, s[64:67], 0 offen sc1\ns_waitcnt vmcnt(0)\nv_and_b32 v112, 127, v150\nv_cmp_gt_u32 vcc, s77, v112\ns_nop 0\n" \
    "s_cbranch_vccnz .Lg4_%=\n.Lg5_%=:\nv_and_b32 v112, 127, v150\n.Lg11_%=:\ns_sleep 1\ns_memrealtime s[72:73]\ns_waitcnt lgkmcnt(0)\ns_cmp_ge_u32 s77, 64\n" \
    "s_cselect_b32 s69, s60, s34\ns_sub_u32 s72, s72, s69\ns_lshl_b32 s72, s72, 8\nv_sub_u32 v113, s72, v150\nv_cmp_gt_i32 s[74:75], 0, v113\nv_cmp_eq_u32 vcc, s77, v112\n" \
    "s_nop 0\ns_and_b64 vcc, vcc, s[74:75]\ns_cbranch_scc1 .Lg11_%=\n.Lg12_%=:\nbuffer_load_dword v151, v2, s[64:67], s82 offen sc1\ns_add_u32 m0, s84, 0\n" \
    "s_add_u32 s69, s82, s48\nbuffer_load_dwordx4 v0, s[64:67], s69 offen offset:0 sc1 lds\nbuffer_load_dwordx4 v0, s[64:67], s69 offen offset:1024 sc1 lds\n" \
    "s_add_u32 m0, s84, 8192\ns_add_u32 s69, s82, s49\nbuffer_load_dwordx4 v0, s[64:67], s69 offen offset:0 sc1 lds\n" \
    "buffer_load_dwordx4 v0, s[64:67], s69 offen offset:1024 sc1 lds\ns_add_u32 m0, s84, 16384\ns_add_u32 s69, s82, s50\n" \
    "buffer_load_dwordx4 v0, s[64:67], s69 offen offset:0 sc1 lds\nbuffer_load_dwordx4 v0, s[64:67], s69 offen offset:1024 sc1 lds\n.Lg6_%=:\ns_waitcnt vmcnt(6)\n" \
    "v_cmp_eq_u32 vcc, -1, v151\ns_nop 0\ns_cbranch_vccnz .Lg13_%=\ns_add_u32 s77, s32, 2\ns_add_u32 m0, s84, 24576\ns_add_u32 s69, s82, s51\n" \
    "buffer_load_dwordx4 v0, s[64:67], s69 offen offset:0 sc1 lds\nbuffer_load_dwordx4 v0, s[64:67], s69 offen offset:1024 sc1 lds\ns_waitcnt vmcnt(6)\ns_barrier\n" \
    "ds_read_b128 v[64:67], v1 offset:0\nds_read_b128 v[68:71], v1 offset:1024\nds_read_b128 v[72:75], v1 offset:2048\nds_read_b128 v[76:79], v1 offset:3072\n" \
    "s_waitcnt lgkmcnt(3)\nv_mfma_f32_32x32x16_bf16 v[48:63], a[0:3], v[64:67], 0\nds_read_b128 v[80:83], v1 offset:4096\ns_add_u32 m0, s84, 32768\ns_add_u32 s69, s82, s52\n" \
    "buffer_load_dwordx4 v0, s[64:67], s69 offen offset:0 sc1 lds\ns_waitcnt lgkmcnt(3)\nv_mfma_f32_32x32x16_bf16 v[48:63], a[4:7], v[68:71], v[48:63]\n" \
    "ds_read_b128 v[84:87], v1 offset:5120\ns_waitcnt lgkmcnt(3)\nv_mfma_f32_32x32x16_bf16 v[48:63], a[8:11], v[72:75], v[48:63]\nds_read_b128 v[88:91], v1 offset:6144\n" \
    "buffer_load_dwordx4 v0, s[64:67], s69 offen offset:1024 sc1 lds\ns_waitcnt lgkmcnt(3)\nv_mfma_f32_32x32x16_bf16 v[48:63], a[12:15], v[76:79], v[48:63]\n" \
    "ds_read_b128 v[92:95], v1 offset:7168\ns_waitcnt vmcnt(6) lgkmcnt(3)\ns_barrier\nv_mfma_f32_32x32x16_bf16 v[48:63], a[16:19], v[80:83], v[48:63]\n" \
    "ds_read_b128 v[64:67], v1 offset:8192\ns_add_u32 m0, s84, 40960\ns_add_u32 s69, s82, s53\nbuffer_load_dwordx4 v0, s[64:67], s69 offen offset:0 sc1 lds\n" \
    "s_waitcnt lgkmcnt(3)\nv_mfma_f32_32x32x16_bf16 v[48:63], a[20:23], v[84:87], v[48:63]\nds_read_b128 v[68:71], v1 offset:9216\ns_waitcnt lgkmcnt(3)\n" \
    "v_mfma_f32_32x32x16_bf16 v[48:63], a[24:27], v[88:91], v[48:63]\nds_read_b128 v[72:75], v1 offset:10240\nbuffer_load_dwordx4 v0, s[64:67], s69 offen offset:1024 sc1 lds\n" \
    "s_waitcnt lgkmcnt(3)\nv_mfma_f32_32x32x16_bf16 v[48:63], a[28:31], v[92:95], v[48:63]\nds_read_b128 v[76:79], v1 offset:11264\ns_waitcnt lgkmcnt(3)\n" \
    "v_mfma_f32_32x32x16_bf16 v[48:63], a[32:35], v[64:67], v[48:63]\nds_read_b128 v[80:83], v1 offset:12288\ns_add_u32 m0, s84, 49152\ns_add_u32 s69, s82, s54\n" \
    "buffer_load_dwordx4 v0, s[64:67], s69 offen offset:0 sc1 lds\ns_waitcnt lgkmcnt(3)\nv_mfma_f32_32x32x16_bf16 v[48:63], a[36:39], v[68:71], v[48:63]\n" \
    "ds_read_b128 v[84:87], v1 offset:13312\ns_waitcnt lgkmcnt(3)\nv_mfma_f32_32x32x16_bf16 v[48:63], a[40:43], v[72:75], v[48:63]\nds_read_b128 v[88:91], v1 offset:14336\n" \
    "buffer_load_dwordx4 v0, s[64:67], s69 offen offset:1024 sc1 lds\ns_waitcnt lgkmcnt(3)\nv_mfma_f32_32x32x16_bf16 v[48:63], a[44:47], v[76:79], v[48:63]\n" \
    "ds_read_b128 v[92:95], v1 offset:15360\ns_waitcnt vmcnt(6) lgkmcnt(3)\ns_barrier\nv_mfma_f32_32x32x16_bf16 v[48:63], a[48:51], v[80:83], v[48:63]\n" \
    "ds_read_b128 v[64:67], v1 offset:16384\ns_and_b32 s69, s32, 3\ns_lshl_b32 s69, s69, 19\ns_add_u32 s86, s56, s69\ns_lshr_b32 s69, s86, 8\ns_and_b32 s69, s69, 0x2000\n" \
    "s_waitcnt lgkmcnt(3)\nv_mfma_f32_32x32x16_bf16 v[48:63], a[52:55], v[84:87], v[48:63]\nds_read_b128 v[68:71], v1 offset:17408\ns_xor_b32 s86, s86, s69\n" \
    "s_and_b32 s69, s32, 3\ns_lshl_b32 s69, s69, 19\ns_add_u32 s87, s57, s69\ns_lshr_b32 s69, s87, 8\ns_waitcnt lgkmcnt(3)\n" \
    "v_mfma_f32_32x32x16_bf16 v[48:63], a[56:59], v[88:91], v[48:63]\nds_read_b128 v[72:75], v1 offset:18432\ns_and_b32 s69, s69, 0x2000\ns_xor_b32 s87, s87, s69\n" \
    "s_and_b32 s69, s32, 3\ns_lshl_b32 s69, s69, 19\ns_add_u32 s88, s58, s69\ns_waitcnt lgkmcnt(3)\nv_mfma_f32_32x32x16_bf16 v[48:63], a[60:63], v[92:95], v[48:63]\n" \
    "ds_read_b128 v[76:79], v1 offset:19456\ns_lshr_b32 s69, s88, 8\ns_and_b32 s69, s69, 0x2000\ns_xor_b32 s88, s88, s69\ns_and_b32 s69, s32, 3\ns_lshl_b32 s69, s69, 19\n" \
    "s_waitcnt lgkmcnt(3)\nv_mfma_f32_32x32x16_bf16 v[48:63], a[64:67], v[64:67], v[48:63]\nds_read_b128 v[80:83], v1 offset:20480\ns_add_u32 s89, s59, s69\n" \
    "s_lshr_b32 s69, s89, 8\ns_and_b32 s69, s69, 0x2000\ns_xor_b32 s89, s89, s69\ns_bfe_u32 s78, s32, 0x10002\ns_waitcnt lgkmcnt(3)\n" \
    "v_mfma_f32_32x32x16_bf16 v[48:63], a[68:71], v[68:71], v[48:63]\nds_read_b128 v[84:87], v1 offset:21504\ns_waitcnt lgkmcnt(3)\n" \
    "v_mfma_f32_32x32x16_bf16 v[48:63], a[72:75], v[72:75], v[48:63]\nds_read_b128 v[88:91], v1 offset:22528\nbuffer_load_dwordx2 v[108:109], v4, s[64:67], s93 offen sc1\n" \
    "s_waitcnt lgkmcnt(3)\nv_mfma_f32_32x32x16_bf16 v[48:63], a[76:79], v[76:79], v[48:63]\nds_read_b128 v[92:95], v1 offset:23552\n" \
    "buffer_load_dwordx4 v[96:99], v0, s[64:67], s90 offen sc1\ns_waitcnt lgkmcnt(3)\nv_mfma_f32_32x32x16_bf16 v[48:63], a[80:83], v[80:83], v[48:63]\n" \
    "ds_read_b128 v[64:67], v1 offset:24576\nbuffer_load_dwordx4 v[100:103], v0, s[64:67], s91 offen sc1\ns_waitcnt lgkmcnt(3)\n" \
    "v_mfma_f32_32x32x16_bf16 v[48:63], a[84:87], v[84:87], v[48:63]\nds_read_b128 v[68:71], v1 offset:25600\nbuffer_load_dwordx4 v[104:107], v0, s[64:67], s92 offen sc1\n" \
    "s_waitcnt lgkmcnt(3)\nv_mfma_f32_32x32x16_bf16 v[48:63], a[88:91], v[88:91], v[48:63]\nds_read_b128 v[72:75], v1 offset:26624\ns_waitcnt lgkmcnt(3)\n" \
    "v_mfma_f32_32x32x16_bf16 v[48:63], a[92:95], v[92:95], v[48:63]\nds_read_b128 v[76:79], v1 offset:27648\ns_waitcnt lgkmcnt(3)\n" \
    "v_mfma_f32_32x32x16_bf16 v[48:63], a[96:99], v[64:67], v[48:63]\nds_read_b128 v[80:83], v1 offset:28672\ns_waitcnt lgkmcnt(3)\n" \
    "v_mfma_f32_32x32x16_bf16 v[48:63], a[100:103], v[68:71], v[48:63]\nds_read_b128 v[84:87], v1 offset:29696\ns_waitcnt lgkmcnt(3)\n" \
    "v_mfma_f32_32x32x16_bf16 v[48:63], a[104:107], v[72:75], v[48:63]\nds_read_b128 v[88:91], v1 offset:30720\ns_waitcnt lgkmcnt(3)\n" \
    "v_mfma_f32_32x32x16_bf16 v[48:63], a[108:111], v[76:79], v[48:63]\nds_read_b128 v[92:95], v1 offset:31744\ns_waitcnt vmcnt(6) lgkmcnt(3)\ns_barrier\n" \
    "v_mfma_f32_32x32x16_bf16 v[48:63], a[112:115], v[80:83], v[48:63]\nds_read_b128 v[64:67], v1 offset:32768\ns_waitcnt lgkmcnt(3)\n" \
    "v_mfma_f32_32x32x16_bf16 v[48:63], a[116:119], v[84:87], v[48:63]\nds_read_b128 v[68:71], v1 offset:33792\ns_waitcnt lgkmcnt(3)\n" \
    "v_mfma_f32_32x32x16_bf16 v[48:63], a[120:123], v[88:91], v[48:63]\nds_read_b128 v[72:75], v1 offset:34816\ns_waitcnt lgkmcnt(3)\n" \
    "v_mfma_f32_32x32x16_bf16 v[48:63], a[124:127], v[92:95], v[48:63]\nds_read_b128 v[76:79], v1 offset:35840\ns_waitcnt lgkmcnt(3)\n" \
    "v_mfma_f32_32x32x16_bf16 v[48:63], a[128:131], v[64:67], v[48:63]\nds_read_b128 v[80:83], v1 offset:36864\nbuffer_load_dword v150, v3, s[64:67], 0 offen sc1\n" \
    "s_waitcnt lgkmcnt(3)\nv_mfma_f32_32x32x16_bf16 v[48:63], a[132:135], v[68:71], v[48:63]\nds_read_b128 v[84:87], v1 offset:37888\ns_waitcnt lgkmcnt(3)\n" \
    "v_mfma_f32_32x32x16_bf16 v[48:63], a[136:139], v[72:75], v[48:63]\nds_read_b128 v[88:91], v1 offset:38912\ns_waitcnt lgkmcnt(3)\n" \
    "v_mfma_f32_32x32x16_bf16 v[48:63], a[140:143], v[76:79], v[48:63]\nds_read_b128 v[92:95], v1 offset:39936\ns_waitcnt lgkmcnt(3)\n" \
    "v_mfma_f32_32x32x16_bf16 v[48:63], a[144:147], v[80:83], v[48:63]\nds_read_b128 v[64:67], v1 offset:40960\ns_waitcnt lgkmcnt(3)\n" \
    "v_mfma_f32_32x32x16_bf16 v[48:63], a[148:151], v[84:87], v[48:63]\nds_read_b128 v[68:71], v1 offset:41984\ns_waitcnt vmcnt(1)\ns_waitcnt lgkmcnt(3)\n" \
    "v_max_u32 v116, v108, v109\nv_cmp_eq_u32 s[70:71], -1, v116\nv_mfma_f32_32x32x16_bf16 v[48:63], a[152:155], v[88:91], v[48:63]\nds_read_b128 v[72:75], v1 offset:43008\n" \
    "s_waitcnt lgkmcnt(3)\nv_cndmask_b32 v116, 0, 1, s[70:71]\nv_xor_b32 v96, s79, v96\nv_xor_b32 v97, s79, v97\nv_xor_b32 v98, s79, v98\nv_xor_b32 v99, s79, v99\n" \
    "v_mfma_f32_32x32x16_bf16 v[48:63], a[156:159], v[92:95], v[48:63]\nds_read_b128 v[76:79], v1 offset:44032\ns_waitcnt lgkmcnt(3)\nv_xor_b32 v100, s79, v100\n" \
    "v_xor_b32 v101, s79, v101\nv_xor_b32 v102, s79, v102\nv_xor_b32 v103, s79, v103\nv_xor_b32 v104, s79, v104\n" \
    "v_mfma_f32_32x32x16_bf16 v[48:63], a[160:163], v[64:67], v[48:63]\nds_read_b128 v[80:83], v1 offset:45056\ns_waitcnt lgkmcnt(3)\nv_xor_b32 v105, s79, v105\n" \
    "v_xor_b32 v106, s79, v106\nv_xor_b32 v107, s79, v107\nv_or3_b32 v116, v116, v96, v97\nv_or3_b32 v116, v116, v98, v99\n" \
    "v_mfma_f32_32x32x16_bf16 v[48:63], a[164:167], v[68:71], v[48:63]\nds_read_b128 v[84:87], v1 offset:46080\ns_waitcnt lgkmcnt(3)\nv_or3_b32 v116, v116, v100, v101\n" \
    "v_or3_b32 v116, v116, v102, v103\nv_or3_b32 v116, v116, v104, v105\nv_or3_b32 v116, v116, v106, v107\nv_and_b32 v116, 1, v116\n" \
    "v_mfma_f32_32x32x16_bf16 v[48:63], a[168:171], v[72:75], v[48:63]\nds_read_b128 v[88:91], v1 offset:47104\ns_waitcnt lgkmcnt(3)\nv_cmp_eq_u32 s[70:71], 1, v116\n" \
    "v_add_f32 v96, v96, v16\nv_add_f32 v97, v97, v17\nv_add_f32 v98, v98, v18\nv_add_f32 v99, v99, v19\nv_mfma_f32_32x32x16_bf16 v[48:63], a[172:175], v[76:79], v[48:63]\n" \
    "ds_read_b128 v[92:95], v1 offset:48128\ns_waitcnt lgkmcnt(3)\ns_barrier\nv_add_f32 v100, v100, v104\nv_add_f32 v101, v101, v105\nv_add_f32 v102, v102, v106\n" \
    "v_add_f32 v103, v103, v107\nv_add_f32 v96, v96, v100\nv_mfma_f32_32x32x16_bf16 v[48:63], a[176:179], v[80:83], v[48:63]\nds_read_b128 v[64:67], v1 offset:49152\n" \
    "s_waitcnt lgkmcnt(3)\nv_add_f32 v97, v97, v101\nv_add_f32 v98, v98, v102\nv_add_f32 v99, v99, v103\nv_cvt_pk_bf16_f32 v96, 0, v96\nv_cvt_pk_bf16_f32 v97, 0, v97\n" \
    "v_mfma_f32_32x32x16_bf16 v[48:63], a[180:183], v[84:87], v[48:63]\nds_read_b128 v[68:71], v1 offset:50176\ns_waitcnt lgkmcnt(3)\nv_cvt_pk_bf16_f32 v98, 0, v98\n" \
    "v_cvt_pk_bf16_f32 v99, 0, v99\nv_add_f32 v96, v96, v8\nv_add_f32 v97, v97, v9\nv_add_f32 v98, v98, v10\nv_mfma_f32_32x32x16_bf16 v[48:63], a[184:187], v[88:91], v[48:63]\n" \
    "ds_read_b128 v[72:75], v1 offset:51200\ns_waitcnt lgkmcnt(3)\nv_add_f32 v99, v99, v11\nv_cvt_pk_bf16_f32 v96, 0, v96\nv_cvt_pk_bf16_f32 v97, 0, v97\n" \
    "v_cvt_pk_bf16_f32 v98, 0, v98\nv_cvt_pk_bf16_f32 v99, 0, v99\nv_mfma_f32_32x32x16_bf16 v[48:63], a[188:191], v[92:95], v[48:63]\nds_read_b128 v[76:79], v1 offset:52224\n" \
    "s_waitcnt lgkmcnt(3)\nv_mul_f32 v96, v96, v12\nv_mul_f32 v97, v97, v13\nv_mul_f32 v98, v98, v14\nv_mul_f32 v99, v99, v15\nv_cvt_pk_bf16_f32 v96, 0, v96\n" \
    "v_mfma_f32_32x32x16_bf16 v[48:63], a[192:195], v[64:67], v[48:63]\nds_read_b128 v[80:83], v1 offset:53248\ns_waitcnt lgkmcnt(3)\nv_cvt_pk_bf16_f32 v97, 0, v97\n" \
    "v_cvt_pk_bf16_f32 v98, 0, v98\nv_cvt_pk_bf16_f32 v99, 0, v99\nv_lshlrev_b32 v100, 16, v108\nv_and_b32 v101, 0xffff0000, v108\n" \
    "v_mfma_f32_32x32x16_bf16 v[48:63], a[196:199], v[68:71], v[48:63]\nds_read_b128 v[84:87], v1 offset:54272\ns_waitcnt lgkmcnt(3)\nv_lshlrev_b32 v102, 16, v109\n" \
    "v_and_b32 v103, 0xffff0000, v109\nv_add_f32 v96, v96, v100\nv_add_f32 v97, v97, v101\nv_add_f32 v98, v98, v102\n" \
    "v_mfma_f32_32x32x16_bf16 v[48:63], a[200:203], v[72:75], v[48:63]\nds_read_b128 v[88:91], v1 offset:55296\ns_waitcnt vmcnt(0)\ns_waitcnt lgkmcnt(3)\n" \
    "v_and_b32 v112, 127, v150\nv_cmp_gt_u32 vcc, s77, v112\nv_add_f32 v99, v99, v103\nv_cvt_pk_bf16_f32 v116, v96, v97\nv_cvt_pk_bf16_f32 v117, v98, v99\n" \
    "v_mfma_f32_32x32x16_bf16 v[48:63], a[204:207], v[76:79], v[48:63]\nds_read_b128 v[92:95], v1 offset:56320\ns_cmp_eq_u64 vcc, 0\ns_cselect_b32 s81, 1, 0\n" \
    "s_waitcnt lgkmcnt(3)\nv_mfma_f32_32x32x16_bf16 v[48:63], a[208:211], v[80:83], v[48:63]\ns_waitcnt lgkmcnt(2)\n" \
    "v_mfma_f32_32x32x16_bf16 v[48:63], a[212:215], v[84:87], v[48:63]\ns_waitcnt lgkmcnt(0)\ns_memrealtime s[72:73]\n" \
    "v_mfma_f32_32x32x16_bf16 v[48:63], a[216:219], v[88:91], v[48:63]\nv_mfma_f32_32x32x16_bf16 v[48:63], a[220:223], v[92:95], v[48:63]\ns_waitcnt lgkmcnt(0)\n" \
    "s_cmp_ge_u32 s77, 64\ns_cselect_b32 s69, s60, s34\ns_sub_u32 s72, s72, s69\ns_lshl_b32 s72, s72, 8\nv_sub_u32 v113, s72, v150\nv_cmp_gt_i32 s[74:75], 0, v113\n" \
    "v_cmp_eq_u32 vcc, s77, v112\ns_nop 0\ns_and_b64 vcc, vcc, s[74:75]\ns_cselect_b32 s69, 1, 0\ns_lshl_b32 s81, s81, s69\ns_nop 7\ns_cmp_eq_u32 s90, 0x7fff0000\n" \
    "s_cbranch_scc1 .Lg15_%=\ns_cmp_lg_u64 s[70:71], 0\ns_cbranch_scc1 .Lg16_%=\n.Lg17_%=:\nglobal_store_dwordx2 v5, v[116:117], s[46:47]\nv_add_u32 v5, 0x10000, v5\n" \
    "s_mov_b32 s90, 0x7fff0000\ns_mov_b32 s91, 0x7fff0000\ns_mov_b32 s92, 0x7fff0000\ns_mov_b32 s93, 0x7fff0000\n.Lg15_%=:\ns_nop 1\nv_cmp_u_f32 vcc, v48, v48\ns_nop 0\n" \
    "s_cbranch_vccnz .Lg7_%=\n.Lg8_%=:\nv_and_or_b32 v52, v52, -2, s78\nv_and_or_b32 v53, v53, -2, s78\nv_and_or_b32 v54, v54, -2, s78\nv_and_or_b32 v55, v55, -2, s78\n" \
    "buffer_store_dwordx4 v[52:55], v0, s[64:67], s87 offen sc1\nv_and_or_b32 v56, v56, -2, s78\nv_and_or_b32 v57, v57, -2, s78\nv_and_or_b32 v58, v58, -2, s78\n" \
    "v_and_or_b32 v59, v59, -2, s78\nbuffer_store_dwordx4 v[56:59], v0, s[64:67], s88 offen sc1\nv_and_or_b32 v60, v60, -2, s78\nv_and_or_b32 v61, v61, -2, s78\n" \
    "v_and_or_b32 v62, v62, -2, s78\nv_and_or_b32 v63, v63, -2, s78\nbuffer_store_dwordx4 v[60:63], v0, s[64:67], s89 offen sc1\ns_cmp_lg_u32 s81, 1\ns_cbranch_scc1 .Lg20_%=\n" \
    "s_xor_b32 s84, s84, 0x10000\nbuffer_load_dword v151, v2, s[64:67], s83 offen sc1\ns_add_u32 m0, s84, 0\ns_add_u32 s69, s83, s48\n" \
    "buffer_load_dwordx4 v0, s[64:67], s69 offen offset:0 sc1 lds\nbuffer_load_dwordx4 v0, s[64:67], s69 offen offset:1024 sc1 lds\ns_add_u32 m0, s84, 8192\n" \
    "s_add_u32 s69, s83, s49\nbuffer_load_dwordx4 v0, s[64:67], s69 offen offset:0 sc1 lds\nbuffer_load_dwordx4 v0, s[64:67], s69 offen offset:1024 sc1 lds\n" \
    "s_add_u32 m0, s84, 16384\ns_add_u32 s69, s83, s50\nbuffer_load_dwordx4 v0, s[64:67], s69 offen offset:0 sc1 lds\n" \
    "buffer_load_dwordx4 v0, s[64:67], s69 offen offset:1024 sc1 lds\n.Lg20_%=:\nv_mov_b64 v[16:17], v[48:49]\nv_mov_b64 v[18:19], v[50:51]\ns_add_u32 s90, s86, 0x1000\n" \
    "s_add_u32 s91, s86, 0x4000\ns_add_u32 s92, s86, 0x5000\ns_mov_b32 s93, s82\ns_mov_b32 s79, s78\ns_cmp_lt_u32 s32, 63\ns_cbranch_scc1 .Lg14_%=\ns_memrealtime s[72:73]\n" \
    "s_waitcnt lgkmcnt(0)\ns_add_u32 s76, s72, s35\n.Lg21_%=:\ns_memrealtime s[72:73]\ns_waitcnt lgkmcnt(0)\ns_sub_u32 s72, s72, s76\ns_cmp_lt_i32 s72, 0\n" \
    "s_cbranch_scc1 .Lg21_%=\n.Lg18_%=:\nbuffer_load_dwordx2 v[108:109], v4, s[64:67], s93 offen sc1\nbuffer_load_dwordx4 v[96:99], v0, s[64:67], s90 offen sc1\n" \
    "buffer_load_dwordx4 v[100:103], v0, s[64:67], s91 offen sc1\nbuffer_load_dwordx4 v[104:107], v0, s[64:67], s92 offen sc1\ns_waitcnt vmcnt(0)\nv_max_u32 v116, v108, v109\n" \
    "v_cmp_eq_u32 s[70:71], -1, v116\ns_nop 1\nv_cndmask_b32 v116, 0, 1, s[70:71]\nv_xor_b32 v96, s79, v96\nv_xor_b32 v97, s79, v97\nv_xor_b32 v98, s79, v98\n" \
    "v_xor_b32 v99, s79, v99\nv_xor_b32 v100, s79, v100\nv_xor_b32 v101, s79, v101\nv_xor_b32 v102, s79, v102\nv_xor_b32 v103, s79, v103\nv_xor_b32 v104, s79, v104\n" \
    "v_xor_b32 v105, s79, v105\nv_xor_b32 v106, s79, v106\nv_xor_b32 v107, s79, v107\nv_or3_b32 v116, v116, v96, v97\nv_or3_b32 v116, v116, v98, v99\n" \
    "v_or3_b32 v116, v116, v100, v101\nv_or3_b32 v116, v116, v102, v103\nv_or3_b32 v116, v116, v104, v105\nv_or3_b32 v116, v116, v106, v107\nv_and_b32 v116, 1, v116\n" \
    "v_cmp_eq_u32 s[70:71], 1, v116\nv_pk_add_f32 v[96:97], v[96:97], v[16:17]\nv_pk_add_f32 v[98:99], v[98:99], v[18:19]\nv_pk_add_f32 v[100:101], v[100:101], v[104:105]\n" \
    "v_pk_add_f32 v[102:103], v[102:103], v[106:107]\nv_pk_add_f32 v[96:97], v[96:97], v[100:101]\nv_pk_add_f32 v[98:99], v[98:99], v[102:103]\nv_cvt_pk_bf16_f32 v96, 0, v96\n" \
    "v_cvt_pk_bf16_f32 v97, 0, v97\nv_cvt_pk_bf16_f32 v98, 0, v98\nv_cvt_pk_bf16_f32 v99, 0, v99\nv_pk_add_f32 v[96:97], v[96:97], v[8:9]\n" \
    "v_pk_add_f32 v[98:99], v[98:99], v[10:11]\nv_cvt_pk_bf16_f32 v96, 0, v96\nv_cvt_pk_bf16_f32 v97, 0, v97\nv_cvt_pk_bf16_f32 v98, 0, v98\nv_cvt_pk_bf16_f32 v99, 0, v99\n" \
    "v_pk_mul_f32 v[96:97], v[96:97], v[12:13]\nv_pk_mul_f32 v[98:99], v[98:99], v[14:15]\nv_cvt_pk_bf16_f32 v96, 0, v96\nv_cvt_pk_bf16_f32 v97, 0, v97\n" \
    "v_cvt_pk_bf16_f32 v98, 0, v98\nv_cvt_pk_bf16_f32 v99, 0, v99\nv_lshlrev_b32 v100, 16, v108\nv_and_b32 v101, 0xffff0000, v108\nv_lshlrev_b32 v102, 16, v109\n" \
    "v_and_b32 v103, 0xffff0000, v109\nv_pk_add_f32 v[96:97], v[96:97], v[100:101]\nv_pk_add_f32 v[98:99], v[98:99], v[102:103]\nv_cvt_pk_bf16_f32 v116, v96, v97\n" \
    "v_cvt_pk_bf16_f32 v117, v98, v99\ns_cmp_lg_u64 s[70:71], 0\ns_cbranch_scc1 .Lg19_%=\nglobal_store_dwordx2 v5, v[116:117], s[46:47]\nv_add_u32 v5, 0x10000, v5\n" \
    "s_mov_b32 s90, 0x7fff0000\ns_mov_b32 s91, 0x7fff0000\ns_mov_b32 s92, 0x7fff0000\ns_mov_b32 s93, 0x7fff0000\n.Lg14_%=:\ns_mov_b32 s38, s85\ns_xor_b32 s80, s80, 0x10000\n" \
    "v_xor_b32 v1, 0x10000, v1\ns_mov_b32 s68, 0\ns_add_u32 s32, s32, 1\ns_cmp_lt_u32 s32, 64\ns_cbranch_scc1 .Lg2_%=\ns_endpgm\n.Lg4_%=:\ns_sleep 8\ns_add_u32 s68, s68, 1\n" \
    "s_and_b32 s69, s68, 63\ns_cbranch_scc1 .Lg3_%=\ns_cmp_gt_u32 s68, 0x400000\ns_cbranch_scc1 .Lg1_%=\nbuffer_load_dword v253, off, s[64:67], 0 sc1\ns_waitcnt vmcnt(0)\n" \
    "v_readfirstlane_b32 s69, v253\ns_nop 0\ns_cmp_eq_u32 s69, 1\ns_cbranch_scc0 .Lg3_%=\ns_endpgm\n.Lg13_%=:\ns_waitcnt vmcnt(0)\ns_add_u32 s68, s68, 1\n" \
    "s_and_b32 s69, s68, 63\ns_cbranch_scc1 .Lg12_%=\ns_cmp_gt_u32 s68, 0x400000\ns_cbranch_scc1 .Lg1_%=\nbuffer_load_dword v253, off, s[64:67], 0 sc1\ns_waitcnt vmcnt(0)\n" \
    "v_readfirstlane_b32 s69, v253\ns_nop 0\ns_cmp_eq_u32 s69, 1\ns_cbranch_scc0 .Lg12_%=\ns_endpgm\n.Lg16_%=:\ns_sleep 4\n" \
    "buffer_load_dwordx2 v[108:109], v4, s[64:67], s93 offen sc1\nbuffer_load_dwordx4 v[96:99], v0, s[64:67], s90 offen sc1\n" \
    "buffer_load_dwordx4 v[100:103], v0, s[64:67], s91 offen sc1\nbuffer_load_dwordx4 v[104:107], v0, s[64:67], s92 offen sc1\ns_waitcnt vmcnt(0)\nv_max_u32 v116, v108, v109\n" \
    "v_cmp_eq_u32 s[70:71], -1, v116\ns_nop 1\nv_cndmask_b32 v116, 0, 1, s[70:71]\nv_xor_b32 v96, s79, v96\nv_xor_b32 v97, s79, v97\nv_xor_b32 v98, s79, v98\n" \
    "v_xor_b32 v99, s79, v99\nv_xor_b32 v100, s79, v100\nv_xor_b32 v101, s79, v101\nv_xor_b32 v102, s79, v102\nv_xor_b32 v103, s79, v103\nv_xor_b32 v104, s79, v104\n" \
    "v_xor_b32 v105, s79, v105\nv_xor_b32 v106, s79, v106\nv_xor_b32 v107, s79, v107\nv_or3_b32 v116, v116, v96, v97\nv_or3_b32 v116, v116, v98, v99\n" \
    "v_or3_b32 v116, v116, v100, v101\nv_or3_b32 v116, v116, v102, v103\nv_or3_b32 v116, v116, v104, v105\nv_or3_b32 v116, v116, v106, v107\nv_and_b32 v116, 1, v116\n" \
    "v_cmp_eq_u32 s[70:71], 1, v116\nv_pk_add_f32 v[96:97], v[96:97], v[16:17]\nv_pk_add_f32 v[98:99], v[98:99], v[18:19]\nv_pk_add_f32 v[100:101], v[100:101], v[104:105]\n" \
    "v_pk_add_f32 v[102:103], v[102:103], v[106:107]\nv_pk_add_f32 v[96:97], v[96:97], v[100:101]\nv_pk_add_f32 v[98:99], v[98:99], v[102:103]\nv_cvt_pk_bf16_f32 v96, 0, v96\n" \
    "v_cvt_pk_bf16_f32 v97, 0, v97\nv_cvt_pk_bf16_f32 v98, 0, v98\nv_cvt_pk_bf16_f32 v99, 0, v99\nv_pk_add_f32 v[96:97], v[96:97], v[8:9]\n" \
    "v_pk_add_f32 v[98:99], v[98:99], v[10:11]\nv_cvt_pk_bf16_f32 v96, 0, v96\nv_cvt_pk_bf16_f32 v97, 0, v97\nv_cvt_pk_bf16_f32 v98, 0, v98\nv_cvt_pk_bf16_f32 v99, 0, v99\n" \
    "v_pk_mul_f32 v[96:97], v[96:97], v[12:13]\nv_pk_mul_f32 v[98:99], v[98:99], v[14:15]\nv_cvt_pk_bf16_f32 v96, 0, v96\nv_cvt_pk_bf16_f32 v97, 0, v97\n" \
    "v_cvt_pk_bf16_f32 v98, 0, v98\nv_cvt_pk_bf16_f32 v99, 0, v99\nv_lshlrev_b32 v100, 16, v108\nv_and_b32 v101, 0xffff0000, v108\nv_lshlrev_b32 v102, 16, v109\n" \
    "v_and_b32 v103, 0xffff0000, v109\nv_pk_add_f32 v[96:97], v[96:97], v[100:101]\nv_pk_add_f32 v[98:99], v[98:99], v[102:103]\nv_cvt_pk_bf16_f32 v116, v96, v97\n" \
    "v_cvt_pk_bf16_f32 v117, v98, v99\ns_cmp_lg_u64 s[70:71], 0\ns_cbranch_scc0 .Lg17_%=\ns_add_u32 s68, s68, 1\ns_and_b32 s69, s68, 63\ns_cbranch_scc1 .Lg16_%=\n" \
    "s_cmp_gt_u32 s68, 0x400000\ns_cbranch_scc1 .Lg1_%=\nbuffer_load_dword v253, off, s[64:67], 0 sc1\ns_waitcnt vmcnt(0)\nv_readfirstlane_b32 s69, v253\ns_nop 0\n" \
    "s_cmp_eq_u32 s69, 1\ns_cbranch_scc0 .Lg16_%=\ns_endpgm\n.Lg19_%=:\ns_sleep 4\ns_add_u32 s68, s68, 1\ns_and_b32 s69, s68, 63\ns_cbranch_scc1 .Lg18_%=\n" \
    "s_cmp_gt_u32 s68, 0x400000\ns_cbranch_scc1 .Lg1_%=\nbuffer_load_dword v253, off, s[64:67], 0 sc1\ns_waitcnt vmcnt(0)\nv_readfirstlane_b32 s69, v253\ns_nop 0\n" \
    "s_cmp_eq_u32 s69, 1\ns_cbranch_scc0 .Lg18_%=\ns_endpgm\n.Lg7_%=:\nv_mov_b32 v120, 0\nv_mov_b32 v121, v1\ns_mov_b32 s76, 14\n.Lg10_%=:\n" \
    "ds_read_b128 v[124:127], v121 offset:0\nds_read_b128 v[128:131], v121 offset:1024\nds_read_b128 v[132:135], v121 offset:2048\nds_read_b128 v[136:139], v121 offset:3072\n" \
    "v_add_u32 v121, 0x1000, v121\ns_waitcnt lgkmcnt(0)\nv_max3_u32 v120, v120, v124, v125\nv_max3_u32 v120, v120, v126, v127\nv_max3_u32 v120, v120, v128, v129\n" \
    "v_max3_u32 v120, v120, v130, v131\nv_max3_u32 v120, v120, v132, v133\nv_max3_u32 v120, v120, v134, v135\nv_max3_u32 v120, v120, v136, v137\n" \
    "v_max3_u32 v120, v120, v138, v139\ns_sub_u32 s76, s76, 1\ns_cmp_lg_u32 s76, 0\ns_cbranch_scc1 .Lg10_%=\nv_cmp_eq_u32 vcc, -1, v120\ns_nop 0\ns_cbranch_vccz .Lg8_%=\n" \
    ".Lg9_%=:\ns_mov_b32 s81, 0\ns_barrier\ns_sleep 4\ns_add_u32 s68, s68, 1\ns_and_b32 s69, s68, 63\ns_cbranch_scc1 .Lg2_%=\ns_cmp_gt_u32 s68, 0x400000\n" \
    "s_cbranch_scc1 .Lg1_%=\nbuffer_load_dword v253, off, s[64:67], 0 sc1\ns_waitcnt vmcnt(0)\nv_readfirstlane_b32 s69, v253\ns_nop 0\ns_cmp_eq_u32 s69, 1\n" \
    "s_cbranch_scc0 .Lg2_%=\ns_endpgm\n.Lg1_%=:\nv_mov_b32 v253, 1\nbuffer_store_dword v253, off, s[64:67], 0 sc1\ns_endpgm\n"
typedef unsigned v32u __attribute__((ext_vector_type(32)));
typedef unsigned v16u __attribute__((ext_vector_type(16)));
#define ORD 1
template <bool isV>
__device__ __forceinline__ void s1_main(const Args& a, unsigned char* lds, const int xg, const int ii) {
    const int wave = a.pad0, head = xg * 4 + (ii >> 2), set = (ii >> 1) & 1;
    char* const ws = a.ws;
    unsigned* const ctl = (unsigned*)(ws + O_CTL);
    const unsigned pol = __builtin_amdgcn_readfirstlane(*(const unsigned*)(ws + O_POL));
    const unsigned pubb = isV ? O_UM + (wave & 1) * 262144 + head * 256 : O_EP + ((wave & 1) * 4 + (head >> 3)) * 32768;
    const unsigned psb = isV ? O_UX + (wave & 1) * MiB + (head >> 3) * 16384 + (head & 7) * 1024 : O_PS + (wave * 4 + (head >> 3)) * 16384;
    v16bf w[52], pend[12];
    v4u rX = mkrs(isV ? a.Wv : wave < 2 ? a.Wq : a.Wk, DM * HD * 2), rO = mkrs(a.Wo, DM * HD * 2);
    unsigned bo[4];
#pragma unroll
    for (int i = 0; i < 4; ++i) {
        const int lane = lane_id(), row = lane >> 3, ch = (lane & 7) ^ ((2 * (i & 1) + (row >> 2)) << 1);
        bo[i] = i < 2 ? (((isV ? 64 * (wave >> 1) : 0) + row) * HD + head * DH + 64 * (wave & 1) + 8 * ch) * 2 : ((head * DH + row) * DM + 256 * wave + 8 * ch) * 2;
    }
    asm volatile("s_nop 4" : "+s"(rX), "+s"(rO));
    const int kx = ORD * set;
    {
        const int lane = lane_id(), l15 = lane & 15, gb = lane >> 4;
        auto bufp = [&](int u, int wv) { const int b = (u + 4) % 5; return lds + (b < 3 ? L_W + wv * 24576 + b * 8192 : L_H + wv * 16384 + (b - 3) * 8192); };
        auto issue = [&](auto uu) {
            constexpr int u = decltype(uu)::v, c = u;
            constexpr bool wo = isV && c >= 8;
            const unsigned d = (unsigned)(size_t)(LAS unsigned char*)bufp(u, wave);
#pragma unroll
            for (int r8 = 0; r8 < 8; ++r8)
                bdma<(NTW & (u < 4 ? 1 : u < 8 ? 2 : 64))>(wo ? rO : rX, wo ? bo[2 + (r8 & 1)] : bo[r8 & 1], wo ? ((64 * (c & 1) + 8 * r8) * DM + 64 * ((c - 8) >> 1)) * 2 : (((isV ? 128 : 64) * c + 8 * r8) ^ (512 * kx)) * HD * 2, d + r8 * 1024);
        };
        const unsigned q = l15 >> 2, p = lane & 3;
        const unsigned row0 = 16 * (gb >> 1) + 4 * (gb & 1) + (q & 1) + 8 * (q >> 1);
        const unsigned sw = ((gb & 1) | ((q >> 1) << 1)) << 1;
        auto flush = [&](int j) {
            const v8u v = __builtin_bit_cast(v8u, pend[j]);
            unsigned char* d = lds + L_W + wave * 24576 + j * 2048 + lane * 16;
            *(LAS v4u*)d = (v4u){v[0], v[1], v[2], v[3]};
            *(LAS v4u*)(d + 1024) = (v4u){v[4], v[5], v[6], v[7]};
        };
        sfor<0, 5>(issue);
        sfor<0, 16>([&](auto cc) {
            constexpr int u = decltype(cc)::v, c = u;
            asm volatile("s_waitcnt vmcnt(%0)\n\ts_barrier" :: "n"(u == 0 ? 32 : u < 13 ? 24 : 8 * (15 - u)) : "memory");
            if constexpr (u > 0 && u + 4 < 16) issue(IC<u + 4>{});
            if (u >= 12 && u < 15) { flush(4 * u - 48); flush(4 * u - 47); flush(4 * u - 46); flush(4 * u - 45); }
            v16bf t[4];
#pragma unroll
            for (int n = 0; n < 4; ++n) {
                constexpr bool pk = isV && c >= 8;
                const unsigned char* buf = bufp(u, pk ? wave : (n & 2) + (wave >> 1));
                const unsigned o = pk ? (32 * (n & 1) + 16 * (gb & 1) + 4 * (gb >> 1) + (q & 1)) * 128 + (((4 * (n >> 1) + 2 * (q >> 1) + (p >> 1)) ^ (gb & 2)) << 4) + (p & 1) * 8
                                      : row0 * 128 + (((4 * (wave & 1) + 2 * (n & 1) + (p >> 1)) ^ sw) << 4) + (p & 1) * 8;
                const unsigned o2 = pk ? (o ^ 64) + 8 * 128 : o + 32 * 128;
                const v2u r0 = tr_rd(buf, o), r1 = tr_rd(buf, o + 2 * 128), r2 = tr_rd(buf, o2), r3 = tr_rd(buf, o2 + 2 * 128);
                const v8u v = {r0.x, r0.y, r1.x, r1.y, r2.x, r2.y, r3.x, r3.y};
                t[n] = __builtin_bit_cast(v16bf, v);
            }
            asm volatile("s_waitcnt lgkmcnt(0)" ::: "memory");
            sfor<0, 4>([&](auto nn) {
                constexpr int n = decltype(nn)::v, j = lsl(isV, c, n), r = ridx(isV, c, n);
                if constexpr (j >= 0) { pend[j] = t[n]; asm volatile("" : "+v"(pend[j])); }
                else {
                    w[r] = t[n];
                    if constexpr (r < 32) asm volatile("" : "+a"(w[r])); else asm volatile("" : "+v"(w[r]));
                }
            });
        });
    }
    RSTAMP(74, 0);
    if (!wave_wait<8>(ctl + C_A, 1, 1, NARM, ctl + C_ABORT)) asm volatile("s_endpgm");
    lds_barrier();
    RSTAMP(74, 1);
    const unsigned lb0 = (unsigned)(size_t)(LAS unsigned char*)lds;
    const unsigned lane = lane_id(), l15 = lane & 15, g = lane >> 4, q = l15 >> 2, p = lane & 3, l16 = lane * 16, to = g * 128 + (l15 & 7) * 16, xb = ((g & 1) << 1) | (g >> 1);
    const unsigned r = 8 * wave + (lane >> 3), ao = r * 2048 + 16 * ((lane & 7) ^ ((r >> 1) & 7)) + 1024, ra = lb0 + L_H + (l15 >> 3) * 2048 + (l15 & 7) * 128;
    const v16u c0 = {(lane & 8) ? 0xEEEEu : 0x4444u, lb0 + L_H + l16, l16, lb0 + L_W + wave * 24576 + l16, lb0 + L_T + wave * 512 + q * 128 + (g & 1) * 64 + (g >> 1) * 32 + p * 2,
                     lb0 + (isV ? L_T + to : L_QX + (wave >> 1) * 4096 + (wave & 1) * 128 + g * 128 + to), isV ? lb0 + L_QX + g * 128 + to : lb0 + L_T + to,
                     isV ? xb * 16384 + l15 * 16 : g < 2 ? (l15 >> 3) * 2048 + (head & 7) * 256 + to : lane < 40 ? 4096 + (head & 7) * 128 + l15 * 16 : 0x7FFF0000u, isV ? xb * 65536 + l15 * 16 : lb0 + L_T + 2048 + (to ^ (l15 >> 3) * 64),
                     ra + 16 * (g ^ (l15 >> 1)), ra + 16 * ((4 + g) ^ (l15 >> 1)), ao, ao - 896,
                     0u, lb0 + (isV ? L_QX : L_KX) + wave * 1024 + l16, 0u};
    const unsigned sd = ((pol ^ (wave >> 1)) & 1) * 8192;
    PARS;
    const v16u s0 = {0x3e0293eeu, 0x3db504f3u, 0x3fb8aa3bu, (unsigned)set, (unsigned)wave, O_E1 + (((xg >> 1) & 1) * 4 + wave) * 16384 + ((pol ^ (xg >> 2)) & 1) * 8192 + 2048 * kx, pubb + sd, psb + (isV ? sd : 0),
                     O_H1 - 1024 + set * 65536, lb0 + L_H + wave * 2048, hw(pol, xg) + H_C + 512 * set, par(P_R), par(P_A) + par(P_B), par(P_X), lb0 + L_ST + (wave == STAMP_WAVE ? 0 : MiB), lb0 + L_ST};
    u64 cm[4];
#pragma unroll
    for (int i = 0; i < 4; ++i) cm[i] = __ballot(16 * (l15 >> 3) + 4 * g + i <= 8 * wave + (l15 & 7));
    const u64 dbg = DBG(a, blockIdx.x);
    const v16u s1 = {isV ? (unsigned)(C_F + 32 * (128 * set + 32 * wave + head)) * 4 : 0x00FFFFFFu, isV ? set : 0x00FF00FFu, isV ? set : (unsigned)cm[0], (unsigned)(cm[0] >> 32), (unsigned)cm[1], (unsigned)(cm[1] >> 32),
                     (unsigned)cm[2], (unsigned)(cm[2] >> 32), (unsigned)cm[3], (unsigned)(cm[3] >> 32), (unsigned)dbg, (unsigned)(dbg >> 32), lb0 + L_T + wave * 512, ii < 4 ? hw(pol, xg) + H_D + 4 * wave : 0u, 1024u * kx,
                     head < (isV ? 20 : 28) ? O_D + (isV ? D_X2 : 0) + head * 8192 + wave * 2048 : 0x70000000u};
    const v4u rws = mkrs(ws, WS_END);
    v32u ww[13];
#pragma unroll
    for (int i = 0; i < 13; ++i) {
        const v8u x0 = __builtin_bit_cast(v8u, w[4 * i]), x1 = __builtin_bit_cast(v8u, w[4 * i + 1]), x2 = __builtin_bit_cast(v8u, w[4 * i + 2]), x3 = __builtin_bit_cast(v8u, w[4 * i + 3]);
        ww[i] = __builtin_shufflevector(__builtin_shufflevector(x0, x1, 0, 1, 2, 3, 4, 5, 6, 7, 8, 9, 10, 11, 12, 13, 14, 15), __builtin_shufflevector(x2, x3, 0, 1, 2, 3, 4, 5, 6, 7, 8, 9, 10, 11, 12, 13, 14, 15),
                                        0, 1, 2, 3, 4, 5, 6, 7, 8, 9, 10, 11, 12, 13, 14, 15, 16, 17, 18, 19, 20, 21, 22, 23, 24, 25, 26, 27, 28, 29, 30, 31);
    }
#define S1_CALL(TXT) asm volatile("s_nop 4\n" TXT :: "{v[0:15]}"(c0), "{s[32:47]}"(s0), "{s[48:63]}"(s1), "{s[64:67]}"(rws), \
              "{a[0:31]}"(ww[0]), "{a[32:63]}"(ww[1]), "{a[64:95]}"(ww[2]), "{a[96:127]}"(ww[3]), "{a[128:159]}"(ww[4]), "{a[160:191]}"(ww[5]), "{a[192:223]}"(ww[6]), "{a[224:255]}"(ww[7]), \
              "{v[92:123]}"(ww[8]), "{v[124:155]}"(ww[9]), "{v[156:187]}"(ww[10]), "{v[188:219]}"(ww[11]), "{v[220:251]}"(ww[12]) : "memory", "s95", "v253")
    if constexpr (isV) S1_CALL(ASM_V); else S1_CALL(ASM_Q);
}

__device__ __forceinline__ float gelu_tanh(float v) {
    const float e = v * (-2.302208198f - 0.1029432397f * (v * v));
    return v * __builtin_amdgcn_rcpf(1.f + __builtin_amdgcn_exp2f(e));
}
__device__ __forceinline__ v4u unsent4(v4u v) { return (v4u){unsent(v.x), unsent(v.y), unsent(v.z), unsent(v.w)}; }
__device__ __forceinline__ unsigned rt_now() { return (unsigned)__builtin_amdgcn_s_memrealtime(); }
__device__ __forceinline__ void rt_delay(unsigned ticks  , unsigned t0 = rt_now()) {
    while ((unsigned)__builtin_amdgcn_s_memrealtime() - t0 < ticks) __builtin_amdgcn_s_sleep(2);
}
__device__ __forceinline__ float wave_sum(float x) {
    x += __uint_as_float(__builtin_amdgcn_update_dpp(0, __float_as_uint(x), 0xB1, 0xF, 0xF, false));
    x += __uint_as_float(__builtin_amdgcn_update_dpp(0, __float_as_uint(x), 0x4E, 0xF, 0xF, false));
    x += __uint_as_float(__builtin_amdgcn_update_dpp(0, __float_as_uint(x), 0x124, 0xF, 0xF, false));
    x += __uint_as_float(__builtin_amdgcn_update_dpp(0, __float_as_uint(x), 0x128, 0xF, 0xF, false));
    return red32_add(red16_add(x));
}
__device__ __forceinline__ void ln_row(const v4u (&xr)[2], const f32x4 (&c1)[4], const f32x4 (&sh)[4], v4u (&o)[2]) {
    f32x2 v[8];
    f32x2 s2 = {0.f, 0.f};
#pragma unroll
    for (int j = 0; j < 8; ++j) { v[j] = (f32x2){lo16f(xr[j >> 2][j & 3]), hi16f(xr[j >> 2][j & 3])}; s2 += v[j]; }
    const float mean = wave_sum(s2[0] + s2[1]) * (1.f / DM);
    f32x2 q2 = {0.f, 0.f};
#pragma unroll
    for (int j = 0; j < 8; ++j) { const f32x2 d = v[j] - mean; q2 += d * d; }
    const float vs = wave_sum(q2[0] + q2[1]);
    const float mb = rbf(mean), vb = rbf(vs * (1.f / DM));
    const float rsd = 1.f / rbf(sqrtf(rbf(vb + 1e-5f)));
    auto r2 = [](f32x2 z) { const unsigned u = pack2(z[0], z[1]); return (f32x2){lo16f(u), hi16f(u)}; };
#pragma unroll
    for (int j = 0; j < 8; ++j) {
        const f32x2 cc = {c1[j >> 1][2 * (j & 1)], c1[j >> 1][2 * (j & 1) + 1]}, ss = {sh[j >> 1][2 * (j & 1)], sh[j >> 1][2 * (j & 1) + 1]};
        const f32x2 y = r2(r2(r2(v[j] - mb) * rsd) * cc) + ss;
        o[j >> 2][j & 3] = pack2(y[0], y[1]);
    }
}
template <int N> __device__ __forceinline__ void ld_mod(rsrc_t rs, unsigned* ctl, const int (&col)[N], f32x4* m) {
    for (unsigned spins = 0;;) {
        unsigned mx = 0;
#pragma unroll
        for (int i = 0; i < N; ++i) { const v4u v = bld16(rs, O_MOD + col[i] * 4); mx = max(mx, umax4(v)); m[i] = __builtin_bit_cast(f32x4, v); }
        if (!__any(mx == SENT)) break;
        __builtin_amdgcn_s_sleep(6);
        if (spin_fail(spins, ctl + C_ABORT)) asm volatile("s_endpgm");
    }
}
__device__ __forceinline__ void set_m0(unsigned v) { asm volatile("s_mov_b32 m0, %0" :: "s"(v) : "m0"); }
__device__ __forceinline__ void mod8(const Args& a, int col, f32x4 (&m)[2]) {
    f32x4 p[8][2];
#pragma unroll
    for (int k = 0; k < 16; ++k) p[k >> 1][k & 1] = *(const f32x4*)(a.ws + O_MODP + ((k >> 1) * 2048 + col + 4 * (k & 1)) * 4);
#pragma unroll
    for (int h = 0; h < 2; ++h) {
        const f32x4 t = ((p[0][h] + p[1][h]) + (p[2][h] + p[3][h])) + ((p[4][h] + p[5][h]) + (p[6][h] + p[7][h]));
#pragma unroll
        for (int i = 0; i < 4; ++i) { float v = rbf(t[i]); if (col >= DM) v = rbf(1.f + v); m[h][i] = v; }
    }
}
__device__ __forceinline__ void mod_cols(const Args& a, rsrc_t rs, unsigned char* lds, int cg) {
    const int tid = otid();
    v4u wv[32];
#pragma unroll
    for (int i = 0; i < 32; ++i) wv[i] = __builtin_nontemporal_load((const v4u*)(a.w_mod + (size_t)((tid >> 3) + 32 * i) * 6144 + cg * 64 + 8 * (tid & 7)));
    {
        const v2u cr = *(const v2u*)(a.c + 4 * tid);
        f32x4 sv = {lo16f(cr.x), hi16f(cr.x), lo16f(cr.y), hi16f(cr.y)};
#pragma unroll
        for (int i = 0; i < 4; ++i) sv[i] = rbf(sv[i] / (1.f + __expf(-sv[i])));
        *(LAS f32x4*)(lds + 16384 + tid * 16) = sv;
    }
    lds_barrier();
    f32x2 acc[4] = {{0.f, 0.f}, {0.f, 0.f}, {0.f, 0.f}, {0.f, 0.f}};
#pragma unroll
    for (int i = 0; i < 32; ++i) {
        const float sv = *(const LAS float*)(lds + 16384 + ((tid >> 3) + 32 * i) * 4);
#pragma unroll
        for (int j = 0; j < 4; ++j) acc[j] += sv * (f32x2){lo16f(wv[i][j]), hi16f(wv[i][j])};
    }
    unsigned char* red = lds + 20480;
    *(LAS f32x4*)(red + (tid >> 3) * 272 + (tid & 7) * 32) = (f32x4){acc[0][0], acc[0][1], acc[1][0], acc[1][1]};
    *(LAS f32x4*)(red + (tid >> 3) * 272 + (tid & 7) * 32 + 16) = (f32x4){acc[2][0], acc[2][1], acc[3][0], acc[3][1]};
    lds_barrier();
    if (tid < 64) {
        f32x4 t = {0.f, 0.f, 0.f, 0.f};
#pragma unroll
        for (int i = 0; i < 8; ++i) t += *(const LAS f32x4*)(red + ((tid >> 4) * 8 + i) * 272 + (tid & 15) * 16);
#pragma unroll
        for (int i = 0; i < 4; ++i) { float v = rbf(red32_add(red16_add(t[i]))); if ((cg >> 4) == 4) v = rbf(1.f + v); t[i] = v; }
        if (tid < 16) bst16(rs, O_MOD + (cg * 64 + 4 * tid) * 4, unsent4(__builtin_bit_cast(v4u, t)));
    }
}
#define TD_A 300
#define TD_L 1150
constexpr int NARM0 = 5120;
__device__ __forceinline__ unsigned arm0(unsigned v) {
    if (v < 1280) return (v < 640 ? O_EP : O_PS - MiB) + v / 5 * 8192 + v % 5 * 1024;
    if (v < 2304) return O_E1 + 131072 + (v - 1280) * 1024;
    if (v < 3072) return O_D + (v - 2304) / 384 * D_SLOT + (v - 2304) % 384 * 1024;
    return O_SL + (v - 3072) * 1024;
}
__device__ __forceinline__ void f_start(const Args& a, rsrc_t rs, unsigned char* lds, int idx  , unsigned t0) {
    const int tid = otid(), lane = tid & 63, wave = a.pad0;
    unsigned* const ctl = (unsigned*)(a.ws + O_CTL);
    const int r0 = 4 * idx + wave;
    v4u xr[6][2];
    auto ldx = [&](int k, int row) { for (int c = 0; c < 2; ++c) xr[k][c] = *(const v4u*)(a.x + (size_t)row * DM + 512 * c + 8 * lane); };
    ldx(0, r0);
    {
        f32x4 m[2];
        mod8(a, 8 * tid, m);
        *(LAS f32x4*)(lds + 32 * tid) = m[0]; *(LAS f32x4*)(lds + 32 * tid + 16) = m[1];
    }
    lds_barrier();
    f32x4 c1[4], sh[4];
#pragma unroll
    for (int i = 0; i < 4; ++i) { const int col = 512 * (i >> 1) + 8 * lane + 4 * (i & 1); sh[i] = *(const LAS f32x4*)(lds + col * 4); c1[i] = *(const LAS f32x4*)(lds + (DM + col) * 4); }
    auto row = [&](int k, int r) {
        v4u o[2];
        ln_row(xr[k], c1, sh, o);
#pragma unroll
        for (int c = 0; c < 2; ++c) {
            const bool od = lane & 1;
            const unsigned y0 = __builtin_amdgcn_update_dpp(0, od ? o[c].x : o[c].z, 0xB1, 0xF, 0xF, false), y1 = __builtin_amdgcn_update_dpp(0, od ? o[c].y : o[c].w, 0xB1, 0xF, 0xF, false);
            bst16(rs, O_H1 + (unsigned)(r * DM + 512 * c + 8 * lane) * 2, od ? (v4u){y0, o[c].z, y1, o[c].w} : (v4u){o[c].x, y0, o[c].y, y1});
        }
    };
    row(0, r0);
    if (idx >= 64) {
        rt_delay(TD_A, t0);
        for (unsigned v = r0 - 256; v < NARM0; v += 256) bst16(rs, arm0(v) + lane * 16, sentv());
    }
    drained(ctl + C_A);
    if (idx >= 64) return;
    RSTAMP(75, 2);
    rt_delay(TD_L, t0);
    mod_cols(a, rs, lds, idx + 32);
    if (idx >= 32) return;
    for (int h = 0; h < 2; ++h) {
#pragma unroll
        for (int k = 0; k < 6; ++k) ldx(k, 512 + r0 + 128 * (6 * h + k));
#pragma unroll
        for (int k = 0; k < 6; ++k) row(k, 512 + r0 + 128 * (6 * h + k));
    }
    drained(ctl + C_H);
}
#define TD_W 1300
__device__ __forceinline__ unsigned r_ew(unsigned pol, unsigned lane) { return ((lane >> 4) & 1) * 65536 + (((O_E1 >> 21) ^ pol ^ (lane >> 5)) & 1) * 8192 + (lane & 15) * 16; }
__device__ __forceinline__ unsigned r_e1(int cb) { return O_E1 + ((cb >> 2) & 3) * 16384 + (cb >> 4) * 1024 + (cb & 3) * 256; }
__device__ __forceinline__ unsigned r_db(int cb) { return D_X2 + (cb >> 3) * 16384 + (cb & 7) * 1024; }
__device__ __forceinline__ unsigned r_st(int cb, unsigned lane) { return D_ST + (cb >> 5) * 16384 + (lane >> 5) * 8192 + (cb & 31) * 256 + (lane & 31) * 8; }
__device__ __forceinline__ void r_main(const Args& a, rsrc_t rs, unsigned char* lds, int xg, int i8, unsigned* ctl) {
    const unsigned lane = lane_id(), wave = a.pad0, l15 = lane & 15, g = lane >> 4, q = l15 >> 2, p = lane & 3, cb = 8 * xg + i8;
    const unsigned pol = __builtin_amdgcn_readfirstlane(*(const unsigned*)(a.ws + O_POL)), lb = (unsigned)(size_t)(LAS unsigned char*)lds;
    *(volatile LAS unsigned*)(lds + 4096) = 0u;
    if (!wave_wait<64>(ctl + C_A, 1, 1, NARM, ctl + C_ABORT)) asm volatile("s_endpgm");
    lds_barrier();
    RSTAMP(74, 0);
    const v16u c0 = {(lane & 8) ? 0xEEEEu : 0x4444u, lane * 16, 4096 + lane * 16, lb + wave * 1024 + l15 * 16 + (g & 1) * 8 + (g >> 1) * 4,
                     lb + wave * 1024 + (8 * (p >> 1) + q) * 16 + (p & 1) * 8, lb + wave * 1024 + ((2 * (g & 1) + (p >> 1)) ^ (cb & 1)) * 256 + (8 * (g >> 1) + q) * 16 + (p & 1) * 8,
                     r_ew(pol, lane), (wave * 32 + (lane & 31)) * 2048 + cb * 32 + (lane >> 5) * 16, (C_F + 32 * (128 * (wave & 1) + 32 * (cb >> 4) + (lane & 31))) * 4, r_st(cb, lane) - r_db(cb), lb + 4096, (g >> 1) * 1024 + (g & 1) * 256 + l15 * 16,
                     O_MOD + (2 * DM + 16 * cb + 8 * (lane >> 5)) * 4, (lane >> 3) * 16384 + 4096 + (lane & 7) * 128, 0u, 0u};
    const unsigned sd = ((pol ^ (xg >> 2)) & 1) * 8192;
    PARS;
    const v16u s0 = {wave, wave, par(P_A), par(P_B), par(P_EU), par(P_DMIN), par(P_R), par(P_TP), par(P_TU), par(P_T0),
                     O_UX + (cb & 31) * 65536 + sd, O_UM + (cb & 31) * 16384 + sd, O_EP + ((xg >> 1) & 1) * 131072 + sd, O_PS + sd, r_e1(cb), O_D + r_db(cb)};
    const u64 dbg = DBG(a, blockIdx.x);
    const v8u s1 = {i8 ? 0u : hw(pol, xg) + H_C + 512 * (wave & 1), (cb & 1) * 65536, lb + 8192 + wave * 32, lb + 8192, (unsigned)dbg, (unsigned)(dbg >> 32), (unsigned)(u64)a.x, (unsigned)((u64)a.x >> 32)};
    const v4u rws = mkrs(a.ws, WS_END);
    const unsigned n0 = __builtin_amdgcn_readfirstlane(rt_now() / par(P_R));
    const v8u s2 = {n0 * par(P_R), (n0 & 1) * 128, par(P_TF), par(P_X2), hw(pol, xg) + H_D, par(P_TT), 0u, 0u};
    asm volatile("s_nop 4\n" ASM_R :: "{v[0:15]}"(c0), "{s[32:47]}"(s0), "{s[48:55]}"(s1), "{s[56:63]}"(s2), "{s[64:67]}"(rws) : "memory", "s95", "v253", "a255");
}

typedef __attribute__((ext_vector_type(16))) float f32x16;
template <bool is2> __device__ __forceinline__ void f_main(const Args& a, rsrc_t rs, unsigned char* lds, int xg, int kq, int cg, unsigned* ctl) {
    constexpr int L_C = 131072, L_S = L_C + 8192, NG = is2 ? 7 : 8, LD = is2 ? DM : FF;
    const int tid = otid(), lane = tid & 63, wave = a.pad0, l15 = lane & 15, g = lane >> 4;
    const unsigned lb = (unsigned)(size_t)(LAS unsigned char*)lds, q = l15 >> 2, p = lane & 3, l16 = lane * 16;
    const unsigned pol = __builtin_amdgcn_readfirstlane(*(const unsigned*)(a.ws + O_POL));
    auto gr = [&](int G) { const int x = G + 2 * ((blockIdx.x >> 3) & 3); return x >= NG ? x - NG : x; };
    lds_barrier();
    v4u w[64];
    f32x16 u1 = {}, u2 = {};
    {
        v4u rW = mkrs(is2 ? a.w2 : a.w1, DM * FF * 2);
        unsigned bo[2];
#pragma unroll
        for (int i = 0; i < 2; ++i) bo[i] = (g * LD + 16 * ((l15 >> 1) ^ (g | i << 2)) + 8 * (lane & 1)) * 2;
        asm volatile("s_nop 4" : "+s"(rW));
        auto issue = [&](int G) {
            const unsigned r0 = 896 * kq + 128 * gr(G) + 32 * wave;
#pragma unroll
            for (int j = 0; j < 8; ++j) bdma<(NTW & 8)>(rW, bo[(j >> 1) & 1], ((r0 + 4 * j) * LD + 128 * cg) * 2, lb + (G & 3) * 32768 + wave * 8192 + j * 1024);
        };
        issue(0); issue(1); issue(2); issue(3);
        if (!is2) {
            f32x4 md[2];
            ld_mod<2>(rs, ctl, {4 * DM + 4 * tid, 3 * DM + 4 * tid}, md);
            *(LAS f32x4*)(lds + L_C + 16 * tid) = md[0]; *(LAS v2u*)(lds + L_C + 4096 + 8 * tid) = (v2u){pack2(md[1][0], md[1][1]), pack2(md[1][2], md[1][3])};
            asm volatile("s_waitcnt lgkmcnt(0)" ::: "memory");
        }
        const unsigned ox = is2 ? (4 * (g >> 1) + q) * 256 + 32 * ((2 * (kq ^ (2 * (g & 1) + (p >> 1))) + (wave >> 1)) ^ q) + 16 * (wave & 1) + 8 * (p & 1)
                                : (8 * (g >> 1) + q) * 256 + 32 * ((2 * wave + (g & 1)) ^ (q | (g >> 1) << 2)) + 8 * p;
        sfor<0, NG>([&](auto gg) {
            constexpr int G = decltype(gg)::v;
            asm volatile("s_waitcnt vmcnt(%0)\n\ts_barrier" :: "n"(G < NG - 3 ? 24 : 8 * (NG - 1 - G)) : "memory");
#pragma unroll
            for (int k = 0; k < 8; ++k) {
                const unsigned o = (G & 3) * 32768 + k * 4096;
                const v2u lo = tr_rd(lds, o + ox), hi = tr_rd(lds, o + (is2 ? (ox ^ 128) + 2048 : ox + 1024));
                w[8 * G + k] = (v4u){lo.x, lo.y, hi.x, hi.y};
                if constexpr (!is2) {
                    const unsigned k0 = 128 * gr(G) + 16 * k + 8 * (g >> 1);
                    const f32x4 ca = *(const LAS f32x4*)(lds + L_C + 4 * k0), cb = *(const LAS f32x4*)(lds + L_C + 4 * k0 + 16);
                    u2 = __builtin_amdgcn_mfma_f32_32x32x16_bf16(__builtin_bit_cast(v8bf, w[8 * G + k]), __builtin_bit_cast(v8bf, lds16(lds + L_C + 4096 + 2 * k0)), u2, 0, 0, 0);
                    w[8 * G + k] = (v4u){pack2(lo16f(lo.x) * ca[0], hi16f(lo.x) * ca[1]), pack2(lo16f(lo.y) * ca[2], hi16f(lo.y) * ca[3]), pack2(lo16f(hi.x) * cb[0], hi16f(hi.x) * cb[1]), pack2(lo16f(hi.y) * cb[2], hi16f(hi.y) * cb[3])};
                    u1 = __builtin_amdgcn_mfma_f32_32x32x16_bf16(__builtin_bit_cast(v8bf, w[8 * G + k]), __builtin_bit_cast(v8bf, (v4u){0x3F803F80u, 0x3F803F80u, 0x3F803F80u, 0x3F803F80u}), u1, 0, 0, 0);
                }
            }
            if constexpr (!is2) asm volatile("" : "+v"(u1), "+v"(u2));
#pragma unroll
            for (int k = 8 * G; k < 8 * G + 8; ++k) asm volatile("" : "+a"(w[k]));
            lds_barrier();
            if (G + 4 < NG) issue(G + 4);
        });
    }
    const unsigned sd = ((pol ^ (xg >> 2)) & 1) * 8192;
    v16u c0 = {l16, lb + l16, is2 ? min(lane, 6) * 16384 + wave * 2048 + 2044 : D_ST - D_X2 + (l15 >> 3) * 16384 + ((l15 & 6) * 4 + 2 * wave + (lane & 1)) * 256, lb + L_S + wave * 256 + l15 * 16, lb + L_S + (lane & 31) * 8, 0u, 0xbdd2d3e8u, 0xbdd2d3e8u, 0xc0135761u, 0xc0135761u, 0x3f800000u, 0x3f800000u, 0u, 0u, 0u, 0u};
    if (is2) {
        const int col = 128 * cg + 32 * kq + 8 * wave + 4 * (lane >> 5);
        const v2u br = *(const v2u*)(a.b2 + col);
        f32x4 gv;
        ld_mod<1>(rs, ctl, {5 * DM + col}, &gv);
        const int f1 = 7 * (kq & 1) + min(lane, 6);
        c0[3] = hw(pol, (xg & 4) + (f1 >> 2)) + H_P + 256 * (f1 & 3); c0[4] = r_db(col >> 4) - (kq & 1) * 7 * 16384 + (wave & 1) * 512 + (lane & 31) * 16 + (lane >> 5) * 8; c0[5] = ((lane & 31) * DM + col) * 2;
        c0[8] = br.x << 16; c0[9] = br.x & 0xFFFF0000u; c0[10] = br.y << 16; c0[11] = br.y & 0xFFFF0000u;
#pragma unroll
        for (int i = 0; i < 4; ++i) c0[12 + i] = __float_as_uint(gv[i]);
    } else {
#pragma unroll
        for (int h = 0; h < 4; ++h) {
            const v2u br = *(const v2u*)(a.b1 + 128 * cg + 32 * wave + 8 * h + 4 * (g >> 1));
            u2[4 * h] += lo16f(br.x); u2[4 * h + 1] += hi16f(br.x); u2[4 * h + 2] += lo16f(br.y); u2[4 * h + 3] += hi16f(br.y);
        }
    }
    if (!wave_wait<8>(ctl + C_A, 1, 1, NARM, ctl + C_ABORT)) asm volatile("s_endpgm");
    RSTAMP(74, 0);
    const u64 dbg = DBG(a, blockIdx.x);
    PARS;
    const v16u s0 = {0u, (unsigned)wave, is2 ? par(P_T1) : -par(P_TZ), par(P_TS), O_D + (is2 ? (kq & 1) * 7 * 16384 : D_X2) + sd, lb + wave * 2048, hw(pol, xg) + H_C, hw(pol, xg) + H_P + 256 * ((blockIdx.x >> 3) & 3),
                     O_D + (cg >= 14 ? cg - 14 : cg) * 16384 + wave * 2048 + sd, D_ST - D_X2 + (wave >> 1) * 16384 + (wave & 1) * 4096, lb + L_S + 2048 + (wave == STAMP_WAVE ? 0 : MiB), lb + L_S + 2048,
                     (unsigned)dbg, (unsigned)(dbg >> 32), (unsigned)(u64)a.out, (unsigned)((u64)a.out >> 32)};
    v16u s1;
#pragma unroll
    for (int i = 0; i < 16; ++i) {
        const int o = kq ^ (i & 3);
        s1[i] = i < 8 ? gr(i) * 16384 + wave * 2048 : O_SL + ((cg * 2 + (o & 1)) * 2 + ((i >> 1) & 1)) * 16384 + ((pol ^ (o >> 1)) & 1) * 8192 + (i & 1) * 4096 + wave * 1024;
    }
    s1[12] = is2 ? par(P_T2) : -par(P_TY);
    const v4u rws = mkrs(a.ws, WS_END);
    v32u ww[8];
#pragma unroll
    for (int i = 0; i < NG; ++i) {
        const v16u x0 = __builtin_shufflevector(__builtin_shufflevector(w[8 * i], w[8 * i + 1], 0, 1, 2, 3, 4, 5, 6, 7), __builtin_shufflevector(w[8 * i + 2], w[8 * i + 3], 0, 1, 2, 3, 4, 5, 6, 7), 0, 1, 2, 3, 4, 5, 6, 7, 8, 9, 10, 11, 12, 13, 14, 15);
        const v16u x1 = __builtin_shufflevector(__builtin_shufflevector(w[8 * i + 4], w[8 * i + 5], 0, 1, 2, 3, 4, 5, 6, 7), __builtin_shufflevector(w[8 * i + 6], w[8 * i + 7], 0, 1, 2, 3, 4, 5, 6, 7), 0, 1, 2, 3, 4, 5, 6, 7, 8, 9, 10, 11, 12, 13, 14, 15);
        ww[i] = __builtin_shufflevector(x0, x1, 0, 1, 2, 3, 4, 5, 6, 7, 8, 9, 10, 11, 12, 13, 14, 15, 16, 17, 18, 19, 20, 21, 22, 23, 24, 25, 26, 27, 28, 29, 30, 31);
    }
#define F_IN "{v[0:15]}"(c0), "{s[32:47]}"(s0), "{s[48:63]}"(s1), "{s[64:67]}"(rws), "{a[0:31]}"(ww[0]), "{a[32:63]}"(ww[1]), "{a[64:95]}"(ww[2]), "{a[96:127]}"(ww[3]), "{a[128:159]}"(ww[4]), "{a[160:191]}"(ww[5]), "{a[192:223]}"(ww[6])
    if constexpr (is2) asm volatile("s_nop 4\n" ASM_F2 :: F_IN : "memory", "s95");
    else asm volatile("s_nop 7\ns_nop 7\n" ASM_F1 :: F_IN, "{a[224:255]}"(ww[7]), "{v[16:31]}"(u1), "{v[32:47]}"(u2) : "memory", "s95");
}

__global__ void __launch_bounds__(256, 1) k_main(Args) {
    extern __shared__ __attribute__((aligned(16))) unsigned char lds[];
    const int xg = blockIdx.x & 7, ii = blockIdx.x >> 3;
    int wv = __builtin_amdgcn_readfirstlane(threadIdx.x >> 6); asm volatile("" : "+s"(wv));
    if (blockIdx.x >= 224) code_touch(lds + LDS_BYTES - 1024, ((blockIdx.x >> 3) - 28) * 4 + wv);
    auto args = [&]() { PARS; Args r = *(const Args*)pp_; r.pad0 = wv; r.mode = blockIdx.x; return r; };
    const Args a = args();
    RSTAMP(75, 0);
    if (ii < 16) {
        if (ii & 1) s1_main<true>(args(), lds, xg, ii); else s1_main<false>(args(), lds, xg, ii);
        return;
    }
    const rsrc_t rs = __builtin_amdgcn_make_buffer_rsrc((void*)a.ws, (short)0, (int)WS_END, 0x00020000);
    unsigned* const ctl = (unsigned*)(a.ws + O_CTL);
    const int i4 = ii & 3, cg = 4 * (xg & 3) + i4;
    const unsigned t0 = rt_now();
    f_start(args(), rs, lds, (blockIdx.x - 64) & 127, t0);
    if (ii < 24) {
        rt_delay(TD_W, t0);
        r_main(args(), rs, lds, xg, ii - 16, ctl);
        return;
    }
    RSTAMP(75, 1);
    if (ii >= 28 && cg >= 14) return;
    if (ii < 28) f_main<true>(args(), rs, lds, xg, xg >> 1, 4 * (xg & 1) + i4, ctl);
    else f_main<false>(args(), rs, lds, xg, 0, 14 * (xg >> 2) + cg, ctl);
}

extern "C" void kernel_launch(void* const* d_in, const int* in_sizes, int n_in,
                              void* d_out, int out_size, void* d_ws, size_t ws_size,
                              hipStream_t stream) {
    Args a;
    a.x = (const u16*)d_in[0]; a.Wq = (const u16*)d_in[2]; a.Wk = (const u16*)d_in[3];
    a.Wv = (const u16*)d_in[4]; a.Wo = (const u16*)d_in[5];
    a.w1 = (const u16*)d_in[8]; a.b1 = (const u16*)d_in[9]; a.w2 = (const u16*)d_in[10]; a.b2 = (const u16*)d_in[11];
    a.out = (u16*)d_out; a.ws = (char*)d_ws; a.mode = 0; a.pad0 = 0;
    a.c = (const u16*)d_in[12]; a.w_mod = (const u16*)d_in[13];
    k_pre<<<280, 256, 0, stream>>>(a.c, a.w_mod, (const u16*)d_in[1], a.ws);
    hipFuncSetAttribute((const void*)k_main, hipFuncAttributeMaxDynamicSharedMemorySize, LDS_BYTES);
    k_main<<<256, 256, LDS_BYTES, stream>>>(a);
}
```

```cpp
#include <hip/hip_runtime.h>
#include <hip/hip_bf16.h>
#include <stdint.h>

typedef unsigned short u16;
typedef unsigned long long u64;
typedef __attribute__((ext_vector_type(8))) short bf16x8;
typedef __attribute__((ext_vector_type(4))) short bf16x4;
typedef __attribute__((ext_vector_type(4))) __bf16 bf16x4_t;
typedef __attribute__((ext_vector_type(8))) __bf16 v8bf;
typedef __attribute__((ext_vector_type(16))) __bf16 v16bf;
typedef __attribute__((ext_vector_type(4))) float f32x4;
typedef __attribute__((ext_vector_type(2))) float f32x2;
typedef unsigned v8u __attribute__((ext_vector_type(8)));
typedef unsigned v4u __attribute__((ext_vector_type(4)));
typedef unsigned v2u __attribute__((ext_vector_type(2)));
#define GAS __attribute__((address_space(1)))
#define LAS __attribute__((address_space(3)))
typedef GAS unsigned gu32;
#define RLX_AGENT __ATOMIC_RELAXED, __HIP_MEMORY_SCOPE_AGENT

constexpr int S_TOK = 2048, DM = 1024, NH = 32, DH = 128, MEMR = 8, SEG = 32, NSEG = 64;
constexpr int HD = NH * DH;
constexpr int QKN = 2 * HD;
constexpr int FF = 3584;

#define NTW 12
constexpr int RG = 16;
#define RGX 16
constexpr unsigned MiB = 1u << 20;
constexpr unsigned O_CTL = 0;
constexpr unsigned O_MOD = 65536;
constexpr unsigned O_POL = O_MOD + 32768;
constexpr unsigned O_MODP = 131072;
constexpr unsigned O_GO = 196608;
constexpr unsigned O_EP = 2 * MiB;
constexpr unsigned O_PS = O_EP + RG * 262144;
constexpr unsigned O_UM = O_PS + RG * 262144;
constexpr unsigned O_E1 = O_UM + RG * 524288;
constexpr unsigned O_H1 = O_E1 + RG * 131072;
constexpr unsigned O_UX = O_H1 + 4 * MiB;
constexpr unsigned O_D = O_UX + RGX * 2 * MiB, D_SLOT = 524288, D_X2 = 14 * 16384, D_ST = 22 * 16384;
constexpr unsigned O_SL = O_D + 32 * MiB;
constexpr unsigned WS_END = O_SL + 2 * MiB;
static_assert(WS_END <= 256 * MiB && O_UX % (2 * MiB) == 0 && O_E1 % (2 * MiB) == 0, "ws");
__device__ __forceinline__ unsigned nearg(unsigned pol, unsigned off, unsigned h) { return off + (((off >> 21) ^ pol ^ h) & 1) * 8192; }
constexpr int C_ABORT = 0;
constexpr int C_H = 32;
constexpr int C_M = 256;
constexpr int C_A = 512;
constexpr int C_F = 1024;
constexpr unsigned O_HW = 49152;
constexpr int H_C = 0;
constexpr int H_D = 256;
constexpr int H_P = 1024;
__device__ __forceinline__ unsigned hw(unsigned pol, int x) { return nearg(pol, O_HW, x >> 2) + (x & 3) * 2048; }

__device__ __forceinline__ float bf2f(u16 v) { return __uint_as_float(((unsigned)v) << 16); }
__device__ __forceinline__ u16 f2bf(float f) {
    __hip_bfloat16 h = __float2bfloat16(f);
    return __builtin_bit_cast(u16, h);
}
__device__ __forceinline__ float rbf(float f) { return bf2f(f2bf(f)); }

#define CODE_KB 112
#define NPR 8
#define CTC 3
__device__ __forceinline__ void code_touch(unsigned char* lds, int part) {
    u64 pc; const unsigned sz = CODE_KB * 1024;
    asm volatile("s_getpc_b64 %0" : "=s"(pc));
    const v4u r = {(unsigned)pc, (unsigned)(pc >> 32) & 0xFFFFu, sz, 0x00020000u};
    asm volatile("s_mov_b32 m0, %2\n\ts_nop 4\n\tbuffer_load_dword %0, %1, 0 offen lds" :: "v"(((threadIdx.x & 63) + 64 * part) * 128), "s"(r), "s"((unsigned)(size_t)(LAS unsigned char*)lds) : "m0");
}
enum { P_A, P_B, P_EU, P_DMIN, P_R, P_TP, P_TU, P_T0, P_X, P_TZ, P_TF, P_X2, P_T1, P_TS, P_TT, P_TY, P_T2, P_17, NPAR };
#define CH_PAR 152, 72, 8, 4, 60, 108, 300, 90, 36, -30u, 10, 72, 190, 30, 24, 136, 140, 0
struct Args {
    const u16* Wq; const u16* Wk; const u16* Wv; const u16* Wo;
    const u16* c; const u16* w_mod; const u16* mem0;
    const u16* x; const u16* w1; const u16* b1; const u16* w2; const u16* b2;
    u16* out; char* ws;
    unsigned mode, pad0;
};
#define PARS auto pp_ = (__attribute__((address_space(4))) const Args*)__builtin_amdgcn_kernarg_segment_ptr(); asm volatile("" : "+s"(pp_))
constexpr unsigned PARV[NPAR] = {CH_PAR};
#define par(i) PARV[i]
#define RSTAMP(sg, i) do { } while (0)
#define STAMP_WAVE 0
#define DBG(a, i) 0ull
constexpr unsigned SPIN_CAP = 1u << 22;
constexpr unsigned SENT = 0xFFFFFFFFu;

__device__ __forceinline__ void lds_barrier() { asm volatile("s_waitcnt lgkmcnt(0)\n\ts_barrier" ::: "memory"); }
__device__ __forceinline__ void wait_vm0() { asm volatile("s_waitcnt vmcnt(0)" ::: "memory"); }
typedef __bf16 bf16x2_t __attribute__((ext_vector_type(2)));
__device__ __forceinline__ unsigned pack2(float a, float b) { const f32x2 v = {a, b}; return __builtin_bit_cast(unsigned, __builtin_convertvector(v, bf16x2_t)); }
__device__ __forceinline__ float lo16f(unsigned u) { return __uint_as_float(u << 16); }
__device__ __forceinline__ float hi16f(unsigned u) { return __uint_as_float(u & 0xFFFF0000u); }
__device__ __forceinline__ unsigned umax4(v4u x) { return max(max(x.x, x.y), max(x.z, x.w)); }
__device__ __forceinline__ unsigned unsent(unsigned x) { return min(x, 0xFFFFFFFEu); }

__device__ __forceinline__ unsigned ldg4_sc1(const void* sb, unsigned vo) {
    unsigned r;
    asm volatile("global_load_dword %0, %1, %2 sc1\n\ts_waitcnt vmcnt(0)" : "=&v"(r) : "v"(vo), "s"(sb) : "memory");
    return r;
}
__device__ __forceinline__ bool spin_fail(unsigned& spins, unsigned* abortw) {
    if (++spins > SPIN_CAP) { __hip_atomic_store((gu32*)abortw, 1u, RLX_AGENT); return true; }
    if ((spins & 63u) == 0 && __builtin_amdgcn_readfirstlane(__hip_atomic_load((gu32*)abortw, RLX_AGENT)) == 1u) return true;
    return false;
}
__device__ __forceinline__ float red16_add(float x) { auto r = __builtin_amdgcn_permlane16_swap(__float_as_uint(x), __float_as_uint(x), false, false); return __uint_as_float(r[0]) + __uint_as_float(r[1]); }
__device__ __forceinline__ float red32_add(float x) { auto r = __builtin_amdgcn_permlane32_swap(__float_as_uint(x), __float_as_uint(x), false, false); return __uint_as_float(r[0]) + __uint_as_float(r[1]); }
__device__ __forceinline__ v2u tr_rd(const unsigned char* lds_base, unsigned off) {
    return __builtin_bit_cast(v2u, __builtin_amdgcn_ds_read_tr16_b64_v4bf16((LAS bf16x4_t*)(lds_base + off)));
}
__device__ __forceinline__ v4u lds16(const unsigned char* p) { return *(const LAS v4u*)p; }
__device__ __forceinline__ void dma16(const void* src, unsigned char* dst, int aux) {
    if (aux) __builtin_amdgcn_global_load_lds((const unsigned*)src, (unsigned*)dst, 16, 0, 2);
    else __builtin_amdgcn_global_load_lds((const unsigned*)src, (unsigned*)dst, 16, 0, 0);
}

__device__ __forceinline__ v4u mkrs(const void* p, unsigned bytes) { return (v4u){(unsigned)(u64)p, (unsigned)((u64)p >> 32) & 0xFFFFu, bytes, 0x00020000u}; }
template <int AUX> __device__ __forceinline__ void bdma(v4u r, unsigned vo, unsigned so, unsigned ldsaddr) {
    if (AUX == 16) asm volatile("s_mov_b32 m0, %0\n\ts_nop 0\n\tbuffer_load_dwordx4 %1, %2, %3 offen sc1 lds" :: "s"(ldsaddr), "v"(vo), "s"(r), "s"(so) : "m0");
    else if (AUX) asm volatile("s_mov_b32 m0, %0\n\ts_nop 0\n\tbuffer_load_dwordx4 %1, %2, %3 offen nt lds" :: "s"(ldsaddr), "v"(vo), "s"(r), "s"(so) : "m0");
    else asm volatile("s_mov_b32 m0, %0\n\ts_nop 0\n\tbuffer_load_dwordx4 %1, %2, %3 offen lds" :: "s"(ldsaddr), "v"(vo), "s"(r), "s"(so) : "m0");
}

template <int I> struct IC { static constexpr int v = I; };
template <int B, int E, class F> __device__ __forceinline__ void sfor(F&& f) { if constexpr (B < E) { f(IC<B>{}); sfor<B + 1, E>(f); } }
#define SB() __builtin_amdgcn_sched_barrier(0)

__device__ __forceinline__ int lane_id() { int l; asm volatile("v_mbcnt_lo_u32_b32 %0, -1, 0\n\tv_mbcnt_hi_u32_b32 %0, -1, %0" : "=v"(l)); return l; }
constexpr int LDS_BYTES = 163840;
typedef __amdgpu_buffer_rsrc_t rsrc_t;
__device__ __forceinline__ v4u bld16(rsrc_t r, unsigned off) { return __builtin_bit_cast(v4u, __builtin_amdgcn_raw_buffer_load_b128(r, off, 0, 16)); }
__device__ __forceinline__ v2u bld8(rsrc_t r, unsigned off) { return __builtin_bit_cast(v2u, __builtin_amdgcn_raw_buffer_load_b64(r, off, 0, 16)); }
__device__ __forceinline__ void bst16(rsrc_t r, unsigned off, v4u v) { __builtin_amdgcn_raw_buffer_store_b128(__builtin_bit_cast(__attribute__((ext_vector_type(4))) unsigned, v), r, off, 0, 16); }
__device__ __forceinline__ void bst8(rsrc_t r, unsigned off, v2u v) { __builtin_amdgcn_raw_buffer_store_b64(__builtin_bit_cast(__attribute__((ext_vector_type(2))) unsigned, v), r, off, 0, 16); }

constexpr int L_FLAG = LDS_BYTES - 64;
#define otid() (a.pad0 * 64 + lane_id())
__device__ __forceinline__ v4u sld16(rsrc_t r, unsigned vo, unsigned so) { return __builtin_bit_cast(v4u, __builtin_amdgcn_raw_buffer_load_b128(r, vo, so, 16)); }
__device__ __forceinline__ v4u sentv() { unsigned x = SENT; asm volatile("" : "+v"(x)); return (v4u){x, x, x, x}; }
#define BW_SLEEP 16
template <int SLP = BW_SLEEP> __device__ __forceinline__ bool wave_wait(const unsigned* p, int stride, int n, unsigned target, unsigned* abortw, unsigned char* lds = nullptr) {
    const int lane = lane_id();
    for (unsigned spins = 0;;) {
        const unsigned v = lane < n ? __hip_atomic_load((gu32*)(p + lane * stride), RLX_AGENT) : target;
        if (!__any(v < target)) { if (lds && lane == 0) *(volatile LAS unsigned*)(lds + L_FLAG + 32) = v; return true; }
        __builtin_amdgcn_s_sleep(SLP);
        if (++spins > (SPIN_CAP << 1)) { __hip_atomic_store((gu32*)abortw, 1u, RLX_AGENT); return false; }
        if ((spins & 63u) == 0 && __builtin_amdgcn_readfirstlane(__hip_atomic_load((gu32*)abortw, RLX_AGENT)) == 1u) return false;
    }
}
__device__ __forceinline__ void wait_go(const char* ws) {
    for (unsigned spins = 0; !__builtin_amdgcn_readfirstlane(ldg4_sc1(ws, O_GO + blockIdx.x * 128));) {
        __builtin_amdgcn_s_sleep(8);
        if (++spins > SPIN_CAP) asm volatile("s_endpgm");
    }
}
__device__ __forceinline__ unsigned wait_set(const char* ws, unsigned* ctl) {
    if (!wave_wait<16>(ctl + C_A, 32, 8, 16, ctl + C_ABORT)) asm volatile("s_endpgm");
    return __builtin_amdgcn_readfirstlane(ldg4_sc1(ws, O_POL));
}
__device__ __forceinline__ void drained(unsigned* cnt) {
    asm volatile("s_waitcnt vmcnt(0)\n\ts_barrier" ::: "memory");
    if (threadIdx.x == 0) __hip_atomic_fetch_add((gu32*)cnt, 1u, RLX_AGENT);
}

constexpr int L_W = 0;
constexpr int L_H = 98304;
constexpr int L_KX = L_H + 40960;
constexpr int L_QX = L_KX + 8192;
constexpr int L_T = L_QX + 8192;
constexpr int L_ST = L_T + 4096;
constexpr int lsl(bool isV, int c, int n) { return isV ? (c < 6 && (n & 1) ? 2 * c + (n >> 1) : -1) : (c < 12 && n == 1 ? c : -1); }
constexpr int ridx(bool isV, int c, int n) { int r = 0; for (int i = 0; i < 4 * c + n; ++i) r += lsl(isV, i >> 2, i & 3) < 0; return r; }
#define ASM_Q \
    "s_add_u32 m0, s41, 0\ns_xor_b32 s85, s62, 0\ns_add_u32 s85, s85, s40\nbuffer_load_dwordx4 v11, s[64:67], s85 offen sc1 lds\n" \
    "buffer_load_dwordx4 v12, s[64:67], s85 offen offset:1024 sc1 lds\ns_add_u32 m0, s41, 8192\ns_xor_b32 s85, s62, 256\ns_add_u32 s85, s85, s40\n" \
    "buffer_load_dwordx4 v11, s[64:67], s85 offen sc1 lds\nbuffer_load_dwordx4 v12, s[64:67], s85 offen offset:1024 sc1 lds\ns_add_u32 m0, s41, 16384\ns_xor_b32 s85, s62, 512\n" \
    "s_add_u32 s85, s85, s40\nbuffer_load_dwordx4 v11, s[64:67], s85 offen sc1 lds\nbuffer_load_dwordx4 v12, s[64:67], s85 offen offset:1024 sc1 lds\ns_memrealtime s[72:73]\n" \
    "s_waitcnt lgkmcnt(0)\ns_mov_b32 s80, s72\ns_mov_b32 s83, 0x7fff0000\ns_branch .Lq8_%=\n.Lq9_%=:\ns_waitcnt vmcnt(0)\ns_barrier\n.Lq2_%=:\n" \
    "ds_read_b128 v[64:67], v3 offset:0\nds_read_b128 v[68:71], v9 offset:0\nds_read_b128 v[72:75], v9 offset:4096\n" \
    "v_mov_b32_dpp v16, v20 quad_perm:[0,1,2,3] row_mask:0xf bank_mask:0xc\nv_mov_b32_dpp v17, v21 quad_perm:[0,1,2,3] row_mask:0xf bank_mask:0xc\n" \
    "v_mov_b32_dpp v18, v22 quad_perm:[0,1,2,3] row_mask:0xf bank_mask:0xc\nv_mov_b32_dpp v19, v23 quad_perm:[0,1,2,3] row_mask:0xf bank_mask:0xc\nv_mov_b32 v254, 0xff800000\n" \
    "v_cvt_pk_bf16_f32 v16, 0, v16\nv_cvt_pk_bf16_f32 v17, 0, v17\nv_cvt_pk_bf16_f32 v18, 0, v18\nv_cvt_pk_bf16_f32 v19, 0, v19\nv_mul_f32 v16, s33, v16\n" \
    "v_mul_f32 v17, s33, v17\nv_mul_f32 v18, s33, v18\ns_waitcnt lgkmcnt(0)\nv_mfma_f32_16x16x32_bf16 v[48:51], a[8:11], v[68:71], 0\ns_add_u32 m0, s41, 24576\n" \
    "v_mul_f32 v19, s33, v19\nv_cvt_pk_bf16_f32 v16, 0, v16\nv_mfma_f32_16x16x32_bf16 v[52:55], a[8:11], v[72:75], 0\nds_read_b128 v[76:79], v3 offset:1024\n" \
    "v_cvt_pk_bf16_f32 v17, 0, v17\nv_cvt_pk_bf16_f32 v18, 0, v18\nv_mfma_f32_16x16x32_bf16 v[56:59], a[16:19], v[68:71], 0\nds_read_b128 v[80:83], v10 offset:0\n" \
    "v_cvt_pk_bf16_f32 v19, 0, v19\nv_cndmask_b32 v16, v254, v16, s[50:51]\nv_mfma_f32_16x16x32_bf16 v[60:63], a[16:19], v[72:75], 0\nds_read_b128 v[84:87], v10 offset:4096\n" \
    "v_cndmask_b32 v17, v254, v17, s[52:53]\nv_cndmask_b32 v18, v254, v18, s[54:55]\nv_mfma_f32_16x16x32_bf16 v[32:35], a[0:3], v[68:71], 0\ns_xor_b32 s85, s62, 768\n" \
    "v_cndmask_b32 v19, v254, v19, s[56:57]\nv_max3_f32 v254, v16, v17, v18\nv_mfma_f32_16x16x32_bf16 v[36:39], a[0:3], v[72:75], 0\ns_add_u32 s85, s85, s40\n" \
    "v_max_f32 v254, v254, v19\nv_mfma_f32_16x16x32_bf16 v[40:43], v[64:67], v[68:71], 0\nbuffer_load_dwordx4 v11, s[64:67], s85 offen sc1 lds\n" \
    "v_max_f32_dpp v252, v254, v254 row_ror:8 row_mask:0xf bank_mask:0xf\nv_max_f32_dpp v253, v254, v254 row_ror:8 row_mask:0xf bank_mask:0xf\n" \
    "v_mfma_f32_16x16x32_bf16 v[44:47], v[64:67], v[72:75], 0\ns_nop 0\ns_waitcnt lgkmcnt(0)\nv_permlane32_swap_b32 v252, v253\n" \
    "v_mfma_f32_16x16x32_bf16 v[48:51], a[12:15], v[80:83], v[48:51]\nv_max_f32 v252, v252, v253\nv_cvt_pk_bf16_f32 v24, 0, v24\n" \
    "v_mfma_f32_16x16x32_bf16 v[52:55], a[12:15], v[84:87], v[52:55]\nds_read_b128 v[88:91], v3 offset:2048\nv_cvt_pk_bf16_f32 v25, 0, v25\nv_cvt_pk_bf16_f32 v26, 0, v26\n" \
    "v_mfma_f32_16x16x32_bf16 v[56:59], a[20:23], v[80:83], v[56:59]\nds_read_b128 v[64:67], v9 offset:1024\nv_cvt_pk_bf16_f32 v27, 0, v27\nv_mul_f32 v24, s33, v24\n" \
    "v_mfma_f32_16x16x32_bf16 v[60:63], a[20:23], v[84:87], v[60:63]\nds_read_b128 v[68:71], v9 offset:5120\nv_mul_f32 v25, s33, v25\nv_mul_f32 v26, s33, v26\n" \
    "v_mfma_f32_16x16x32_bf16 v[32:35], a[4:7], v[80:83], v[32:35]\nbuffer_load_dwordx4 v12, s[64:67], s85 offen offset:1024 sc1 lds\nv_mul_f32 v27, s33, v27\n" \
    "v_cvt_pk_bf16_f32 v24, 0, v24\nv_mfma_f32_16x16x32_bf16 v[36:39], a[4:7], v[84:87], v[36:39]\ns_add_u32 m0, s41, 32768\nv_cvt_pk_bf16_f32 v25, 0, v25\n" \
    "v_cvt_pk_bf16_f32 v26, 0, v26\nv_mfma_f32_16x16x32_bf16 v[40:43], v[76:79], v[80:83], v[40:43]\ns_xor_b32 s85, s62, 1024\nv_cvt_pk_bf16_f32 v27, 0, v27\n" \
    "v_max3_f32 v254, v252, v24, v25\nv_mfma_f32_16x16x32_bf16 v[44:47], v[76:79], v[84:87], v[44:47]\ns_add_u32 s85, s85, s40\ns_waitcnt lgkmcnt(0)\n" \
    "v_max3_f32 v252, v254, v26, v27\nv_mfma_f32_16x16x32_bf16 v[48:51], a[32:35], v[64:67], v[48:51]\nbuffer_load_dwordx4 v11, s[64:67], s85 offen sc1 lds\n" \
    "v_max3_f32 v253, v254, v26, v27\nv_mfma_f32_16x16x32_bf16 v[52:55], a[32:35], v[68:71], v[52:55]\nds_read_b128 v[72:75], v3 offset:3072\nv_permlane16_swap_b32 v252, v253\n" \
    "v_mfma_f32_16x16x32_bf16 v[56:59], a[40:43], v[64:67], v[56:59]\nds_read_b128 v[76:79], v10 offset:1024\nv_max_f32 v252, v252, v253\nv_sub_f32 v16, v16, v252\n" \
    "v_mfma_f32_16x16x32_bf16 v[60:63], a[40:43], v[68:71], v[60:63]\nds_read_b128 v[80:83], v10 offset:5120\nv_sub_f32 v17, v17, v252\nv_sub_f32 v18, v18, v252\n" \
    "v_mfma_f32_16x16x32_bf16 v[32:35], a[24:27], v[64:67], v[32:35]\nbuffer_load_dwordx4 v12, s[64:67], s85 offen offset:1024 sc1 lds\nv_sub_f32 v19, v19, v252\n" \
    "v_sub_f32 v24, v24, v252\nv_mfma_f32_16x16x32_bf16 v[36:39], a[24:27], v[68:71], v[36:39]\ns_add_u32 s84, s40, 0x20000\nv_sub_f32 v25, v25, v252\nv_sub_f32 v26, v26, v252\n" \
    "v_mfma_f32_16x16x32_bf16 v[40:43], v[88:91], v[64:67], v[40:43]\ns_cmp_lt_u32 s35, 62\ns_cselect_b32 s84, s84, s40\nv_sub_f32 v27, v27, v252\n" \
    "v_mfma_f32_16x16x32_bf16 v[44:47], v[88:91], v[68:71], v[44:47]\ns_waitcnt lgkmcnt(0)\nv_mul_f32 v16, s34, v16\nv_mul_f32 v17, s34, v17\n" \
    "v_mfma_f32_16x16x32_bf16 v[48:51], a[36:39], v[76:79], v[48:51]\nv_mul_f32 v18, s34, v18\nv_mul_f32 v19, s34, v19\n" \
    "v_mfma_f32_16x16x32_bf16 v[52:55], a[36:39], v[80:83], v[52:55]\nds_read_b128 v[64:67], v3 offset:4096\nv_mul_f32 v24, s34, v24\nv_mul_f32 v25, s34, v25\n" \
    "v_mfma_f32_16x16x32_bf16 v[56:59], a[44:47], v[76:79], v[56:59]\nds_read_b128 v[68:71], v9 offset:8192\nv_mul_f32 v26, s34, v26\nv_mul_f32 v27, s34, v27\n" \
    "v_mfma_f32_16x16x32_bf16 v[60:63], a[44:47], v[80:83], v[60:63]\nds_read_b128 v[84:87], v9 offset:12288\nv_exp_f32 v16, v16\n" \
    "v_mfma_f32_16x16x32_bf16 v[32:35], a[28:31], v[76:79], v[32:35]\nv_exp_f32 v17, v17\nv_mfma_f32_16x16x32_bf16 v[36:39], a[28:31], v[80:83], v[36:39]\nv_exp_f32 v18, v18\n" \
    "v_mfma_f32_16x16x32_bf16 v[40:43], v[72:75], v[76:79], v[40:43]\nv_exp_f32 v19, v19\nv_mfma_f32_16x16x32_bf16 v[44:47], v[72:75], v[80:83], v[44:47]\ns_waitcnt lgkmcnt(0)\n" \
    "v_exp_f32 v24, v24\nv_mfma_f32_16x16x32_bf16 v[48:51], a[56:59], v[68:71], v[48:51]\nv_add_f32 v254, v16, v18\nv_add_f32 v255, v17, v19\n" \
    "v_mfma_f32_16x16x32_bf16 v[52:55], a[56:59], v[84:87], v[52:55]\nds_read_b128 v[88:91], v3 offset:5120\nv_exp_f32 v25, v25\n" \
    "v_mfma_f32_16x16x32_bf16 v[56:59], a[64:67], v[68:71], v[56:59]\nds_read_b128 v[72:75], v10 offset:8192\nv_add_f32 v254, v254, v255\nv_exp_f32 v26, v26\n" \
    "v_mfma_f32_16x16x32_bf16 v[60:63], a[64:67], v[84:87], v[60:63]\nds_read_b128 v[76:79], v10 offset:12288\nv_exp_f32 v27, v27\n" \
    "v_mfma_f32_16x16x32_bf16 v[32:35], a[48:51], v[68:71], v[32:35]\nv_add_f32_dpp v252, v254, v254 row_ror:8 row_mask:0xf bank_mask:0xf\n" \
    "v_add_f32_dpp v253, v254, v254 row_ror:8 row_mask:0xf bank_mask:0xf\nv_mfma_f32_16x16x32_bf16 v[36:39], a[48:51], v[84:87], v[36:39]\nv_add_f32 v254, v24, v26\n" \
    "v_add_f32 v255, v25, v27\nv_mfma_f32_16x16x32_bf16 v[40:43], v[64:67], v[68:71], v[40:43]\nv_permlane32_swap_b32 v252, v253\n" \
    "v_mfma_f32_16x16x32_bf16 v[44:47], v[64:67], v[84:87], v[44:47]\ns_waitcnt lgkmcnt(0)\nv_add_f32 v254, v254, v252\nv_add_f32 v255, v255, v253\n" \
    "v_mfma_f32_16x16x32_bf16 v[48:51], a[60:63], v[72:75], v[48:51]\nv_add_f32 v252, v254, v255\nv_add_f32 v253, v255, v254\n" \
    "v_mfma_f32_16x16x32_bf16 v[52:55], a[60:63], v[76:79], v[52:55]\nds_read_b128 v[64:67], v3 offset:6144\nv_permlane16_swap_b32 v252, v253\n" \
    "v_mfma_f32_16x16x32_bf16 v[56:59], a[68:71], v[72:75], v[56:59]\nds_read_b128 v[68:71], v9 offset:9216\nv_add_f32 v252, v252, v253\nv_rcp_f32 v252, v252\n" \
    "v_mfma_f32_16x16x32_bf16 v[60:63], a[68:71], v[76:79], v[60:63]\nds_read_b128 v[80:83], v9 offset:13312\nv_mul_f32 v16, v16, v252\nv_mul_f32 v17, v17, v252\n" \
    "v_mfma_f32_16x16x32_bf16 v[32:35], a[52:55], v[72:75], v[32:35]\nv_mul_f32 v24, v24, v252\nv_mul_f32 v25, v25, v252\n" \
    "v_mfma_f32_16x16x32_bf16 v[36:39], a[52:55], v[76:79], v[36:39]\nv_cvt_pk_bf16_f32 v20, v16, v17\nv_cvt_pk_bf16_f32 v22, v24, v25\n" \
    "v_mfma_f32_16x16x32_bf16 v[40:43], v[88:91], v[72:75], v[40:43]\nv_mul_f32 v18, v18, v252\nv_mul_f32 v19, v19, v252\n" \
    "v_mfma_f32_16x16x32_bf16 v[44:47], v[88:91], v[76:79], v[44:47]\ns_waitcnt lgkmcnt(0)\nv_mul_f32 v26, v26, v252\nv_mul_f32 v27, v27, v252\n" \
    "v_mfma_f32_16x16x32_bf16 v[48:51], a[80:83], v[68:71], v[48:51]\nv_cvt_pk_bf16_f32 v21, v18, v19\nv_cvt_pk_bf16_f32 v23, v26, v27\n" \
    "v_mfma_f32_16x16x32_bf16 v[52:55], a[80:83], v[80:83], v[52:55]\nds_read_b128 v[72:75], v3 offset:7168\nv_permlane32_swap_b32 v20, v22\n" \
    "v_mfma_f32_16x16x32_bf16 v[56:59], a[88:91], v[68:71], v[56:59]\nds_read_b128 v[76:79], v10 offset:9216\nv_permlane32_swap_b32 v21, v23\n" \
    "v_mfma_f32_16x16x32_bf16 v[60:63], a[88:91], v[80:83], v[60:63]\nds_read_b128 v[84:87], v10 offset:13312\ns_xor_b32 s74, s83, 0x2000\n" \
    "v_mfma_f32_16x16x32_bf16 v[32:35], a[72:75], v[68:71], v[32:35]\nbuffer_store_dwordx4 v[20:23], v7, s[64:67], s83 offen sc1\n" \
    "v_mfma_f32_16x16x32_bf16 v[36:39], a[72:75], v[80:83], v[36:39]\nbuffer_store_dwordx4 v[20:23], v7, s[64:67], s74 offen sc1\nv_mov_b64 v[28:29], -1\n" \
    "v_mov_b64 v[30:31], -1\nv_mfma_f32_16x16x32_bf16 v[40:43], v[64:67], v[68:71], v[40:43]\ns_add_u32 s69, s35, 2\ns_and_b32 s69, s69, 15\n" \
    "v_mfma_f32_16x16x32_bf16 v[44:47], v[64:67], v[80:83], v[44:47]\ns_lshl_b32 s69, s69, 18\ns_add_u32 s82, s38, s69\ns_waitcnt lgkmcnt(0)\n" \
    "v_mfma_f32_16x16x32_bf16 v[48:51], a[84:87], v[76:79], v[48:51]\ns_lshr_b32 s69, s82, 8\ns_and_b32 s69, s69, 0x2000\n" \
    "v_mfma_f32_16x16x32_bf16 v[52:55], a[84:87], v[84:87], v[52:55]\nds_read_b128 v[80:83], v3 offset:8192\ns_xor_b32 s82, s82, s69\n" \
    "v_mfma_f32_16x16x32_bf16 v[56:59], a[92:95], v[76:79], v[56:59]\nds_read_b128 v[88:91], v9 offset:16384\ns_add_u32 s69, s35, 2\n" \
    "v_mfma_f32_16x16x32_bf16 v[60:63], a[92:95], v[84:87], v[60:63]\nds_read_b128 v[64:67], v9 offset:20480\ns_and_b32 s69, s69, 15\n" \
    "v_mfma_f32_16x16x32_bf16 v[32:35], a[76:79], v[76:79], v[32:35]\ns_lshl_b32 s69, s69, 18\ns_add_u32 s83, s39, s69\n" \
    "v_mfma_f32_16x16x32_bf16 v[36:39], a[76:79], v[84:87], v[36:39]\ns_xor_b32 s74, s82, 0x4000\ns_xor_b32 s75, s83, 0x2000\n" \
    "v_mfma_f32_16x16x32_bf16 v[40:43], v[72:75], v[76:79], v[40:43]\nv_mfma_f32_16x16x32_bf16 v[44:47], v[72:75], v[84:87], v[44:47]\ns_waitcnt lgkmcnt(0)\n" \
    "v_mfma_f32_16x16x32_bf16 v[48:51], a[104:107], v[88:91], v[48:51]\nv_mfma_f32_16x16x32_bf16 v[52:55], a[104:107], v[64:67], v[52:55]\n" \
    "ds_read_b128 v[84:87], v3 offset:9216\nv_mfma_f32_16x16x32_bf16 v[56:59], a[112:115], v[88:91], v[56:59]\nds_read_b128 v[68:71], v10 offset:16384\n" \
    "v_mfma_f32_16x16x32_bf16 v[60:63], a[112:115], v[64:67], v[60:63]\nds_read_b128 v[72:75], v10 offset:20480\nbuffer_store_dwordx4 v[28:31], v7, s[64:67], s82 offen sc1\n" \
    "v_mfma_f32_16x16x32_bf16 v[32:35], a[96:99], v[88:91], v[32:35]\nv_mfma_f32_16x16x32_bf16 v[36:39], a[96:99], v[64:67], v[36:39]\n" \
    "v_mfma_f32_16x16x32_bf16 v[40:43], v[80:83], v[88:91], v[40:43]\nv_mfma_f32_16x16x32_bf16 v[44:47], v[80:83], v[64:67], v[44:47]\ns_waitcnt lgkmcnt(0)\n" \
    "v_mfma_f32_16x16x32_bf16 v[48:51], a[108:111], v[68:71], v[48:51]\nv_mfma_f32_16x16x32_bf16 v[52:55], a[108:111], v[72:75], v[52:55]\n" \
    "ds_read_b128 v[64:67], v3 offset:10240\nbuffer_store_dwordx4 v[28:31], v7, s[64:67], s74 offen sc1\nv_mfma_f32_16x16x32_bf16 v[56:59], a[116:119], v[68:71], v[56:59]\n" \
    "ds_read_b128 v[88:91], v9 offset:17408\nv_mfma_f32_16x16x32_bf16 v[60:63], a[116:119], v[72:75], v[60:63]\nds_read_b128 v[76:79], v9 offset:21504\n" \
    "v_mfma_f32_16x16x32_bf16 v[32:35], a[100:103], v[68:71], v[32:35]\nv_mfma_f32_16x16x32_bf16 v[36:39], a[100:103], v[72:75], v[36:39]\n" \
    "v_mfma_f32_16x16x32_bf16 v[40:43], v[84:87], v[68:71], v[40:43]\nv_mfma_f32_16x16x32_bf16 v[44:47], v[84:87], v[72:75], v[44:47]\n" \
    "buffer_store_dwordx4 v[28:31], v7, s[64:67], s83 offen sc1\ns_waitcnt lgkmcnt(0)\nv_mfma_f32_16x16x32_bf16 v[48:51], a[128:131], v[88:91], v[48:51]\n" \
    "v_mfma_f32_16x16x32_bf16 v[52:55], a[128:131], v[76:79], v[52:55]\nds_read_b128 v[68:71], v3 offset:11264\n" \
    "v_mfma_f32_16x16x32_bf16 v[56:59], a[136:139], v[88:91], v[56:59]\nds_read_b128 v[72:75], v10 offset:17408\n" \
    "v_mfma_f32_16x16x32_bf16 v[60:63], a[136:139], v[76:79], v[60:63]\nds_read_b128 v[80:83], v10 offset:21504\n" \
    "v_mfma_f32_16x16x32_bf16 v[32:35], a[120:123], v[88:91], v[32:35]\nv_mfma_f32_16x16x32_bf16 v[36:39], a[120:123], v[76:79], v[36:39]\n" \
    "buffer_store_dwordx4 v[28:31], v7, s[64:67], s75 offen sc1\nv_lshrrev_b32 v16, 1, v2\nv_add_u32 v16, s60, v16\n" \
    "v_mfma_f32_16x16x32_bf16 v[40:43], v[64:67], v[88:91], v[40:43]\nds_write_b64 v16, v[28:29]\nv_mfma_f32_16x16x32_bf16 v[44:47], v[64:67], v[76:79], v[44:47]\n" \
    "ds_write_b64 v16, v[28:29] offset:2048\ns_add_u32 s69, s35, 2\ns_waitcnt lgkmcnt(2)\nv_mfma_f32_16x16x32_bf16 v[48:51], a[132:135], v[72:75], v[48:51]\n" \
    "s_and_b32 s69, s69, 255\ns_lshl_b32 s69, s69, 19\nv_mfma_f32_16x16x32_bf16 v[52:55], a[132:135], v[80:83], v[52:55]\nds_read_b128 v[64:67], v3 offset:12288\n" \
    "s_add_u32 s74, s63, s69\ns_waitcnt vmcnt(6)\ns_barrier\nv_mfma_f32_16x16x32_bf16 v[56:59], a[140:143], v[72:75], v[56:59]\nds_read_b128 v[76:79], v9 offset:24576\n" \
    "s_cmp_lt_u32 s35, 62\ns_cselect_b32 s74, s74, 0x7fff0000\nv_mfma_f32_16x16x32_bf16 v[60:63], a[140:143], v[80:83], v[60:63]\nds_read_b128 v[84:87], v9 offset:28672\n" \
    "s_add_u32 m0, s41, 0\nv_mfma_f32_16x16x32_bf16 v[32:35], a[124:127], v[72:75], v[32:35]\ns_xor_b32 s85, s62, 1280\ns_add_u32 s85, s85, s40\n" \
    "v_mfma_f32_16x16x32_bf16 v[36:39], a[124:127], v[80:83], v[36:39]\nbuffer_load_dwordx4 v11, s[64:67], s85 offen sc1 lds\n" \
    "v_mfma_f32_16x16x32_bf16 v[40:43], v[68:71], v[72:75], v[40:43]\nv_mfma_f32_16x16x32_bf16 v[44:47], v[68:71], v[80:83], v[44:47]\ns_waitcnt lgkmcnt(0)\n" \
    "v_mfma_f32_16x16x32_bf16 v[48:51], a[152:155], v[76:79], v[48:51]\nbuffer_load_dwordx4 v12, s[64:67], s85 offen offset:1024 sc1 lds\ns_add_u32 m0, s41, 8192\n" \
    "v_mfma_f32_16x16x32_bf16 v[52:55], a[152:155], v[84:87], v[52:55]\nds_read_b128 v[80:83], v3 offset:13312\ns_xor_b32 s85, s62, 1536\n" \
    "v_mfma_f32_16x16x32_bf16 v[56:59], a[160:163], v[76:79], v[56:59]\nds_read_b128 v[88:91], v10 offset:24576\ns_add_u32 s85, s85, s40\n" \
    "v_mfma_f32_16x16x32_bf16 v[60:63], a[160:163], v[84:87], v[60:63]\nds_read_b128 v[68:71], v10 offset:28672\nbuffer_load_dwordx4 v11, s[64:67], s85 offen sc1 lds\n" \
    "v_mfma_f32_16x16x32_bf16 v[32:35], a[144:147], v[76:79], v[32:35]\nbuffer_store_dwordx4 v[28:31], v2, s[64:67], s74 offen offset:0 sc1\n" \
    "v_mfma_f32_16x16x32_bf16 v[36:39], a[144:147], v[84:87], v[36:39]\nv_mfma_f32_16x16x32_bf16 v[40:43], v[64:67], v[76:79], v[40:43]\n" \
    "buffer_load_dwordx4 v12, s[64:67], s85 offen offset:1024 sc1 lds\nv_mfma_f32_16x16x32_bf16 v[44:47], v[64:67], v[84:87], v[44:47]\ns_waitcnt lgkmcnt(0)\n" \
    "v_mfma_f32_16x16x32_bf16 v[48:51], a[156:159], v[88:91], v[48:51]\nv_mfma_f32_16x16x32_bf16 v[52:55], a[156:159], v[68:71], v[52:55]\n" \
    "ds_read_b128 v[64:67], v3 offset:14336\nv_mfma_f32_16x16x32_bf16 v[56:59], a[164:167], v[88:91], v[56:59]\nds_read_b128 v[84:87], v9 offset:25600\n" \
    "buffer_store_dwordx4 v[28:31], v2, s[64:67], s74 offen offset:1024 sc1\nv_mfma_f32_16x16x32_bf16 v[60:63], a[164:167], v[68:71], v[60:63]\n" \
    "ds_read_b128 v[72:75], v9 offset:29696\ns_and_b32 s69, s35, 15\nv_mfma_f32_16x16x32_bf16 v[32:35], a[148:151], v[88:91], v[32:35]\ns_lshl_b32 s69, s69, 18\n" \
    "s_add_u32 s82, s38, s69\nv_mfma_f32_16x16x32_bf16 v[36:39], a[148:151], v[68:71], v[36:39]\ns_lshr_b32 s69, s82, 8\ns_and_b32 s69, s69, 0x2000\n" \
    "v_mfma_f32_16x16x32_bf16 v[40:43], v[80:83], v[88:91], v[40:43]\ns_xor_b32 s82, s82, s69\ns_and_b32 s69, s35, 15\n" \
    "v_mfma_f32_16x16x32_bf16 v[44:47], v[80:83], v[68:71], v[44:47]\ns_lshl_b32 s69, s69, 18\ns_add_u32 s83, s39, s69\ns_waitcnt lgkmcnt(0)\n" \
    "v_mfma_f32_16x16x32_bf16 v[48:51], a[176:179], v[84:87], v[48:51]\nv_mfma_f32_16x16x32_bf16 v[52:55], a[176:179], v[72:75], v[52:55]\n" \
    "ds_read_b128 v[68:71], v3 offset:15360\nv_mfma_f32_16x16x32_bf16 v[56:59], a[184:187], v[84:87], v[56:59]\nds_read_b128 v[88:91], v10 offset:25600\n" \
    "v_mfma_f32_16x16x32_bf16 v[60:63], a[184:187], v[72:75], v[60:63]\nds_read_b128 v[76:79], v10 offset:29696\n" \
    "v_mfma_f32_16x16x32_bf16 v[32:35], a[168:171], v[84:87], v[32:35]\nv_mfma_f32_16x16x32_bf16 v[36:39], a[168:171], v[72:75], v[36:39]\n" \
    "v_mfma_f32_16x16x32_bf16 v[40:43], v[64:67], v[84:87], v[40:43]\nv_mfma_f32_16x16x32_bf16 v[44:47], v[64:67], v[72:75], v[44:47]\ns_waitcnt lgkmcnt(0)\n" \
    "v_mfma_f32_16x16x32_bf16 v[48:51], a[180:183], v[88:91], v[48:51]\nv_mfma_f32_16x16x32_bf16 v[52:55], a[180:183], v[76:79], v[52:55]\n" \
    "ds_read_b128 v[64:67], v3 offset:16384\nv_mfma_f32_16x16x32_bf16 v[56:59], a[188:191], v[88:91], v[56:59]\nds_read_b128 v[72:75], v9 offset:32768\n" \
    "v_mfma_f32_16x16x32_bf16 v[60:63], a[188:191], v[76:79], v[60:63]\nds_read_b128 v[80:83], v9 offset:36864\n" \
    "v_mfma_f32_16x16x32_bf16 v[32:35], a[172:175], v[88:91], v[32:35]\nv_mfma_f32_16x16x32_bf16 v[36:39], a[172:175], v[76:79], v[36:39]\n" \
    "v_mfma_f32_16x16x32_bf16 v[40:43], v[68:71], v[88:91], v[40:43]\nv_mfma_f32_16x16x32_bf16 v[44:47], v[68:71], v[76:79], v[44:47]\ns_waitcnt lgkmcnt(0)\n" \
    "v_mfma_f32_16x16x32_bf16 v[48:51], a[200:203], v[72:75], v[48:51]\nv_mfma_f32_16x16x32_bf16 v[52:55], a[200:203], v[80:83], v[52:55]\n" \
    "ds_read_b128 v[68:71], v3 offset:17408\nv_mfma_f32_16x16x32_bf16 v[56:59], a[208:211], v[72:75], v[56:59]\nds_read_b128 v[76:79], v10 offset:32768\n" \
    "v_mfma_f32_16x16x32_bf16 v[60:63], a[208:211], v[80:83], v[60:63]\nds_read_b128 v[84:87], v10 offset:36864\n" \
    "v_mfma_f32_16x16x32_bf16 v[32:35], a[192:195], v[72:75], v[32:35]\nv_mfma_f32_16x16x32_bf16 v[36:39], a[192:195], v[80:83], v[36:39]\n" \
    "v_mfma_f32_16x16x32_bf16 v[40:43], v[64:67], v[72:75], v[40:43]\nv_mfma_f32_16x16x32_bf16 v[44:47], v[64:67], v[80:83], v[44:47]\ns_waitcnt lgkmcnt(0)\n" \
    "v_mfma_f32_16x16x32_bf16 v[48:51], a[204:207], v[76:79], v[48:51]\nv_mfma_f32_16x16x32_bf16 v[52:55], a[204:207], v[84:87], v[52:55]\n" \
    "ds_read_b128 v[64:67], v3 offset:18432\nv_mfma_f32_16x16x32_bf16 v[56:59], a[212:215], v[76:79], v[56:59]\nds_read_b128 v[72:75], v9 offset:33792\n" \
    "v_mfma_f32_16x16x32_bf16 v[60:63], a[212:215], v[84:87], v[60:63]\nds_read_b128 v[80:83], v9 offset:37888\n" \
    "v_mfma_f32_16x16x32_bf16 v[32:35], a[196:199], v[76:79], v[32:35]\nv_mfma_f32_16x16x32_bf16 v[36:39], a[196:199], v[84:87], v[36:39]\n" \
    "v_mfma_f32_16x16x32_bf16 v[40:43], v[68:71], v[76:79], v[40:43]\nv_mfma_f32_16x16x32_bf16 v[44:47], v[68:71], v[84:87], v[44:47]\ns_waitcnt lgkmcnt(0)\n" \
    "v_mfma_f32_16x16x32_bf16 v[48:51], a[224:227], v[72:75], v[48:51]\nv_mfma_f32_16x16x32_bf16 v[52:55], a[224:227], v[80:83], v[52:55]\n" \
    "ds_read_b128 v[68:71], v3 offset:19456\nv_mfma_f32_16x16x32_bf16 v[56:59], a[232:235], v[72:75], v[56:59]\nds_read_b128 v[76:79], v10 offset:33792\n" \
    "v_mfma_f32_16x16x32_bf16 v[60:63], a[232:235], v[80:83], v[60:63]\nds_read_b128 v[84:87], v10 offset:37888\n" \
    "v_mfma_f32_16x16x32_bf16 v[32:35], a[216:219], v[72:75], v[32:35]\nv_mfma_f32_16x16x32_bf16 v[36:39], a[216:219], v[80:83], v[36:39]\n" \
    "v_mfma_f32_16x16x32_bf16 v[40:43], v[64:67], v[72:75], v[40:43]\nv_mfma_f32_16x16x32_bf16 v[44:47], v[64:67], v[80:83], v[44:47]\ns_waitcnt lgkmcnt(0)\n" \
    "v_mfma_f32_16x16x32_bf16 v[48:51], a[228:231], v[76:79], v[48:51]\nv_mfma_f32_16x16x32_bf16 v[52:55], a[228:231], v[84:87], v[52:55]\n" \
    "ds_read_b128 v[64:67], v3 offset:20480\ns_waitcnt vmcnt(1)\ns_barrier\nv_mfma_f32_16x16x32_bf16 v[56:59], a[236:239], v[76:79], v[56:59]\n" \
    "ds_read_b128 v[72:75], v9 offset:0\nv_mfma_f32_16x16x32_bf16 v[60:63], a[236:239], v[84:87], v[60:63]\nds_read_b128 v[80:83], v9 offset:4096\ns_add_u32 m0, s41, 16384\n" \
    "v_mfma_f32_16x16x32_bf16 v[32:35], a[220:223], v[76:79], v[32:35]\ns_xor_b32 s85, s62, 1792\ns_add_u32 s85, s85, s40\n" \
    "v_mfma_f32_16x16x32_bf16 v[36:39], a[220:223], v[84:87], v[36:39]\nbuffer_load_dwordx4 v11, s[64:67], s85 offen sc1 lds\n" \
    "v_mfma_f32_16x16x32_bf16 v[40:43], v[68:71], v[76:79], v[40:43]\nv_mfma_f32_16x16x32_bf16 v[44:47], v[68:71], v[84:87], v[44:47]\ns_waitcnt lgkmcnt(0)\n" \
    "v_mfma_f32_16x16x32_bf16 v[48:51], a[248:251], v[72:75], v[48:51]\nbuffer_load_dwordx4 v12, s[64:67], s85 offen offset:1024 sc1 lds\n" \
    "v_mfma_f32_16x16x32_bf16 v[52:55], a[248:251], v[80:83], v[52:55]\nds_read_b128 v[68:71], v3 offset:21504\nv_mfma_f32_16x16x32_bf16 v[56:59], v[92:95], v[72:75], v[56:59]\n" \
    "ds_read_b128 v[76:79], v10 offset:0\nv_mfma_f32_16x16x32_bf16 v[60:63], v[92:95], v[80:83], v[60:63]\nds_read_b128 v[84:87], v10 offset:4096\n" \
    "v_mfma_f32_16x16x32_bf16 v[32:35], a[240:243], v[72:75], v[32:35]\nv_mfma_f32_16x16x32_bf16 v[36:39], a[240:243], v[80:83], v[36:39]\n" \
    "v_mfma_f32_16x16x32_bf16 v[40:43], v[64:67], v[72:75], v[40:43]\nv_mfma_f32_16x16x32_bf16 v[44:47], v[64:67], v[80:83], v[44:47]\ns_waitcnt lgkmcnt(0)\n" \
    "v_mfma_f32_16x16x32_bf16 v[48:51], a[252:255], v[76:79], v[48:51]\nv_mfma_f32_16x16x32_bf16 v[52:55], a[252:255], v[84:87], v[52:55]\n" \
    "ds_read_b128 v[64:67], v3 offset:22528\nv_mfma_f32_16x16x32_bf16 v[56:59], v[96:99], v[76:79], v[56:59]\nds_read_b128 v[72:75], v9 offset:1024\n" \
    "v_mfma_f32_16x16x32_bf16 v[60:63], v[96:99], v[84:87], v[60:63]\nds_read_b128 v[80:83], v9 offset:5120\nv_mfma_f32_16x16x32_bf16 v[32:35], a[244:247], v[76:79], v[32:35]\n" \
    "v_mfma_f32_16x16x32_bf16 v[36:39], a[244:247], v[84:87], v[36:39]\nv_mfma_f32_16x16x32_bf16 v[40:43], v[68:71], v[76:79], v[40:43]\n" \
    "v_mfma_f32_16x16x32_bf16 v[44:47], v[68:71], v[84:87], v[44:47]\ns_waitcnt lgkmcnt(0)\nv_mfma_f32_16x16x32_bf16 v[48:51], v[108:111], v[72:75], v[48:51]\n" \
    "v_mfma_f32_16x16x32_bf16 v[52:55], v[108:111], v[80:83], v[52:55]\nds_read_b128 v[68:71], v3 offset:23552\n" \
    "v_mfma_f32_16x16x32_bf16 v[56:59], v[116:119], v[72:75], v[56:59]\nds_read_b128 v[76:79], v10 offset:1024\n" \
    "v_mfma_f32_16x16x32_bf16 v[60:63], v[116:119], v[80:83], v[60:63]\nds_read_b128 v[84:87], v10 offset:5120\n" \
    "v_mfma_f32_16x16x32_bf16 v[32:35], v[100:103], v[72:75], v[32:35]\nv_mfma_f32_16x16x32_bf16 v[36:39], v[100:103], v[80:83], v[36:39]\n" \
    "v_mfma_f32_16x16x32_bf16 v[40:43], v[64:67], v[72:75], v[40:43]\nv_mfma_f32_16x16x32_bf16 v[44:47], v[64:67], v[80:83], v[44:47]\ns_waitcnt lgkmcnt(0)\n" \
    "v_mfma_f32_16x16x32_bf16 v[48:51], v[112:115], v[76:79], v[48:51]\nv_mfma_f32_16x16x32_bf16 v[52:55], v[112:115], v[84:87], v[52:55]\n" \
    "v_mfma_f32_16x16x32_bf16 v[56:59], v[120:123], v[76:79], v[56:59]\nds_read_b128 v[64:67], v9 offset:8192\n" \
    "v_mfma_f32_16x16x32_bf16 v[60:63], v[120:123], v[84:87], v[60:63]\nds_read_b128 v[72:75], v9 offset:12288\n" \
    "v_mfma_f32_16x16x32_bf16 v[32:35], v[104:107], v[76:79], v[32:35]\nv_mfma_f32_16x16x32_bf16 v[36:39], v[104:107], v[84:87], v[36:39]\n" \
    "v_mfma_f32_16x16x32_bf16 v[40:43], v[68:71], v[76:79], v[40:43]\nv_mfma_f32_16x16x32_bf16 v[44:47], v[68:71], v[84:87], v[44:47]\ns_waitcnt lgkmcnt(0)\n" \
    "v_mfma_f32_16x16x32_bf16 v[48:51], v[140:143], v[64:67], v[48:51]\nv_mfma_f32_16x16x32_bf16 v[52:55], v[140:143], v[72:75], v[52:55]\n" \
    "v_mfma_f32_16x16x32_bf16 v[56:59], v[148:151], v[64:67], v[56:59]\nds_read_b128 v[68:71], v10 offset:8192\n" \
    "v_mfma_f32_16x16x32_bf16 v[60:63], v[148:151], v[72:75], v[60:63]\nds_read_b128 v[76:79], v10 offset:12288\n" \
    "v_mfma_f32_16x16x32_bf16 v[32:35], v[124:127], v[64:67], v[32:35]\nv_mfma_f32_16x16x32_bf16 v[36:39], v[124:127], v[72:75], v[36:39]\n" \
    "v_mfma_f32_16x16x32_bf16 v[40:43], v[132:135], v[64:67], v[40:43]\nv_mfma_f32_16x16x32_bf16 v[44:47], v[132:135], v[72:75], v[44:47]\ns_waitcnt lgkmcnt(0)\n" \
    "v_mfma_f32_16x16x32_bf16 v[48:51], v[144:147], v[68:71], v[48:51]\nbuffer_load_dword v13, off, s[64:67], s42 sc1\ns_and_b32 s69, s35, 15\n" \
    "v_mfma_f32_16x16x32_bf16 v[52:55], v[144:147], v[76:79], v[52:55]\ns_lshl_b32 s69, s69, 17\ns_add_u32 s81, s37, s69\n" \
    "v_mfma_f32_16x16x32_bf16 v[56:59], v[152:155], v[68:71], v[56:59]\nds_read_b128 v[64:67], v9 offset:9216\ns_lshr_b32 s69, s81, 8\n" \
    "v_mfma_f32_16x16x32_bf16 v[60:63], v[152:155], v[76:79], v[60:63]\nds_read_b128 v[72:75], v9 offset:13312\ns_and_b32 s69, s69, 0x2000\n" \
    "v_mfma_f32_16x16x32_bf16 v[32:35], v[128:131], v[68:71], v[32:35]\ns_xor_b32 s81, s81, s69\ns_mov_b32 s68, 0\n" \
    "v_mfma_f32_16x16x32_bf16 v[36:39], v[128:131], v[76:79], v[36:39]\nv_mfma_f32_16x16x32_bf16 v[40:43], v[136:139], v[68:71], v[40:43]\n" \
    "v_mfma_f32_16x16x32_bf16 v[44:47], v[136:139], v[76:79], v[44:47]\ns_waitcnt lgkmcnt(0)\nv_mfma_f32_16x16x32_bf16 v[48:51], v[172:175], v[64:67], v[48:51]\n" \
    "v_mfma_f32_16x16x32_bf16 v[52:55], v[172:175], v[72:75], v[52:55]\nv_mfma_f32_16x16x32_bf16 v[56:59], v[180:183], v[64:67], v[56:59]\n" \
    "ds_read_b128 v[68:71], v10 offset:9216\nv_mfma_f32_16x16x32_bf16 v[60:63], v[180:183], v[72:75], v[60:63]\nds_read_b128 v[76:79], v10 offset:13312\n" \
    "v_mfma_f32_16x16x32_bf16 v[32:35], v[156:159], v[64:67], v[32:35]\nv_mfma_f32_16x16x32_bf16 v[36:39], v[156:159], v[72:75], v[36:39]\n" \
    "v_mfma_f32_16x16x32_bf16 v[40:43], v[164:167], v[64:67], v[40:43]\nv_mfma_f32_16x16x32_bf16 v[44:47], v[164:167], v[72:75], v[44:47]\ns_waitcnt lgkmcnt(0)\n" \
    "v_mfma_f32_16x16x32_bf16 v[48:51], v[176:179], v[68:71], v[48:51]\nv_mfma_f32_16x16x32_bf16 v[52:55], v[176:179], v[76:79], v[52:55]\ns_waitcnt vmcnt(1)\ns_barrier\n" \
    "v_mfma_f32_16x16x32_bf16 v[56:59], v[184:187], v[68:71], v[56:59]\nds_read_b128 v[64:67], v9 offset:16384\n" \
    "v_mfma_f32_16x16x32_bf16 v[60:63], v[184:187], v[76:79], v[60:63]\nds_read_b128 v[72:75], v9 offset:20480\ns_add_u32 m0, s41, 0\n" \
    "v_mfma_f32_16x16x32_bf16 v[32:35], v[160:163], v[68:71], v[32:35]\ns_xor_b32 s85, s62, 0\ns_add_u32 s85, s85, s84\n" \
    "v_mfma_f32_16x16x32_bf16 v[36:39], v[160:163], v[76:79], v[36:39]\nbuffer_load_dwordx4 v11, s[64:67], s85 offen sc1 lds\n" \
    "v_mfma_f32_16x16x32_bf16 v[40:43], v[168:171], v[68:71], v[40:43]\nv_mfma_f32_16x16x32_bf16 v[44:47], v[168:171], v[76:79], v[44:47]\ns_waitcnt lgkmcnt(0)\n" \
    "v_mfma_f32_16x16x32_bf16 v[48:51], v[204:207], v[64:67], v[48:51]\nbuffer_load_dwordx4 v12, s[64:67], s85 offen offset:1024 sc1 lds\n" \
    "v_mfma_f32_16x16x32_bf16 v[52:55], v[204:207], v[72:75], v[52:55]\nv_mfma_f32_16x16x32_bf16 v[56:59], v[212:215], v[64:67], v[56:59]\n" \
    "ds_read_b128 v[68:71], v10 offset:16384\nv_mfma_f32_16x16x32_bf16 v[60:63], v[212:215], v[72:75], v[60:63]\nds_read_b128 v[76:79], v10 offset:20480\n" \
    "v_mfma_f32_16x16x32_bf16 v[32:35], v[188:191], v[64:67], v[32:35]\ns_waitcnt vmcnt(2)\nv_readfirstlane_b32 s74, v13\n" \
    "v_mfma_f32_16x16x32_bf16 v[36:39], v[188:191], v[72:75], v[36:39]\ns_and_b32 s75, s74, 127\ns_lshr_b32 s69, s74, 8\n" \
    "v_mfma_f32_16x16x32_bf16 v[40:43], v[196:199], v[64:67], v[40:43]\ns_sub_u32 s69, s69, s80\ns_bfe_i32 s69, s69, 0x180000\n" \
    "v_mfma_f32_16x16x32_bf16 v[44:47], v[196:199], v[72:75], v[44:47]\ns_cmp_eq_u32 s75, 0\ns_cselect_b32 s69, 0, s69\ns_waitcnt lgkmcnt(0)\n" \
    "v_mfma_f32_16x16x32_bf16 v[48:51], v[208:211], v[68:71], v[48:51]\ns_add_u32 s80, s80, s69\ns_lshl_b32 s74, s74, 5\n" \
    "v_mfma_f32_16x16x32_bf16 v[52:55], v[208:211], v[76:79], v[52:55]\ns_and_b32 s74, s74, 0x1000\ns_andn2_b32 s81, s81, 0x1000\n" \
    "v_mfma_f32_16x16x32_bf16 v[56:59], v[216:219], v[68:71], v[56:59]\nds_read_b128 v[64:67], v9 offset:17408\ns_or_b32 s81, s81, s74\n" \
    "v_mfma_f32_16x16x32_bf16 v[60:63], v[216:219], v[76:79], v[60:63]\nds_read_b128 v[72:75], v9 offset:21504\ns_lshl_b32 s74, s74, 2\n" \
    "v_mfma_f32_16x16x32_bf16 v[32:35], v[192:195], v[68:71], v[32:35]\ns_andn2_b32 s82, s82, 0x4000\ns_or_b32 s82, s82, s74\n" \
    "v_mfma_f32_16x16x32_bf16 v[36:39], v[192:195], v[76:79], v[36:39]\nv_mfma_f32_16x16x32_bf16 v[40:43], v[200:203], v[68:71], v[40:43]\n" \
    "ds_read_b128 v[68:71], v10 offset:17408\nv_mfma_f32_16x16x32_bf16 v[44:47], v[200:203], v[76:79], v[44:47]\nds_read_b128 v[76:79], v10 offset:21504\ns_waitcnt lgkmcnt(2)\n" \
    "v_mfma_f32_16x16x32_bf16 v[48:51], v[236:239], v[64:67], v[48:51]\nv_mfma_f32_16x16x32_bf16 v[52:55], v[236:239], v[72:75], v[52:55]\n" \
    "v_mfma_f32_16x16x32_bf16 v[56:59], v[244:247], v[64:67], v[56:59]\nv_mfma_f32_16x16x32_bf16 v[60:63], v[244:247], v[72:75], v[60:63]\ns_waitcnt lgkmcnt(0)\ns_barrier\n" \
    "v_mfma_f32_16x16x32_bf16 v[48:51], v[240:243], v[68:71], v[48:51]\ns_add_u32 m0, s41, 8192\ns_xor_b32 s85, s62, 256\n" \
    "v_mfma_f32_16x16x32_bf16 v[52:55], v[240:243], v[76:79], v[52:55]\ns_add_u32 s85, s85, s84\nbuffer_load_dwordx4 v11, s[64:67], s85 offen sc1 lds\n" \
    "v_mfma_f32_16x16x32_bf16 v[56:59], v[248:251], v[68:71], v[56:59]\nv_mfma_f32_16x16x32_bf16 v[60:63], v[248:251], v[76:79], v[60:63]\ns_nop 0\n" \
    "v_cvt_pk_bf16_f32 v16, v48, v49\nv_cvt_pk_bf16_f32 v17, v50, v51\nv_mfma_f32_16x16x32_bf16 v[32:35], v[220:223], v[64:67], v[32:35]\n" \
    "buffer_load_dwordx4 v12, s[64:67], s85 offen offset:1024 sc1 lds\ns_add_u32 m0, s41, 16384\nv_mfma_f32_16x16x32_bf16 v[36:39], v[220:223], v[72:75], v[36:39]\n" \
    "s_xor_b32 s85, s62, 512\ns_add_u32 s85, s85, s84\nv_cvt_pk_bf16_f32 v18, v56, v57\nv_mfma_f32_16x16x32_bf16 v[40:43], v[228:231], v[64:67], v[40:43]\n" \
    "buffer_load_dwordx4 v11, s[64:67], s85 offen sc1 lds\nv_cvt_pk_bf16_f32 v19, v58, v59\nv_mfma_f32_16x16x32_bf16 v[44:47], v[228:231], v[72:75], v[44:47]\n" \
    "ds_write_b128 v14, v[16:19] offset:0\nv_cvt_pk_bf16_f32 v20, v52, v53\nv_cvt_pk_bf16_f32 v21, v54, v55\nv_mfma_f32_16x16x32_bf16 v[32:35], v[224:227], v[68:71], v[32:35]\n" \
    "v_cvt_pk_bf16_f32 v22, v60, v61\nv_cvt_pk_bf16_f32 v23, v62, v63\nv_mfma_f32_16x16x32_bf16 v[36:39], v[224:227], v[76:79], v[36:39]\n" \
    "buffer_load_dwordx4 v12, s[64:67], s85 offen offset:1024 sc1 lds\nds_write_b128 v14, v[20:23] offset:4096\n" \
    "v_mfma_f32_16x16x32_bf16 v[40:43], v[232:235], v[68:71], v[40:43]\nv_mfma_f32_16x16x32_bf16 v[44:47], v[232:235], v[76:79], v[44:47]\ns_nop 0\n" \
    "v_cvt_pk_bf16_f32 v24, v32, v33\nv_cvt_pk_bf16_f32 v25, v34, v35\ns_nop 3\nv_cvt_pk_bf16_f32 v26, v40, v41\nv_cvt_pk_bf16_f32 v27, v42, v43\n" \
    "ds_write_b128 v14, v[24:27] offset:8192\nv_cvt_pk_bf16_f32 v28, v36, v37\nv_cvt_pk_bf16_f32 v29, v38, v39\nv_cvt_pk_bf16_f32 v30, v44, v45\n" \
    "v_cvt_pk_bf16_f32 v31, v46, v47\nds_write_b128 v14, v[28:31] offset:12288\ns_lshr_b32 s69, s35, 1\ns_cmp_eq_u32 s69, 5\ns_cbranch_scc1 .Lq10_%=\n.Lq11_%=:\n" \
    "s_cmp_lt_u32 s75, s35\ns_cbranch_scc1 .Lq12_%=\ns_cmp_eq_u32 s36, 1\ns_cbranch_scc1 .Lq5_%=\ns_cmp_eq_u32 s36, 2\ns_cbranch_scc1 .Lq6_%=\ns_cmp_eq_u32 s36, 3\n" \
    "s_cbranch_scc1 .Lq7_%=\n.Lq4_%=:\nds_read_b128 v[32:35], v3 offset:0\nds_read_b128 v[36:39], v3 offset:1024\nds_read_b128 v[40:43], v3 offset:8192\n" \
    "ds_read_b128 v[44:47], v3 offset:9216\nds_read_b128 v[48:51], v3 offset:16384\nds_read_b128 v[52:55], v3 offset:17408\nds_read_b128 v[56:59], v3 offset:2048\n" \
    "ds_read_b128 v[60:63], v3 offset:3072\nds_read_b128 v[64:67], v3 offset:4096\nds_read_b128 v[68:71], v3 offset:5120\n.Lq15_%=:\ns_memrealtime s[72:73]\n" \
    "s_waitcnt lgkmcnt(0)\ns_mov_b32 s68, 0\ns_sub_u32 s76, s80, s45\ns_sub_u32 s69, s72, s76\ns_cmp_lt_i32 s69, -8\ns_cbranch_scc1 .Lq18_%=\n.Lq16_%=:\n" \
    "s_xor_b32 s90, s81, 0x800\n.Lq20_%=:\ns_memrealtime s[72:73]\ns_waitcnt lgkmcnt(0)\ns_sub_u32 s72, s72, s80\ns_cmp_lt_i32 s72, 0\ns_cbranch_scc1 .Lq20_%=\n" \
    "buffer_load_dwordx4 v[72:75], v2, s[64:67], s81 offen offset:0 sc1\ns_nop 7\nbuffer_load_dwordx4 v[76:79], v2, s[64:67], s81 offen offset:1024 sc1\ns_nop 7\n" \
    "buffer_load_dwordx4 v[80:83], v2, s[64:67], s90 offen offset:0 sc1\ns_nop 7\nbuffer_load_dwordx4 v[84:87], v2, s[64:67], s90 offen offset:1024 sc1\nv_mov_b64 v[16:17], 0\n" \
    "v_mov_b64 v[18:19], 0\nv_mov_b64 v[20:21], 0\nv_mov_b64 v[22:23], 0\nv_mov_b64 v[24:25], 0\nv_mov_b64 v[26:27], 0\nv_mov_b64 v[28:29], 0\nv_mov_b64 v[30:31], 0\n" \
    "s_waitcnt vmcnt(3)\nv_smfmac_f32_16x16x64_bf16 v[20:23], v[72:75], v[32:39], v0\nds_write_b128 v1, v[72:75] offset:24576\nv_max3_u32 v252, v72, v73, v74\n" \
    "v_smfmac_f32_16x16x64_bf16 v[16:19], v[72:75], a[0:7], v0\nv_smfmac_f32_16x16x64_bf16 v[24:27], v[72:75], a[8:15], v0\n" \
    "v_smfmac_f32_16x16x64_bf16 v[28:31], v[72:75], a[16:23], v0\ns_waitcnt vmcnt(2)\nds_write_b128 v1, v[76:79] offset:28672\nv_max3_u32 v252, v252, v75, v76\n" \
    "v_smfmac_f32_16x16x64_bf16 v[20:23], v[76:79], v[40:47], v0\nv_max3_u32 v252, v252, v77, v78\nv_smfmac_f32_16x16x64_bf16 v[16:19], v[76:79], a[96:103], v0\n" \
    "v_cmp_u_f32 vcc, v252, v79\nv_smfmac_f32_16x16x64_bf16 v[24:27], v[76:79], a[104:111], v0\ns_cbranch_vccnz .Lq26_%=\n.Lq22_%=:\n" \
    "v_smfmac_f32_16x16x64_bf16 v[28:31], v[76:79], a[112:119], v0\nds_read_b128 v[36:39], v3 offset:7168\ns_waitcnt vmcnt(1)\nv_max3_u32 v252, v80, v81, v82\n" \
    "v_cmp_u_f32 vcc, v252, v83\ns_nop 0\ns_cbranch_vccnz .Lq27_%=\n.Lq23_%=:\nv_smfmac_f32_16x16x64_bf16 v[20:23], v[80:83], v[48:55], v0\n" \
    "ds_write_b128 v1, v[80:83] offset:32768\nv_smfmac_f32_16x16x64_bf16 v[16:19], v[80:83], a[192:199], v0\nds_read_b128 v[32:35], v3 offset:6144\ns_waitcnt lgkmcnt(3)\n" \
    "s_barrier\nds_read_b128 v[88:91], v1 offset:25600\nv_smfmac_f32_16x16x64_bf16 v[24:27], v[80:83], a[200:207], v0\nds_read_b128 v[40:43], v3 offset:10240\n" \
    "v_smfmac_f32_16x16x64_bf16 v[28:31], v[80:83], a[208:215], v0\nds_read_b128 v[48:51], v1 offset:26624\ns_waitcnt vmcnt(0)\nv_max3_u32 v252, v84, v85, v86\n" \
    "v_cmp_u_f32 vcc, v252, v87\ns_nop 0\ns_cbranch_vccnz .Lq28_%=\n.Lq24_%=:\nv_smfmac_f32_16x16x64_bf16 v[20:23], v[84:87], v[132:139], v0\n" \
    "ds_write_b128 v1, v[84:87] offset:36864\nv_smfmac_f32_16x16x64_bf16 v[16:19], v[84:87], v[124:131], v0\nds_read_b128 v[52:55], v1 offset:27648\n" \
    "v_smfmac_f32_16x16x64_bf16 v[24:27], v[84:87], v[140:147], v0\nds_read_b128 v[72:75], v1 offset:29696\nv_smfmac_f32_16x16x64_bf16 v[28:31], v[84:87], v[148:155], v0\n" \
    "ds_read_b128 v[44:47], v3 offset:11264\ns_waitcnt lgkmcnt(6)\nv_smfmac_f32_16x16x64_bf16 v[20:23], v[88:91], v[56:63], v0\nds_read_b128 v[56:59], v1 offset:30720\n" \
    "v_smfmac_f32_16x16x64_bf16 v[16:19], v[88:91], a[24:31], v0\nds_read_b128 v[76:79], v3 offset:12288\nv_smfmac_f32_16x16x64_bf16 v[24:27], v[88:91], a[32:39], v0\n" \
    "ds_read_b128 v[80:83], v3 offset:13312\nv_smfmac_f32_16x16x64_bf16 v[28:31], v[88:91], a[40:47], v0\nds_read_b128 v[60:63], v1 offset:31744\ns_waitcnt lgkmcnt(7)\n" \
    "s_barrier\nv_smfmac_f32_16x16x64_bf16 v[20:23], v[48:51], v[64:71], v0\nds_read_b128 v[64:67], v3 offset:14336\n" \
    "v_smfmac_f32_16x16x64_bf16 v[16:19], v[48:51], a[48:55], v0\nds_read_b128 v[68:71], v3 offset:15360\ns_waitcnt lgkmcnt(8)\n" \
    "v_smfmac_f32_16x16x64_bf16 v[20:23], v[52:55], v[32:39], v0\nds_read_b128 v[32:35], v1 offset:33792\nv_smfmac_f32_16x16x64_bf16 v[16:19], v[52:55], a[72:79], v0\n" \
    "ds_read_b128 v[84:87], v3 offset:18432\ns_waitcnt lgkmcnt(8)\nv_smfmac_f32_16x16x64_bf16 v[20:23], v[72:75], v[40:47], v0\nds_read_b128 v[88:91], v3 offset:19456\n" \
    "v_smfmac_f32_16x16x64_bf16 v[16:19], v[72:75], a[120:127], v0\nds_read_b128 v[36:39], v1 offset:34816\ns_waitcnt lgkmcnt(7)\n" \
    "v_smfmac_f32_16x16x64_bf16 v[20:23], v[56:59], v[76:83], v0\nds_read_b128 v[40:43], v3 offset:20480\nv_smfmac_f32_16x16x64_bf16 v[16:19], v[56:59], a[144:151], v0\n" \
    "ds_read_b128 v[44:47], v3 offset:21504\ns_waitcnt lgkmcnt(6)\nv_smfmac_f32_16x16x64_bf16 v[20:23], v[60:63], v[64:71], v0\nds_read_b128 v[64:67], v1 offset:35840\n" \
    "v_smfmac_f32_16x16x64_bf16 v[16:19], v[60:63], a[168:175], v0\nds_read_b128 v[76:79], v3 offset:22528\ns_waitcnt lgkmcnt(5)\n" \
    "v_smfmac_f32_16x16x64_bf16 v[20:23], v[32:35], v[84:91], v0\nds_read_b128 v[80:83], v3 offset:23552\nv_smfmac_f32_16x16x64_bf16 v[16:19], v[32:35], a[216:223], v0\n" \
    "ds_read_b128 v[68:71], v1 offset:37888\ns_waitcnt lgkmcnt(4)\nv_smfmac_f32_16x16x64_bf16 v[20:23], v[36:39], v[40:47], v0\nds_read_b128 v[40:43], v1 offset:38912\n" \
    "v_smfmac_f32_16x16x64_bf16 v[16:19], v[36:39], a[240:247], v0\nds_read_b128 v[44:47], v1 offset:39936\ns_waitcnt lgkmcnt(3)\n" \
    "v_smfmac_f32_16x16x64_bf16 v[20:23], v[64:67], v[76:83], v0\nv_smfmac_f32_16x16x64_bf16 v[16:19], v[64:67], v[100:107], v0\ns_waitcnt lgkmcnt(2)\n" \
    "v_smfmac_f32_16x16x64_bf16 v[20:23], v[68:71], v[164:171], v0\ns_waitcnt lgkmcnt(1)\nv_smfmac_f32_16x16x64_bf16 v[20:23], v[40:43], v[196:203], v0\ns_waitcnt lgkmcnt(0)\n" \
    "v_smfmac_f32_16x16x64_bf16 v[20:23], v[44:47], v[228:235], v0\ns_nop 2\nv_smfmac_f32_16x16x64_bf16 v[16:19], v[68:71], v[156:163], v0\ns_nop 2\n" \
    "v_smfmac_f32_16x16x64_bf16 v[16:19], v[40:43], v[188:195], v0\nv_permlane32_swap_b32 v20, v22\nv_smfmac_f32_16x16x64_bf16 v[16:19], v[44:47], v[220:227], v0\ns_nop 1\n" \
    "v_permlane32_swap_b32 v21, v23\nv_smfmac_f32_16x16x64_bf16 v[24:27], v[48:51], a[56:63], v0\ns_nop 1\nv_add_f32 v20, v20, v22\n" \
    "v_smfmac_f32_16x16x64_bf16 v[28:31], v[48:51], a[64:71], v0\nv_add_f32 v21, v21, v23\nv_smfmac_f32_16x16x64_bf16 v[24:27], v[52:55], a[80:87], v0\nv_mul_f32 v20, s32, v20\n" \
    "v_smfmac_f32_16x16x64_bf16 v[28:31], v[52:55], a[88:95], v0\nv_mul_f32 v21, s32, v21\nv_smfmac_f32_16x16x64_bf16 v[24:27], v[72:75], a[128:135], v0\n" \
    "v_cvt_pk_bf16_f32 v20, v20, v21\nv_smfmac_f32_16x16x64_bf16 v[28:31], v[72:75], a[136:143], v0\nds_write_b16 v4, v20 offset:8\nv_permlane32_swap_b32 v16, v18\n" \
    "v_smfmac_f32_16x16x64_bf16 v[24:27], v[56:59], a[152:159], v0\nds_write_b16_d16_hi v4, v20 offset:24\nv_permlane32_swap_b32 v17, v19\n" \
    "v_smfmac_f32_16x16x64_bf16 v[24:27], v[60:63], a[176:183], v0\nds_read_b128 v[48:51], v1 offset:40960\nv_add_f32 v16, v16, v18\n" \
    "v_smfmac_f32_16x16x64_bf16 v[24:27], v[32:35], a[224:231], v0\nds_read_b128 v[52:55], v1 offset:45056\nv_add_f32 v17, v17, v19\n" \
    "v_smfmac_f32_16x16x64_bf16 v[24:27], v[36:39], a[248:255], v0\nds_read_b128 v[72:75], v1 offset:41984\nv_mul_f32 v16, s32, v16\n" \
    "v_smfmac_f32_16x16x64_bf16 v[24:27], v[64:67], v[108:115], v0\nds_read_b128 v[76:79], v1 offset:46080\nv_mul_f32 v17, s32, v17\n" \
    "v_smfmac_f32_16x16x64_bf16 v[24:27], v[68:71], v[172:179], v0\nds_read_b128 v[80:83], v1 offset:43008\nv_cvt_pk_bf16_f32 v16, v16, v17\n" \
    "v_smfmac_f32_16x16x64_bf16 v[24:27], v[40:43], v[204:211], v0\nds_write_b16 v4, v16 offset:0\nv_smfmac_f32_16x16x64_bf16 v[24:27], v[44:47], v[236:243], v0\n" \
    "ds_write_b16_d16_hi v4, v16 offset:16\ns_nop 1\nv_smfmac_f32_16x16x64_bf16 v[28:31], v[56:59], a[160:167], v0\nds_read_b128 v[56:59], v1 offset:47104\ns_nop 1\n" \
    "v_smfmac_f32_16x16x64_bf16 v[28:31], v[60:63], a[184:191], v0\nds_read_b128 v[60:63], v1 offset:44032\nv_permlane32_swap_b32 v24, v26\n" \
    "v_smfmac_f32_16x16x64_bf16 v[28:31], v[32:35], a[232:239], v0\nds_read_b128 v[32:35], v1 offset:48128\nv_permlane32_swap_b32 v25, v27\n" \
    "v_smfmac_f32_16x16x64_bf16 v[28:31], v[36:39], v[92:99], v0\nds_read_b128 v[36:39], v6 offset:0\nv_add_f32 v24, v24, v26\n" \
    "v_smfmac_f32_16x16x64_bf16 v[28:31], v[64:67], v[116:123], v0\nds_read_b128 v[64:67], v6 offset:512\nv_add_f32 v25, v25, v27\n" \
    "v_smfmac_f32_16x16x64_bf16 v[28:31], v[68:71], v[180:187], v0\nds_read_b128 v[68:71], v6 offset:1024\nv_cvt_pk_bf16_f32 v24, v24, v25\n" \
    "v_smfmac_f32_16x16x64_bf16 v[28:31], v[40:43], v[212:219], v0\nds_read_b128 v[40:43], v6 offset:1536\nv_smfmac_f32_16x16x64_bf16 v[28:31], v[44:47], v[244:251], v0\n" \
    "ds_write_b16 v4, v24 offset:2048\nds_write_b16_d16_hi v4, v24 offset:2064\ns_waitcnt lgkmcnt(5)\nv_mfma_f32_16x16x32_bf16 v[16:19], v[48:51], v[36:39], 0\ns_nop 2\n" \
    "v_mfma_f32_16x16x32_bf16 v[20:23], v[52:55], v[36:39], 0\nv_permlane32_swap_b32 v28, v30\nv_permlane32_swap_b32 v29, v31\nv_add_f32 v28, v28, v30\nv_add_f32 v29, v29, v31\n" \
    "v_cvt_pk_bf16_f32 v28, v28, v29\nds_write_b16 v4, v28 offset:2056\nds_write_b16_d16_hi v4, v28 offset:2072\ns_waitcnt lgkmcnt(6)\n" \
    "v_mfma_f32_16x16x32_bf16 v[16:19], v[72:75], v[64:67], v[16:19]\nv_mfma_f32_16x16x32_bf16 v[20:23], v[76:79], v[64:67], v[20:23]\ns_waitcnt lgkmcnt(5)\n" \
    "v_mfma_f32_16x16x32_bf16 v[16:19], v[80:83], v[68:71], v[16:19]\nds_read_b128 v[44:47], v8 offset:0\nds_read_b128 v[84:87], v8 offset:512\n" \
    "v_mfma_f32_16x16x32_bf16 v[20:23], v[56:59], v[68:71], v[20:23]\nds_read_b128 v[88:91], v8 offset:1024\nds_read_b128 v[28:31], v8 offset:1536\ns_waitcnt lgkmcnt(8)\n" \
    "v_mfma_f32_16x16x32_bf16 v[16:19], v[60:63], v[40:43], v[16:19]\nv_mfma_f32_16x16x32_bf16 v[20:23], v[32:35], v[40:43], v[20:23]\nv_mov_b32 v15, v15\nv_mov_b32 v15, v15\n" \
    "s_waitcnt lgkmcnt(3)\nv_mfma_f32_16x16x32_bf16 v[24:27], v[44:47], v[36:39], 0\ns_nop 2\ns_waitcnt lgkmcnt(2)\n" \
    "v_mov_b32_dpp v16, v20 quad_perm:[0,1,2,3] row_mask:0xf bank_mask:0xc\nv_mov_b32_dpp v17, v21 quad_perm:[0,1,2,3] row_mask:0xf bank_mask:0xc\n" \
    "v_mfma_f32_16x16x32_bf16 v[24:27], v[84:87], v[64:67], v[24:27]\ns_waitcnt lgkmcnt(1)\nv_mov_b32_dpp v18, v22 quad_perm:[0,1,2,3] row_mask:0xf bank_mask:0xc\n" \
    "v_mov_b32_dpp v19, v23 quad_perm:[0,1,2,3] row_mask:0xf bank_mask:0xc\nv_mfma_f32_16x16x32_bf16 v[24:27], v[88:91], v[68:71], v[24:27]\ns_waitcnt lgkmcnt(0)\n" \
    "v_max3_f32 v254, v16, v17, v18\nv_max_f32 v254, v254, v19\nv_mfma_f32_16x16x32_bf16 v[24:27], v[28:31], v[40:43], v[24:27]\ns_nop 0\n" \
    "v_max_f32_dpp v252, v254, v254 row_ror:8 row_mask:0xf bank_mask:0xf\nv_max_f32_dpp v253, v254, v254 row_ror:8 row_mask:0xf bank_mask:0xf\ns_nop 1\n" \
    "v_permlane32_swap_b32 v252, v253\nv_max_f32 v252, v252, v253\ns_xor_b32 s74, s82, 0x4000\nv_max3_f32 v254, v252, v24, v25\nv_max3_f32 v252, v254, v26, v27\n" \
    "v_max3_f32 v253, v254, v26, v27\ns_nop 1\nv_permlane16_swap_b32 v252, v253\nv_max_f32 v252, v252, v253\n" \
    "v_pk_add_f32 v[16:17], v[16:17], v[252:253] op_sel_hi:[1,0] neg_lo:[0,1] neg_hi:[0,1]\n" \
    "v_pk_add_f32 v[18:19], v[18:19], v[252:253] op_sel_hi:[1,0] neg_lo:[0,1] neg_hi:[0,1]\n" \
    "v_pk_add_f32 v[24:25], v[24:25], v[252:253] op_sel_hi:[1,0] neg_lo:[0,1] neg_hi:[0,1]\n" \
    "v_pk_add_f32 v[26:27], v[26:27], v[252:253] op_sel_hi:[1,0] neg_lo:[0,1] neg_hi:[0,1]\nv_exp_f32 v16, v16\nv_exp_f32 v17, v17\nv_exp_f32 v18, v18\nv_exp_f32 v19, v19\n" \
    "v_exp_f32 v24, v24\nv_pk_add_f32 v[254:255], v[16:17], v[18:19]\nv_exp_f32 v25, v25\nv_add_f32 v254, v254, v255\nv_exp_f32 v26, v26\nv_exp_f32 v27, v27\n" \
    "v_add_f32_dpp v252, v254, v254 row_ror:8 row_mask:0xf bank_mask:0xf\nv_add_f32_dpp v253, v254, v254 row_ror:8 row_mask:0xf bank_mask:0xf\n" \
    "v_pk_add_f32 v[254:255], v[24:25], v[26:27]\ns_nop 0\nv_permlane32_swap_b32 v252, v253\nv_pk_add_f32 v[254:255], v[254:255], v[252:253]\n" \
    "v_pk_add_f32 v[252:253], v[254:255], v[254:255] op_sel:[0,1] op_sel_hi:[1,0]\ns_nop 1\nv_permlane16_swap_b32 v252, v253\nv_add_f32 v252, v252, v253\n" \
    "v_cmp_u_f32 vcc, v252, v252\nv_rcp_f32 v252, v252\ns_cbranch_vccnz .Lq14_%=\nv_pk_mul_f32 v[16:17], v[16:17], v[252:253] op_sel_hi:[1,0]\n" \
    "v_pk_mul_f32 v[24:25], v[24:25], v[252:253] op_sel_hi:[1,0]\nv_cvt_pk_bf16_f32 v20, v16, v17\nv_cvt_pk_bf16_f32 v22, v24, v25\n" \
    "v_pk_mul_f32 v[18:19], v[18:19], v[252:253] op_sel_hi:[1,0]\nv_pk_mul_f32 v[26:27], v[26:27], v[252:253] op_sel_hi:[1,0]\nv_cvt_pk_bf16_f32 v21, v18, v19\n" \
    "v_cvt_pk_bf16_f32 v23, v26, v27\nv_permlane32_swap_b32 v20, v22\ns_nop 0\nv_permlane32_swap_b32 v21, v23\nbuffer_store_dwordx4 v[20:23], v7, s[64:67], s82 offen sc1\n" \
    "buffer_store_dwordx4 v[20:23], v7, s[64:67], s74 offen sc1\ns_branch .Lq3_%=\n.Lq5_%=:\nds_read_b128 v[32:35], v3 offset:2048\nds_read_b128 v[36:39], v3 offset:3072\n" \
    "ds_read_b128 v[40:43], v3 offset:10240\nds_read_b128 v[44:47], v3 offset:11264\nds_read_b128 v[48:51], v3 offset:18432\nds_read_b128 v[52:55], v3 offset:19456\n" \
    "ds_read_b128 v[56:59], v3 offset:0\nds_read_b128 v[60:63], v3 offset:1024\nds_read_b128 v[64:67], v3 offset:4096\nds_read_b128 v[68:71], v3 offset:5120\n.Lq34_%=:\n" \
    "s_memrealtime s[72:73]\ns_waitcnt lgkmcnt(0)\ns_mov_b32 s68, 0\ns_sub_u32 s76, s80, s45\ns_sub_u32 s69, s72, s76\ns_cmp_lt_i32 s69, -8\ns_cbranch_scc1 .Lq37_%=\n.Lq35_%=:\n" \
    "s_xor_b32 s90, s81, 0x800\n.Lq39_%=:\ns_memrealtime s[72:73]\ns_waitcnt lgkmcnt(0)\ns_sub_u32 s72, s72, s80\ns_cmp_lt_i32 s72, 0\ns_cbranch_scc1 .Lq39_%=\n" \
    "buffer_load_dwordx4 v[72:75], v2, s[64:67], s81 offen offset:0 sc1\ns_nop 7\nbuffer_load_dwordx4 v[76:79], v2, s[64:67], s81 offen offset:1024 sc1\ns_nop 7\n" \
    "buffer_load_dwordx4 v[80:83], v2, s[64:67], s90 offen offset:0 sc1\ns_nop 7\nbuffer_load_dwordx4 v[84:87], v2, s[64:67], s90 offen offset:1024 sc1\nv_mov_b64 v[16:17], 0\n" \
    "v_mov_b64 v[18:19], 0\nv_mov_b64 v[20:21], 0\nv_mov_b64 v[22:23], 0\nv_mov_b64 v[24:25], 0\nv_mov_b64 v[26:27], 0\nv_mov_b64 v[28:29], 0\nv_mov_b64 v[30:31], 0\n" \
    "s_waitcnt vmcnt(3)\nv_smfmac_f32_16x16x64_bf16 v[20:23], v[72:75], v[32:39], v0\nds_write_b128 v1, v[72:75] offset:25600\nv_max3_u32 v252, v72, v73, v74\n" \
    "v_smfmac_f32_16x16x64_bf16 v[16:19], v[72:75], a[24:31], v0\nv_smfmac_f32_16x16x64_bf16 v[24:27], v[72:75], a[32:39], v0\n" \
    "v_smfmac_f32_16x16x64_bf16 v[28:31], v[72:75], a[40:47], v0\ns_waitcnt vmcnt(2)\nds_write_b128 v1, v[76:79] offset:29696\nv_max3_u32 v252, v252, v75, v76\n" \
    "v_smfmac_f32_16x16x64_bf16 v[20:23], v[76:79], v[40:47], v0\nv_max3_u32 v252, v252, v77, v78\nv_smfmac_f32_16x16x64_bf16 v[16:19], v[76:79], a[120:127], v0\n" \
    "v_cmp_u_f32 vcc, v252, v79\nv_smfmac_f32_16x16x64_bf16 v[24:27], v[76:79], a[128:135], v0\ns_cbranch_vccnz .Lq45_%=\n.Lq41_%=:\n" \
    "v_smfmac_f32_16x16x64_bf16 v[28:31], v[76:79], a[136:143], v0\nds_read_b128 v[36:39], v3 offset:7168\ns_waitcnt vmcnt(1)\nv_max3_u32 v252, v80, v81, v82\n" \
    "v_cmp_u_f32 vcc, v252, v83\ns_nop 0\ns_cbranch_vccnz .Lq46_%=\n.Lq42_%=:\nv_smfmac_f32_16x16x64_bf16 v[20:23], v[80:83], v[48:55], v0\n" \
    "ds_write_b128 v1, v[80:83] offset:33792\nv_smfmac_f32_16x16x64_bf16 v[16:19], v[80:83], a[216:223], v0\nds_read_b128 v[32:35], v3 offset:6144\ns_waitcnt lgkmcnt(3)\n" \
    "s_barrier\nds_read_b128 v[88:91], v1 offset:24576\nv_smfmac_f32_16x16x64_bf16 v[24:27], v[80:83], a[224:231], v0\nds_read_b128 v[40:43], v3 offset:8192\n" \
    "v_smfmac_f32_16x16x64_bf16 v[28:31], v[80:83], a[232:239], v0\nds_read_b128 v[48:51], v1 offset:26624\ns_waitcnt vmcnt(0)\nv_max3_u32 v252, v84, v85, v86\n" \
    "v_cmp_u_f32 vcc, v252, v87\ns_nop 0\ns_cbranch_vccnz .Lq47_%=\n.Lq43_%=:\nv_smfmac_f32_16x16x64_bf16 v[20:23], v[84:87], v[164:171], v0\n" \
    "ds_write_b128 v1, v[84:87] offset:37888\nv_smfmac_f32_16x16x64_bf16 v[16:19], v[84:87], v[156:163], v0\nds_read_b128 v[52:55], v1 offset:27648\n" \
    "v_smfmac_f32_16x16x64_bf16 v[24:27], v[84:87], v[172:179], v0\nds_read_b128 v[72:75], v1 offset:28672\nv_smfmac_f32_16x16x64_bf16 v[28:31], v[84:87], v[180:187], v0\n" \
    "ds_read_b128 v[44:47], v3 offset:9216\ns_waitcnt lgkmcnt(6)\nv_smfmac_f32_16x16x64_bf16 v[20:23], v[88:91], v[56:63], v0\nds_read_b128 v[56:59], v1 offset:30720\n" \
    "v_smfmac_f32_16x16x64_bf16 v[16:19], v[88:91], a[0:7], v0\nds_read_b128 v[76:79], v3 offset:12288\nv_smfmac_f32_16x16x64_bf16 v[24:27], v[88:91], a[8:15], v0\n" \
    "ds_read_b128 v[80:83], v3 offset:13312\nv_smfmac_f32_16x16x64_bf16 v[28:31], v[88:91], a[16:23], v0\nds_read_b128 v[60:63], v1 offset:31744\ns_waitcnt lgkmcnt(7)\n" \
    "s_barrier\nv_smfmac_f32_16x16x64_bf16 v[20:23], v[48:51], v[64:71], v0\nds_read_b128 v[64:67], v3 offset:14336\n" \
    "v_smfmac_f32_16x16x64_bf16 v[16:19], v[48:51], a[48:55], v0\nds_read_b128 v[68:71], v3 offset:15360\ns_waitcnt lgkmcnt(8)\n" \
    "v_smfmac_f32_16x16x64_bf16 v[20:23], v[52:55], v[32:39], v0\nds_read_b128 v[32:35], v1 offset:32768\nv_smfmac_f32_16x16x64_bf16 v[16:19], v[52:55], a[72:79], v0\n" \
    "ds_read_b128 v[84:87], v3 offset:16384\ns_waitcnt lgkmcnt(8)\nv_smfmac_f32_16x16x64_bf16 v[20:23], v[72:75], v[40:47], v0\nds_read_b128 v[88:91], v3 offset:17408\n" \
    "v_smfmac_f32_16x16x64_bf16 v[16:19], v[72:75], a[96:103], v0\nds_read_b128 v[36:39], v1 offset:34816\ns_waitcnt lgkmcnt(7)\n" \
    "v_smfmac_f32_16x16x64_bf16 v[20:23], v[56:59], v[76:83], v0\nds_read_b128 v[40:43], v3 offset:20480\nv_smfmac_f32_16x16x64_bf16 v[16:19], v[56:59], a[144:151], v0\n" \
    "ds_read_b128 v[44:47], v3 offset:21504\ns_waitcnt lgkmcnt(6)\nv_smfmac_f32_16x16x64_bf16 v[20:23], v[60:63], v[64:71], v0\nds_read_b128 v[64:67], v1 offset:35840\n" \
    "v_smfmac_f32_16x16x64_bf16 v[16:19], v[60:63], a[168:175], v0\nds_read_b128 v[76:79], v3 offset:22528\ns_waitcnt lgkmcnt(5)\n" \
    "v_smfmac_f32_16x16x64_bf16 v[20:23], v[32:35], v[84:91], v0\nds_read_b128 v[80:83], v3 offset:23552\nv_smfmac_f32_16x16x64_bf16 v[16:19], v[32:35], a[192:199], v0\n" \
    "ds_read_b128 v[68:71], v1 offset:36864\ns_waitcnt lgkmcnt(4)\nv_smfmac_f32_16x16x64_bf16 v[20:23], v[36:39], v[40:47], v0\nds_read_b128 v[40:43], v1 offset:38912\n" \
    "v_smfmac_f32_16x16x64_bf16 v[16:19], v[36:39], a[240:247], v0\nds_read_b128 v[44:47], v1 offset:39936\ns_waitcnt lgkmcnt(3)\n" \
    "v_smfmac_f32_16x16x64_bf16 v[20:23], v[64:67], v[76:83], v0\nv_smfmac_f32_16x16x64_bf16 v[16:19], v[64:67], v[100:107], v0\ns_waitcnt lgkmcnt(2)\n" \
    "v_smfmac_f32_16x16x64_bf16 v[20:23], v[68:71], v[132:139], v0\ns_waitcnt lgkmcnt(1)\nv_smfmac_f32_16x16x64_bf16 v[20:23], v[40:43], v[196:203], v0\ns_waitcnt lgkmcnt(0)\n" \
    "v_smfmac_f32_16x16x64_bf16 v[20:23], v[44:47], v[228:235], v0\ns_nop 2\nv_smfmac_f32_16x16x64_bf16 v[16:19], v[68:71], v[124:131], v0\ns_nop 2\n" \
    "v_smfmac_f32_16x16x64_bf16 v[16:19], v[40:43], v[188:195], v0\nv_permlane32_swap_b32 v20, v22\nv_smfmac_f32_16x16x64_bf16 v[16:19], v[44:47], v[220:227], v0\ns_nop 1\n" \
    "v_permlane32_swap_b32 v21, v23\nv_smfmac_f32_16x16x64_bf16 v[24:27], v[48:51], a[56:63], v0\ns_nop 1\nv_add_f32 v20, v20, v22\n" \
    "v_smfmac_f32_16x16x64_bf16 v[28:31], v[48:51], a[64:71], v0\nv_add_f32 v21, v21, v23\nv_smfmac_f32_16x16x64_bf16 v[24:27], v[52:55], a[80:87], v0\nv_mul_f32 v20, s32, v20\n" \
    "v_smfmac_f32_16x16x64_bf16 v[28:31], v[52:55], a[88:95], v0\nv_mul_f32 v21, s32, v21\nv_smfmac_f32_16x16x64_bf16 v[24:27], v[72:75], a[104:111], v0\n" \
    "v_cvt_pk_bf16_f32 v20, v20, v21\nv_smfmac_f32_16x16x64_bf16 v[28:31], v[72:75], a[112:119], v0\nds_write_b16 v4, v20 offset:8\nv_permlane32_swap_b32 v16, v18\n" \
    "v_smfmac_f32_16x16x64_bf16 v[24:27], v[56:59], a[152:159], v0\nds_write_b16_d16_hi v4, v20 offset:24\nv_permlane32_swap_b32 v17, v19\n" \
    "v_smfmac_f32_16x16x64_bf16 v[24:27], v[60:63], a[176:183], v0\nds_read_b128 v[48:51], v1 offset:40960\nv_add_f32 v16, v16, v18\n" \
    "v_smfmac_f32_16x16x64_bf16 v[24:27], v[32:35], a[200:207], v0\nds_read_b128 v[52:55], v1 offset:45056\nv_add_f32 v17, v17, v19\n" \
    "v_smfmac_f32_16x16x64_bf16 v[24:27], v[36:39], a[248:255], v0\nds_read_b128 v[72:75], v1 offset:41984\nv_mul_f32 v16, s32, v16\n" \
    "v_smfmac_f32_16x16x64_bf16 v[24:27], v[64:67], v[108:115], v0\nds_read_b128 v[76:79], v1 offset:46080\nv_mul_f32 v17, s32, v17\n" \
    "v_smfmac_f32_16x16x64_bf16 v[24:27], v[68:71], v[140:147], v0\nds_read_b128 v[80:83], v1 offset:43008\nv_cvt_pk_bf16_f32 v16, v16, v17\n" \
    "v_smfmac_f32_16x16x64_bf16 v[24:27], v[40:43], v[204:211], v0\nds_write_b16 v4, v16 offset:0\nv_smfmac_f32_16x16x64_bf16 v[24:27], v[44:47], v[236:243], v0\n" \
    "ds_write_b16_d16_hi v4, v16 offset:16\ns_nop 1\nv_smfmac_f32_16x16x64_bf16 v[28:31], v[56:59], a[160:167], v0\nds_read_b128 v[56:59], v1 offset:47104\ns_nop 1\n" \
    "v_smfmac_f32_16x16x64_bf16 v[28:31], v[60:63], a[184:191], v0\nds_read_b128 v[60:63], v1 offset:44032\nv_permlane32_swap_b32 v24, v26\n" \
    "v_smfmac_f32_16x16x64_bf16 v[28:31], v[32:35], a[208:215], v0\nds_read_b128 v[32:35], v1 offset:48128\nv_permlane32_swap_b32 v25, v27\n" \
    "v_smfmac_f32_16x16x64_bf16 v[28:31], v[36:39], v[92:99], v0\nds_read_b128 v[36:39], v6 offset:0\nv_add_f32 v24, v24, v26\n" \
    "v_smfmac_f32_16x16x64_bf16 v[28:31], v[64:67], v[116:123], v0\nds_read_b128 v[64:67], v6 offset:512\nv_add_f32 v25, v25, v27\n" \
    "v_smfmac_f32_16x16x64_bf16 v[28:31], v[68:71], v[148:155], v0\nds_read_b128 v[68:71], v6 offset:1024\nv_cvt_pk_bf16_f32 v24, v24, v25\n" \
    "v_smfmac_f32_16x16x64_bf16 v[28:31], v[40:43], v[212:219], v0\nds_read_b128 v[40:43], v6 offset:1536\nv_smfmac_f32_16x16x64_bf16 v[28:31], v[44:47], v[244:251], v0\n" \
    "ds_write_b16 v4, v24 offset:2048\nds_write_b16_d16_hi v4, v24 offset:2064\ns_waitcnt lgkmcnt(5)\nv_mfma_f32_16x16x32_bf16 v[16:19], v[48:51], v[36:39], 0\ns_nop 2\n" \
    "v_mfma_f32_16x16x32_bf16 v[20:23], v[52:55], v[36:39], 0\nv_permlane32_swap_b32 v28, v30\nv_permlane32_swap_b32 v29, v31\nv_add_f32 v28, v28, v30\nv_add_f32 v29, v29, v31\n" \
    "v_cvt_pk_bf16_f32 v28, v28, v29\nds_write_b16 v4, v28 offset:2056\nds_write_b16_d16_hi v4, v28 offset:2072\ns_waitcnt lgkmcnt(6)\n" \
    "v_mfma_f32_16x16x32_bf16 v[16:19], v[72:75], v[64:67], v[16:19]\nv_mfma_f32_16x16x32_bf16 v[20:23], v[76:79], v[64:67], v[20:23]\ns_waitcnt lgkmcnt(5)\n" \
    "v_mfma_f32_16x16x32_bf16 v[16:19], v[80:83], v[68:71], v[16:19]\nds_read_b128 v[44:47], v8 offset:0\nds_read_b128 v[84:87], v8 offset:512\n" \
    "v_mfma_f32_16x16x32_bf16 v[20:23], v[56:59], v[68:71], v[20:23]\nds_read_b128 v[88:91], v8 offset:1024\nds_read_b128 v[28:31], v8 offset:1536\ns_waitcnt lgkmcnt(8)\n" \
    "v_mfma_f32_16x16x32_bf16 v[16:19], v[60:63], v[40:43], v[16:19]\nv_mfma_f32_16x16x32_bf16 v[20:23], v[32:35], v[40:43], v[20:23]\nv_mov_b32 v15, v15\nv_mov_b32 v15, v15\n" \
    "s_waitcnt lgkmcnt(3)\nv_mfma_f32_16x16x32_bf16 v[24:27], v[44:47], v[36:39], 0\ns_nop 2\ns_waitcnt lgkmcnt(2)\n" \
    "v_mov_b32_dpp v16, v20 quad_perm:[0,1,2,3] row_mask:0xf bank_mask:0xc\nv_mov_b32_dpp v17, v21 quad_perm:[0,1,2,3] row_mask:0xf bank_mask:0xc\n" \
    "v_mfma_f32_16x16x32_bf16 v[24:27], v[84:87], v[64:67], v[24:27]\ns_waitcnt lgkmcnt(1)\nv_mov_b32_dpp v18, v22 quad_perm:[0,1,2,3] row_mask:0xf bank_mask:0xc\n" \
    "v_mov_b32_dpp v19, v23 quad_perm:[0,1,2,3] row_mask:0xf bank_mask:0xc\nv_mfma_f32_16x16x32_bf16 v[24:27], v[88:91], v[68:71], v[24:27]\ns_waitcnt lgkmcnt(0)\n" \
    "v_max3_f32 v254, v16, v17, v18\nv_max_f32 v254, v254, v19\nv_mfma_f32_16x16x32_bf16 v[24:27], v[28:31], v[40:43], v[24:27]\ns_nop 0\n" \
    "v_max_f32_dpp v252, v254, v254 row_ror:8 row_mask:0xf bank_mask:0xf\nv_max_f32_dpp v253, v254, v254 row_ror:8 row_mask:0xf bank_mask:0xf\ns_nop 1\n" \
    "v_permlane32_swap_b32 v252, v253\nv_max_f32 v252, v252, v253\ns_xor_b32 s74, s82, 0x4000\nv_max3_f32 v254, v252, v24, v25\nv_max3_f32 v252, v254, v26, v27\n" \
    "v_max3_f32 v253, v254, v26, v27\ns_nop 1\nv_permlane16_swap_b32 v252, v253\nv_max_f32 v252, v252, v253\n" \
    "v_pk_add_f32 v[16:17], v[16:17], v[252:253] op_sel_hi:[1,0] neg_lo:[0,1] neg_hi:[0,1]\n" \
    "v_pk_add_f32 v[18:19], v[18:19], v[252:253] op_sel_hi:[1,0] neg_lo:[0,1] neg_hi:[0,1]\n" \
    "v_pk_add_f32 v[24:25], v[24:25], v[252:253] op_sel_hi:[1,0] neg_lo:[0,1] neg_hi:[0,1]\n" \
    "v_pk_add_f32 v[26:27], v[26:27], v[252:253] op_sel_hi:[1,0] neg_lo:[0,1] neg_hi:[0,1]\nv_exp_f32 v16, v16\nv_exp_f32 v17, v17\nv_exp_f32 v18, v18\nv_exp_f32 v19, v19\n" \
    "v_exp_f32 v24, v24\nv_pk_add_f32 v[254:255], v[16:17], v[18:19]\nv_exp_f32 v25, v25\nv_add_f32 v254, v254, v255\nv_exp_f32 v26, v26\nv_exp_f32 v27, v27\n" \
    "v_add_f32_dpp v252, v254, v254 row_ror:8 row_mask:0xf bank_mask:0xf\nv_add_f32_dpp v253, v254, v254 row_ror:8 row_mask:0xf bank_mask:0xf\n" \
    "v_pk_add_f32 v[254:255], v[24:25], v[26:27]\ns_nop 0\nv_permlane32_swap_b32 v252, v253\nv_pk_add_f32 v[254:255], v[254:255], v[252:253]\n" \
    "v_pk_add_f32 v[252:253], v[254:255], v[254:255] op_sel:[0,1] op_sel_hi:[1,0]\ns_nop 1\nv_permlane16_swap_b32 v252, v253\nv_add_f32 v252, v252, v253\n" \
    "v_cmp_u_f32 vcc, v252, v252\nv_rcp_f32 v252, v252\ns_cbranch_vccnz .Lq14_%=\nv_pk_mul_f32 v[16:17], v[16:17], v[252:253] op_sel_hi:[1,0]\n" \
    "v_pk_mul_f32 v[24:25], v[24:25], v[252:253] op_sel_hi:[1,0]\nv_cvt_pk_bf16_f32 v20, v16, v17\nv_cvt_pk_bf16_f32 v22, v24, v25\n" \
    "v_pk_mul_f32 v[18:19], v[18:19], v[252:253] op_sel_hi:[1,0]\nv_pk_mul_f32 v[26:27], v[26:27], v[252:253] op_sel_hi:[1,0]\nv_cvt_pk_bf16_f32 v21, v18, v19\n" \
    "v_cvt_pk_bf16_f32 v23, v26, v27\nv_permlane32_swap_b32 v20, v22\ns_nop 0\nv_permlane32_swap_b32 v21, v23\nbuffer_store_dwordx4 v[20:23], v7, s[64:67], s82 offen sc1\n" \
    "buffer_store_dwordx4 v[20:23], v7, s[64:67], s74 offen sc1\ns_branch .Lq3_%=\n.Lq6_%=:\nds_read_b128 v[32:35], v3 offset:4096\nds_read_b128 v[36:39], v3 offset:5120\n" \
    "ds_read_b128 v[40:43], v3 offset:12288\nds_read_b128 v[44:47], v3 offset:13312\nds_read_b128 v[48:51], v3 offset:20480\nds_read_b128 v[52:55], v3 offset:21504\n" \
    "ds_read_b128 v[56:59], v3 offset:0\nds_read_b128 v[60:63], v3 offset:1024\nds_read_b128 v[64:67], v3 offset:2048\nds_read_b128 v[68:71], v3 offset:3072\n.Lq53_%=:\n" \
    "s_memrealtime s[72:73]\ns_waitcnt lgkmcnt(0)\ns_mov_b32 s68, 0\ns_sub_u32 s76, s80, s45\ns_sub_u32 s69, s72, s76\ns_cmp_lt_i32 s69, -8\ns_cbranch_scc1 .Lq56_%=\n.Lq54_%=:\n" \
    "s_xor_b32 s90, s81, 0x800\n.Lq58_%=:\ns_memrealtime s[72:73]\ns_waitcnt lgkmcnt(0)\ns_sub_u32 s72, s72, s80\ns_cmp_lt_i32 s72, 0\ns_cbranch_scc1 .Lq58_%=\n" \
    "buffer_load_dwordx4 v[72:75], v2, s[64:67], s81 offen offset:0 sc1\ns_nop 7\nbuffer_load_dwordx4 v[76:79], v2, s[64:67], s81 offen offset:1024 sc1\ns_nop 7\n" \
    "buffer_load_dwordx4 v[80:83], v2, s[64:67], s90 offen offset:0 sc1\ns_nop 7\nbuffer_load_dwordx4 v[84:87], v2, s[64:67], s90 offen offset:1024 sc1\nv_mov_b64 v[16:17], 0\n" \
    "v_mov_b64 v[18:19], 0\nv_mov_b64 v[20:21], 0\nv_mov_b64 v[22:23], 0\nv_mov_b64 v[24:25], 0\nv_mov_b64 v[26:27], 0\nv_mov_b64 v[28:29], 0\nv_mov_b64 v[30:31], 0\n" \
    "s_waitcnt vmcnt(3)\nv_smfmac_f32_16x16x64_bf16 v[20:23], v[72:75], v[32:39], v0\nds_write_b128 v1, v[72:75] offset:26624\nv_max3_u32 v252, v72, v73, v74\n" \
    "v_smfmac_f32_16x16x64_bf16 v[16:19], v[72:75], a[48:55], v0\nv_smfmac_f32_16x16x64_bf16 v[24:27], v[72:75], a[56:63], v0\n" \
    "v_smfmac_f32_16x16x64_bf16 v[28:31], v[72:75], a[64:71], v0\ns_waitcnt vmcnt(2)\nds_write_b128 v1, v[76:79] offset:30720\nv_max3_u32 v252, v252, v75, v76\n" \
    "v_smfmac_f32_16x16x64_bf16 v[20:23], v[76:79], v[40:47], v0\nv_max3_u32 v252, v252, v77, v78\nv_smfmac_f32_16x16x64_bf16 v[16:19], v[76:79], a[144:151], v0\n" \
    "v_cmp_u_f32 vcc, v252, v79\nv_smfmac_f32_16x16x64_bf16 v[24:27], v[76:79], a[152:159], v0\ns_cbranch_vccnz .Lq64_%=\n.Lq60_%=:\n" \
    "v_smfmac_f32_16x16x64_bf16 v[28:31], v[76:79], a[160:167], v0\nds_read_b128 v[36:39], v3 offset:7168\ns_waitcnt vmcnt(1)\nv_max3_u32 v252, v80, v81, v82\n" \
    "v_cmp_u_f32 vcc, v252, v83\ns_nop 0\ns_cbranch_vccnz .Lq65_%=\n.Lq61_%=:\nv_smfmac_f32_16x16x64_bf16 v[20:23], v[80:83], v[48:55], v0\n" \
    "ds_write_b128 v1, v[80:83] offset:34816\nv_smfmac_f32_16x16x64_bf16 v[16:19], v[80:83], a[240:247], v0\nds_read_b128 v[32:35], v3 offset:6144\ns_waitcnt lgkmcnt(3)\n" \
    "s_barrier\nds_read_b128 v[88:91], v1 offset:24576\nv_smfmac_f32_16x16x64_bf16 v[24:27], v[80:83], a[248:255], v0\nds_read_b128 v[40:43], v3 offset:8192\n" \
    "v_smfmac_f32_16x16x64_bf16 v[28:31], v[80:83], v[92:99], v0\nds_read_b128 v[48:51], v1 offset:25600\ns_waitcnt vmcnt(0)\nv_max3_u32 v252, v84, v85, v86\n" \
    "v_cmp_u_f32 vcc, v252, v87\ns_nop 0\ns_cbranch_vccnz .Lq66_%=\n.Lq62_%=:\nv_smfmac_f32_16x16x64_bf16 v[20:23], v[84:87], v[196:203], v0\n" \
    "ds_write_b128 v1, v[84:87] offset:38912\nv_smfmac_f32_16x16x64_bf16 v[16:19], v[84:87], v[188:195], v0\nds_read_b128 v[52:55], v1 offset:27648\n" \
    "v_smfmac_f32_16x16x64_bf16 v[24:27], v[84:87], v[204:211], v0\nds_read_b128 v[72:75], v1 offset:28672\nv_smfmac_f32_16x16x64_bf16 v[28:31], v[84:87], v[212:219], v0\n" \
    "ds_read_b128 v[44:47], v3 offset:9216\ns_waitcnt lgkmcnt(6)\nv_smfmac_f32_16x16x64_bf16 v[20:23], v[88:91], v[56:63], v0\nds_read_b128 v[56:59], v1 offset:29696\n" \
    "v_smfmac_f32_16x16x64_bf16 v[16:19], v[88:91], a[0:7], v0\nds_read_b128 v[76:79], v3 offset:10240\nv_smfmac_f32_16x16x64_bf16 v[24:27], v[88:91], a[8:15], v0\n" \
    "ds_read_b128 v[80:83], v3 offset:11264\nv_smfmac_f32_16x16x64_bf16 v[28:31], v[88:91], a[16:23], v0\nds_read_b128 v[60:63], v1 offset:31744\ns_waitcnt lgkmcnt(7)\n" \
    "s_barrier\nv_smfmac_f32_16x16x64_bf16 v[20:23], v[48:51], v[64:71], v0\nds_read_b128 v[64:67], v3 offset:14336\n" \
    "v_smfmac_f32_16x16x64_bf16 v[16:19], v[48:51], a[24:31], v0\nds_read_b128 v[68:71], v3 offset:15360\ns_waitcnt lgkmcnt(8)\n" \
    "v_smfmac_f32_16x16x64_bf16 v[20:23], v[52:55], v[32:39], v0\nds_read_b128 v[32:35], v1 offset:32768\nv_smfmac_f32_16x16x64_bf16 v[16:19], v[52:55], a[72:79], v0\n" \
    "ds_read_b128 v[84:87], v3 offset:16384\ns_waitcnt lgkmcnt(8)\nv_smfmac_f32_16x16x64_bf16 v[20:23], v[72:75], v[40:47], v0\nds_read_b128 v[88:91], v3 offset:17408\n" \
    "v_smfmac_f32_16x16x64_bf16 v[16:19], v[72:75], a[96:103], v0\nds_read_b128 v[36:39], v1 offset:33792\ns_waitcnt lgkmcnt(7)\n" \
    "v_smfmac_f32_16x16x64_bf16 v[20:23], v[56:59], v[76:83], v0\nds_read_b128 v[40:43], v3 offset:18432\nv_smfmac_f32_16x16x64_bf16 v[16:19], v[56:59], a[120:127], v0\n" \
    "ds_read_b128 v[44:47], v3 offset:19456\ns_waitcnt lgkmcnt(6)\nv_smfmac_f32_16x16x64_bf16 v[20:23], v[60:63], v[64:71], v0\nds_read_b128 v[64:67], v1 offset:35840\n" \
    "v_smfmac_f32_16x16x64_bf16 v[16:19], v[60:63], a[168:175], v0\nds_read_b128 v[76:79], v3 offset:22528\ns_waitcnt lgkmcnt(5)\n" \
    "v_smfmac_f32_16x16x64_bf16 v[20:23], v[32:35], v[84:91], v0\nds_read_b128 v[80:83], v3 offset:23552\nv_smfmac_f32_16x16x64_bf16 v[16:19], v[32:35], a[192:199], v0\n" \
    "ds_read_b128 v[68:71], v1 offset:36864\ns_waitcnt lgkmcnt(4)\nv_smfmac_f32_16x16x64_bf16 v[20:23], v[36:39], v[40:47], v0\nds_read_b128 v[40:43], v1 offset:37888\n" \
    "v_smfmac_f32_16x16x64_bf16 v[16:19], v[36:39], a[216:223], v0\nds_read_b128 v[44:47], v1 offset:39936\ns_waitcnt lgkmcnt(3)\n" \
    "v_smfmac_f32_16x16x64_bf16 v[20:23], v[64:67], v[76:83], v0\nv_smfmac_f32_16x16x64_bf16 v[16:19], v[64:67], v[100:107], v0\ns_waitcnt lgkmcnt(2)\n" \
    "v_smfmac_f32_16x16x64_bf16 v[20:23], v[68:71], v[132:139], v0\ns_waitcnt lgkmcnt(1)\nv_smfmac_f32_16x16x64_bf16 v[20:23], v[40:43], v[164:171], v0\ns_waitcnt lgkmcnt(0)\n" \
    "v_smfmac_f32_16x16x64_bf16 v[20:23], v[44:47], v[228:235], v0\ns_nop 2\nv_smfmac_f32_16x16x64_bf16 v[16:19], v[68:71], v[124:131], v0\ns_nop 2\n" \
    "v_smfmac_f32_16x16x64_bf16 v[16:19], v[40:43], v[156:163], v0\nv_permlane32_swap_b32 v20, v22\nv_smfmac_f32_16x16x64_bf16 v[16:19], v[44:47], v[220:227], v0\ns_nop 1\n" \
    "v_permlane32_swap_b32 v21, v23\nv_smfmac_f32_16x16x64_bf16 v[24:27], v[48:51], a[32:39], v0\ns_nop 1\nv_add_f32 v20, v20, v22\n" \
    "v_smfmac_f32_16x16x64_bf16 v[28:31], v[48:51], a[40:47], v0\nv_add_f32 v21, v21, v23\nv_smfmac_f32_16x16x64_bf16 v[24:27], v[52:55], a[80:87], v0\nv_mul_f32 v20, s32, v20\n" \
    "v_smfmac_f32_16x16x64_bf16 v[28:31], v[52:55], a[88:95], v0\nv_mul_f32 v21, s32, v21\nv_smfmac_f32_16x16x64_bf16 v[24:27], v[72:75], a[104:111], v0\n" \
    "v_cvt_pk_bf16_f32 v20, v20, v21\nv_smfmac_f32_16x16x64_bf16 v[28:31], v[72:75], a[112:119], v0\nds_write_b16 v4, v20 offset:8\nv_permlane32_swap_b32 v16, v18\n" \
    "v_smfmac_f32_16x16x64_bf16 v[24:27], v[56:59], a[128:135], v0\nds_write_b16_d16_hi v4, v20 offset:24\nv_permlane32_swap_b32 v17, v19\n" \
    "v_smfmac_f32_16x16x64_bf16 v[24:27], v[60:63], a[176:183], v0\nds_read_b128 v[48:51], v1 offset:40960\nv_add_f32 v16, v16, v18\n" \
    "v_smfmac_f32_16x16x64_bf16 v[24:27], v[32:35], a[200:207], v0\nds_read_b128 v[52:55], v1 offset:45056\nv_add_f32 v17, v17, v19\n" \
    "v_smfmac_f32_16x16x64_bf16 v[24:27], v[36:39], a[224:231], v0\nds_read_b128 v[72:75], v1 offset:41984\nv_mul_f32 v16, s32, v16\n" \
    "v_smfmac_f32_16x16x64_bf16 v[24:27], v[64:67], v[108:115], v0\nds_read_b128 v[76:79], v1 offset:46080\nv_mul_f32 v17, s32, v17\n" \
    "v_smfmac_f32_16x16x64_bf16 v[24:27], v[68:71], v[140:147], v0\nds_read_b128 v[80:83], v1 offset:43008\nv_cvt_pk_bf16_f32 v16, v16, v17\n" \
    "v_smfmac_f32_16x16x64_bf16 v[24:27], v[40:43], v[172:179], v0\nds_write_b16 v4, v16 offset:0\nv_smfmac_f32_16x16x64_bf16 v[24:27], v[44:47], v[236:243], v0\n" \
    "ds_write_b16_d16_hi v4, v16 offset:16\ns_nop 1\nv_smfmac_f32_16x16x64_bf16 v[28:31], v[56:59], a[136:143], v0\nds_read_b128 v[56:59], v1 offset:47104\ns_nop 1\n" \
    "v_smfmac_f32_16x16x64_bf16 v[28:31], v[60:63], a[184:191], v0\nds_read_b128 v[60:63], v1 offset:44032\nv_permlane32_swap_b32 v24, v26\n" \
    "v_smfmac_f32_16x16x64_bf16 v[28:31], v[32:35], a[208:215], v0\nds_read_b128 v[32:35], v1 offset:48128\nv_permlane32_swap_b32 v25, v27\n" \
    "v_smfmac_f32_16x16x64_bf16 v[28:31], v[36:39], a[232:239], v0\nds_read_b128 v[36:39], v6 offset:0\nv_add_f32 v24, v24, v26\n" \
    "v_smfmac_f32_16x16x64_bf16 v[28:31], v[64:67], v[116:123], v0\nds_read_b128 v[64:67], v6 offset:512\nv_add_f32 v25, v25, v27\n" \
    "v_smfmac_f32_16x16x64_bf16 v[28:31], v[68:71], v[148:155], v0\nds_read_b128 v[68:71], v6 offset:1024\nv_cvt_pk_bf16_f32 v24, v24, v25\n" \
    "v_smfmac_f32_16x16x64_bf16 v[28:31], v[40:43], v[180:187], v0\nds_read_b128 v[40:43], v6 offset:1536\nv_smfmac_f32_16x16x64_bf16 v[28:31], v[44:47], v[244:251], v0\n" \
    "ds_write_b16 v4, v24 offset:2048\nds_write_b16_d16_hi v4, v24 offset:2064\ns_waitcnt lgkmcnt(5)\nv_mfma_f32_16x16x32_bf16 v[16:19], v[48:51], v[36:39], 0\ns_nop 2\n" \
    "v_mfma_f32_16x16x32_bf16 v[20:23], v[52:55], v[36:39], 0\nv_permlane32_swap_b32 v28, v30\nv_permlane32_swap_b32 v29, v31\nv_add_f32 v28, v28, v30\nv_add_f32 v29, v29, v31\n" \
    "v_cvt_pk_bf16_f32 v28, v28, v29\nds_write_b16 v4, v28 offset:2056\nds_write_b16_d16_hi v4, v28 offset:2072\ns_waitcnt lgkmcnt(6)\n" \
    "v_mfma_f32_16x16x32_bf16 v[16:19], v[72:75], v[64:67], v[16:19]\nv_mfma_f32_16x16x32_bf16 v[20:23], v[76:79], v[64:67], v[20:23]\ns_waitcnt lgkmcnt(5)\n" \
    "v_mfma_f32_16x16x32_bf16 v[16:19], v[80:83], v[68:71], v[16:19]\nds_read_b128 v[44:47], v8 offset:0\nds_read_b128 v[84:87], v8 offset:512\n" \
    "v_mfma_f32_16x16x32_bf16 v[20:23], v[56:59], v[68:71], v[20:23]\nds_read_b128 v[88:91], v8 offset:1024\nds_read_b128 v[28:31], v8 offset:1536\ns_waitcnt lgkmcnt(8)\n" \
    "v_mfma_f32_16x16x32_bf16 v[16:19], v[60:63], v[40:43], v[16:19]\nv_mfma_f32_16x16x32_bf16 v[20:23], v[32:35], v[40:43], v[20:23]\nv_mov_b32 v15, v15\nv_mov_b32 v15, v15\n" \
    "s_waitcnt lgkmcnt(3)\nv_mfma_f32_16x16x32_bf16 v[24:27], v[44:47], v[36:39], 0\ns_nop 2\ns_waitcnt lgkmcnt(2)\n" \
    "v_mov_b32_dpp v16, v20 quad_perm:[0,1,2,3] row_mask:0xf bank_mask:0xc\nv_mov_b32_dpp v17, v21 quad_perm:[0,1,2,3] row_mask:0xf bank_mask:0xc\n" \
    "v_mfma_f32_16x16x32_bf16 v[24:27], v[84:87], v[64:67], v[24:27]\ns_waitcnt lgkmcnt(1)\nv_mov_b32_dpp v18, v22 quad_perm:[0,1,2,3] row_mask:0xf bank_mask:0xc\n" \
    "v_mov_b32_dpp v19, v23 quad_perm:[0,1,2,3] row_mask:0xf bank_mask:0xc\nv_mfma_f32_16x16x32_bf16 v[24:27], v[88:91], v[68:71], v[24:27]\ns_waitcnt lgkmcnt(0)\n" \
    "v_max3_f32 v254, v16, v17, v18\nv_max_f32 v254, v254, v19\nv_mfma_f32_16x16x32_bf16 v[24:27], v[28:31], v[40:43], v[24:27]\ns_nop 0\n" \
    "v_max_f32_dpp v252, v254, v254 row_ror:8 row_mask:0xf bank_mask:0xf\nv_max_f32_dpp v253, v254, v254 row_ror:8 row_mask:0xf bank_mask:0xf\ns_nop 1\n" \
    "v_permlane32_swap_b32 v252, v253\nv_max_f32 v252, v252, v253\ns_xor_b32 s74, s82, 0x4000\nv_max3_f32 v254, v252, v24, v25\nv_max3_f32 v252, v254, v26, v27\n" \
    "v_max3_f32 v253, v254, v26, v27\ns_nop 1\nv_permlane16_swap_b32 v252, v253\nv_max_f32 v252, v252, v253\n" \
    "v_pk_add_f32 v[16:17], v[16:17], v[252:253] op_sel_hi:[1,0] neg_lo:[0,1] neg_hi:[0,1]\n" \
    "v_pk_add_f32 v[18:19], v[18:19], v[252:253] op_sel_hi:[1,0] neg_lo:[0,1] neg_hi:[0,1]\n" \
    "v_pk_add_f32 v[24:25], v[24:25], v[252:253] op_sel_hi:[1,0] neg_lo:[0,1] neg_hi:[0,1]\n" \
    "v_pk_add_f32 v[26:27], v[26:27], v[252:253] op_sel_hi:[1,0] neg_lo:[0,1] neg_hi:[0,1]\nv_exp_f32 v16, v16\nv_exp_f32 v17, v17\nv_exp_f32 v18, v18\nv_exp_f32 v19, v19\n" \
    "v_exp_f32 v24, v24\nv_pk_add_f32 v[254:255], v[16:17], v[18:19]\nv_exp_f32 v25, v25\nv_add_f32 v254, v254, v255\nv_exp_f32 v26, v26\nv_exp_f32 v27, v27\n" \
    "v_add_f32_dpp v252, v254, v254 row_ror:8 row_mask:0xf bank_mask:0xf\nv_add_f32_dpp v253, v254, v254 row_ror:8 row_mask:0xf bank_mask:0xf\n" \
    "v_pk_add_f32 v[254:255], v[24:25], v[26:27]\ns_nop 0\nv_permlane32_swap_b32 v252, v253\nv_pk_add_f32 v[254:255], v[254:255], v[252:253]\n" \
    "v_pk_add_f32 v[252:253], v[254:255], v[254:255] op_sel:[0,1] op_sel_hi:[1,0]\ns_nop 1\nv_permlane16_swap_b32 v252, v253\nv_add_f32 v252, v252, v253\n" \
    "v_cmp_u_f32 vcc, v252, v252\nv_rcp_f32 v252, v252\ns_cbranch_vccnz .Lq14_%=\nv_pk_mul_f32 v[16:17], v[16:17], v[252:253] op_sel_hi:[1,0]\n" \
    "v_pk_mul_f32 v[24:25], v[24:25], v[252:253] op_sel_hi:[1,0]\nv_cvt_pk_bf16_f32 v20, v16, v17\nv_cvt_pk_bf16_f32 v22, v24, v25\n" \
    "v_pk_mul_f32 v[18:19], v[18:19], v[252:253] op_sel_hi:[1,0]\nv_pk_mul_f32 v[26:27], v[26:27], v[252:253] op_sel_hi:[1,0]\nv_cvt_pk_bf16_f32 v21, v18, v19\n" \
    "v_cvt_pk_bf16_f32 v23, v26, v27\nv_permlane32_swap_b32 v20, v22\ns_nop 0\nv_permlane32_swap_b32 v21, v23\nbuffer_store_dwordx4 v[20:23], v7, s[64:67], s82 offen sc1\n" \
    "buffer_store_dwordx4 v[20:23], v7, s[64:67], s74 offen sc1\ns_branch .Lq3_%=\n.Lq7_%=:\nds_read_b128 v[32:35], v3 offset:6144\nds_read_b128 v[36:39], v3 offset:7168\n" \
    "ds_read_b128 v[40:43], v3 offset:14336\nds_read_b128 v[44:47], v3 offset:15360\nds_read_b128 v[48:51], v3 offset:22528\nds_read_b128 v[52:55], v3 offset:23552\n" \
    "ds_read_b128 v[56:59], v3 offset:0\nds_read_b128 v[60:63], v3 offset:1024\nds_read_b128 v[64:67], v3 offset:2048\nds_read_b128 v[68:71], v3 offset:3072\n.Lq72_%=:\n" \
    "s_memrealtime s[72:73]\ns_waitcnt lgkmcnt(0)\ns_mov_b32 s68, 0\ns_sub_u32 s76, s80, s45\ns_sub_u32 s69, s72, s76\ns_cmp_lt_i32 s69, -8\ns_cbranch_scc1 .Lq75_%=\n.Lq73_%=:\n" \
    "s_xor_b32 s90, s81, 0x800\n.Lq77_%=:\ns_memrealtime s[72:73]\ns_waitcnt lgkmcnt(0)\ns_sub_u32 s72, s72, s80\ns_cmp_lt_i32 s72, 0\ns_cbranch_scc1 .Lq77_%=\n" \
    "buffer_load_dwordx4 v[72:75], v2, s[64:67], s81 offen offset:0 sc1\ns_nop 7\nbuffer_load_dwordx4 v[76:79], v2, s[64:67], s81 offen offset:1024 sc1\ns_nop 7\n" \
    "buffer_load_dwordx4 v[80:83], v2, s[64:67], s90 offen offset:0 sc1\ns_nop 7\nbuffer_load_dwordx4 v[84:87], v2, s[64:67], s90 offen offset:1024 sc1\nv_mov_b64 v[16:17], 0\n" \
    "v_mov_b64 v[18:19], 0\nv_mov_b64 v[20:21], 0\nv_mov_b64 v[22:23], 0\nv_mov_b64 v[24:25], 0\nv_mov_b64 v[26:27], 0\nv_mov_b64 v[28:29], 0\nv_mov_b64 v[30:31], 0\n" \
    "s_waitcnt vmcnt(3)\nv_smfmac_f32_16x16x64_bf16 v[20:23], v[72:75], v[32:39], v0\nds_write_b128 v1, v[72:75] offset:27648\nv_max3_u32 v252, v72, v73, v74\n" \
    "v_smfmac_f32_16x16x64_bf16 v[16:19], v[72:75], a[72:79], v0\nv_smfmac_f32_16x16x64_bf16 v[24:27], v[72:75], a[80:87], v0\n" \
    "v_smfmac_f32_16x16x64_bf16 v[28:31], v[72:75], a[88:95], v0\ns_waitcnt vmcnt(2)\nds_write_b128 v1, v[76:79] offset:31744\nv_max3_u32 v252, v252, v75, v76\n" \
    "v_smfmac_f32_16x16x64_bf16 v[20:23], v[76:79], v[40:47], v0\nv_max3_u32 v252, v252, v77, v78\nv_smfmac_f32_16x16x64_bf16 v[16:19], v[76:79], a[168:175], v0\n" \
    "v_cmp_u_f32 vcc, v252, v79\nv_smfmac_f32_16x16x64_bf16 v[24:27], v[76:79], a[176:183], v0\ns_cbranch_vccnz .Lq83_%=\n.Lq79_%=:\n" \
    "v_smfmac_f32_16x16x64_bf16 v[28:31], v[76:79], a[184:191], v0\nds_read_b128 v[36:39], v3 offset:5120\ns_waitcnt vmcnt(1)\nv_max3_u32 v252, v80, v81, v82\n" \
    "v_cmp_u_f32 vcc, v252, v83\ns_nop 0\ns_cbranch_vccnz .Lq84_%=\n.Lq80_%=:\nv_smfmac_f32_16x16x64_bf16 v[20:23], v[80:83], v[48:55], v0\n" \
    "ds_write_b128 v1, v[80:83] offset:35840\nv_smfmac_f32_16x16x64_bf16 v[16:19], v[80:83], v[100:107], v0\nds_read_b128 v[32:35], v3 offset:4096\ns_waitcnt lgkmcnt(3)\n" \
    "s_barrier\nds_read_b128 v[88:91], v1 offset:24576\nv_smfmac_f32_16x16x64_bf16 v[24:27], v[80:83], v[108:115], v0\nds_read_b128 v[40:43], v3 offset:8192\n" \
    "v_smfmac_f32_16x16x64_bf16 v[28:31], v[80:83], v[116:123], v0\nds_read_b128 v[48:51], v1 offset:25600\ns_waitcnt vmcnt(0)\nv_max3_u32 v252, v84, v85, v86\n" \
    "v_cmp_u_f32 vcc, v252, v87\ns_nop 0\ns_cbranch_vccnz .Lq85_%=\n.Lq81_%=:\nv_smfmac_f32_16x16x64_bf16 v[20:23], v[84:87], v[228:235], v0\n" \
    "ds_write_b128 v1, v[84:87] offset:39936\nv_smfmac_f32_16x16x64_bf16 v[16:19], v[84:87], v[220:227], v0\nds_read_b128 v[52:55], v1 offset:26624\n" \
    "v_smfmac_f32_16x16x64_bf16 v[24:27], v[84:87], v[236:243], v0\nds_read_b128 v[72:75], v1 offset:28672\nv_smfmac_f32_16x16x64_bf16 v[28:31], v[84:87], v[244:251], v0\n" \
    "ds_read_b128 v[44:47], v3 offset:9216\ns_waitcnt lgkmcnt(6)\nv_smfmac_f32_16x16x64_bf16 v[20:23], v[88:91], v[56:63], v0\nds_read_b128 v[56:59], v1 offset:29696\n" \
    "v_smfmac_f32_16x16x64_bf16 v[16:19], v[88:91], a[0:7], v0\nds_read_b128 v[76:79], v3 offset:10240\nv_smfmac_f32_16x16x64_bf16 v[24:27], v[88:91], a[8:15], v0\n" \
    "ds_read_b128 v[80:83], v3 offset:11264\nv_smfmac_f32_16x16x64_bf16 v[28:31], v[88:91], a[16:23], v0\nds_read_b128 v[60:63], v1 offset:30720\ns_waitcnt lgkmcnt(7)\n" \
    "s_barrier\nv_smfmac_f32_16x16x64_bf16 v[20:23], v[48:51], v[64:71], v0\nds_read_b128 v[64:67], v3 offset:12288\n" \
    "v_smfmac_f32_16x16x64_bf16 v[16:19], v[48:51], a[24:31], v0\nds_read_b128 v[68:71], v3 offset:13312\ns_waitcnt lgkmcnt(8)\n" \
    "v_smfmac_f32_16x16x64_bf16 v[20:23], v[52:55], v[32:39], v0\nds_read_b128 v[32:35], v1 offset:32768\nv_smfmac_f32_16x16x64_bf16 v[16:19], v[52:55], a[48:55], v0\n" \
    "ds_read_b128 v[84:87], v3 offset:16384\ns_waitcnt lgkmcnt(8)\nv_smfmac_f32_16x16x64_bf16 v[20:23], v[72:75], v[40:47], v0\nds_read_b128 v[88:91], v3 offset:17408\n" \
    "v_smfmac_f32_16x16x64_bf16 v[16:19], v[72:75], a[96:103], v0\nds_read_b128 v[36:39], v1 offset:33792\ns_waitcnt lgkmcnt(7)\n" \
    "v_smfmac_f32_16x16x64_bf16 v[20:23], v[56:59], v[76:83], v0\nds_read_b128 v[40:43], v3 offset:18432\nv_smfmac_f32_16x16x64_bf16 v[16:19], v[56:59], a[120:127], v0\n" \
    "ds_read_b128 v[44:47], v3 offset:19456\ns_waitcnt lgkmcnt(6)\nv_smfmac_f32_16x16x64_bf16 v[20:23], v[60:63], v[64:71], v0\nds_read_b128 v[64:67], v1 offset:34816\n" \
    "v_smfmac_f32_16x16x64_bf16 v[16:19], v[60:63], a[144:151], v0\nds_read_b128 v[76:79], v3 offset:20480\ns_waitcnt lgkmcnt(5)\n" \
    "v_smfmac_f32_16x16x64_bf16 v[20:23], v[32:35], v[84:91], v0\nds_read_b128 v[80:83], v3 offset:21504\nv_smfmac_f32_16x16x64_bf16 v[16:19], v[32:35], a[192:199], v0\n" \
    "ds_read_b128 v[68:71], v1 offset:36864\ns_waitcnt lgkmcnt(4)\nv_smfmac_f32_16x16x64_bf16 v[20:23], v[36:39], v[40:47], v0\nds_read_b128 v[40:43], v1 offset:37888\n" \
    "v_smfmac_f32_16x16x64_bf16 v[16:19], v[36:39], a[216:223], v0\nds_read_b128 v[44:47], v1 offset:38912\ns_waitcnt lgkmcnt(3)\n" \
    "v_smfmac_f32_16x16x64_bf16 v[20:23], v[64:67], v[76:83], v0\nv_smfmac_f32_16x16x64_bf16 v[16:19], v[64:67], a[240:247], v0\ns_waitcnt lgkmcnt(2)\n" \
    "v_smfmac_f32_16x16x64_bf16 v[20:23], v[68:71], v[132:139], v0\ns_waitcnt lgkmcnt(1)\nv_smfmac_f32_16x16x64_bf16 v[20:23], v[40:43], v[164:171], v0\ns_waitcnt lgkmcnt(0)\n" \
    "v_smfmac_f32_16x16x64_bf16 v[20:23], v[44:47], v[196:203], v0\ns_nop 2\nv_smfmac_f32_16x16x64_bf16 v[16:19], v[68:71], v[124:131], v0\ns_nop 2\n" \
    "v_smfmac_f32_16x16x64_bf16 v[16:19], v[40:43], v[156:163], v0\nv_permlane32_swap_b32 v20, v22\nv_smfmac_f32_16x16x64_bf16 v[16:19], v[44:47], v[188:195], v0\ns_nop 1\n" \
    "v_permlane32_swap_b32 v21, v23\nv_smfmac_f32_16x16x64_bf16 v[24:27], v[48:51], a[32:39], v0\ns_nop 1\nv_add_f32 v20, v20, v22\n" \
    "v_smfmac_f32_16x16x64_bf16 v[28:31], v[48:51], a[40:47], v0\nv_add_f32 v21, v21, v23\nv_smfmac_f32_16x16x64_bf16 v[24:27], v[52:55], a[56:63], v0\nv_mul_f32 v20, s32, v20\n" \
    "v_smfmac_f32_16x16x64_bf16 v[28:31], v[52:55], a[64:71], v0\nv_mul_f32 v21, s32, v21\nv_smfmac_f32_16x16x64_bf16 v[24:27], v[72:75], a[104:111], v0\n" \
    "v_cvt_pk_bf16_f32 v20, v20, v21\nv_smfmac_f32_16x16x64_bf16 v[28:31], v[72:75], a[112:119], v0\nds_write_b16 v4, v20 offset:8\nv_permlane32_swap_b32 v16, v18\n" \
    "v_smfmac_f32_16x16x64_bf16 v[24:27], v[56:59], a[128:135], v0\nds_write_b16_d16_hi v4, v20 offset:24\nv_permlane32_swap_b32 v17, v19\n" \
    "v_smfmac_f32_16x16x64_bf16 v[24:27], v[60:63], a[152:159], v0\nds_read_b128 v[48:51], v1 offset:40960\nv_add_f32 v16, v16, v18\n" \
    "v_smfmac_f32_16x16x64_bf16 v[24:27], v[32:35], a[200:207], v0\nds_read_b128 v[52:55], v1 offset:45056\nv_add_f32 v17, v17, v19\n" \
    "v_smfmac_f32_16x16x64_bf16 v[24:27], v[36:39], a[224:231], v0\nds_read_b128 v[72:75], v1 offset:41984\nv_mul_f32 v16, s32, v16\n" \
    "v_smfmac_f32_16x16x64_bf16 v[24:27], v[64:67], a[248:255], v0\nds_read_b128 v[76:79], v1 offset:46080\nv_mul_f32 v17, s32, v17\n" \
    "v_smfmac_f32_16x16x64_bf16 v[24:27], v[68:71], v[140:147], v0\nds_read_b128 v[80:83], v1 offset:43008\nv_cvt_pk_bf16_f32 v16, v16, v17\n" \
    "v_smfmac_f32_16x16x64_bf16 v[24:27], v[40:43], v[172:179], v0\nds_write_b16 v4, v16 offset:0\nv_smfmac_f32_16x16x64_bf16 v[24:27], v[44:47], v[204:211], v0\n" \
    "ds_write_b16_d16_hi v4, v16 offset:16\ns_nop 1\nv_smfmac_f32_16x16x64_bf16 v[28:31], v[56:59], a[136:143], v0\nds_read_b128 v[56:59], v1 offset:47104\ns_nop 1\n" \
    "v_smfmac_f32_16x16x64_bf16 v[28:31], v[60:63], a[160:167], v0\nds_read_b128 v[60:63], v1 offset:44032\nv_permlane32_swap_b32 v24, v26\n" \
    "v_smfmac_f32_16x16x64_bf16 v[28:31], v[32:35], a[208:215], v0\nds_read_b128 v[32:35], v1 offset:48128\nv_permlane32_swap_b32 v25, v27\n" \
    "v_smfmac_f32_16x16x64_bf16 v[28:31], v[36:39], a[232:239], v0\nds_read_b128 v[36:39], v6 offset:0\nv_add_f32 v24, v24, v26\n" \
    "v_smfmac_f32_16x16x64_bf16 v[28:31], v[64:67], v[92:99], v0\nds_read_b128 v[64:67], v6 offset:512\nv_add_f32 v25, v25, v27\n" \
    "v_smfmac_f32_16x16x64_bf16 v[28:31], v[68:71], v[148:155], v0\nds_read_b128 v[68:71], v6 offset:1024\nv_cvt_pk_bf16_f32 v24, v24, v25\n" \
    "v_smfmac_f32_16x16x64_bf16 v[28:31], v[40:43], v[180:187], v0\nds_read_b128 v[40:43], v6 offset:1536\nv_smfmac_f32_16x16x64_bf16 v[28:31], v[44:47], v[212:219], v0\n" \
    "ds_write_b16 v4, v24 offset:2048\nds_write_b16_d16_hi v4, v24 offset:2064\ns_waitcnt lgkmcnt(5)\nv_mfma_f32_16x16x32_bf16 v[16:19], v[48:51], v[36:39], 0\ns_nop 2\n" \
    "v_mfma_f32_16x16x32_bf16 v[20:23], v[52:55], v[36:39], 0\nv_permlane32_swap_b32 v28, v30\nv_permlane32_swap_b32 v29, v31\nv_add_f32 v28, v28, v30\nv_add_f32 v29, v29, v31\n" \
    "v_cvt_pk_bf16_f32 v28, v28, v29\nds_write_b16 v4, v28 offset:2056\nds_write_b16_d16_hi v4, v28 offset:2072\ns_waitcnt lgkmcnt(6)\n" \
    "v_mfma_f32_16x16x32_bf16 v[16:19], v[72:75], v[64:67], v[16:19]\nv_mfma_f32_16x16x32_bf16 v[20:23], v[76:79], v[64:67], v[20:23]\ns_waitcnt lgkmcnt(5)\n" \
    "v_mfma_f32_16x16x32_bf16 v[16:19], v[80:83], v[68:71], v[16:19]\nds_read_b128 v[44:47], v8 offset:0\nds_read_b128 v[84:87], v8 offset:512\n" \
    "v_mfma_f32_16x16x32_bf16 v[20:23], v[56:59], v[68:71], v[20:23]\nds_read_b128 v[88:91], v8 offset:1024\nds_read_b128 v[28:31], v8 offset:1536\ns_waitcnt lgkmcnt(8)\n" \
    "v_mfma_f32_16x16x32_bf16 v[16:19], v[60:63], v[40:43], v[16:19]\nv_mfma_f32_16x16x32_bf16 v[20:23], v[32:35], v[40:43], v[20:23]\nv_mov_b32 v15, v15\nv_mov_b32 v15, v15\n" \
    "s_waitcnt lgkmcnt(3)\nv_mfma_f32_16x16x32_bf16 v[24:27], v[44:47], v[36:39], 0\ns_nop 2\ns_waitcnt lgkmcnt(2)\n" \
    "v_mov_b32_dpp v16, v20 quad_perm:[0,1,2,3] row_mask:0xf bank_mask:0xc\nv_mov_b32_dpp v17, v21 quad_perm:[0,1,2,3] row_mask:0xf bank_mask:0xc\n" \
    "v_mfma_f32_16x16x32_bf16 v[24:27], v[84:87], v[64:67], v[24:27]\ns_waitcnt lgkmcnt(1)\nv_mov_b32_dpp v18, v22 quad_perm:[0,1,2,3] row_mask:0xf bank_mask:0xc\n" \
    "v_mov_b32_dpp v19, v23 quad_perm:[0,1,2,3] row_mask:0xf bank_mask:0xc\nv_mfma_f32_16x16x32_bf16 v[24:27], v[88:91], v[68:71], v[24:27]\ns_waitcnt lgkmcnt(0)\n" \
    "v_max3_f32 v254, v16, v17, v18\nv_max_f32 v254, v254, v19\nv_mfma_f32_16x16x32_bf16 v[24:27], v[28:31], v[40:43], v[24:27]\ns_nop 0\n" \
    "v_max_f32_dpp v252, v254, v254 row_ror:8 row_mask:0xf bank_mask:0xf\nv_max_f32_dpp v253, v254, v254 row_ror:8 row_mask:0xf bank_mask:0xf\ns_nop 1\n" \
    "v_permlane32_swap_b32 v252, v253\nv_max_f32 v252, v252, v253\ns_xor_b32 s74, s82, 0x4000\nv_max3_f32 v254, v252, v24, v25\nv_max3_f32 v252, v254, v26, v27\n" \
    "v_max3_f32 v253, v254, v26, v27\ns_nop 1\nv_permlane16_swap_b32 v252, v253\nv_max_f32 v252, v252, v253\n" \
    "v_pk_add_f32 v[16:17], v[16:17], v[252:253] op_sel_hi:[1,0] neg_lo:[0,1] neg_hi:[0,1]\n" \
    "v_pk_add_f32 v[18:19], v[18:19], v[252:253] op_sel_hi:[1,0] neg_lo:[0,1] neg_hi:[0,1]\n" \
    "v_pk_add_f32 v[24:25], v[24:25], v[252:253] op_sel_hi:[1,0] neg_lo:[0,1] neg_hi:[0,1]\n" \
    "v_pk_add_f32 v[26:27], v[26:27], v[252:253] op_sel_hi:[1,0] neg_lo:[0,1] neg_hi:[0,1]\nv_exp_f32 v16, v16\nv_exp_f32 v17, v17\nv_exp_f32 v18, v18\nv_exp_f32 v19, v19\n" \
    "v_exp_f32 v24, v24\nv_pk_add_f32 v[254:255], v[16:17], v[18:19]\nv_exp_f32 v25, v25\nv_add_f32 v254, v254, v255\nv_exp_f32 v26, v26\nv_exp_f32 v27, v27\n" \
    "v_add_f32_dpp v252, v254, v254 row_ror:8 row_mask:0xf bank_mask:0xf\nv_add_f32_dpp v253, v254, v254 row_ror:8 row_mask:0xf bank_mask:0xf\n" \
    "v_pk_add_f32 v[254:255], v[24:25], v[26:27]\ns_nop 0\nv_permlane32_swap_b32 v252, v253\nv_pk_add_f32 v[254:255], v[254:255], v[252:253]\n" \
    "v_pk_add_f32 v[252:253], v[254:255], v[254:255] op_sel:[0,1] op_sel_hi:[1,0]\ns_nop 1\nv_permlane16_swap_b32 v252, v253\nv_add_f32 v252, v252, v253\n" \
    "v_cmp_u_f32 vcc, v252, v252\nv_rcp_f32 v252, v252\ns_cbranch_vccnz .Lq14_%=\nv_pk_mul_f32 v[16:17], v[16:17], v[252:253] op_sel_hi:[1,0]\n" \
    "v_pk_mul_f32 v[24:25], v[24:25], v[252:253] op_sel_hi:[1,0]\nv_cvt_pk_bf16_f32 v20, v16, v17\nv_cvt_pk_bf16_f32 v22, v24, v25\n" \
    "v_pk_mul_f32 v[18:19], v[18:19], v[252:253] op_sel_hi:[1,0]\nv_pk_mul_f32 v[26:27], v[26:27], v[252:253] op_sel_hi:[1,0]\nv_cvt_pk_bf16_f32 v21, v18, v19\n" \
    "v_cvt_pk_bf16_f32 v23, v26, v27\nv_permlane32_swap_b32 v20, v22\ns_nop 0\nv_permlane32_swap_b32 v21, v23\nbuffer_store_dwordx4 v[20:23], v7, s[64:67], s82 offen sc1\n" \
    "buffer_store_dwordx4 v[20:23], v7, s[64:67], s74 offen sc1\n.Lq3_%=:\nds_read_b128 v[36:39], v5 offset:0\nds_read_b128 v[64:67], v5 offset:1024\n" \
    "ds_read_b128 v[68:71], v5 offset:2048\nds_read_b128 v[40:43], v5 offset:3072\ns_mov_b32 s40, s84\ns_add_u32 s35, s35, 2\ns_barrier\ns_waitcnt lgkmcnt(3)\n" \
    "v_mfma_f32_16x16x32_bf16 v[16:19], v[48:51], v[36:39], 0\nv_mfma_f32_16x16x32_bf16 v[20:23], v[52:55], v[36:39], 0\ns_waitcnt lgkmcnt(2)\n" \
    "v_mfma_f32_16x16x32_bf16 v[16:19], v[72:75], v[64:67], v[16:19]\nv_mfma_f32_16x16x32_bf16 v[20:23], v[76:79], v[64:67], v[20:23]\ns_waitcnt lgkmcnt(1)\n" \
    "v_mfma_f32_16x16x32_bf16 v[16:19], v[80:83], v[68:71], v[16:19]\nv_mfma_f32_16x16x32_bf16 v[20:23], v[56:59], v[68:71], v[20:23]\ns_waitcnt lgkmcnt(0)\n" \
    "v_mfma_f32_16x16x32_bf16 v[16:19], v[60:63], v[40:43], v[16:19]\nv_mfma_f32_16x16x32_bf16 v[20:23], v[32:35], v[40:43], v[20:23]\n" \
    "v_mfma_f32_16x16x32_bf16 v[24:27], v[44:47], v[36:39], 0\nv_mfma_f32_16x16x32_bf16 v[24:27], v[84:87], v[64:67], v[24:27]\n" \
    "v_mfma_f32_16x16x32_bf16 v[24:27], v[88:91], v[68:71], v[24:27]\nv_mfma_f32_16x16x32_bf16 v[24:27], v[28:31], v[40:43], v[24:27]\ns_cmp_lt_u32 s35, 64\n" \
    "s_cbranch_scc1 .Lq2_%=\n.Lq8_%=:\ns_nop 1\nv_mov_b32_dpp v16, v20 quad_perm:[0,1,2,3] row_mask:0xf bank_mask:0xc\n" \
    "v_mov_b32_dpp v17, v21 quad_perm:[0,1,2,3] row_mask:0xf bank_mask:0xc\nv_mov_b32_dpp v18, v22 quad_perm:[0,1,2,3] row_mask:0xf bank_mask:0xc\n" \
    "v_mov_b32_dpp v19, v23 quad_perm:[0,1,2,3] row_mask:0xf bank_mask:0xc\nv_mov_b32 v254, 0xff800000\nv_cvt_pk_bf16_f32 v16, 0, v16\nv_cvt_pk_bf16_f32 v17, 0, v17\n" \
    "v_cvt_pk_bf16_f32 v18, 0, v18\nv_cvt_pk_bf16_f32 v19, 0, v19\nv_mul_f32 v16, s33, v16\nv_mul_f32 v17, s33, v17\nv_mul_f32 v18, s33, v18\nv_mul_f32 v19, s33, v19\n" \
    "v_cvt_pk_bf16_f32 v16, 0, v16\nv_cvt_pk_bf16_f32 v17, 0, v17\nv_cvt_pk_bf16_f32 v18, 0, v18\nv_cvt_pk_bf16_f32 v19, 0, v19\nv_cndmask_b32 v16, v254, v16, s[50:51]\n" \
    "v_cndmask_b32 v17, v254, v17, s[52:53]\nv_cndmask_b32 v18, v254, v18, s[54:55]\nv_cndmask_b32 v19, v254, v19, s[56:57]\nv_max3_f32 v254, v16, v17, v18\n" \
    "v_max_f32 v254, v254, v19\ns_nop 1\nv_max_f32_dpp v252, v254, v254 row_ror:8 row_mask:0xf bank_mask:0xf\n" \
    "v_max_f32_dpp v253, v254, v254 row_ror:8 row_mask:0xf bank_mask:0xf\ns_nop 1\nv_permlane32_swap_b32 v252, v253\nv_max_f32 v252, v252, v253\nv_cvt_pk_bf16_f32 v24, 0, v24\n" \
    "v_cvt_pk_bf16_f32 v25, 0, v25\nv_cvt_pk_bf16_f32 v26, 0, v26\nv_cvt_pk_bf16_f32 v27, 0, v27\nv_mul_f32 v24, s33, v24\nv_mul_f32 v25, s33, v25\nv_mul_f32 v26, s33, v26\n" \
    "v_mul_f32 v27, s33, v27\nv_cvt_pk_bf16_f32 v24, 0, v24\nv_cvt_pk_bf16_f32 v25, 0, v25\nv_cvt_pk_bf16_f32 v26, 0, v26\nv_cvt_pk_bf16_f32 v27, 0, v27\n" \
    "v_max3_f32 v254, v252, v24, v25\nv_max3_f32 v252, v254, v26, v27\nv_max3_f32 v253, v254, v26, v27\ns_nop 1\nv_permlane16_swap_b32 v252, v253\nv_max_f32 v252, v252, v253\n" \
    "v_pk_add_f32 v[16:17], v[16:17], v[252:253] op_sel_hi:[1,0] neg_lo:[0,1] neg_hi:[0,1]\n" \
    "v_pk_add_f32 v[18:19], v[18:19], v[252:253] op_sel_hi:[1,0] neg_lo:[0,1] neg_hi:[0,1]\n" \
    "v_pk_add_f32 v[24:25], v[24:25], v[252:253] op_sel_hi:[1,0] neg_lo:[0,1] neg_hi:[0,1]\n" \
    "v_pk_add_f32 v[26:27], v[26:27], v[252:253] op_sel_hi:[1,0] neg_lo:[0,1] neg_hi:[0,1]\nv_mul_f32 v16, s34, v16\nv_mul_f32 v17, s34, v17\nv_mul_f32 v18, s34, v18\n" \
    "v_mul_f32 v19, s34, v19\nv_mul_f32 v24, s34, v24\nv_mul_f32 v25, s34, v25\nv_mul_f32 v26, s34, v26\nv_mul_f32 v27, s34, v27\nv_exp_f32 v16, v16\nv_exp_f32 v17, v17\n" \
    "v_exp_f32 v18, v18\nv_exp_f32 v19, v19\nv_exp_f32 v24, v24\nv_pk_add_f32 v[254:255], v[16:17], v[18:19]\nv_exp_f32 v25, v25\nv_add_f32 v254, v254, v255\n" \
    "v_exp_f32 v26, v26\nv_exp_f32 v27, v27\nv_add_f32_dpp v252, v254, v254 row_ror:8 row_mask:0xf bank_mask:0xf\n" \
    "v_add_f32_dpp v253, v254, v254 row_ror:8 row_mask:0xf bank_mask:0xf\nv_pk_add_f32 v[254:255], v[24:25], v[26:27]\ns_nop 0\nv_permlane32_swap_b32 v252, v253\n" \
    "v_pk_add_f32 v[254:255], v[254:255], v[252:253]\nv_pk_add_f32 v[252:253], v[254:255], v[254:255] op_sel:[0,1] op_sel_hi:[1,0]\ns_nop 1\nv_permlane16_swap_b32 v252, v253\n" \
    "v_add_f32 v252, v252, v253\nv_rcp_f32 v252, v252\ns_nop 0\nv_pk_mul_f32 v[16:17], v[16:17], v[252:253] op_sel_hi:[1,0]\n" \
    "v_pk_mul_f32 v[24:25], v[24:25], v[252:253] op_sel_hi:[1,0]\nv_cvt_pk_bf16_f32 v20, v16, v17\nv_cvt_pk_bf16_f32 v22, v24, v25\n" \
    "v_pk_mul_f32 v[18:19], v[18:19], v[252:253] op_sel_hi:[1,0]\nv_pk_mul_f32 v[26:27], v[26:27], v[252:253] op_sel_hi:[1,0]\nv_cvt_pk_bf16_f32 v21, v18, v19\n" \
    "v_cvt_pk_bf16_f32 v23, v26, v27\nv_permlane32_swap_b32 v20, v22\ns_nop 0\nv_permlane32_swap_b32 v21, v23\ns_xor_b32 s74, s83, 0x2000\n" \
    "buffer_store_dwordx4 v[20:23], v7, s[64:67], s83 offen sc1\nbuffer_store_dwordx4 v[20:23], v7, s[64:67], s74 offen sc1\ns_cmp_lt_u32 s35, 64\ns_cbranch_scc1 .Lq9_%=\n" \
    "s_endpgm\n.Lq10_%=:\ns_mov_b32 s76, 128\nbuffer_load_dword v253, off, s[64:67], s76 sc1\ns_waitcnt vmcnt(0)\nv_readfirstlane_b32 s74, v253\ns_nop 0\ns_cmp_ge_u32 s74, 32\n" \
    "s_cbranch_scc1 .Lq11_%=\ns_sleep 8\ns_add_u32 s68, s68, 1\ns_and_b32 s69, s68, 63\ns_cbranch_scc1 .Lq10_%=\ns_cmp_gt_u32 s68, 0x400000\ns_cbranch_scc1 .Lq1_%=\n" \
    "buffer_load_dword v253, off, s[64:67], 0 sc1\ns_waitcnt vmcnt(0)\nv_readfirstlane_b32 s69, v253\ns_nop 0\ns_cmp_eq_u32 s69, 1\ns_cbranch_scc0 .Lq10_%=\ns_endpgm\n" \
    ".Lq12_%=:\ns_sleep 6\nbuffer_load_dword v13, off, s[64:67], s42 sc1\ns_waitcnt vmcnt(0)\nv_readfirstlane_b32 s74, v13\ns_nop 0\ns_and_b32 s75, s74, 127\n" \
    "s_lshr_b32 s69, s74, 8\ns_sub_u32 s69, s69, s80\ns_bfe_i32 s69, s69, 0x180000\ns_cmp_eq_u32 s75, 0\ns_cselect_b32 s69, 0, s69\ns_add_u32 s80, s80, s69\n" \
    "s_lshl_b32 s74, s74, 5\ns_and_b32 s74, s74, 0x1000\ns_andn2_b32 s81, s81, 0x1000\ns_or_b32 s81, s81, s74\ns_lshl_b32 s74, s74, 2\ns_andn2_b32 s82, s82, 0x4000\n" \
    "s_or_b32 s82, s82, s74\ns_add_u32 s68, s68, 1\ns_and_b32 s69, s68, 63\ns_cbranch_scc1 .Lq11_%=\ns_cmp_gt_u32 s68, 0x400000\ns_cbranch_scc1 .Lq1_%=\n" \
    "buffer_load_dword v253, off, s[64:67], 0 sc1\ns_waitcnt vmcnt(0)\nv_readfirstlane_b32 s69, v253\ns_nop 0\ns_cmp_eq_u32 s69, 1\ns_cbranch_scc0 .Lq11_%=\ns_endpgm\n" \
    ".Lq18_%=:\ns_memrealtime s[72:73]\ns_waitcnt lgkmcnt(0)\ns_sub_u32 s72, s72, s76\ns_cmp_lt_i32 s72, 0\ns_cbranch_scc1 .Lq18_%=\n" \
    "buffer_load_dword v13, off, s[64:67], s42 sc1\ns_waitcnt vmcnt(0)\nv_readfirstlane_b32 s74, v13\ns_nop 0\ns_and_b32 s75, s74, 127\ns_lshr_b32 s69, s74, 8\n" \
    "s_sub_u32 s69, s69, s80\ns_bfe_i32 s69, s69, 0x180000\ns_cmp_eq_u32 s75, 0\ns_cselect_b32 s69, 0, s69\ns_add_u32 s80, s80, s69\ns_lshl_b32 s74, s74, 5\n" \
    "s_and_b32 s74, s74, 0x1000\ns_andn2_b32 s81, s81, 0x1000\ns_or_b32 s81, s81, s74\ns_lshl_b32 s74, s74, 2\ns_andn2_b32 s82, s82, 0x4000\ns_or_b32 s82, s82, s74\n" \
    "s_branch .Lq15_%=\n.Lq26_%=:\nv_mov_b32 v252, 0\nv_max3_u32 v252, v252, v72, v73\nv_max3_u32 v252, v252, v74, v75\nv_max3_u32 v252, v252, v76, v77\n" \
    "v_max3_u32 v252, v252, v78, v79\nv_cmp_eq_u32 vcc, -1, v252\ns_nop 0\ns_cbranch_vccz .Lq22_%=\ns_add_u32 s80, s80, s43\ns_xor_b32 s81, s81, 0x1000\n" \
    "s_xor_b32 s90, s90, 0x1000\ns_xor_b32 s82, s82, 0x4000\ns_cmp_eq_u32 s61, 0\ns_cbranch_scc1 .Lq29_%=\ns_lshr_b32 s69, s81, 5\ns_and_b32 s69, s69, 128\n" \
    "s_lshl_b32 s74, s80, 8\ns_add_u32 s74, s74, s69\ns_add_u32 s74, s74, s35\nv_mov_b32 v253, s74\ns_mov_b64 exec, 1\nbuffer_store_dword v253, off, s[64:67], s61 sc1\n" \
    "s_mov_b64 exec, -1\n.Lq29_%=:\ns_add_u32 s68, s68, 1\ns_and_b32 s69, s68, 63\ns_cbranch_scc1 .Lq16_%=\ns_cmp_gt_u32 s68, 0x400000\ns_cbranch_scc1 .Lq1_%=\n" \
    "buffer_load_dword v253, off, s[64:67], 0 sc1\ns_waitcnt vmcnt(0)\nv_readfirstlane_b32 s69, v253\ns_nop 0\ns_cmp_eq_u32 s69, 1\ns_cbranch_scc0 .Lq16_%=\ns_endpgm\n" \
    ".Lq27_%=:\nv_mov_b32 v252, 0\nv_max3_u32 v252, v252, v80, v81\nv_max3_u32 v252, v252, v82, v83\nv_cmp_eq_u32 vcc, -1, v252\ns_nop 0\ns_cbranch_vccz .Lq23_%=\n" \
    "s_add_u32 s80, s80, s43\ns_xor_b32 s81, s81, 0x1000\ns_xor_b32 s90, s90, 0x1000\ns_xor_b32 s82, s82, 0x4000\ns_cmp_eq_u32 s61, 0\ns_cbranch_scc1 .Lq31_%=\n" \
    "s_lshr_b32 s69, s81, 5\ns_and_b32 s69, s69, 128\ns_lshl_b32 s74, s80, 8\ns_add_u32 s74, s74, s69\ns_add_u32 s74, s74, s35\nv_mov_b32 v253, s74\ns_mov_b64 exec, 1\n" \
    "buffer_store_dword v253, off, s[64:67], s61 sc1\ns_mov_b64 exec, -1\n.Lq31_%=:\n.Lq30_%=:\ns_memrealtime s[72:73]\ns_waitcnt vmcnt(0) lgkmcnt(0)\ns_sub_u32 s72, s72, s80\n" \
    "s_cmp_lt_i32 s72, 0\ns_cbranch_scc1 .Lq30_%=\nbuffer_load_dwordx4 v[80:83], v2, s[64:67], s90 offen offset:0 sc1\n" \
    "buffer_load_dwordx4 v[84:87], v2, s[64:67], s90 offen offset:1024 sc1\ns_waitcnt vmcnt(0)\ns_add_u32 s68, s68, 1\ns_and_b32 s69, s68, 63\ns_cbranch_scc1 .Lq27_%=\n" \
    "s_cmp_gt_u32 s68, 0x400000\ns_cbranch_scc1 .Lq1_%=\nbuffer_load_dword v253, off, s[64:67], 0 sc1\ns_waitcnt vmcnt(0)\nv_readfirstlane_b32 s69, v253\ns_nop 0\n" \
    "s_cmp_eq_u32 s69, 1\ns_cbranch_scc0 .Lq27_%=\ns_endpgm\n.Lq28_%=:\nv_mov_b32 v252, 0\nv_max3_u32 v252, v252, v84, v85\nv_max3_u32 v252, v252, v86, v87\n" \
    "v_cmp_eq_u32 vcc, -1, v252\ns_nop 0\ns_cbranch_vccz .Lq24_%=\ns_add_u32 s80, s80, s43\ns_xor_b32 s81, s81, 0x1000\ns_xor_b32 s90, s90, 0x1000\ns_xor_b32 s82, s82, 0x4000\n" \
    "s_cmp_eq_u32 s61, 0\ns_cbranch_scc1 .Lq33_%=\ns_lshr_b32 s69, s81, 5\ns_and_b32 s69, s69, 128\ns_lshl_b32 s74, s80, 8\ns_add_u32 s74, s74, s69\ns_add_u32 s74, s74, s35\n" \
    "v_mov_b32 v253, s74\ns_mov_b64 exec, 1\nbuffer_store_dword v253, off, s[64:67], s61 sc1\ns_mov_b64 exec, -1\n.Lq33_%=:\n.Lq32_%=:\ns_memrealtime s[72:73]\n" \
    "s_waitcnt vmcnt(0) lgkmcnt(0)\ns_sub_u32 s72, s72, s80\ns_cmp_lt_i32 s72, 0\ns_cbranch_scc1 .Lq32_%=\n" \
    "buffer_load_dwordx4 v[84:87], v2, s[64:67], s90 offen offset:1024 sc1\ns_waitcnt vmcnt(0)\ns_add_u32 s68, s68, 1\ns_and_b32 s69, s68, 63\ns_cbranch_scc1 .Lq28_%=\n" \
    "s_cmp_gt_u32 s68, 0x400000\ns_cbranch_scc1 .Lq1_%=\nbuffer_load_dword v253, off, s[64:67], 0 sc1\ns_waitcnt vmcnt(0)\nv_readfirstlane_b32 s69, v253\ns_nop 0\n" \
    "s_cmp_eq_u32 s69, 1\ns_cbranch_scc0 .Lq28_%=\ns_endpgm\n.Lq37_%=:\ns_memrealtime s[72:73]\ns_waitcnt lgkmcnt(0)\ns_sub_u32 s72, s72, s76\ns_cmp_lt_i32 s72, 0\n" \
    "s_cbranch_scc1 .Lq37_%=\nbuffer_load_dword v13, off, s[64:67], s42 sc1\ns_waitcnt vmcnt(0)\nv_readfirstlane_b32 s74, v13\ns_nop 0\ns_and_b32 s75, s74, 127\n" \
    "s_lshr_b32 s69, s74, 8\ns_sub_u32 s69, s69, s80\ns_bfe_i32 s69, s69, 0x180000\ns_cmp_eq_u32 s75, 0\ns_cselect_b32 s69, 0, s69\ns_add_u32 s80, s80, s69\n" \
    "s_lshl_b32 s74, s74, 5\ns_and_b32 s74, s74, 0x1000\ns_andn2_b32 s81, s81, 0x1000\ns_or_b32 s81, s81, s74\ns_lshl_b32 s74, s74, 2\ns_andn2_b32 s82, s82, 0x4000\n" \
    "s_or_b32 s82, s82, s74\ns_branch .Lq34_%=\n.Lq45_%=:\nv_mov_b32 v252, 0\nv_max3_u32 v252, v252, v72, v73\nv_max3_u32 v252, v252, v74, v75\nv_max3_u32 v252, v252, v76, v77\n" \
    "v_max3_u32 v252, v252, v78, v79\nv_cmp_eq_u32 vcc, -1, v252\ns_nop 0\ns_cbranch_vccz .Lq41_%=\ns_add_u32 s80, s80, s43\ns_xor_b32 s81, s81, 0x1000\n" \
    "s_xor_b32 s90, s90, 0x1000\ns_xor_b32 s82, s82, 0x4000\ns_cmp_eq_u32 s61, 0\ns_cbranch_scc1 .Lq48_%=\ns_lshr_b32 s69, s81, 5\ns_and_b32 s69, s69, 128\n" \
    "s_lshl_b32 s74, s80, 8\ns_add_u32 s74, s74, s69\ns_add_u32 s74, s74, s35\nv_mov_b32 v253, s74\ns_mov_b64 exec, 1\nbuffer_store_dword v253, off, s[64:67], s61 sc1\n" \
    "s_mov_b64 exec, -1\n.Lq48_%=:\ns_add_u32 s68, s68, 1\ns_and_b32 s69, s68, 63\ns_cbranch_scc1 .Lq35_%=\ns_cmp_gt_u32 s68, 0x400000\ns_cbranch_scc1 .Lq1_%=\n" \
    "buffer_load_dword v253, off, s[64:67], 0 sc1\ns_waitcnt vmcnt(0)\nv_readfirstlane_b32 s69, v253\ns_nop 0\ns_cmp_eq_u32 s69, 1\ns_cbranch_scc0 .Lq35_%=\ns_endpgm\n" \
    ".Lq46_%=:\nv_mov_b32 v252, 0\nv_max3_u32 v252, v252, v80, v81\nv_max3_u32 v252, v252, v82, v83\nv_cmp_eq_u32 vcc, -1, v252\ns_nop 0\ns_cbranch_vccz .Lq42_%=\n" \
    "s_add_u32 s80, s80, s43\ns_xor_b32 s81, s81, 0x1000\ns_xor_b32 s90, s90, 0x1000\ns_xor_b32 s82, s82, 0x4000\ns_cmp_eq_u32 s61, 0\ns_cbranch_scc1 .Lq50_%=\n" \
    "s_lshr_b32 s69, s81, 5\ns_and_b32 s69, s69, 128\ns_lshl_b32 s74, s80, 8\ns_add_u32 s74, s74, s69\ns_add_u32 s74, s74, s35\nv_mov_b32 v253, s74\ns_mov_b64 exec, 1\n" \
    "buffer_store_dword v253, off, s[64:67], s61 sc1\ns_mov_b64 exec, -1\n.Lq50_%=:\n.Lq49_%=:\ns_memrealtime s[72:73]\ns_waitcnt vmcnt(0) lgkmcnt(0)\ns_sub_u32 s72, s72, s80\n" \
    "s_cmp_lt_i32 s72, 0\ns_cbranch_scc1 .Lq49_%=\nbuffer_load_dwordx4 v[80:83], v2, s[64:67], s90 offen offset:0 sc1\n" \
    "buffer_load_dwordx4 v[84:87], v2, s[64:67], s90 offen offset:1024 sc1\ns_waitcnt vmcnt(0)\ns_add_u32 s68, s68, 1\ns_and_b32 s69, s68, 63\ns_cbranch_scc1 .Lq46_%=\n" \
    "s_cmp_gt_u32 s68, 0x400000\ns_cbranch_scc1 .Lq1_%=\nbuffer_load_dword v253, off, s[64:67], 0 sc1\ns_waitcnt vmcnt(0)\nv_readfirstlane_b32 s69, v253\ns_nop 0\n" \
    "s_cmp_eq_u32 s69, 1\ns_cbranch_scc0 .Lq46_%=\ns_endpgm\n.Lq47_%=:\nv_mov_b32 v252, 0\nv_max3_u32 v252, v252, v84, v85\nv_max3_u32 v252, v252, v86, v87\n" \
    "v_cmp_eq_u32 vcc, -1, v252\ns_nop 0\ns_cbranch_vccz .Lq43_%=\ns_add_u32 s80, s80, s43\ns_xor_b32 s81, s81, 0x1000\ns_xor_b32 s90, s90, 0x1000\ns_xor_b32 s82, s82, 0x4000\n" \
    "s_cmp_eq_u32 s61, 0\ns_cbranch_scc1 .Lq52_%=\ns_lshr_b32 s69, s81, 5\ns_and_b32 s69, s69, 128\ns_lshl_b32 s74, s80, 8\ns_add_u32 s74, s74, s69\ns_add_u32 s74, s74, s35\n" \
    "v_mov_b32 v253, s74\ns_mov_b64 exec, 1\nbuffer_store_dword v253, off, s[64:67], s61 sc1\ns_mov_b64 exec, -1\n.Lq52_%=:\n.Lq51_%=:\ns_memrealtime s[72:73]\n" \
    "s_waitcnt vmcnt(0) lgkmcnt(0)\ns_sub_u32 s72, s72, s80\ns_cmp_lt_i32 s72, 0\ns_cbranch_scc1 .Lq51_%=\n" \
    "buffer_load_dwordx4 v[84:87], v2, s[64:67], s90 offen offset:1024 sc1\ns_waitcnt vmcnt(0)\ns_add_u32 s68, s68, 1\ns_and_b32 s69, s68, 63\ns_cbranch_scc1 .Lq47_%=\n" \
    "s_cmp_gt_u32 s68, 0x400000\ns_cbranch_scc1 .Lq1_%=\nbuffer_load_dword v253, off, s[64:67], 0 sc1\ns_waitcnt vmcnt(0)\nv_readfirstlane_b32 s69, v253\ns_nop 0\n" \
    "s_cmp_eq_u32 s69, 1\ns_cbranch_scc0 .Lq47_%=\ns_endpgm\n.Lq56_%=:\ns_memrealtime s[72:73]\ns_waitcnt lgkmcnt(0)\ns_sub_u32 s72, s72, s76\ns_cmp_lt_i32 s72, 0\n" \
    "s_cbranch_scc1 .Lq56_%=\nbuffer_load_dword v13, off, s[64:67], s42 sc1\ns_waitcnt vmcnt(0)\nv_readfirstlane_b32 s74, v13\ns_nop 0\ns_and_b32 s75, s74, 127\n" \
    "s_lshr_b32 s69, s74, 8\ns_sub_u32 s69, s69, s80\ns_bfe_i32 s69, s69, 0x180000\ns_cmp_eq_u32 s75, 0\ns_cselect_b32 s69, 0, s69\ns_add_u32 s80, s80, s69\n" \
    "s_lshl_b32 s74, s74, 5\ns_and_b32 s74, s74, 0x1000\ns_andn2_b32 s81, s81, 0x1000\ns_or_b32 s81, s81, s74\ns_lshl_b32 s74, s74, 2\ns_andn2_b32 s82, s82, 0x4000\n" \
    "s_or_b32 s82, s82, s74\ns_branch .Lq53_%=\n.Lq64_%=:\nv_mov_b32 v252, 0\nv_max3_u32 v252, v252, v72, v73\nv_max3_u32 v252, v252, v74, v75\nv_max3_u32 v252, v252, v76, v77\n" \
    "v_max3_u32 v252, v252, v78, v79\nv_cmp_eq_u32 vcc, -1, v252\ns_nop 0\ns_cbranch_vccz .Lq60_%=\ns_add_u32 s80, s80, s43\ns_xor_b32 s81, s81, 0x1000\n" \
    "s_xor_b32 s90, s90, 0x1000\ns_xor_b32 s82, s82, 0x4000\ns_cmp_eq_u32 s61, 0\ns_cbranch_scc1 .Lq67_%=\ns_lshr_b32 s69, s81, 5\ns_and_b32 s69, s69, 128\n" \
    "s_lshl_b32 s74, s80, 8\ns_add_u32 s74, s74, s69\ns_add_u32 s74, s74, s35\nv_mov_b32 v253, s74\ns_mov_b64 exec, 1\nbuffer_store_dword v253, off, s[64:67], s61 sc1\n" \
    "s_mov_b64 exec, -1\n.Lq67_%=:\ns_add_u32 s68, s68, 1\ns_and_b32 s69, s68, 63\ns_cbranch_scc1 .Lq54_%=\ns_cmp_gt_u32 s68, 0x400000\ns_cbranch_scc1 .Lq1_%=\n" \
    "buffer_load_dword v253, off, s[64:67], 0 sc1\ns_waitcnt vmcnt(0)\nv_readfirstlane_b32 s69, v253\ns_nop 0\ns_cmp_eq_u32 s69, 1\ns_cbranch_scc0 .Lq54_%=\ns_endpgm\n" \
    ".Lq65_%=:\nv_mov_b32 v252, 0\nv_max3_u32 v252, v252, v80, v81\nv_max3_u32 v252, v252, v82, v83\nv_cmp_eq_u32 vcc, -1, v252\ns_nop 0\ns_cbranch_vccz .Lq61_%=\n" \
    "s_add_u32 s80, s80, s43\ns_xor_b32 s81, s81, 0x1000\ns_xor_b32 s90, s90, 0x1000\ns_xor_b32 s82, s82, 0x4000\ns_cmp_eq_u32 s61, 0\ns_cbranch_scc1 .Lq69_%=\n" \
    "s_lshr_b32 s69, s81, 5\ns_and_b32 s69, s69, 128\ns_lshl_b32 s74, s80, 8\ns_add_u32 s74, s74, s69\ns_add_u32 s74, s74, s35\nv_mov_b32 v253, s74\ns_mov_b64 exec, 1\n" \
    "buffer_store_dword v253, off, s[64:67], s61 sc1\ns_mov_b64 exec, -1\n.Lq69_%=:\n.Lq68_%=:\ns_memrealtime s[72:73]\ns_waitcnt vmcnt(0) lgkmcnt(0)\ns_sub_u32 s72, s72, s80\n" \
    "s_cmp_lt_i32 s72, 0\ns_cbranch_scc1 .Lq68_%=\nbuffer_load_dwordx4 v[80:83], v2, s[64:67], s90 offen offset:0 sc1\n" \
    "buffer_load_dwordx4 v[84:87], v2, s[64:67], s90 offen offset:1024 sc1\ns_waitcnt vmcnt(0)\ns_add_u32 s68, s68, 1\ns_and_b32 s69, s68, 63\ns_cbranch_scc1 .Lq65_%=\n" \
    "s_cmp_gt_u32 s68, 0x400000\ns_cbranch_scc1 .Lq1_%=\nbuffer_load_dword v253, off, s[64:67], 0 sc1\ns_waitcnt vmcnt(0)\nv_readfirstlane_b32 s69, v253\ns_nop 0\n" \
    "s_cmp_eq_u32 s69, 1\ns_cbranch_scc0 .Lq65_%=\ns_endpgm\n.Lq66_%=:\nv_mov_b32 v252, 0\nv_max3_u32 v252, v252, v84, v85\nv_max3_u32 v252, v252, v86, v87\n" \
    "v_cmp_eq_u32 vcc, -1, v252\ns_nop 0\ns_cbranch_vccz .Lq62_%=\ns_add_u32 s80, s80, s43\ns_xor_b32 s81, s81, 0x1000\ns_xor_b32 s90, s90, 0x1000\ns_xor_b32 s82, s82, 0x4000\n" \
    "s_cmp_eq_u32 s61, 0\ns_cbranch_scc1 .Lq71_%=\ns_lshr_b32 s69, s81, 5\ns_and_b32 s69, s69, 128\ns_lshl_b32 s74, s80, 8\ns_add_u32 s74, s74, s69\ns_add_u32 s74, s74, s35\n" \
    "v_mov_b32 v253, s74\ns_mov_b64 exec, 1\nbuffer_store_dword v253, off, s[64:67], s61 sc1\ns_mov_b64 exec, -1\n.Lq71_%=:\n.Lq70_%=:\ns_memrealtime s[72:73]\n" \
    "s_waitcnt vmcnt(0) lgkmcnt(0)\ns_sub_u32 s72, s72, s80\ns_cmp_lt_i32 s72, 0\ns_cbranch_scc1 .Lq70_%=\n" \
    "buffer_load_dwordx4 v[84:87], v2, s[64:67], s90 offen offset:1024 sc1\ns_waitcnt vmcnt(0)\ns_add_u32 s68, s68, 1\ns_and_b32 s69, s68, 63\ns_cbranch_scc1 .Lq66_%=\n" \
    "s_cmp_gt_u32 s68, 0x400000\ns_cbranch_scc1 .Lq1_%=\nbuffer_load_dword v253, off, s[64:67], 0 sc1\ns_waitcnt vmcnt(0)\nv_readfirstlane_b32 s69, v253\ns_nop 0\n" \
    "s_cmp_eq_u32 s69, 1\ns_cbranch_scc0 .Lq66_%=\ns_endpgm\n.Lq75_%=:\ns_memrealtime s[72:73]\ns_waitcnt lgkmcnt(0)\ns_sub_u32 s72, s72, s76\ns_cmp_lt_i32 s72, 0\n" \
    "s_cbranch_scc1 .Lq75_%=\nbuffer_load_dword v13, off, s[64:67], s42 sc1\ns_waitcnt vmcnt(0)\nv_readfirstlane_b32 s74, v13\ns_nop 0\ns_and_b32 s75, s74, 127\n" \
    "s_lshr_b32 s69, s74, 8\ns_sub_u32 s69, s69, s80\ns_bfe_i32 s69, s69, 0x180000\ns_cmp_eq_u32 s75, 0\ns_cselect_b32 s69, 0, s69\ns_add_u32 s80, s80, s69\n" \
    "s_lshl_b32 s74, s74, 5\ns_and_b32 s74, s74, 0x1000\ns_andn2_b32 s81, s81, 0x1000\ns_or_b32 s81, s81, s74\ns_lshl_b32 s74, s74, 2\ns_andn2_b32 s82, s82, 0x4000\n" \
    "s_or_b32 s82, s82, s74\ns_branch .Lq72_%=\n.Lq83_%=:\nv_mov_b32 v252, 0\nv_max3_u32 v252, v252, v72, v73\nv_max3_u32 v252, v252, v74, v75\nv_max3_u32 v252, v252, v76, v77\n" \
    "v_max3_u32 v252, v252, v78, v79\nv_cmp_eq_u32 vcc, -1, v252\ns_nop 0\ns_cbranch_vccz .Lq79_%=\ns_add_u32 s80, s80, s43\ns_xor_b32 s81, s81, 0x1000\n" \
    "s_xor_b32 s90, s90, 0x1000\ns_xor_b32 s82, s82, 0x4000\ns_cmp_eq_u32 s61, 0\ns_cbranch_scc1 .Lq86_%=\ns_lshr_b32 s69, s81, 5\ns_and_b32 s69, s69, 128\n" \
    "s_lshl_b32 s74, s80, 8\ns_add_u32 s74, s74, s69\ns_add_u32 s74, s74, s35\nv_mov_b32 v253, s74\ns_mov_b64 exec, 1\nbuffer_store_dword v253, off, s[64:67], s61 sc1\n" \
    "s_mov_b64 exec, -1\n.Lq86_%=:\ns_add_u32 s68, s68, 1\ns_and_b32 s69, s68, 63\ns_cbranch_scc1 .Lq73_%=\ns_cmp_gt_u32 s68, 0x400000\ns_cbranch_scc1 .Lq1_%=\n" \
    "buffer_load_dword v253, off, s[64:67], 0 sc1\ns_waitcnt vmcnt(0)\nv_readfirstlane_b32 s69, v253\ns_nop 0\ns_cmp_eq_u32 s69, 1\ns_cbranch_scc0 .Lq73_%=\ns_endpgm\n" \
    ".Lq84_%=:\nv_mov_b32 v252, 0\nv_max3_u32 v252, v252, v80, v81\nv_max3_u32 v252, v252, v82, v83\nv_cmp_eq_u32 vcc, -1, v252\ns_nop 0\ns_cbranch_vccz .Lq80_%=\n" \
    "s_add_u32 s80, s80, s43\ns_xor_b32 s81, s81, 0x1000\ns_xor_b32 s90, s90, 0x1000\ns_xor_b32 s82, s82, 0x4000\ns_cmp_eq_u32 s61, 0\ns_cbranch_scc1 .Lq88_%=\n" \
    "s_lshr_b32 s69, s81, 5\ns_and_b32 s69, s69, 128\ns_lshl_b32 s74, s80, 8\ns_add_u32 s74, s74, s69\ns_add_u32 s74, s74, s35\nv_mov_b32 v253, s74\ns_mov_b64 exec, 1\n" \
    "buffer_store_dword v253, off, s[64:67], s61 sc1\ns_mov_b64 exec, -1\n.Lq88_%=:\n.Lq87_%=:\ns_memrealtime s[72:73]\ns_waitcnt vmcnt(0) lgkmcnt(0)\ns_sub_u32 s72, s72, s80\n" \
    "s_cmp_lt_i32 s72, 0\ns_cbranch_scc1 .Lq87_%=\nbuffer_load_dwordx4 v[80:83], v2, s[64:67], s90 offen offset:0 sc1\n" \
    "buffer_load_dwordx4 v[84:87], v2, s[64:67], s90 offen offset:1024 sc1\ns_waitcnt vmcnt(0)\ns_add_u32 s68, s68, 1\ns_and_b32 s69, s68, 63\ns_cbranch_scc1 .Lq84_%=\n" \
    "s_cmp_gt_u32 s68, 0x400000\ns_cbranch_scc1 .Lq1_%=\nbuffer_load_dword v253, off, s[64:67], 0 sc1\ns_waitcnt vmcnt(0)\nv_readfirstlane_b32 s69, v253\ns_nop 0\n" \
    "s_cmp_eq_u32 s69, 1\ns_cbranch_scc0 .Lq84_%=\ns_endpgm\n.Lq85_%=:\nv_mov_b32 v252, 0\nv_max3_u32 v252, v252, v84, v85\nv_max3_u32 v252, v252, v86, v87\n" \
    "v_cmp_eq_u32 vcc, -1, v252\ns_nop 0\ns_cbranch_vccz .Lq81_%=\ns_add_u32 s80, s80, s43\ns_xor_b32 s81, s81, 0x1000\ns_xor_b32 s90, s90, 0x1000\ns_xor_b32 s82, s82, 0x4000\n" \
    "s_cmp_eq_u32 s61, 0\ns_cbranch_scc1 .Lq90_%=\ns_lshr_b32 s69, s81, 5\ns_and_b32 s69, s69, 128\ns_lshl_b32 s74, s80, 8\ns_add_u32 s74, s74, s69\ns_add_u32 s74, s74, s35\n" \
    "v_mov_b32 v253, s74\ns_mov_b64 exec, 1\nbuffer_store_dword v253, off, s[64:67], s61 sc1\ns_mov_b64 exec, -1\n.Lq90_%=:\n.Lq89_%=:\ns_memrealtime s[72:73]\n" \
    "s_waitcnt vmcnt(0) lgkmcnt(0)\ns_sub_u32 s72, s72, s80\ns_cmp_lt_i32 s72, 0\ns_cbranch_scc1 .Lq89_%=\n" \
    "buffer_load_dwordx4 v[84:87], v2, s[64:67], s90 offen offset:1024 sc1\ns_waitcnt vmcnt(0)\ns_add_u32 s68, s68, 1\ns_and_b32 s69, s68, 63\ns_cbranch_scc1 .Lq85_%=\n" \
    "s_cmp_gt_u32 s68, 0x400000\ns_cbranch_scc1 .Lq1_%=\nbuffer_load_dword v253, off, s[64:67], 0 sc1\ns_waitcnt vmcnt(0)\nv_readfirstlane_b32 s69, v253\ns_nop 0\n" \
    "s_cmp_eq_u32 s69, 1\ns_cbranch_scc0 .Lq85_%=\ns_endpgm\n.Lq14_%=:\ns_mov_b32 s68, 0\n.Lq91_%=:\nds_read_b128 v[36:39], v6 offset:0\nds_read_b128 v[44:47], v8 offset:0\n" \
    "ds_read_b128 v[64:67], v6 offset:512\nds_read_b128 v[84:87], v8 offset:512\nds_read_b128 v[68:71], v6 offset:1024\nds_read_b128 v[88:91], v8 offset:1024\n" \
    "ds_read_b128 v[40:43], v6 offset:1536\nds_read_b128 v[28:31], v8 offset:1536\nv_mov_b32 v252, 0\ns_waitcnt lgkmcnt(0)\nv_pk_max_u16 v252, v252, v36\n" \
    "v_pk_max_u16 v252, v252, v37\nv_pk_max_u16 v252, v252, v38\nv_pk_max_u16 v252, v252, v39\nv_pk_max_u16 v252, v252, v64\nv_pk_max_u16 v252, v252, v65\n" \
    "v_pk_max_u16 v252, v252, v66\nv_pk_max_u16 v252, v252, v67\nv_pk_max_u16 v252, v252, v68\nv_pk_max_u16 v252, v252, v69\nv_pk_max_u16 v252, v252, v70\n" \
    "v_pk_max_u16 v252, v252, v71\nv_pk_max_u16 v252, v252, v40\nv_pk_max_u16 v252, v252, v41\nv_pk_max_u16 v252, v252, v42\nv_pk_max_u16 v252, v252, v43\n" \
    "v_pk_max_u16 v252, v252, v44\nv_pk_max_u16 v252, v252, v45\nv_pk_max_u16 v252, v252, v46\nv_pk_max_u16 v252, v252, v47\nv_pk_max_u16 v252, v252, v84\n" \
    "v_pk_max_u16 v252, v252, v85\nv_pk_max_u16 v252, v252, v86\nv_pk_max_u16 v252, v252, v87\nv_pk_max_u16 v252, v252, v88\nv_pk_max_u16 v252, v252, v89\n" \
    "v_pk_max_u16 v252, v252, v90\nv_pk_max_u16 v252, v252, v91\nv_pk_max_u16 v252, v252, v28\nv_pk_max_u16 v252, v252, v29\nv_pk_max_u16 v252, v252, v30\n" \
    "v_pk_max_u16 v252, v252, v31\nv_pk_max_u16 v252, v252, v252 op_sel:[0,1] op_sel_hi:[1,0]\nv_cmp_eq_u32 vcc, -1, v252\ns_nop 0\ns_cbranch_vccnz .Lq92_%=\n" \
    "v_mfma_f32_16x16x32_bf16 v[16:19], v[48:51], v[36:39], 0\nv_mfma_f32_16x16x32_bf16 v[20:23], v[52:55], v[36:39], 0\n" \
    "v_mfma_f32_16x16x32_bf16 v[16:19], v[72:75], v[64:67], v[16:19]\nv_mfma_f32_16x16x32_bf16 v[20:23], v[76:79], v[64:67], v[20:23]\n" \
    "v_mfma_f32_16x16x32_bf16 v[16:19], v[80:83], v[68:71], v[16:19]\nv_mfma_f32_16x16x32_bf16 v[20:23], v[56:59], v[68:71], v[20:23]\n" \
    "v_mfma_f32_16x16x32_bf16 v[16:19], v[60:63], v[40:43], v[16:19]\nv_mfma_f32_16x16x32_bf16 v[20:23], v[32:35], v[40:43], v[20:23]\n" \
    "v_mfma_f32_16x16x32_bf16 v[24:27], v[44:47], v[36:39], 0\nv_mfma_f32_16x16x32_bf16 v[24:27], v[84:87], v[64:67], v[24:27]\n" \
    "v_mfma_f32_16x16x32_bf16 v[24:27], v[88:91], v[68:71], v[24:27]\nv_mfma_f32_16x16x32_bf16 v[24:27], v[28:31], v[40:43], v[24:27]\ns_nop 3\n" \
    "v_mov_b32_dpp v16, v20 quad_perm:[0,1,2,3] row_mask:0xf bank_mask:0xc\nv_mov_b32_dpp v17, v21 quad_perm:[0,1,2,3] row_mask:0xf bank_mask:0xc\n" \
    "v_mov_b32_dpp v18, v22 quad_perm:[0,1,2,3] row_mask:0xf bank_mask:0xc\nv_mov_b32_dpp v19, v23 quad_perm:[0,1,2,3] row_mask:0xf bank_mask:0xc\n" \
    "v_max3_f32 v254, v16, v17, v18\nv_max_f32 v254, v254, v19\ns_nop 1\nv_max_f32_dpp v252, v254, v254 row_ror:8 row_mask:0xf bank_mask:0xf\n" \
    "v_max_f32_dpp v253, v254, v254 row_ror:8 row_mask:0xf bank_mask:0xf\ns_nop 1\nv_permlane32_swap_b32 v252, v253\nv_max_f32 v252, v252, v253\n" \
    "v_max3_f32 v254, v252, v24, v25\nv_max3_f32 v252, v254, v26, v27\nv_max3_f32 v253, v254, v26, v27\ns_nop 1\nv_permlane16_swap_b32 v252, v253\nv_max_f32 v252, v252, v253\n" \
    "v_pk_add_f32 v[16:17], v[16:17], v[252:253] op_sel_hi:[1,0] neg_lo:[0,1] neg_hi:[0,1]\n" \
    "v_pk_add_f32 v[18:19], v[18:19], v[252:253] op_sel_hi:[1,0] neg_lo:[0,1] neg_hi:[0,1]\n" \
    "v_pk_add_f32 v[24:25], v[24:25], v[252:253] op_sel_hi:[1,0] neg_lo:[0,1] neg_hi:[0,1]\n" \
    "v_pk_add_f32 v[26:27], v[26:27], v[252:253] op_sel_hi:[1,0] neg_lo:[0,1] neg_hi:[0,1]\nv_exp_f32 v16, v16\nv_exp_f32 v17, v17\nv_exp_f32 v18, v18\nv_exp_f32 v19, v19\n" \
    "v_exp_f32 v24, v24\nv_pk_add_f32 v[254:255], v[16:17], v[18:19]\nv_exp_f32 v25, v25\nv_add_f32 v254, v254, v255\nv_exp_f32 v26, v26\nv_exp_f32 v27, v27\n" \
    "v_add_f32_dpp v252, v254, v254 row_ror:8 row_mask:0xf bank_mask:0xf\nv_add_f32_dpp v253, v254, v254 row_ror:8 row_mask:0xf bank_mask:0xf\n" \
    "v_pk_add_f32 v[254:255], v[24:25], v[26:27]\ns_nop 0\nv_permlane32_swap_b32 v252, v253\nv_pk_add_f32 v[254:255], v[254:255], v[252:253]\n" \
    "v_pk_add_f32 v[252:253], v[254:255], v[254:255] op_sel:[0,1] op_sel_hi:[1,0]\ns_nop 1\nv_permlane16_swap_b32 v252, v253\nv_add_f32 v252, v252, v253\nv_rcp_f32 v252, v252\n" \
    "s_nop 0\nv_pk_mul_f32 v[16:17], v[16:17], v[252:253] op_sel_hi:[1,0]\nv_pk_mul_f32 v[24:25], v[24:25], v[252:253] op_sel_hi:[1,0]\nv_cvt_pk_bf16_f32 v20, v16, v17\n" \
    "v_cvt_pk_bf16_f32 v22, v24, v25\nv_pk_mul_f32 v[18:19], v[18:19], v[252:253] op_sel_hi:[1,0]\nv_pk_mul_f32 v[26:27], v[26:27], v[252:253] op_sel_hi:[1,0]\n" \
    "v_cvt_pk_bf16_f32 v21, v18, v19\nv_cvt_pk_bf16_f32 v23, v26, v27\nv_permlane32_swap_b32 v20, v22\ns_nop 0\nv_permlane32_swap_b32 v21, v23\n" \
    "buffer_store_dwordx4 v[20:23], v7, s[64:67], s82 offen sc1\nbuffer_store_dwordx4 v[20:23], v7, s[64:67], s74 offen sc1\ns_branch .Lq3_%=\n.Lq92_%=:\ns_add_u32 s68, s68, 1\n" \
    "s_and_b32 s69, s68, 63\ns_cbranch_scc1 .Lq91_%=\ns_cmp_gt_u32 s68, 0x400000\ns_cbranch_scc1 .Lq1_%=\nbuffer_load_dword v253, off, s[64:67], 0 sc1\ns_waitcnt vmcnt(0)\n" \
    "v_readfirstlane_b32 s69, v253\ns_nop 0\ns_cmp_eq_u32 s69, 1\ns_cbranch_scc0 .Lq91_%=\ns_endpgm\n.Lq1_%=:\nv_mov_b32 v253, 1\nbuffer_store_dword v253, off, s[64:67], 0 sc1\n" \
    "s_endpgm\n"
#define ASM_V \
    "s_add_u32 m0, s41, 0\ns_xor_b32 s85, s62, 0\ns_add_u32 s85, s85, s40\nbuffer_load_dwordx4 v11, s[64:67], s85 offen sc1 lds\n" \
    "buffer_load_dwordx4 v12, s[64:67], s85 offen offset:1024 sc1 lds\ns_add_u32 m0, s41, 8192\ns_xor_b32 s85, s62, 256\ns_add_u32 s85, s85, s40\n" \
    "buffer_load_dwordx4 v11, s[64:67], s85 offen sc1 lds\nbuffer_load_dwordx4 v12, s[64:67], s85 offen offset:1024 sc1 lds\ns_add_u32 m0, s41, 16384\ns_xor_b32 s85, s62, 512\n" \
    "s_add_u32 s85, s85, s40\nbuffer_load_dwordx4 v11, s[64:67], s85 offen sc1 lds\nbuffer_load_dwordx4 v12, s[64:67], s85 offen offset:1024 sc1 lds\ns_add_u32 m0, s41, 40960\n" \
    "s_xor_b32 s85, s62, 768\ns_add_u32 s85, s85, s40\nbuffer_load_dwordx4 v11, s[64:67], s85 offen sc1 lds\nbuffer_load_dwordx4 v12, s[64:67], s85 offen offset:1024 sc1 lds\n" \
    "s_memrealtime s[72:73]\ns_waitcnt lgkmcnt(0)\ns_mov_b32 s80, s72\ns_waitcnt vmcnt(0)\ns_barrier\nds_read_b128 v[56:59], v3 offset:0\nds_read_b128 v[60:63], v9 offset:0\n" \
    "ds_read_b128 v[64:67], v9 offset:4096\nds_read_b128 v[68:71], v3 offset:1024\nds_read_b128 v[72:75], v10 offset:0\nds_read_b128 v[76:79], v10 offset:4096\n" \
    "s_waitcnt lgkmcnt(3)\nv_mfma_f32_16x16x32_bf16 v[16:19], a[0:3], v[60:63], 0\ns_add_u32 m0, s41, 24576\ns_xor_b32 s85, s62, 1024\n" \
    "v_mfma_f32_16x16x32_bf16 v[24:27], v[56:59], v[60:63], 0\nds_read_b128 v[60:63], v3 offset:2048\ns_add_u32 s85, s85, s40\n" \
    "v_mfma_f32_16x16x32_bf16 v[20:23], a[0:3], v[64:67], 0\nds_read_b128 v[80:83], v9 offset:1024\nbuffer_load_dwordx4 v11, s[64:67], s85 offen sc1 lds\n" \
    "v_mfma_f32_16x16x32_bf16 v[28:31], v[56:59], v[64:67], 0\nds_read_b128 v[56:59], v9 offset:5120\ns_waitcnt lgkmcnt(3)\n" \
    "v_mfma_f32_16x16x32_bf16 v[16:19], a[4:7], v[72:75], v[16:19]\nv_mfma_f32_16x16x32_bf16 v[24:27], v[68:71], v[72:75], v[24:27]\nds_read_b128 v[64:67], v3 offset:3072\n" \
    "buffer_load_dwordx4 v12, s[64:67], s85 offen offset:1024 sc1 lds\nv_mfma_f32_16x16x32_bf16 v[20:23], a[4:7], v[76:79], v[20:23]\nds_read_b128 v[72:75], v10 offset:1024\n" \
    "s_add_u32 m0, s41, 32768\nv_mfma_f32_16x16x32_bf16 v[28:31], v[68:71], v[76:79], v[28:31]\nds_read_b128 v[68:71], v10 offset:5120\ns_xor_b32 s85, s62, 1280\n" \
    "s_waitcnt lgkmcnt(3)\nv_mfma_f32_16x16x32_bf16 v[16:19], a[8:11], v[80:83], v[16:19]\ns_add_u32 s85, s85, s40\nbuffer_load_dwordx4 v11, s[64:67], s85 offen sc1 lds\n" \
    "v_mfma_f32_16x16x32_bf16 v[24:27], v[60:63], v[80:83], v[24:27]\nds_read_b128 v[76:79], v3 offset:4096\nv_mfma_f32_16x16x32_bf16 v[20:23], a[8:11], v[56:59], v[20:23]\n" \
    "ds_read_b128 v[80:83], v9 offset:8192\nv_mfma_f32_16x16x32_bf16 v[28:31], v[60:63], v[56:59], v[28:31]\nds_read_b128 v[56:59], v9 offset:12288\n" \
    "buffer_load_dwordx4 v12, s[64:67], s85 offen offset:1024 sc1 lds\ns_waitcnt lgkmcnt(3)\nv_mfma_f32_16x16x32_bf16 v[16:19], a[12:15], v[72:75], v[16:19]\n" \
    "s_add_u32 s84, s40, 0x20000\ns_cmp_lt_u32 s49, 62\ns_cselect_b32 s84, s84, s40\nv_mfma_f32_16x16x32_bf16 v[24:27], v[64:67], v[72:75], v[24:27]\n" \
    "ds_read_b128 v[60:63], v3 offset:5120\nv_mov_b64 v[252:253], -1\nv_mov_b64 v[254:255], -1\nv_mfma_f32_16x16x32_bf16 v[20:23], a[12:15], v[68:71], v[20:23]\n" \
    "ds_read_b128 v[72:75], v10 offset:8192\ns_and_b32 s69, s49, 15\nv_mfma_f32_16x16x32_bf16 v[28:31], v[64:67], v[68:71], v[28:31]\nds_read_b128 v[64:67], v10 offset:12288\n" \
    "s_lshl_b32 s69, s69, 19\ns_waitcnt lgkmcnt(3)\nv_mfma_f32_16x16x32_bf16 v[16:19], a[16:19], v[80:83], v[16:19]\ns_add_u32 s82, s38, s69\ns_lshr_b32 s69, s82, 8\n" \
    "v_mfma_f32_16x16x32_bf16 v[24:27], v[76:79], v[80:83], v[24:27]\nds_read_b128 v[68:71], v3 offset:6144\ns_and_b32 s69, s69, 0x2000\n" \
    "v_mfma_f32_16x16x32_bf16 v[20:23], a[16:19], v[56:59], v[20:23]\nds_read_b128 v[80:83], v9 offset:9216\ns_xor_b32 s82, s82, s69\n" \
    "v_mfma_f32_16x16x32_bf16 v[28:31], v[76:79], v[56:59], v[28:31]\nds_read_b128 v[56:59], v9 offset:13312\ns_and_b32 s69, s35, 15\ns_waitcnt lgkmcnt(3)\n" \
    "v_mfma_f32_16x16x32_bf16 v[16:19], a[20:23], v[72:75], v[16:19]\ns_lshl_b32 s69, s69, 19\ns_add_u32 s0, s38, s69\n" \
    "v_mfma_f32_16x16x32_bf16 v[24:27], v[60:63], v[72:75], v[24:27]\nds_read_b128 v[72:75], v3 offset:7168\ns_lshr_b32 s69, s0, 8\n" \
    "v_mfma_f32_16x16x32_bf16 v[20:23], a[20:23], v[64:67], v[20:23]\nds_read_b128 v[76:79], v10 offset:9216\ns_and_b32 s69, s69, 0x2000\n" \
    "v_mfma_f32_16x16x32_bf16 v[28:31], v[60:63], v[64:67], v[28:31]\nds_read_b128 v[60:63], v10 offset:13312\ns_xor_b32 s0, s0, s69\ns_waitcnt lgkmcnt(3)\n" \
    "v_mfma_f32_16x16x32_bf16 v[16:19], a[24:27], v[80:83], v[16:19]\ns_add_u32 s1, s0, 0x10000\ns_add_u32 s2, s0, 0x20000\n" \
    "v_mfma_f32_16x16x32_bf16 v[24:27], v[68:71], v[80:83], v[24:27]\nds_read_b128 v[64:67], v3 offset:8192\ns_add_u32 s3, s0, 0x30000\nv_lshrrev_b32 v15, 1, v2\n" \
    "v_mfma_f32_16x16x32_bf16 v[20:23], a[24:27], v[56:59], v[20:23]\nds_read_b128 v[80:83], v9 offset:16384\nv_add_u32 v15, s60, v15\n" \
    "v_mfma_f32_16x16x32_bf16 v[28:31], v[68:71], v[56:59], v[28:31]\nds_read_b128 v[56:59], v9 offset:20480\ns_waitcnt lgkmcnt(3)\n" \
    "v_mfma_f32_16x16x32_bf16 v[16:19], a[28:31], v[76:79], v[16:19]\nds_write_b64 v15, v[252:253]\nv_mfma_f32_16x16x32_bf16 v[24:27], v[72:75], v[76:79], v[24:27]\n" \
    "ds_read_b128 v[68:71], v3 offset:9216\nv_mfma_f32_16x16x32_bf16 v[20:23], a[28:31], v[60:63], v[20:23]\nds_read_b128 v[76:79], v10 offset:16384\n" \
    "v_mfma_f32_16x16x32_bf16 v[28:31], v[72:75], v[60:63], v[28:31]\nds_read_b128 v[60:63], v10 offset:20480\ns_waitcnt lgkmcnt(4)\n" \
    "v_mfma_f32_16x16x32_bf16 v[16:19], a[32:35], v[80:83], v[16:19]\nv_mfma_f32_16x16x32_bf16 v[24:27], v[64:67], v[80:83], v[24:27]\nds_read_b128 v[72:75], v3 offset:10240\n" \
    "v_mfma_f32_16x16x32_bf16 v[20:23], a[32:35], v[56:59], v[20:23]\nds_read_b128 v[80:83], v9 offset:17408\nbuffer_load_dword v13, off, s[64:67], s42 sc1\n" \
    "v_mfma_f32_16x16x32_bf16 v[28:31], v[64:67], v[56:59], v[28:31]\nds_read_b128 v[56:59], v9 offset:21504\ns_and_b32 s69, s49, 255\ns_waitcnt lgkmcnt(3)\n" \
    "v_mfma_f32_16x16x32_bf16 v[16:19], a[36:39], v[76:79], v[16:19]\ns_lshl_b32 s69, s69, 19\ns_add_u32 s74, s63, s69\n" \
    "v_mfma_f32_16x16x32_bf16 v[24:27], v[68:71], v[76:79], v[24:27]\nds_read_b128 v[64:67], v3 offset:11264\ns_cmp_lt_u32 s49, 64\ns_cselect_b32 s74, s74, 0x7fff0000\n" \
    "v_mfma_f32_16x16x32_bf16 v[20:23], a[36:39], v[60:63], v[20:23]\nds_read_b128 v[76:79], v10 offset:17408\nv_mfma_f32_16x16x32_bf16 v[28:31], v[68:71], v[60:63], v[28:31]\n" \
    "ds_read_b128 v[60:63], v10 offset:21504\ns_waitcnt lgkmcnt(3)\nv_mfma_f32_16x16x32_bf16 v[16:19], a[40:43], v[80:83], v[16:19]\n" \
    "v_mfma_f32_16x16x32_bf16 v[24:27], v[72:75], v[80:83], v[24:27]\nds_read_b128 v[68:71], v3 offset:12288\ns_barrier\n" \
    "v_mfma_f32_16x16x32_bf16 v[20:23], a[40:43], v[56:59], v[20:23]\nds_read_b128 v[80:83], v9 offset:40960\nv_mfma_f32_16x16x32_bf16 v[28:31], v[72:75], v[56:59], v[28:31]\n" \
    "ds_read_b128 v[56:59], v9 offset:45056\ns_add_u32 m0, s41, 0\ns_waitcnt lgkmcnt(3)\nv_mfma_f32_16x16x32_bf16 v[16:19], a[44:47], v[76:79], v[16:19]\n" \
    "s_xor_b32 s85, s62, 1536\ns_add_u32 s85, s85, s40\nv_mfma_f32_16x16x32_bf16 v[24:27], v[64:67], v[76:79], v[24:27]\nds_read_b128 v[72:75], v3 offset:13312\n" \
    "buffer_load_dwordx4 v11, s[64:67], s85 offen sc1 lds\nv_mfma_f32_16x16x32_bf16 v[20:23], a[44:47], v[60:63], v[20:23]\nds_read_b128 v[76:79], v10 offset:40960\n" \
    "buffer_store_dwordx4 v[252:255], v2, s[64:67], s74 offen offset:0 sc1\nv_mfma_f32_16x16x32_bf16 v[28:31], v[64:67], v[60:63], v[28:31]\n" \
    "ds_read_b128 v[60:63], v10 offset:45056\ns_waitcnt lgkmcnt(3)\nv_mfma_f32_16x16x32_bf16 v[16:19], a[48:51], v[80:83], v[16:19]\n" \
    "buffer_load_dwordx4 v12, s[64:67], s85 offen offset:1024 sc1 lds\ns_add_u32 m0, s41, 8192\nv_mfma_f32_16x16x32_bf16 v[24:27], v[68:71], v[80:83], v[24:27]\n" \
    "ds_read_b128 v[64:67], v3 offset:14336\ns_xor_b32 s85, s62, 1792\nv_mfma_f32_16x16x32_bf16 v[20:23], a[48:51], v[56:59], v[20:23]\nds_read_b128 v[80:83], v9 offset:41984\n" \
    "s_add_u32 s85, s85, s40\nv_mfma_f32_16x16x32_bf16 v[28:31], v[68:71], v[56:59], v[28:31]\nds_read_b128 v[56:59], v9 offset:46080\n" \
    "buffer_load_dwordx4 v11, s[64:67], s85 offen sc1 lds\ns_waitcnt lgkmcnt(3)\nv_mfma_f32_16x16x32_bf16 v[16:19], a[52:55], v[76:79], v[16:19]\n" \
    "buffer_store_dwordx4 v[252:255], v2, s[64:67], s74 offen offset:1024 sc1\nv_mfma_f32_16x16x32_bf16 v[24:27], v[72:75], v[76:79], v[24:27]\n" \
    "ds_read_b128 v[68:71], v3 offset:15360\nv_mfma_f32_16x16x32_bf16 v[20:23], a[52:55], v[60:63], v[20:23]\nds_read_b128 v[76:79], v10 offset:41984\n" \
    "buffer_load_dwordx4 v12, s[64:67], s85 offen offset:1024 sc1 lds\nv_mfma_f32_16x16x32_bf16 v[28:31], v[72:75], v[60:63], v[28:31]\nds_read_b128 v[60:63], v10 offset:46080\n" \
    "s_waitcnt lgkmcnt(3)\nv_mfma_f32_16x16x32_bf16 v[16:19], a[56:59], v[80:83], v[16:19]\nbuffer_store_dwordx4 v[252:255], v7, s[64:67], s82 offen sc1\n" \
    "s_add_u32 s82, s82, 0x10000\nv_mfma_f32_16x16x32_bf16 v[24:27], v[64:67], v[80:83], v[24:27]\nds_read_b128 v[72:75], v3 offset:16384\ns_waitcnt vmcnt(8)\ns_barrier\n" \
    "v_mfma_f32_16x16x32_bf16 v[20:23], a[56:59], v[56:59], v[20:23]\nds_read_b128 v[80:83], v9 offset:24576\nv_mfma_f32_16x16x32_bf16 v[28:31], v[64:67], v[56:59], v[28:31]\n" \
    "ds_read_b128 v[56:59], v9 offset:28672\ns_waitcnt lgkmcnt(3)\nv_mfma_f32_16x16x32_bf16 v[16:19], a[60:63], v[76:79], v[16:19]\n" \
    "buffer_store_dwordx4 v[252:255], v7, s[64:67], s82 offen sc1\ns_add_u32 s82, s82, 0x10000\nv_mfma_f32_16x16x32_bf16 v[24:27], v[68:71], v[76:79], v[24:27]\n" \
    "ds_read_b128 v[64:67], v3 offset:17408\nv_mfma_f32_16x16x32_bf16 v[20:23], a[60:63], v[60:63], v[20:23]\nds_read_b128 v[76:79], v10 offset:24576\n" \
    "v_mfma_f32_16x16x32_bf16 v[28:31], v[68:71], v[60:63], v[28:31]\nds_read_b128 v[60:63], v10 offset:28672\ns_waitcnt lgkmcnt(3)\n" \
    "v_mfma_f32_16x16x32_bf16 v[16:19], a[64:67], v[80:83], v[16:19]\nbuffer_store_dwordx4 v[252:255], v7, s[64:67], s82 offen sc1\ns_add_u32 s82, s82, 0x10000\n" \
    "v_mfma_f32_16x16x32_bf16 v[24:27], v[72:75], v[80:83], v[24:27]\nds_read_b128 v[68:71], v3 offset:18432\nv_mfma_f32_16x16x32_bf16 v[20:23], a[64:67], v[56:59], v[20:23]\n" \
    "ds_read_b128 v[80:83], v9 offset:25600\nv_mfma_f32_16x16x32_bf16 v[28:31], v[72:75], v[56:59], v[28:31]\nds_read_b128 v[56:59], v9 offset:29696\ns_waitcnt lgkmcnt(3)\n" \
    "v_mfma_f32_16x16x32_bf16 v[16:19], a[68:71], v[76:79], v[16:19]\nbuffer_store_dwordx4 v[252:255], v7, s[64:67], s82 offen sc1\ns_add_u32 s82, s82, 0x10000\n" \
    "v_mfma_f32_16x16x32_bf16 v[24:27], v[64:67], v[76:79], v[24:27]\nds_read_b128 v[72:75], v3 offset:19456\ns_add_u32 s49, s49, 2\n" \
    "v_mfma_f32_16x16x32_bf16 v[20:23], a[68:71], v[60:63], v[20:23]\nds_read_b128 v[76:79], v10 offset:25600\ns_and_b32 s69, s35, 15\n" \
    "v_mfma_f32_16x16x32_bf16 v[28:31], v[64:67], v[60:63], v[28:31]\nds_read_b128 v[60:63], v10 offset:29696\ns_lshl_b32 s69, s69, 17\ns_waitcnt lgkmcnt(3)\n" \
    "v_mfma_f32_16x16x32_bf16 v[16:19], a[72:75], v[80:83], v[16:19]\ns_add_u32 s81, s37, s69\ns_lshr_b32 s69, s81, 8\n" \
    "v_mfma_f32_16x16x32_bf16 v[24:27], v[68:71], v[80:83], v[24:27]\nds_read_b128 v[64:67], v3 offset:20480\ns_and_b32 s69, s69, 0x2000\n" \
    "v_mfma_f32_16x16x32_bf16 v[20:23], a[72:75], v[56:59], v[20:23]\nds_read_b128 v[80:83], v9 offset:32768\ns_xor_b32 s81, s81, s69\n" \
    "v_mfma_f32_16x16x32_bf16 v[28:31], v[68:71], v[56:59], v[28:31]\nds_read_b128 v[56:59], v9 offset:36864\ns_mov_b32 s68, 0\ns_waitcnt lgkmcnt(3)\n" \
    "v_mfma_f32_16x16x32_bf16 v[16:19], a[76:79], v[76:79], v[16:19]\nv_mfma_f32_16x16x32_bf16 v[24:27], v[72:75], v[76:79], v[24:27]\nds_read_b128 v[68:71], v3 offset:21504\n" \
    "v_mfma_f32_16x16x32_bf16 v[20:23], a[76:79], v[60:63], v[20:23]\nds_read_b128 v[76:79], v10 offset:32768\nv_mfma_f32_16x16x32_bf16 v[28:31], v[72:75], v[60:63], v[28:31]\n" \
    "ds_read_b128 v[60:63], v10 offset:36864\ns_waitcnt lgkmcnt(3)\nv_mfma_f32_16x16x32_bf16 v[16:19], a[80:83], v[80:83], v[16:19]\n" \
    "v_mfma_f32_16x16x32_bf16 v[24:27], v[64:67], v[80:83], v[24:27]\nds_read_b128 v[72:75], v3 offset:22528\nv_mfma_f32_16x16x32_bf16 v[20:23], a[80:83], v[56:59], v[20:23]\n" \
    "ds_read_b128 v[80:83], v9 offset:33792\nv_mfma_f32_16x16x32_bf16 v[28:31], v[64:67], v[56:59], v[28:31]\nds_read_b128 v[56:59], v9 offset:37888\ns_waitcnt lgkmcnt(3)\n" \
    "v_mfma_f32_16x16x32_bf16 v[16:19], a[84:87], v[76:79], v[16:19]\nv_mfma_f32_16x16x32_bf16 v[24:27], v[68:71], v[76:79], v[24:27]\nds_read_b128 v[64:67], v3 offset:23552\n" \
    "v_mfma_f32_16x16x32_bf16 v[20:23], a[84:87], v[60:63], v[20:23]\nds_read_b128 v[76:79], v10 offset:33792\nv_mfma_f32_16x16x32_bf16 v[28:31], v[68:71], v[60:63], v[28:31]\n" \
    "ds_read_b128 v[60:63], v10 offset:37888\ns_waitcnt lgkmcnt(3)\nv_mfma_f32_16x16x32_bf16 v[16:19], a[88:91], v[80:83], v[16:19]\n" \
    "v_mfma_f32_16x16x32_bf16 v[24:27], v[72:75], v[80:83], v[24:27]\ns_waitcnt vmcnt(4)\ns_barrier\nv_mfma_f32_16x16x32_bf16 v[20:23], a[88:91], v[56:59], v[20:23]\n" \
    "ds_read_b128 v[68:71], v9 offset:0\nv_mfma_f32_16x16x32_bf16 v[28:31], v[72:75], v[56:59], v[28:31]\nds_read_b128 v[56:59], v9 offset:4096\ns_add_u32 m0, s41, 16384\n" \
    "s_waitcnt lgkmcnt(2)\nv_mfma_f32_16x16x32_bf16 v[16:19], a[92:95], v[76:79], v[16:19]\ns_xor_b32 s85, s62, 512\ns_add_u32 s85, s85, s84\n" \
    "v_mfma_f32_16x16x32_bf16 v[24:27], v[64:67], v[76:79], v[24:27]\nbuffer_load_dwordx4 v11, s[64:67], s85 offen sc1 lds\n" \
    "v_mfma_f32_16x16x32_bf16 v[20:23], a[92:95], v[60:63], v[20:23]\nds_read_b128 v[72:75], v10 offset:0\nv_mfma_f32_16x16x32_bf16 v[28:31], v[64:67], v[60:63], v[28:31]\n" \
    "ds_read_b128 v[60:63], v10 offset:4096\ns_waitcnt lgkmcnt(2)\nv_mfma_f32_16x16x32_bf16 v[16:19], a[96:99], v[68:71], v[16:19]\n" \
    "buffer_load_dwordx4 v12, s[64:67], s85 offen offset:1024 sc1 lds\ns_add_u32 m0, s41, 40960\nv_mfma_f32_16x16x32_bf16 v[24:27], a[104:107], v[68:71], v[24:27]\n" \
    "s_xor_b32 s85, s62, 768\ns_add_u32 s85, s85, s84\nv_mfma_f32_16x16x32_bf16 v[20:23], a[96:99], v[56:59], v[20:23]\nds_read_b128 v[64:67], v9 offset:1024\n" \
    "buffer_load_dwordx4 v11, s[64:67], s85 offen sc1 lds\nv_mfma_f32_16x16x32_bf16 v[28:31], a[104:107], v[56:59], v[28:31]\nds_read_b128 v[56:59], v9 offset:5120\n" \
    "s_waitcnt lgkmcnt(2)\nv_mfma_f32_16x16x32_bf16 v[16:19], a[100:103], v[72:75], v[16:19]\nv_mfma_f32_16x16x32_bf16 v[24:27], a[108:111], v[72:75], v[24:27]\n" \
    "buffer_load_dwordx4 v12, s[64:67], s85 offen offset:1024 sc1 lds\nv_mfma_f32_16x16x32_bf16 v[20:23], a[100:103], v[60:63], v[20:23]\n" \
    "ds_read_b128 v[68:71], v10 offset:1024\nv_mfma_f32_16x16x32_bf16 v[28:31], a[108:111], v[60:63], v[28:31]\nds_read_b128 v[60:63], v10 offset:5120\ns_waitcnt lgkmcnt(2)\n" \
    "v_mfma_f32_16x16x32_bf16 v[16:19], a[112:115], v[64:67], v[16:19]\nv_mfma_f32_16x16x32_bf16 v[24:27], a[120:123], v[64:67], v[24:27]\n" \
    "v_mfma_f32_16x16x32_bf16 v[20:23], a[112:115], v[56:59], v[20:23]\nds_read_b128 v[64:67], v9 offset:8192\n" \
    "v_mfma_f32_16x16x32_bf16 v[28:31], a[120:123], v[56:59], v[28:31]\nds_read_b128 v[56:59], v9 offset:12288\ns_waitcnt lgkmcnt(2)\n" \
    "v_mfma_f32_16x16x32_bf16 v[16:19], a[116:119], v[68:71], v[16:19]\nv_mfma_f32_16x16x32_bf16 v[24:27], a[124:127], v[68:71], v[24:27]\n" \
    "v_mfma_f32_16x16x32_bf16 v[20:23], a[116:119], v[60:63], v[20:23]\nds_read_b128 v[68:71], v10 offset:8192\n" \
    "v_mfma_f32_16x16x32_bf16 v[28:31], a[124:127], v[60:63], v[28:31]\nds_read_b128 v[60:63], v10 offset:12288\ns_waitcnt lgkmcnt(2)\n" \
    "v_mfma_f32_16x16x32_bf16 v[16:19], a[128:131], v[64:67], v[16:19]\nv_readfirstlane_b32 s74, v13\nv_mfma_f32_16x16x32_bf16 v[24:27], a[136:139], v[64:67], v[24:27]\n" \
    "s_and_b32 s75, s74, 127\ns_lshr_b32 s69, s74, 8\nv_mfma_f32_16x16x32_bf16 v[20:23], a[128:131], v[56:59], v[20:23]\nds_read_b128 v[64:67], v9 offset:9216\n" \
    "s_sub_u32 s69, s69, s80\nv_mfma_f32_16x16x32_bf16 v[28:31], a[136:139], v[56:59], v[28:31]\nds_read_b128 v[56:59], v9 offset:13312\ns_bfe_i32 s69, s69, 0x180000\n" \
    "s_waitcnt lgkmcnt(2)\nv_mfma_f32_16x16x32_bf16 v[16:19], a[132:135], v[68:71], v[16:19]\nds_read_b128 v[72:75], v10 offset:9216\ns_cmp_eq_u32 s75, 0\n" \
    "s_cselect_b32 s69, 0, s69\nv_mfma_f32_16x16x32_bf16 v[24:27], a[140:143], v[68:71], v[24:27]\nds_read_b128 v[68:71], v10 offset:13312\ns_add_u32 s80, s80, s69\n" \
    "v_mfma_f32_16x16x32_bf16 v[20:23], a[132:135], v[60:63], v[20:23]\ns_lshl_b32 s74, s74, 5\ns_and_b32 s74, s74, 0x1000\n" \
    "v_mfma_f32_16x16x32_bf16 v[28:31], a[140:143], v[60:63], v[28:31]\ns_andn2_b32 s81, s81, 0x1000\ns_or_b32 s81, s81, s74\ns_waitcnt lgkmcnt(2)\n" \
    "v_mfma_f32_16x16x32_bf16 v[16:19], a[144:147], v[64:67], v[16:19]\nv_mfma_f32_16x16x32_bf16 v[24:27], a[152:155], v[64:67], v[24:27]\ns_waitcnt lgkmcnt(0)\ns_barrier\n" \
    "v_mfma_f32_16x16x32_bf16 v[16:19], a[148:151], v[72:75], v[16:19]\ns_add_u32 m0, s41, 0\ns_xor_b32 s85, s62, 0\n" \
    "v_mfma_f32_16x16x32_bf16 v[24:27], a[156:159], v[72:75], v[24:27]\ns_add_u32 s85, s85, s84\nbuffer_load_dwordx4 v11, s[64:67], s85 offen sc1 lds\n" \
    "v_mfma_f32_16x16x32_bf16 v[20:23], a[144:147], v[56:59], v[20:23]\ns_nop 1\nv_cvt_pk_bf16_f32 v32, v16, v17\nv_cvt_pk_bf16_f32 v33, v18, v19\n" \
    "v_mfma_f32_16x16x32_bf16 v[28:31], a[152:155], v[56:59], v[28:31]\nv_cvt_pk_bf16_f32 v34, v24, v25\nv_cvt_pk_bf16_f32 v35, v26, v27\n" \
    "v_mfma_f32_16x16x32_bf16 v[20:23], a[148:151], v[68:71], v[20:23]\nbuffer_load_dwordx4 v12, s[64:67], s85 offen offset:1024 sc1 lds\ns_add_u32 m0, s41, 8192\n" \
    "v_mfma_f32_16x16x32_bf16 v[28:31], a[156:159], v[68:71], v[28:31]\ns_xor_b32 s85, s62, 256\ns_add_u32 s85, s85, s84\nds_write_b128 v14, v[32:35] offset:0\n" \
    "buffer_load_dwordx4 v11, s[64:67], s85 offen sc1 lds\nbuffer_load_dwordx4 v12, s[64:67], s85 offen offset:1024 sc1 lds\nv_cvt_pk_bf16_f32 v36, v20, v21\n" \
    "v_cvt_pk_bf16_f32 v37, v22, v23\ns_mov_b32 s40, s84\nv_cvt_pk_bf16_f32 v38, v28, v29\nv_cvt_pk_bf16_f32 v39, v30, v31\nds_write_b128 v14, v[36:39] offset:4096\n" \
    "s_waitcnt vmcnt(0) lgkmcnt(0)\ns_barrier\n.Lv5_%=:\nds_read_b128 v[56:59], v6 offset:0\nds_read_b128 v[60:63], v6 offset:1024\nds_read_b128 v[64:67], v6 offset:2048\n" \
    "v_mov_b64 v[32:33], 0\nv_mov_b64 v[34:35], 0\nv_mov_b64 v[36:37], 0\nv_mov_b64 v[38:39], 0\ns_and_b32 s69, s50, 15\ns_lshl_b32 s69, s69, 21\ns_add_u32 s86, s39, s69\n" \
    "s_lshr_b32 s69, s86, 8\ns_and_b32 s69, s69, 0x2000\ns_xor_b32 s86, s86, s69\ns_waitcnt lgkmcnt(2)\nv_smfmac_f32_16x16x64_bf16 v[32:35], v[56:59], a[160:167], v0\n" \
    "ds_read_b128 v[68:71], v6 offset:3072\nv_mov_b64 v[40:41], 0\nv_smfmac_f32_16x16x64_bf16 v[36:39], v[56:59], a[176:183], v0\nds_read_b128 v[72:75], v3 offset:0\n" \
    "s_waitcnt lgkmcnt(3)\nv_mov_b64 v[42:43], 0\nv_smfmac_f32_16x16x64_bf16 v[32:35], v[60:63], a[168:175], v0\nds_read_b128 v[76:79], v9 offset:0\nv_mov_b64 v[44:45], 0\n" \
    "v_smfmac_f32_16x16x64_bf16 v[36:39], v[60:63], a[184:191], v0\nds_read_b128 v[80:83], v9 offset:4096\ns_waitcnt lgkmcnt(4)\nv_mov_b64 v[46:47], 0\n" \
    "v_smfmac_f32_16x16x64_bf16 v[32:35], v[64:67], a[192:199], v0\ns_add_u32 s87, s86, 0x40000\ns_add_u32 s88, s86, 0x80000\n" \
    "v_smfmac_f32_16x16x64_bf16 v[36:39], v[64:67], a[208:215], v0\ns_add_u32 s89, s86, 0xc0000\ns_add_u32 s50, s50, 2\ns_waitcnt lgkmcnt(3)\n" \
    "v_smfmac_f32_16x16x64_bf16 v[32:35], v[68:71], a[200:207], v0\ns_add_u32 m0, s41, 24576\ns_xor_b32 s85, s62, 1024\n" \
    "v_smfmac_f32_16x16x64_bf16 v[36:39], v[68:71], a[216:223], v0\ns_add_u32 s85, s85, s40\nbuffer_load_dwordx4 v11, s[64:67], s85 offen sc1 lds\ns_waitcnt lgkmcnt(0)\n" \
    "v_mfma_f32_16x16x32_bf16 v[16:19], a[0:3], v[76:79], 0\ns_nop 0\nv_cvt_pk_bf16_f32 v48, v32, v33\nv_cvt_pk_bf16_f32 v50, v34, v35\n" \
    "v_mfma_f32_16x16x32_bf16 v[24:27], v[72:75], v[76:79], 0\nv_cvt_pk_bf16_f32 v49, v36, v37\nv_cvt_pk_bf16_f32 v51, v38, v39\n" \
    "v_smfmac_f32_16x16x64_bf16 v[40:43], v[56:59], a[224:231], v0\nbuffer_load_dwordx4 v12, s[64:67], s85 offen offset:1024 sc1 lds\nv_permlane16_swap_b32 v48, v49\n" \
    "v_smfmac_f32_16x16x64_bf16 v[44:47], v[56:59], a[240:247], v0\ns_add_u32 m0, s41, 32768\nv_permlane16_swap_b32 v50, v51\n" \
    "v_smfmac_f32_16x16x64_bf16 v[40:43], v[60:63], a[232:239], v0\nbuffer_store_dwordx4 v[48:51], v8, s[64:67], s86 offen offset:0 sc1\nv_mov_b64 v[32:33], 0\n" \
    "v_smfmac_f32_16x16x64_bf16 v[44:47], v[60:63], a[248:255], v0\ns_xor_b32 s85, s62, 1280\nv_mov_b64 v[34:35], 0\n" \
    "v_smfmac_f32_16x16x64_bf16 v[40:43], v[64:67], v[92:99], v0\ns_add_u32 s85, s85, s40\nv_mov_b64 v[36:37], 0\nv_smfmac_f32_16x16x64_bf16 v[44:47], v[64:67], v[108:115], v0\n" \
    "buffer_load_dwordx4 v11, s[64:67], s85 offen sc1 lds\nv_mov_b64 v[38:39], 0\nv_smfmac_f32_16x16x64_bf16 v[40:43], v[68:71], v[100:107], v0\n" \
    "v_smfmac_f32_16x16x64_bf16 v[44:47], v[68:71], v[116:123], v0\ns_nop 1\nv_mfma_f32_16x16x32_bf16 v[20:23], a[0:3], v[80:83], 0\n" \
    "buffer_load_dwordx4 v12, s[64:67], s85 offen offset:1024 sc1 lds\ns_add_u32 s84, s40, 0x20000\nv_mfma_f32_16x16x32_bf16 v[28:31], v[72:75], v[80:83], 0\n" \
    "s_cmp_lt_u32 s49, 62\ns_cselect_b32 s84, s84, s40\nv_cvt_pk_bf16_f32 v52, v40, v41\nv_smfmac_f32_16x16x64_bf16 v[32:35], v[56:59], v[124:131], v0\n" \
    "v_cvt_pk_bf16_f32 v54, v42, v43\nv_smfmac_f32_16x16x64_bf16 v[36:39], v[56:59], v[140:147], v0\nds_read_b128 v[72:75], v3 offset:1024\nv_cvt_pk_bf16_f32 v53, v44, v45\n" \
    "v_smfmac_f32_16x16x64_bf16 v[32:35], v[60:63], v[132:139], v0\nds_read_b128 v[76:79], v10 offset:0\nv_cvt_pk_bf16_f32 v55, v46, v47\n" \
    "v_smfmac_f32_16x16x64_bf16 v[36:39], v[60:63], v[148:155], v0\nds_read_b128 v[80:83], v10 offset:4096\nv_permlane16_swap_b32 v52, v53\n" \
    "v_smfmac_f32_16x16x64_bf16 v[32:35], v[64:67], v[156:163], v0\nv_permlane16_swap_b32 v54, v55\nv_smfmac_f32_16x16x64_bf16 v[36:39], v[64:67], v[172:179], v0\n" \
    "buffer_store_dwordx4 v[52:55], v8, s[64:67], s87 offen offset:0 sc1\nv_mov_b64 v[40:41], 0\nv_smfmac_f32_16x16x64_bf16 v[32:35], v[68:71], v[164:171], v0\n" \
    "v_mov_b64 v[42:43], 0\nv_smfmac_f32_16x16x64_bf16 v[36:39], v[68:71], v[180:187], v0\ns_waitcnt lgkmcnt(0)\nv_mov_b64 v[44:45], 0\n" \
    "v_mfma_f32_16x16x32_bf16 v[16:19], a[4:7], v[76:79], v[16:19]\nv_mov_b64 v[46:47], 0\nv_mov_b64 v[252:253], -1\n" \
    "v_mfma_f32_16x16x32_bf16 v[24:27], v[72:75], v[76:79], v[24:27]\nv_cvt_pk_bf16_f32 v48, v32, v33\nv_cvt_pk_bf16_f32 v50, v34, v35\n" \
    "v_smfmac_f32_16x16x64_bf16 v[40:43], v[56:59], v[188:195], v0\nv_cvt_pk_bf16_f32 v49, v36, v37\nv_smfmac_f32_16x16x64_bf16 v[44:47], v[56:59], v[204:211], v0\n" \
    "v_cvt_pk_bf16_f32 v51, v38, v39\nv_smfmac_f32_16x16x64_bf16 v[40:43], v[60:63], v[196:203], v0\nv_permlane16_swap_b32 v48, v49\n" \
    "v_smfmac_f32_16x16x64_bf16 v[44:47], v[60:63], v[212:219], v0\nv_permlane16_swap_b32 v50, v51\nv_smfmac_f32_16x16x64_bf16 v[40:43], v[64:67], v[220:227], v0\n" \
    "buffer_store_dwordx4 v[48:51], v8, s[64:67], s88 offen offset:0 sc1\nv_mov_b64 v[32:33], 0\nv_smfmac_f32_16x16x64_bf16 v[44:47], v[64:67], v[236:243], v0\n" \
    "ds_read_b128 v[76:79], v6 offset:128\nv_mov_b64 v[34:35], 0\nv_smfmac_f32_16x16x64_bf16 v[40:43], v[68:71], v[228:235], v0\nv_mov_b64 v[36:37], 0\n" \
    "v_smfmac_f32_16x16x64_bf16 v[44:47], v[68:71], v[244:251], v0\nds_read_b128 v[56:59], v6 offset:1152\nv_mov_b64 v[38:39], 0\n" \
    "v_mfma_f32_16x16x32_bf16 v[20:23], a[4:7], v[80:83], v[20:23]\ns_nop 0\nv_mov_b64 v[254:255], -1\nv_mfma_f32_16x16x32_bf16 v[28:31], v[72:75], v[80:83], v[28:31]\n" \
    "ds_read_b128 v[60:63], v6 offset:2176\ns_waitcnt lgkmcnt(2)\nv_cvt_pk_bf16_f32 v52, v40, v41\nv_smfmac_f32_16x16x64_bf16 v[32:35], v[76:79], a[160:167], v0\n" \
    "ds_read_b128 v[64:67], v6 offset:3200\nv_cvt_pk_bf16_f32 v54, v42, v43\nv_smfmac_f32_16x16x64_bf16 v[36:39], v[76:79], a[176:183], v0\n" \
    "ds_read_b128 v[68:71], v3 offset:2048\ns_waitcnt lgkmcnt(3)\nv_cvt_pk_bf16_f32 v53, v44, v45\nv_smfmac_f32_16x16x64_bf16 v[32:35], v[56:59], a[168:175], v0\n" \
    "ds_read_b128 v[72:75], v9 offset:1024\nv_cvt_pk_bf16_f32 v55, v46, v47\nv_smfmac_f32_16x16x64_bf16 v[36:39], v[56:59], a[184:191], v0\n" \
    "ds_read_b128 v[80:83], v9 offset:5120\ns_waitcnt lgkmcnt(4)\nv_permlane16_swap_b32 v52, v53\nv_smfmac_f32_16x16x64_bf16 v[32:35], v[60:63], a[192:199], v0\n" \
    "s_and_b32 s69, s49, 15\nv_permlane16_swap_b32 v54, v55\nv_smfmac_f32_16x16x64_bf16 v[36:39], v[60:63], a[208:215], v0\n" \
    "buffer_store_dwordx4 v[52:55], v8, s[64:67], s89 offen offset:0 sc1\ns_waitcnt lgkmcnt(3)\nv_mov_b64 v[40:41], 0\n" \
    "v_smfmac_f32_16x16x64_bf16 v[32:35], v[64:67], a[200:207], v0\ns_lshl_b32 s69, s69, 19\nv_mov_b64 v[42:43], 0\n" \
    "v_smfmac_f32_16x16x64_bf16 v[36:39], v[64:67], a[216:223], v0\ns_add_u32 s82, s38, s69\ns_waitcnt lgkmcnt(0)\nv_mov_b64 v[44:45], 0\n" \
    "v_mfma_f32_16x16x32_bf16 v[16:19], a[8:11], v[72:75], v[16:19]\ns_lshr_b32 s69, s82, 8\nv_mov_b64 v[46:47], 0\nv_cvt_pk_bf16_f32 v48, v32, v33\n" \
    "v_mfma_f32_16x16x32_bf16 v[24:27], v[68:71], v[72:75], v[24:27]\ns_and_b32 s69, s69, 0x2000\nv_cvt_pk_bf16_f32 v50, v34, v35\nv_cvt_pk_bf16_f32 v49, v36, v37\n" \
    "v_smfmac_f32_16x16x64_bf16 v[40:43], v[76:79], a[224:231], v0\ns_xor_b32 s82, s82, s69\nv_cvt_pk_bf16_f32 v51, v38, v39\n" \
    "v_smfmac_f32_16x16x64_bf16 v[44:47], v[76:79], a[240:247], v0\ns_and_b32 s69, s35, 15\nv_permlane16_swap_b32 v48, v49\n" \
    "v_smfmac_f32_16x16x64_bf16 v[40:43], v[56:59], a[232:239], v0\ns_lshl_b32 s69, s69, 19\nv_permlane16_swap_b32 v50, v51\n" \
    "v_smfmac_f32_16x16x64_bf16 v[44:47], v[56:59], a[248:255], v0\nbuffer_store_dwordx4 v[48:51], v8, s[64:67], s86 offen offset:256 sc1\nv_mov_b64 v[32:33], 0\n" \
    "v_smfmac_f32_16x16x64_bf16 v[40:43], v[60:63], v[92:99], v0\ns_add_u32 s0, s38, s69\nv_mov_b64 v[34:35], 0\nv_smfmac_f32_16x16x64_bf16 v[44:47], v[60:63], v[108:115], v0\n" \
    "s_lshr_b32 s69, s0, 8\nv_mov_b64 v[36:37], 0\nv_smfmac_f32_16x16x64_bf16 v[40:43], v[64:67], v[100:107], v0\ns_and_b32 s69, s69, 0x2000\nv_mov_b64 v[38:39], 0\n" \
    "v_smfmac_f32_16x16x64_bf16 v[44:47], v[64:67], v[116:123], v0\ns_xor_b32 s0, s0, s69\ns_add_u32 s1, s0, 0x10000\n" \
    "v_mfma_f32_16x16x32_bf16 v[20:23], a[8:11], v[80:83], v[20:23]\ns_add_u32 s2, s0, 0x20000\ns_add_u32 s3, s0, 0x30000\nv_cvt_pk_bf16_f32 v52, v40, v41\n" \
    "v_mfma_f32_16x16x32_bf16 v[28:31], v[68:71], v[80:83], v[28:31]\nv_cvt_pk_bf16_f32 v54, v42, v43\nv_cvt_pk_bf16_f32 v53, v44, v45\n" \
    "v_smfmac_f32_16x16x64_bf16 v[32:35], v[76:79], v[124:131], v0\nv_cvt_pk_bf16_f32 v55, v46, v47\nv_smfmac_f32_16x16x64_bf16 v[36:39], v[76:79], v[140:147], v0\n" \
    "ds_read_b128 v[68:71], v3 offset:3072\nv_permlane16_swap_b32 v52, v53\nv_smfmac_f32_16x16x64_bf16 v[32:35], v[56:59], v[132:139], v0\n" \
    "ds_read_b128 v[72:75], v10 offset:1024\nv_permlane16_swap_b32 v54, v55\nv_smfmac_f32_16x16x64_bf16 v[36:39], v[56:59], v[148:155], v0\n" \
    "ds_read_b128 v[80:83], v10 offset:5120\nbuffer_store_dwordx4 v[52:55], v8, s[64:67], s87 offen offset:256 sc1\nv_mov_b64 v[40:41], 0\n" \
    "v_smfmac_f32_16x16x64_bf16 v[32:35], v[60:63], v[156:163], v0\nv_mov_b64 v[42:43], 0\nv_smfmac_f32_16x16x64_bf16 v[36:39], v[60:63], v[172:179], v0\nv_mov_b64 v[44:45], 0\n" \
    "v_smfmac_f32_16x16x64_bf16 v[32:35], v[64:67], v[164:171], v0\nv_mov_b64 v[46:47], 0\nv_smfmac_f32_16x16x64_bf16 v[36:39], v[64:67], v[180:187], v0\ns_nop 0\n" \
    "s_waitcnt lgkmcnt(0)\nv_lshrrev_b32 v15, 1, v2\nv_mfma_f32_16x16x32_bf16 v[16:19], a[12:15], v[72:75], v[16:19]\ns_nop 0\nv_add_u32 v15, s60, v15\n" \
    "v_cvt_pk_bf16_f32 v48, v32, v33\nv_mfma_f32_16x16x32_bf16 v[24:27], v[68:71], v[72:75], v[24:27]\nds_write_b64 v15, v[252:253]\nv_cvt_pk_bf16_f32 v50, v34, v35\n" \
    "v_cvt_pk_bf16_f32 v49, v36, v37\nv_smfmac_f32_16x16x64_bf16 v[40:43], v[76:79], v[188:195], v0\nv_cvt_pk_bf16_f32 v51, v38, v39\n" \
    "v_smfmac_f32_16x16x64_bf16 v[44:47], v[76:79], v[204:211], v0\nv_permlane16_swap_b32 v48, v49\nv_smfmac_f32_16x16x64_bf16 v[40:43], v[56:59], v[196:203], v0\n" \
    "v_permlane16_swap_b32 v50, v51\nv_smfmac_f32_16x16x64_bf16 v[44:47], v[56:59], v[212:219], v0\nbuffer_store_dwordx4 v[48:51], v8, s[64:67], s88 offen offset:256 sc1\n" \
    "v_mov_b64 v[32:33], 0\nv_smfmac_f32_16x16x64_bf16 v[40:43], v[60:63], v[220:227], v0\nv_mov_b64 v[34:35], 0\nv_smfmac_f32_16x16x64_bf16 v[44:47], v[60:63], v[236:243], v0\n" \
    "ds_read_b128 v[56:59], v6 offset:4096\nv_mov_b64 v[36:37], 0\nv_smfmac_f32_16x16x64_bf16 v[40:43], v[64:67], v[228:235], v0\nv_mov_b64 v[38:39], 0\n" \
    "v_smfmac_f32_16x16x64_bf16 v[44:47], v[64:67], v[244:251], v0\nds_read_b128 v[60:63], v6 offset:5120\ns_nop 0\n" \
    "v_mfma_f32_16x16x32_bf16 v[20:23], a[12:15], v[80:83], v[20:23]\ns_nop 1\nv_mfma_f32_16x16x32_bf16 v[28:31], v[68:71], v[80:83], v[28:31]\n" \
    "ds_read_b128 v[64:67], v6 offset:6144\ns_waitcnt lgkmcnt(2)\nv_cvt_pk_bf16_f32 v52, v40, v41\nv_smfmac_f32_16x16x64_bf16 v[32:35], v[56:59], a[160:167], v0\n" \
    "ds_read_b128 v[68:71], v6 offset:7168\nv_cvt_pk_bf16_f32 v54, v42, v43\nv_smfmac_f32_16x16x64_bf16 v[36:39], v[56:59], a[176:183], v0\n" \
    "ds_read_b128 v[72:75], v3 offset:4096\ns_waitcnt lgkmcnt(3)\nv_cvt_pk_bf16_f32 v53, v44, v45\nv_smfmac_f32_16x16x64_bf16 v[32:35], v[60:63], a[168:175], v0\n" \
    "ds_read_b128 v[76:79], v9 offset:8192\nv_cvt_pk_bf16_f32 v55, v46, v47\nv_smfmac_f32_16x16x64_bf16 v[36:39], v[60:63], a[184:191], v0\n" \
    "ds_read_b128 v[80:83], v9 offset:12288\ns_waitcnt lgkmcnt(4)\nv_permlane16_swap_b32 v52, v53\nv_smfmac_f32_16x16x64_bf16 v[32:35], v[64:67], a[192:199], v0\n" \
    "v_permlane16_swap_b32 v54, v55\nv_smfmac_f32_16x16x64_bf16 v[36:39], v[64:67], a[208:215], v0\nbuffer_store_dwordx4 v[52:55], v8, s[64:67], s89 offen offset:256 sc1\n" \
    "s_waitcnt lgkmcnt(3)\nv_mov_b64 v[40:41], 0\nv_smfmac_f32_16x16x64_bf16 v[32:35], v[68:71], a[200:207], v0\nv_mov_b64 v[42:43], 0\n" \
    "v_smfmac_f32_16x16x64_bf16 v[36:39], v[68:71], a[216:223], v0\ns_waitcnt lgkmcnt(0)\nv_mov_b64 v[44:45], 0\n" \
    "v_mfma_f32_16x16x32_bf16 v[16:19], a[16:19], v[76:79], v[16:19]\nv_mov_b64 v[46:47], 0\nv_mfma_f32_16x16x32_bf16 v[24:27], v[72:75], v[76:79], v[24:27]\ns_nop 0\n" \
    "v_cvt_pk_bf16_f32 v48, v32, v33\nv_cvt_pk_bf16_f32 v50, v34, v35\nv_smfmac_f32_16x16x64_bf16 v[40:43], v[56:59], a[224:231], v0\nv_cvt_pk_bf16_f32 v49, v36, v37\n" \
    "v_smfmac_f32_16x16x64_bf16 v[44:47], v[56:59], a[240:247], v0\nv_cvt_pk_bf16_f32 v51, v38, v39\nv_smfmac_f32_16x16x64_bf16 v[40:43], v[60:63], a[232:239], v0\n" \
    "v_permlane16_swap_b32 v48, v49\nv_smfmac_f32_16x16x64_bf16 v[44:47], v[60:63], a[248:255], v0\nv_permlane16_swap_b32 v50, v51\n" \
    "v_smfmac_f32_16x16x64_bf16 v[40:43], v[64:67], v[92:99], v0\nbuffer_store_dwordx4 v[48:51], v8, s[64:67], s86 offen offset:512 sc1\nv_mov_b64 v[32:33], 0\n" \
    "v_smfmac_f32_16x16x64_bf16 v[44:47], v[64:67], v[108:115], v0\nv_mov_b64 v[34:35], 0\nv_smfmac_f32_16x16x64_bf16 v[40:43], v[68:71], v[100:107], v0\nv_mov_b64 v[36:37], 0\n" \
    "v_smfmac_f32_16x16x64_bf16 v[44:47], v[68:71], v[116:123], v0\nv_mov_b64 v[38:39], 0\nv_mfma_f32_16x16x32_bf16 v[20:23], a[16:19], v[80:83], v[20:23]\ns_nop 1\n" \
    "v_mfma_f32_16x16x32_bf16 v[28:31], v[72:75], v[80:83], v[28:31]\ns_nop 0\nv_cvt_pk_bf16_f32 v52, v40, v41\nv_cvt_pk_bf16_f32 v54, v42, v43\n" \
    "v_smfmac_f32_16x16x64_bf16 v[32:35], v[56:59], v[124:131], v0\nv_cvt_pk_bf16_f32 v53, v44, v45\nv_smfmac_f32_16x16x64_bf16 v[36:39], v[56:59], v[140:147], v0\n" \
    "ds_read_b128 v[72:75], v3 offset:5120\nv_cvt_pk_bf16_f32 v55, v46, v47\nv_smfmac_f32_16x16x64_bf16 v[32:35], v[60:63], v[132:139], v0\n" \
    "ds_read_b128 v[76:79], v10 offset:8192\nv_permlane16_swap_b32 v52, v53\nv_smfmac_f32_16x16x64_bf16 v[36:39], v[60:63], v[148:155], v0\n" \
    "ds_read_b128 v[80:83], v10 offset:12288\nv_permlane16_swap_b32 v54, v55\nv_smfmac_f32_16x16x64_bf16 v[32:35], v[64:67], v[156:163], v0\n" \
    "buffer_store_dwordx4 v[52:55], v8, s[64:67], s87 offen offset:512 sc1\nv_mov_b64 v[40:41], 0\nv_smfmac_f32_16x16x64_bf16 v[36:39], v[64:67], v[172:179], v0\n" \
    "v_mov_b64 v[42:43], 0\nv_smfmac_f32_16x16x64_bf16 v[32:35], v[68:71], v[164:171], v0\nv_mov_b64 v[44:45], 0\nv_smfmac_f32_16x16x64_bf16 v[36:39], v[68:71], v[180:187], v0\n" \
    "s_waitcnt lgkmcnt(0)\nv_mov_b64 v[46:47], 0\nv_mfma_f32_16x16x32_bf16 v[16:19], a[20:23], v[76:79], v[16:19]\ns_nop 1\n" \
    "v_mfma_f32_16x16x32_bf16 v[24:27], v[72:75], v[76:79], v[24:27]\nv_cvt_pk_bf16_f32 v48, v32, v33\nv_cvt_pk_bf16_f32 v50, v34, v35\n" \
    "v_smfmac_f32_16x16x64_bf16 v[40:43], v[56:59], v[188:195], v0\nv_cvt_pk_bf16_f32 v49, v36, v37\nv_smfmac_f32_16x16x64_bf16 v[44:47], v[56:59], v[204:211], v0\n" \
    "v_cvt_pk_bf16_f32 v51, v38, v39\nv_smfmac_f32_16x16x64_bf16 v[40:43], v[60:63], v[196:203], v0\nv_permlane16_swap_b32 v48, v49\n" \
    "v_smfmac_f32_16x16x64_bf16 v[44:47], v[60:63], v[212:219], v0\nv_permlane16_swap_b32 v50, v51\nv_smfmac_f32_16x16x64_bf16 v[40:43], v[64:67], v[220:227], v0\n" \
    "buffer_store_dwordx4 v[48:51], v8, s[64:67], s88 offen offset:512 sc1\nv_mov_b64 v[32:33], 0\nv_smfmac_f32_16x16x64_bf16 v[44:47], v[64:67], v[236:243], v0\n" \
    "ds_read_b128 v[76:79], v6 offset:4224\nv_mov_b64 v[34:35], 0\nv_smfmac_f32_16x16x64_bf16 v[40:43], v[68:71], v[228:235], v0\nv_mov_b64 v[36:37], 0\n" \
    "v_smfmac_f32_16x16x64_bf16 v[44:47], v[68:71], v[244:251], v0\nds_read_b128 v[56:59], v6 offset:5248\nv_mov_b64 v[38:39], 0\n" \
    "v_mfma_f32_16x16x32_bf16 v[20:23], a[20:23], v[80:83], v[20:23]\ns_nop 1\nv_mfma_f32_16x16x32_bf16 v[28:31], v[72:75], v[80:83], v[28:31]\n" \
    "ds_read_b128 v[60:63], v6 offset:6272\ns_waitcnt lgkmcnt(2)\nv_cvt_pk_bf16_f32 v52, v40, v41\nv_smfmac_f32_16x16x64_bf16 v[32:35], v[76:79], a[160:167], v0\n" \
    "ds_read_b128 v[64:67], v6 offset:7296\nv_cvt_pk_bf16_f32 v54, v42, v43\nv_smfmac_f32_16x16x64_bf16 v[36:39], v[76:79], a[176:183], v0\n" \
    "ds_read_b128 v[68:71], v3 offset:6144\ns_waitcnt lgkmcnt(3)\nv_cvt_pk_bf16_f32 v53, v44, v45\nv_smfmac_f32_16x16x64_bf16 v[32:35], v[56:59], a[168:175], v0\n" \
    "ds_read_b128 v[72:75], v9 offset:9216\nv_cvt_pk_bf16_f32 v55, v46, v47\nv_smfmac_f32_16x16x64_bf16 v[36:39], v[56:59], a[184:191], v0\n" \
    "ds_read_b128 v[80:83], v9 offset:13312\ns_waitcnt lgkmcnt(4)\nv_permlane16_swap_b32 v52, v53\nv_smfmac_f32_16x16x64_bf16 v[32:35], v[60:63], a[192:199], v0\n" \
    "v_permlane16_swap_b32 v54, v55\nv_smfmac_f32_16x16x64_bf16 v[36:39], v[60:63], a[208:215], v0\nbuffer_store_dwordx4 v[52:55], v8, s[64:67], s89 offen offset:512 sc1\n" \
    "s_waitcnt lgkmcnt(3)\nv_mov_b64 v[40:41], 0\nv_smfmac_f32_16x16x64_bf16 v[32:35], v[64:67], a[200:207], v0\nv_mov_b64 v[42:43], 0\n" \
    "v_smfmac_f32_16x16x64_bf16 v[36:39], v[64:67], a[216:223], v0\ns_waitcnt lgkmcnt(0)\nv_mov_b64 v[44:45], 0\n" \
    "v_mfma_f32_16x16x32_bf16 v[16:19], a[24:27], v[72:75], v[16:19]\nv_mov_b64 v[46:47], 0\nv_mfma_f32_16x16x32_bf16 v[24:27], v[68:71], v[72:75], v[24:27]\ns_nop 0\n" \
    "v_cvt_pk_bf16_f32 v48, v32, v33\nv_cvt_pk_bf16_f32 v50, v34, v35\nv_smfmac_f32_16x16x64_bf16 v[40:43], v[76:79], a[224:231], v0\nv_cvt_pk_bf16_f32 v49, v36, v37\n" \
    "v_smfmac_f32_16x16x64_bf16 v[44:47], v[76:79], a[240:247], v0\nv_cvt_pk_bf16_f32 v51, v38, v39\nv_smfmac_f32_16x16x64_bf16 v[40:43], v[56:59], a[232:239], v0\n" \
    "v_permlane16_swap_b32 v48, v49\nv_smfmac_f32_16x16x64_bf16 v[44:47], v[56:59], a[248:255], v0\nv_permlane16_swap_b32 v50, v51\n" \
    "v_smfmac_f32_16x16x64_bf16 v[40:43], v[60:63], v[92:99], v0\nbuffer_store_dwordx4 v[48:51], v8, s[64:67], s86 offen offset:768 sc1\nv_mov_b64 v[32:33], 0\n" \
    "v_smfmac_f32_16x16x64_bf16 v[44:47], v[60:63], v[108:115], v0\nv_mov_b64 v[34:35], 0\nv_smfmac_f32_16x16x64_bf16 v[40:43], v[64:67], v[100:107], v0\nv_mov_b64 v[36:37], 0\n" \
    "v_smfmac_f32_16x16x64_bf16 v[44:47], v[64:67], v[116:123], v0\nv_mov_b64 v[38:39], 0\nv_mfma_f32_16x16x32_bf16 v[20:23], a[24:27], v[80:83], v[20:23]\ns_nop 1\n" \
    "v_mfma_f32_16x16x32_bf16 v[28:31], v[68:71], v[80:83], v[28:31]\ns_nop 0\nv_cvt_pk_bf16_f32 v52, v40, v41\nv_cvt_pk_bf16_f32 v54, v42, v43\n" \
    "v_smfmac_f32_16x16x64_bf16 v[32:35], v[76:79], v[124:131], v0\nv_cvt_pk_bf16_f32 v53, v44, v45\nv_smfmac_f32_16x16x64_bf16 v[36:39], v[76:79], v[140:147], v0\n" \
    "ds_read_b128 v[68:71], v3 offset:7168\nv_cvt_pk_bf16_f32 v55, v46, v47\nv_smfmac_f32_16x16x64_bf16 v[32:35], v[56:59], v[132:139], v0\n" \
    "ds_read_b128 v[72:75], v10 offset:9216\nv_permlane16_swap_b32 v52, v53\nv_smfmac_f32_16x16x64_bf16 v[36:39], v[56:59], v[148:155], v0\n" \
    "ds_read_b128 v[80:83], v10 offset:13312\nv_permlane16_swap_b32 v54, v55\nv_smfmac_f32_16x16x64_bf16 v[32:35], v[60:63], v[156:163], v0\n" \
    "buffer_store_dwordx4 v[52:55], v8, s[64:67], s87 offen offset:768 sc1\nv_mov_b64 v[40:41], 0\nv_smfmac_f32_16x16x64_bf16 v[36:39], v[60:63], v[172:179], v0\n" \
    "v_mov_b64 v[42:43], 0\nv_smfmac_f32_16x16x64_bf16 v[32:35], v[64:67], v[164:171], v0\nv_mov_b64 v[44:45], 0\nv_smfmac_f32_16x16x64_bf16 v[36:39], v[64:67], v[180:187], v0\n" \
    "s_waitcnt lgkmcnt(0)\nv_mov_b64 v[46:47], 0\nv_mfma_f32_16x16x32_bf16 v[16:19], a[28:31], v[72:75], v[16:19]\ns_nop 1\n" \
    "v_mfma_f32_16x16x32_bf16 v[24:27], v[68:71], v[72:75], v[24:27]\nv_cvt_pk_bf16_f32 v48, v32, v33\nv_cvt_pk_bf16_f32 v50, v34, v35\n" \
    "v_smfmac_f32_16x16x64_bf16 v[40:43], v[76:79], v[188:195], v0\nv_cvt_pk_bf16_f32 v49, v36, v37\nv_smfmac_f32_16x16x64_bf16 v[44:47], v[76:79], v[204:211], v0\n" \
    "v_cvt_pk_bf16_f32 v51, v38, v39\nv_smfmac_f32_16x16x64_bf16 v[40:43], v[56:59], v[196:203], v0\nv_permlane16_swap_b32 v48, v49\n" \
    "v_smfmac_f32_16x16x64_bf16 v[44:47], v[56:59], v[212:219], v0\nds_read_b128 v[56:59], v3 offset:8192\nv_permlane16_swap_b32 v50, v51\n" \
    "v_smfmac_f32_16x16x64_bf16 v[40:43], v[60:63], v[220:227], v0\nds_read_b128 v[72:75], v9 offset:16384\n" \
    "buffer_store_dwordx4 v[48:51], v8, s[64:67], s88 offen offset:768 sc1\nv_smfmac_f32_16x16x64_bf16 v[44:47], v[60:63], v[236:243], v0\n" \
    "ds_read_b128 v[60:63], v9 offset:20480\nv_smfmac_f32_16x16x64_bf16 v[40:43], v[64:67], v[228:235], v0\nv_smfmac_f32_16x16x64_bf16 v[44:47], v[64:67], v[244:251], v0\n" \
    "ds_read_b128 v[64:67], v3 offset:9216\ns_nop 0\nv_mfma_f32_16x16x32_bf16 v[20:23], a[28:31], v[80:83], v[20:23]\nds_read_b128 v[76:79], v10 offset:16384\ns_nop 0\n" \
    "v_mfma_f32_16x16x32_bf16 v[28:31], v[68:71], v[80:83], v[28:31]\nds_read_b128 v[68:71], v10 offset:20480\ns_waitcnt lgkmcnt(3)\nv_cvt_pk_bf16_f32 v52, v40, v41\n" \
    "v_mfma_f32_16x16x32_bf16 v[16:19], a[32:35], v[72:75], v[16:19]\nv_cvt_pk_bf16_f32 v54, v42, v43\nv_cvt_pk_bf16_f32 v53, v44, v45\n" \
    "v_mfma_f32_16x16x32_bf16 v[24:27], v[56:59], v[72:75], v[24:27]\nds_read_b128 v[72:75], v3 offset:10240\nv_cvt_pk_bf16_f32 v55, v46, v47\nv_permlane16_swap_b32 v52, v53\n" \
    "v_mfma_f32_16x16x32_bf16 v[20:23], a[32:35], v[60:63], v[20:23]\nds_read_b128 v[80:83], v9 offset:17408\nv_permlane16_swap_b32 v54, v55\n" \
    "v_mfma_f32_16x16x32_bf16 v[28:31], v[56:59], v[60:63], v[28:31]\nds_read_b128 v[56:59], v9 offset:21504\n" \
    "buffer_store_dwordx4 v[52:55], v8, s[64:67], s89 offen offset:768 sc1\ns_waitcnt lgkmcnt(3)\nv_mfma_f32_16x16x32_bf16 v[16:19], a[36:39], v[76:79], v[16:19]\n" \
    "buffer_load_dword v13, off, s[64:67], s42 sc1\ns_and_b32 s69, s49, 255\nv_mfma_f32_16x16x32_bf16 v[24:27], v[64:67], v[76:79], v[24:27]\n" \
    "ds_read_b128 v[60:63], v3 offset:11264\ns_lshl_b32 s69, s69, 19\nv_mfma_f32_16x16x32_bf16 v[20:23], a[36:39], v[68:71], v[20:23]\nds_read_b128 v[76:79], v10 offset:17408\n" \
    "s_add_u32 s74, s63, s69\nv_mfma_f32_16x16x32_bf16 v[28:31], v[64:67], v[68:71], v[28:31]\nds_read_b128 v[64:67], v10 offset:21504\ns_cmp_lt_u32 s49, 64\n" \
    "s_cselect_b32 s74, s74, 0x7fff0000\ns_waitcnt lgkmcnt(3)\nv_mfma_f32_16x16x32_bf16 v[16:19], a[40:43], v[80:83], v[16:19]\n" \
    "v_mfma_f32_16x16x32_bf16 v[24:27], v[72:75], v[80:83], v[24:27]\nds_read_b128 v[68:71], v3 offset:12288\ns_waitcnt vmcnt(16)\ns_barrier\n" \
    "v_mfma_f32_16x16x32_bf16 v[20:23], a[40:43], v[56:59], v[20:23]\nds_read_b128 v[80:83], v9 offset:40960\nv_mfma_f32_16x16x32_bf16 v[28:31], v[72:75], v[56:59], v[28:31]\n" \
    "ds_read_b128 v[56:59], v9 offset:45056\ns_add_u32 m0, s41, 0\ns_waitcnt lgkmcnt(3)\nv_mfma_f32_16x16x32_bf16 v[16:19], a[44:47], v[76:79], v[16:19]\n" \
    "s_xor_b32 s85, s62, 1536\ns_add_u32 s85, s85, s40\nv_mfma_f32_16x16x32_bf16 v[24:27], v[60:63], v[76:79], v[24:27]\nds_read_b128 v[72:75], v3 offset:13312\n" \
    "buffer_load_dwordx4 v11, s[64:67], s85 offen sc1 lds\nv_mfma_f32_16x16x32_bf16 v[20:23], a[44:47], v[64:67], v[20:23]\nds_read_b128 v[76:79], v10 offset:40960\n" \
    "buffer_store_dwordx4 v[252:255], v2, s[64:67], s74 offen offset:0 sc1\nv_mfma_f32_16x16x32_bf16 v[28:31], v[60:63], v[64:67], v[28:31]\n" \
    "ds_read_b128 v[60:63], v10 offset:45056\ns_waitcnt lgkmcnt(3)\nv_mfma_f32_16x16x32_bf16 v[16:19], a[48:51], v[80:83], v[16:19]\n" \
    "buffer_load_dwordx4 v12, s[64:67], s85 offen offset:1024 sc1 lds\ns_add_u32 m0, s41, 8192\nv_mfma_f32_16x16x32_bf16 v[24:27], v[68:71], v[80:83], v[24:27]\n" \
    "ds_read_b128 v[64:67], v3 offset:14336\ns_xor_b32 s85, s62, 1792\nv_mfma_f32_16x16x32_bf16 v[20:23], a[48:51], v[56:59], v[20:23]\nds_read_b128 v[80:83], v9 offset:41984\n" \
    "s_add_u32 s85, s85, s40\nv_mfma_f32_16x16x32_bf16 v[28:31], v[68:71], v[56:59], v[28:31]\nds_read_b128 v[56:59], v9 offset:46080\n" \
    "buffer_load_dwordx4 v11, s[64:67], s85 offen sc1 lds\ns_waitcnt lgkmcnt(3)\nv_mfma_f32_16x16x32_bf16 v[16:19], a[52:55], v[76:79], v[16:19]\n" \
    "buffer_store_dwordx4 v[252:255], v2, s[64:67], s74 offen offset:1024 sc1\nv_mfma_f32_16x16x32_bf16 v[24:27], v[72:75], v[76:79], v[24:27]\n" \
    "ds_read_b128 v[68:71], v3 offset:15360\nv_mfma_f32_16x16x32_bf16 v[20:23], a[52:55], v[60:63], v[20:23]\nds_read_b128 v[76:79], v10 offset:41984\n" \
    "buffer_load_dwordx4 v12, s[64:67], s85 offen offset:1024 sc1 lds\nv_mfma_f32_16x16x32_bf16 v[28:31], v[72:75], v[60:63], v[28:31]\nds_read_b128 v[60:63], v10 offset:46080\n" \
    "s_waitcnt lgkmcnt(3)\nv_mfma_f32_16x16x32_bf16 v[16:19], a[56:59], v[80:83], v[16:19]\nbuffer_store_dwordx4 v[252:255], v7, s[64:67], s82 offen sc1\n" \
    "s_add_u32 s82, s82, 0x10000\nv_mfma_f32_16x16x32_bf16 v[24:27], v[64:67], v[80:83], v[24:27]\nds_read_b128 v[72:75], v3 offset:16384\n" \
    "v_mfma_f32_16x16x32_bf16 v[20:23], a[56:59], v[56:59], v[20:23]\nds_read_b128 v[80:83], v9 offset:24576\nv_mfma_f32_16x16x32_bf16 v[28:31], v[64:67], v[56:59], v[28:31]\n" \
    "ds_read_b128 v[56:59], v9 offset:28672\ns_waitcnt lgkmcnt(3)\nv_mfma_f32_16x16x32_bf16 v[16:19], a[60:63], v[76:79], v[16:19]\n" \
    "buffer_store_dwordx4 v[252:255], v7, s[64:67], s82 offen sc1\ns_add_u32 s82, s82, 0x10000\nv_mfma_f32_16x16x32_bf16 v[24:27], v[68:71], v[76:79], v[24:27]\n" \
    "ds_read_b128 v[64:67], v3 offset:17408\nv_mfma_f32_16x16x32_bf16 v[20:23], a[60:63], v[60:63], v[20:23]\nds_read_b128 v[76:79], v10 offset:24576\n" \
    "v_mfma_f32_16x16x32_bf16 v[28:31], v[68:71], v[60:63], v[28:31]\nds_read_b128 v[60:63], v10 offset:28672\ns_waitcnt lgkmcnt(3)\n" \
    "v_mfma_f32_16x16x32_bf16 v[16:19], a[64:67], v[80:83], v[16:19]\nbuffer_store_dwordx4 v[252:255], v7, s[64:67], s82 offen sc1\ns_add_u32 s82, s82, 0x10000\n" \
    "v_mfma_f32_16x16x32_bf16 v[24:27], v[72:75], v[80:83], v[24:27]\nds_read_b128 v[68:71], v3 offset:18432\nv_mfma_f32_16x16x32_bf16 v[20:23], a[64:67], v[56:59], v[20:23]\n" \
    "ds_read_b128 v[80:83], v9 offset:25600\nv_mfma_f32_16x16x32_bf16 v[28:31], v[72:75], v[56:59], v[28:31]\nds_read_b128 v[56:59], v9 offset:29696\ns_waitcnt lgkmcnt(3)\n" \
    "v_mfma_f32_16x16x32_bf16 v[16:19], a[68:71], v[76:79], v[16:19]\nbuffer_store_dwordx4 v[252:255], v7, s[64:67], s82 offen sc1\ns_add_u32 s82, s82, 0x10000\n" \
    "v_mfma_f32_16x16x32_bf16 v[24:27], v[64:67], v[76:79], v[24:27]\nds_read_b128 v[72:75], v3 offset:19456\ns_add_u32 s49, s49, 2\n" \
    "v_mfma_f32_16x16x32_bf16 v[20:23], a[68:71], v[60:63], v[20:23]\nds_read_b128 v[76:79], v10 offset:25600\ns_and_b32 s69, s35, 15\n" \
    "v_mfma_f32_16x16x32_bf16 v[28:31], v[64:67], v[60:63], v[28:31]\nds_read_b128 v[60:63], v10 offset:29696\ns_lshl_b32 s69, s69, 17\ns_waitcnt lgkmcnt(3)\n" \
    "v_mfma_f32_16x16x32_bf16 v[16:19], a[72:75], v[80:83], v[16:19]\ns_add_u32 s81, s37, s69\ns_lshr_b32 s69, s81, 8\n" \
    "v_mfma_f32_16x16x32_bf16 v[24:27], v[68:71], v[80:83], v[24:27]\nds_read_b128 v[64:67], v3 offset:20480\ns_and_b32 s69, s69, 0x2000\n" \
    "v_mfma_f32_16x16x32_bf16 v[20:23], a[72:75], v[56:59], v[20:23]\nds_read_b128 v[80:83], v9 offset:32768\ns_xor_b32 s81, s81, s69\n" \
    "v_mfma_f32_16x16x32_bf16 v[28:31], v[68:71], v[56:59], v[28:31]\nds_read_b128 v[56:59], v9 offset:36864\ns_mov_b32 s68, 0\ns_waitcnt lgkmcnt(3)\n" \
    "v_mfma_f32_16x16x32_bf16 v[16:19], a[76:79], v[76:79], v[16:19]\ns_waitcnt vmcnt(11)\nv_mov_b32 v15, s50\ns_mov_b64 exec, 1\n" \
    "buffer_store_dword v15, off, s[64:67], s48 sc1\ns_mov_b64 exec, -1\nv_mfma_f32_16x16x32_bf16 v[24:27], v[72:75], v[76:79], v[24:27]\n" \
    "ds_read_b128 v[68:71], v3 offset:21504\nv_mfma_f32_16x16x32_bf16 v[20:23], a[76:79], v[60:63], v[20:23]\nds_read_b128 v[76:79], v10 offset:32768\n" \
    "v_mfma_f32_16x16x32_bf16 v[28:31], v[72:75], v[60:63], v[28:31]\nds_read_b128 v[60:63], v10 offset:36864\ns_waitcnt lgkmcnt(3)\n" \
    "v_mfma_f32_16x16x32_bf16 v[16:19], a[80:83], v[80:83], v[16:19]\nv_mfma_f32_16x16x32_bf16 v[24:27], v[64:67], v[80:83], v[24:27]\nds_read_b128 v[72:75], v3 offset:22528\n" \
    "v_mfma_f32_16x16x32_bf16 v[20:23], a[80:83], v[56:59], v[20:23]\nds_read_b128 v[80:83], v9 offset:33792\nv_mfma_f32_16x16x32_bf16 v[28:31], v[64:67], v[56:59], v[28:31]\n" \
    "ds_read_b128 v[56:59], v9 offset:37888\ns_waitcnt lgkmcnt(3)\nv_mfma_f32_16x16x32_bf16 v[16:19], a[84:87], v[76:79], v[16:19]\n" \
    "v_mfma_f32_16x16x32_bf16 v[24:27], v[68:71], v[76:79], v[24:27]\nds_read_b128 v[64:67], v3 offset:23552\nv_mfma_f32_16x16x32_bf16 v[20:23], a[84:87], v[60:63], v[20:23]\n" \
    "ds_read_b128 v[76:79], v10 offset:33792\nv_mfma_f32_16x16x32_bf16 v[28:31], v[68:71], v[60:63], v[28:31]\nds_read_b128 v[60:63], v10 offset:37888\ns_waitcnt lgkmcnt(3)\n" \
    "v_mfma_f32_16x16x32_bf16 v[16:19], a[88:91], v[80:83], v[16:19]\nv_mfma_f32_16x16x32_bf16 v[24:27], v[72:75], v[80:83], v[24:27]\ns_waitcnt vmcnt(5)\ns_barrier\n" \
    "v_mfma_f32_16x16x32_bf16 v[20:23], a[88:91], v[56:59], v[20:23]\nds_read_b128 v[68:71], v9 offset:0\nv_mfma_f32_16x16x32_bf16 v[28:31], v[72:75], v[56:59], v[28:31]\n" \
    "ds_read_b128 v[56:59], v9 offset:4096\ns_add_u32 m0, s41, 16384\ns_waitcnt lgkmcnt(2)\nv_mfma_f32_16x16x32_bf16 v[16:19], a[92:95], v[76:79], v[16:19]\n" \
    "s_xor_b32 s85, s62, 512\ns_add_u32 s85, s85, s84\nv_mfma_f32_16x16x32_bf16 v[24:27], v[64:67], v[76:79], v[24:27]\nbuffer_load_dwordx4 v11, s[64:67], s85 offen sc1 lds\n" \
    "v_mfma_f32_16x16x32_bf16 v[20:23], a[92:95], v[60:63], v[20:23]\nds_read_b128 v[72:75], v10 offset:0\nv_mfma_f32_16x16x32_bf16 v[28:31], v[64:67], v[60:63], v[28:31]\n" \
    "ds_read_b128 v[60:63], v10 offset:4096\ns_waitcnt lgkmcnt(2)\nv_mfma_f32_16x16x32_bf16 v[16:19], a[96:99], v[68:71], v[16:19]\n" \
    "buffer_load_dwordx4 v12, s[64:67], s85 offen offset:1024 sc1 lds\ns_add_u32 m0, s41, 40960\nv_mfma_f32_16x16x32_bf16 v[24:27], a[104:107], v[68:71], v[24:27]\n" \
    "s_xor_b32 s85, s62, 768\ns_add_u32 s85, s85, s84\nv_mfma_f32_16x16x32_bf16 v[20:23], a[96:99], v[56:59], v[20:23]\nds_read_b128 v[64:67], v9 offset:1024\n" \
    "buffer_load_dwordx4 v11, s[64:67], s85 offen sc1 lds\nv_mfma_f32_16x16x32_bf16 v[28:31], a[104:107], v[56:59], v[28:31]\nds_read_b128 v[56:59], v9 offset:5120\n" \
    "s_waitcnt lgkmcnt(2)\nv_mfma_f32_16x16x32_bf16 v[16:19], a[100:103], v[72:75], v[16:19]\nv_mfma_f32_16x16x32_bf16 v[24:27], a[108:111], v[72:75], v[24:27]\n" \
    "buffer_load_dwordx4 v12, s[64:67], s85 offen offset:1024 sc1 lds\nv_mfma_f32_16x16x32_bf16 v[20:23], a[100:103], v[60:63], v[20:23]\n" \
    "ds_read_b128 v[68:71], v10 offset:1024\nv_mfma_f32_16x16x32_bf16 v[28:31], a[108:111], v[60:63], v[28:31]\nds_read_b128 v[60:63], v10 offset:5120\ns_waitcnt lgkmcnt(2)\n" \
    "v_mfma_f32_16x16x32_bf16 v[16:19], a[112:115], v[64:67], v[16:19]\nv_mfma_f32_16x16x32_bf16 v[24:27], a[120:123], v[64:67], v[24:27]\n" \
    "v_mfma_f32_16x16x32_bf16 v[20:23], a[112:115], v[56:59], v[20:23]\nds_read_b128 v[64:67], v9 offset:8192\n" \
    "v_mfma_f32_16x16x32_bf16 v[28:31], a[120:123], v[56:59], v[28:31]\nds_read_b128 v[56:59], v9 offset:12288\ns_waitcnt lgkmcnt(2)\n" \
    "v_mfma_f32_16x16x32_bf16 v[16:19], a[116:119], v[68:71], v[16:19]\nv_mfma_f32_16x16x32_bf16 v[24:27], a[124:127], v[68:71], v[24:27]\n" \
    "v_mfma_f32_16x16x32_bf16 v[20:23], a[116:119], v[60:63], v[20:23]\nds_read_b128 v[68:71], v10 offset:8192\n" \
    "v_mfma_f32_16x16x32_bf16 v[28:31], a[124:127], v[60:63], v[28:31]\nds_read_b128 v[60:63], v10 offset:12288\ns_waitcnt lgkmcnt(2)\n" \
    "v_mfma_f32_16x16x32_bf16 v[16:19], a[128:131], v[64:67], v[16:19]\nv_readfirstlane_b32 s74, v13\nv_mfma_f32_16x16x32_bf16 v[24:27], a[136:139], v[64:67], v[24:27]\n" \
    "s_and_b32 s75, s74, 127\ns_lshr_b32 s69, s74, 8\nv_mfma_f32_16x16x32_bf16 v[20:23], a[128:131], v[56:59], v[20:23]\nds_read_b128 v[64:67], v9 offset:9216\n" \
    "s_sub_u32 s69, s69, s80\nv_mfma_f32_16x16x32_bf16 v[28:31], a[136:139], v[56:59], v[28:31]\nds_read_b128 v[56:59], v9 offset:13312\ns_bfe_i32 s69, s69, 0x180000\n" \
    "s_waitcnt lgkmcnt(2)\nv_mfma_f32_16x16x32_bf16 v[16:19], a[132:135], v[68:71], v[16:19]\nds_read_b128 v[72:75], v10 offset:9216\ns_cmp_eq_u32 s75, 0\n" \
    "s_cselect_b32 s69, 0, s69\nv_mfma_f32_16x16x32_bf16 v[24:27], a[140:143], v[68:71], v[24:27]\nds_read_b128 v[68:71], v10 offset:13312\ns_add_u32 s80, s80, s69\n" \
    "v_mfma_f32_16x16x32_bf16 v[20:23], a[132:135], v[60:63], v[20:23]\ns_lshl_b32 s74, s74, 5\ns_and_b32 s74, s74, 0x1000\n" \
    "v_mfma_f32_16x16x32_bf16 v[28:31], a[140:143], v[60:63], v[28:31]\ns_andn2_b32 s81, s81, 0x1000\ns_or_b32 s81, s81, s74\ns_waitcnt lgkmcnt(2)\n" \
    "v_mfma_f32_16x16x32_bf16 v[16:19], a[144:147], v[64:67], v[16:19]\nv_mfma_f32_16x16x32_bf16 v[24:27], a[152:155], v[64:67], v[24:27]\ns_waitcnt lgkmcnt(0)\ns_barrier\n" \
    "v_mfma_f32_16x16x32_bf16 v[16:19], a[148:151], v[72:75], v[16:19]\ns_add_u32 m0, s41, 0\ns_xor_b32 s85, s62, 0\n" \
    "v_mfma_f32_16x16x32_bf16 v[24:27], a[156:159], v[72:75], v[24:27]\ns_add_u32 s85, s85, s84\nbuffer_load_dwordx4 v11, s[64:67], s85 offen sc1 lds\n" \
    "v_mfma_f32_16x16x32_bf16 v[20:23], a[144:147], v[56:59], v[20:23]\ns_nop 1\nv_cvt_pk_bf16_f32 v32, v16, v17\nv_cvt_pk_bf16_f32 v33, v18, v19\n" \
    "v_mfma_f32_16x16x32_bf16 v[28:31], a[152:155], v[56:59], v[28:31]\nv_cvt_pk_bf16_f32 v34, v24, v25\nv_cvt_pk_bf16_f32 v35, v26, v27\n" \
    "v_mfma_f32_16x16x32_bf16 v[20:23], a[148:151], v[68:71], v[20:23]\nbuffer_load_dwordx4 v12, s[64:67], s85 offen offset:1024 sc1 lds\ns_add_u32 m0, s41, 8192\n" \
    "v_mfma_f32_16x16x32_bf16 v[28:31], a[156:159], v[68:71], v[28:31]\ns_xor_b32 s85, s62, 256\ns_add_u32 s85, s85, s84\nds_write_b128 v14, v[32:35] offset:0\n" \
    "buffer_load_dwordx4 v11, s[64:67], s85 offen sc1 lds\nbuffer_load_dwordx4 v12, s[64:67], s85 offen offset:1024 sc1 lds\nv_cvt_pk_bf16_f32 v36, v20, v21\n" \
    "v_cvt_pk_bf16_f32 v37, v22, v23\ns_mov_b32 s40, s84\nv_cvt_pk_bf16_f32 v38, v28, v29\nv_cvt_pk_bf16_f32 v39, v30, v31\nds_write_b128 v14, v[36:39] offset:4096\n.Lv4_%=:\n" \
    "s_lshr_b32 s69, s35, 1\ns_cmp_eq_u32 s69, 5\ns_cbranch_scc1 .Lv12_%=\n.Lv13_%=:\ns_cmp_lt_u32 s75, s35\ns_cbranch_scc1 .Lv14_%=\ns_cmp_eq_u32 s36, 1\n" \
    "s_cbranch_scc1 .Lv9_%=\ns_cmp_eq_u32 s36, 2\ns_cbranch_scc1 .Lv10_%=\ns_cmp_eq_u32 s36, 3\ns_cbranch_scc1 .Lv11_%=\n.Lv8_%=:\nds_read_b128 v[24:27], v3 offset:0\n" \
    "ds_read_b128 v[28:31], v3 offset:1024\nds_read_b128 v[32:35], v3 offset:8192\nds_read_b128 v[36:39], v3 offset:9216\nds_read_b128 v[40:43], v3 offset:16384\n" \
    "ds_read_b128 v[44:47], v3 offset:17408\nds_read_b128 v[48:51], v3 offset:2048\nds_read_b128 v[52:55], v3 offset:3072\nds_read_b128 v[56:59], v3 offset:4096\n" \
    "ds_read_b128 v[60:63], v3 offset:5120\nds_read_b128 v[64:67], v3 offset:6144\nds_read_b128 v[68:71], v3 offset:7168\n.Lv16_%=:\ns_memrealtime s[72:73]\n" \
    "s_waitcnt lgkmcnt(0)\ns_mov_b32 s68, 0\ns_sub_u32 s76, s80, s45\ns_sub_u32 s69, s72, s76\ns_cmp_lt_i32 s69, -8\ns_cbranch_scc1 .Lv19_%=\n.Lv17_%=:\n" \
    "s_xor_b32 s90, s81, 0x800\n.Lv21_%=:\ns_memrealtime s[72:73]\ns_waitcnt lgkmcnt(0)\ns_sub_u32 s72, s72, s80\ns_cmp_lt_i32 s72, 0\ns_cbranch_scc1 .Lv21_%=\n" \
    "buffer_load_dwordx4 v[72:75], v2, s[64:67], s81 offen offset:0 sc1\ns_nop 7\nbuffer_load_dwordx4 v[76:79], v2, s[64:67], s81 offen offset:1024 sc1\ns_nop 7\n" \
    "buffer_load_dwordx4 v[80:83], v2, s[64:67], s90 offen offset:0 sc1\ns_nop 7\nbuffer_load_dwordx4 v[84:87], v2, s[64:67], s90 offen offset:1024 sc1\nv_mov_b64 v[16:17], 0\n" \
    "v_mov_b64 v[18:19], 0\nv_mov_b64 v[20:21], 0\nv_mov_b64 v[22:23], 0\ns_waitcnt vmcnt(3)\ns_nop 0\nv_smfmac_f32_16x16x64_bf16 v[20:23], v[72:75], v[24:31], v0\n" \
    "ds_write_b128 v1, v[72:75] offset:24576\nv_max3_u32 v252, v72, v73, v74\nv_smfmac_f32_16x16x64_bf16 v[16:19], v[72:75], a[0:7], v0\ns_waitcnt vmcnt(2)\n" \
    "ds_write_b128 v1, v[76:79] offset:28672\nv_max3_u32 v252, v252, v75, v76\nv_max3_u32 v252, v252, v77, v78\nv_cmp_u_f32 vcc, v252, v79\n" \
    "v_smfmac_f32_16x16x64_bf16 v[20:23], v[76:79], v[32:39], v0\ns_cbranch_vccnz .Lv27_%=\n.Lv23_%=:\nv_smfmac_f32_16x16x64_bf16 v[16:19], v[76:79], a[32:39], v0\n" \
    "ds_read_b128 v[24:27], v3 offset:10240\nds_read_b128 v[28:31], v3 offset:11264\ns_waitcnt vmcnt(1)\nv_max3_u32 v252, v80, v81, v82\nv_cmp_u_f32 vcc, v252, v83\ns_nop 0\n" \
    "s_cbranch_vccnz .Lv28_%=\n.Lv24_%=:\nv_smfmac_f32_16x16x64_bf16 v[20:23], v[80:83], v[40:47], v0\nds_write_b128 v1, v[80:83] offset:32768\n" \
    "ds_read_b128 v[36:39], v3 offset:13312\ns_waitcnt lgkmcnt(4)\ns_barrier\nds_read_b128 v[88:91], v1 offset:25600\nds_read_b128 v[40:43], v1 offset:26624\n" \
    "v_smfmac_f32_16x16x64_bf16 v[16:19], v[80:83], a[64:71], v0\nds_read_b128 v[32:35], v3 offset:12288\nds_read_b128 v[44:47], v1 offset:27648\ns_waitcnt vmcnt(0)\n" \
    "v_max3_u32 v252, v84, v85, v86\nv_cmp_u_f32 vcc, v252, v87\ns_nop 0\ns_cbranch_vccnz .Lv29_%=\n.Lv25_%=:\nv_smfmac_f32_16x16x64_bf16 v[20:23], v[84:87], a[104:111], v0\n" \
    "ds_write_b128 v1, v[84:87] offset:36864\nv_smfmac_f32_16x16x64_bf16 v[16:19], v[84:87], a[96:103], v0\nds_read_b128 v[72:75], v1 offset:29696\ns_waitcnt lgkmcnt(5)\n" \
    "v_smfmac_f32_16x16x64_bf16 v[20:23], v[88:91], v[48:55], v0\nds_read_b128 v[48:51], v1 offset:30720\nv_smfmac_f32_16x16x64_bf16 v[16:19], v[88:91], a[8:15], v0\n" \
    "ds_read_b128 v[52:55], v1 offset:31744\ns_waitcnt lgkmcnt(6)\nv_smfmac_f32_16x16x64_bf16 v[20:23], v[40:43], v[56:63], v0\nds_read_b128 v[56:59], v3 offset:14336\n" \
    "s_waitcnt lgkmcnt(4)\ns_barrier\nv_smfmac_f32_16x16x64_bf16 v[16:19], v[40:43], a[16:23], v0\nds_read_b128 v[60:63], v3 offset:15360\n" \
    "v_smfmac_f32_16x16x64_bf16 v[20:23], v[44:47], v[64:71], v0\nds_read_b128 v[40:43], v1 offset:33792\nv_smfmac_f32_16x16x64_bf16 v[16:19], v[44:47], a[24:31], v0\n" \
    "ds_read_b128 v[64:67], v3 offset:18432\ns_waitcnt lgkmcnt(6)\nv_smfmac_f32_16x16x64_bf16 v[20:23], v[72:75], v[24:31], v0\nds_read_b128 v[68:71], v3 offset:19456\n" \
    "v_smfmac_f32_16x16x64_bf16 v[16:19], v[72:75], a[40:47], v0\nds_read_b128 v[44:47], v1 offset:34816\ns_waitcnt lgkmcnt(7)\n" \
    "v_smfmac_f32_16x16x64_bf16 v[20:23], v[48:51], v[32:39], v0\nds_read_b128 v[24:27], v3 offset:20480\nv_smfmac_f32_16x16x64_bf16 v[16:19], v[48:51], a[48:55], v0\n" \
    "ds_read_b128 v[28:31], v3 offset:21504\ns_waitcnt lgkmcnt(6)\nv_smfmac_f32_16x16x64_bf16 v[20:23], v[52:55], v[56:63], v0\nds_read_b128 v[48:51], v1 offset:35840\n" \
    "v_smfmac_f32_16x16x64_bf16 v[16:19], v[52:55], a[56:63], v0\nds_read_b128 v[32:35], v3 offset:22528\ns_waitcnt lgkmcnt(5)\n" \
    "v_smfmac_f32_16x16x64_bf16 v[20:23], v[40:43], v[64:71], v0\nds_read_b128 v[36:39], v3 offset:23552\nv_smfmac_f32_16x16x64_bf16 v[16:19], v[40:43], a[72:79], v0\n" \
    "ds_read_b128 v[40:43], v1 offset:37888\ns_waitcnt lgkmcnt(4)\nv_smfmac_f32_16x16x64_bf16 v[20:23], v[44:47], v[24:31], v0\nds_read_b128 v[24:27], v1 offset:38912\n" \
    "v_smfmac_f32_16x16x64_bf16 v[16:19], v[44:47], a[80:87], v0\nds_read_b128 v[28:31], v1 offset:39936\ns_waitcnt lgkmcnt(3)\n" \
    "v_smfmac_f32_16x16x64_bf16 v[20:23], v[48:51], v[32:39], v0\ns_waitcnt lgkmcnt(2)\nv_smfmac_f32_16x16x64_bf16 v[20:23], v[40:43], a[120:127], v0\ns_waitcnt lgkmcnt(1)\n" \
    "v_smfmac_f32_16x16x64_bf16 v[20:23], v[24:27], a[136:143], v0\ns_waitcnt lgkmcnt(0)\nv_smfmac_f32_16x16x64_bf16 v[20:23], v[28:31], a[152:159], v0\ns_nop 2\n" \
    "v_smfmac_f32_16x16x64_bf16 v[16:19], v[48:51], a[88:95], v0\ns_nop 2\nv_smfmac_f32_16x16x64_bf16 v[16:19], v[40:43], a[112:119], v0\nv_permlane32_swap_b32 v20, v22\n" \
    "v_smfmac_f32_16x16x64_bf16 v[16:19], v[24:27], a[128:135], v0\nv_permlane32_swap_b32 v21, v23\nv_smfmac_f32_16x16x64_bf16 v[16:19], v[28:31], a[144:151], v0\n" \
    "v_add_f32 v20, v20, v22\nv_add_f32 v21, v21, v23\nv_cvt_pk_bf16_f32 v20, v20, v21\nds_write_b16 v4, v20 offset:8\nds_write_b16_d16_hi v4, v20 offset:24\n" \
    "v_mov_b32 v15, v15\nv_mov_b32 v15, v15\nv_mov_b32 v15, v15\nv_permlane32_swap_b32 v16, v18\nv_permlane32_swap_b32 v17, v19\nv_pk_add_f32 v[16:17], v[16:17], v[18:19]\n" \
    "v_cvt_pk_bf16_f32 v16, v16, v17\nds_write_b16 v4, v16 offset:0\nds_write_b16_d16_hi v4, v16 offset:16\ns_branch .Lv7_%=\n.Lv9_%=:\nds_read_b128 v[24:27], v3 offset:2048\n" \
    "ds_read_b128 v[28:31], v3 offset:3072\nds_read_b128 v[32:35], v3 offset:10240\nds_read_b128 v[36:39], v3 offset:11264\nds_read_b128 v[40:43], v3 offset:18432\n" \
    "ds_read_b128 v[44:47], v3 offset:19456\nds_read_b128 v[48:51], v3 offset:0\nds_read_b128 v[52:55], v3 offset:1024\nds_read_b128 v[56:59], v3 offset:4096\n" \
    "ds_read_b128 v[60:63], v3 offset:5120\nds_read_b128 v[64:67], v3 offset:6144\nds_read_b128 v[68:71], v3 offset:7168\n.Lv32_%=:\ns_memrealtime s[72:73]\n" \
    "s_waitcnt lgkmcnt(0)\ns_mov_b32 s68, 0\ns_sub_u32 s76, s80, s45\ns_sub_u32 s69, s72, s76\ns_cmp_lt_i32 s69, -8\ns_cbranch_scc1 .Lv35_%=\n.Lv33_%=:\n" \
    "s_xor_b32 s90, s81, 0x800\n.Lv37_%=:\ns_memrealtime s[72:73]\ns_waitcnt lgkmcnt(0)\ns_sub_u32 s72, s72, s80\ns_cmp_lt_i32 s72, 0\ns_cbranch_scc1 .Lv37_%=\n" \
    "buffer_load_dwordx4 v[72:75], v2, s[64:67], s81 offen offset:0 sc1\ns_nop 7\nbuffer_load_dwordx4 v[76:79], v2, s[64:67], s81 offen offset:1024 sc1\ns_nop 7\n" \
    "buffer_load_dwordx4 v[80:83], v2, s[64:67], s90 offen offset:0 sc1\ns_nop 7\nbuffer_load_dwordx4 v[84:87], v2, s[64:67], s90 offen offset:1024 sc1\nv_mov_b64 v[16:17], 0\n" \
    "v_mov_b64 v[18:19], 0\nv_mov_b64 v[20:21], 0\nv_mov_b64 v[22:23], 0\ns_waitcnt vmcnt(3)\ns_nop 0\nv_smfmac_f32_16x16x64_bf16 v[20:23], v[72:75], v[24:31], v0\n" \
    "ds_write_b128 v1, v[72:75] offset:25600\nv_max3_u32 v252, v72, v73, v74\nv_smfmac_f32_16x16x64_bf16 v[16:19], v[72:75], a[8:15], v0\ns_waitcnt vmcnt(2)\n" \
    "ds_write_b128 v1, v[76:79] offset:29696\nv_max3_u32 v252, v252, v75, v76\nv_max3_u32 v252, v252, v77, v78\nv_cmp_u_f32 vcc, v252, v79\n" \
    "v_smfmac_f32_16x16x64_bf16 v[20:23], v[76:79], v[32:39], v0\ns_cbranch_vccnz .Lv43_%=\n.Lv39_%=:\nv_smfmac_f32_16x16x64_bf16 v[16:19], v[76:79], a[40:47], v0\n" \
    "ds_read_b128 v[24:27], v3 offset:8192\nds_read_b128 v[28:31], v3 offset:9216\ns_waitcnt vmcnt(1)\nv_max3_u32 v252, v80, v81, v82\nv_cmp_u_f32 vcc, v252, v83\ns_nop 0\n" \
    "s_cbranch_vccnz .Lv44_%=\n.Lv40_%=:\nv_smfmac_f32_16x16x64_bf16 v[20:23], v[80:83], v[40:47], v0\nds_write_b128 v1, v[80:83] offset:33792\n" \
    "ds_read_b128 v[36:39], v3 offset:13312\ns_waitcnt lgkmcnt(4)\ns_barrier\nds_read_b128 v[88:91], v1 offset:24576\nds_read_b128 v[40:43], v1 offset:26624\n" \
    "v_smfmac_f32_16x16x64_bf16 v[16:19], v[80:83], a[72:79], v0\nds_read_b128 v[32:35], v3 offset:12288\nds_read_b128 v[44:47], v1 offset:27648\ns_waitcnt vmcnt(0)\n" \
    "v_max3_u32 v252, v84, v85, v86\nv_cmp_u_f32 vcc, v252, v87\ns_nop 0\ns_cbranch_vccnz .Lv45_%=\n.Lv41_%=:\nv_smfmac_f32_16x16x64_bf16 v[20:23], v[84:87], a[120:127], v0\n" \
    "ds_write_b128 v1, v[84:87] offset:37888\nv_smfmac_f32_16x16x64_bf16 v[16:19], v[84:87], a[112:119], v0\nds_read_b128 v[72:75], v1 offset:28672\ns_waitcnt lgkmcnt(5)\n" \
    "v_smfmac_f32_16x16x64_bf16 v[20:23], v[88:91], v[48:55], v0\nds_read_b128 v[48:51], v1 offset:30720\nv_smfmac_f32_16x16x64_bf16 v[16:19], v[88:91], a[0:7], v0\n" \
    "ds_read_b128 v[52:55], v1 offset:31744\ns_waitcnt lgkmcnt(6)\nv_smfmac_f32_16x16x64_bf16 v[20:23], v[40:43], v[56:63], v0\nds_read_b128 v[56:59], v3 offset:14336\n" \
    "s_waitcnt lgkmcnt(4)\ns_barrier\nv_smfmac_f32_16x16x64_bf16 v[16:19], v[40:43], a[16:23], v0\nds_read_b128 v[60:63], v3 offset:15360\n" \
    "v_smfmac_f32_16x16x64_bf16 v[20:23], v[44:47], v[64:71], v0\nds_read_b128 v[40:43], v1 offset:32768\nv_smfmac_f32_16x16x64_bf16 v[16:19], v[44:47], a[24:31], v0\n" \
    "ds_read_b128 v[64:67], v3 offset:16384\ns_waitcnt lgkmcnt(6)\nv_smfmac_f32_16x16x64_bf16 v[20:23], v[72:75], v[24:31], v0\nds_read_b128 v[68:71], v3 offset:17408\n" \
    "v_smfmac_f32_16x16x64_bf16 v[16:19], v[72:75], a[32:39], v0\nds_read_b128 v[44:47], v1 offset:34816\ns_waitcnt lgkmcnt(7)\n" \
    "v_smfmac_f32_16x16x64_bf16 v[20:23], v[48:51], v[32:39], v0\nds_read_b128 v[24:27], v3 offset:20480\nv_smfmac_f32_16x16x64_bf16 v[16:19], v[48:51], a[48:55], v0\n" \
    "ds_read_b128 v[28:31], v3 offset:21504\ns_waitcnt lgkmcnt(6)\nv_smfmac_f32_16x16x64_bf16 v[20:23], v[52:55], v[56:63], v0\nds_read_b128 v[48:51], v1 offset:35840\n" \
    "v_smfmac_f32_16x16x64_bf16 v[16:19], v[52:55], a[56:63], v0\nds_read_b128 v[32:35], v3 offset:22528\ns_waitcnt lgkmcnt(5)\n" \
    "v_smfmac_f32_16x16x64_bf16 v[20:23], v[40:43], v[64:71], v0\nds_read_b128 v[36:39], v3 offset:23552\nv_smfmac_f32_16x16x64_bf16 v[16:19], v[40:43], a[64:71], v0\n" \
    "ds_read_b128 v[40:43], v1 offset:36864\ns_waitcnt lgkmcnt(4)\nv_smfmac_f32_16x16x64_bf16 v[20:23], v[44:47], v[24:31], v0\nds_read_b128 v[24:27], v1 offset:38912\n" \
    "v_smfmac_f32_16x16x64_bf16 v[16:19], v[44:47], a[80:87], v0\nds_read_b128 v[28:31], v1 offset:39936\ns_waitcnt lgkmcnt(3)\n" \
    "v_smfmac_f32_16x16x64_bf16 v[20:23], v[48:51], v[32:39], v0\ns_waitcnt lgkmcnt(2)\nv_smfmac_f32_16x16x64_bf16 v[20:23], v[40:43], a[104:111], v0\ns_waitcnt lgkmcnt(1)\n" \
    "v_smfmac_f32_16x16x64_bf16 v[20:23], v[24:27], a[136:143], v0\ns_waitcnt lgkmcnt(0)\nv_smfmac_f32_16x16x64_bf16 v[20:23], v[28:31], a[152:159], v0\ns_nop 2\n" \
    "v_smfmac_f32_16x16x64_bf16 v[16:19], v[48:51], a[88:95], v0\ns_nop 2\nv_smfmac_f32_16x16x64_bf16 v[16:19], v[40:43], a[96:103], v0\nv_permlane32_swap_b32 v20, v22\n" \
    "v_smfmac_f32_16x16x64_bf16 v[16:19], v[24:27], a[128:135], v0\nv_permlane32_swap_b32 v21, v23\nv_smfmac_f32_16x16x64_bf16 v[16:19], v[28:31], a[144:151], v0\n" \
    "v_add_f32 v20, v20, v22\nv_add_f32 v21, v21, v23\nv_cvt_pk_bf16_f32 v20, v20, v21\nds_write_b16 v4, v20 offset:8\nds_write_b16_d16_hi v4, v20 offset:24\n" \
    "v_mov_b32 v15, v15\nv_mov_b32 v15, v15\nv_mov_b32 v15, v15\nv_permlane32_swap_b32 v16, v18\nv_permlane32_swap_b32 v17, v19\nv_pk_add_f32 v[16:17], v[16:17], v[18:19]\n" \
    "v_cvt_pk_bf16_f32 v16, v16, v17\nds_write_b16 v4, v16 offset:0\nds_write_b16_d16_hi v4, v16 offset:16\ns_branch .Lv7_%=\n.Lv10_%=:\nds_read_b128 v[24:27], v3 offset:4096\n" \
    "ds_read_b128 v[28:31], v3 offset:5120\nds_read_b128 v[32:35], v3 offset:12288\nds_read_b128 v[36:39], v3 offset:13312\nds_read_b128 v[40:43], v3 offset:20480\n" \
    "ds_read_b128 v[44:47], v3 offset:21504\nds_read_b128 v[48:51], v3 offset:0\nds_read_b128 v[52:55], v3 offset:1024\nds_read_b128 v[56:59], v3 offset:2048\n" \
    "ds_read_b128 v[60:63], v3 offset:3072\nds_read_b128 v[64:67], v3 offset:6144\nds_read_b128 v[68:71], v3 offset:7168\n.Lv48_%=:\ns_memrealtime s[72:73]\n" \
    "s_waitcnt lgkmcnt(0)\ns_mov_b32 s68, 0\ns_sub_u32 s76, s80, s45\ns_sub_u32 s69, s72, s76\ns_cmp_lt_i32 s69, -8\ns_cbranch_scc1 .Lv51_%=\n.Lv49_%=:\n" \
    "s_xor_b32 s90, s81, 0x800\n.Lv53_%=:\ns_memrealtime s[72:73]\ns_waitcnt lgkmcnt(0)\ns_sub_u32 s72, s72, s80\ns_cmp_lt_i32 s72, 0\ns_cbranch_scc1 .Lv53_%=\n" \
    "buffer_load_dwordx4 v[72:75], v2, s[64:67], s81 offen offset:0 sc1\ns_nop 7\nbuffer_load_dwordx4 v[76:79], v2, s[64:67], s81 offen offset:1024 sc1\ns_nop 7\n" \
    "buffer_load_dwordx4 v[80:83], v2, s[64:67], s90 offen offset:0 sc1\ns_nop 7\nbuffer_load_dwordx4 v[84:87], v2, s[64:67], s90 offen offset:1024 sc1\nv_mov_b64 v[16:17], 0\n" \
    "v_mov_b64 v[18:19], 0\nv_mov_b64 v[20:21], 0\nv_mov_b64 v[22:23], 0\ns_waitcnt vmcnt(3)\ns_nop 0\nv_smfmac_f32_16x16x64_bf16 v[20:23], v[72:75], v[24:31], v0\n" \
    "ds_write_b128 v1, v[72:75] offset:26624\nv_max3_u32 v252, v72, v73, v74\nv_smfmac_f32_16x16x64_bf16 v[16:19], v[72:75], a[16:23], v0\ns_waitcnt vmcnt(2)\n" \
    "ds_write_b128 v1, v[76:79] offset:30720\nv_max3_u32 v252, v252, v75, v76\nv_max3_u32 v252, v252, v77, v78\nv_cmp_u_f32 vcc, v252, v79\n" \
    "v_smfmac_f32_16x16x64_bf16 v[20:23], v[76:79], v[32:39], v0\ns_cbranch_vccnz .Lv59_%=\n.Lv55_%=:\nv_smfmac_f32_16x16x64_bf16 v[16:19], v[76:79], a[48:55], v0\n" \
    "ds_read_b128 v[24:27], v3 offset:8192\nds_read_b128 v[28:31], v3 offset:9216\ns_waitcnt vmcnt(1)\nv_max3_u32 v252, v80, v81, v82\nv_cmp_u_f32 vcc, v252, v83\ns_nop 0\n" \
    "s_cbranch_vccnz .Lv60_%=\n.Lv56_%=:\nv_smfmac_f32_16x16x64_bf16 v[20:23], v[80:83], v[40:47], v0\nds_write_b128 v1, v[80:83] offset:34816\n" \
    "ds_read_b128 v[36:39], v3 offset:11264\ns_waitcnt lgkmcnt(4)\ns_barrier\nds_read_b128 v[88:91], v1 offset:24576\nds_read_b128 v[40:43], v1 offset:25600\n" \
    "v_smfmac_f32_16x16x64_bf16 v[16:19], v[80:83], a[80:87], v0\nds_read_b128 v[32:35], v3 offset:10240\nds_read_b128 v[44:47], v1 offset:27648\ns_waitcnt vmcnt(0)\n" \
    "v_max3_u32 v252, v84, v85, v86\nv_cmp_u_f32 vcc, v252, v87\ns_nop 0\ns_cbranch_vccnz .Lv61_%=\n.Lv57_%=:\nv_smfmac_f32_16x16x64_bf16 v[20:23], v[84:87], a[136:143], v0\n" \
    "ds_write_b128 v1, v[84:87] offset:38912\nv_smfmac_f32_16x16x64_bf16 v[16:19], v[84:87], a[128:135], v0\nds_read_b128 v[72:75], v1 offset:28672\ns_waitcnt lgkmcnt(5)\n" \
    "v_smfmac_f32_16x16x64_bf16 v[20:23], v[88:91], v[48:55], v0\nds_read_b128 v[48:51], v1 offset:29696\nv_smfmac_f32_16x16x64_bf16 v[16:19], v[88:91], a[0:7], v0\n" \
    "ds_read_b128 v[52:55], v1 offset:31744\ns_waitcnt lgkmcnt(6)\nv_smfmac_f32_16x16x64_bf16 v[20:23], v[40:43], v[56:63], v0\nds_read_b128 v[56:59], v3 offset:14336\n" \
    "s_waitcnt lgkmcnt(4)\ns_barrier\nv_smfmac_f32_16x16x64_bf16 v[16:19], v[40:43], a[8:15], v0\nds_read_b128 v[60:63], v3 offset:15360\n" \
    "v_smfmac_f32_16x16x64_bf16 v[20:23], v[44:47], v[64:71], v0\nds_read_b128 v[40:43], v1 offset:32768\nv_smfmac_f32_16x16x64_bf16 v[16:19], v[44:47], a[24:31], v0\n" \
    "ds_read_b128 v[64:67], v3 offset:16384\ns_waitcnt lgkmcnt(6)\nv_smfmac_f32_16x16x64_bf16 v[20:23], v[72:75], v[24:31], v0\nds_read_b128 v[68:71], v3 offset:17408\n" \
    "v_smfmac_f32_16x16x64_bf16 v[16:19], v[72:75], a[32:39], v0\nds_read_b128 v[44:47], v1 offset:33792\ns_waitcnt lgkmcnt(7)\n" \
    "v_smfmac_f32_16x16x64_bf16 v[20:23], v[48:51], v[32:39], v0\nds_read_b128 v[24:27], v3 offset:18432\nv_smfmac_f32_16x16x64_bf16 v[16:19], v[48:51], a[40:47], v0\n" \
    "ds_read_b128 v[28:31], v3 offset:19456\ns_waitcnt lgkmcnt(6)\nv_smfmac_f32_16x16x64_bf16 v[20:23], v[52:55], v[56:63], v0\nds_read_b128 v[48:51], v1 offset:35840\n" \
    "v_smfmac_f32_16x16x64_bf16 v[16:19], v[52:55], a[56:63], v0\nds_read_b128 v[32:35], v3 offset:22528\ns_waitcnt lgkmcnt(5)\n" \
    "v_smfmac_f32_16x16x64_bf16 v[20:23], v[40:43], v[64:71], v0\nds_read_b128 v[36:39], v3 offset:23552\nv_smfmac_f32_16x16x64_bf16 v[16:19], v[40:43], a[64:71], v0\n" \
    "ds_read_b128 v[40:43], v1 offset:36864\ns_waitcnt lgkmcnt(4)\nv_smfmac_f32_16x16x64_bf16 v[20:23], v[44:47], v[24:31], v0\nds_read_b128 v[24:27], v1 offset:37888\n" \
    "v_smfmac_f32_16x16x64_bf16 v[16:19], v[44:47], a[72:79], v0\nds_read_b128 v[28:31], v1 offset:39936\ns_waitcnt lgkmcnt(3)\n" \
    "v_smfmac_f32_16x16x64_bf16 v[20:23], v[48:51], v[32:39], v0\ns_waitcnt lgkmcnt(2)\nv_smfmac_f32_16x16x64_bf16 v[20:23], v[40:43], a[104:111], v0\ns_waitcnt lgkmcnt(1)\n" \
    "v_smfmac_f32_16x16x64_bf16 v[20:23], v[24:27], a[120:127], v0\ns_waitcnt lgkmcnt(0)\nv_smfmac_f32_16x16x64_bf16 v[20:23], v[28:31], a[152:159], v0\ns_nop 2\n" \
    "v_smfmac_f32_16x16x64_bf16 v[16:19], v[48:51], a[88:95], v0\ns_nop 2\nv_smfmac_f32_16x16x64_bf16 v[16:19], v[40:43], a[96:103], v0\nv_permlane32_swap_b32 v20, v22\n" \
    "v_smfmac_f32_16x16x64_bf16 v[16:19], v[24:27], a[112:119], v0\nv_permlane32_swap_b32 v21, v23\nv_smfmac_f32_16x16x64_bf16 v[16:19], v[28:31], a[144:151], v0\n" \
    "v_add_f32 v20, v20, v22\nv_add_f32 v21, v21, v23\nv_cvt_pk_bf16_f32 v20, v20, v21\nds_write_b16 v4, v20 offset:8\nds_write_b16_d16_hi v4, v20 offset:24\n" \
    "v_mov_b32 v15, v15\nv_mov_b32 v15, v15\nv_mov_b32 v15, v15\nv_permlane32_swap_b32 v16, v18\nv_permlane32_swap_b32 v17, v19\nv_pk_add_f32 v[16:17], v[16:17], v[18:19]\n" \
    "v_cvt_pk_bf16_f32 v16, v16, v17\nds_write_b16 v4, v16 offset:0\nds_write_b16_d16_hi v4, v16 offset:16\ns_branch .Lv7_%=\n.Lv11_%=:\nds_read_b128 v[24:27], v3 offset:6144\n" \
    "ds_read_b128 v[28:31], v3 offset:7168\nds_read_b128 v[32:35], v3 offset:14336\nds_read_b128 v[36:39], v3 offset:15360\nds_read_b128 v[40:43], v3 offset:22528\n" \
    "ds_read_b128 v[44:47], v3 offset:23552\nds_read_b128 v[48:51], v3 offset:0\nds_read_b128 v[52:55], v3 offset:1024\nds_read_b128 v[56:59], v3 offset:2048\n" \
    "ds_read_b128 v[60:63], v3 offset:3072\nds_read_b128 v[64:67], v3 offset:4096\nds_read_b128 v[68:71], v3 offset:5120\n.Lv64_%=:\ns_memrealtime s[72:73]\n" \
    "s_waitcnt lgkmcnt(0)\ns_mov_b32 s68, 0\ns_sub_u32 s76, s80, s45\ns_sub_u32 s69, s72, s76\ns_cmp_lt_i32 s69, -8\ns_cbranch_scc1 .Lv67_%=\n.Lv65_%=:\n" \
    "s_xor_b32 s90, s81, 0x800\n.Lv69_%=:\ns_memrealtime s[72:73]\ns_waitcnt lgkmcnt(0)\ns_sub_u32 s72, s72, s80\ns_cmp_lt_i32 s72, 0\ns_cbranch_scc1 .Lv69_%=\n" \
    "buffer_load_dwordx4 v[72:75], v2, s[64:67], s81 offen offset:0 sc1\ns_nop 7\nbuffer_load_dwordx4 v[76:79], v2, s[64:67], s81 offen offset:1024 sc1\ns_nop 7\n" \
    "buffer_load_dwordx4 v[80:83], v2, s[64:67], s90 offen offset:0 sc1\ns_nop 7\nbuffer_load_dwordx4 v[84:87], v2, s[64:67], s90 offen offset:1024 sc1\nv_mov_b64 v[16:17], 0\n" \
    "v_mov_b64 v[18:19], 0\nv_mov_b64 v[20:21], 0\nv_mov_b64 v[22:23], 0\ns_waitcnt vmcnt(3)\ns_nop 0\nv_smfmac_f32_16x16x64_bf16 v[20:23], v[72:75], v[24:31], v0\n" \
    "ds_write_b128 v1, v[72:75] offset:27648\nv_max3_u32 v252, v72, v73, v74\nv_smfmac_f32_16x16x64_bf16 v[16:19], v[72:75], a[24:31], v0\ns_waitcnt vmcnt(2)\n" \
    "ds_write_b128 v1, v[76:79] offset:31744\nv_max3_u32 v252, v252, v75, v76\nv_max3_u32 v252, v252, v77, v78\nv_cmp_u_f32 vcc, v252, v79\n" \
    "v_smfmac_f32_16x16x64_bf16 v[20:23], v[76:79], v[32:39], v0\ns_cbranch_vccnz .Lv75_%=\n.Lv71_%=:\nv_smfmac_f32_16x16x64_bf16 v[16:19], v[76:79], a[56:63], v0\n" \
    "ds_read_b128 v[24:27], v3 offset:8192\nds_read_b128 v[28:31], v3 offset:9216\ns_waitcnt vmcnt(1)\nv_max3_u32 v252, v80, v81, v82\nv_cmp_u_f32 vcc, v252, v83\ns_nop 0\n" \
    "s_cbranch_vccnz .Lv76_%=\n.Lv72_%=:\nv_smfmac_f32_16x16x64_bf16 v[20:23], v[80:83], v[40:47], v0\nds_write_b128 v1, v[80:83] offset:35840\n" \
    "ds_read_b128 v[36:39], v3 offset:11264\ns_waitcnt lgkmcnt(4)\ns_barrier\nds_read_b128 v[88:91], v1 offset:24576\nds_read_b128 v[40:43], v1 offset:25600\n" \
    "v_smfmac_f32_16x16x64_bf16 v[16:19], v[80:83], a[88:95], v0\nds_read_b128 v[32:35], v3 offset:10240\nds_read_b128 v[44:47], v1 offset:26624\ns_waitcnt vmcnt(0)\n" \
    "v_max3_u32 v252, v84, v85, v86\nv_cmp_u_f32 vcc, v252, v87\ns_nop 0\ns_cbranch_vccnz .Lv77_%=\n.Lv73_%=:\nv_smfmac_f32_16x16x64_bf16 v[20:23], v[84:87], a[152:159], v0\n" \
    "ds_write_b128 v1, v[84:87] offset:39936\nv_smfmac_f32_16x16x64_bf16 v[16:19], v[84:87], a[144:151], v0\nds_read_b128 v[72:75], v1 offset:28672\ns_waitcnt lgkmcnt(5)\n" \
    "v_smfmac_f32_16x16x64_bf16 v[20:23], v[88:91], v[48:55], v0\nds_read_b128 v[48:51], v1 offset:29696\nv_smfmac_f32_16x16x64_bf16 v[16:19], v[88:91], a[0:7], v0\n" \
    "ds_read_b128 v[52:55], v1 offset:30720\ns_waitcnt lgkmcnt(6)\nv_smfmac_f32_16x16x64_bf16 v[20:23], v[40:43], v[56:63], v0\nds_read_b128 v[56:59], v3 offset:12288\n" \
    "s_waitcnt lgkmcnt(4)\ns_barrier\nv_smfmac_f32_16x16x64_bf16 v[16:19], v[40:43], a[8:15], v0\nds_read_b128 v[60:63], v3 offset:13312\n" \
    "v_smfmac_f32_16x16x64_bf16 v[20:23], v[44:47], v[64:71], v0\nds_read_b128 v[40:43], v1 offset:32768\nv_smfmac_f32_16x16x64_bf16 v[16:19], v[44:47], a[16:23], v0\n" \
    "ds_read_b128 v[64:67], v3 offset:16384\ns_waitcnt lgkmcnt(6)\nv_smfmac_f32_16x16x64_bf16 v[20:23], v[72:75], v[24:31], v0\nds_read_b128 v[68:71], v3 offset:17408\n" \
    "v_smfmac_f32_16x16x64_bf16 v[16:19], v[72:75], a[32:39], v0\nds_read_b128 v[44:47], v1 offset:33792\ns_waitcnt lgkmcnt(7)\n" \
    "v_smfmac_f32_16x16x64_bf16 v[20:23], v[48:51], v[32:39], v0\nds_read_b128 v[24:27], v3 offset:18432\nv_smfmac_f32_16x16x64_bf16 v[16:19], v[48:51], a[40:47], v0\n" \
    "ds_read_b128 v[28:31], v3 offset:19456\ns_waitcnt lgkmcnt(6)\nv_smfmac_f32_16x16x64_bf16 v[20:23], v[52:55], v[56:63], v0\nds_read_b128 v[48:51], v1 offset:34816\n" \
    "v_smfmac_f32_16x16x64_bf16 v[16:19], v[52:55], a[48:55], v0\nds_read_b128 v[32:35], v3 offset:20480\ns_waitcnt lgkmcnt(5)\n" \
    "v_smfmac_f32_16x16x64_bf16 v[20:23], v[40:43], v[64:71], v0\nds_read_b128 v[36:39], v3 offset:21504\nv_smfmac_f32_16x16x64_bf16 v[16:19], v[40:43], a[64:71], v0\n" \
    "ds_read_b128 v[40:43], v1 offset:36864\ns_waitcnt lgkmcnt(4)\nv_smfmac_f32_16x16x64_bf16 v[20:23], v[44:47], v[24:31], v0\nds_read_b128 v[24:27], v1 offset:37888\n" \
    "v_smfmac_f32_16x16x64_bf16 v[16:19], v[44:47], a[72:79], v0\nds_read_b128 v[28:31], v1 offset:38912\ns_waitcnt lgkmcnt(3)\n" \
    "v_smfmac_f32_16x16x64_bf16 v[20:23], v[48:51], v[32:39], v0\ns_waitcnt lgkmcnt(2)\nv_smfmac_f32_16x16x64_bf16 v[20:23], v[40:43], a[104:111], v0\ns_waitcnt lgkmcnt(1)\n" \
    "v_smfmac_f32_16x16x64_bf16 v[20:23], v[24:27], a[120:127], v0\ns_waitcnt lgkmcnt(0)\nv_smfmac_f32_16x16x64_bf16 v[20:23], v[28:31], a[136:143], v0\ns_nop 2\n" \
    "v_smfmac_f32_16x16x64_bf16 v[16:19], v[48:51], a[80:87], v0\ns_nop 2\nv_smfmac_f32_16x16x64_bf16 v[16:19], v[40:43], a[96:103], v0\nv_permlane32_swap_b32 v20, v22\n" \
    "v_smfmac_f32_16x16x64_bf16 v[16:19], v[24:27], a[112:119], v0\nv_permlane32_swap_b32 v21, v23\nv_smfmac_f32_16x16x64_bf16 v[16:19], v[28:31], a[128:135], v0\n" \
    "v_add_f32 v20, v20, v22\nv_add_f32 v21, v21, v23\nv_cvt_pk_bf16_f32 v20, v20, v21\nds_write_b16 v4, v20 offset:8\nds_write_b16_d16_hi v4, v20 offset:24\n" \
    "v_mov_b32 v15, v15\nv_mov_b32 v15, v15\nv_mov_b32 v15, v15\nv_permlane32_swap_b32 v16, v18\nv_permlane32_swap_b32 v17, v19\nv_pk_add_f32 v[16:17], v[16:17], v[18:19]\n" \
    "v_cvt_pk_bf16_f32 v16, v16, v17\nds_write_b16 v4, v16 offset:0\nds_write_b16_d16_hi v4, v16 offset:16\nv_mov_b32 v15, v15\nv_mov_b32 v15, v15\n.Lv7_%=:\n.Lv80_%=:\n" \
    "v_mov_b64 v[54:55], 0\nv_mov_b64 v[52:53], 0\nv_mov_b64 v[50:51], 0\nv_mov_b64 v[48:49], 0\nv_mov_b64 v[70:71], 0\nv_mov_b64 v[68:69], 0\nv_mov_b64 v[66:67], 0\n" \
    "v_mov_b64 v[64:65], 0\nds_read_b128 v[32:35], v5 offset:0\nds_read_b128 v[36:39], v5 offset:512\nds_read_b128 v[40:43], v5 offset:1024\n" \
    "ds_read_b128 v[44:47], v5 offset:1536\nv_mov_b64 v[30:31], 0\nv_mov_b64 v[28:29], 0\nv_mov_b64 v[26:27], 0\nv_mov_b64 v[24:25], 0\nv_mov_b64 v[22:23], 0\n" \
    "v_mov_b64 v[20:21], 0\nv_mov_b64 v[18:19], 0\nv_mov_b64 v[16:17], 0\ns_waitcnt lgkmcnt(3)\ns_nop 0\nv_smfmac_f32_16x16x64_bf16 v[16:19], v[32:35], a[160:167], v0\n" \
    "v_smfmac_f32_16x16x64_bf16 v[20:23], v[32:35], a[176:183], v0\ns_waitcnt lgkmcnt(2)\nv_smfmac_f32_16x16x64_bf16 v[16:19], v[36:39], a[168:175], v0\n" \
    "v_smfmac_f32_16x16x64_bf16 v[20:23], v[36:39], a[184:191], v0\ns_waitcnt lgkmcnt(1)\nv_smfmac_f32_16x16x64_bf16 v[16:19], v[40:43], a[192:199], v0\n" \
    "v_smfmac_f32_16x16x64_bf16 v[20:23], v[40:43], a[208:215], v0\ns_waitcnt lgkmcnt(0)\nv_smfmac_f32_16x16x64_bf16 v[16:19], v[44:47], a[200:207], v0\n" \
    "v_smfmac_f32_16x16x64_bf16 v[20:23], v[44:47], a[216:223], v0\ns_nop 2\nv_smfmac_f32_16x16x64_bf16 v[24:27], v[32:35], a[224:231], v0\ns_nop 2\n" \
    "v_cmp_u_f32 s[4:5], v16, v17\nv_smfmac_f32_16x16x64_bf16 v[28:31], v[32:35], a[240:247], v0\nv_cmp_u_f32 vcc, v18, v19\n" \
    "v_smfmac_f32_16x16x64_bf16 v[24:27], v[36:39], a[232:239], v0\nv_cvt_pk_bf16_f32 v72, v16, v17\nv_smfmac_f32_16x16x64_bf16 v[28:31], v[36:39], a[248:255], v0\n" \
    "v_cvt_pk_bf16_f32 v74, v18, v19\nv_smfmac_f32_16x16x64_bf16 v[24:27], v[40:43], v[92:99], v0\ns_or_b64 vcc, vcc, s[4:5]\ns_cbranch_scc1 .Lv81_%=\n.Lv82_%=:\n" \
    "v_cvt_pk_bf16_f32 v73, v20, v21\nv_smfmac_f32_16x16x64_bf16 v[28:31], v[40:43], v[108:115], v0\nv_cvt_pk_bf16_f32 v75, v22, v23\n" \
    "v_smfmac_f32_16x16x64_bf16 v[24:27], v[44:47], v[100:107], v0\nv_permlane16_swap_b32 v72, v73\nv_smfmac_f32_16x16x64_bf16 v[28:31], v[44:47], v[116:123], v0\n" \
    "v_permlane16_swap_b32 v74, v75\nv_smfmac_f32_16x16x64_bf16 v[64:67], v[32:35], v[124:131], v0\nbuffer_store_dwordx4 v[72:75], v7, s[64:67], s0 offen offset:0 sc1\ns_nop 1\n" \
    "v_smfmac_f32_16x16x64_bf16 v[68:71], v[32:35], v[140:147], v0\nv_cvt_pk_bf16_f32 v76, v24, v25\nv_smfmac_f32_16x16x64_bf16 v[64:67], v[36:39], v[132:139], v0\n" \
    "v_cvt_pk_bf16_f32 v78, v26, v27\nv_smfmac_f32_16x16x64_bf16 v[68:71], v[36:39], v[148:155], v0\nv_cvt_pk_bf16_f32 v77, v28, v29\n" \
    "v_smfmac_f32_16x16x64_bf16 v[64:67], v[40:43], v[156:163], v0\nv_cvt_pk_bf16_f32 v79, v30, v31\nv_smfmac_f32_16x16x64_bf16 v[68:71], v[40:43], v[172:179], v0\n" \
    "v_permlane16_swap_b32 v76, v77\nv_smfmac_f32_16x16x64_bf16 v[64:67], v[44:47], v[164:171], v0\nv_permlane16_swap_b32 v78, v79\n" \
    "v_smfmac_f32_16x16x64_bf16 v[68:71], v[44:47], v[180:187], v0\nbuffer_store_dwordx4 v[76:79], v7, s[64:67], s1 offen offset:0 sc1\ns_nop 1\n" \
    "v_smfmac_f32_16x16x64_bf16 v[48:51], v[32:35], v[188:195], v0\ns_nop 1\nv_cvt_pk_bf16_f32 v72, v64, v65\nv_smfmac_f32_16x16x64_bf16 v[52:55], v[32:35], v[204:211], v0\n" \
    "v_cvt_pk_bf16_f32 v74, v66, v67\nv_smfmac_f32_16x16x64_bf16 v[48:51], v[36:39], v[196:203], v0\nv_cvt_pk_bf16_f32 v73, v68, v69\n" \
    "v_smfmac_f32_16x16x64_bf16 v[52:55], v[36:39], v[212:219], v0\nv_cvt_pk_bf16_f32 v75, v70, v71\nv_smfmac_f32_16x16x64_bf16 v[48:51], v[40:43], v[220:227], v0\n" \
    "v_permlane16_swap_b32 v72, v73\nv_smfmac_f32_16x16x64_bf16 v[52:55], v[40:43], v[236:243], v0\nv_permlane16_swap_b32 v74, v75\n" \
    "v_smfmac_f32_16x16x64_bf16 v[48:51], v[44:47], v[228:235], v0\nbuffer_store_dwordx4 v[72:75], v7, s[64:67], s2 offen offset:0 sc1\n" \
    "v_smfmac_f32_16x16x64_bf16 v[52:55], v[44:47], v[244:251], v0\ns_nop 5\nv_cvt_pk_bf16_f32 v76, v48, v49\nv_cvt_pk_bf16_f32 v78, v50, v51\nv_cvt_pk_bf16_f32 v77, v52, v53\n" \
    "v_cvt_pk_bf16_f32 v79, v54, v55\ns_nop 0\nv_permlane16_swap_b32 v76, v77\nv_permlane16_swap_b32 v78, v79\n" \
    "buffer_store_dwordx4 v[76:79], v7, s[64:67], s3 offen offset:0 sc1\ns_barrier\ns_add_u32 s35, s35, 2\ns_cmp_lt_u32 s35, 62\ns_cbranch_scc1 .Lv5_%=\ns_cmp_lt_u32 s35, 64\n" \
    "s_cbranch_scc0 .Lv6_%=\nds_read_b128 v[56:59], v6 offset:0\nds_read_b128 v[60:63], v6 offset:1024\nds_read_b128 v[64:67], v6 offset:2048\nv_mov_b64 v[32:33], 0\n" \
    "v_mov_b64 v[34:35], 0\nv_mov_b64 v[36:37], 0\nv_mov_b64 v[38:39], 0\ns_and_b32 s69, s50, 15\ns_lshl_b32 s69, s69, 21\ns_add_u32 s86, s39, s69\ns_lshr_b32 s69, s86, 8\n" \
    "s_and_b32 s69, s69, 0x2000\ns_xor_b32 s86, s86, s69\ns_waitcnt lgkmcnt(2)\nv_smfmac_f32_16x16x64_bf16 v[32:35], v[56:59], a[160:167], v0\ns_add_u32 s87, s86, 0x40000\n" \
    "s_add_u32 s88, s86, 0x80000\nv_mov_b64 v[40:41], 0\nv_mov_b64 v[42:43], 0\nv_smfmac_f32_16x16x64_bf16 v[36:39], v[56:59], a[176:183], v0\n" \
    "ds_read_b128 v[68:71], v6 offset:3072\ns_add_u32 s89, s86, 0xc0000\ns_waitcnt lgkmcnt(2)\nv_mov_b64 v[44:45], 0\nv_mov_b64 v[46:47], 0\n" \
    "v_smfmac_f32_16x16x64_bf16 v[32:35], v[60:63], a[168:175], v0\ns_add_u32 s50, s50, 2\nv_mov_b64 v[252:253], -1\n" \
    "v_smfmac_f32_16x16x64_bf16 v[36:39], v[60:63], a[184:191], v0\ns_and_b32 s69, s35, 15\ns_lshl_b32 s69, s69, 19\ns_add_u32 s0, s38, s69\ns_waitcnt lgkmcnt(1)\n" \
    "v_smfmac_f32_16x16x64_bf16 v[32:35], v[64:67], a[192:199], v0\ns_lshr_b32 s69, s0, 8\ns_and_b32 s69, s69, 0x2000\ns_xor_b32 s0, s0, s69\n" \
    "v_smfmac_f32_16x16x64_bf16 v[36:39], v[64:67], a[208:215], v0\ns_add_u32 s1, s0, 0x10000\ns_add_u32 s2, s0, 0x20000\ns_add_u32 s3, s0, 0x30000\ns_waitcnt lgkmcnt(0)\n" \
    "v_smfmac_f32_16x16x64_bf16 v[32:35], v[68:71], a[200:207], v0\nv_smfmac_f32_16x16x64_bf16 v[36:39], v[68:71], a[216:223], v0\ns_nop 2\n" \
    "v_smfmac_f32_16x16x64_bf16 v[40:43], v[56:59], a[224:231], v0\ns_nop 2\nv_cvt_pk_bf16_f32 v48, v32, v33\nv_cvt_pk_bf16_f32 v50, v34, v35\n" \
    "v_smfmac_f32_16x16x64_bf16 v[44:47], v[56:59], a[240:247], v0\nv_cvt_pk_bf16_f32 v49, v36, v37\nv_cvt_pk_bf16_f32 v51, v38, v39\n" \
    "v_smfmac_f32_16x16x64_bf16 v[40:43], v[60:63], a[232:239], v0\nv_permlane16_swap_b32 v48, v49\nv_smfmac_f32_16x16x64_bf16 v[44:47], v[60:63], a[248:255], v0\n" \
    "v_permlane16_swap_b32 v50, v51\nv_smfmac_f32_16x16x64_bf16 v[40:43], v[64:67], v[92:99], v0\nbuffer_store_dwordx4 v[48:51], v8, s[64:67], s86 offen offset:0 sc1\n" \
    "v_mov_b64 v[32:33], 0\nv_mov_b64 v[34:35], 0\nv_smfmac_f32_16x16x64_bf16 v[44:47], v[64:67], v[108:115], v0\nv_mov_b64 v[36:37], 0\nv_mov_b64 v[38:39], 0\n" \
    "v_smfmac_f32_16x16x64_bf16 v[40:43], v[68:71], v[100:107], v0\nv_smfmac_f32_16x16x64_bf16 v[44:47], v[68:71], v[116:123], v0\ns_nop 2\n" \
    "v_smfmac_f32_16x16x64_bf16 v[32:35], v[56:59], v[124:131], v0\ns_nop 2\nv_cvt_pk_bf16_f32 v52, v40, v41\nv_cvt_pk_bf16_f32 v54, v42, v43\n" \
    "v_smfmac_f32_16x16x64_bf16 v[36:39], v[56:59], v[140:147], v0\nv_cvt_pk_bf16_f32 v53, v44, v45\nv_cvt_pk_bf16_f32 v55, v46, v47\n" \
    "v_smfmac_f32_16x16x64_bf16 v[32:35], v[60:63], v[132:139], v0\nv_permlane16_swap_b32 v52, v53\nv_smfmac_f32_16x16x64_bf16 v[36:39], v[60:63], v[148:155], v0\n" \
    "v_permlane16_swap_b32 v54, v55\nv_smfmac_f32_16x16x64_bf16 v[32:35], v[64:67], v[156:163], v0\nbuffer_store_dwordx4 v[52:55], v8, s[64:67], s87 offen offset:0 sc1\n" \
    "v_mov_b64 v[40:41], 0\nv_mov_b64 v[42:43], 0\nv_smfmac_f32_16x16x64_bf16 v[36:39], v[64:67], v[172:179], v0\nv_mov_b64 v[44:45], 0\nv_mov_b64 v[46:47], 0\n" \
    "v_smfmac_f32_16x16x64_bf16 v[32:35], v[68:71], v[164:171], v0\nv_smfmac_f32_16x16x64_bf16 v[36:39], v[68:71], v[180:187], v0\ns_nop 2\n" \
    "v_smfmac_f32_16x16x64_bf16 v[40:43], v[56:59], v[188:195], v0\ns_nop 2\nv_cvt_pk_bf16_f32 v48, v32, v33\nv_cvt_pk_bf16_f32 v50, v34, v35\n" \
    "v_smfmac_f32_16x16x64_bf16 v[44:47], v[56:59], v[204:211], v0\nv_cvt_pk_bf16_f32 v49, v36, v37\nv_cvt_pk_bf16_f32 v51, v38, v39\n" \
    "v_smfmac_f32_16x16x64_bf16 v[40:43], v[60:63], v[196:203], v0\nv_permlane16_swap_b32 v48, v49\nv_smfmac_f32_16x16x64_bf16 v[44:47], v[60:63], v[212:219], v0\n" \
    "ds_read_b128 v[56:59], v6 offset:128\nv_permlane16_swap_b32 v50, v51\nv_smfmac_f32_16x16x64_bf16 v[40:43], v[64:67], v[220:227], v0\n" \
    "buffer_store_dwordx4 v[48:51], v8, s[64:67], s88 offen offset:0 sc1\nv_mov_b64 v[32:33], 0\nv_mov_b64 v[34:35], 0\n" \
    "v_smfmac_f32_16x16x64_bf16 v[44:47], v[64:67], v[236:243], v0\nds_read_b128 v[60:63], v6 offset:1152\nv_mov_b64 v[36:37], 0\nv_mov_b64 v[38:39], 0\n" \
    "v_smfmac_f32_16x16x64_bf16 v[40:43], v[68:71], v[228:235], v0\nv_smfmac_f32_16x16x64_bf16 v[44:47], v[68:71], v[244:251], v0\nds_read_b128 v[64:67], v6 offset:2176\n" \
    "s_nop 1\ns_waitcnt lgkmcnt(2)\nv_smfmac_f32_16x16x64_bf16 v[32:35], v[56:59], a[160:167], v0\ns_nop 1\nv_cvt_pk_bf16_f32 v52, v40, v41\nv_cvt_pk_bf16_f32 v54, v42, v43\n" \
    "v_smfmac_f32_16x16x64_bf16 v[36:39], v[56:59], a[176:183], v0\nds_read_b128 v[68:71], v6 offset:3200\ns_waitcnt lgkmcnt(2)\nv_cvt_pk_bf16_f32 v53, v44, v45\n" \
    "v_cvt_pk_bf16_f32 v55, v46, v47\nv_smfmac_f32_16x16x64_bf16 v[32:35], v[60:63], a[168:175], v0\nv_permlane16_swap_b32 v52, v53\n" \
    "v_smfmac_f32_16x16x64_bf16 v[36:39], v[60:63], a[184:191], v0\ns_waitcnt lgkmcnt(1)\nv_permlane16_swap_b32 v54, v55\n" \
    "v_smfmac_f32_16x16x64_bf16 v[32:35], v[64:67], a[192:199], v0\nbuffer_store_dwordx4 v[52:55], v8, s[64:67], s89 offen offset:0 sc1\nv_mov_b64 v[40:41], 0\n" \
    "v_mov_b64 v[42:43], 0\nv_smfmac_f32_16x16x64_bf16 v[36:39], v[64:67], a[208:215], v0\ns_waitcnt lgkmcnt(0)\nv_mov_b64 v[44:45], 0\nv_mov_b64 v[46:47], 0\n" \
    "v_smfmac_f32_16x16x64_bf16 v[32:35], v[68:71], a[200:207], v0\nv_smfmac_f32_16x16x64_bf16 v[36:39], v[68:71], a[216:223], v0\ns_nop 2\n" \
    "v_smfmac_f32_16x16x64_bf16 v[40:43], v[56:59], a[224:231], v0\ns_nop 2\nv_cvt_pk_bf16_f32 v48, v32, v33\nv_cvt_pk_bf16_f32 v50, v34, v35\n" \
    "v_smfmac_f32_16x16x64_bf16 v[44:47], v[56:59], a[240:247], v0\nv_cvt_pk_bf16_f32 v49, v36, v37\nv_cvt_pk_bf16_f32 v51, v38, v39\n" \
    "v_smfmac_f32_16x16x64_bf16 v[40:43], v[60:63], a[232:239], v0\nv_permlane16_swap_b32 v48, v49\nv_smfmac_f32_16x16x64_bf16 v[44:47], v[60:63], a[248:255], v0\n" \
    "v_permlane16_swap_b32 v50, v51\nv_smfmac_f32_16x16x64_bf16 v[40:43], v[64:67], v[92:99], v0\nbuffer_store_dwordx4 v[48:51], v8, s[64:67], s86 offen offset:256 sc1\n" \
    "v_mov_b64 v[32:33], 0\nv_mov_b64 v[34:35], 0\nv_smfmac_f32_16x16x64_bf16 v[44:47], v[64:67], v[108:115], v0\nv_mov_b64 v[36:37], 0\nv_mov_b64 v[38:39], 0\n" \
    "v_smfmac_f32_16x16x64_bf16 v[40:43], v[68:71], v[100:107], v0\nv_smfmac_f32_16x16x64_bf16 v[44:47], v[68:71], v[116:123], v0\ns_nop 2\n" \
    "v_smfmac_f32_16x16x64_bf16 v[32:35], v[56:59], v[124:131], v0\ns_nop 2\nv_cvt_pk_bf16_f32 v52, v40, v41\nv_cvt_pk_bf16_f32 v54, v42, v43\n" \
    "v_smfmac_f32_16x16x64_bf16 v[36:39], v[56:59], v[140:147], v0\nv_cvt_pk_bf16_f32 v53, v44, v45\nv_cvt_pk_bf16_f32 v55, v46, v47\n" \
    "v_smfmac_f32_16x16x64_bf16 v[32:35], v[60:63], v[132:139], v0\nv_permlane16_swap_b32 v52, v53\nv_smfmac_f32_16x16x64_bf16 v[36:39], v[60:63], v[148:155], v0\n" \
    "v_permlane16_swap_b32 v54, v55\nv_smfmac_f32_16x16x64_bf16 v[32:35], v[64:67], v[156:163], v0\nbuffer_store_dwordx4 v[52:55], v8, s[64:67], s87 offen offset:256 sc1\n" \
    "v_mov_b64 v[40:41], 0\nv_mov_b64 v[42:43], 0\nv_smfmac_f32_16x16x64_bf16 v[36:39], v[64:67], v[172:179], v0\nv_mov_b64 v[44:45], 0\nv_mov_b64 v[46:47], 0\n" \
    "v_smfmac_f32_16x16x64_bf16 v[32:35], v[68:71], v[164:171], v0\nv_smfmac_f32_16x16x64_bf16 v[36:39], v[68:71], v[180:187], v0\ns_nop 2\n" \
    "v_smfmac_f32_16x16x64_bf16 v[40:43], v[56:59], v[188:195], v0\ns_nop 2\nv_cvt_pk_bf16_f32 v48, v32, v33\nv_cvt_pk_bf16_f32 v50, v34, v35\n" \
    "v_smfmac_f32_16x16x64_bf16 v[44:47], v[56:59], v[204:211], v0\nv_cvt_pk_bf16_f32 v49, v36, v37\nv_cvt_pk_bf16_f32 v51, v38, v39\n" \
    "v_smfmac_f32_16x16x64_bf16 v[40:43], v[60:63], v[196:203], v0\nv_permlane16_swap_b32 v48, v49\nv_smfmac_f32_16x16x64_bf16 v[44:47], v[60:63], v[212:219], v0\n" \
    "ds_read_b128 v[56:59], v6 offset:4096\nv_permlane16_swap_b32 v50, v51\nv_smfmac_f32_16x16x64_bf16 v[40:43], v[64:67], v[220:227], v0\n" \
    "buffer_store_dwordx4 v[48:51], v8, s[64:67], s88 offen offset:256 sc1\nv_mov_b64 v[32:33], 0\nv_mov_b64 v[34:35], 0\n" \
    "v_smfmac_f32_16x16x64_bf16 v[44:47], v[64:67], v[236:243], v0\nds_read_b128 v[60:63], v6 offset:5120\nv_mov_b64 v[36:37], 0\nv_mov_b64 v[38:39], 0\n" \
    "v_smfmac_f32_16x16x64_bf16 v[40:43], v[68:71], v[228:235], v0\nv_smfmac_f32_16x16x64_bf16 v[44:47], v[68:71], v[244:251], v0\nds_read_b128 v[64:67], v6 offset:6144\n" \
    "s_nop 1\ns_waitcnt lgkmcnt(2)\nv_smfmac_f32_16x16x64_bf16 v[32:35], v[56:59], a[160:167], v0\ns_nop 1\nv_cvt_pk_bf16_f32 v52, v40, v41\nv_cvt_pk_bf16_f32 v54, v42, v43\n" \
    "v_smfmac_f32_16x16x64_bf16 v[36:39], v[56:59], a[176:183], v0\nds_read_b128 v[68:71], v6 offset:7168\ns_waitcnt lgkmcnt(2)\nv_cvt_pk_bf16_f32 v53, v44, v45\n" \
    "v_cvt_pk_bf16_f32 v55, v46, v47\nv_smfmac_f32_16x16x64_bf16 v[32:35], v[60:63], a[168:175], v0\nv_permlane16_swap_b32 v52, v53\n" \
    "v_smfmac_f32_16x16x64_bf16 v[36:39], v[60:63], a[184:191], v0\ns_waitcnt lgkmcnt(1)\nv_permlane16_swap_b32 v54, v55\n" \
    "v_smfmac_f32_16x16x64_bf16 v[32:35], v[64:67], a[192:199], v0\nbuffer_store_dwordx4 v[52:55], v8, s[64:67], s89 offen offset:256 sc1\nv_mov_b64 v[40:41], 0\n" \
    "v_mov_b64 v[42:43], 0\nv_smfmac_f32_16x16x64_bf16 v[36:39], v[64:67], a[208:215], v0\ns_waitcnt lgkmcnt(0)\nv_mov_b64 v[44:45], 0\nv_mov_b64 v[46:47], 0\n" \
    "v_smfmac_f32_16x16x64_bf16 v[32:35], v[68:71], a[200:207], v0\nv_smfmac_f32_16x16x64_bf16 v[36:39], v[68:71], a[216:223], v0\ns_nop 2\n" \
    "v_smfmac_f32_16x16x64_bf16 v[40:43], v[56:59], a[224:231], v0\ns_nop 2\nv_cvt_pk_bf16_f32 v48, v32, v33\nv_cvt_pk_bf16_f32 v50, v34, v35\n" \
    "v_smfmac_f32_16x16x64_bf16 v[44:47], v[56:59], a[240:247], v0\nv_cvt_pk_bf16_f32 v49, v36, v37\nv_cvt_pk_bf16_f32 v51, v38, v39\n" \
    "v_smfmac_f32_16x16x64_bf16 v[40:43], v[60:63], a[232:239], v0\nv_permlane16_swap_b32 v48, v49\nv_smfmac_f32_16x16x64_bf16 v[44:47], v[60:63], a[248:255], v0\n" \
    "v_permlane16_swap_b32 v50, v51\nv_smfmac_f32_16x16x64_bf16 v[40:43], v[64:67], v[92:99], v0\nbuffer_store_dwordx4 v[48:51], v8, s[64:67], s86 offen offset:512 sc1\n" \
    "v_mov_b64 v[32:33], 0\nv_mov_b64 v[34:35], 0\nv_smfmac_f32_16x16x64_bf16 v[44:47], v[64:67], v[108:115], v0\nv_mov_b64 v[36:37], 0\nv_mov_b64 v[38:39], 0\n" \
    "v_smfmac_f32_16x16x64_bf16 v[40:43], v[68:71], v[100:107], v0\nv_smfmac_f32_16x16x64_bf16 v[44:47], v[68:71], v[116:123], v0\ns_nop 2\n" \
    "v_smfmac_f32_16x16x64_bf16 v[32:35], v[56:59], v[124:131], v0\ns_nop 2\nv_cvt_pk_bf16_f32 v52, v40, v41\nv_cvt_pk_bf16_f32 v54, v42, v43\n" \
    "v_smfmac_f32_16x16x64_bf16 v[36:39], v[56:59], v[140:147], v0\nv_cvt_pk_bf16_f32 v53, v44, v45\nv_cvt_pk_bf16_f32 v55, v46, v47\n" \
    "v_smfmac_f32_16x16x64_bf16 v[32:35], v[60:63], v[132:139], v0\nv_permlane16_swap_b32 v52, v53\nv_smfmac_f32_16x16x64_bf16 v[36:39], v[60:63], v[148:155], v0\n" \
    "v_permlane16_swap_b32 v54, v55\nv_smfmac_f32_16x16x64_bf16 v[32:35], v[64:67], v[156:163], v0\nbuffer_store_dwordx4 v[52:55], v8, s[64:67], s87 offen offset:512 sc1\n" \
    "buffer_load_dword v13, off, s[64:67], s42 sc1\nv_mov_b64 v[40:41], 0\nv_mov_b64 v[42:43], 0\nv_smfmac_f32_16x16x64_bf16 v[36:39], v[64:67], v[172:179], v0\n" \
    "s_and_b32 s69, s35, 15\ns_lshl_b32 s69, s69, 17\nv_mov_b64 v[44:45], 0\nv_mov_b64 v[46:47], 0\nv_smfmac_f32_16x16x64_bf16 v[32:35], v[68:71], v[164:171], v0\n" \
    "s_add_u32 s81, s37, s69\ns_lshr_b32 s69, s81, 8\ns_and_b32 s69, s69, 0x2000\nv_smfmac_f32_16x16x64_bf16 v[36:39], v[68:71], v[180:187], v0\ns_xor_b32 s81, s81, s69\n" \
    "s_mov_b32 s68, 0\ns_nop 0\nv_smfmac_f32_16x16x64_bf16 v[40:43], v[56:59], v[188:195], v0\nv_cvt_pk_bf16_f32 v48, v32, v33\nv_cvt_pk_bf16_f32 v50, v34, v35\n" \
    "v_smfmac_f32_16x16x64_bf16 v[44:47], v[56:59], v[204:211], v0\ns_nop 0\nv_cvt_pk_bf16_f32 v49, v36, v37\nv_cvt_pk_bf16_f32 v51, v38, v39\n" \
    "v_smfmac_f32_16x16x64_bf16 v[40:43], v[60:63], v[196:203], v0\nv_permlane16_swap_b32 v48, v49\nv_smfmac_f32_16x16x64_bf16 v[44:47], v[60:63], v[212:219], v0\n" \
    "ds_read_b128 v[56:59], v6 offset:4224\nv_permlane16_swap_b32 v50, v51\nv_smfmac_f32_16x16x64_bf16 v[40:43], v[64:67], v[220:227], v0\n" \
    "buffer_store_dwordx4 v[48:51], v8, s[64:67], s88 offen offset:512 sc1\nv_mov_b64 v[32:33], 0\nv_mov_b64 v[34:35], 0\n" \
    "v_smfmac_f32_16x16x64_bf16 v[44:47], v[64:67], v[236:243], v0\nds_read_b128 v[60:63], v6 offset:5248\nv_mov_b64 v[36:37], 0\nv_mov_b64 v[38:39], 0\n" \
    "v_smfmac_f32_16x16x64_bf16 v[40:43], v[68:71], v[228:235], v0\nv_smfmac_f32_16x16x64_bf16 v[44:47], v[68:71], v[244:251], v0\nds_read_b128 v[64:67], v6 offset:6272\n" \
    "s_nop 1\ns_waitcnt lgkmcnt(2)\nv_smfmac_f32_16x16x64_bf16 v[32:35], v[56:59], a[160:167], v0\ns_nop 1\nv_cvt_pk_bf16_f32 v52, v40, v41\nv_cvt_pk_bf16_f32 v54, v42, v43\n" \
    "v_smfmac_f32_16x16x64_bf16 v[36:39], v[56:59], a[176:183], v0\nds_read_b128 v[68:71], v6 offset:7296\ns_waitcnt lgkmcnt(2)\nv_cvt_pk_bf16_f32 v53, v44, v45\n" \
    "v_cvt_pk_bf16_f32 v55, v46, v47\nv_smfmac_f32_16x16x64_bf16 v[32:35], v[60:63], a[168:175], v0\nv_permlane16_swap_b32 v52, v53\n" \
    "v_smfmac_f32_16x16x64_bf16 v[36:39], v[60:63], a[184:191], v0\ns_waitcnt lgkmcnt(1)\nv_permlane16_swap_b32 v54, v55\n" \
    "v_smfmac_f32_16x16x64_bf16 v[32:35], v[64:67], a[192:199], v0\nbuffer_store_dwordx4 v[52:55], v8, s[64:67], s89 offen offset:512 sc1\nv_mov_b64 v[40:41], 0\n" \
    "v_mov_b64 v[42:43], 0\nv_smfmac_f32_16x16x64_bf16 v[36:39], v[64:67], a[208:215], v0\ns_waitcnt lgkmcnt(0)\nv_mov_b64 v[44:45], 0\nv_mov_b64 v[46:47], 0\n" \
    "v_smfmac_f32_16x16x64_bf16 v[32:35], v[68:71], a[200:207], v0\nv_smfmac_f32_16x16x64_bf16 v[36:39], v[68:71], a[216:223], v0\ns_nop 2\n" \
    "v_smfmac_f32_16x16x64_bf16 v[40:43], v[56:59], a[224:231], v0\ns_nop 2\nv_cvt_pk_bf16_f32 v48, v32, v33\nv_cvt_pk_bf16_f32 v50, v34, v35\n" \
    "v_smfmac_f32_16x16x64_bf16 v[44:47], v[56:59], a[240:247], v0\nv_cvt_pk_bf16_f32 v49, v36, v37\nv_cvt_pk_bf16_f32 v51, v38, v39\n" \
    "v_smfmac_f32_16x16x64_bf16 v[40:43], v[60:63], a[232:239], v0\nv_permlane16_swap_b32 v48, v49\nv_smfmac_f32_16x16x64_bf16 v[44:47], v[60:63], a[248:255], v0\n" \
    "v_permlane16_swap_b32 v50, v51\nv_smfmac_f32_16x16x64_bf16 v[40:43], v[64:67], v[92:99], v0\nbuffer_store_dwordx4 v[48:51], v8, s[64:67], s86 offen offset:768 sc1\n" \
    "v_mov_b64 v[32:33], 0\nv_mov_b64 v[34:35], 0\nv_smfmac_f32_16x16x64_bf16 v[44:47], v[64:67], v[108:115], v0\nv_mov_b64 v[36:37], 0\nv_mov_b64 v[38:39], 0\n" \
    "v_smfmac_f32_16x16x64_bf16 v[40:43], v[68:71], v[100:107], v0\nv_smfmac_f32_16x16x64_bf16 v[44:47], v[68:71], v[116:123], v0\ns_nop 2\n" \
    "v_smfmac_f32_16x16x64_bf16 v[32:35], v[56:59], v[124:131], v0\ns_waitcnt vmcnt(3)\ns_nop 0\nv_readfirstlane_b32 s74, v13\nv_cvt_pk_bf16_f32 v52, v40, v41\n" \
    "v_smfmac_f32_16x16x64_bf16 v[36:39], v[56:59], v[140:147], v0\ns_and_b32 s75, s74, 127\ns_lshr_b32 s69, s74, 8\nv_cvt_pk_bf16_f32 v54, v42, v43\n" \
    "v_cvt_pk_bf16_f32 v53, v44, v45\nv_smfmac_f32_16x16x64_bf16 v[32:35], v[60:63], v[132:139], v0\ns_sub_u32 s69, s69, s80\ns_bfe_i32 s69, s69, 0x180000\n" \
    "v_cvt_pk_bf16_f32 v55, v46, v47\nv_permlane16_swap_b32 v52, v53\nv_smfmac_f32_16x16x64_bf16 v[36:39], v[60:63], v[148:155], v0\ns_cmp_eq_u32 s75, 0\n" \
    "s_cselect_b32 s69, 0, s69\nv_permlane16_swap_b32 v54, v55\nv_smfmac_f32_16x16x64_bf16 v[32:35], v[64:67], v[156:163], v0\n" \
    "buffer_store_dwordx4 v[52:55], v8, s[64:67], s87 offen offset:768 sc1\ns_add_u32 s80, s80, s69\nv_mov_b64 v[40:41], 0\nv_mov_b64 v[42:43], 0\n" \
    "v_smfmac_f32_16x16x64_bf16 v[36:39], v[64:67], v[172:179], v0\ns_lshl_b32 s74, s74, 5\ns_and_b32 s74, s74, 0x1000\nv_mov_b64 v[44:45], 0\nv_mov_b64 v[46:47], 0\n" \
    "v_smfmac_f32_16x16x64_bf16 v[32:35], v[68:71], v[164:171], v0\ns_andn2_b32 s81, s81, 0x1000\ns_or_b32 s81, s81, s74\nv_lshrrev_b32 v15, 1, v2\nv_add_u32 v15, s60, v15\n" \
    "v_smfmac_f32_16x16x64_bf16 v[36:39], v[68:71], v[180:187], v0\nds_write_b64 v15, v[252:253]\ns_nop 1\nv_cvt_pk_bf16_f32 v48, v32, v33\nv_cvt_pk_bf16_f32 v50, v34, v35\n" \
    "v_smfmac_f32_16x16x64_bf16 v[40:43], v[56:59], v[188:195], v0\ns_nop 1\nv_cvt_pk_bf16_f32 v49, v36, v37\nv_cvt_pk_bf16_f32 v51, v38, v39\n" \
    "v_smfmac_f32_16x16x64_bf16 v[44:47], v[56:59], v[204:211], v0\nv_permlane16_swap_b32 v48, v49\nv_smfmac_f32_16x16x64_bf16 v[40:43], v[60:63], v[196:203], v0\n" \
    "v_permlane16_swap_b32 v50, v51\nv_smfmac_f32_16x16x64_bf16 v[44:47], v[60:63], v[212:219], v0\nbuffer_store_dwordx4 v[48:51], v8, s[64:67], s88 offen offset:768 sc1\n" \
    "v_smfmac_f32_16x16x64_bf16 v[40:43], v[64:67], v[220:227], v0\nv_smfmac_f32_16x16x64_bf16 v[44:47], v[64:67], v[236:243], v0\n" \
    "v_smfmac_f32_16x16x64_bf16 v[40:43], v[68:71], v[228:235], v0\nv_smfmac_f32_16x16x64_bf16 v[44:47], v[68:71], v[244:251], v0\ns_nop 6\nv_cvt_pk_bf16_f32 v52, v40, v41\n" \
    "v_cvt_pk_bf16_f32 v54, v42, v43\nv_cvt_pk_bf16_f32 v53, v44, v45\nv_cvt_pk_bf16_f32 v55, v46, v47\ns_nop 0\nv_permlane16_swap_b32 v52, v53\nv_permlane16_swap_b32 v54, v55\n" \
    "buffer_store_dwordx4 v[52:55], v8, s[64:67], s89 offen offset:768 sc1\ns_waitcnt lgkmcnt(0)\ns_waitcnt vmcnt(0)\nv_mov_b32 v15, s50\ns_mov_b64 exec, 1\n" \
    "buffer_store_dword v15, off, s[64:67], s48 sc1\ns_mov_b64 exec, -1\ns_branch .Lv4_%=\n.Lv6_%=:\ns_endpgm\n.Lv12_%=:\ns_mov_b32 s76, 128\n" \
    "buffer_load_dword v253, off, s[64:67], s76 sc1\ns_waitcnt vmcnt(0)\nv_readfirstlane_b32 s74, v253\ns_nop 0\ns_cmp_ge_u32 s74, 32\ns_cbranch_scc1 .Lv13_%=\ns_sleep 8\n" \
    "s_add_u32 s68, s68, 1\ns_and_b32 s69, s68, 63\ns_cbranch_scc1 .Lv12_%=\ns_cmp_gt_u32 s68, 0x400000\ns_cbranch_scc1 .Lv1_%=\nbuffer_load_dword v253, off, s[64:67], 0 sc1\n" \
    "s_waitcnt vmcnt(0)\nv_readfirstlane_b32 s69, v253\ns_nop 0\ns_cmp_eq_u32 s69, 1\ns_cbranch_scc0 .Lv12_%=\ns_endpgm\n.Lv14_%=:\ns_sleep 6\n" \
    "buffer_load_dword v13, off, s[64:67], s42 sc1\ns_waitcnt vmcnt(0)\nv_readfirstlane_b32 s74, v13\ns_nop 0\ns_and_b32 s75, s74, 127\ns_lshr_b32 s69, s74, 8\n" \
    "s_sub_u32 s69, s69, s80\ns_bfe_i32 s69, s69, 0x180000\ns_cmp_eq_u32 s75, 0\ns_cselect_b32 s69, 0, s69\ns_add_u32 s80, s80, s69\ns_lshl_b32 s74, s74, 5\n" \
    "s_and_b32 s74, s74, 0x1000\ns_andn2_b32 s81, s81, 0x1000\ns_or_b32 s81, s81, s74\ns_add_u32 s68, s68, 1\ns_and_b32 s69, s68, 63\ns_cbranch_scc1 .Lv13_%=\n" \
    "s_cmp_gt_u32 s68, 0x400000\ns_cbranch_scc1 .Lv1_%=\nbuffer_load_dword v253, off, s[64:67], 0 sc1\ns_waitcnt vmcnt(0)\nv_readfirstlane_b32 s69, v253\ns_nop 0\n" \
    "s_cmp_eq_u32 s69, 1\ns_cbranch_scc0 .Lv13_%=\ns_endpgm\n.Lv19_%=:\ns_memrealtime s[72:73]\ns_waitcnt lgkmcnt(0)\ns_sub_u32 s72, s72, s76\ns_cmp_lt_i32 s72, 0\n" \
    "s_cbranch_scc1 .Lv19_%=\nbuffer_load_dword v13, off, s[64:67], s42 sc1\ns_waitcnt vmcnt(0)\nv_readfirstlane_b32 s74, v13\ns_nop 0\ns_and_b32 s75, s74, 127\n" \
    "s_lshr_b32 s69, s74, 8\ns_sub_u32 s69, s69, s80\ns_bfe_i32 s69, s69, 0x180000\ns_cmp_eq_u32 s75, 0\ns_cselect_b32 s69, 0, s69\ns_add_u32 s80, s80, s69\n" \
    "s_lshl_b32 s74, s74, 5\ns_and_b32 s74, s74, 0x1000\ns_andn2_b32 s81, s81, 0x1000\ns_or_b32 s81, s81, s74\ns_branch .Lv16_%=\n.Lv27_%=:\nv_mov_b32 v252, 0\n" \
    "v_max3_u32 v252, v252, v72, v73\nv_max3_u32 v252, v252, v74, v75\nv_max3_u32 v252, v252, v76, v77\nv_max3_u32 v252, v252, v78, v79\nv_cmp_eq_u32 vcc, -1, v252\ns_nop 0\n" \
    "s_cbranch_vccz .Lv23_%=\ns_add_u32 s80, s80, s43\ns_xor_b32 s81, s81, 0x1000\ns_xor_b32 s90, s90, 0x1000\ns_add_u32 s68, s68, 1\ns_and_b32 s69, s68, 63\n" \
    "s_cbranch_scc1 .Lv17_%=\ns_cmp_gt_u32 s68, 0x400000\ns_cbranch_scc1 .Lv1_%=\nbuffer_load_dword v253, off, s[64:67], 0 sc1\ns_waitcnt vmcnt(0)\n" \
    "v_readfirstlane_b32 s69, v253\ns_nop 0\ns_cmp_eq_u32 s69, 1\ns_cbranch_scc0 .Lv17_%=\ns_endpgm\n.Lv28_%=:\nv_mov_b32 v252, 0\nv_max3_u32 v252, v252, v80, v81\n" \
    "v_max3_u32 v252, v252, v82, v83\nv_cmp_eq_u32 vcc, -1, v252\ns_nop 0\ns_cbranch_vccz .Lv24_%=\ns_add_u32 s80, s80, s43\ns_xor_b32 s81, s81, 0x1000\n" \
    "s_xor_b32 s90, s90, 0x1000\n.Lv30_%=:\ns_memrealtime s[72:73]\ns_waitcnt vmcnt(0) lgkmcnt(0)\ns_sub_u32 s72, s72, s80\ns_cmp_lt_i32 s72, 0\ns_cbranch_scc1 .Lv30_%=\n" \
    "buffer_load_dwordx4 v[80:83], v2, s[64:67], s90 offen offset:0 sc1\nbuffer_load_dwordx4 v[84:87], v2, s[64:67], s90 offen offset:1024 sc1\ns_waitcnt vmcnt(0)\n" \
    "s_add_u32 s68, s68, 1\ns_and_b32 s69, s68, 63\ns_cbranch_scc1 .Lv28_%=\ns_cmp_gt_u32 s68, 0x400000\ns_cbranch_scc1 .Lv1_%=\nbuffer_load_dword v253, off, s[64:67], 0 sc1\n" \
    "s_waitcnt vmcnt(0)\nv_readfirstlane_b32 s69, v253\ns_nop 0\ns_cmp_eq_u32 s69, 1\ns_cbranch_scc0 .Lv28_%=\ns_endpgm\n.Lv29_%=:\nv_mov_b32 v252, 0\n" \
    "v_max3_u32 v252, v252, v84, v85\nv_max3_u32 v252, v252, v86, v87\nv_cmp_eq_u32 vcc, -1, v252\ns_nop 0\ns_cbranch_vccz .Lv25_%=\ns_add_u32 s80, s80, s43\n" \
    "s_xor_b32 s81, s81, 0x1000\ns_xor_b32 s90, s90, 0x1000\n.Lv31_%=:\ns_memrealtime s[72:73]\ns_waitcnt vmcnt(0) lgkmcnt(0)\ns_sub_u32 s72, s72, s80\ns_cmp_lt_i32 s72, 0\n" \
    "s_cbranch_scc1 .Lv31_%=\nbuffer_load_dwordx4 v[84:87], v2, s[64:67], s90 offen offset:1024 sc1\ns_waitcnt vmcnt(0)\ns_add_u32 s68, s68, 1\ns_and_b32 s69, s68, 63\n" \
    "s_cbranch_scc1 .Lv29_%=\ns_cmp_gt_u32 s68, 0x400000\ns_cbranch_scc1 .Lv1_%=\nbuffer_load_dword v253, off, s[64:67], 0 sc1\ns_waitcnt vmcnt(0)\n" \
    "v_readfirstlane_b32 s69, v253\ns_nop 0\ns_cmp_eq_u32 s69, 1\ns_cbranch_scc0 .Lv29_%=\ns_endpgm\n.Lv35_%=:\ns_memrealtime s[72:73]\ns_waitcnt lgkmcnt(0)\n" \
    "s_sub_u32 s72, s72, s76\ns_cmp_lt_i32 s72, 0\ns_cbranch_scc1 .Lv35_%=\nbuffer_load_dword v13, off, s[64:67], s42 sc1\ns_waitcnt vmcnt(0)\nv_readfirstlane_b32 s74, v13\n" \
    "s_nop 0\ns_and_b32 s75, s74, 127\ns_lshr_b32 s69, s74, 8\ns_sub_u32 s69, s69, s80\ns_bfe_i32 s69, s69, 0x180000\ns_cmp_eq_u32 s75, 0\ns_cselect_b32 s69, 0, s69\n" \
    "s_add_u32 s80, s80, s69\ns_lshl_b32 s74, s74, 5\ns_and_b32 s74, s74, 0x1000\ns_andn2_b32 s81, s81, 0x1000\ns_or_b32 s81, s81, s74\ns_branch .Lv32_%=\n.Lv43_%=:\n" \
    "v_mov_b32 v252, 0\nv_max3_u32 v252, v252, v72, v73\nv_max3_u32 v252, v252, v74, v75\nv_max3_u32 v252, v252, v76, v77\nv_max3_u32 v252, v252, v78, v79\n" \
    "v_cmp_eq_u32 vcc, -1, v252\ns_nop 0\ns_cbranch_vccz .Lv39_%=\ns_add_u32 s80, s80, s43\ns_xor_b32 s81, s81, 0x1000\ns_xor_b32 s90, s90, 0x1000\ns_add_u32 s68, s68, 1\n" \
    "s_and_b32 s69, s68, 63\ns_cbranch_scc1 .Lv33_%=\ns_cmp_gt_u32 s68, 0x400000\ns_cbranch_scc1 .Lv1_%=\nbuffer_load_dword v253, off, s[64:67], 0 sc1\ns_waitcnt vmcnt(0)\n" \
    "v_readfirstlane_b32 s69, v253\ns_nop 0\ns_cmp_eq_u32 s69, 1\ns_cbranch_scc0 .Lv33_%=\ns_endpgm\n.Lv44_%=:\nv_mov_b32 v252, 0\nv_max3_u32 v252, v252, v80, v81\n" \
    "v_max3_u32 v252, v252, v82, v83\nv_cmp_eq_u32 vcc, -1, v252\ns_nop 0\ns_cbranch_vccz .Lv40_%=\ns_add_u32 s80, s80, s43\ns_xor_b32 s81, s81, 0x1000\n" \
    "s_xor_b32 s90, s90, 0x1000\n.Lv46_%=:\ns_memrealtime s[72:73]\ns_waitcnt vmcnt(0) lgkmcnt(0)\ns_sub_u32 s72, s72, s80\ns_cmp_lt_i32 s72, 0\ns_cbranch_scc1 .Lv46_%=\n" \
    "buffer_load_dwordx4 v[80:83], v2, s[64:67], s90 offen offset:0 sc1\nbuffer_load_dwordx4 v[84:87], v2, s[64:67], s90 offen offset:1024 sc1\ns_waitcnt vmcnt(0)\n" \
    "s_add_u32 s68, s68, 1\ns_and_b32 s69, s68, 63\ns_cbranch_scc1 .Lv44_%=\ns_cmp_gt_u32 s68, 0x400000\ns_cbranch_scc1 .Lv1_%=\nbuffer_load_dword v253, off, s[64:67], 0 sc1\n" \
    "s_waitcnt vmcnt(0)\nv_readfirstlane_b32 s69, v253\ns_nop 0\ns_cmp_eq_u32 s69, 1\ns_cbranch_scc0 .Lv44_%=\ns_endpgm\n.Lv45_%=:\nv_mov_b32 v252, 0\n" \
    "v_max3_u32 v252, v252, v84, v85\nv_max3_u32 v252, v252, v86, v87\nv_cmp_eq_u32 vcc, -1, v252\ns_nop 0\ns_cbranch_vccz .Lv41_%=\ns_add_u32 s80, s80, s43\n" \
    "s_xor_b32 s81, s81, 0x1000\ns_xor_b32 s90, s90, 0x1000\n.Lv47_%=:\ns_memrealtime s[72:73]\ns_waitcnt vmcnt(0) lgkmcnt(0)\ns_sub_u32 s72, s72, s80\ns_cmp_lt_i32 s72, 0\n" \
    "s_cbranch_scc1 .Lv47_%=\nbuffer_load_dwordx4 v[84:87], v2, s[64:67], s90 offen offset:1024 sc1\ns_waitcnt vmcnt(0)\ns_add_u32 s68, s68, 1\ns_and_b32 s69, s68, 63\n" \
    "s_cbranch_scc1 .Lv45_%=\ns_cmp_gt_u32 s68, 0x400000\ns_cbranch_scc1 .Lv1_%=\nbuffer_load_dword v253, off, s[64:67], 0 sc1\ns_waitcnt vmcnt(0)\n" \
    "v_readfirstlane_b32 s69, v253\ns_nop 0\ns_cmp_eq_u32 s69, 1\ns_cbranch_scc0 .Lv45_%=\ns_endpgm\n.Lv51_%=:\ns_memrealtime s[72:73]\ns_waitcnt lgkmcnt(0)\n" \
    "s_sub_u32 s72, s72, s76\ns_cmp_lt_i32 s72, 0\ns_cbranch_scc1 .Lv51_%=\nbuffer_load_dword v13, off, s[64:67], s42 sc1\ns_waitcnt vmcnt(0)\nv_readfirstlane_b32 s74, v13\n" \
    "s_nop 0\ns_and_b32 s75, s74, 127\ns_lshr_b32 s69, s74, 8\ns_sub_u32 s69, s69, s80\ns_bfe_i32 s69, s69, 0x180000\ns_cmp_eq_u32 s75, 0\ns_cselect_b32 s69, 0, s69\n" \
    "s_add_u32 s80, s80, s69\ns_lshl_b32 s74, s74, 5\ns_and_b32 s74, s74, 0x1000\ns_andn2_b32 s81, s81, 0x1000\ns_or_b32 s81, s81, s74\ns_branch .Lv48_%=\n.Lv59_%=:\n" \
    "v_mov_b32 v252, 0\nv_max3_u32 v252, v252, v72, v73\nv_max3_u32 v252, v252, v74, v75\nv_max3_u32 v252, v252, v76, v77\nv_max3_u32 v252, v252, v78, v79\n" \
    "v_cmp_eq_u32 vcc, -1, v252\ns_nop 0\ns_cbranch_vccz .Lv55_%=\ns_add_u32 s80, s80, s43\ns_xor_b32 s81, s81, 0x1000\ns_xor_b32 s90, s90, 0x1000\ns_add_u32 s68, s68, 1\n" \
    "s_and_b32 s69, s68, 63\ns_cbranch_scc1 .Lv49_%=\ns_cmp_gt_u32 s68, 0x400000\ns_cbranch_scc1 .Lv1_%=\nbuffer_load_dword v253, off, s[64:67], 0 sc1\ns_waitcnt vmcnt(0)\n" \
    "v_readfirstlane_b32 s69, v253\ns_nop 0\ns_cmp_eq_u32 s69, 1\ns_cbranch_scc0 .Lv49_%=\ns_endpgm\n.Lv60_%=:\nv_mov_b32 v252, 0\nv_max3_u32 v252, v252, v80, v81\n" \
    "v_max3_u32 v252, v252, v82, v83\nv_cmp_eq_u32 vcc, -1, v252\ns_nop 0\ns_cbranch_vccz .Lv56_%=\ns_add_u32 s80, s80, s43\ns_xor_b32 s81, s81, 0x1000\n" \
    "s_xor_b32 s90, s90, 0x1000\n.Lv62_%=:\ns_memrealtime s[72:73]\ns_waitcnt vmcnt(0) lgkmcnt(0)\ns_sub_u32 s72, s72, s80\ns_cmp_lt_i32 s72, 0\ns_cbranch_scc1 .Lv62_%=\n" \
    "buffer_load_dwordx4 v[80:83], v2, s[64:67], s90 offen offset:0 sc1\nbuffer_load_dwordx4 v[84:87], v2, s[64:67], s90 offen offset:1024 sc1\ns_waitcnt vmcnt(0)\n" \
    "s_add_u32 s68, s68, 1\ns_and_b32 s69, s68, 63\ns_cbranch_scc1 .Lv60_%=\ns_cmp_gt_u32 s68, 0x400000\ns_cbranch_scc1 .Lv1_%=\nbuffer_load_dword v253, off, s[64:67], 0 sc1\n" \
    "s_waitcnt vmcnt(0)\nv_readfirstlane_b32 s69, v253\ns_nop 0\ns_cmp_eq_u32 s69, 1\ns_cbranch_scc0 .Lv60_%=\ns_endpgm\n.Lv61_%=:\nv_mov_b32 v252, 0\n" \
    "v_max3_u32 v252, v252, v84, v85\nv_max3_u32 v252, v252, v86, v87\nv_cmp_eq_u32 vcc, -1, v252\ns_nop 0\ns_cbranch_vccz .Lv57_%=\ns_add_u32 s80, s80, s43\n" \
    "s_xor_b32 s81, s81, 0x1000\ns_xor_b32 s90, s90, 0x1000\n.Lv63_%=:\ns_memrealtime s[72:73]\ns_waitcnt vmcnt(0) lgkmcnt(0)\ns_sub_u32 s72, s72, s80\ns_cmp_lt_i32 s72, 0\n" \
    "s_cbranch_scc1 .Lv63_%=\nbuffer_load_dwordx4 v[84:87], v2, s[64:67], s90 offen offset:1024 sc1\ns_waitcnt vmcnt(0)\ns_add_u32 s68, s68, 1\ns_and_b32 s69, s68, 63\n" \
    "s_cbranch_scc1 .Lv61_%=\ns_cmp_gt_u32 s68, 0x400000\ns_cbranch_scc1 .Lv1_%=\nbuffer_load_dword v253, off, s[64:67], 0 sc1\ns_waitcnt vmcnt(0)\n" \
    "v_readfirstlane_b32 s69, v253\ns_nop 0\ns_cmp_eq_u32 s69, 1\ns_cbranch_scc0 .Lv61_%=\ns_endpgm\n.Lv67_%=:\ns_memrealtime s[72:73]\ns_waitcnt lgkmcnt(0)\n" \
    "s_sub_u32 s72, s72, s76\ns_cmp_lt_i32 s72, 0\ns_cbranch_scc1 .Lv67_%=\nbuffer_load_dword v13, off, s[64:67], s42 sc1\ns_waitcnt vmcnt(0)\nv_readfirstlane_b32 s74, v13\n" \
    "s_nop 0\ns_and_b32 s75, s74, 127\ns_lshr_b32 s69, s74, 8\ns_sub_u32 s69, s69, s80\ns_bfe_i32 s69, s69, 0x180000\ns_cmp_eq_u32 s75, 0\ns_cselect_b32 s69, 0, s69\n" \
    "s_add_u32 s80, s80, s69\ns_lshl_b32 s74, s74, 5\ns_and_b32 s74, s74, 0x1000\ns_andn2_b32 s81, s81, 0x1000\ns_or_b32 s81, s81, s74\ns_branch .Lv64_%=\n.Lv75_%=:\n" \
    "v_mov_b32 v252, 0\nv_max3_u32 v252, v252, v72, v73\nv_max3_u32 v252, v252, v74, v75\nv_max3_u32 v252, v252, v76, v77\nv_max3_u32 v252, v252, v78, v79\n" \
    "v_cmp_eq_u32 vcc, -1, v252\ns_nop 0\ns_cbranch_vccz .Lv71_%=\ns_add_u32 s80, s80, s43\ns_xor_b32 s81, s81, 0x1000\ns_xor_b32 s90, s90, 0x1000\ns_add_u32 s68, s68, 1\n" \
    "s_and_b32 s69, s68, 63\ns_cbranch_scc1 .Lv65_%=\ns_cmp_gt_u32 s68, 0x400000\ns_cbranch_scc1 .Lv1_%=\nbuffer_load_dword v253, off, s[64:67], 0 sc1\ns_waitcnt vmcnt(0)\n" \
    "v_readfirstlane_b32 s69, v253\ns_nop 0\ns_cmp_eq_u32 s69, 1\ns_cbranch_scc0 .Lv65_%=\ns_endpgm\n.Lv76_%=:\nv_mov_b32 v252, 0\nv_max3_u32 v252, v252, v80, v81\n" \
    "v_max3_u32 v252, v252, v82, v83\nv_cmp_eq_u32 vcc, -1, v252\ns_nop 0\ns_cbranch_vccz .Lv72_%=\ns_add_u32 s80, s80, s43\ns_xor_b32 s81, s81, 0x1000\n" \
    "s_xor_b32 s90, s90, 0x1000\n.Lv78_%=:\ns_memrealtime s[72:73]\ns_waitcnt vmcnt(0) lgkmcnt(0)\ns_sub_u32 s72, s72, s80\ns_cmp_lt_i32 s72, 0\ns_cbranch_scc1 .Lv78_%=\n" \
    "buffer_load_dwordx4 v[80:83], v2, s[64:67], s90 offen offset:0 sc1\nbuffer_load_dwordx4 v[84:87], v2, s[64:67], s90 offen offset:1024 sc1\ns_waitcnt vmcnt(0)\n" \
    "s_add_u32 s68, s68, 1\ns_and_b32 s69, s68, 63\ns_cbranch_scc1 .Lv76_%=\ns_cmp_gt_u32 s68, 0x400000\ns_cbranch_scc1 .Lv1_%=\nbuffer_load_dword v253, off, s[64:67], 0 sc1\n" \
    "s_waitcnt vmcnt(0)\nv_readfirstlane_b32 s69, v253\ns_nop 0\ns_cmp_eq_u32 s69, 1\ns_cbranch_scc0 .Lv76_%=\ns_endpgm\n.Lv77_%=:\nv_mov_b32 v252, 0\n" \
    "v_max3_u32 v252, v252, v84, v85\nv_max3_u32 v252, v252, v86, v87\nv_cmp_eq_u32 vcc, -1, v252\ns_nop 0\ns_cbranch_vccz .Lv73_%=\ns_add_u32 s80, s80, s43\n" \
    "s_xor_b32 s81, s81, 0x1000\ns_xor_b32 s90, s90, 0x1000\n.Lv79_%=:\ns_memrealtime s[72:73]\ns_waitcnt vmcnt(0) lgkmcnt(0)\ns_sub_u32 s72, s72, s80\ns_cmp_lt_i32 s72, 0\n" \
    "s_cbranch_scc1 .Lv79_%=\nbuffer_load_dwordx4 v[84:87], v2, s[64:67], s90 offen offset:1024 sc1\ns_waitcnt vmcnt(0)\ns_add_u32 s68, s68, 1\ns_and_b32 s69, s68, 63\n" \
    "s_cbranch_scc1 .Lv77_%=\ns_cmp_gt_u32 s68, 0x400000\ns_cbranch_scc1 .Lv1_%=\nbuffer_load_dword v253, off, s[64:67], 0 sc1\ns_waitcnt vmcnt(0)\n" \
    "v_readfirstlane_b32 s69, v253\ns_nop 0\ns_cmp_eq_u32 s69, 1\ns_cbranch_scc0 .Lv77_%=\ns_endpgm\n.Lv81_%=:\nv_mov_b32 v252, 0\nv_pk_max_u16 v252, v252, v32\n" \
    "v_pk_max_u16 v252, v252, v33\nv_pk_max_u16 v252, v252, v34\nv_pk_max_u16 v252, v252, v35\nv_pk_max_u16 v252, v252, v36\nv_pk_max_u16 v252, v252, v37\n" \
    "v_pk_max_u16 v252, v252, v38\nv_pk_max_u16 v252, v252, v39\nv_pk_max_u16 v252, v252, v40\nv_pk_max_u16 v252, v252, v41\nv_pk_max_u16 v252, v252, v42\n" \
    "v_pk_max_u16 v252, v252, v43\nv_pk_max_u16 v252, v252, v44\nv_pk_max_u16 v252, v252, v45\nv_pk_max_u16 v252, v252, v46\nv_pk_max_u16 v252, v252, v47\n" \
    "v_pk_max_u16 v252, v252, v252 op_sel:[0,1] op_sel_hi:[1,0]\nv_cmp_eq_u32 vcc, -1, v252\ns_nop 0\ns_cbranch_vccz .Lv82_%=\ns_add_u32 s68, s68, 1\ns_and_b32 s69, s68, 63\n" \
    "s_cbranch_scc1 .Lv80_%=\ns_cmp_gt_u32 s68, 0x400000\ns_cbranch_scc1 .Lv1_%=\nbuffer_load_dword v253, off, s[64:67], 0 sc1\ns_waitcnt vmcnt(0)\n" \
    "v_readfirstlane_b32 s69, v253\ns_nop 0\ns_cmp_eq_u32 s69, 1\ns_cbranch_scc0 .Lv80_%=\ns_endpgm\n.Lv1_%=:\nv_mov_b32 v253, 1\nbuffer_store_dword v253, off, s[64:67], 0 sc1\n" \
    "s_endpgm\n"
#define ASM_R \
    ".Lr2_%=:\ns_and_b32 s69, s32, 15\ns_lshl_b32 s69, s69, 21\ns_add_u32 s82, s42, s69\ns_lshr_b32 s69, s82, 8\ns_and_b32 s69, s69, 0x2000\ns_xor_b32 s82, s82, s69\n" \
    "s_and_b32 s69, s32, 15\ns_lshl_b32 s69, s69, 19\ns_add_u32 s83, s43, s69\ns_lshr_b32 s69, s83, 8\ns_and_b32 s69, s69, 0x2000\ns_xor_b32 s83, s83, s69\n" \
    "s_and_b32 s69, s32, 15\ns_lshl_b32 s69, s69, 18\ns_add_u32 s84, s44, s69\ns_lshr_b32 s69, s84, 8\ns_and_b32 s69, s69, 0x2000\ns_xor_b32 s84, s84, s69\n" \
    "s_and_b32 s69, s32, 15\ns_lshl_b32 s69, s69, 18\ns_add_u32 s94, s45, s69\ns_lshr_b32 s69, s94, 8\ns_and_b32 s69, s69, 0x2000\ns_xor_b32 s94, s94, s69\n" \
    "s_add_u32 s69, s32, 1\ns_and_b32 s69, s69, 15\ns_lshl_b32 s69, s69, 17\ns_add_u32 s92, s46, s69\ns_and_b32 s69, s32, 63\ns_lshl_b32 s69, s69, 19\ns_add_u32 s93, s47, s69\n" \
    "s_mov_b32 s68, 0\ns_cmp_lt_u32 s32, 2\ns_cbranch_scc1 .Lr6_%=\ns_sub_u32 s77, s32, 1\n.Lr4_%=:\nds_read_b32 v253, v10\ns_waitcnt lgkmcnt(0)\nv_readfirstlane_b32 s74, v253\n" \
    "s_nop 0\ns_and_b32 s75, s74, 127\ns_cmp_lt_u32 s75, s77\ns_cbranch_scc1 .Lr5_%=\ns_cmp_gt_u32 s75, s77\ns_cbranch_scc1 .Lr6_%=\ns_memrealtime s[72:73]\n" \
    "s_waitcnt lgkmcnt(0)\ns_lshr_b32 s69, s74, 8\ns_sub_u32 s69, s69, s72\ns_bfe_i32 s69, s69, 0x180000\ns_add_u32 s76, s72, s69\ns_add_u32 s76, s76, s58\n.Lr21_%=:\n" \
    "s_memrealtime s[72:73]\ns_waitcnt lgkmcnt(0)\ns_sub_u32 s72, s72, s76\ns_cmp_lt_i32 s72, 0\ns_cbranch_scc1 .Lr21_%=\n.Lr6_%=:\n" \
    "buffer_load_dword v253, v8, s[64:67], 0 offen sc1\ns_waitcnt vmcnt(0)\nv_cmp_ge_u32 vcc, s32, v253\ns_nop 0\ns_cbranch_vccnz .Lr7_%=\ns_cmp_eq_u32 s32, 0\n" \
    "s_cbranch_scc1 .Lr15_%=\n.Lr13_%=:\nds_read_b32 v253, v10\ns_waitcnt lgkmcnt(0)\nv_readfirstlane_b32 s74, v253\ns_nop 0\ns_and_b32 s75, s74, 127\ns_cmp_lt_u32 s75, s32\n" \
    "s_cbranch_scc1 .Lr14_%=\ns_and_b32 s57, s74, 128\ns_memrealtime s[72:73]\ns_waitcnt lgkmcnt(0)\ns_lshr_b32 s69, s74, 8\ns_sub_u32 s69, s69, s72\n" \
    "s_bfe_i32 s69, s69, 0x180000\ns_add_u32 s56, s72, s69\ns_sub_u32 s76, s56, s40\n.Lr22_%=:\ns_memrealtime s[72:73]\ns_waitcnt lgkmcnt(0)\ns_sub_u32 s72, s72, s76\n" \
    "s_cmp_lt_i32 s72, 0\ns_cbranch_scc1 .Lr22_%=\n.Lr15_%=:\nbuffer_load_dwordx4 a[0:3], v11, s[64:67], s82 offen offset:0 sc1\n" \
    "buffer_load_dwordx4 a[16:19], v11, s[64:67], s82 offen offset:512 sc1\nbuffer_load_dwordx4 a[4:7], v11, s[64:67], s82 offen offset:2048 sc1\n" \
    "buffer_load_dwordx4 a[20:23], v11, s[64:67], s82 offen offset:2560 sc1\ns_add_u32 s82, s82, 4096\nbuffer_load_dwordx4 a[8:11], v11, s[64:67], s82 offen offset:0 sc1\n" \
    "buffer_load_dwordx4 a[24:27], v11, s[64:67], s82 offen offset:512 sc1\nbuffer_load_dwordx4 a[12:15], v11, s[64:67], s82 offen offset:2048 sc1\n" \
    "buffer_load_dwordx4 a[28:31], v11, s[64:67], s82 offen offset:2560 sc1\ns_add_u32 s82, s82, 12288\nbuffer_load_dwordx4 a[32:35], v11, s[64:67], s82 offen offset:0 sc1\n" \
    "buffer_load_dwordx4 a[48:51], v11, s[64:67], s82 offen offset:512 sc1\nbuffer_load_dwordx4 a[36:39], v11, s[64:67], s82 offen offset:2048 sc1\n" \
    "buffer_load_dwordx4 a[52:55], v11, s[64:67], s82 offen offset:2560 sc1\ns_add_u32 s82, s82, 4096\nbuffer_load_dwordx4 a[40:43], v11, s[64:67], s82 offen offset:0 sc1\n" \
    "buffer_load_dwordx4 a[56:59], v11, s[64:67], s82 offen offset:512 sc1\nbuffer_load_dwordx4 a[44:47], v11, s[64:67], s82 offen offset:2048 sc1\n" \
    "buffer_load_dwordx4 a[60:63], v11, s[64:67], s82 offen offset:2560 sc1\ns_add_u32 s82, s82, 12288\nbuffer_load_dwordx4 a[64:67], v11, s[64:67], s82 offen offset:0 sc1\n" \
    "buffer_load_dwordx4 a[80:83], v11, s[64:67], s82 offen offset:512 sc1\nbuffer_load_dwordx4 a[68:71], v11, s[64:67], s82 offen offset:2048 sc1\n" \
    "buffer_load_dwordx4 a[84:87], v11, s[64:67], s82 offen offset:2560 sc1\ns_add_u32 s82, s82, 4096\nbuffer_load_dwordx4 a[72:75], v11, s[64:67], s82 offen offset:0 sc1\n" \
    "buffer_load_dwordx4 a[88:91], v11, s[64:67], s82 offen offset:512 sc1\nbuffer_load_dwordx4 a[76:79], v11, s[64:67], s82 offen offset:2048 sc1\n" \
    "buffer_load_dwordx4 a[92:95], v11, s[64:67], s82 offen offset:2560 sc1\ns_add_u32 s82, s82, 12288\nbuffer_load_dwordx4 a[96:99], v11, s[64:67], s82 offen offset:0 sc1\n" \
    "buffer_load_dwordx4 a[112:115], v11, s[64:67], s82 offen offset:512 sc1\nbuffer_load_dwordx4 a[100:103], v11, s[64:67], s82 offen offset:2048 sc1\n" \
    "buffer_load_dwordx4 a[116:119], v11, s[64:67], s82 offen offset:2560 sc1\ns_add_u32 s82, s82, 4096\nbuffer_load_dwordx4 a[104:107], v11, s[64:67], s82 offen offset:0 sc1\n" \
    "buffer_load_dwordx4 a[120:123], v11, s[64:67], s82 offen offset:512 sc1\nbuffer_load_dwordx4 a[108:111], v11, s[64:67], s82 offen offset:2048 sc1\n" \
    "buffer_load_dwordx4 a[124:127], v11, s[64:67], s82 offen offset:2560 sc1\nglobal_load_dwordx4 v[224:227], v7, s[54:55]\ns_cmp_lg_u32 s32, 0\ns_cbranch_scc1 .Lr3_%=\n" \
    "s_memrealtime s[72:73]\ns_waitcnt lgkmcnt(0)\ns_add_u32 s72, s72, s41\n.Lr20_%=:\ns_add_u32 s56, s56, s38\ns_xor_b32 s57, s57, 128\ns_sub_u32 s69, s56, s72\n" \
    "s_cmp_lt_i32 s69, 0\ns_cbranch_scc1 .Lr20_%=\ns_add_u32 s76, s56, s35\ns_cmp_eq_u32 s48, 0\ns_cbranch_scc1 .Lr23_%=\ns_lshl_b32 s74, s76, 8\ns_add_u32 s74, s74, s57\n" \
    "s_add_u32 s74, s74, s32\ns_add_u32 s74, s74, 1\nv_mov_b32 v253, s74\ns_xor_b32 s69, s48, 512\ns_mov_b64 exec, 1\nbuffer_store_dword v253, off, s[64:67], s69 sc1\n" \
    "s_mov_b64 exec, -1\n.Lr23_%=:\n.Lr3_%=:\ns_waitcnt vmcnt(0)\ns_sub_u32 s76, s56, s59\n.Lr25_%=:\ns_memrealtime s[72:73]\ns_waitcnt lgkmcnt(0)\ns_sub_u32 s72, s72, s76\n" \
    "s_cmp_lt_i32 s72, 0\ns_cbranch_scc1 .Lr25_%=\nbuffer_load_dwordx4 v[24:27], off, s[64:67], s60 sc1\ns_sub_u32 s76, s56, s34\ns_mov_b32 s77, 0\ns_waitcnt vmcnt(0)\n" \
    "v_readfirstlane_b32 s74, v24\ns_nop 0\ns_and_b32 s75, s74, 127\ns_cmp_lg_u32 s75, s32\ns_cbranch_scc1 .Lr26_%=\ns_lshr_b32 s69, s74, 8\ns_sub_u32 s69, s69, s76\n" \
    "s_bfe_i32 s69, s69, 0x180000\ns_cmp_le_i32 s69, s77\ns_cbranch_scc1 .Lr26_%=\ns_mov_b32 s77, s69\ns_and_b32 s57, s74, 128\n.Lr26_%=:\nv_readfirstlane_b32 s74, v25\n" \
    "s_nop 0\ns_and_b32 s75, s74, 127\ns_cmp_lg_u32 s75, s32\ns_cbranch_scc1 .Lr27_%=\ns_lshr_b32 s69, s74, 8\ns_sub_u32 s69, s69, s76\ns_bfe_i32 s69, s69, 0x180000\n" \
    "s_cmp_le_i32 s69, s77\ns_cbranch_scc1 .Lr27_%=\ns_mov_b32 s77, s69\ns_and_b32 s57, s74, 128\n.Lr27_%=:\nv_readfirstlane_b32 s74, v26\ns_nop 0\ns_and_b32 s75, s74, 127\n" \
    "s_cmp_lg_u32 s75, s32\ns_cbranch_scc1 .Lr28_%=\ns_lshr_b32 s69, s74, 8\ns_sub_u32 s69, s69, s76\ns_bfe_i32 s69, s69, 0x180000\ns_cmp_le_i32 s69, s77\n" \
    "s_cbranch_scc1 .Lr28_%=\ns_mov_b32 s77, s69\ns_and_b32 s57, s74, 128\n.Lr28_%=:\nv_readfirstlane_b32 s74, v27\ns_nop 0\ns_and_b32 s75, s74, 127\ns_cmp_lg_u32 s75, s32\n" \
    "s_cbranch_scc1 .Lr29_%=\ns_lshr_b32 s69, s74, 8\ns_sub_u32 s69, s69, s76\ns_bfe_i32 s69, s69, 0x180000\ns_cmp_le_i32 s69, s77\ns_cbranch_scc1 .Lr29_%=\ns_mov_b32 s77, s69\n" \
    "s_and_b32 s57, s74, 128\n.Lr29_%=:\ns_cmp_eq_u32 s77, 0\ns_cbranch_scc1 .Lr24_%=\ns_cmp_eq_u32 s32, 0\ns_cbranch_scc1 .Lr24_%=\ns_add_u32 s56, s56, s77\n" \
    "s_add_u32 s76, s56, s35\ns_cmp_eq_u32 s48, 0\ns_cbranch_scc1 .Lr30_%=\ns_lshl_b32 s74, s76, 8\ns_add_u32 s74, s74, s57\ns_add_u32 s74, s74, s32\ns_add_u32 s74, s74, 1\n" \
    "v_mov_b32 v253, s74\ns_xor_b32 s69, s48, 512\ns_mov_b64 exec, 1\nbuffer_store_dword v253, off, s[64:67], s69 sc1\ns_mov_b64 exec, -1\n.Lr30_%=:\n.Lr24_%=:\n" \
    "s_lshl_b32 s69, s57, 7\ns_xor_b32 s84, s84, s69\ns_lshl_b32 s69, s57, 5\ns_xor_b32 s92, s92, s69\ns_add_u32 s75, s83, 4096\ns_mov_b32 s68, 0\n.Lr8_%=:\n" \
    "s_add_u32 s85, s84, 0x8000\ns_add_u32 s86, s84, 0x10000\ns_add_u32 s87, s84, 0x18000\ns_sub_u32 s76, s56, s36\n.Lr31_%=:\ns_memrealtime s[72:73]\ns_waitcnt lgkmcnt(0)\n" \
    "s_sub_u32 s72, s72, s76\ns_cmp_lt_i32 s72, 0\ns_cbranch_scc1 .Lr31_%=\nbuffer_load_dwordx4 v[32:35], v1, s[64:67], s83 offen offset:0 sc1\n" \
    "buffer_load_dwordx4 v[36:39], v1, s[64:67], s83 offen offset:1024 sc1\nbuffer_load_dwordx4 v[40:43], v1, s[64:67], s83 offen offset:2048 sc1\n" \
    "buffer_load_dwordx4 v[44:47], v1, s[64:67], s83 offen offset:3072 sc1\nbuffer_load_dwordx4 v[48:51], v1, s[64:67], s75 offen offset:0 sc1\n" \
    "buffer_load_dwordx4 v[52:55], v1, s[64:67], s75 offen offset:1024 sc1\nbuffer_load_dwordx4 v[56:59], v1, s[64:67], s75 offen offset:2048 sc1\n" \
    "buffer_load_dwordx4 v[60:63], v1, s[64:67], s75 offen offset:3072 sc1\ns_cmp_eq_u32 s32, 63\ns_cbranch_scc1 .Lr32_%=\n.Lr33_%=:\ns_memrealtime s[72:73]\n" \
    "s_waitcnt lgkmcnt(0)\ns_sub_u32 s72, s72, s56\ns_cmp_lt_i32 s72, 0\ns_cbranch_scc1 .Lr33_%=\nbuffer_load_dwordx4 v[68:71], v1, s[64:67], s84 offen offset:0 sc1\n" \
    "buffer_load_dwordx4 v[72:75], v1, s[64:67], s84 offen offset:1024 sc1\nbuffer_load_dwordx4 v[76:79], v1, s[64:67], s84 offen offset:2048 sc1\n" \
    "buffer_load_dwordx4 v[80:83], v1, s[64:67], s84 offen offset:3072 sc1\nbuffer_load_dwordx4 v[88:91], v1, s[64:67], s85 offen offset:0 sc1\n" \
    "buffer_load_dwordx4 v[92:95], v1, s[64:67], s85 offen offset:1024 sc1\nbuffer_load_dwordx4 v[96:99], v1, s[64:67], s85 offen offset:2048 sc1\n" \
    "buffer_load_dwordx4 v[100:103], v1, s[64:67], s85 offen offset:3072 sc1\nbuffer_load_dwordx4 v[108:111], v1, s[64:67], s86 offen offset:0 sc1\n" \
    "buffer_load_dwordx4 v[112:115], v1, s[64:67], s86 offen offset:1024 sc1\nbuffer_load_dwordx4 v[116:119], v1, s[64:67], s86 offen offset:2048 sc1\n" \
    "buffer_load_dwordx4 v[120:123], v1, s[64:67], s86 offen offset:3072 sc1\nbuffer_load_dwordx4 v[128:131], v1, s[64:67], s87 offen offset:0 sc1\n" \
    "buffer_load_dwordx4 v[132:135], v1, s[64:67], s87 offen offset:1024 sc1\nbuffer_load_dwordx4 v[136:139], v1, s[64:67], s87 offen offset:2048 sc1\n" \
    "buffer_load_dwordx4 v[140:143], v1, s[64:67], s87 offen offset:3072 sc1\nbuffer_load_dwordx4 v[64:67], v2, s[64:67], s84 offen offset:0 sc1\n" \
    "buffer_load_dwordx4 v[84:87], v2, s[64:67], s85 offen offset:0 sc1\nbuffer_load_dwordx4 v[104:107], v2, s[64:67], s86 offen offset:0 sc1\n" \
    "buffer_load_dwordx4 v[124:127], v2, s[64:67], s87 offen offset:0 sc1\nv_mov_b64 v[24:25], 0\nv_mov_b64 v[26:27], 0\ns_xor_b32 s77, s92, 0x1000\ns_waitcnt vmcnt(26)\n" \
    "v_permlane16_swap_b32 v32, v33\nv_permlane16_swap_b32 v34, v35\nv_permlane16_swap_b32 v36, v37\nv_permlane16_swap_b32 v38, v39\ns_waitcnt vmcnt(24)\n" \
    "v_permlane16_swap_b32 v40, v41\nv_permlane16_swap_b32 v42, v43\nv_permlane16_swap_b32 v44, v45\nv_permlane16_swap_b32 v46, v47\ns_waitcnt vmcnt(22)\n" \
    "v_permlane16_swap_b32 v48, v49\nv_permlane16_swap_b32 v50, v51\nv_permlane16_swap_b32 v52, v53\nv_permlane16_swap_b32 v54, v55\ns_waitcnt vmcnt(20)\n" \
    "v_permlane16_swap_b32 v56, v57\nv_permlane16_swap_b32 v58, v59\nv_permlane16_swap_b32 v60, v61\nv_permlane16_swap_b32 v62, v63\ns_waitcnt vmcnt(19)\n" \
    "v_smfmac_f32_16x16x64_bf16 v[24:27], v[68:71], a[0:7], v0\ns_waitcnt vmcnt(18)\nv_smfmac_f32_16x16x64_bf16 v[24:27], v[72:75], a[8:15], v0\ns_waitcnt vmcnt(17)\n" \
    "v_smfmac_f32_16x16x64_bf16 v[24:27], v[76:79], a[16:23], v0\ns_waitcnt vmcnt(16)\nv_smfmac_f32_16x16x64_bf16 v[24:27], v[80:83], a[24:31], v0\ns_waitcnt vmcnt(15)\n" \
    "v_smfmac_f32_16x16x64_bf16 v[24:27], v[88:91], a[32:39], v0\ns_waitcnt vmcnt(14)\nv_smfmac_f32_16x16x64_bf16 v[24:27], v[92:95], a[40:47], v0\ns_waitcnt vmcnt(13)\n" \
    "v_smfmac_f32_16x16x64_bf16 v[24:27], v[96:99], a[48:55], v0\ns_waitcnt vmcnt(12)\nv_smfmac_f32_16x16x64_bf16 v[24:27], v[100:103], a[56:63], v0\ns_waitcnt vmcnt(11)\n" \
    "v_smfmac_f32_16x16x64_bf16 v[24:27], v[108:111], a[64:71], v0\ns_waitcnt vmcnt(10)\nv_smfmac_f32_16x16x64_bf16 v[24:27], v[112:115], a[72:79], v0\ns_waitcnt vmcnt(9)\n" \
    "v_smfmac_f32_16x16x64_bf16 v[24:27], v[116:119], a[80:87], v0\ns_waitcnt vmcnt(8)\nv_smfmac_f32_16x16x64_bf16 v[24:27], v[120:123], a[88:95], v0\ns_waitcnt vmcnt(7)\n" \
    "v_smfmac_f32_16x16x64_bf16 v[24:27], v[128:131], a[96:103], v0\ns_waitcnt vmcnt(6)\nv_smfmac_f32_16x16x64_bf16 v[24:27], v[132:135], a[104:111], v0\ns_waitcnt vmcnt(5)\n" \
    "v_smfmac_f32_16x16x64_bf16 v[24:27], v[136:139], a[112:119], v0\ns_waitcnt vmcnt(4)\nv_smfmac_f32_16x16x64_bf16 v[24:27], v[140:143], a[120:127], v0\ns_waitcnt vmcnt(3)\n" \
    "v_smfmac_f32_16x16x64_bf16 v[24:27], v[64:67], v[32:39], v0\ns_waitcnt vmcnt(2)\nv_smfmac_f32_16x16x64_bf16 v[24:27], v[84:87], v[40:47], v0\ns_waitcnt vmcnt(1)\n" \
    "v_smfmac_f32_16x16x64_bf16 v[24:27], v[104:107], v[48:55], v0\ns_waitcnt vmcnt(0)\nv_smfmac_f32_16x16x64_bf16 v[24:27], v[124:127], v[56:63], v0\ns_nop 7\n" \
    "v_permlane32_swap_b32 v24, v26\nv_permlane32_swap_b32 v25, v27\nv_pk_add_f32 v[24:25], v[24:25], v[26:27]\nv_cvt_pk_bf16_f32 v26, v24, v25\nds_write_b32 v3, v26\n" \
    "ds_read_b64_tr_b16 v[28:29], v4\nds_read_b64_tr_b16 v[30:31], v4 offset:64\nv_cmp_u_f32 vcc, v24, v25\ns_nop 0\ns_cbranch_vccnz .Lr17_%=\n.Lr16_%=:\ns_waitcnt lgkmcnt(0)\n" \
    "buffer_store_dwordx4 v[28:31], v6, s[64:67], s92 offen sc1\nbuffer_store_dwordx4 v[28:31], v6, s[64:67], s77 offen sc1\n.Lr10_%=:\ns_memrealtime s[72:73]\n" \
    "s_waitcnt lgkmcnt(0)\ns_add_u32 s76, s56, s35\n.Lr35_%=:\ns_sub_u32 s69, s76, s72\ns_cmp_ge_i32 s69, s37\ns_cbranch_scc1 .Lr36_%=\ns_add_u32 s76, s76, s38\n" \
    "s_xor_b32 s57, s57, 128\ns_branch .Lr35_%=\n.Lr36_%=:\ns_add_u32 s76, s76, s34\ns_lshl_b32 s74, s76, 8\ns_add_u32 s74, s74, s57\ns_add_u32 s74, s74, s32\n" \
    "s_add_u32 s74, s74, 1\nv_mov_b32 v253, s74\nds_write_b32 v10, v253\ns_add_u32 s76, s76, s35\ns_cmp_eq_u32 s48, 0\ns_cbranch_scc1 .Lr37_%=\ns_lshl_b32 s74, s76, 8\n" \
    "s_add_u32 s74, s74, s57\ns_add_u32 s74, s74, s32\ns_add_u32 s74, s74, 2\nv_mov_b32 v253, s74\ns_xor_b32 s69, s48, 0\ns_mov_b64 exec, 1\n" \
    "buffer_store_dword v253, off, s[64:67], s69 sc1\ns_mov_b64 exec, -1\n.Lr37_%=:\ns_xor_b32 s69, s49, 0x20000\ns_add_u32 s0, s94, s69\ns_add_u32 s1, s0, 0x4000\n" \
    "s_add_u32 s2, s0, 0x8000\ns_add_u32 s3, s0, 0xc000\ns_xor_b32 s69, s49, 0x30000\ns_add_u32 s4, s94, s69\ns_add_u32 s5, s4, 0x4000\ns_add_u32 s6, s4, 0x8000\n" \
    "s_add_u32 s7, s4, 0xc000\ns_xor_b32 s69, s49, 0x0\ns_add_u32 s8, s94, s69\ns_add_u32 s9, s8, 0x4000\ns_add_u32 s10, s8, 0x8000\ns_add_u32 s11, s8, 0xc000\n" \
    "s_xor_b32 s69, s49, 0x10000\ns_add_u32 s12, s94, s69\ns_add_u32 s13, s12, 0x4000\ns_add_u32 s14, s12, 0x8000\ns_add_u32 s15, s12, 0xc000\ns_cmp_ge_u32 s32, 62\n" \
    "s_cselect_b32 s76, s61, s39\ns_add_u32 s76, s76, s56\ns_mov_b32 s68, 0\ns_mov_b32 s80, 0\n.Lr38_%=:\ns_memrealtime s[72:73]\ns_waitcnt lgkmcnt(0)\ns_sub_u32 s72, s72, s76\n" \
    "s_cmp_lt_i32 s72, 0\ns_cbranch_scc1 .Lr38_%=\n.Lr11_%=:\nv_mov_b64 v[24:25], 0\nv_mov_b64 v[26:27], 0\nv_mov_b64 v[28:29], 0\nv_mov_b64 v[30:31], 0\n" \
    "v_mov_b64 v[228:229], 0\nv_mov_b64 v[230:231], 0\nv_mov_b64 v[232:233], 0\nv_mov_b64 v[234:235], 0\nv_mov_b32 v252, 0\nv_mov_b32 v250, 0\n" \
    "buffer_load_dwordx4 v[64:67], v2, s[64:67], s0 offen offset:0 sc1\nbuffer_load_dwordx4 v[84:87], v2, s[64:67], s1 offen offset:0 sc1\n" \
    "buffer_load_dwordx4 v[104:107], v2, s[64:67], s2 offen offset:0 sc1\nbuffer_load_dwordx4 v[124:127], v2, s[64:67], s3 offen offset:0 sc1\n" \
    "buffer_load_dwordx4 v[144:147], v2, s[64:67], s4 offen offset:0 sc1\nbuffer_load_dwordx4 v[148:151], v2, s[64:67], s5 offen offset:0 sc1\n" \
    "buffer_load_dwordx4 v[152:155], v2, s[64:67], s6 offen offset:0 sc1\nbuffer_load_dwordx4 v[156:159], v2, s[64:67], s7 offen offset:0 sc1\n" \
    "buffer_load_dwordx4 v[160:163], v2, s[64:67], s8 offen offset:0 sc1\nbuffer_load_dwordx4 v[164:167], v2, s[64:67], s9 offen offset:0 sc1\n" \
    "buffer_load_dwordx4 v[168:171], v2, s[64:67], s10 offen offset:0 sc1\nbuffer_load_dwordx4 v[172:175], v2, s[64:67], s11 offen offset:0 sc1\n" \
    "buffer_load_dwordx4 v[176:179], v2, s[64:67], s12 offen offset:0 sc1\nbuffer_load_dwordx4 v[180:183], v2, s[64:67], s13 offen offset:0 sc1\n" \
    "buffer_load_dwordx4 v[184:187], v2, s[64:67], s14 offen offset:0 sc1\nbuffer_load_dwordx4 v[188:191], v2, s[64:67], s15 offen offset:0 sc1\n" \
    "buffer_load_dwordx4 v[68:71], v1, s[64:67], s0 offen offset:0 sc1\nbuffer_load_dwordx4 v[72:75], v1, s[64:67], s0 offen offset:1024 sc1\n" \
    "buffer_load_dwordx4 v[76:79], v1, s[64:67], s0 offen offset:2048 sc1\nbuffer_load_dwordx4 v[80:83], v1, s[64:67], s0 offen offset:3072 sc1\n" \
    "buffer_load_dwordx4 v[88:91], v1, s[64:67], s1 offen offset:0 sc1\nbuffer_load_dwordx4 v[92:95], v1, s[64:67], s1 offen offset:1024 sc1\n" \
    "buffer_load_dwordx4 v[96:99], v1, s[64:67], s1 offen offset:2048 sc1\nbuffer_load_dwordx4 v[100:103], v1, s[64:67], s1 offen offset:3072 sc1\n" \
    "buffer_load_dwordx4 v[108:111], v1, s[64:67], s2 offen offset:0 sc1\nbuffer_load_dwordx4 v[112:115], v1, s[64:67], s2 offen offset:1024 sc1\n" \
    "buffer_load_dwordx4 v[116:119], v1, s[64:67], s2 offen offset:2048 sc1\nbuffer_load_dwordx4 v[120:123], v1, s[64:67], s2 offen offset:3072 sc1\n" \
    "buffer_load_dwordx4 v[128:131], v1, s[64:67], s3 offen offset:0 sc1\nbuffer_load_dwordx4 v[132:135], v1, s[64:67], s3 offen offset:1024 sc1\n" \
    "buffer_load_dwordx4 v[136:139], v1, s[64:67], s3 offen offset:2048 sc1\nbuffer_load_dwordx4 v[140:143], v1, s[64:67], s3 offen offset:3072 sc1\ns_waitcnt vmcnt(30)\n" \
    "v_smfmac_f32_16x16x64_bf16 v[24:27], v[64:67], v[32:39], v0\nbuffer_load_dwordx4 a[128:131], v1, s[64:67], s4 offen offset:0 sc1\n" \
    "v_smfmac_f32_16x16x64_bf16 v[24:27], v[84:87], v[40:47], v0\nbuffer_load_dwordx4 a[132:135], v1, s[64:67], s4 offen offset:1024 sc1\ns_waitcnt vmcnt(30)\n" \
    "v_smfmac_f32_16x16x64_bf16 v[24:27], v[104:107], v[48:55], v0\nbuffer_load_dwordx4 a[136:139], v1, s[64:67], s4 offen offset:2048 sc1\nv_max3_u32 v250, v250, v64, v84\n" \
    "v_smfmac_f32_16x16x64_bf16 v[24:27], v[124:127], v[56:63], v0\nbuffer_load_dwordx4 a[140:143], v1, s[64:67], s4 offen offset:3072 sc1\ns_waitcnt vmcnt(30)\n" \
    "v_max3_u32 v250, v250, v104, v124\nv_smfmac_f32_16x16x64_bf16 v[28:31], v[144:147], v[32:39], v0\nbuffer_load_dwordx4 a[144:147], v1, s[64:67], s5 offen offset:0 sc1\n" \
    "v_smfmac_f32_16x16x64_bf16 v[28:31], v[148:151], v[40:47], v0\nbuffer_load_dwordx4 a[148:151], v1, s[64:67], s5 offen offset:1024 sc1\ns_waitcnt vmcnt(30)\n" \
    "v_smfmac_f32_16x16x64_bf16 v[28:31], v[152:155], v[48:55], v0\nbuffer_load_dwordx4 a[152:155], v1, s[64:67], s5 offen offset:2048 sc1\nv_max3_u32 v250, v250, v144, v148\n" \
    "v_smfmac_f32_16x16x64_bf16 v[28:31], v[156:159], v[56:63], v0\nbuffer_load_dwordx4 a[156:159], v1, s[64:67], s5 offen offset:3072 sc1\ns_waitcnt vmcnt(30)\n" \
    "v_max3_u32 v250, v250, v152, v156\nv_smfmac_f32_16x16x64_bf16 v[228:231], v[160:163], v[32:39], v0\nbuffer_load_dwordx4 a[160:163], v1, s[64:67], s6 offen offset:0 sc1\n" \
    "v_smfmac_f32_16x16x64_bf16 v[228:231], v[164:167], v[40:47], v0\nbuffer_load_dwordx4 a[164:167], v1, s[64:67], s6 offen offset:1024 sc1\ns_waitcnt vmcnt(30)\n" \
    "v_smfmac_f32_16x16x64_bf16 v[228:231], v[168:171], v[48:55], v0\nbuffer_load_dwordx4 a[168:171], v1, s[64:67], s6 offen offset:2048 sc1\nv_max3_u32 v250, v250, v160, v164\n" \
    "v_smfmac_f32_16x16x64_bf16 v[228:231], v[172:175], v[56:63], v0\nbuffer_load_dwordx4 a[172:175], v1, s[64:67], s6 offen offset:3072 sc1\ns_waitcnt vmcnt(30)\n" \
    "v_max3_u32 v250, v250, v168, v172\nv_smfmac_f32_16x16x64_bf16 v[232:235], v[176:179], v[32:39], v0\nbuffer_load_dwordx4 a[176:179], v1, s[64:67], s7 offen offset:0 sc1\n" \
    "v_smfmac_f32_16x16x64_bf16 v[232:235], v[180:183], v[40:47], v0\nbuffer_load_dwordx4 a[180:183], v1, s[64:67], s7 offen offset:1024 sc1\ns_waitcnt vmcnt(30)\n" \
    "v_smfmac_f32_16x16x64_bf16 v[232:235], v[184:187], v[48:55], v0\nbuffer_load_dwordx4 a[184:187], v1, s[64:67], s7 offen offset:2048 sc1\nv_max3_u32 v250, v250, v176, v180\n" \
    "v_smfmac_f32_16x16x64_bf16 v[232:235], v[188:191], v[56:63], v0\nbuffer_load_dwordx4 a[188:191], v1, s[64:67], s7 offen offset:3072 sc1\ns_waitcnt vmcnt(30)\n" \
    "v_max3_u32 v250, v250, v184, v188\nv_smfmac_f32_16x16x64_bf16 v[24:27], v[68:71], a[0:7], v0\nbuffer_load_dwordx4 a[192:195], v1, s[64:67], s8 offen offset:0 sc1\n" \
    "v_cmp_eq_u32 vcc, -1, v250\nv_smfmac_f32_16x16x64_bf16 v[24:27], v[72:75], a[8:15], v0\nbuffer_load_dwordx4 a[196:199], v1, s[64:67], s8 offen offset:1024 sc1\n" \
    "s_waitcnt vmcnt(30)\nv_smfmac_f32_16x16x64_bf16 v[24:27], v[76:79], a[16:23], v0\nbuffer_load_dwordx4 a[200:203], v1, s[64:67], s9 offen offset:0 sc1\n" \
    "s_cbranch_vccnz .Lr39_%=\nv_smfmac_f32_16x16x64_bf16 v[24:27], v[80:83], a[24:31], v0\nbuffer_load_dwordx4 a[204:207], v1, s[64:67], s9 offen offset:1024 sc1\n" \
    "s_waitcnt vmcnt(30)\nv_smfmac_f32_16x16x64_bf16 v[24:27], v[88:91], a[32:39], v0\nbuffer_load_dwordx4 a[208:211], v1, s[64:67], s10 offen offset:0 sc1\n" \
    "v_smfmac_f32_16x16x64_bf16 v[24:27], v[92:95], a[40:47], v0\nbuffer_load_dwordx4 a[212:215], v1, s[64:67], s10 offen offset:1024 sc1\ns_waitcnt vmcnt(30)\n" \
    "v_smfmac_f32_16x16x64_bf16 v[24:27], v[96:99], a[48:55], v0\nbuffer_load_dwordx4 a[216:219], v1, s[64:67], s11 offen offset:0 sc1\n" \
    "v_smfmac_f32_16x16x64_bf16 v[24:27], v[100:103], a[56:63], v0\nbuffer_load_dwordx4 a[220:223], v1, s[64:67], s11 offen offset:1024 sc1\ns_waitcnt vmcnt(30)\n" \
    "v_smfmac_f32_16x16x64_bf16 v[24:27], v[108:111], a[64:71], v0\nbuffer_load_dwordx4 a[224:227], v1, s[64:67], s12 offen offset:0 sc1\n" \
    "v_smfmac_f32_16x16x64_bf16 v[24:27], v[112:115], a[72:79], v0\nbuffer_load_dwordx4 a[228:231], v1, s[64:67], s12 offen offset:1024 sc1\ns_waitcnt vmcnt(30)\n" \
    "v_smfmac_f32_16x16x64_bf16 v[24:27], v[116:119], a[80:87], v0\nbuffer_load_dwordx4 a[232:235], v1, s[64:67], s13 offen offset:0 sc1\n" \
    "v_smfmac_f32_16x16x64_bf16 v[24:27], v[120:123], a[88:95], v0\nbuffer_load_dwordx4 a[236:239], v1, s[64:67], s13 offen offset:1024 sc1\ns_waitcnt vmcnt(30)\n" \
    "v_smfmac_f32_16x16x64_bf16 v[24:27], v[128:131], a[96:103], v0\nbuffer_load_dwordx4 a[240:243], v1, s[64:67], s14 offen offset:0 sc1\n" \
    "v_smfmac_f32_16x16x64_bf16 v[24:27], v[132:135], a[104:111], v0\nbuffer_load_dwordx4 a[244:247], v1, s[64:67], s14 offen offset:1024 sc1\ns_waitcnt vmcnt(30)\n" \
    "v_smfmac_f32_16x16x64_bf16 v[24:27], v[136:139], a[112:119], v0\nbuffer_load_dwordx4 a[248:251], v1, s[64:67], s15 offen offset:0 sc1\n" \
    "v_smfmac_f32_16x16x64_bf16 v[24:27], v[140:143], a[120:127], v0\nbuffer_load_dwordx4 a[252:255], v1, s[64:67], s15 offen offset:1024 sc1\ns_nop 1\ns_waitcnt vmcnt(30)\n" \
    "v_smfmac_f32_16x16x64_bf16 v[28:31], a[128:131], a[0:7], v0\ns_nop 2\nv_permlane32_swap_b32 v24, v26\nv_smfmac_f32_16x16x64_bf16 v[28:31], a[132:135], a[8:15], v0\n" \
    "s_waitcnt vmcnt(28)\nv_permlane32_swap_b32 v25, v27\nv_smfmac_f32_16x16x64_bf16 v[28:31], a[136:139], a[16:23], v0\nv_add_f32 v24, v24, v26\n" \
    "v_smfmac_f32_16x16x64_bf16 v[28:31], a[140:143], a[24:31], v0\ns_waitcnt vmcnt(26)\nv_add_f32 v25, v25, v27\nv_smfmac_f32_16x16x64_bf16 v[28:31], a[144:147], a[32:39], v0\n" \
    "v_fma_f32 v252, v24, v25, v252\nv_smfmac_f32_16x16x64_bf16 v[28:31], a[148:151], a[40:47], v0\ns_waitcnt vmcnt(24)\nv_cvt_pk_bf16_f32 v24, v24, v25\n" \
    "v_smfmac_f32_16x16x64_bf16 v[28:31], a[152:155], a[48:55], v0\nds_write_b32 v3, v24 offset:512\nv_smfmac_f32_16x16x64_bf16 v[28:31], a[156:159], a[56:63], v0\n" \
    "s_waitcnt vmcnt(22)\nv_smfmac_f32_16x16x64_bf16 v[28:31], a[160:163], a[64:71], v0\nv_smfmac_f32_16x16x64_bf16 v[28:31], a[164:167], a[72:79], v0\ns_waitcnt vmcnt(20)\n" \
    "v_smfmac_f32_16x16x64_bf16 v[28:31], a[168:171], a[80:87], v0\nv_smfmac_f32_16x16x64_bf16 v[28:31], a[172:175], a[88:95], v0\ns_waitcnt vmcnt(18)\n" \
    "v_smfmac_f32_16x16x64_bf16 v[28:31], a[176:179], a[96:103], v0\nv_smfmac_f32_16x16x64_bf16 v[28:31], a[180:183], a[104:111], v0\ns_waitcnt vmcnt(16)\n" \
    "v_smfmac_f32_16x16x64_bf16 v[28:31], a[184:187], a[112:119], v0\nv_smfmac_f32_16x16x64_bf16 v[28:31], a[188:191], a[120:127], v0\ns_nop 2\ns_waitcnt vmcnt(14)\n" \
    "v_smfmac_f32_16x16x64_bf16 v[228:231], a[192:195], a[0:7], v0\ns_nop 2\nv_permlane32_swap_b32 v28, v30\nv_smfmac_f32_16x16x64_bf16 v[228:231], a[196:199], a[8:15], v0\n" \
    "s_waitcnt vmcnt(12)\nv_permlane32_swap_b32 v29, v31\nv_smfmac_f32_16x16x64_bf16 v[228:231], a[200:203], a[32:39], v0\nv_add_f32 v28, v28, v30\n" \
    "v_smfmac_f32_16x16x64_bf16 v[228:231], a[204:207], a[40:47], v0\ns_waitcnt vmcnt(10)\nv_add_f32 v29, v29, v31\n" \
    "v_smfmac_f32_16x16x64_bf16 v[228:231], a[208:211], a[64:71], v0\nv_fma_f32 v252, v28, v29, v252\nv_smfmac_f32_16x16x64_bf16 v[228:231], a[212:215], a[72:79], v0\n" \
    "s_waitcnt vmcnt(8)\nv_cvt_pk_bf16_f32 v28, v28, v29\nv_smfmac_f32_16x16x64_bf16 v[228:231], a[216:219], a[96:103], v0\nds_write_b32 v3, v28 offset:768\n" \
    "v_smfmac_f32_16x16x64_bf16 v[228:231], a[220:223], a[104:111], v0\ns_nop 2\ns_waitcnt vmcnt(6)\nv_smfmac_f32_16x16x64_bf16 v[232:235], a[224:227], a[0:7], v0\ns_nop 2\n" \
    "v_permlane32_swap_b32 v228, v230\nv_smfmac_f32_16x16x64_bf16 v[232:235], a[228:231], a[8:15], v0\ns_waitcnt vmcnt(4)\nv_permlane32_swap_b32 v229, v231\n" \
    "v_smfmac_f32_16x16x64_bf16 v[232:235], a[232:235], a[32:39], v0\nv_add_f32 v228, v228, v230\nv_smfmac_f32_16x16x64_bf16 v[232:235], a[236:239], a[40:47], v0\n" \
    "s_waitcnt vmcnt(2)\nv_add_f32 v229, v229, v231\nv_smfmac_f32_16x16x64_bf16 v[232:235], a[240:243], a[64:71], v0\nv_fma_f32 v252, v228, v229, v252\n" \
    "v_smfmac_f32_16x16x64_bf16 v[232:235], a[244:247], a[72:79], v0\ns_waitcnt vmcnt(0)\nv_cvt_pk_bf16_f32 v228, v228, v229\n" \
    "v_smfmac_f32_16x16x64_bf16 v[232:235], a[248:251], a[96:103], v0\nds_write_b32 v3, v228 offset:0\nv_smfmac_f32_16x16x64_bf16 v[232:235], a[252:255], a[104:111], v0\n" \
    "s_nop 7\nv_permlane32_swap_b32 v232, v234\nv_permlane32_swap_b32 v233, v235\nv_add_f32 v232, v232, v234\nv_add_f32 v233, v233, v235\nv_fma_f32 v252, v232, v233, v252\n" \
    "v_cvt_pk_bf16_f32 v232, v232, v233\nv_cmp_u_f32 vcc, v252, v252\nds_write_b32 v3, v232 offset:256\nds_read_b64_tr_b16 v[24:25], v5\n" \
    "ds_read_b64_tr_b16 v[26:27], v5 offset:64\ns_cbranch_vccnz .Lr12_%=\n.Lr18_%=:\ns_cmp_lt_u32 s32, 4\ns_cbranch_scc1 .Lr40_%=\n.Lr41_%=:\nv_lshlrev_b32 v72, 16, v224\n" \
    "v_and_b32 v73, 0xffff0000, v224\nv_lshlrev_b32 v74, 16, v225\nv_and_b32 v75, 0xffff0000, v225\nv_lshlrev_b32 v76, 16, v226\nv_and_b32 v77, 0xffff0000, v226\n" \
    "v_lshlrev_b32 v78, 16, v227\nv_and_b32 v79, 0xffff0000, v227\ns_xor_b32 s77, s93, 0x2000\ns_waitcnt lgkmcnt(0)\nv_lshlrev_b32 v64, 16, v24\nv_and_b32 v65, 0xffff0000, v24\n" \
    "v_lshlrev_b32 v66, 16, v25\nv_and_b32 v67, 0xffff0000, v25\nv_lshlrev_b32 v68, 16, v26\nv_and_b32 v69, 0xffff0000, v26\nv_lshlrev_b32 v70, 16, v27\n" \
    "v_and_b32 v71, 0xffff0000, v27\nv_mul_f32 v64, v64, v16\nv_mul_f32 v65, v65, v17\nv_mul_f32 v66, v66, v18\nv_mul_f32 v67, v67, v19\nv_mul_f32 v68, v68, v20\n" \
    "v_mul_f32 v69, v69, v21\nv_mul_f32 v70, v70, v22\nv_mul_f32 v71, v71, v23\nv_cvt_pk_bf16_f32 v64, 0, v64\nv_cvt_pk_bf16_f32 v65, 0, v65\nv_cvt_pk_bf16_f32 v66, 0, v66\n" \
    "v_cvt_pk_bf16_f32 v67, 0, v67\nv_cvt_pk_bf16_f32 v68, 0, v68\nv_cvt_pk_bf16_f32 v69, 0, v69\nv_cvt_pk_bf16_f32 v70, 0, v70\nv_cvt_pk_bf16_f32 v71, 0, v71\n" \
    "v_add_f32 v64, v64, v72\nv_add_f32 v65, v65, v73\nv_add_f32 v66, v66, v74\nv_add_f32 v67, v67, v75\nv_add_f32 v68, v68, v76\nv_add_f32 v69, v69, v77\n" \
    "v_add_f32 v70, v70, v78\nv_add_f32 v71, v71, v79\nv_cvt_pk_bf16_f32 v24, v64, v65\nv_cvt_pk_bf16_f32 v25, v66, v67\nv_cvt_pk_bf16_f32 v26, v68, v69\n" \
    "v_cvt_pk_bf16_f32 v27, v70, v71\nv_min_u32 v28, -2, v24\nv_min_u32 v29, -2, v25\nv_min_u32 v30, -2, v26\nv_min_u32 v31, -2, v27\ns_cmp_lt_u32 s32, 62\n" \
    "s_cbranch_scc1 .Lr42_%=\nbuffer_store_dwordx4 v[28:31], v1, s[64:67], s93 offen sc1\nbuffer_store_dwordx4 v[28:31], v1, s[64:67], s77 offen sc1\n.Lr42_%=:\n" \
    "v_lshlrev_b32 v64, 16, v24\nv_and_b32 v65, 0xffff0000, v24\nv_lshlrev_b32 v66, 16, v25\nv_and_b32 v67, 0xffff0000, v25\nv_lshlrev_b32 v68, 16, v26\n" \
    "v_and_b32 v69, 0xffff0000, v26\nv_lshlrev_b32 v70, 16, v27\nv_and_b32 v71, 0xffff0000, v27\nv_add_f32 v72, v64, v65\nv_add_f32 v73, v66, v67\nv_add_f32 v74, v68, v69\n" \
    "v_add_f32 v75, v70, v71\nv_add_f32 v72, v72, v73\nv_add_f32 v74, v74, v75\nv_add_f32 v76, v72, v74\nv_add_f32 v77, v72, v74\ns_nop 1\nv_permlane32_swap_b32 v76, v77\n" \
    "v_add_f32 v76, v76, v77\nv_mul_f32 v76, 0x3d800000, v76\nv_sub_f32 v64, v64, v76\nv_sub_f32 v65, v65, v76\nv_sub_f32 v66, v66, v76\nv_sub_f32 v67, v67, v76\n" \
    "v_sub_f32 v68, v68, v76\nv_sub_f32 v69, v69, v76\nv_sub_f32 v70, v70, v76\nv_sub_f32 v71, v71, v76\nv_mul_f32 v72, v64, v64\nv_mul_f32 v73, v65, v65\n" \
    "v_fmac_f32 v72, v66, v66\nv_fmac_f32 v73, v67, v67\nv_fmac_f32 v72, v68, v68\nv_fmac_f32 v73, v69, v69\nv_fmac_f32 v72, v70, v70\nv_fmac_f32 v73, v71, v71\n" \
    "v_add_f32 v74, v72, v73\nv_add_f32 v75, v72, v73\ns_nop 1\nv_permlane32_swap_b32 v74, v75\nv_add_f32 v77, v74, v75\nv_min_u32 v76, -2, v76\nv_min_u32 v77, -2, v77\n" \
    "s_cmp_ge_u32 s32, 62\ns_cbranch_scc1 .Lr43_%=\ns_add_u32 s76, s32, 2\n.Lr45_%=:\nds_read_b32 v253, v10\ns_waitcnt lgkmcnt(0)\nv_readfirstlane_b32 s74, v253\ns_nop 0\n" \
    "s_and_b32 s75, s74, 127\ns_cmp_lt_u32 s75, s76\ns_cbranch_scc1 .Lr46_%=\nbuffer_store_dwordx4 v[28:31], v1, s[64:67], s93 offen sc1\n" \
    "buffer_store_dwordx4 v[28:31], v1, s[64:67], s77 offen sc1\n.Lr43_%=:\nbuffer_store_dwordx2 v[76:77], v9, s[64:67], s93 offen sc1\ns_cmp_ge_u32 s32, 56\n" \
    "s_cbranch_scc1 .Lr47_%=\nv_mov_b64 v[80:81], -1\nv_mov_b64 v[82:83], -1\ns_add_u32 s69, s32, 9\ns_and_b32 s69, s69, 15\ns_lshl_b32 s69, s69, 17\ns_add_u32 s92, s46, s69\n" \
    "s_xor_b32 s74, s92, 0x1000\nbuffer_store_dwordx4 v[80:83], v6, s[64:67], s92 offen sc1\nbuffer_store_dwordx4 v[80:83], v6, s[64:67], s74 offen sc1\n.Lr47_%=:\n" \
    "v_add_u32 v7, 0x40000, v7\ns_add_u32 s32, s32, 4\ns_cmp_lt_u32 s32, 64\ns_cbranch_scc1 .Lr2_%=\ns_endpgm\n.Lr5_%=:\ns_sleep 2\ns_add_u32 s68, s68, 1\n" \
    "s_and_b32 s69, s68, 63\ns_cbranch_scc1 .Lr4_%=\ns_cmp_gt_u32 s68, 0x400000\ns_cbranch_scc1 .Lr1_%=\nbuffer_load_dword v253, off, s[64:67], 0 sc1\ns_waitcnt vmcnt(0)\n" \
    "v_readfirstlane_b32 s69, v253\ns_nop 0\ns_cmp_eq_u32 s69, 1\ns_cbranch_scc0 .Lr4_%=\ns_endpgm\n.Lr7_%=:\ns_sleep 6\ns_add_u32 s68, s68, 1\ns_and_b32 s69, s68, 63\n" \
    "s_cbranch_scc1 .Lr6_%=\ns_cmp_gt_u32 s68, 0x400000\ns_cbranch_scc1 .Lr1_%=\nbuffer_load_dword v253, off, s[64:67], 0 sc1\ns_waitcnt vmcnt(0)\n" \
    "v_readfirstlane_b32 s69, v253\ns_nop 0\ns_cmp_eq_u32 s69, 1\ns_cbranch_scc0 .Lr6_%=\ns_endpgm\n.Lr14_%=:\ns_sleep 2\ns_add_u32 s68, s68, 1\ns_and_b32 s69, s68, 63\n" \
    "s_cbranch_scc1 .Lr13_%=\ns_cmp_gt_u32 s68, 0x400000\ns_cbranch_scc1 .Lr1_%=\nbuffer_load_dword v253, off, s[64:67], 0 sc1\ns_waitcnt vmcnt(0)\n" \
    "v_readfirstlane_b32 s69, v253\ns_nop 0\ns_cmp_eq_u32 s69, 1\ns_cbranch_scc0 .Lr13_%=\ns_endpgm\n.Lr32_%=:\ns_waitcnt vmcnt(0)\nv_permlane16_swap_b32 v32, v33\n" \
    "v_permlane16_swap_b32 v34, v35\nv_permlane16_swap_b32 v36, v37\nv_permlane16_swap_b32 v38, v39\nv_permlane16_swap_b32 v40, v41\nv_permlane16_swap_b32 v42, v43\n" \
    "v_permlane16_swap_b32 v44, v45\nv_permlane16_swap_b32 v46, v47\nv_permlane16_swap_b32 v48, v49\nv_permlane16_swap_b32 v50, v51\nv_permlane16_swap_b32 v52, v53\n" \
    "v_permlane16_swap_b32 v54, v55\nv_permlane16_swap_b32 v56, v57\nv_permlane16_swap_b32 v58, v59\nv_permlane16_swap_b32 v60, v61\nv_permlane16_swap_b32 v62, v63\n" \
    "s_branch .Lr10_%=\n.Lr17_%=:\ns_cmp_lt_u32 s68, 3\ns_cbranch_scc1 .Lr9_%=\nv_max3_u32 v252, v32, v33, v34\nv_max3_u32 v253, v35, v36, v37\nv_max3_u32 v252, v252, v38, v39\n" \
    "v_max3_u32 v253, v253, v40, v41\nv_max3_u32 v252, v252, v42, v43\nv_max3_u32 v253, v253, v44, v45\nv_max3_u32 v252, v252, v46, v47\nv_max3_u32 v253, v253, v48, v49\n" \
    "v_max3_u32 v252, v252, v50, v51\nv_max3_u32 v253, v253, v52, v53\nv_max3_u32 v252, v252, v54, v55\nv_max3_u32 v253, v253, v56, v57\nv_max3_u32 v252, v252, v58, v59\n" \
    "v_max3_u32 v253, v253, v60, v61\nv_max3_u32 v252, v252, v62, v63\nv_max3_u32 v252, v252, v64, v65\nv_max3_u32 v253, v253, v66, v67\nv_max3_u32 v252, v252, v68, v69\n" \
    "v_max3_u32 v253, v253, v70, v71\nv_max3_u32 v252, v252, v72, v73\nv_max3_u32 v253, v253, v74, v75\nv_max3_u32 v252, v252, v76, v77\nv_max3_u32 v253, v253, v78, v79\n" \
    "v_max3_u32 v252, v252, v80, v81\nv_max3_u32 v253, v253, v82, v83\nv_max3_u32 v252, v252, v84, v85\nv_max3_u32 v253, v253, v86, v87\nv_max3_u32 v252, v252, v88, v89\n" \
    "v_max3_u32 v253, v253, v90, v91\nv_max3_u32 v252, v252, v92, v93\nv_max3_u32 v253, v253, v94, v95\nv_max3_u32 v252, v252, v96, v97\nv_max3_u32 v253, v253, v98, v99\n" \
    "v_max3_u32 v252, v252, v100, v101\nv_max3_u32 v253, v253, v102, v103\nv_max3_u32 v252, v252, v104, v105\nv_max3_u32 v253, v253, v106, v107\n" \
    "v_max3_u32 v252, v252, v108, v109\nv_max3_u32 v253, v253, v110, v111\nv_max3_u32 v252, v252, v112, v113\nv_max3_u32 v253, v253, v114, v115\n" \
    "v_max3_u32 v252, v252, v116, v117\nv_max3_u32 v253, v253, v118, v119\nv_max3_u32 v252, v252, v120, v121\nv_max3_u32 v253, v253, v122, v123\n" \
    "v_max3_u32 v252, v252, v124, v125\nv_max3_u32 v253, v253, v126, v127\nv_max3_u32 v252, v252, v128, v129\nv_max3_u32 v253, v253, v130, v131\n" \
    "v_max3_u32 v252, v252, v132, v133\nv_max3_u32 v253, v253, v134, v135\nv_max3_u32 v252, v252, v136, v137\nv_max3_u32 v253, v253, v138, v139\n" \
    "v_max3_u32 v252, v252, v140, v141\nv_max3_u32 v253, v253, v142, v143\nv_max_u32 v252, v252, v253\nv_cmp_eq_u32 vcc, -1, v252\ns_nop 0\ns_cbranch_vccz .Lr16_%=\n.Lr9_%=:\n" \
    "s_add_u32 s56, s56, s38\ns_xor_b32 s57, s57, 128\ns_xor_b32 s84, s84, 0x4000\ns_xor_b32 s92, s92, 0x1000\ns_add_u32 s76, s56, s35\ns_cmp_eq_u32 s48, 0\n" \
    "s_cbranch_scc1 .Lr34_%=\ns_lshl_b32 s74, s76, 8\ns_add_u32 s74, s74, s57\ns_add_u32 s74, s74, s32\ns_add_u32 s74, s74, 1\nv_mov_b32 v253, s74\ns_xor_b32 s69, s48, 512\n" \
    "s_mov_b64 exec, 1\nbuffer_store_dword v253, off, s[64:67], s69 sc1\ns_mov_b64 exec, -1\n.Lr34_%=:\ns_add_u32 s68, s68, 1\ns_and_b32 s69, s68, 63\ns_cbranch_scc1 .Lr8_%=\n" \
    "s_cmp_gt_u32 s68, 0x400000\ns_cbranch_scc1 .Lr1_%=\nbuffer_load_dword v253, off, s[64:67], 0 sc1\ns_waitcnt vmcnt(0)\nv_readfirstlane_b32 s69, v253\ns_nop 0\n" \
    "s_cmp_eq_u32 s69, 1\ns_cbranch_scc0 .Lr8_%=\ns_endpgm\n.Lr39_%=:\ns_waitcnt vmcnt(0)\ns_add_u32 s68, s68, 1\ns_and_b32 s69, s68, 63\ns_cbranch_scc1 .Lr11_%=\n" \
    "s_cmp_gt_u32 s68, 0x400000\ns_cbranch_scc1 .Lr1_%=\nbuffer_load_dword v253, off, s[64:67], 0 sc1\ns_waitcnt vmcnt(0)\nv_readfirstlane_b32 s69, v253\ns_nop 0\n" \
    "s_cmp_eq_u32 s69, 1\ns_cbranch_scc0 .Lr11_%=\ns_endpgm\n.Lr12_%=:\ns_cmp_lg_u32 s80, 0\ns_cbranch_scc1 .Lr18_%=\ns_sleep 4\n" \
    "buffer_load_dwordx4 v[32:35], v1, s[64:67], s83 offen offset:0 sc1\nbuffer_load_dwordx4 v[36:39], v1, s[64:67], s83 offen offset:1024 sc1\n" \
    "buffer_load_dwordx4 v[40:43], v1, s[64:67], s83 offen offset:2048 sc1\nbuffer_load_dwordx4 v[44:47], v1, s[64:67], s83 offen offset:3072 sc1\n" \
    "buffer_load_dwordx4 v[48:51], v1, s[64:67], s75 offen offset:0 sc1\nbuffer_load_dwordx4 v[52:55], v1, s[64:67], s75 offen offset:1024 sc1\n" \
    "buffer_load_dwordx4 v[56:59], v1, s[64:67], s75 offen offset:2048 sc1\nbuffer_load_dwordx4 v[60:63], v1, s[64:67], s75 offen offset:3072 sc1\ns_waitcnt vmcnt(0)\n" \
    "v_permlane16_swap_b32 v32, v33\nv_permlane16_swap_b32 v34, v35\nv_permlane16_swap_b32 v36, v37\nv_permlane16_swap_b32 v38, v39\nv_permlane16_swap_b32 v40, v41\n" \
    "v_permlane16_swap_b32 v42, v43\nv_permlane16_swap_b32 v44, v45\nv_permlane16_swap_b32 v46, v47\nv_permlane16_swap_b32 v48, v49\nv_permlane16_swap_b32 v50, v51\n" \
    "v_permlane16_swap_b32 v52, v53\nv_permlane16_swap_b32 v54, v55\nv_permlane16_swap_b32 v56, v57\nv_permlane16_swap_b32 v58, v59\nv_permlane16_swap_b32 v60, v61\n" \
    "v_permlane16_swap_b32 v62, v63\nv_max3_u32 v252, v32, v33, v34\nv_max3_u32 v253, v35, v36, v37\nv_max3_u32 v252, v252, v38, v39\nv_max3_u32 v253, v253, v40, v41\n" \
    "v_max3_u32 v252, v252, v42, v43\nv_max3_u32 v253, v253, v44, v45\nv_max3_u32 v252, v252, v46, v47\nv_max3_u32 v253, v253, v48, v49\nv_max3_u32 v252, v252, v50, v51\n" \
    "v_max3_u32 v253, v253, v52, v53\nv_max3_u32 v252, v252, v54, v55\nv_max3_u32 v253, v253, v56, v57\nv_max3_u32 v252, v252, v58, v59\nv_max3_u32 v253, v253, v60, v61\n" \
    "v_max3_u32 v252, v252, v62, v63\ns_mov_b32 s76, s94\n.Lr19_%=:\ns_add_u32 s84, s76, 0x0\ns_add_u32 s85, s76, 0x4000\ns_add_u32 s86, s76, 0x8000\n" \
    "s_add_u32 s87, s76, 0xc000\nbuffer_load_dwordx4 v[64:67], v2, s[64:67], s84 offen offset:0 sc1\nbuffer_load_dwordx4 v[68:71], v1, s[64:67], s84 offen offset:0 sc1\n" \
    "buffer_load_dwordx4 v[72:75], v1, s[64:67], s84 offen offset:1024 sc1\nbuffer_load_dwordx4 v[76:79], v1, s[64:67], s84 offen offset:2048 sc1\n" \
    "buffer_load_dwordx4 v[80:83], v1, s[64:67], s84 offen offset:3072 sc1\nbuffer_load_dwordx4 v[84:87], v2, s[64:67], s85 offen offset:0 sc1\n" \
    "buffer_load_dwordx4 v[88:91], v1, s[64:67], s85 offen offset:0 sc1\nbuffer_load_dwordx4 v[92:95], v1, s[64:67], s85 offen offset:1024 sc1\n" \
    "buffer_load_dwordx4 v[96:99], v1, s[64:67], s85 offen offset:2048 sc1\nbuffer_load_dwordx4 v[100:103], v1, s[64:67], s85 offen offset:3072 sc1\n" \
    "buffer_load_dwordx4 v[104:107], v2, s[64:67], s86 offen offset:0 sc1\nbuffer_load_dwordx4 v[108:111], v1, s[64:67], s86 offen offset:0 sc1\n" \
    "buffer_load_dwordx4 v[112:115], v1, s[64:67], s86 offen offset:1024 sc1\nbuffer_load_dwordx4 v[116:119], v1, s[64:67], s86 offen offset:2048 sc1\n" \
    "buffer_load_dwordx4 v[120:123], v1, s[64:67], s86 offen offset:3072 sc1\nbuffer_load_dwordx4 v[124:127], v2, s[64:67], s87 offen offset:0 sc1\n" \
    "buffer_load_dwordx4 v[128:131], v1, s[64:67], s87 offen offset:0 sc1\nbuffer_load_dwordx4 v[132:135], v1, s[64:67], s87 offen offset:1024 sc1\n" \
    "buffer_load_dwordx4 v[136:139], v1, s[64:67], s87 offen offset:2048 sc1\nbuffer_load_dwordx4 v[140:143], v1, s[64:67], s87 offen offset:3072 sc1\ns_waitcnt vmcnt(0)\n" \
    "v_max3_u32 v252, v252, v64, v65\nv_max3_u32 v253, v253, v66, v67\nv_max3_u32 v252, v252, v68, v69\nv_max3_u32 v253, v253, v70, v71\nv_max3_u32 v252, v252, v72, v73\n" \
    "v_max3_u32 v253, v253, v74, v75\nv_max3_u32 v252, v252, v76, v77\nv_max3_u32 v253, v253, v78, v79\nv_max3_u32 v252, v252, v80, v81\nv_max3_u32 v253, v253, v82, v83\n" \
    "v_max3_u32 v252, v252, v84, v85\nv_max3_u32 v253, v253, v86, v87\nv_max3_u32 v252, v252, v88, v89\nv_max3_u32 v253, v253, v90, v91\nv_max3_u32 v252, v252, v92, v93\n" \
    "v_max3_u32 v253, v253, v94, v95\nv_max3_u32 v252, v252, v96, v97\nv_max3_u32 v253, v253, v98, v99\nv_max3_u32 v252, v252, v100, v101\nv_max3_u32 v253, v253, v102, v103\n" \
    "v_max3_u32 v252, v252, v104, v105\nv_max3_u32 v253, v253, v106, v107\nv_max3_u32 v252, v252, v108, v109\nv_max3_u32 v253, v253, v110, v111\n" \
    "v_max3_u32 v252, v252, v112, v113\nv_max3_u32 v253, v253, v114, v115\nv_max3_u32 v252, v252, v116, v117\nv_max3_u32 v253, v253, v118, v119\n" \
    "v_max3_u32 v252, v252, v120, v121\nv_max3_u32 v253, v253, v122, v123\nv_max3_u32 v252, v252, v124, v125\nv_max3_u32 v253, v253, v126, v127\n" \
    "v_max3_u32 v252, v252, v128, v129\nv_max3_u32 v253, v253, v130, v131\nv_max3_u32 v252, v252, v132, v133\nv_max3_u32 v253, v253, v134, v135\n" \
    "v_max3_u32 v252, v252, v136, v137\nv_max3_u32 v253, v253, v138, v139\nv_max3_u32 v252, v252, v140, v141\nv_max3_u32 v253, v253, v142, v143\ns_add_u32 s76, s76, 0x10000\n" \
    "s_sub_u32 s69, s76, s94\ns_cmp_lt_u32 s69, 0x40000\ns_cbranch_scc1 .Lr19_%=\nv_max_u32 v252, v252, v253\nv_cmp_eq_u32 vcc, -1, v252\ns_nop 0\ns_cmp_eq_u64 vcc, 0\n" \
    "s_cselect_b32 s80, 1, 0\ns_add_u32 s68, s68, 1\ns_and_b32 s69, s68, 63\ns_cbranch_scc1 .Lr11_%=\ns_cmp_gt_u32 s68, 0x400000\ns_cbranch_scc1 .Lr1_%=\n" \
    "buffer_load_dword v253, off, s[64:67], 0 sc1\ns_waitcnt vmcnt(0)\nv_readfirstlane_b32 s69, v253\ns_nop 0\ns_cmp_eq_u32 s69, 1\ns_cbranch_scc0 .Lr11_%=\ns_endpgm\n" \
    ".Lr40_%=:\nbuffer_load_dwordx4 v[16:19], v12, s[64:67], 0 offen offset:0 sc1\nbuffer_load_dwordx4 v[20:23], v12, s[64:67], 0 offen offset:16 sc1\ns_waitcnt vmcnt(0)\n" \
    "v_max3_u32 v252, v16, v17, v18\nv_max3_u32 v253, v19, v20, v21\nv_max3_u32 v252, v252, v22, v23\nv_max_u32 v252, v252, v253\nv_cmp_eq_u32 vcc, -1, v252\ns_nop 0\n" \
    "s_cbranch_vccz .Lr41_%=\ns_sleep 8\ns_add_u32 s68, s68, 1\ns_and_b32 s69, s68, 63\ns_cbranch_scc1 .Lr40_%=\ns_cmp_gt_u32 s68, 0x400000\ns_cbranch_scc1 .Lr1_%=\n" \
    "buffer_load_dword v253, off, s[64:67], 0 sc1\ns_waitcnt vmcnt(0)\nv_readfirstlane_b32 s69, v253\ns_nop 0\ns_cmp_eq_u32 s69, 1\ns_cbranch_scc0 .Lr40_%=\ns_endpgm\n" \
    ".Lr46_%=:\ns_sleep 2\ns_add_u32 s68, s68, 1\ns_and_b32 s69, s68, 63\ns_cbranch_scc1 .Lr45_%=\ns_cmp_gt_u32 s68, 0x400000\ns_cbranch_scc1 .Lr1_%=\n" \
    "buffer_load_dword v253, off, s[64:67], 0 sc1\ns_waitcnt vmcnt(0)\nv_readfirstlane_b32 s69, v253\ns_nop 0\ns_cmp_eq_u32 s69, 1\ns_cbranch_scc0 .Lr45_%=\ns_endpgm\n.Lr1_%=:\n" \
    "v_mov_b32 v253, 1\nbuffer_store_dword v253, off, s[64:67], 0 sc1\ns_endpgm\n"
#define ASM_F1 \
    "s_mov_b32 s90, 0x7fff0000\ns_mov_b32 s91, 0x7fff0000\ns_mov_b32 s92, 0x7fff0000\ns_mov_b32 s93, 0x7fff0000\ns_mov_b32 s80, 0\ns_mov_b32 s81, 0\ns_mov_b32 s68, 0\n.Lf2_%=:\n" \
    "s_xor_b32 s85, s38, 512\ns_and_b32 s69, s32, 63\ns_lshl_b32 s69, s69, 19\ns_add_u32 s82, s36, s69\ns_lshr_b32 s69, s82, 8\ns_and_b32 s69, s69, 0x2000\n" \
    "s_xor_b32 s82, s82, s69\ns_add_u32 s69, s32, 1\ns_and_b32 s69, s69, 63\ns_lshl_b32 s69, s69, 19\ns_add_u32 s83, s36, s69\ns_lshr_b32 s69, s83, 8\n" \
    "s_and_b32 s69, s69, 0x2000\ns_xor_b32 s83, s83, s69\ns_add_u32 s84, s37, s80\ns_cmp_eq_u32 s81, 1\ns_cbranch_scc1 .Lf6_%=\ns_add_u32 s77, s32, 2\ns_cmp_eq_u32 s81, 2\n" \
    "s_cbranch_scc1 .Lf5_%=\n.Lf3_%=:\nbuffer_load_dword v150, off, s[64:67], s38 sc1\ns_waitcnt vmcnt(0)\nv_and_b32 v112, 127, v150\nv_cmp_gt_u32 vcc, s77, v112\ns_nop 0\n" \
    "s_cbranch_vccnz .Lf4_%=\n.Lf5_%=:\nv_and_b32 v112, 127, v150\n.Lf11_%=:\ns_sleep 1\ns_memrealtime s[72:73]\ns_waitcnt lgkmcnt(0)\ns_cmp_ge_u32 s77, 64\n" \
    "s_cselect_b32 s69, s60, s34\ns_sub_u32 s72, s72, s69\ns_lshl_b32 s72, s72, 8\nv_sub_u32 v113, s72, v150\nv_cmp_gt_i32 s[74:75], 0, v113\nv_cmp_eq_u32 vcc, s77, v112\n" \
    "s_nop 0\ns_and_b64 vcc, vcc, s[74:75]\ns_cbranch_scc1 .Lf11_%=\n.Lf12_%=:\nbuffer_load_dword v151, v2, s[64:67], s82 offen sc1\ns_add_u32 m0, s84, 0\n" \
    "s_add_u32 s69, s82, s48\nbuffer_load_dwordx4 v0, s[64:67], s69 offen offset:0 sc1 lds\nbuffer_load_dwordx4 v0, s[64:67], s69 offen offset:1024 sc1 lds\n" \
    "s_add_u32 m0, s84, 8192\ns_add_u32 s69, s82, s49\nbuffer_load_dwordx4 v0, s[64:67], s69 offen offset:0 sc1 lds\n" \
    "buffer_load_dwordx4 v0, s[64:67], s69 offen offset:1024 sc1 lds\ns_add_u32 m0, s84, 16384\ns_add_u32 s69, s82, s50\n" \
    "buffer_load_dwordx4 v0, s[64:67], s69 offen offset:0 sc1 lds\nbuffer_load_dwordx4 v0, s[64:67], s69 offen offset:1024 sc1 lds\n.Lf6_%=:\ns_waitcnt vmcnt(6)\n" \
    "v_cmp_eq_u32 vcc, -1, v151\ns_nop 0\ns_cbranch_vccnz .Lf13_%=\ns_cmp_lg_u32 s33, 0\ns_cbranch_scc1 .Lf14_%=\ns_memrealtime s[72:73]\ns_waitcnt lgkmcnt(0)\n" \
    "s_lshl_b32 s72, s72, 8\ns_add_u32 s72, s72, s32\ns_add_u32 s72, s72, 1\nv_mov_b32 v112, s72\ns_mov_b64 exec, 1\nbuffer_store_dword v112, off, s[64:67], s39 sc1\n" \
    "s_mov_b64 exec, -1\n.Lf14_%=:\ns_add_u32 s77, s32, 3\ns_add_u32 m0, s84, 24576\ns_add_u32 s69, s82, s51\nbuffer_load_dwordx4 v0, s[64:67], s69 offen offset:0 sc1 lds\n" \
    "buffer_load_dwordx4 v0, s[64:67], s69 offen offset:1024 sc1 lds\ns_waitcnt vmcnt(6)\ns_barrier\nds_read_b128 v[64:67], v1 offset:0\nds_read_b128 v[68:71], v1 offset:1024\n" \
    "ds_read_b128 v[72:75], v1 offset:2048\nds_read_b128 v[76:79], v1 offset:3072\ns_waitcnt lgkmcnt(3)\nv_mfma_f32_32x32x16_bf16 v[48:63], a[0:3], v[64:67], 0\n" \
    "ds_read_b128 v[80:83], v1 offset:4096\ns_add_u32 m0, s84, 32768\ns_add_u32 s69, s82, s52\nbuffer_load_dwordx4 v0, s[64:67], s69 offen offset:0 sc1 lds\n" \
    "s_waitcnt lgkmcnt(3)\nv_mfma_f32_32x32x16_bf16 v[48:63], a[4:7], v[68:71], v[48:63]\nds_read_b128 v[84:87], v1 offset:5120\ns_waitcnt lgkmcnt(3)\n" \
    "v_mfma_f32_32x32x16_bf16 v[48:63], a[8:11], v[72:75], v[48:63]\nds_read_b128 v[88:91], v1 offset:6144\nbuffer_load_dwordx4 v0, s[64:67], s69 offen offset:1024 sc1 lds\n" \
    "s_waitcnt lgkmcnt(3)\nv_mfma_f32_32x32x16_bf16 v[48:63], a[12:15], v[76:79], v[48:63]\nds_read_b128 v[92:95], v1 offset:7168\ns_waitcnt vmcnt(6) lgkmcnt(3)\ns_barrier\n" \
    "v_mfma_f32_32x32x16_bf16 v[48:63], a[16:19], v[80:83], v[48:63]\nds_read_b128 v[64:67], v1 offset:8192\ns_add_u32 s69, s82, s41\n" \
    "buffer_load_dwordx4 v[96:99], v0, s[64:67], s69 offen offset:0 sc1\ns_waitcnt lgkmcnt(3)\nv_mfma_f32_32x32x16_bf16 v[48:63], a[20:23], v[84:87], v[48:63]\n" \
    "ds_read_b128 v[68:71], v1 offset:9216\ns_waitcnt lgkmcnt(3)\nv_mfma_f32_32x32x16_bf16 v[48:63], a[24:27], v[88:91], v[48:63]\nds_read_b128 v[72:75], v1 offset:10240\n" \
    "buffer_load_dwordx4 v[100:103], v0, s[64:67], s69 offen offset:1024 sc1\ns_waitcnt lgkmcnt(3)\nv_mfma_f32_32x32x16_bf16 v[48:63], a[28:31], v[92:95], v[48:63]\n" \
    "ds_read_b128 v[76:79], v1 offset:11264\ns_waitcnt lgkmcnt(3)\nv_mfma_f32_32x32x16_bf16 v[48:63], a[32:35], v[64:67], v[48:63]\nds_read_b128 v[80:83], v1 offset:12288\n" \
    "buffer_load_dwordx4 v[104:107], v0, s[64:67], s69 offen offset:2048 sc1\ns_waitcnt lgkmcnt(3)\nv_mfma_f32_32x32x16_bf16 v[48:63], a[36:39], v[68:71], v[48:63]\n" \
    "ds_read_b128 v[84:87], v1 offset:13312\ns_waitcnt lgkmcnt(3)\nv_mfma_f32_32x32x16_bf16 v[48:63], a[40:43], v[72:75], v[48:63]\nds_read_b128 v[88:91], v1 offset:14336\n" \
    "buffer_load_dwordx4 v[108:111], v0, s[64:67], s69 offen offset:3072 sc1\ns_waitcnt lgkmcnt(3)\nv_mfma_f32_32x32x16_bf16 v[48:63], a[44:47], v[76:79], v[48:63]\n" \
    "ds_read_b128 v[92:95], v1 offset:15360\ns_waitcnt vmcnt(6) lgkmcnt(3)\ns_barrier\nv_mfma_f32_32x32x16_bf16 v[48:63], a[48:51], v[80:83], v[48:63]\n" \
    "ds_read_b128 v[64:67], v1 offset:16384\ns_add_u32 m0, s84, 40960\ns_add_u32 s69, s82, s53\nbuffer_load_dwordx4 v0, s[64:67], s69 offen offset:0 sc1 lds\n" \
    "s_waitcnt lgkmcnt(3)\nv_mfma_f32_32x32x16_bf16 v[48:63], a[52:55], v[84:87], v[48:63]\nds_read_b128 v[68:71], v1 offset:17408\ns_waitcnt lgkmcnt(3)\n" \
    "v_mfma_f32_32x32x16_bf16 v[48:63], a[56:59], v[88:91], v[48:63]\nds_read_b128 v[72:75], v1 offset:18432\nbuffer_load_dwordx4 v0, s[64:67], s69 offen offset:1024 sc1 lds\n" \
    "s_waitcnt lgkmcnt(3)\nv_mfma_f32_32x32x16_bf16 v[48:63], a[60:63], v[92:95], v[48:63]\nds_read_b128 v[76:79], v1 offset:19456\ns_waitcnt lgkmcnt(3)\n" \
    "v_mfma_f32_32x32x16_bf16 v[48:63], a[64:67], v[64:67], v[48:63]\nds_read_b128 v[80:83], v1 offset:20480\ns_add_u32 m0, s84, 49152\ns_add_u32 s69, s82, s54\n" \
    "buffer_load_dwordx4 v0, s[64:67], s69 offen offset:0 sc1 lds\ns_waitcnt lgkmcnt(3)\nv_mfma_f32_32x32x16_bf16 v[48:63], a[68:71], v[68:71], v[48:63]\n" \
    "ds_read_b128 v[84:87], v1 offset:21504\ns_waitcnt lgkmcnt(3)\nv_mfma_f32_32x32x16_bf16 v[48:63], a[72:75], v[72:75], v[48:63]\nds_read_b128 v[88:91], v1 offset:22528\n" \
    "buffer_load_dwordx4 v0, s[64:67], s69 offen offset:1024 sc1 lds\ns_waitcnt lgkmcnt(3)\nv_mfma_f32_32x32x16_bf16 v[48:63], a[76:79], v[76:79], v[48:63]\n" \
    "ds_read_b128 v[92:95], v1 offset:23552\ns_waitcnt lgkmcnt(3)\nv_mfma_f32_32x32x16_bf16 v[48:63], a[80:83], v[80:83], v[48:63]\nds_read_b128 v[64:67], v1 offset:24576\n" \
    "s_add_u32 m0, s84, 57344\ns_add_u32 s69, s82, s55\nbuffer_load_dwordx4 v0, s[64:67], s69 offen offset:0 sc1 lds\ns_waitcnt lgkmcnt(3)\n" \
    "v_mfma_f32_32x32x16_bf16 v[48:63], a[84:87], v[84:87], v[48:63]\nds_read_b128 v[68:71], v1 offset:25600\ns_waitcnt lgkmcnt(3)\n" \
    "v_mfma_f32_32x32x16_bf16 v[48:63], a[88:91], v[88:91], v[48:63]\nds_read_b128 v[72:75], v1 offset:26624\nbuffer_load_dwordx4 v0, s[64:67], s69 offen offset:1024 sc1 lds\n" \
    "s_waitcnt lgkmcnt(3)\nv_mfma_f32_32x32x16_bf16 v[48:63], a[92:95], v[92:95], v[48:63]\nds_read_b128 v[76:79], v1 offset:27648\ns_waitcnt lgkmcnt(3)\n" \
    "v_mfma_f32_32x32x16_bf16 v[48:63], a[96:99], v[64:67], v[48:63]\nds_read_b128 v[80:83], v1 offset:28672\ns_and_b32 s69, s32, 63\ns_lshl_b32 s69, s69, 19\n" \
    "s_add_u32 s82, s40, s69\ns_lshr_b32 s69, s82, 8\ns_and_b32 s69, s69, 0x2000\ns_waitcnt lgkmcnt(3)\nv_mfma_f32_32x32x16_bf16 v[48:63], a[100:103], v[68:71], v[48:63]\n" \
    "ds_read_b128 v[84:87], v1 offset:29696\ns_xor_b32 s82, s82, s69\ns_waitcnt lgkmcnt(3)\nv_mfma_f32_32x32x16_bf16 v[48:63], a[104:107], v[72:75], v[48:63]\n" \
    "ds_read_b128 v[88:91], v1 offset:30720\ns_waitcnt vmcnt(6)\ns_waitcnt lgkmcnt(3)\nv_max3_u32 v116, v96, v97, v98\nv_max3_u32 v116, v116, v99, v100\n" \
    "v_max3_u32 v116, v116, v101, v102\nv_max3_u32 v116, v116, v103, v104\nv_max3_u32 v116, v116, v105, v106\nv_mfma_f32_32x32x16_bf16 v[48:63], a[108:111], v[76:79], v[48:63]\n" \
    "ds_read_b128 v[92:95], v1 offset:31744\ns_waitcnt vmcnt(4) lgkmcnt(3)\ns_barrier\nv_max3_u32 v116, v116, v107, v108\nv_max3_u32 v116, v116, v109, v110\n" \
    "v_max_u32 v116, v116, v111\nv_cmp_eq_u32 s[70:71], -1, v116\nv_sub_f32 v114, v96, v100\nv_mfma_f32_32x32x16_bf16 v[48:63], a[112:115], v[80:83], v[48:63]\n" \
    "ds_read_b128 v[64:67], v1 offset:32768\ns_waitcnt lgkmcnt(3)\nv_add_f32 v96, v96, v100\nv_add_f32 v97, v97, v101\nv_mul_f32 v114, v114, v114\nv_mul_f32 v96, 0.5, v96\n" \
    "v_fmac_f32 v97, 0x41000000, v114\nv_mfma_f32_32x32x16_bf16 v[48:63], a[116:119], v[84:87], v[48:63]\nds_read_b128 v[68:71], v1 offset:33792\ns_waitcnt lgkmcnt(3)\n" \
    "v_sub_f32 v114, v104, v108\nv_add_f32 v104, v104, v108\nv_add_f32 v105, v105, v109\nv_mul_f32 v114, v114, v114\nv_mul_f32 v104, 0.5, v104\n" \
    "v_mfma_f32_32x32x16_bf16 v[48:63], a[120:123], v[88:91], v[48:63]\nds_read_b128 v[72:75], v1 offset:34816\ns_waitcnt lgkmcnt(3)\nv_fmac_f32 v105, 0x41000000, v114\n" \
    "v_sub_f32 v114, v96, v104\nv_add_f32 v96, v96, v104\nv_add_f32 v97, v97, v105\nv_mul_f32 v114, v114, v114\n" \
    "v_mfma_f32_32x32x16_bf16 v[48:63], a[124:127], v[92:95], v[48:63]\nds_read_b128 v[76:79], v1 offset:35840\ns_waitcnt lgkmcnt(3)\nv_mul_f32 v96, 0.5, v96\n" \
    "v_fmac_f32 v97, 0x41800000, v114\nv_sub_f32 v114, v98, v102\nv_add_f32 v98, v98, v102\nv_add_f32 v99, v99, v103\n" \
    "v_mfma_f32_32x32x16_bf16 v[48:63], a[128:131], v[64:67], v[48:63]\nds_read_b128 v[80:83], v1 offset:36864\ns_waitcnt lgkmcnt(3)\nv_mul_f32 v114, v114, v114\n" \
    "v_mul_f32 v98, 0.5, v98\nv_fmac_f32 v99, 0x41000000, v114\nv_sub_f32 v114, v106, v110\nv_add_f32 v106, v106, v110\n" \
    "v_mfma_f32_32x32x16_bf16 v[48:63], a[132:135], v[68:71], v[48:63]\nds_read_b128 v[84:87], v1 offset:37888\ns_waitcnt lgkmcnt(3)\nv_add_f32 v107, v107, v111\n" \
    "v_mul_f32 v114, v114, v114\nv_mul_f32 v106, 0.5, v106\nv_fmac_f32 v107, 0x41000000, v114\nv_sub_f32 v114, v98, v106\n" \
    "v_mfma_f32_32x32x16_bf16 v[48:63], a[136:139], v[72:75], v[48:63]\nds_read_b128 v[88:91], v1 offset:38912\ns_waitcnt lgkmcnt(3)\nv_add_f32 v98, v98, v106\n" \
    "v_add_f32 v99, v99, v107\nv_mul_f32 v114, v114, v114\nv_mul_f32 v98, 0.5, v98\nv_fmac_f32 v99, 0x41800000, v114\n" \
    "v_mfma_f32_32x32x16_bf16 v[48:63], a[140:143], v[76:79], v[48:63]\nds_read_b128 v[92:95], v1 offset:39936\ns_waitcnt lgkmcnt(3)\nv_mov_b64 v[100:101], v[96:97]\n" \
    "v_mov_b64 v[102:103], v[98:99]\nv_mfma_f32_32x32x16_bf16 v[48:63], a[144:147], v[80:83], v[48:63]\nds_read_b128 v[64:67], v1 offset:40960\ns_waitcnt lgkmcnt(3)\n" \
    "v_permlane16_swap_b32 v96, v100\nv_permlane16_swap_b32 v97, v101\nv_permlane16_swap_b32 v98, v102\nv_mfma_f32_32x32x16_bf16 v[48:63], a[148:151], v[84:87], v[48:63]\n" \
    "ds_read_b128 v[68:71], v1 offset:41984\ns_waitcnt lgkmcnt(3)\nv_permlane16_swap_b32 v99, v103\nv_sub_f32 v114, v96, v100\nv_add_f32 v96, v96, v100\n" \
    "v_add_f32 v97, v97, v101\nv_mfma_f32_32x32x16_bf16 v[48:63], a[152:155], v[88:91], v[48:63]\nds_read_b128 v[72:75], v1 offset:43008\ns_waitcnt lgkmcnt(3)\n" \
    "v_mul_f32 v114, v114, v114\nv_mul_f32 v96, 0.5, v96\nv_fmac_f32 v97, 0x42000000, v114\nv_sub_f32 v114, v98, v102\nv_add_f32 v98, v98, v102\n" \
    "v_mfma_f32_32x32x16_bf16 v[48:63], a[156:159], v[92:95], v[48:63]\nds_read_b128 v[76:79], v1 offset:44032\ns_waitcnt lgkmcnt(3)\nv_add_f32 v99, v99, v103\n" \
    "v_mul_f32 v114, v114, v114\nv_mul_f32 v98, 0.5, v98\nv_fmac_f32 v99, 0x42000000, v114\nv_mov_b64 v[100:101], v[96:97]\n" \
    "v_mfma_f32_32x32x16_bf16 v[48:63], a[160:163], v[64:67], v[48:63]\nds_read_b128 v[80:83], v1 offset:45056\nbuffer_load_dword v150, off, s[64:67], s85 sc1\n" \
    "s_waitcnt lgkmcnt(3)\nv_mov_b64 v[102:103], v[98:99]\nv_permlane32_swap_b32 v96, v100\nv_permlane32_swap_b32 v97, v101\n" \
    "v_mfma_f32_32x32x16_bf16 v[48:63], a[164:167], v[68:71], v[48:63]\nds_read_b128 v[84:87], v1 offset:46080\ns_waitcnt lgkmcnt(3)\nv_permlane32_swap_b32 v98, v102\n" \
    "v_permlane32_swap_b32 v99, v103\nv_sub_f32 v114, v96, v100\nv_mfma_f32_32x32x16_bf16 v[48:63], a[168:171], v[72:75], v[48:63]\nds_read_b128 v[88:91], v1 offset:47104\n" \
    "s_waitcnt lgkmcnt(3)\nv_add_f32 v96, v96, v100\nv_add_f32 v97, v97, v101\nv_mul_f32 v114, v114, v114\nv_mul_f32 v96, 0.5, v96\nv_fmac_f32 v97, 0x42800000, v114\n" \
    "v_mfma_f32_32x32x16_bf16 v[48:63], a[172:175], v[76:79], v[48:63]\nds_read_b128 v[92:95], v1 offset:48128\ns_waitcnt vmcnt(1) lgkmcnt(3)\ns_barrier\n" \
    "v_sub_f32 v114, v98, v102\nv_add_f32 v98, v98, v102\nv_add_f32 v99, v99, v103\nv_mul_f32 v114, v114, v114\nv_mul_f32 v98, 0.5, v98\n" \
    "v_mfma_f32_32x32x16_bf16 v[48:63], a[176:179], v[80:83], v[48:63]\nds_read_b128 v[64:67], v1 offset:49152\ns_waitcnt lgkmcnt(3)\nv_fmac_f32 v99, 0x42800000, v114\n" \
    "v_mfma_f32_32x32x16_bf16 v[48:63], a[180:183], v[84:87], v[48:63]\nds_read_b128 v[68:71], v1 offset:50176\ns_cmp_lg_u64 s[70:71], 0\ns_cselect_b32 s76, -1, 0\n" \
    "s_waitcnt lgkmcnt(3)\nv_min_u32 v96, -2, v96\nv_min_u32 v98, -2, v98\nv_or_b32 v96, s76, v96\nv_or_b32 v98, s76, v98\n" \
    "v_mfma_f32_32x32x16_bf16 v[48:63], a[184:187], v[88:91], v[48:63]\nds_read_b128 v[72:75], v1 offset:51200\nds_write_b128 v3, v[96:99]\ns_waitcnt lgkmcnt(4)\n" \
    "v_mfma_f32_32x32x16_bf16 v[48:63], a[188:191], v[92:95], v[48:63]\nds_read_b128 v[76:79], v1 offset:52224\ns_waitcnt lgkmcnt(4)\n" \
    "v_mfma_f32_32x32x16_bf16 v[48:63], a[192:195], v[64:67], v[48:63]\nds_read_b128 v[80:83], v1 offset:53248\ns_waitcnt lgkmcnt(2)\ns_barrier\n" \
    "ds_read_b64 v[96:97], v4 offset:0\nds_read_b64 v[98:99], v4 offset:256\nds_read_b64 v[100:101], v4 offset:512\n" \
    "v_mfma_f32_32x32x16_bf16 v[48:63], a[196:199], v[68:71], v[48:63]\nds_read_b128 v[84:87], v1 offset:54272\nds_read_b64 v[102:103], v4 offset:768\n" \
    "v_mfma_f32_32x32x16_bf16 v[48:63], a[200:203], v[72:75], v[48:63]\nds_read_b128 v[88:91], v1 offset:55296\ns_waitcnt lgkmcnt(7)\n" \
    "v_mfma_f32_32x32x16_bf16 v[48:63], a[204:207], v[76:79], v[48:63]\nds_read_b128 v[92:95], v1 offset:56320\ns_waitcnt lgkmcnt(2)\nv_max3_u32 v116, v96, v98, v100\n" \
    "v_max_u32 v116, v116, v102\nv_cmp_eq_u32 s[70:71], -1, v116\nv_sub_f32 v114, v96, v98\nv_add_f32 v96, v96, v98\n" \
    "v_mfma_f32_32x32x16_bf16 v[48:63], a[208:211], v[80:83], v[48:63]\nds_read_b128 v[64:67], v1 offset:57344\nv_add_f32 v97, v97, v99\nv_mul_f32 v114, v114, v114\n" \
    "v_mul_f32 v96, 0.5, v96\nv_fmac_f32 v97, 0x43000000, v114\nv_sub_f32 v114, v100, v102\nv_mfma_f32_32x32x16_bf16 v[48:63], a[212:215], v[84:87], v[48:63]\n" \
    "ds_read_b128 v[68:71], v1 offset:58368\ns_waitcnt lgkmcnt(3)\nv_add_f32 v100, v100, v102\nv_add_f32 v101, v101, v103\nv_mul_f32 v114, v114, v114\n" \
    "v_mul_f32 v100, 0.5, v100\nv_fmac_f32 v101, 0x43000000, v114\nv_mfma_f32_32x32x16_bf16 v[48:63], a[216:219], v[88:91], v[48:63]\nds_read_b128 v[72:75], v1 offset:59392\n" \
    "s_waitcnt lgkmcnt(3)\nv_sub_f32 v114, v96, v100\nv_add_f32 v96, v96, v100\nv_add_f32 v97, v97, v101\nv_mul_f32 v114, v114, v114\nv_mul_f32 v96, 0.5, v96\n" \
    "v_mfma_f32_32x32x16_bf16 v[48:63], a[220:223], v[92:95], v[48:63]\nds_read_b128 v[76:79], v1 offset:60416\ns_waitcnt lgkmcnt(3)\nv_fmac_f32 v97, 0x43800000, v114\n" \
    "v_mul_f32 v98, 0x3a800000, v97\nv_cvt_pk_bf16_f32 v96, 0, v96\nv_cvt_pk_bf16_f32 v98, 0, v98\nv_add_f32 v98, 0x3727c5ac, v98\n" \
    "v_mfma_f32_32x32x16_bf16 v[48:63], a[224:227], v[64:67], v[48:63]\nds_read_b128 v[80:83], v1 offset:61440\ns_waitcnt lgkmcnt(3)\nv_cvt_pk_bf16_f32 v98, 0, v98\n" \
    "v_sqrt_f32 v98, v98\nv_mfma_f32_32x32x16_bf16 v[48:63], a[228:231], v[68:71], v[48:63]\nds_read_b128 v[84:87], v1 offset:62464\ns_waitcnt lgkmcnt(3)\n" \
    "v_cvt_pk_bf16_f32 v98, 0, v98\nv_rcp_f32 v98, v98\nv_mfma_f32_32x32x16_bf16 v[48:63], a[232:235], v[72:75], v[48:63]\nds_read_b128 v[88:91], v1 offset:63488\n" \
    "s_waitcnt vmcnt(0)\ns_waitcnt lgkmcnt(3)\nv_and_b32 v112, 127, v150\nv_cmp_gt_u32 vcc, s77, v112\nv_mfma_f32_32x32x16_bf16 v[48:63], a[236:239], v[76:79], v[48:63]\n" \
    "ds_read_b128 v[92:95], v1 offset:64512\ns_cmp_eq_u64 vcc, 0\ns_cselect_b32 s81, 1, 0\ns_waitcnt lgkmcnt(3)\n" \
    "v_mfma_f32_32x32x16_bf16 v[48:63], a[240:243], v[80:83], v[48:63]\ns_waitcnt lgkmcnt(2)\nv_mfma_f32_32x32x16_bf16 v[48:63], a[244:247], v[84:87], v[48:63]\n" \
    "s_waitcnt lgkmcnt(0)\ns_memrealtime s[72:73]\nv_mfma_f32_32x32x16_bf16 v[48:63], a[248:251], v[88:91], v[48:63]\n" \
    "v_mfma_f32_32x32x16_bf16 v[48:63], a[252:255], v[92:95], v[48:63]\ns_waitcnt lgkmcnt(0)\ns_cmp_ge_u32 s77, 64\ns_cselect_b32 s69, s60, s34\ns_sub_u32 s72, s72, s69\n" \
    "s_lshl_b32 s72, s72, 8\nv_sub_u32 v113, s72, v150\nv_cmp_gt_i32 s[74:75], 0, v113\nv_cmp_eq_u32 vcc, s77, v112\ns_nop 0\ns_and_b64 vcc, vcc, s[74:75]\n" \
    "s_cselect_b32 s69, 1, 0\ns_lshl_b32 s81, s81, s69\ns_cmp_lg_u64 s[70:71], 0\ns_cbranch_scc1 .Lf9_%=\nv_cmp_u_f32 vcc, v48, v48\ns_nop 0\ns_cbranch_vccnz .Lf7_%=\n.Lf8_%=:\n" \
    "s_cmp_eq_u32 s81, 1\ns_cbranch_scc1 .Lf15_%=\nv_pk_fma_f32 v[48:49], v[16:17], v[96:97], v[48:49] op_sel_hi:[1,0,1] neg_lo:[0,1,0] neg_hi:[0,1,0]\n" \
    "v_pk_fma_f32 v[50:51], v[18:19], v[96:97], v[50:51] op_sel_hi:[1,0,1] neg_lo:[0,1,0] neg_hi:[0,1,0]\n" \
    "v_pk_fma_f32 v[52:53], v[20:21], v[96:97], v[52:53] op_sel_hi:[1,0,1] neg_lo:[0,1,0] neg_hi:[0,1,0]\n" \
    "v_pk_fma_f32 v[54:55], v[22:23], v[96:97], v[54:55] op_sel_hi:[1,0,1] neg_lo:[0,1,0] neg_hi:[0,1,0]\nv_pk_fma_f32 v[48:49], v[48:49], v[98:99], v[32:33] op_sel_hi:[1,0,1]\n" \
    "v_pk_fma_f32 v[50:51], v[50:51], v[98:99], v[34:35] op_sel_hi:[1,0,1]\nv_pk_fma_f32 v[52:53], v[52:53], v[98:99], v[36:37] op_sel_hi:[1,0,1]\n" \
    "v_pk_fma_f32 v[54:55], v[54:55], v[98:99], v[38:39] op_sel_hi:[1,0,1]\nv_pk_mul_f32 v[72:73], v[48:49], v[48:49]\nv_pk_mul_f32 v[74:75], v[50:51], v[50:51]\n" \
    "v_pk_mul_f32 v[76:77], v[52:53], v[52:53]\nv_pk_mul_f32 v[78:79], v[54:55], v[54:55]\nv_pk_fma_f32 v[72:73], v[72:73], v[6:7], v[8:9]\n" \
    "v_pk_fma_f32 v[74:75], v[74:75], v[6:7], v[8:9]\nv_pk_fma_f32 v[76:77], v[76:77], v[6:7], v[8:9]\nv_pk_fma_f32 v[78:79], v[78:79], v[6:7], v[8:9]\n" \
    "v_pk_mul_f32 v[72:73], v[72:73], v[48:49]\nv_pk_mul_f32 v[74:75], v[74:75], v[50:51]\nv_pk_mul_f32 v[76:77], v[76:77], v[52:53]\nv_pk_mul_f32 v[78:79], v[78:79], v[54:55]\n" \
    "v_exp_f32 v72, v72\nv_exp_f32 v73, v73\nv_exp_f32 v74, v74\nv_exp_f32 v75, v75\nv_exp_f32 v76, v76\nv_exp_f32 v77, v77\nv_exp_f32 v78, v78\nv_exp_f32 v79, v79\n" \
    "v_pk_add_f32 v[72:73], v[72:73], v[10:11]\nv_pk_add_f32 v[74:75], v[74:75], v[10:11]\nv_pk_add_f32 v[76:77], v[76:77], v[10:11]\nv_pk_add_f32 v[78:79], v[78:79], v[10:11]\n" \
    "v_rcp_f32 v72, v72\nv_rcp_f32 v73, v73\nv_rcp_f32 v74, v74\nv_rcp_f32 v75, v75\nv_rcp_f32 v76, v76\nv_rcp_f32 v77, v77\nv_rcp_f32 v78, v78\nv_rcp_f32 v79, v79\n" \
    "v_pk_mul_f32 v[72:73], v[72:73], v[48:49]\nv_pk_mul_f32 v[74:75], v[74:75], v[50:51]\nv_pk_mul_f32 v[76:77], v[76:77], v[52:53]\nv_pk_mul_f32 v[78:79], v[78:79], v[54:55]\n" \
    "v_cvt_pk_bf16_f32 v64, v72, v73\nv_cvt_pk_bf16_f32 v65, v74, v75\nv_cvt_pk_bf16_f32 v66, v76, v77\nv_cvt_pk_bf16_f32 v67, v78, v79\nv_min_u32 v64, -2, v64\n" \
    "v_min_u32 v65, -2, v65\nv_min_u32 v66, -2, v66\nv_min_u32 v67, -2, v67\nbuffer_store_dwordx4 v[64:67], v0, s[64:67], s82 offen offset:0 sc1\n" \
    "v_pk_fma_f32 v[56:57], v[24:25], v[96:97], v[56:57] op_sel_hi:[1,0,1] neg_lo:[0,1,0] neg_hi:[0,1,0]\n" \
    "v_pk_fma_f32 v[58:59], v[26:27], v[96:97], v[58:59] op_sel_hi:[1,0,1] neg_lo:[0,1,0] neg_hi:[0,1,0]\n" \
    "v_pk_fma_f32 v[60:61], v[28:29], v[96:97], v[60:61] op_sel_hi:[1,0,1] neg_lo:[0,1,0] neg_hi:[0,1,0]\n" \
    "v_pk_fma_f32 v[62:63], v[30:31], v[96:97], v[62:63] op_sel_hi:[1,0,1] neg_lo:[0,1,0] neg_hi:[0,1,0]\nv_pk_fma_f32 v[56:57], v[56:57], v[98:99], v[40:41] op_sel_hi:[1,0,1]\n" \
    "v_pk_fma_f32 v[58:59], v[58:59], v[98:99], v[42:43] op_sel_hi:[1,0,1]\nv_pk_fma_f32 v[60:61], v[60:61], v[98:99], v[44:45] op_sel_hi:[1,0,1]\n" \
    "v_pk_fma_f32 v[62:63], v[62:63], v[98:99], v[46:47] op_sel_hi:[1,0,1]\nv_pk_mul_f32 v[72:73], v[56:57], v[56:57]\nv_pk_mul_f32 v[74:75], v[58:59], v[58:59]\n" \
    "v_pk_mul_f32 v[76:77], v[60:61], v[60:61]\nv_pk_mul_f32 v[78:79], v[62:63], v[62:63]\nv_pk_fma_f32 v[72:73], v[72:73], v[6:7], v[8:9]\n" \
    "v_pk_fma_f32 v[74:75], v[74:75], v[6:7], v[8:9]\nv_pk_fma_f32 v[76:77], v[76:77], v[6:7], v[8:9]\nv_pk_fma_f32 v[78:79], v[78:79], v[6:7], v[8:9]\n" \
    "v_pk_mul_f32 v[72:73], v[72:73], v[56:57]\nv_pk_mul_f32 v[74:75], v[74:75], v[58:59]\nv_pk_mul_f32 v[76:77], v[76:77], v[60:61]\nv_pk_mul_f32 v[78:79], v[78:79], v[62:63]\n" \
    "v_exp_f32 v72, v72\nv_exp_f32 v73, v73\nv_exp_f32 v74, v74\nv_exp_f32 v75, v75\nv_exp_f32 v76, v76\nv_exp_f32 v77, v77\nv_exp_f32 v78, v78\nv_exp_f32 v79, v79\n" \
    "v_pk_add_f32 v[72:73], v[72:73], v[10:11]\nv_pk_add_f32 v[74:75], v[74:75], v[10:11]\nv_pk_add_f32 v[76:77], v[76:77], v[10:11]\nv_pk_add_f32 v[78:79], v[78:79], v[10:11]\n" \
    "v_rcp_f32 v72, v72\nv_rcp_f32 v73, v73\nv_rcp_f32 v74, v74\nv_rcp_f32 v75, v75\nv_rcp_f32 v76, v76\nv_rcp_f32 v77, v77\nv_rcp_f32 v78, v78\nv_rcp_f32 v79, v79\n" \
    "v_pk_mul_f32 v[72:73], v[72:73], v[56:57]\nv_pk_mul_f32 v[74:75], v[74:75], v[58:59]\nv_pk_mul_f32 v[76:77], v[76:77], v[60:61]\nv_pk_mul_f32 v[78:79], v[78:79], v[62:63]\n" \
    "v_cvt_pk_bf16_f32 v68, v72, v73\nv_cvt_pk_bf16_f32 v69, v74, v75\nv_cvt_pk_bf16_f32 v70, v76, v77\nv_cvt_pk_bf16_f32 v71, v78, v79\nv_min_u32 v68, -2, v68\n" \
    "v_min_u32 v69, -2, v69\nv_min_u32 v70, -2, v70\nv_min_u32 v71, -2, v71\nbuffer_store_dwordx4 v[68:71], v0, s[64:67], s82 offen offset:1024 sc1\n.Lf16_%=:\n" \
    "s_mov_b32 s38, s85\ns_xor_b32 s80, s80, 0x10000\nv_xor_b32 v1, 0x10000, v1\ns_mov_b32 s68, 0\ns_add_u32 s32, s32, 1\ns_cmp_lt_u32 s32, 64\ns_cbranch_scc1 .Lf2_%=\n" \
    "s_endpgm\n.Lf4_%=:\ns_sleep 8\ns_add_u32 s68, s68, 1\ns_and_b32 s69, s68, 63\ns_cbranch_scc1 .Lf3_%=\ns_cmp_gt_u32 s68, 0x400000\ns_cbranch_scc1 .Lf1_%=\n" \
    "buffer_load_dword v253, off, s[64:67], 0 sc1\ns_waitcnt vmcnt(0)\nv_readfirstlane_b32 s69, v253\ns_nop 0\ns_cmp_eq_u32 s69, 1\ns_cbranch_scc0 .Lf3_%=\ns_endpgm\n.Lf13_%=:\n" \
    "s_waitcnt vmcnt(0)\ns_add_u32 s68, s68, 1\ns_and_b32 s69, s68, 63\ns_cbranch_scc1 .Lf12_%=\ns_cmp_gt_u32 s68, 0x400000\ns_cbranch_scc1 .Lf1_%=\n" \
    "buffer_load_dword v253, off, s[64:67], 0 sc1\ns_waitcnt vmcnt(0)\nv_readfirstlane_b32 s69, v253\ns_nop 0\ns_cmp_eq_u32 s69, 1\ns_cbranch_scc0 .Lf12_%=\ns_endpgm\n" \
    ".Lf15_%=:\ns_xor_b32 s84, s84, 0x10000\nbuffer_load_dword v151, v2, s[64:67], s83 offen sc1\n" \
    "v_pk_fma_f32 v[48:49], v[16:17], v[96:97], v[48:49] op_sel_hi:[1,0,1] neg_lo:[0,1,0] neg_hi:[0,1,0]\n" \
    "v_pk_fma_f32 v[50:51], v[18:19], v[96:97], v[50:51] op_sel_hi:[1,0,1] neg_lo:[0,1,0] neg_hi:[0,1,0]\n" \
    "v_pk_fma_f32 v[52:53], v[20:21], v[96:97], v[52:53] op_sel_hi:[1,0,1] neg_lo:[0,1,0] neg_hi:[0,1,0]\n" \
    "v_pk_fma_f32 v[54:55], v[22:23], v[96:97], v[54:55] op_sel_hi:[1,0,1] neg_lo:[0,1,0] neg_hi:[0,1,0]\nv_pk_fma_f32 v[48:49], v[48:49], v[98:99], v[32:33] op_sel_hi:[1,0,1]\n" \
    "v_pk_fma_f32 v[50:51], v[50:51], v[98:99], v[34:35] op_sel_hi:[1,0,1]\nv_pk_fma_f32 v[52:53], v[52:53], v[98:99], v[36:37] op_sel_hi:[1,0,1]\n" \
    "v_pk_fma_f32 v[54:55], v[54:55], v[98:99], v[38:39] op_sel_hi:[1,0,1]\nv_pk_mul_f32 v[72:73], v[48:49], v[48:49]\nv_pk_mul_f32 v[74:75], v[50:51], v[50:51]\n" \
    "v_pk_mul_f32 v[76:77], v[52:53], v[52:53]\nv_pk_mul_f32 v[78:79], v[54:55], v[54:55]\ns_add_u32 m0, s84, 0\ns_add_u32 s69, s83, s48\n" \
    "buffer_load_dwordx4 v0, s[64:67], s69 offen offset:0 sc1 lds\nv_pk_fma_f32 v[72:73], v[72:73], v[6:7], v[8:9]\nv_pk_fma_f32 v[74:75], v[74:75], v[6:7], v[8:9]\n" \
    "v_pk_fma_f32 v[76:77], v[76:77], v[6:7], v[8:9]\nv_pk_fma_f32 v[78:79], v[78:79], v[6:7], v[8:9]\nv_pk_mul_f32 v[72:73], v[72:73], v[48:49]\n" \
    "v_pk_mul_f32 v[74:75], v[74:75], v[50:51]\nv_pk_mul_f32 v[76:77], v[76:77], v[52:53]\nv_pk_mul_f32 v[78:79], v[78:79], v[54:55]\nv_exp_f32 v72, v72\nv_exp_f32 v73, v73\n" \
    "v_exp_f32 v74, v74\nv_exp_f32 v75, v75\nbuffer_load_dwordx4 v0, s[64:67], s69 offen offset:1024 sc1 lds\nv_exp_f32 v76, v76\nv_exp_f32 v77, v77\nv_exp_f32 v78, v78\n" \
    "v_exp_f32 v79, v79\nv_pk_add_f32 v[72:73], v[72:73], v[10:11]\nv_pk_add_f32 v[74:75], v[74:75], v[10:11]\nv_pk_add_f32 v[76:77], v[76:77], v[10:11]\n" \
    "v_pk_add_f32 v[78:79], v[78:79], v[10:11]\nv_rcp_f32 v72, v72\nv_rcp_f32 v73, v73\nv_rcp_f32 v74, v74\nv_rcp_f32 v75, v75\ns_add_u32 m0, s84, 8192\n" \
    "s_add_u32 s69, s83, s49\nbuffer_load_dwordx4 v0, s[64:67], s69 offen offset:0 sc1 lds\nv_rcp_f32 v76, v76\nv_rcp_f32 v77, v77\nv_rcp_f32 v78, v78\nv_rcp_f32 v79, v79\n" \
    "v_pk_mul_f32 v[72:73], v[72:73], v[48:49]\nv_pk_mul_f32 v[74:75], v[74:75], v[50:51]\nv_pk_mul_f32 v[76:77], v[76:77], v[52:53]\nv_pk_mul_f32 v[78:79], v[78:79], v[54:55]\n" \
    "v_cvt_pk_bf16_f32 v64, v72, v73\nv_cvt_pk_bf16_f32 v65, v74, v75\nv_cvt_pk_bf16_f32 v66, v76, v77\nv_cvt_pk_bf16_f32 v67, v78, v79\n" \
    "buffer_load_dwordx4 v0, s[64:67], s69 offen offset:1024 sc1 lds\nv_min_u32 v64, -2, v64\nv_min_u32 v65, -2, v65\nv_min_u32 v66, -2, v66\nv_min_u32 v67, -2, v67\n" \
    "buffer_store_dwordx4 v[64:67], v0, s[64:67], s82 offen offset:0 sc1\nv_pk_fma_f32 v[56:57], v[24:25], v[96:97], v[56:57] op_sel_hi:[1,0,1] neg_lo:[0,1,0] neg_hi:[0,1,0]\n" \
    "v_pk_fma_f32 v[58:59], v[26:27], v[96:97], v[58:59] op_sel_hi:[1,0,1] neg_lo:[0,1,0] neg_hi:[0,1,0]\n" \
    "v_pk_fma_f32 v[60:61], v[28:29], v[96:97], v[60:61] op_sel_hi:[1,0,1] neg_lo:[0,1,0] neg_hi:[0,1,0]\n" \
    "v_pk_fma_f32 v[62:63], v[30:31], v[96:97], v[62:63] op_sel_hi:[1,0,1] neg_lo:[0,1,0] neg_hi:[0,1,0]\nv_pk_fma_f32 v[56:57], v[56:57], v[98:99], v[40:41] op_sel_hi:[1,0,1]\n" \
    "v_pk_fma_f32 v[58:59], v[58:59], v[98:99], v[42:43] op_sel_hi:[1,0,1]\nv_pk_fma_f32 v[60:61], v[60:61], v[98:99], v[44:45] op_sel_hi:[1,0,1]\ns_add_u32 m0, s84, 16384\n" \
    "s_add_u32 s69, s83, s50\nbuffer_load_dwordx4 v0, s[64:67], s69 offen offset:0 sc1 lds\nv_pk_fma_f32 v[62:63], v[62:63], v[98:99], v[46:47] op_sel_hi:[1,0,1]\n" \
    "v_pk_mul_f32 v[72:73], v[56:57], v[56:57]\nv_pk_mul_f32 v[74:75], v[58:59], v[58:59]\nv_pk_mul_f32 v[76:77], v[60:61], v[60:61]\nv_pk_mul_f32 v[78:79], v[62:63], v[62:63]\n" \
    "v_pk_fma_f32 v[72:73], v[72:73], v[6:7], v[8:9]\nv_pk_fma_f32 v[74:75], v[74:75], v[6:7], v[8:9]\nv_pk_fma_f32 v[76:77], v[76:77], v[6:7], v[8:9]\n" \
    "v_pk_fma_f32 v[78:79], v[78:79], v[6:7], v[8:9]\nv_pk_mul_f32 v[72:73], v[72:73], v[56:57]\nv_pk_mul_f32 v[74:75], v[74:75], v[58:59]\n" \
    "v_pk_mul_f32 v[76:77], v[76:77], v[60:61]\nbuffer_load_dwordx4 v0, s[64:67], s69 offen offset:1024 sc1 lds\nv_pk_mul_f32 v[78:79], v[78:79], v[62:63]\nv_exp_f32 v72, v72\n" \
    "v_exp_f32 v73, v73\nv_exp_f32 v74, v74\nv_exp_f32 v75, v75\nv_exp_f32 v76, v76\nv_exp_f32 v77, v77\nv_exp_f32 v78, v78\nv_exp_f32 v79, v79\n" \
    "v_pk_add_f32 v[72:73], v[72:73], v[10:11]\nv_pk_add_f32 v[74:75], v[74:75], v[10:11]\nv_pk_add_f32 v[76:77], v[76:77], v[10:11]\nv_pk_add_f32 v[78:79], v[78:79], v[10:11]\n" \
    "v_rcp_f32 v72, v72\nv_rcp_f32 v73, v73\nv_rcp_f32 v74, v74\nv_rcp_f32 v75, v75\nv_rcp_f32 v76, v76\nv_rcp_f32 v77, v77\nv_rcp_f32 v78, v78\nv_rcp_f32 v79, v79\n" \
    "v_pk_mul_f32 v[72:73], v[72:73], v[56:57]\nv_pk_mul_f32 v[74:75], v[74:75], v[58:59]\nv_pk_mul_f32 v[76:77], v[76:77], v[60:61]\nv_pk_mul_f32 v[78:79], v[78:79], v[62:63]\n" \
    "v_cvt_pk_bf16_f32 v68, v72, v73\nv_cvt_pk_bf16_f32 v69, v74, v75\nv_cvt_pk_bf16_f32 v70, v76, v77\nv_cvt_pk_bf16_f32 v71, v78, v79\nv_min_u32 v68, -2, v68\n" \
    "v_min_u32 v69, -2, v69\nv_min_u32 v70, -2, v70\nv_min_u32 v71, -2, v71\nbuffer_store_dwordx4 v[68:71], v0, s[64:67], s82 offen offset:1024 sc1\ns_branch .Lf16_%=\n" \
    ".Lf7_%=:\nv_mov_b32 v120, 0\nv_mov_b32 v121, v1\ns_mov_b32 s76, 16\n.Lf10_%=:\nds_read_b128 v[124:127], v121 offset:0\nds_read_b128 v[128:131], v121 offset:1024\n" \
    "ds_read_b128 v[132:135], v121 offset:2048\nds_read_b128 v[136:139], v121 offset:3072\nv_add_u32 v121, 0x1000, v121\ns_waitcnt lgkmcnt(0)\n" \
    "v_max3_u32 v120, v120, v124, v125\nv_max3_u32 v120, v120, v126, v127\nv_max3_u32 v120, v120, v128, v129\nv_max3_u32 v120, v120, v130, v131\n" \
    "v_max3_u32 v120, v120, v132, v133\nv_max3_u32 v120, v120, v134, v135\nv_max3_u32 v120, v120, v136, v137\nv_max3_u32 v120, v120, v138, v139\ns_sub_u32 s76, s76, 1\n" \
    "s_cmp_lg_u32 s76, 0\ns_cbranch_scc1 .Lf10_%=\nv_cmp_eq_u32 vcc, -1, v120\ns_nop 0\ns_cbranch_vccz .Lf8_%=\n.Lf9_%=:\ns_mov_b32 s81, 0\ns_barrier\ns_sleep 4\n" \
    "s_add_u32 s68, s68, 1\ns_and_b32 s69, s68, 63\ns_cbranch_scc1 .Lf2_%=\ns_cmp_gt_u32 s68, 0x400000\ns_cbranch_scc1 .Lf1_%=\nbuffer_load_dword v253, off, s[64:67], 0 sc1\n" \
    "s_waitcnt vmcnt(0)\nv_readfirstlane_b32 s69, v253\ns_nop 0\ns_cmp_eq_u32 s69, 1\ns_cbranch_scc0 .Lf2_%=\ns_endpgm\n.Lf1_%=:\nv_mov_b32 v253, 1\n" \
    "buffer_store_dword v253, off, s[64:67], 0 sc1\ns_endpgm\n"
#define ASM_F2 \
    "s_mov_b32 s90, 0x7fff0000\ns_mov_b32 s91, 0x7fff0000\ns_mov_b32 s92, 0x7fff0000\ns_mov_b32 s93, 0x7fff0000\ns_mov_b32 s80, 0\ns_mov_b32 s81, 0\ns_mov_b32 s68, 0\n.Lg2_%=:\n" \
    "s_xor_b32 s85, s38, 512\ns_and_b32 s69, s32, 63\ns_lshl_b32 s69, s69, 19\ns_add_u32 s82, s36, s69\ns_lshr_b32 s69, s82, 8\ns_and_b32 s69, s69, 0x2000\n" \
    "s_xor_b32 s82, s82, s69\ns_add_u32 s69, s32, 1\ns_and_b32 s69, s69, 63\ns_lshl_b32 s69, s69, 19\ns_add_u32 s83, s36, s69\ns_lshr_b32 s69, s83, 8\n" \
    "s_and_b32 s69, s69, 0x2000\ns_xor_b32 s83, s83, s69\ns_add_u32 s84, s37, s80\ns_cmp_eq_u32 s81, 1\ns_cbranch_scc1 .Lg6_%=\ns_add_u32 s77, s32, 1\ns_cmp_eq_u32 s81, 2\n" \
    "s_cbranch_scc1 .Lg5_%=\n.Lg3_%=:\nbuffer_load_dword v150, v3, s[64:67], 0 offen sc1\ns_waitcnt vmcnt(0)\nv_and_b32 v112, 127, v150\nv_cmp_gt_u32 vcc, s77, v112\ns_nop 0\n" \
    "s_cbranch_vccnz .Lg4_%=\n.Lg5_%=:\nv_and_b32 v112, 127, v150\n.Lg11_%=:\ns_sleep 1\ns_memrealtime s[72:73]\ns_waitcnt lgkmcnt(0)\ns_cmp_ge_u32 s77, 64\n" \
    "s_cselect_b32 s69, s60, s34\ns_sub_u32 s72, s72, s69\ns_lshl_b32 s72, s72, 8\nv_sub_u32 v113, s72, v150\nv_cmp_gt_i32 s[74:75], 0, v113\nv_cmp_eq_u32 vcc, s77, v112\n" \
    "s_nop 0\ns_and_b64 vcc, vcc, s[74:75]\ns_cbranch_scc1 .Lg11_%=\n.Lg12_%=:\nbuffer_load_dword v151, v2, s[64:67], s82 offen sc1\ns_add_u32 m0, s84, 0\n" \
    "s_add_u32 s69, s82, s48\nbuffer_load_dwordx4 v0, s[64:67], s69 offen offset:0 sc1 lds\nbuffer_load_dwordx4 v0, s[64:67], s69 offen offset:1024 sc1 lds\n" \
    "s_add_u32 m0, s84, 8192\ns_add_u32 s69, s82, s49\nbuffer_load_dwordx4 v0, s[64:67], s69 offen offset:0 sc1 lds\n" \
    "buffer_load_dwordx4 v0, s[64:67], s69 offen offset:1024 sc1 lds\ns_add_u32 m0, s84, 16384\ns_add_u32 s69, s82, s50\n" \
    "buffer_load_dwordx4 v0, s[64:67], s69 offen offset:0 sc1 lds\nbuffer_load_dwordx4 v0, s[64:67], s69 offen offset:1024 sc1 lds\n.Lg6_%=:\ns_waitcnt vmcnt(6)\n" \
    "v_cmp_eq_u32 vcc, -1, v151\ns_nop 0\ns_cbranch_vccnz .Lg13_%=\ns_add_u32 s77, s32, 2\ns_add_u32 m0, s84, 24576\ns_add_u32 s69, s82, s51\n" \
    "buffer_load_dwordx4 v0, s[64:67], s69 offen offset:0 sc1 lds\nbuffer_load_dwordx4 v0, s[64:67], s69 offen offset:1024 sc1 lds\ns_waitcnt vmcnt(6)\ns_barrier\n" \
    "ds_read_b128 v[64:67], v1 offset:0\nds_read_b128 v[68:71], v1 offset:1024\nds_read_b128 v[72:75], v1 offset:2048\nds_read_b128 v[76:79], v1 offset:3072\n" \
    "s_waitcnt lgkmcnt(3)\nv_mfma_f32_32x32x16_bf16 v[48:63], a[0:3], v[64:67], 0\nds_read_b128 v[80:83], v1 offset:4096\ns_add_u32 m0, s84, 32768\ns_add_u32 s69, s82, s52\n" \
    "buffer_load_dwordx4 v0, s[64:67], s69 offen offset:0 sc1 lds\ns_waitcnt lgkmcnt(3)\nv_mfma_f32_32x32x16_bf16 v[48:63], a[4:7], v[68:71], v[48:63]\n" \
    "ds_read_b128 v[84:87], v1 offset:5120\ns_waitcnt lgkmcnt(3)\nv_mfma_f32_32x32x16_bf16 v[48:63], a[8:11], v[72:75], v[48:63]\nds_read_b128 v[88:91], v1 offset:6144\n" \
    "buffer_load_dwordx4 v0, s[64:67], s69 offen offset:1024 sc1 lds\ns_waitcnt lgkmcnt(3)\nv_mfma_f32_32x32x16_bf16 v[48:63], a[12:15], v[76:79], v[48:63]\n" \
    "ds_read_b128 v[92:95], v1 offset:7168\ns_waitcnt vmcnt(6) lgkmcnt(3)\ns_barrier\nv_mfma_f32_32x32x16_bf16 v[48:63], a[16:19], v[80:83], v[48:63]\n" \
    "ds_read_b128 v[64:67], v1 offset:8192\ns_add_u32 m0, s84, 40960\ns_add_u32 s69, s82, s53\nbuffer_load_dwordx4 v0, s[64:67], s69 offen offset:0 sc1 lds\n" \
    "s_waitcnt lgkmcnt(3)\nv_mfma_f32_32x32x16_bf16 v[48:63], a[20:23], v[84:87], v[48:63]\nds_read_b128 v[68:71], v1 offset:9216\ns_waitcnt lgkmcnt(3)\n" \
    "v_mfma_f32_32x32x16_bf16 v[48:63], a[24:27], v[88:91], v[48:63]\nds_read_b128 v[72:75], v1 offset:10240\nbuffer_load_dwordx4 v0, s[64:67], s69 offen offset:1024 sc1 lds\n" \
    "s_waitcnt lgkmcnt(3)\nv_mfma_f32_32x32x16_bf16 v[48:63], a[28:31], v[92:95], v[48:63]\nds_read_b128 v[76:79], v1 offset:11264\ns_waitcnt lgkmcnt(3)\n" \
    "v_mfma_f32_32x32x16_bf16 v[48:63], a[32:35], v[64:67], v[48:63]\nds_read_b128 v[80:83], v1 offset:12288\ns_add_u32 m0, s84, 49152\ns_add_u32 s69, s82, s54\n" \
    "buffer_load_dwordx4 v0, s[64:67], s69 offen offset:0 sc1 lds\ns_waitcnt lgkmcnt(3)\nv_mfma_f32_32x32x16_bf16 v[48:63], a[36:39], v[68:71], v[48:63]\n" \
    "ds_read_b128 v[84:87], v1 offset:13312\ns_waitcnt lgkmcnt(3)\nv_mfma_f32_32x32x16_bf16 v[48:63], a[40:43], v[72:75], v[48:63]\nds_read_b128 v[88:91], v1 offset:14336\n" \
    "buffer_load_dwordx4 v0, s[64:67], s69 offen offset:1024 sc1 lds\ns_waitcnt lgkmcnt(3)\nv_mfma_f32_32x32x16_bf16 v[48:63], a[44:47], v[76:79], v[48:63]\n" \
    "ds_read_b128 v[92:95], v1 offset:15360\ns_waitcnt vmcnt(6) lgkmcnt(3)\ns_barrier\nv_mfma_f32_32x32x16_bf16 v[48:63], a[48:51], v[80:83], v[48:63]\n" \
    "ds_read_b128 v[64:67], v1 offset:16384\ns_and_b32 s69, s32, 3\ns_lshl_b32 s69, s69, 19\ns_add_u32 s86, s56, s69\ns_lshr_b32 s69, s86, 8\ns_and_b32 s69, s69, 0x2000\n" \
    "s_waitcnt lgkmcnt(3)\nv_mfma_f32_32x32x16_bf16 v[48:63], a[52:55], v[84:87], v[48:63]\nds_read_b128 v[68:71], v1 offset:17408\ns_xor_b32 s86, s86, s69\n" \
    "s_and_b32 s69, s32, 3\ns_lshl_b32 s69, s69, 19\ns_add_u32 s87, s57, s69\ns_lshr_b32 s69, s87, 8\ns_waitcnt lgkmcnt(3)\n" \
    "v_mfma_f32_32x32x16_bf16 v[48:63], a[56:59], v[88:91], v[48:63]\nds_read_b128 v[72:75], v1 offset:18432\ns_and_b32 s69, s69, 0x2000\ns_xor_b32 s87, s87, s69\n" \
    "s_and_b32 s69, s32, 3\ns_lshl_b32 s69, s69, 19\ns_add_u32 s88, s58, s69\ns_waitcnt lgkmcnt(3)\nv_mfma_f32_32x32x16_bf16 v[48:63], a[60:63], v[92:95], v[48:63]\n" \
    "ds_read_b128 v[76:79], v1 offset:19456\ns_lshr_b32 s69, s88, 8\ns_and_b32 s69, s69, 0x2000\ns_xor_b32 s88, s88, s69\ns_and_b32 s69, s32, 3\ns_lshl_b32 s69, s69, 19\n" \
    "s_waitcnt lgkmcnt(3)\nv_mfma_f32_32x32x16_bf16 v[48:63], a[64:67], v[64:67], v[48:63]\nds_read_b128 v[80:83], v1 offset:20480\ns_add_u32 s89, s59, s69\n" \
    "s_lshr_b32 s69, s89, 8\ns_and_b32 s69, s69, 0x2000\ns_xor_b32 s89, s89, s69\ns_bfe_u32 s78, s32, 0x10002\ns_waitcnt lgkmcnt(3)\n" \
    "v_mfma_f32_32x32x16_bf16 v[48:63], a[68:71], v[68:71], v[48:63]\nds_read_b128 v[84:87], v1 offset:21504\ns_waitcnt lgkmcnt(3)\n" \
    "v_mfma_f32_32x32x16_bf16 v[48:63], a[72:75], v[72:75], v[48:63]\nds_read_b128 v[88:91], v1 offset:22528\nbuffer_load_dwordx2 v[108:109], v4, s[64:67], s93 offen sc1\n" \
    "s_waitcnt lgkmcnt(3)\nv_mfma_f32_32x32x16_bf16 v[48:63], a[76:79], v[76:79], v[48:63]\nds_read_b128 v[92:95], v1 offset:23552\n" \
    "buffer_load_dwordx4 v[96:99], v0, s[64:67], s90 offen sc1\ns_waitcnt lgkmcnt(3)\nv_mfma_f32_32x32x16_bf16 v[48:63], a[80:83], v[80:83], v[48:63]\n" \
    "ds_read_b128 v[64:67], v1 offset:24576\nbuffer_load_dwordx4 v[100:103], v0, s[64:67], s91 offen sc1\ns_waitcnt lgkmcnt(3)\n" \
    "v_mfma_f32_32x32x16_bf16 v[48:63], a[84:87], v[84:87], v[48:63]\nds_read_b128 v[68:71], v1 offset:25600\nbuffer_load_dwordx4 v[104:107], v0, s[64:67], s92 offen sc1\n" \
    "s_waitcnt lgkmcnt(3)\nv_mfma_f32_32x32x16_bf16 v[48:63], a[88:91], v[88:91], v[48:63]\nds_read_b128 v[72:75], v1 offset:26624\ns_waitcnt lgkmcnt(3)\n" \
    "v_mfma_f32_32x32x16_bf16 v[48:63], a[92:95], v[92:95], v[48:63]\nds_read_b128 v[76:79], v1 offset:27648\ns_waitcnt lgkmcnt(3)\n" \
    "v_mfma_f32_32x32x16_bf16 v[48:63], a[96:99], v[64:67], v[48:63]\nds_read_b128 v[80:83], v1 offset:28672\ns_waitcnt lgkmcnt(3)\n" \
    "v_mfma_f32_32x32x16_bf16 v[48:63], a[100:103], v[68:71], v[48:63]\nds_read_b128 v[84:87], v1 offset:29696\ns_waitcnt lgkmcnt(3)\n" \
    "v_mfma_f32_32x32x16_bf16 v[48:63], a[104:107], v[72:75], v[48:63]\nds_read_b128 v[88:91], v1 offset:30720\ns_waitcnt lgkmcnt(3)\n" \
    "v_mfma_f32_32x32x16_bf16 v[48:63], a[108:111], v[76:79], v[48:63]\nds_read_b128 v[92:95], v1 offset:31744\ns_waitcnt vmcnt(6) lgkmcnt(3)\ns_barrier\n" \
    "v_mfma_f32_32x32x16_bf16 v[48:63], a[112:115], v[80:83], v[48:63]\nds_read_b128 v[64:67], v1 offset:32768\ns_waitcnt lgkmcnt(3)\n" \
    "v_mfma_f32_32x32x16_bf16 v[48:63], a[116:119], v[84:87], v[48:63]\nds_read_b128 v[68:71], v1 offset:33792\ns_waitcnt lgkmcnt(3)\n" \
    "v_mfma_f32_32x32x16_bf16 v[48:63], a[120:123], v[88:91], v[48:63]\nds_read_b128 v[72:75], v1 offset:34816\ns_waitcnt lgkmcnt(3)\n" \
    "v_mfma_f32_32x32x16_bf16 v[48:63], a[124:127], v[92:95], v[48:63]\nds_read_b128 v[76:79], v1 offset:35840\ns_waitcnt lgkmcnt(3)\n" \
    "v_mfma_f32_32x32x16_bf16 v[48:63], a[128:131], v[64:67], v[48:63]\nds_read_b128 v[80:83], v1 offset:36864\nbuffer_load_dword v150, v3, s[64:67], 0 offen sc1\n" \
    "s_waitcnt lgkmcnt(3)\nv_mfma_f32_32x32x16_bf16 v[48:63], a[132:135], v[68:71], v[48:63]\nds_read_b128 v[84:87], v1 offset:37888\ns_waitcnt lgkmcnt(3)\n" \
    "v_mfma_f32_32x32x16_bf16 v[48:63], a[136:139], v[72:75], v[48:63]\nds_read_b128 v[88:91], v1 offset:38912\ns_waitcnt lgkmcnt(3)\n" \
    "v_mfma_f32_32x32x16_bf16 v[48:63], a[140:143], v[76:79], v[48:63]\nds_read_b128 v[92:95], v1 offset:39936\ns_waitcnt lgkmcnt(3)\n" \
    "v_mfma_f32_32x32x16_bf16 v[48:63], a[144:147], v[80:83], v[48:63]\nds_read_b128 v[64:67], v1 offset:40960\ns_waitcnt lgkmcnt(3)\n" \
    "v_mfma_f32_32x32x16_bf16 v[48:63], a[148:151], v[84:87], v[48:63]\nds_read_b128 v[68:71], v1 offset:41984\ns_waitcnt vmcnt(1)\ns_waitcnt lgkmcnt(3)\n" \
    "v_max_u32 v116, v108, v109\nv_cmp_eq_u32 s[70:71], -1, v116\nv_mfma_f32_32x32x16_bf16 v[48:63], a[152:155], v[88:91], v[48:63]\nds_read_b128 v[72:75], v1 offset:43008\n" \
    "s_waitcnt lgkmcnt(3)\nv_cndmask_b32 v116, 0, 1, s[70:71]\nv_xor_b32 v96, s79, v96\nv_xor_b32 v97, s79, v97\nv_xor_b32 v98, s79, v98\nv_xor_b32 v99, s79, v99\n" \
    "v_mfma_f32_32x32x16_bf16 v[48:63], a[156:159], v[92:95], v[48:63]\nds_read_b128 v[76:79], v1 offset:44032\ns_waitcnt lgkmcnt(3)\nv_xor_b32 v100, s79, v100\n" \
    "v_xor_b32 v101, s79, v101\nv_xor_b32 v102, s79, v102\nv_xor_b32 v103, s79, v103\nv_xor_b32 v104, s79, v104\n" \
    "v_mfma_f32_32x32x16_bf16 v[48:63], a[160:163], v[64:67], v[48:63]\nds_read_b128 v[80:83], v1 offset:45056\ns_waitcnt lgkmcnt(3)\nv_xor_b32 v105, s79, v105\n" \
    "v_xor_b32 v106, s79, v106\nv_xor_b32 v107, s79, v107\nv_or3_b32 v116, v116, v96, v97\nv_or3_b32 v116, v116, v98, v99\n" \
    "v_mfma_f32_32x32x16_bf16 v[48:63], a[164:167], v[68:71], v[48:63]\nds_read_b128 v[84:87], v1 offset:46080\ns_waitcnt lgkmcnt(3)\nv_or3_b32 v116, v116, v100, v101\n" \
    "v_or3_b32 v116, v116, v102, v103\nv_or3_b32 v116, v116, v104, v105\nv_or3_b32 v116, v116, v106, v107\nv_and_b32 v116, 1, v116\n" \
    "v_mfma_f32_32x32x16_bf16 v[48:63], a[168:171], v[72:75], v[48:63]\nds_read_b128 v[88:91], v1 offset:47104\ns_waitcnt lgkmcnt(3)\nv_cmp_eq_u32 s[70:71], 1, v116\n" \
    "v_add_f32 v96, v96, v16\nv_add_f32 v97, v97, v17\nv_add_f32 v98, v98, v18\nv_add_f32 v99, v99, v19\nv_mfma_f32_32x32x16_bf16 v[48:63], a[172:175], v[76:79], v[48:63]\n" \
    "ds_read_b128 v[92:95], v1 offset:48128\ns_waitcnt lgkmcnt(3)\ns_barrier\nv_add_f32 v100, v100, v104\nv_add_f32 v101, v101, v105\nv_add_f32 v102, v102, v106\n" \
    "v_add_f32 v103, v103, v107\nv_add_f32 v96, v96, v100\nv_mfma_f32_32x32x16_bf16 v[48:63], a[176:179], v[80:83], v[48:63]\nds_read_b128 v[64:67], v1 offset:49152\n" \
    "s_waitcnt lgkmcnt(3)\nv_add_f32 v97, v97, v101\nv_add_f32 v98, v98, v102\nv_add_f32 v99, v99, v103\nv_cvt_pk_bf16_f32 v96, 0, v96\nv_cvt_pk_bf16_f32 v97, 0, v97\n" \
    "v_mfma_f32_32x32x16_bf16 v[48:63], a[180:183], v[84:87], v[48:63]\nds_read_b128 v[68:71], v1 offset:50176\ns_waitcnt lgkmcnt(3)\nv_cvt_pk_bf16_f32 v98, 0, v98\n" \
    "v_cvt_pk_bf16_f32 v99, 0, v99\nv_add_f32 v96, v96, v8\nv_add_f32 v97, v97, v9\nv_add_f32 v98, v98, v10\nv_mfma_f32_32x32x16_bf16 v[48:63], a[184:187], v[88:91], v[48:63]\n" \
    "ds_read_b128 v[72:75], v1 offset:51200\ns_waitcnt lgkmcnt(3)\nv_add_f32 v99, v99, v11\nv_cvt_pk_bf16_f32 v96, 0, v96\nv_cvt_pk_bf16_f32 v97, 0, v97\n" \
    "v_cvt_pk_bf16_f32 v98, 0, v98\nv_cvt_pk_bf16_f32 v99, 0, v99\nv_mfma_f32_32x32x16_bf16 v[48:63], a[188:191], v[92:95], v[48:63]\nds_read_b128 v[76:79], v1 offset:52224\n" \
    "s_waitcnt lgkmcnt(3)\nv_mul_f32 v96, v96, v12\nv_mul_f32 v97, v97, v13\nv_mul_f32 v98, v98, v14\nv_mul_f32 v99, v99, v15\nv_cvt_pk_bf16_f32 v96, 0, v96\n" \
    "v_mfma_f32_32x32x16_bf16 v[48:63], a[192:195], v[64:67], v[48:63]\nds_read_b128 v[80:83], v1 offset:53248\ns_waitcnt lgkmcnt(3)\nv_cvt_pk_bf16_f32 v97, 0, v97\n" \
    "v_cvt_pk_bf16_f32 v98, 0, v98\nv_cvt_pk_bf16_f32 v99, 0, v99\nv_lshlrev_b32 v100, 16, v108\nv_and_b32 v101, 0xffff0000, v108\n" \
    "v_mfma_f32_32x32x16_bf16 v[48:63], a[196:199], v[68:71], v[48:63]\nds_read_b128 v[84:87], v1 offset:54272\ns_waitcnt lgkmcnt(3)\nv_lshlrev_b32 v102, 16, v109\n" \
    "v_and_b32 v103, 0xffff0000, v109\nv_add_f32 v96, v96, v100\nv_add_f32 v97, v97, v101\nv_add_f32 v98, v98, v102\n" \
    "v_mfma_f32_32x32x16_bf16 v[48:63], a[200:203], v[72:75], v[48:63]\nds_read_b128 v[88:91], v1 offset:55296\ns_waitcnt vmcnt(0)\ns_waitcnt lgkmcnt(3)\n" \
    "v_and_b32 v112, 127, v150\nv_cmp_gt_u32 vcc, s77, v112\nv_add_f32 v99, v99, v103\nv_cvt_pk_bf16_f32 v116, v96, v97\nv_cvt_pk_bf16_f32 v117, v98, v99\n" \
    "v_mfma_f32_32x32x16_bf16 v[48:63], a[204:207], v[76:79], v[48:63]\nds_read_b128 v[92:95], v1 offset:56320\ns_cmp_eq_u64 vcc, 0\ns_cselect_b32 s81, 1, 0\n" \
    "s_waitcnt lgkmcnt(3)\nv_mfma_f32_32x32x16_bf16 v[48:63], a[208:211], v[80:83], v[48:63]\ns_waitcnt lgkmcnt(2)\n" \
    "v_mfma_f32_32x32x16_bf16 v[48:63], a[212:215], v[84:87], v[48:63]\ns_waitcnt lgkmcnt(0)\ns_memrealtime s[72:73]\n" \
    "v_mfma_f32_32x32x16_bf16 v[48:63], a[216:219], v[88:91], v[48:63]\nv_mfma_f32_32x32x16_bf16 v[48:63], a[220:223], v[92:95], v[48:63]\ns_waitcnt lgkmcnt(0)\n" \
    "s_cmp_ge_u32 s77, 64\ns_cselect_b32 s69, s60, s34\ns_sub_u32 s72, s72, s69\ns_lshl_b32 s72, s72, 8\nv_sub_u32 v113, s72, v150\nv_cmp_gt_i32 s[74:75], 0, v113\n" \
    "v_cmp_eq_u32 vcc, s77, v112\ns_nop 0\ns_and_b64 vcc, vcc, s[74:75]\ns_cselect_b32 s69, 1, 0\ns_lshl_b32 s81, s81, s69\ns_nop 7\ns_cmp_eq_u32 s90, 0x7fff0000\n" \
    "s_cbranch_scc1 .Lg15_%=\ns_cmp_lg_u64 s[70:71], 0\ns_cbranch_scc1 .Lg16_%=\n.Lg17_%=:\nglobal_store_dwordx2 v5, v[116:117], s[46:47]\nv_add_u32 v5, 0x10000, v5\n" \
    "s_mov_b32 s90, 0x7fff0000\ns_mov_b32 s91, 0x7fff0000\ns_mov_b32 s92, 0x7fff0000\ns_mov_b32 s93, 0x7fff0000\n.Lg15_%=:\ns_nop 1\nv_cmp_u_f32 vcc, v48, v48\ns_nop 0\n" \
    "s_cbranch_vccnz .Lg7_%=\n.Lg8_%=:\nv_and_or_b32 v52, v52, -2, s78\nv_and_or_b32 v53, v53, -2, s78\nv_and_or_b32 v54, v54, -2, s78\nv_and_or_b32 v55, v55, -2, s78\n" \
    "buffer_store_dwordx4 v[52:55], v0, s[64:67], s87 offen sc1\nv_and_or_b32 v56, v56, -2, s78\nv_and_or_b32 v57, v57, -2, s78\nv_and_or_b32 v58, v58, -2, s78\n" \
    "v_and_or_b32 v59, v59, -2, s78\nbuffer_store_dwordx4 v[56:59], v0, s[64:67], s88 offen sc1\nv_and_or_b32 v60, v60, -2, s78\nv_and_or_b32 v61, v61, -2, s78\n" \
    "v_and_or_b32 v62, v62, -2, s78\nv_and_or_b32 v63, v63, -2, s78\nbuffer_store_dwordx4 v[60:63], v0, s[64:67], s89 offen sc1\ns_cmp_lg_u32 s81, 1\ns_cbranch_scc1 .Lg20_%=\n" \
    "s_xor_b32 s84, s84, 0x10000\nbuffer_load_dword v151, v2, s[64:67], s83 offen sc1\ns_add_u32 m0, s84, 0\ns_add_u32 s69, s83, s48\n" \
    "buffer_load_dwordx4 v0, s[64:67], s69 offen offset:0 sc1 lds\nbuffer_load_dwordx4 v0, s[64:67], s69 offen offset:1024 sc1 lds\ns_add_u32 m0, s84, 8192\n" \
    "s_add_u32 s69, s83, s49\nbuffer_load_dwordx4 v0, s[64:67], s69 offen offset:0 sc1 lds\nbuffer_load_dwordx4 v0, s[64:67], s69 offen offset:1024 sc1 lds\n" \
    "s_add_u32 m0, s84, 16384\ns_add_u32 s69, s83, s50\nbuffer_load_dwordx4 v0, s[64:67], s69 offen offset:0 sc1 lds\n" \
    "buffer_load_dwordx4 v0, s[64:67], s69 offen offset:1024 sc1 lds\n.Lg20_%=:\nv_mov_b64 v[16:17], v[48:49]\nv_mov_b64 v[18:19], v[50:51]\ns_add_u32 s90, s86, 0x1000\n" \
    "s_add_u32 s91, s86, 0x4000\ns_add_u32 s92, s86, 0x5000\ns_mov_b32 s93, s82\ns_mov_b32 s79, s78\ns_cmp_lt_u32 s32, 63\ns_cbranch_scc1 .Lg14_%=\ns_memrealtime s[72:73]\n" \
    "s_waitcnt lgkmcnt(0)\ns_add_u32 s76, s72, s35\n.Lg21_%=:\ns_memrealtime s[72:73]\ns_waitcnt lgkmcnt(0)\ns_sub_u32 s72, s72, s76\ns_cmp_lt_i32 s72, 0\n" \
    "s_cbranch_scc1 .Lg21_%=\n.Lg18_%=:\nbuffer_load_dwordx2 v[108:109], v4, s[64:67], s93 offen sc1\nbuffer_load_dwordx4 v[96:99], v0, s[64:67], s90 offen sc1\n" \
    "buffer_load_dwordx4 v[100:103], v0, s[64:67], s91 offen sc1\nbuffer_load_dwordx4 v[104:107], v0, s[64:67], s92 offen sc1\ns_waitcnt vmcnt(0)\nv_max_u32 v116, v108, v109\n" \
    "v_cmp_eq_u32 s[70:71], -1, v116\ns_nop 1\nv_cndmask_b32 v116, 0, 1, s[70:71]\nv_xor_b32 v96, s79, v96\nv_xor_b32 v97, s79, v97\nv_xor_b32 v98, s79, v98\n" \
    "v_xor_b32 v99, s79, v99\nv_xor_b32 v100, s79, v100\nv_xor_b32 v101, s79, v101\nv_xor_b32 v102, s79, v102\nv_xor_b32 v103, s79, v103\nv_xor_b32 v104, s79, v104\n" \
    "v_xor_b32 v105, s79, v105\nv_xor_b32 v106, s79, v106\nv_xor_b32 v107, s79, v107\nv_or3_b32 v116, v116, v96, v97\nv_or3_b32 v116, v116, v98, v99\n" \
    "v_or3_b32 v116, v116, v100, v101\nv_or3_b32 v116, v116, v102, v103\nv_or3_b32 v116, v116, v104, v105\nv_or3_b32 v116, v116, v106, v107\nv_and_b32 v116, 1, v116\n" \
    "v_cmp_eq_u32 s[70:71], 1, v116\nv_pk_add_f32 v[96:97], v[96:97], v[16:17]\nv_pk_add_f32 v[98:99], v[98:99], v[18:19]\nv_pk_add_f32 v[100:101], v[100:101], v[104:105]\n" \
    "v_pk_add_f32 v[102:103], v[102:103], v[106:107]\nv_pk_add_f32 v[96:97], v[96:97], v[100:101]\nv_pk_add_f32 v[98:99], v[98:99], v[102:103]\nv_cvt_pk_bf16_f32 v96, 0, v96\n" \
    "v_cvt_pk_bf16_f32 v97, 0, v97\nv_cvt_pk_bf16_f32 v98, 0, v98\nv_cvt_pk_bf16_f32 v99, 0, v99\nv_pk_add_f32 v[96:97], v[96:97], v[8:9]\n" \
    "v_pk_add_f32 v[98:99], v[98:99], v[10:11]\nv_cvt_pk_bf16_f32 v96, 0, v96\nv_cvt_pk_bf16_f32 v97, 0, v97\nv_cvt_pk_bf16_f32 v98, 0, v98\nv_cvt_pk_bf16_f32 v99, 0, v99\n" \
    "v_pk_mul_f32 v[96:97], v[96:97], v[12:13]\nv_pk_mul_f32 v[98:99], v[98:99], v[14:15]\nv_cvt_pk_bf16_f32 v96, 0, v96\nv_cvt_pk_bf16_f32 v97, 0, v97\n" \
    "v_cvt_pk_bf16_f32 v98, 0, v98\nv_cvt_pk_bf16_f32 v99, 0, v99\nv_lshlrev_b32 v100, 16, v108\nv_and_b32 v101, 0xffff0000, v108\nv_lshlrev_b32 v102, 16, v109\n" \
    "v_and_b32 v103, 0xffff0000, v109\nv_pk_add_f32 v[96:97], v[96:97], v[100:101]\nv_pk_add_f32 v[98:99], v[98:99], v[102:103]\nv_cvt_pk_bf16_f32 v116, v96, v97\n" \
    "v_cvt_pk_bf16_f32 v117, v98, v99\ns_cmp_lg_u64 s[70:71], 0\ns_cbranch_scc1 .Lg19_%=\nglobal_store_dwordx2 v5, v[116:117], s[46:47]\nv_add_u32 v5, 0x10000, v5\n" \
    "s_mov_b32 s90, 0x7fff0000\ns_mov_b32 s91, 0x7fff0000\ns_mov_b32 s92, 0x7fff0000\ns_mov_b32 s93, 0x7fff0000\n.Lg14_%=:\ns_mov_b32 s38, s85\ns_xor_b32 s80, s80, 0x10000\n" \
    "v_xor_b32 v1, 0x10000, v1\ns_mov_b32 s68, 0\ns_add_u32 s32, s32, 1\ns_cmp_lt_u32 s32, 64\ns_cbranch_scc1 .Lg2_%=\ns_endpgm\n.Lg4_%=:\ns_sleep 8\ns_add_u32 s68, s68, 1\n" \
    "s_and_b32 s69, s68, 63\ns_cbranch_scc1 .Lg3_%=\ns_cmp_gt_u32 s68, 0x400000\ns_cbranch_scc1 .Lg1_%=\nbuffer_load_dword v253, off, s[64:67], 0 sc1\ns_waitcnt vmcnt(0)\n" \
    "v_readfirstlane_b32 s69, v253\ns_nop 0\ns_cmp_eq_u32 s69, 1\ns_cbranch_scc0 .Lg3_%=\ns_endpgm\n.Lg13_%=:\ns_waitcnt vmcnt(0)\ns_add_u32 s68, s68, 1\n" \
    "s_and_b32 s69, s68, 63\ns_cbranch_scc1 .Lg12_%=\ns_cmp_gt_u32 s68, 0x400000\ns_cbranch_scc1 .Lg1_%=\nbuffer_load_dword v253, off, s[64:67], 0 sc1\ns_waitcnt vmcnt(0)\n" \
    "v_readfirstlane_b32 s69, v253\ns_nop 0\ns_cmp_eq_u32 s69, 1\ns_cbranch_scc0 .Lg12_%=\ns_endpgm\n.Lg16_%=:\ns_sleep 4\n" \
    "buffer_load_dwordx2 v[108:109], v4, s[64:67], s93 offen sc1\nbuffer_load_dwordx4 v[96:99], v0, s[64:67], s90 offen sc1\n" \
    "buffer_load_dwordx4 v[100:103], v0, s[64:67], s91 offen sc1\nbuffer_load_dwordx4 v[104:107], v0, s[64:67], s92 offen sc1\ns_waitcnt vmcnt(0)\nv_max_u32 v116, v108, v109\n" \
    "v_cmp_eq_u32 s[70:71], -1, v116\ns_nop 1\nv_cndmask_b32 v116, 0, 1, s[70:71]\nv_xor_b32 v96, s79, v96\nv_xor_b32 v97, s79, v97\nv_xor_b32 v98, s79, v98\n" \
    "v_xor_b32 v99, s79, v99\nv_xor_b32 v100, s79, v100\nv_xor_b32 v101, s79, v101\nv_xor_b32 v102, s79, v102\nv_xor_b32 v103, s79, v103\nv_xor_b32 v104, s79, v104\n" \
    "v_xor_b32 v105, s79, v105\nv_xor_b32 v106, s79, v106\nv_xor_b32 v107, s79, v107\nv_or3_b32 v116, v116, v96, v97\nv_or3_b32 v116, v116, v98, v99\n" \
    "v_or3_b32 v116, v116, v100, v101\nv_or3_b32 v116, v116, v102, v103\nv_or3_b32 v116, v116, v104, v105\nv_or3_b32 v116, v116, v106, v107\nv_and_b32 v116, 1, v116\n" \
    "v_cmp_eq_u32 s[70:71], 1, v116\nv_pk_add_f32 v[96:97], v[96:97], v[16:17]\nv_pk_add_f32 v[98:99], v[98:99], v[18:19]\nv_pk_add_f32 v[100:101], v[100:101], v[104:105]\n" \
    "v_pk_add_f32 v[102:103], v[102:103], v[106:107]\nv_pk_add_f32 v[96:97], v[96:97], v[100:101]\nv_pk_add_f32 v[98:99], v[98:99], v[102:103]\nv_cvt_pk_bf16_f32 v96, 0, v96\n" \
    "v_cvt_pk_bf16_f32 v97, 0, v97\nv_cvt_pk_bf16_f32 v98, 0, v98\nv_cvt_pk_bf16_f32 v99, 0, v99\nv_pk_add_f32 v[96:97], v[96:97], v[8:9]\n" \
    "v_pk_add_f32 v[98:99], v[98:99], v[10:11]\nv_cvt_pk_bf16_f32 v96, 0, v96\nv_cvt_pk_bf16_f32 v97, 0, v97\nv_cvt_pk_bf16_f32 v98, 0, v98\nv_cvt_pk_bf16_f32 v99, 0, v99\n" \
    "v_pk_mul_f32 v[96:97], v[96:97], v[12:13]\nv_pk_mul_f32 v[98:99], v[98:99], v[14:15]\nv_cvt_pk_bf16_f32 v96, 0, v96\nv_cvt_pk_bf16_f32 v97, 0, v97\n" \
    "v_cvt_pk_bf16_f32 v98, 0, v98\nv_cvt_pk_bf16_f32 v99, 0, v99\nv_lshlrev_b32 v100, 16, v108\nv_and_b32 v101, 0xffff0000, v108\nv_lshlrev_b32 v102, 16, v109\n" \
    "v_and_b32 v103, 0xffff0000, v109\nv_pk_add_f32 v[96:97], v[96:97], v[100:101]\nv_pk_add_f32 v[98:99], v[98:99], v[102:103]\nv_cvt_pk_bf16_f32 v116, v96, v97\n" \
    "v_cvt_pk_bf16_f32 v117, v98, v99\ns_cmp_lg_u64 s[70:71], 0\ns_cbranch_scc0 .Lg17_%=\ns_add_u32 s68, s68, 1\ns_and_b32 s69, s68, 63\ns_cbranch_scc1 .Lg16_%=\n" \
    "s_cmp_gt_u32 s68, 0x400000\ns_cbranch_scc1 .Lg1_%=\nbuffer_load_dword v253, off, s[64:67], 0 sc1\ns_waitcnt vmcnt(0)\nv_readfirstlane_b32 s69, v253\ns_nop 0\n" \
    "s_cmp_eq_u32 s69, 1\ns_cbranch_scc0 .Lg16_%=\ns_endpgm\n.Lg19_%=:\ns_sleep 4\ns_add_u32 s68, s68, 1\ns_and_b32 s69, s68, 63\ns_cbranch_scc1 .Lg18_%=\n" \
    "s_cmp_gt_u32 s68, 0x400000\ns_cbranch_scc1 .Lg1_%=\nbuffer_load_dword v253, off, s[64:67], 0 sc1\ns_waitcnt vmcnt(0)\nv_readfirstlane_b32 s69, v253\ns_nop 0\n" \
    "s_cmp_eq_u32 s69, 1\ns_cbranch_scc0 .Lg18_%=\ns_endpgm\n.Lg7_%=:\nv_mov_b32 v120, 0\nv_mov_b32 v121, v1\ns_mov_b32 s76, 14\n.Lg10_%=:\n" \
    "ds_read_b128 v[124:127], v121 offset:0\nds_read_b128 v[128:131], v121 offset:1024\nds_read_b128 v[132:135], v121 offset:2048\nds_read_b128 v[136:139], v121 offset:3072\n" \
    "v_add_u32 v121, 0x1000, v121\ns_waitcnt lgkmcnt(0)\nv_max3_u32 v120, v120, v124, v125\nv_max3_u32 v120, v120, v126, v127\nv_max3_u32 v120, v120, v128, v129\n" \
    "v_max3_u32 v120, v120, v130, v131\nv_max3_u32 v120, v120, v132, v133\nv_max3_u32 v120, v120, v134, v135\nv_max3_u32 v120, v120, v136, v137\n" \
    "v_max3_u32 v120, v120, v138, v139\ns_sub_u32 s76, s76, 1\ns_cmp_lg_u32 s76, 0\ns_cbranch_scc1 .Lg10_%=\nv_cmp_eq_u32 vcc, -1, v120\ns_nop 0\ns_cbranch_vccz .Lg8_%=\n" \
    ".Lg9_%=:\ns_mov_b32 s81, 0\ns_barrier\ns_sleep 4\ns_add_u32 s68, s68, 1\ns_and_b32 s69, s68, 63\ns_cbranch_scc1 .Lg2_%=\ns_cmp_gt_u32 s68, 0x400000\n" \
    "s_cbranch_scc1 .Lg1_%=\nbuffer_load_dword v253, off, s[64:67], 0 sc1\ns_waitcnt vmcnt(0)\nv_readfirstlane_b32 s69, v253\ns_nop 0\ns_cmp_eq_u32 s69, 1\n" \
    "s_cbranch_scc0 .Lg2_%=\ns_endpgm\n.Lg1_%=:\nv_mov_b32 v253, 1\nbuffer_store_dword v253, off, s[64:67], 0 sc1\ns_endpgm\n"
typedef unsigned v32u __attribute__((ext_vector_type(32)));
typedef unsigned v16u __attribute__((ext_vector_type(16)));
#define ORD 1
template <bool isV>
__device__ __forceinline__ void s1_main(const Args& a, unsigned char* lds, const int xg, const int ii) {
    const int wave = a.pad0, head = xg * 4 + (ii >> 2), set = (ii >> 1) & 1;
    char* const ws = a.ws;
    unsigned* const ctl = (unsigned*)(ws + O_CTL);
    const unsigned pubb = isV ? O_UM + (wave & 1) * 262144 + head * 256 : O_EP + ((wave & 1) * 4 + (head >> 3)) * 32768;
    const unsigned psb = isV ? O_UX + (wave & 1) * MiB + (head >> 3) * 16384 + (head & 7) * 1024 : O_PS + (wave * 4 + (head >> 3)) * 16384;
    v16bf w[52], pend[12];
    v4u rX = mkrs(isV ? a.Wv : wave < 2 ? a.Wq : a.Wk, DM * HD * 2), rO = mkrs(a.Wo, DM * HD * 2);
    unsigned bo[4];
#pragma unroll
    for (int i = 0; i < 4; ++i) {
        const int lane = lane_id(), row = lane >> 3, ch = (lane & 7) ^ ((2 * (i & 1) + (row >> 2)) << 1);
        bo[i] = i < 2 ? (((isV ? 64 * (wave >> 1) : 0) + row) * HD + head * DH + 64 * (wave & 1) + 8 * ch) * 2 : ((head * DH + row) * DM + 256 * wave + 8 * ch) * 2;
    }
    asm volatile("s_nop 4" : "+s"(rX), "+s"(rO));
    const int kx = ORD * set;
    {
        const int lane = lane_id(), l15 = lane & 15, gb = lane >> 4;
        auto bufp = [&](int u, int wv) { const int b = (u + 4) % 5; return lds + (b < 3 ? L_W + wv * 24576 + b * 8192 : L_H + wv * 16384 + (b - 3) * 8192); };
        auto issue = [&](auto uu) {
            constexpr int u = decltype(uu)::v, c = u;
            constexpr bool wo = isV && c >= 8;
            const unsigned d = (unsigned)(size_t)(LAS unsigned char*)bufp(u, wave);
#pragma unroll
            for (int r8 = 0; r8 < 8; ++r8)
                bdma<(NTW & (u < 4 ? 1 : u < 8 ? 2 : 64))>(wo ? rO : rX, wo ? bo[2 + (r8 & 1)] : bo[r8 & 1], wo ? ((64 * (c & 1) + 8 * r8) * DM + 64 * ((c - 8) >> 1)) * 2 : (((isV ? 128 : 64) * c + 8 * r8) ^ (512 * kx)) * HD * 2, d + r8 * 1024);
        };
        const unsigned q = l15 >> 2, p = lane & 3;
        const unsigned row0 = 16 * (gb >> 1) + 4 * (gb & 1) + (q & 1) + 8 * (q >> 1);
        const unsigned sw = ((gb & 1) | ((q >> 1) << 1)) << 1;
        auto flush = [&](int j) {
            const v8u v = __builtin_bit_cast(v8u, pend[j]);
            unsigned char* d = lds + L_W + wave * 24576 + j * 2048 + lane * 16;
            *(LAS v4u*)d = (v4u){v[0], v[1], v[2], v[3]};
            *(LAS v4u*)(d + 1024) = (v4u){v[4], v[5], v[6], v[7]};
        };
        sfor<0, 5>(issue);
        sfor<0, 16>([&](auto cc) {
            constexpr int u = decltype(cc)::v, c = u;
            asm volatile("s_waitcnt vmcnt(%0)\n\ts_barrier" :: "n"(u == 0 ? 32 : u < 13 ? 24 : 8 * (15 - u)) : "memory");
            if constexpr (u > 0 && u + 4 < 16) issue(IC<u + 4>{});
            if (u >= 12 && u < 15) { flush(4 * u - 48); flush(4 * u - 47); flush(4 * u - 46); flush(4 * u - 45); }
            v16bf t[4];
#pragma unroll
            for (int n = 0; n < 4; ++n) {
                constexpr bool pk = isV && c >= 8;
                const unsigned char* buf = bufp(u, pk ? wave : (n & 2) + (wave >> 1));
                const unsigned o = pk ? (32 * (n & 1) + 16 * (gb & 1) + 4 * (gb >> 1) + (q & 1)) * 128 + (((4 * (n >> 1) + 2 * (q >> 1) + (p >> 1)) ^ (gb & 2)) << 4) + (p & 1) * 8
                                      : row0 * 128 + (((4 * (wave & 1) + 2 * (n & 1) + (p >> 1)) ^ sw) << 4) + (p & 1) * 8;
                const unsigned o2 = pk ? (o ^ 64) + 8 * 128 : o + 32 * 128;
                const v2u r0 = tr_rd(buf, o), r1 = tr_rd(buf, o + 2 * 128), r2 = tr_rd(buf, o2), r3 = tr_rd(buf, o2 + 2 * 128);
                const v8u v = {r0.x, r0.y, r1.x, r1.y, r2.x, r2.y, r3.x, r3.y};
                t[n] = __builtin_bit_cast(v16bf, v);
            }
            asm volatile("s_waitcnt lgkmcnt(0)" ::: "memory");
            sfor<0, 4>([&](auto nn) {
                constexpr int n = decltype(nn)::v, j = lsl(isV, c, n), r = ridx(isV, c, n);
                if constexpr (j >= 0) { pend[j] = t[n]; asm volatile("" : "+v"(pend[j])); }
                else {
                    w[r] = t[n];
                    if constexpr (r < 32) asm volatile("" : "+a"(w[r])); else asm volatile("" : "+v"(w[r]));
                }
            });
        });
    }
    RSTAMP(74, 0);
    wait_go(ws);
    const unsigned pol = wait_set(ws, ctl);
    lds_barrier();
    RSTAMP(74, 1);
    const unsigned lb0 = (unsigned)(size_t)(LAS unsigned char*)lds;
    const unsigned lane = lane_id(), l15 = lane & 15, g = lane >> 4, q = l15 >> 2, p = lane & 3, l16 = lane * 16, to = g * 128 + (l15 & 7) * 16, xb = ((g & 1) << 1) | (g >> 1);
    const unsigned r = 8 * wave + (lane >> 3), ao = r * 2048 + 16 * ((lane & 7) ^ ((r >> 1) & 7)) + 1024, ra = lb0 + L_H + (l15 >> 3) * 2048 + (l15 & 7) * 128;
    const v16u c0 = {(lane & 8) ? 0xEEEEu : 0x4444u, lb0 + L_H + l16, l16, lb0 + L_W + wave * 24576 + l16, lb0 + L_T + wave * 512 + q * 128 + (g & 1) * 64 + (g >> 1) * 32 + p * 2,
                     lb0 + (isV ? L_T + to : L_QX + (wave >> 1) * 4096 + (wave & 1) * 128 + g * 128 + to), isV ? lb0 + L_QX + g * 128 + to : lb0 + L_T + to,
                     isV ? xb * 16384 + l15 * 16 : g < 2 ? (l15 >> 3) * 2048 + (head & 7) * 256 + to : lane < 40 ? 4096 + (head & 7) * 128 + l15 * 16 : 0x7FFF0000u, isV ? xb * 65536 + l15 * 16 : lb0 + L_T + 2048 + (to ^ (l15 >> 3) * 64),
                     ra + 16 * (g ^ (l15 >> 1)), ra + 16 * ((4 + g) ^ (l15 >> 1)), ao, ao - 896,
                     0u, lb0 + (isV ? L_QX : L_KX) + wave * 1024 + l16, 0u};
    const unsigned sd = ((pol ^ (wave >> 1)) & 1) * 8192;
    PARS;
    const v16u s0 = {0x3e0293eeu, 0x3db504f3u, 0x3fb8aa3bu, (unsigned)set, (unsigned)wave, O_E1 + (((xg >> 1) & 1) * 4 + wave) * 16384 + ((pol ^ (xg >> 2)) & 1) * 8192 + 2048 * kx, pubb + sd, psb + (isV ? sd : 0),
                     O_H1 - 1024 + set * 65536, lb0 + L_H + wave * 2048, hw(pol, xg) + H_C + 512 * set, par(P_R), par(P_A) + par(P_B), par(P_X), lb0 + L_ST + (wave == STAMP_WAVE ? 0 : MiB), lb0 + L_ST};
    u64 cm[4];
#pragma unroll
    for (int i = 0; i < 4; ++i) cm[i] = __ballot(16 * (l15 >> 3) + 4 * g + i <= 8 * wave + (l15 & 7));
    const u64 dbg = DBG(a, blockIdx.x);
    const v16u s1 = {isV ? (unsigned)(C_F + 32 * (128 * set + 32 * wave + head)) * 4 : 0x00FFFFFFu, isV ? set : 0x00FF00FFu, isV ? set : (unsigned)cm[0], (unsigned)(cm[0] >> 32), (unsigned)cm[1], (unsigned)(cm[1] >> 32),
                     (unsigned)cm[2], (unsigned)(cm[2] >> 32), (unsigned)cm[3], (unsigned)(cm[3] >> 32), (unsigned)dbg, (unsigned)(dbg >> 32), lb0 + L_T + wave * 512, ii < 4 ? hw(pol, xg) + H_D + 4 * wave : 0u, 1024u * kx,
                     head < (isV ? 20 : 28) ? O_D + (isV ? D_X2 : 0) + head * 8192 + wave * 2048 : 0x70000000u};
    const v4u rws = mkrs(ws, WS_END);
    v32u ww[13];
#pragma unroll
    for (int i = 0; i < 13; ++i) {
        const v8u x0 = __builtin_bit_cast(v8u, w[4 * i]), x1 = __builtin_bit_cast(v8u, w[4 * i + 1]), x2 = __builtin_bit_cast(v8u, w[4 * i + 2]), x3 = __builtin_bit_cast(v8u, w[4 * i + 3]);
        ww[i] = __builtin_shufflevector(__builtin_shufflevector(x0, x1, 0, 1, 2, 3, 4, 5, 6, 7, 8, 9, 10, 11, 12, 13, 14, 15), __builtin_shufflevector(x2, x3, 0, 1, 2, 3, 4, 5, 6, 7, 8, 9, 10, 11, 12, 13, 14, 15),
                                        0, 1, 2, 3, 4, 5, 6, 7, 8, 9, 10, 11, 12, 13, 14, 15, 16, 17, 18, 19, 20, 21, 22, 23, 24, 25, 26, 27, 28, 29, 30, 31);
    }
#define S1_CALL(TXT) asm volatile("s_nop 4\n" TXT :: "{v[0:15]}"(c0), "{s[32:47]}"(s0), "{s[48:63]}"(s1), "{s[64:67]}"(rws), \
              "{a[0:31]}"(ww[0]), "{a[32:63]}"(ww[1]), "{a[64:95]}"(ww[2]), "{a[96:127]}"(ww[3]), "{a[128:159]}"(ww[4]), "{a[160:191]}"(ww[5]), "{a[192:223]}"(ww[6]), "{a[224:255]}"(ww[7]), \
              "{v[92:123]}"(ww[8]), "{v[124:155]}"(ww[9]), "{v[156:187]}"(ww[10]), "{v[188:219]}"(ww[11]), "{v[220:251]}"(ww[12]) : "memory", "s95", "v253")
    if constexpr (isV) S1_CALL(ASM_V); else S1_CALL(ASM_Q);
}

__device__ __forceinline__ float gelu_tanh(float v) {
    const float e = v * (-2.302208198f - 0.1029432397f * (v * v));
    return v * __builtin_amdgcn_rcpf(1.f + __builtin_amdgcn_exp2f(e));
}
__device__ __forceinline__ v4u unsent4(v4u v) { return (v4u){unsent(v.x), unsent(v.y), unsent(v.z), unsent(v.w)}; }
__device__ __forceinline__ unsigned rt_now() { return (unsigned)__builtin_amdgcn_s_memrealtime(); }
__device__ __forceinline__ void rt_delay(unsigned ticks  , unsigned t0 = rt_now()) {
    while ((unsigned)__builtin_amdgcn_s_memrealtime() - t0 < ticks) __builtin_amdgcn_s_sleep(2);
}
__device__ __forceinline__ float wave_sum(float x) {
    x += __uint_as_float(__builtin_amdgcn_update_dpp(0, __float_as_uint(x), 0xB1, 0xF, 0xF, false));
    x += __uint_as_float(__builtin_amdgcn_update_dpp(0, __float_as_uint(x), 0x4E, 0xF, 0xF, false));
    x += __uint_as_float(__builtin_amdgcn_update_dpp(0, __float_as_uint(x), 0x124, 0xF, 0xF, false));
    x += __uint_as_float(__builtin_amdgcn_update_dpp(0, __float_as_uint(x), 0x128, 0xF, 0xF, false));
    return red32_add(red16_add(x));
}
__device__ __forceinline__ void ln_row(const v4u (&xr)[2], const f32x4 (&c1)[4], const f32x4 (&sh)[4], v4u (&o)[2]) {
    f32x2 v[8];
    f32x2 s2 = {0.f, 0.f};
#pragma unroll
    for (int j = 0; j < 8; ++j) { v[j] = (f32x2){lo16f(xr[j >> 2][j & 3]), hi16f(xr[j >> 2][j & 3])}; s2 += v[j]; }
    const float mean = wave_sum(s2[0] + s2[1]) * (1.f / DM);
    f32x2 q2 = {0.f, 0.f};
#pragma unroll
    for (int j = 0; j < 8; ++j) { const f32x2 d = v[j] - mean; q2 += d * d; }
    const float vs = wave_sum(q2[0] + q2[1]);
    const float mb = rbf(mean), vb = rbf(vs * (1.f / DM));
    const float rsd = 1.f / rbf(sqrtf(rbf(vb + 1e-5f)));
    auto r2 = [](f32x2 z) { const unsigned u = pack2(z[0], z[1]); return (f32x2){lo16f(u), hi16f(u)}; };
#pragma unroll
    for (int j = 0; j < 8; ++j) {
        const f32x2 cc = {c1[j >> 1][2 * (j & 1)], c1[j >> 1][2 * (j & 1) + 1]}, ss = {sh[j >> 1][2 * (j & 1)], sh[j >> 1][2 * (j & 1) + 1]};
        const f32x2 y = r2(r2(r2(v[j] - mb) * rsd) * cc) + ss;
        o[j >> 2][j & 3] = pack2(y[0], y[1]);
    }
}
template <int N> __device__ __forceinline__ void ld_mod(rsrc_t rs, unsigned* ctl, const int (&col)[N], f32x4* m) {
    for (unsigned spins = 0;;) {
        unsigned mx = 0;
#pragma unroll
        for (int i = 0; i < N; ++i) { const v4u v = bld16(rs, O_MOD + col[i] * 4); mx = max(mx, umax4(v)); m[i] = __builtin_bit_cast(f32x4, v); }
        if (!__any(mx == SENT)) break;
        __builtin_amdgcn_s_sleep(6);
        if (spin_fail(spins, ctl + C_ABORT)) asm volatile("s_endpgm");
    }
}
__device__ __forceinline__ void set_m0(unsigned v) { asm volatile("s_mov_b32 m0, %0" :: "s"(v) : "m0"); }
__device__ __forceinline__ void mod8(rsrc_t rs, int col, f32x4 (&m)[2]) {
    f32x4 p[4][2];
#pragma unroll
    for (int k = 0; k < 8; ++k) p[k >> 1][k & 1] = __builtin_bit_cast(f32x4, bld16(rs, O_MODP + ((k >> 1) * 2048 + col + 4 * (k & 1)) * 4));
#pragma unroll
    for (int h = 0; h < 2; ++h) {
        const f32x4 t = (p[0][h] + p[1][h]) + (p[2][h] + p[3][h]);
#pragma unroll
        for (int i = 0; i < 4; ++i) { float v = rbf(t[i]); if (col >= DM) v = rbf(1.f + v); m[h][i] = v; }
    }
}
template <int N, class F> __device__ __forceinline__ void mod_cols(const Args& a, rsrc_t rs, unsigned char* lds, int cg, int k0, F&& mid) {
    const int tid = otid();
    v4u wv[N];
    const v2u cr = *(const v2u*)(a.c + 4 * tid);
#pragma unroll
    for (int i = 0; i < N; ++i) {
        const v4u* p = (const v4u*)(a.w_mod + (size_t)(k0 + (tid >> 3) + 32 * i) * 6144 + cg * 64 + 8 * (tid & 7));
        wv[i] = NTW & 4 ? __builtin_nontemporal_load(p) : *p;
    }
    SB(); mid(); SB();
    f32x4 sv = {lo16f(cr.x), hi16f(cr.x), lo16f(cr.y), hi16f(cr.y)};
#pragma unroll
    for (int i = 0; i < 4; ++i) sv[i] = rbf(sv[i] / (1.f + __expf(-sv[i])));
    *(LAS f32x4*)(lds + 16384 + tid * 16) = sv;
    lds_barrier();
    f32x2 acc[4] = {{0.f, 0.f}, {0.f, 0.f}, {0.f, 0.f}, {0.f, 0.f}};
#pragma unroll
    for (int i = 0; i < N; ++i) {
        const float sv = *(const LAS float*)(lds + 16384 + (k0 + (tid >> 3) + 32 * i) * 4);
#pragma unroll
        for (int j = 0; j < 4; ++j) acc[j] += sv * (f32x2){lo16f(wv[i][j]), hi16f(wv[i][j])};
    }
    unsigned char* red = lds + 20480;
    *(LAS f32x4*)(red + (tid >> 3) * 272 + (tid & 7) * 32) = (f32x4){acc[0][0], acc[0][1], acc[1][0], acc[1][1]};
    *(LAS f32x4*)(red + (tid >> 3) * 272 + (tid & 7) * 32 + 16) = (f32x4){acc[2][0], acc[2][1], acc[3][0], acc[3][1]};
    lds_barrier();
    if (tid < 64) {
        f32x4 t = {0.f, 0.f, 0.f, 0.f};
#pragma unroll
        for (int i = 0; i < 8; ++i) t += *(const LAS f32x4*)(red + ((tid >> 4) * 8 + i) * 272 + (tid & 15) * 16);
#pragma unroll
        for (int i = 0; i < 4; ++i) { float v = red32_add(red16_add(t[i])); if (N == 32) { v = rbf(v); if ((cg >> 4) == 4) v = rbf(1.f + v); } t[i] = v; }
        if (tid < 16) bst16(rs, (N == 32 ? O_MOD : O_MODP + k0 * 32) + (cg * 64 + 4 * tid) * 4, unsent4(__builtin_bit_cast(v4u, t)));
    }
}
#define TD_A 300
#define TD_L 1150
#define TD_G 120
constexpr int NARM0 = 2068, NARM1 = 4564;
__device__ __forceinline__ unsigned arm0(unsigned v) {
    if (v < 84) return 4096 + v * 1024;
    if ((v -= 84) < 640) return (v < 320 ? O_EP : O_PS - MiB / 2) + v / 5 * 8192 + v % 5 * 1024;
    if ((v -= 640) < 1024) return O_E1 + 131072 + v * 1024;
    if ((v -= 1024) < 320) return O_D + D_X2 + v / 160 * D_SLOT + v % 160 * 1024;
    if ((v -= 320) < 448) return O_D + v / 224 * D_SLOT + v % 224 * 1024;
    return O_SL + (v - 448) * 1024;
}
__device__ __forceinline__ void f_start(const Args& a, rsrc_t rs, unsigned char* lds, int idx  , unsigned t0) {
    const int tid = otid(), lane = tid & 63, wave = a.pad0;
    unsigned* const ctl = (unsigned*)(a.ws + O_CTL);
    const int r0 = 4 * idx + wave;
    const bool boss = idx == 63;
    auto serve = [&](bool all) { if (all || !ldg4_sc1(a.ws, O_GO + tid * 128)) __hip_atomic_store((gu32*)(a.ws + O_GO + tid * 128), 1u, RLX_AGENT); };
    if (boss) bst16(rs, tid * 16, (v4u){0u, 0u, 0u, 0u});
    v4u xr[6][2];
    auto ldx = [&](int k, int row) { for (int c = 0; c < 2; ++c) xr[k][c] = *(const v4u*)(a.x + (size_t)row * DM + 512 * c + 8 * lane); };
    SB();
    mod_cols<8>(a, rs, lds, idx & 31, (idx >> 5) * 256, [&]() {
        if (boss) { asm volatile("s_waitcnt vmcnt(8)\n\ts_barrier" ::: "memory"); rt_delay(TD_G, t0); serve(true); }
        ldx(0, r0); if (idx == 55) ldx(1, r0 + 16);
    });
    RSTAMP(73, 1);
    if (boss) serve(false); else if (wave == 0) { wait_vm0(); wait_go(a.ws); }
    drained(ctl + C_M + 32 * (idx & 7));
    RSTAMP(73, 2);
    if (idx >= 64) {
        for (unsigned v = r0 - 256; v < NARM0; v += 256) bst16(rs, arm0(v) + lane * 16, v < 60 ? (v4u){0u, 0u, 0u, 0u} : sentv());
        if (idx < 80) {
            const unsigned u = wave * 256 + ((idx >> 1) & 3) * 64 + lane;
            const v4u m = unsent4(*(const v4u*)(a.mem0 + (u & 7) * DM + (u >> 4) * 16 + ((u >> 3) & 1) * 8));
            bst16(rs, O_E1 + (idx - 64) * 8192 + tid * 16, m); bst16(rs, O_E1 + (idx - 64) * 8192 + 4096 + tid * 16, m);
        }
    }
    if (idx == 59) {
        unsigned best[2] = {~0u, ~0u};
        for (int r = 0; r < NPR; ++r) {
            const u64 t0 = __builtin_readcyclecounter();
            asm volatile("s_mov_b64 exec, 1\n\ts_nop 4\n\tglobal_store_dword %0, %1, %2 sc1\n\ts_waitcnt vmcnt(0)\n\ts_mov_b64 exec, -1" :: "v"(O_POL + 256 + ((r ^ wave) & 1) * 8192 + wave * 1280), "v"(0u), "s"(a.ws) : "memory");
            const unsigned d = (unsigned)(__builtin_readcyclecounter() - t0);
            if ((r ^ wave) & 1) best[1] = min(best[1], d); else best[0] = min(best[0], d);
        }
        *(LAS v2u*)(lds + 40960 + wave * 8) = (v2u){best[0], best[1]};
        lds_barrier();
        const v4u p = lds16(lds + 40960), q = lds16(lds + 40976);
        const bool second = min(min(p.y, p.w), min(q.y, q.w)) < min(min(p.x, p.z), min(q.x, q.z));
        if (tid == 0) __hip_atomic_store((gu32*)(a.ws + O_POL), (second ^ (__builtin_amdgcn_s_getreg((3 << 11) | 20) >> 2)) & 1, RLX_AGENT);
    }
    RSTAMP(72, 0);
    for (unsigned spins = 0; idx != 59 && __any(lane < 8 && __hip_atomic_load((gu32*)(ctl + C_M + 32 * (lane & 7)), RLX_AGENT) < 16u);) {
        if (boss) serve(false);
        __builtin_amdgcn_s_sleep(8);
        if (spin_fail(spins, ctl + C_ABORT)) asm volatile("s_endpgm");
    }
    RSTAMP(73, 0);
    {
        f32x4 m[2];
        mod8(rs, 8 * tid, m);
        *(LAS f32x4*)(lds + 32 * tid) = m[0]; *(LAS f32x4*)(lds + 32 * tid + 16) = m[1];
    }
    lds_barrier();
    f32x4 c1[4], sh[4];
#pragma unroll
    for (int i = 0; i < 4; ++i) { const int col = 512 * (i >> 1) + 8 * lane + 4 * (i & 1); sh[i] = *(const LAS f32x4*)(lds + col * 4); c1[i] = *(const LAS f32x4*)(lds + (DM + col) * 4); }
    auto row = [&](int k, int r) {
        v4u o[2];
        ln_row(xr[k], c1, sh, o);
#pragma unroll
        for (int c = 0; c < 2; ++c) {
            const bool od = lane & 1;
            const unsigned y0 = __builtin_amdgcn_update_dpp(0, od ? o[c].x : o[c].z, 0xB1, 0xF, 0xF, false), y1 = __builtin_amdgcn_update_dpp(0, od ? o[c].y : o[c].w, 0xB1, 0xF, 0xF, false);
            bst16(rs, O_H1 + (unsigned)(r * DM + 512 * c + 8 * lane) * 2, od ? (v4u){y0, o[c].z, y1, o[c].w} : (v4u){o[c].x, y0, o[c].y, y1});
        }
    };
    if (idx != 59) row(0, r0);
    if (idx == 55) row(1, r0 + 16);
    drained(ctl + C_A + 32 * (idx & 7));
    RSTAMP(75, 2);
    if (idx >= 64) return;
    if (boss) serve(false);
    rt_delay(TD_L, t0);
    const unsigned pol = wait_set(a.ws, ctl);
    mod_cols<32>(a, rs, lds, idx + 32, 0, []() {});
    if (boss) {
        for (unsigned spins = 0; (__builtin_amdgcn_readfirstlane(ldg4_sc1(a.ws, hw(pol, 7) + H_C + 512)) & 127) < 3;) {
            serve(false);
            __builtin_amdgcn_s_sleep(64);
            if (spin_fail(spins, ctl + C_ABORT)) break;
        }
    }
    if (idx >= 32) return;
    for (unsigned v = NARM0 + r0; v < NARM1; v += 128) bst16(rs, arm0(v) + lane * 16, sentv());
    for (int h = 0; h < 2; ++h) {
#pragma unroll
        for (int k = 0; k < 6; ++k) ldx(k, 512 + r0 + 128 * (6 * h + k));
#pragma unroll
        for (int k = 0; k < 6; ++k) row(k, 512 + r0 + 128 * (6 * h + k));
    }
    drained(ctl + C_H);
}
#define TD_W 1300
__device__ __forceinline__ unsigned r_ew(unsigned pol, unsigned lane) { return ((lane >> 4) & 1) * 65536 + (((O_E1 >> 21) ^ pol ^ (lane >> 5)) & 1) * 8192 + (lane & 15) * 16; }
__device__ __forceinline__ unsigned r_e1(int cb) { return O_E1 + ((cb >> 2) & 3) * 16384 + (cb >> 4) * 1024 + (cb & 3) * 256; }
__device__ __forceinline__ unsigned r_db(int cb) { return D_X2 + (cb >> 3) * 16384 + (cb & 7) * 1024; }
__device__ __forceinline__ unsigned r_st(int cb, unsigned lane) { return D_ST + (cb >> 5) * 16384 + (lane >> 5) * 8192 + (cb & 31) * 256 + (lane & 31) * 8; }
__device__ __forceinline__ void r_main(const Args& a, rsrc_t rs, unsigned char* lds, int xg, int i8, unsigned* ctl, unsigned t0) {
    const unsigned lane = lane_id(), wave = a.pad0, l15 = lane & 15, g = lane >> 4, q = l15 >> 2, p = lane & 3, cb = 8 * xg + i8;
    const unsigned pol = wait_set(a.ws, ctl), lb = (unsigned)(size_t)(LAS unsigned char*)lds;
    *(volatile LAS unsigned*)(lds + 4096) = 0u;
    lds_barrier();
    RSTAMP(74, 0);
    const v16u c0 = {(lane & 8) ? 0xEEEEu : 0x4444u, lane * 16, 4096 + lane * 16, lb + wave * 1024 + l15 * 16 + (g & 1) * 8 + (g >> 1) * 4,
                     lb + wave * 1024 + (8 * (p >> 1) + q) * 16 + (p & 1) * 8, lb + wave * 1024 + ((2 * (g & 1) + (p >> 1)) ^ (cb & 1)) * 256 + (8 * (g >> 1) + q) * 16 + (p & 1) * 8,
                     r_ew(pol, lane), (wave * 32 + (lane & 31)) * 2048 + cb * 32 + (lane >> 5) * 16, (C_F + 32 * (128 * (wave & 1) + 32 * (cb >> 4) + (lane & 31))) * 4, r_st(cb, lane) - r_db(cb), lb + 4096, (g >> 1) * 1024 + (g & 1) * 256 + l15 * 16,
                     O_MOD + (2 * DM + 16 * cb + 8 * (lane >> 5)) * 4, (lane >> 3) * 16384 + 4096 + (lane & 7) * 128, 0u, 0u};
    const unsigned sd = ((pol ^ (xg >> 2)) & 1) * 8192;
    PARS;
    const v16u s0 = {wave, wave, par(P_A), par(P_B), par(P_EU), par(P_DMIN), par(P_R), par(P_TP), par(P_TU), par(P_T0),
                     O_UX + (cb & 31) * 65536 + sd, O_UM + (cb & 31) * 16384 + sd, O_EP + ((xg >> 1) & 1) * 131072 + sd, O_PS + sd, r_e1(cb), O_D + r_db(cb)};
    const u64 dbg = DBG(a, blockIdx.x);
    const v8u s1 = {i8 ? 0u : hw(pol, xg) + H_C + 512 * (wave & 1), (cb & 1) * 65536, lb + 8192 + wave * 32, lb + 8192, (unsigned)dbg, (unsigned)(dbg >> 32), (unsigned)(u64)a.x, (unsigned)((u64)a.x >> 32)};
    const v4u rws = mkrs(a.ws, WS_END);
    const unsigned n0 = __builtin_amdgcn_readfirstlane(rt_now() / par(P_R));
    const v8u s2 = {n0 * par(P_R), (n0 & 1) * 128, par(P_TF), par(P_X2), hw(pol, xg) + H_D, par(P_TT), t0, t0};
    asm volatile("s_nop 4\n" ASM_R :: "{v[0:15]}"(c0), "{s[32:47]}"(s0), "{s[48:55]}"(s1), "{s[56:63]}"(s2), "{s[64:67]}"(rws) : "memory", "s95", "v253", "a255");
}

typedef __attribute__((ext_vector_type(16))) float f32x16;
template <bool is2> __device__ __forceinline__ void f_main(const Args& a, rsrc_t rs, unsigned char* lds, int xg, int kq, int cg, unsigned* ctl) {
    constexpr int L_C = 131072, L_S = L_C + 8192, NG = is2 ? 7 : 8, LD = is2 ? DM : FF;
    const int tid = otid(), lane = tid & 63, wave = a.pad0, l15 = lane & 15, g = lane >> 4;
    const unsigned lb = (unsigned)(size_t)(LAS unsigned char*)lds, q = l15 >> 2, p = lane & 3, l16 = lane * 16;
    const unsigned pol = __builtin_amdgcn_readfirstlane(ldg4_sc1(a.ws, O_POL));
    auto gr = [&](int G) { const int x = G + 2 * ((blockIdx.x >> 3) & 3); return x >= NG ? x - NG : x; };
    lds_barrier();
    v4u w[64];
    f32x16 u1 = {}, u2 = {};
    {
        v4u rW = mkrs(is2 ? a.w2 : a.w1, DM * FF * 2);
        unsigned bo[2];
#pragma unroll
        for (int i = 0; i < 2; ++i) bo[i] = (g * LD + 16 * ((l15 >> 1) ^ (g | i << 2)) + 8 * (lane & 1)) * 2;
        asm volatile("s_nop 4" : "+s"(rW));
        auto issue = [&](int G) {
            const unsigned r0 = 896 * kq + 128 * gr(G) + 32 * wave;
#pragma unroll
            for (int j = 0; j < 8; ++j) bdma<(NTW & 8)>(rW, bo[(j >> 1) & 1], ((r0 + 4 * j) * LD + 128 * cg) * 2, lb + (G & 3) * 32768 + wave * 8192 + j * 1024);
        };
        issue(0); issue(1); issue(2); issue(3);
        if (!is2) {
            f32x4 md[2];
            ld_mod<2>(rs, ctl, {4 * DM + 4 * tid, 3 * DM + 4 * tid}, md);
            *(LAS f32x4*)(lds + L_C + 16 * tid) = md[0]; *(LAS v2u*)(lds + L_C + 4096 + 8 * tid) = (v2u){pack2(md[1][0], md[1][1]), pack2(md[1][2], md[1][3])};
            asm volatile("s_waitcnt lgkmcnt(0)" ::: "memory");
        }
        const unsigned ox = is2 ? (4 * (g >> 1) + q) * 256 + 32 * ((2 * (kq ^ (2 * (g & 1) + (p >> 1))) + (wave >> 1)) ^ q) + 16 * (wave & 1) + 8 * (p & 1)
                                : (8 * (g >> 1) + q) * 256 + 32 * ((2 * wave + (g & 1)) ^ (q | (g >> 1) << 2)) + 8 * p;
        sfor<0, NG>([&](auto gg) {
            constexpr int G = decltype(gg)::v;
            asm volatile("s_waitcnt vmcnt(%0)\n\ts_barrier" :: "n"(G < NG - 3 ? 24 : 8 * (NG - 1 - G)) : "memory");
#pragma unroll
            for (int k = 0; k < 8; ++k) {
                const unsigned o = (G & 3) * 32768 + k * 4096;
                const v2u lo = tr_rd(lds, o + ox), hi = tr_rd(lds, o + (is2 ? (ox ^ 128) + 2048 : ox + 1024));
                w[8 * G + k] = (v4u){lo.x, lo.y, hi.x, hi.y};
                if constexpr (!is2) {
                    const unsigned k0 = 128 * gr(G) + 16 * k + 8 * (g >> 1);
                    const f32x4 ca = *(const LAS f32x4*)(lds + L_C + 4 * k0), cb = *(const LAS f32x4*)(lds + L_C + 4 * k0 + 16);
                    u2 = __builtin_amdgcn_mfma_f32_32x32x16_bf16(__builtin_bit_cast(v8bf, w[8 * G + k]), __builtin_bit_cast(v8bf, lds16(lds + L_C + 4096 + 2 * k0)), u2, 0, 0, 0);
                    w[8 * G + k] = (v4u){pack2(lo16f(lo.x) * ca[0], hi16f(lo.x) * ca[1]), pack2(lo16f(lo.y) * ca[2], hi16f(lo.y) * ca[3]), pack2(lo16f(hi.x) * cb[0], hi16f(hi.x) * cb[1]), pack2(lo16f(hi.y) * cb[2], hi16f(hi.y) * cb[3])};
                    u1 = __builtin_amdgcn_mfma_f32_32x32x16_bf16(__builtin_bit_cast(v8bf, w[8 * G + k]), __builtin_bit_cast(v8bf, (v4u){0x3F803F80u, 0x3F803F80u, 0x3F803F80u, 0x3F803F80u}), u1, 0, 0, 0);
                }
            }
            if constexpr (!is2) asm volatile("" : "+v"(u1), "+v"(u2));
#pragma unroll
            for (int k = 8 * G; k < 8 * G + 8; ++k) asm volatile("" : "+a"(w[k]));
            lds_barrier();
            if (G + 4 < NG) issue(G + 4);
        });
    }
    const unsigned sd = ((pol ^ (xg >> 2)) & 1) * 8192;
    v16u c0 = {l16, lb + l16, is2 ? min(lane, 6) * 16384 + wave * 2048 + 2044 : D_ST - D_X2 + (l15 >> 3) * 16384 + ((l15 & 6) * 4 + 2 * wave + (lane & 1)) * 256, lb + L_S + wave * 256 + l15 * 16, lb + L_S + (lane & 31) * 8, 0u, 0xbdd2d3e8u, 0xbdd2d3e8u, 0xc0135761u, 0xc0135761u, 0x3f800000u, 0x3f800000u, 0u, 0u, 0u, 0u};
    if (is2) {
        const int col = 128 * cg + 32 * kq + 8 * wave + 4 * (lane >> 5);
        const v2u br = *(const v2u*)(a.b2 + col);
        f32x4 gv;
        ld_mod<1>(rs, ctl, {5 * DM + col}, &gv);
        const int f1 = 7 * (kq & 1) + min(lane, 6);
        c0[3] = hw(pol, (xg & 4) + (f1 >> 2)) + H_P + 256 * (f1 & 3); c0[4] = r_db(col >> 4) - (kq & 1) * 7 * 16384 + (wave & 1) * 512 + (lane & 31) * 16 + (lane >> 5) * 8; c0[5] = ((lane & 31) * DM + col) * 2;
        c0[8] = br.x << 16; c0[9] = br.x & 0xFFFF0000u; c0[10] = br.y << 16; c0[11] = br.y & 0xFFFF0000u;
#pragma unroll
        for (int i = 0; i < 4; ++i) c0[12 + i] = __float_as_uint(gv[i]);
    } else {
#pragma unroll
        for (int h = 0; h < 4; ++h) {
            const v2u br = *(const v2u*)(a.b1 + 128 * cg + 32 * wave + 8 * h + 4 * (g >> 1));
            u2[4 * h] += lo16f(br.x); u2[4 * h + 1] += hi16f(br.x); u2[4 * h + 2] += lo16f(br.y); u2[4 * h + 3] += hi16f(br.y);
        }
    }
    if (!wave_wait<16>(ctl + C_H, 1, 1, 32, ctl + C_ABORT)) asm volatile("s_endpgm");
    RSTAMP(74, 0);
    const u64 dbg = DBG(a, blockIdx.x);
    PARS;
    const v16u s0 = {0u, (unsigned)wave, is2 ? par(P_T1) : -par(P_TZ), par(P_TS), O_D + (is2 ? (kq & 1) * 7 * 16384 : D_X2) + sd, lb + wave * 2048, hw(pol, xg) + H_C, hw(pol, xg) + H_P + 256 * ((blockIdx.x >> 3) & 3),
                     O_D + (cg >= 14 ? cg - 14 : cg) * 16384 + wave * 2048 + sd, D_ST - D_X2 + (wave >> 1) * 16384 + (wave & 1) * 4096, lb + L_S + 2048 + (wave == STAMP_WAVE ? 0 : MiB), lb + L_S + 2048,
                     (unsigned)dbg, (unsigned)(dbg >> 32), (unsigned)(u64)a.out, (unsigned)((u64)a.out >> 32)};
    v16u s1;
#pragma unroll
    for (int i = 0; i < 16; ++i) {
        const int o = kq ^ (i & 3);
        s1[i] = i < 8 ? gr(i) * 16384 + wave * 2048 : O_SL + ((cg * 2 + (o & 1)) * 2 + ((i >> 1) & 1)) * 16384 + ((pol ^ (o >> 1)) & 1) * 8192 + (i & 1) * 4096 + wave * 1024;
    }
    s1[12] = is2 ? par(P_T2) : -par(P_TY);
    const v4u rws = mkrs(a.ws, WS_END);
    v32u ww[8];
#pragma unroll
    for (int i = 0; i < NG; ++i) {
        const v16u x0 = __builtin_shufflevector(__builtin_shufflevector(w[8 * i], w[8 * i + 1], 0, 1, 2, 3, 4, 5, 6, 7), __builtin_shufflevector(w[8 * i + 2], w[8 * i + 3], 0, 1, 2, 3, 4, 5, 6, 7), 0, 1, 2, 3, 4, 5, 6, 7, 8, 9, 10, 11, 12, 13, 14, 15);
        const v16u x1 = __builtin_shufflevector(__builtin_shufflevector(w[8 * i + 4], w[8 * i + 5], 0, 1, 2, 3, 4, 5, 6, 7), __builtin_shufflevector(w[8 * i + 6], w[8 * i + 7], 0, 1, 2, 3, 4, 5, 6, 7), 0, 1, 2, 3, 4, 5, 6, 7, 8, 9, 10, 11, 12, 13, 14, 15);
        ww[i] = __builtin_shufflevector(x0, x1, 0, 1, 2, 3, 4, 5, 6, 7, 8, 9, 10, 11, 12, 13, 14, 15, 16, 17, 18, 19, 20, 21, 22, 23, 24, 25, 26, 27, 28, 29, 30, 31);
    }
#define F_IN "{v[0:15]}"(c0), "{s[32:47]}"(s0), "{s[48:63]}"(s1), "{s[64:67]}"(rws), "{a[0:31]}"(ww[0]), "{a[32:63]}"(ww[1]), "{a[64:95]}"(ww[2]), "{a[96:127]}"(ww[3]), "{a[128:159]}"(ww[4]), "{a[160:191]}"(ww[5]), "{a[192:223]}"(ww[6])
    if constexpr (is2) asm volatile("s_nop 4\n" ASM_F2 :: F_IN : "memory", "s95");
    else asm volatile("s_nop 7\ns_nop 7\n" ASM_F1 :: F_IN, "{a[224:255]}"(ww[7]), "{v[16:31]}"(u1), "{v[32:47]}"(u2) : "memory", "s95");
}

__global__ void __launch_bounds__(256, 1) k_main(Args) {
    extern __shared__ __attribute__((aligned(16))) unsigned char lds[];
    const int xg = blockIdx.x & 7, ii = blockIdx.x >> 3;
    int wv = __builtin_amdgcn_readfirstlane(threadIdx.x >> 6); asm volatile("" : "+s"(wv));
    if (blockIdx.x >= 224) code_touch(lds + LDS_BYTES - 1024, ((blockIdx.x >> 3) - 28) * 4 + wv);
    auto args = [&]() { PARS; Args r = *(const Args*)pp_; r.pad0 = wv; r.mode = blockIdx.x; return r; };
    const Args a = args();
    if (threadIdx.x == 0) __hip_atomic_store((gu32*)(a.ws + O_GO + blockIdx.x * 128), 0u, RLX_AGENT);
    RSTAMP(75, 0);
    if (ii < 16) {
        if (ii & 1) s1_main<true>(args(), lds, xg, ii); else s1_main<false>(args(), lds, xg, ii);
        return;
    }
    const rsrc_t rs = __builtin_amdgcn_make_buffer_rsrc((void*)a.ws, (short)0, (int)WS_END, 0x00020000);
    unsigned* const ctl = (unsigned*)(a.ws + O_CTL);
    const int i4 = ii & 3, cg = 4 * (xg & 3) + i4;
    const unsigned t0 = rt_now();
    f_start(args(), rs, lds, (blockIdx.x - 64) & 127, t0);
    if (ii < 24) {
        rt_delay(TD_W, t0);
        r_main(args(), rs, lds, xg, ii - 16, ctl, t0);
        return;
    }
    RSTAMP(75, 1);
    if (ii >= 28 && cg >= 14) return;
    if (ii < 28) f_main<true>(args(), rs, lds, xg, xg >> 1, 4 * (xg & 1) + i4, ctl);
    else f_main<false>(args(), rs, lds, xg, 0, 14 * (xg >> 2) + cg, ctl);
}

extern "C" void kernel_launch(void* const* d_in, const int* in_sizes, int n_in,
                              void* d_out, int out_size, void* d_ws, size_t ws_size,
                              hipStream_t stream) {
    Args a;
    a.x = (const u16*)d_in[0]; a.mem0 = (const u16*)d_in[1]; a.Wq = (const u16*)d_in[2]; a.Wk = (const u16*)d_in[3];
    a.Wv = (const u16*)d_in[4]; a.Wo = (const u16*)d_in[5];
    a.w1 = (const u16*)d_in[8]; a.b1 = (const u16*)d_in[9]; a.w2 = (const u16*)d_in[10]; a.b2 = (const u16*)d_in[11];
    a.out = (u16*)d_out; a.ws = (char*)d_ws; a.mode = 0; a.pad0 = 0;
    a.c = (const u16*)d_in[12]; a.w_mod = (const u16*)d_in[13];
    hipFuncSetAttribute((const void*)k_main, hipFuncAttributeMaxDynamicSharedMemorySize, LDS_BYTES);
    k_main<<<256, 256, LDS_BYTES, stream>>>(a);
}
```

```cpp
#include <hip/hip_runtime.h>
#include <hip/hip_bf16.h>
#include <stdint.h>

typedef unsigned short u16;
typedef unsigned long long u64;
typedef __attribute__((ext_vector_type(8))) short bf16x8;
typedef __attribute__((ext_vector_type(4))) short bf16x4;
typedef __attribute__((ext_vector_type(4))) __bf16 bf16x4_t;
typedef __attribute__((ext_vector_type(8))) __bf16 v8bf;
typedef __attribute__((ext_vector_type(16))) __bf16 v16bf;
typedef __attribute__((ext_vector_type(4))) float f32x4;
typedef __attribute__((ext_vector_type(2))) float f32x2;
typedef unsigned v8u __attribute__((ext_vector_type(8)));
typedef unsigned v4u __attribute__((ext_vector_type(4)));
typedef unsigned v2u __attribute__((ext_vector_type(2)));
#define GAS __attribute__((address_space(1)))
#define LAS __attribute__((address_space(3)))
typedef GAS unsigned gu32;
#define RLX_AGENT __ATOMIC_RELAXED, __HIP_MEMORY_SCOPE_AGENT

constexpr int S_TOK = 2048, DM = 1024, NH = 32, DH = 128, MEMR = 8, SEG = 32, NSEG = 64;
constexpr int HD = NH * DH;
constexpr int QKN = 2 * HD;
constexpr int FF = 3584;

#define NTW 12
constexpr int RG = 16;
#define RGX 8
constexpr unsigned MiB = 1u << 20;
constexpr unsigned O_CTL = 0;
constexpr unsigned O_MOD = 65536;
constexpr unsigned O_POL = O_MOD + 32768;
constexpr unsigned O_MODP = 131072;
constexpr unsigned O_GO = 196608;
constexpr unsigned O_FL = 262144;
constexpr unsigned O_EP = 2 * MiB;
constexpr unsigned O_PS = O_EP + RG * 262144;
constexpr unsigned O_UM = O_PS + RG * 262144;
constexpr unsigned O_E1 = O_UM + RG * 524288;
constexpr unsigned O_H1 = O_E1 + RG * 131072;
constexpr unsigned O_UX = O_H1 + 4 * MiB;
constexpr unsigned O_D = O_UX + RGX * 2 * MiB, D_SLOT = 524288, D_X2 = 14 * 16384, D_ST = 22 * 16384;
constexpr unsigned O_SL = O_D + 32 * MiB;
constexpr unsigned WS_END = O_SL + 2 * MiB;
#define WSB 0
static_assert(WSB * MiB + WS_END <= 256 * MiB && O_UX % (2 * MiB) == 0 && O_E1 % (2 * MiB) == 0, "ws");
__device__ __forceinline__ unsigned nearg(unsigned pol, unsigned off, unsigned h) { return off + (((off >> 21) ^ pol ^ h) & 1) * 8192; }
constexpr int C_ABORT = 0;
constexpr int C_H = 32;
constexpr int C_Q = 64;
constexpr int C_T = 192;
constexpr int C_M = 256;
constexpr int C_A = 512;
constexpr unsigned O_HW = 49152;
constexpr int H_C = 0;
constexpr int H_D = 256;
constexpr int H_P = 1024;
__device__ __forceinline__ unsigned hw(unsigned pol, int x) { return nearg(pol, O_HW, x >> 2) + (x & 3) * 2048; }

__device__ __forceinline__ float bf2f(u16 v) { return __uint_as_float(((unsigned)v) << 16); }
__device__ __forceinline__ u16 f2bf(float f) {
    __hip_bfloat16 h = __float2bfloat16(f);
    return __builtin_bit_cast(u16, h);
}
__device__ __forceinline__ float rbf(float f) { return bf2f(f2bf(f)); }

#define CODE_KB 96
#define NPR 8
enum { P_A, P_B, P_EU, P_DMIN, P_R, P_TP, P_TU, P_T0, P_X, P_TZ, P_TF, P_X2, P_T1, P_TS, P_TT, P_TY, P_T2, P_A0, P_A1, P_M, P_B2, P_21, NPAR };
#define CH_PAR 152, 76, 8, 4, 60, 100, 300, 360, 36, -30u, 40, 72, 190, 30, 24, 136, 140, 20, 168, 6, 72, 0
struct Args {
    const u16* Wq; const u16* Wk; const u16* Wv; const u16* Wo;
    const u16* c; const u16* w_mod; const u16* mem0;
    const u16* x; const u16* w1; const u16* b1; const u16* w2; const u16* b2;
    u16* out; char* ws;
    unsigned mode, pad0;
};
#define PARS auto pp_ = (__attribute__((address_space(4))) const Args*)__builtin_amdgcn_kernarg_segment_ptr(); asm volatile("" : "+s"(pp_))
constexpr unsigned PARV[NPAR] = {CH_PAR};
#define par(i) PARV[i]
#define RSTAMP(sg, i) do { } while (0)
#define STAMP_WAVE 0
#define DBG(a, i) 0ull
constexpr unsigned SPIN_CAP = 1u << 22;
constexpr unsigned SENT = 0xFFFFFFFFu;

__device__ __forceinline__ void lds_barrier() { asm volatile("s_waitcnt lgkmcnt(0)\n\ts_barrier" ::: "memory"); }
__device__ __forceinline__ void wait_vm0() { asm volatile("s_waitcnt vmcnt(0)" ::: "memory"); }
typedef __bf16 bf16x2_t __attribute__((ext_vector_type(2)));
__device__ __forceinline__ unsigned pack2(float a, float b) { const f32x2 v = {a, b}; return __builtin_bit_cast(unsigned, __builtin_convertvector(v, bf16x2_t)); }
__device__ __forceinline__ float lo16f(unsigned u) { return __uint_as_float(u << 16); }
__device__ __forceinline__ float hi16f(unsigned u) { return __uint_as_float(u & 0xFFFF0000u); }
__device__ __forceinline__ unsigned umax4(v4u x) { return max(max(x.x, x.y), max(x.z, x.w)); }
__device__ __forceinline__ unsigned unsent(unsigned x) { return min(x, 0xFFFFFFFEu); }

__device__ __forceinline__ unsigned ldg4_sc1(const void* sb, unsigned vo) {
    unsigned r;
    asm volatile("global_load_dword %0, %1, %2 sc1\n\ts_waitcnt vmcnt(0)" : "=&v"(r) : "v"(vo), "s"(sb) : "memory");
    return r;
}
__device__ __forceinline__ bool spin_fail(unsigned& spins, unsigned* abortw) {
    if (++spins > SPIN_CAP) { __hip_atomic_store((gu32*)abortw, 1u, RLX_AGENT); return true; }
    if ((spins & 63u) == 0 && __builtin_amdgcn_readfirstlane(__hip_atomic_load((gu32*)abortw, RLX_AGENT)) == 1u) return true;
    return false;
}
__device__ __forceinline__ float red16_add(float x) { auto r = __builtin_amdgcn_permlane16_swap(__float_as_uint(x), __float_as_uint(x), false, false); return __uint_as_float(r[0]) + __uint_as_float(r[1]); }
__device__ __forceinline__ float red32_add(float x) { auto r = __builtin_amdgcn_permlane32_swap(__float_as_uint(x), __float_as_uint(x), false, false); return __uint_as_float(r[0]) + __uint_as_float(r[1]); }
__device__ __forceinline__ v2u tr_rd(const unsigned char* lds_base, unsigned off) {
    return __builtin_bit_cast(v2u, __builtin_amdgcn_ds_read_tr16_b64_v4bf16((LAS bf16x4_t*)(lds_base + off)));
}
__device__ __forceinline__ v4u lds16(const unsigned char* p) { return *(const LAS v4u*)p; }
__device__ __forceinline__ void dma16(const void* src, unsigned char* dst, int aux) {
    if (aux) __builtin_amdgcn_global_load_lds((const unsigned*)src, (unsigned*)dst, 16, 0, 2);
    else __builtin_amdgcn_global_load_lds((const unsigned*)src, (unsigned*)dst, 16, 0, 0);
}

__device__ __forceinline__ v4u mkrs(const void* p, unsigned bytes) { return (v4u){(unsigned)(u64)p, (unsigned)((u64)p >> 32) & 0xFFFFu, bytes, 0x00020000u}; }
template <int AUX> __device__ __forceinline__ void bdma(v4u r, unsigned vo, unsigned so, unsigned ldsaddr) {
    if (AUX == 16) asm volatile("s_mov_b32 m0, %0\n\ts_nop 0\n\tbuffer_load_dwordx4 %1, %2, %3 offen sc1 lds" :: "s"(ldsaddr), "v"(vo), "s"(r), "s"(so) : "m0");
    else if (AUX) asm volatile("s_mov_b32 m0, %0\n\ts_nop 0\n\tbuffer_load_dwordx4 %1, %2, %3 offen nt lds" :: "s"(ldsaddr), "v"(vo), "s"(r), "s"(so) : "m0");
    else asm volatile("s_mov_b32 m0, %0\n\ts_nop 0\n\tbuffer_load_dwordx4 %1, %2, %3 offen lds" :: "s"(ldsaddr), "v"(vo), "s"(r), "s"(so) : "m0");
}

template <int I> struct IC { static constexpr int v = I; };
template <int B, int E, class F> __device__ __forceinline__ void sfor(F&& f) { if constexpr (B < E) { f(IC<B>{}); sfor<B + 1, E>(f); } }
#define SB() __builtin_amdgcn_sched_barrier(0)

__device__ __forceinline__ int lane_id() { int l; asm volatile("v_mbcnt_lo_u32_b32 %0, -1, 0\n\tv_mbcnt_hi_u32_b32 %0, -1, %0" : "=v"(l)); return l; }
constexpr int LDS_BYTES = 163840;
typedef __amdgpu_buffer_rsrc_t rsrc_t;
__device__ __forceinline__ v4u bld16(rsrc_t r, unsigned off) { return __builtin_bit_cast(v4u, __builtin_amdgcn_raw_buffer_load_b128(r, off, 0, 16)); }
__device__ __forceinline__ v2u bld8(rsrc_t r, unsigned off) { return __builtin_bit_cast(v2u, __builtin_amdgcn_raw_buffer_load_b64(r, off, 0, 16)); }
__device__ __forceinline__ void bst16(rsrc_t r, unsigned off, v4u v) { __builtin_amdgcn_raw_buffer_store_b128(__builtin_bit_cast(__attribute__((ext_vector_type(4))) unsigned, v), r, off, 0, 16); }
__device__ __forceinline__ void bst8(rsrc_t r, unsigned off, v2u v) { __builtin_amdgcn_raw_buffer_store_b64(__builtin_bit_cast(__attribute__((ext_vector_type(2))) unsigned, v), r, off, 0, 16); }

constexpr int L_FLAG = LDS_BYTES - 64;
#define otid() (a.pad0 * 64 + lane_id())
__device__ __forceinline__ v4u sld16(rsrc_t r, unsigned vo, unsigned so) { return __builtin_bit_cast(v4u, __builtin_amdgcn_raw_buffer_load_b128(r, vo, so, 16)); }
__device__ __forceinline__ v4u sentv() { unsigned x = SENT; asm volatile("" : "+v"(x)); return (v4u){x, x, x, x}; }
#define BW_SLEEP 16
template <int SLP = BW_SLEEP> __device__ __forceinline__ bool wave_wait(const unsigned* p, int stride, int n, unsigned target, unsigned* abortw, unsigned char* lds = nullptr) {
    const int lane = lane_id();
    for (unsigned spins = 0;;) {
        const unsigned v = lane < n ? __hip_atomic_load((gu32*)(p + lane * stride), RLX_AGENT) : target;
        if (!__any(v < target)) { if (lds && lane == 0) *(volatile LAS unsigned*)(lds + L_FLAG + 32) = v; return true; }
        __builtin_amdgcn_s_sleep(SLP);
        if (++spins > (SPIN_CAP << 1)) { __hip_atomic_store((gu32*)abortw, 1u, RLX_AGENT); return false; }
        if ((spins & 63u) == 0 && __builtin_amdgcn_readfirstlane(__hip_atomic_load((gu32*)abortw, RLX_AGENT)) == 1u) return false;
    }
}
__device__ __forceinline__ unsigned wait_go(const char* ws) {
    for (unsigned spins = 0;;) {
        const unsigned v = __builtin_amdgcn_readfirstlane(ldg4_sc1(ws, O_GO + blockIdx.x * 128));
        if (v) return v;
        __builtin_amdgcn_s_sleep(8);
        if (++spins > SPIN_CAP) asm volatile("s_endpgm");
    }
}
template <int SLP = 16> __device__ __forceinline__ unsigned wait_set(const char* ws, unsigned* ctl) {
    if (!wave_wait<SLP>(ctl + C_A, 32, 8, 16, ctl + C_ABORT)) asm volatile("s_endpgm");
    return __builtin_amdgcn_readfirstlane(ldg4_sc1(ws, O_POL));
}
__device__ __forceinline__ void drained(unsigned* cnt) {
    asm volatile("s_waitcnt vmcnt(0)\n\ts_barrier" ::: "memory");
    if (threadIdx.x == 0) __hip_atomic_fetch_add((gu32*)cnt, 1u, RLX_AGENT);
}

constexpr int L_W = 0;
constexpr int L_H = 98304;
constexpr int L_KX = L_H + 40960;
constexpr int L_QX = L_KX + 8192;
constexpr int L_T = L_QX + 8192;
constexpr int L_ST = L_T + 4096;
constexpr int lsl(bool isV, int c, int n) { return isV ? (c < 6 && (n & 1) ? 2 * c + (n >> 1) : -1) : (c < 12 && n == 1 ? c : -1); }
constexpr int ridx(bool isV, int c, int n) { int r = 0; for (int i = 0; i < 4 * c + n; ++i) r += lsl(isV, i >> 2, i & 3) < 0; return r; }
#define ASM_Q \
    "s_add_u32 m0, s41, 0\ns_xor_b32 s85, s62, 0\ns_add_u32 s85, s85, s40\nbuffer_load_dwordx4 v11, s[64:67], s85 offen sc1 lds\n" \
    "buffer_load_dwordx4 v12, s[64:67], s85 offen offset:1024 sc1 lds\ns_add_u32 m0, s41, 8192\ns_xor_b32 s85, s62, 256\ns_add_u32 s85, s85, s40\n" \
    "buffer_load_dwordx4 v11, s[64:67], s85 offen sc1 lds\nbuffer_load_dwordx4 v12, s[64:67], s85 offen offset:1024 sc1 lds\ns_add_u32 m0, s41, 16384\ns_xor_b32 s85, s62, 512\n" \
    "s_add_u32 s85, s85, s40\nbuffer_load_dwordx4 v11, s[64:67], s85 offen sc1 lds\nbuffer_load_dwordx4 v12, s[64:67], s85 offen offset:1024 sc1 lds\ns_memrealtime s[72:73]\n" \
    "s_waitcnt lgkmcnt(0)\ns_mov_b32 s80, s72\ns_mov_b32 s83, 0x7fff0000\ns_branch .Lq8_%=\n.Lq9_%=:\ns_waitcnt vmcnt(0)\nv_mov_b32 v253, 1\ns_or_b32 s69, s35, s36\n" \
    "s_cselect_b32 s69, 0x7fff0000, 0\ns_mov_b64 exec, 1\nbuffer_atomic_add v253, off, s[64:67], s69 offset:256\ns_mov_b64 exec, -1\ns_mov_b32 s16, 0\ns_cmp_eq_u32 s36, 0\n" \
    "s_cselect_b32 s17, 511, -1\ns_mov_b32 s18, s44\ns_mov_b32 s72, s80\ns_barrier\n.Lq2_%=:\nds_read_b128 v[64:67], v3 offset:0\nds_read_b128 v[68:71], v9 offset:0\n" \
    "ds_read_b128 v[72:75], v9 offset:4096\nv_mov_b32_dpp v16, v20 quad_perm:[0,1,2,3] row_mask:0xf bank_mask:0xc\n" \
    "v_mov_b32_dpp v17, v21 quad_perm:[0,1,2,3] row_mask:0xf bank_mask:0xc\nv_mov_b32_dpp v18, v22 quad_perm:[0,1,2,3] row_mask:0xf bank_mask:0xc\n" \
    "v_mov_b32_dpp v19, v23 quad_perm:[0,1,2,3] row_mask:0xf bank_mask:0xc\nv_mov_b32 v254, 0xff800000\nv_cvt_pk_bf16_f32 v16, 0, v16\nv_cvt_pk_bf16_f32 v17, 0, v17\n" \
    "v_cvt_pk_bf16_f32 v18, 0, v18\nv_cvt_pk_bf16_f32 v19, 0, v19\nv_mul_f32 v16, s33, v16\nv_mul_f32 v17, s33, v17\nv_mul_f32 v18, s33, v18\ns_waitcnt lgkmcnt(0)\n" \
    "v_mfma_f32_16x16x32_bf16 v[48:51], a[8:11], v[68:71], 0\ns_add_u32 m0, s41, 24576\nv_mul_f32 v19, s33, v19\nv_cvt_pk_bf16_f32 v16, 0, v16\n" \
    "v_mfma_f32_16x16x32_bf16 v[52:55], a[8:11], v[72:75], 0\nds_read_b128 v[76:79], v3 offset:1024\nv_cvt_pk_bf16_f32 v17, 0, v17\nv_cvt_pk_bf16_f32 v18, 0, v18\n" \
    "v_mfma_f32_16x16x32_bf16 v[56:59], a[16:19], v[68:71], 0\nds_read_b128 v[80:83], v10 offset:0\nv_cvt_pk_bf16_f32 v19, 0, v19\nv_cndmask_b32 v16, v254, v16, s[50:51]\n" \
    "v_mfma_f32_16x16x32_bf16 v[60:63], a[16:19], v[72:75], 0\nds_read_b128 v[84:87], v10 offset:4096\nv_cndmask_b32 v17, v254, v17, s[52:53]\n" \
    "v_cndmask_b32 v18, v254, v18, s[54:55]\nv_mfma_f32_16x16x32_bf16 v[32:35], a[0:3], v[68:71], 0\ns_xor_b32 s85, s62, 768\nv_cndmask_b32 v19, v254, v19, s[56:57]\n" \
    "v_max3_f32 v254, v16, v17, v18\nv_mfma_f32_16x16x32_bf16 v[36:39], a[0:3], v[72:75], 0\ns_add_u32 s85, s85, s40\nv_max_f32 v254, v254, v19\n" \
    "v_mfma_f32_16x16x32_bf16 v[40:43], v[64:67], v[68:71], 0\nbuffer_load_dwordx4 v11, s[64:67], s85 offen sc1 lds\n" \
    "v_max_f32_dpp v252, v254, v254 row_ror:8 row_mask:0xf bank_mask:0xf\nv_max_f32_dpp v253, v254, v254 row_ror:8 row_mask:0xf bank_mask:0xf\n" \
    "v_mfma_f32_16x16x32_bf16 v[44:47], v[64:67], v[72:75], 0\ns_nop 0\ns_waitcnt lgkmcnt(0)\nv_permlane32_swap_b32 v252, v253\n" \
    "v_mfma_f32_16x16x32_bf16 v[48:51], a[12:15], v[80:83], v[48:51]\nv_max_f32 v252, v252, v253\nv_cvt_pk_bf16_f32 v24, 0, v24\n" \
    "v_mfma_f32_16x16x32_bf16 v[52:55], a[12:15], v[84:87], v[52:55]\nds_read_b128 v[88:91], v3 offset:2048\nv_cvt_pk_bf16_f32 v25, 0, v25\nv_cvt_pk_bf16_f32 v26, 0, v26\n" \
    "v_mfma_f32_16x16x32_bf16 v[56:59], a[20:23], v[80:83], v[56:59]\nds_read_b128 v[64:67], v9 offset:1024\nv_cvt_pk_bf16_f32 v27, 0, v27\nv_mul_f32 v24, s33, v24\n" \
    "v_mfma_f32_16x16x32_bf16 v[60:63], a[20:23], v[84:87], v[60:63]\nds_read_b128 v[68:71], v9 offset:5120\nv_mul_f32 v25, s33, v25\nv_mul_f32 v26, s33, v26\n" \
    "v_mfma_f32_16x16x32_bf16 v[32:35], a[4:7], v[80:83], v[32:35]\nbuffer_load_dwordx4 v12, s[64:67], s85 offen offset:1024 sc1 lds\nv_mul_f32 v27, s33, v27\n" \
    "v_cvt_pk_bf16_f32 v24, 0, v24\nv_mfma_f32_16x16x32_bf16 v[36:39], a[4:7], v[84:87], v[36:39]\ns_add_u32 m0, s41, 32768\nv_cvt_pk_bf16_f32 v25, 0, v25\n" \
    "v_cvt_pk_bf16_f32 v26, 0, v26\nv_mfma_f32_16x16x32_bf16 v[40:43], v[76:79], v[80:83], v[40:43]\ns_xor_b32 s85, s62, 1024\nv_cvt_pk_bf16_f32 v27, 0, v27\n" \
    "v_max3_f32 v254, v252, v24, v25\nv_mfma_f32_16x16x32_bf16 v[44:47], v[76:79], v[84:87], v[44:47]\ns_add_u32 s85, s85, s40\ns_waitcnt lgkmcnt(0)\n" \
    "v_max3_f32 v252, v254, v26, v27\nv_mfma_f32_16x16x32_bf16 v[48:51], a[32:35], v[64:67], v[48:51]\nbuffer_load_dwordx4 v11, s[64:67], s85 offen sc1 lds\n" \
    "v_max3_f32 v253, v254, v26, v27\nv_mfma_f32_16x16x32_bf16 v[52:55], a[32:35], v[68:71], v[52:55]\nds_read_b128 v[72:75], v3 offset:3072\nv_permlane16_swap_b32 v252, v253\n" \
    "v_mfma_f32_16x16x32_bf16 v[56:59], a[40:43], v[64:67], v[56:59]\nds_read_b128 v[76:79], v10 offset:1024\nv_max_f32 v252, v252, v253\nv_sub_f32 v16, v16, v252\n" \
    "v_mfma_f32_16x16x32_bf16 v[60:63], a[40:43], v[68:71], v[60:63]\nds_read_b128 v[80:83], v10 offset:5120\nv_sub_f32 v17, v17, v252\nv_sub_f32 v18, v18, v252\n" \
    "v_mfma_f32_16x16x32_bf16 v[32:35], a[24:27], v[64:67], v[32:35]\nbuffer_load_dwordx4 v12, s[64:67], s85 offen offset:1024 sc1 lds\nv_sub_f32 v19, v19, v252\n" \
    "v_sub_f32 v24, v24, v252\nv_mfma_f32_16x16x32_bf16 v[36:39], a[24:27], v[68:71], v[36:39]\ns_add_u32 s84, s40, 0x20000\nv_sub_f32 v25, v25, v252\nv_sub_f32 v26, v26, v252\n" \
    "v_mfma_f32_16x16x32_bf16 v[40:43], v[88:91], v[64:67], v[40:43]\ns_cmp_lt_u32 s35, 62\ns_cselect_b32 s84, s84, s40\nv_sub_f32 v27, v27, v252\n" \
    "v_mfma_f32_16x16x32_bf16 v[44:47], v[88:91], v[68:71], v[44:47]\ns_sub_u32 s20, s72, s80\ns_waitcnt lgkmcnt(0)\nv_mul_f32 v16, s34, v16\n" \
    "v_mfma_f32_16x16x32_bf16 v[48:51], a[36:39], v[76:79], v[48:51]\ns_add_u32 s20, s20, 0\nv_mul_f32 v17, s34, v17\nv_mul_f32 v18, s34, v18\n" \
    "v_mfma_f32_16x16x32_bf16 v[52:55], a[36:39], v[80:83], v[52:55]\nds_read_b128 v[64:67], v3 offset:4096\nv_mul_f32 v19, s34, v19\nv_mul_f32 v24, s34, v24\n" \
    "v_mfma_f32_16x16x32_bf16 v[56:59], a[44:47], v[76:79], v[56:59]\nds_read_b128 v[68:71], v9 offset:8192\nv_mul_f32 v25, s34, v25\nv_mul_f32 v26, s34, v26\n" \
    "v_mfma_f32_16x16x32_bf16 v[60:63], a[44:47], v[80:83], v[60:63]\nds_read_b128 v[84:87], v9 offset:12288\nv_mul_f32 v27, s34, v27\nv_exp_f32 v16, v16\n" \
    "v_mfma_f32_16x16x32_bf16 v[32:35], a[28:31], v[76:79], v[32:35]\ns_min_u32 s21, s20, s16\nv_exp_f32 v17, v17\n" \
    "v_mfma_f32_16x16x32_bf16 v[36:39], a[28:31], v[80:83], v[36:39]\ns_mov_b32 s16, s20\nv_exp_f32 v18, v18\nv_mfma_f32_16x16x32_bf16 v[40:43], v[72:75], v[76:79], v[40:43]\n" \
    "s_cmp_gt_u32 s21, 156\ns_cselect_b32 s21, 0, s21\nv_exp_f32 v19, v19\nv_mfma_f32_16x16x32_bf16 v[44:47], v[72:75], v[80:83], v[44:47]\ns_lshr_b32 s20, s35, 2\n" \
    "s_waitcnt lgkmcnt(0)\nv_exp_f32 v24, v24\nv_mfma_f32_16x16x32_bf16 v[48:51], a[56:59], v[68:71], v[48:51]\ns_lshl_b32 s22, s20, 8\nv_add_f32 v254, v16, v18\n" \
    "v_add_f32 v255, v17, v19\nv_mfma_f32_16x16x32_bf16 v[52:55], a[56:59], v[84:87], v[52:55]\nds_read_b128 v[88:91], v3 offset:5120\nv_exp_f32 v25, v25\n" \
    "v_mfma_f32_16x16x32_bf16 v[56:59], a[64:67], v[68:71], v[56:59]\nds_read_b128 v[72:75], v10 offset:8192\nv_add_f32 v254, v254, v255\nv_exp_f32 v26, v26\n" \
    "v_mfma_f32_16x16x32_bf16 v[60:63], a[64:67], v[84:87], v[60:63]\nds_read_b128 v[76:79], v10 offset:12288\nv_exp_f32 v27, v27\n" \
    "v_mfma_f32_16x16x32_bf16 v[32:35], a[48:51], v[68:71], v[32:35]\ns_or_b32 s21, s21, s22\nv_add_f32_dpp v252, v254, v254 row_ror:8 row_mask:0xf bank_mask:0xf\n" \
    "v_add_f32_dpp v253, v254, v254 row_ror:8 row_mask:0xf bank_mask:0xf\nv_mfma_f32_16x16x32_bf16 v[36:39], a[48:51], v[84:87], v[36:39]\ns_and_b32 s20, s20, 1\n" \
    "v_add_f32 v254, v24, v26\nv_add_f32 v255, v25, v27\nv_mfma_f32_16x16x32_bf16 v[40:43], v[64:67], v[68:71], v[40:43]\ns_lshl_b32 s20, s20, 2\n" \
    "v_permlane32_swap_b32 v252, v253\nv_mfma_f32_16x16x32_bf16 v[44:47], v[64:67], v[84:87], v[44:47]\ns_cmp_gt_u32 s21, s17\ns_cselect_b32 s20, s20, 0x7fff0000\n" \
    "s_waitcnt lgkmcnt(0)\nv_mfma_f32_16x16x32_bf16 v[48:51], a[60:63], v[72:75], v[48:51]\ns_max_u32 s17, s17, s21\nv_add_f32 v254, v254, v252\nv_add_f32 v255, v255, v253\n" \
    "v_mfma_f32_16x16x32_bf16 v[52:55], a[60:63], v[76:79], v[52:55]\nds_read_b128 v[64:67], v3 offset:6144\nv_add_f32 v252, v254, v255\nv_add_f32 v253, v255, v254\n" \
    "v_mfma_f32_16x16x32_bf16 v[56:59], a[68:71], v[72:75], v[56:59]\nds_read_b128 v[68:71], v9 offset:9216\nv_permlane16_swap_b32 v252, v253\n" \
    "v_mfma_f32_16x16x32_bf16 v[60:63], a[68:71], v[76:79], v[60:63]\nds_read_b128 v[80:83], v9 offset:13312\nv_add_f32 v252, v252, v253\nv_rcp_f32 v252, v252\n" \
    "v_mfma_f32_16x16x32_bf16 v[32:35], a[52:55], v[72:75], v[32:35]\nv_mov_b32 v15, s21\ns_mov_b64 exec, 1\nbuffer_atomic_umax v15, off, s[64:67], s20 offset:768\n" \
    "s_mov_b64 exec, -1\nv_mfma_f32_16x16x32_bf16 v[36:39], a[52:55], v[76:79], v[36:39]\nv_mul_f32 v16, v16, v252\nv_mul_f32 v17, v17, v252\n" \
    "v_mfma_f32_16x16x32_bf16 v[40:43], v[88:91], v[72:75], v[40:43]\nv_mul_f32 v24, v24, v252\nv_mul_f32 v25, v25, v252\n" \
    "v_mfma_f32_16x16x32_bf16 v[44:47], v[88:91], v[76:79], v[44:47]\ns_waitcnt lgkmcnt(0)\nv_cvt_pk_bf16_f32 v20, v16, v17\nv_cvt_pk_bf16_f32 v22, v24, v25\n" \
    "v_mfma_f32_16x16x32_bf16 v[48:51], a[80:83], v[68:71], v[48:51]\nv_mul_f32 v18, v18, v252\nv_mul_f32 v19, v19, v252\n" \
    "v_mfma_f32_16x16x32_bf16 v[52:55], a[80:83], v[80:83], v[52:55]\nds_read_b128 v[72:75], v3 offset:7168\nv_mul_f32 v26, v26, v252\nv_mul_f32 v27, v27, v252\n" \
    "v_mfma_f32_16x16x32_bf16 v[56:59], a[88:91], v[68:71], v[56:59]\nds_read_b128 v[76:79], v10 offset:9216\nv_cvt_pk_bf16_f32 v21, v18, v19\nv_cvt_pk_bf16_f32 v23, v26, v27\n" \
    "v_mfma_f32_16x16x32_bf16 v[60:63], a[88:91], v[80:83], v[60:63]\nds_read_b128 v[84:87], v10 offset:13312\nv_permlane32_swap_b32 v20, v22\n" \
    "v_mfma_f32_16x16x32_bf16 v[32:35], a[72:75], v[68:71], v[32:35]\nv_permlane32_swap_b32 v21, v23\nv_mfma_f32_16x16x32_bf16 v[36:39], a[72:75], v[80:83], v[36:39]\n" \
    "v_writelane_b32 v20, s18, 40\nv_mfma_f32_16x16x32_bf16 v[40:43], v[64:67], v[68:71], v[40:43]\ns_xor_b32 s74, s83, 0x2000\n" \
    "buffer_store_dwordx4 v[20:23], v7, s[64:67], s83 offen sc1\nv_mfma_f32_16x16x32_bf16 v[44:47], v[64:67], v[80:83], v[44:47]\n" \
    "buffer_store_dwordx4 v[20:23], v7, s[64:67], s74 offen sc1\ns_waitcnt lgkmcnt(0)\nv_mov_b64 v[28:29], -1\nv_mfma_f32_16x16x32_bf16 v[48:51], a[84:87], v[76:79], v[48:51]\n" \
    "v_mov_b64 v[30:31], -1\nv_mfma_f32_16x16x32_bf16 v[52:55], a[84:87], v[84:87], v[52:55]\nds_read_b128 v[80:83], v3 offset:8192\ns_add_u32 s69, s35, 2\n" \
    "v_mfma_f32_16x16x32_bf16 v[56:59], a[92:95], v[76:79], v[56:59]\nds_read_b128 v[88:91], v9 offset:16384\ns_and_b32 s69, s69, 15\n" \
    "v_mfma_f32_16x16x32_bf16 v[60:63], a[92:95], v[84:87], v[60:63]\nds_read_b128 v[64:67], v9 offset:20480\ns_lshl_b32 s69, s69, 18\n" \
    "v_mfma_f32_16x16x32_bf16 v[32:35], a[76:79], v[76:79], v[32:35]\ns_add_u32 s82, s38, s69\ns_lshr_b32 s69, s82, 8\n" \
    "v_mfma_f32_16x16x32_bf16 v[36:39], a[76:79], v[84:87], v[36:39]\ns_and_b32 s69, s69, 0x2000\ns_xor_b32 s82, s82, s69\n" \
    "v_mfma_f32_16x16x32_bf16 v[40:43], v[72:75], v[76:79], v[40:43]\ns_add_u32 s69, s35, 2\ns_and_b32 s69, s69, 15\n" \
    "v_mfma_f32_16x16x32_bf16 v[44:47], v[72:75], v[84:87], v[44:47]\ns_lshl_b32 s69, s69, 18\ns_add_u32 s83, s39, s69\ns_waitcnt lgkmcnt(0)\n" \
    "v_mfma_f32_16x16x32_bf16 v[48:51], a[104:107], v[88:91], v[48:51]\ns_xor_b32 s74, s82, 0x4000\ns_xor_b32 s75, s83, 0x2000\n" \
    "v_mfma_f32_16x16x32_bf16 v[52:55], a[104:107], v[64:67], v[52:55]\nds_read_b128 v[84:87], v3 offset:9216\n" \
    "v_mfma_f32_16x16x32_bf16 v[56:59], a[112:115], v[88:91], v[56:59]\nds_read_b128 v[68:71], v10 offset:16384\n" \
    "v_mfma_f32_16x16x32_bf16 v[60:63], a[112:115], v[64:67], v[60:63]\nds_read_b128 v[72:75], v10 offset:20480\n" \
    "v_mfma_f32_16x16x32_bf16 v[32:35], a[96:99], v[88:91], v[32:35]\nv_mfma_f32_16x16x32_bf16 v[36:39], a[96:99], v[64:67], v[36:39]\n" \
    "v_mfma_f32_16x16x32_bf16 v[40:43], v[80:83], v[88:91], v[40:43]\nbuffer_store_dwordx4 v[28:31], v7, s[64:67], s82 offen sc1\n" \
    "v_mfma_f32_16x16x32_bf16 v[44:47], v[80:83], v[64:67], v[44:47]\ns_waitcnt lgkmcnt(0)\nv_mfma_f32_16x16x32_bf16 v[48:51], a[108:111], v[68:71], v[48:51]\n" \
    "v_mfma_f32_16x16x32_bf16 v[52:55], a[108:111], v[72:75], v[52:55]\nds_read_b128 v[64:67], v3 offset:10240\n" \
    "v_mfma_f32_16x16x32_bf16 v[56:59], a[116:119], v[68:71], v[56:59]\nds_read_b128 v[88:91], v9 offset:17408\n" \
    "v_mfma_f32_16x16x32_bf16 v[60:63], a[116:119], v[72:75], v[60:63]\nds_read_b128 v[76:79], v9 offset:21504\n" \
    "v_mfma_f32_16x16x32_bf16 v[32:35], a[100:103], v[68:71], v[32:35]\nbuffer_store_dwordx4 v[28:31], v7, s[64:67], s74 offen sc1\n" \
    "v_mfma_f32_16x16x32_bf16 v[36:39], a[100:103], v[72:75], v[36:39]\nv_mfma_f32_16x16x32_bf16 v[40:43], v[84:87], v[68:71], v[40:43]\n" \
    "v_mfma_f32_16x16x32_bf16 v[44:47], v[84:87], v[72:75], v[44:47]\ns_waitcnt lgkmcnt(0)\nv_mfma_f32_16x16x32_bf16 v[48:51], a[128:131], v[88:91], v[48:51]\n" \
    "v_mfma_f32_16x16x32_bf16 v[52:55], a[128:131], v[76:79], v[52:55]\nds_read_b128 v[68:71], v3 offset:11264\n" \
    "v_mfma_f32_16x16x32_bf16 v[56:59], a[136:139], v[88:91], v[56:59]\nds_read_b128 v[72:75], v10 offset:17408\nbuffer_store_dwordx4 v[28:31], v7, s[64:67], s83 offen sc1\n" \
    "v_mfma_f32_16x16x32_bf16 v[60:63], a[136:139], v[76:79], v[60:63]\nds_read_b128 v[80:83], v10 offset:21504\n" \
    "v_mfma_f32_16x16x32_bf16 v[32:35], a[120:123], v[88:91], v[32:35]\nv_mfma_f32_16x16x32_bf16 v[36:39], a[120:123], v[76:79], v[36:39]\n" \
    "v_mfma_f32_16x16x32_bf16 v[40:43], v[64:67], v[88:91], v[40:43]\nv_mfma_f32_16x16x32_bf16 v[44:47], v[64:67], v[76:79], v[44:47]\ns_waitcnt lgkmcnt(0)\n" \
    "v_mfma_f32_16x16x32_bf16 v[48:51], a[132:135], v[72:75], v[48:51]\nbuffer_store_dwordx4 v[28:31], v7, s[64:67], s75 offen sc1\nv_lshrrev_b32 v16, 1, v2\n" \
    "v_add_u32 v16, s60, v16\nv_mfma_f32_16x16x32_bf16 v[52:55], a[132:135], v[80:83], v[52:55]\nds_read_b128 v[64:67], v3 offset:12288\ns_waitcnt vmcnt(7)\ns_barrier\n" \
    "v_mfma_f32_16x16x32_bf16 v[56:59], a[140:143], v[72:75], v[56:59]\nds_read_b128 v[76:79], v9 offset:24576\n" \
    "v_mfma_f32_16x16x32_bf16 v[60:63], a[140:143], v[80:83], v[60:63]\nds_read_b128 v[84:87], v9 offset:28672\ns_add_u32 m0, s41, 0\n" \
    "v_mfma_f32_16x16x32_bf16 v[32:35], a[124:127], v[72:75], v[32:35]\ns_xor_b32 s85, s62, 1280\ns_add_u32 s85, s85, s40\n" \
    "v_mfma_f32_16x16x32_bf16 v[36:39], a[124:127], v[80:83], v[36:39]\nbuffer_load_dwordx4 v11, s[64:67], s85 offen sc1 lds\nds_write_b64 v16, v[28:29]\n" \
    "v_mfma_f32_16x16x32_bf16 v[40:43], v[68:71], v[72:75], v[40:43]\nds_write_b64 v16, v[28:29] offset:2048\ns_add_u32 s69, s35, 2\n" \
    "v_mfma_f32_16x16x32_bf16 v[44:47], v[68:71], v[80:83], v[44:47]\ns_and_b32 s69, s69, 255\ns_lshl_b32 s69, s69, 19\ns_waitcnt lgkmcnt(2)\n" \
    "v_mfma_f32_16x16x32_bf16 v[48:51], a[152:155], v[76:79], v[48:51]\nbuffer_load_dwordx4 v12, s[64:67], s85 offen offset:1024 sc1 lds\ns_add_u32 m0, s41, 8192\n" \
    "v_mfma_f32_16x16x32_bf16 v[52:55], a[152:155], v[84:87], v[52:55]\nds_read_b128 v[80:83], v3 offset:13312\ns_xor_b32 s85, s62, 1536\n" \
    "v_mfma_f32_16x16x32_bf16 v[56:59], a[160:163], v[76:79], v[56:59]\nds_read_b128 v[88:91], v10 offset:24576\ns_add_u32 s85, s85, s40\n" \
    "v_mfma_f32_16x16x32_bf16 v[60:63], a[160:163], v[84:87], v[60:63]\nds_read_b128 v[68:71], v10 offset:28672\nbuffer_load_dwordx4 v11, s[64:67], s85 offen sc1 lds\n" \
    "v_mfma_f32_16x16x32_bf16 v[32:35], a[144:147], v[76:79], v[32:35]\ns_add_u32 s74, s63, s69\ns_cmp_lt_u32 s35, 62\ns_cselect_b32 s74, s74, 0x7fff0000\n" \
    "v_mfma_f32_16x16x32_bf16 v[36:39], a[144:147], v[84:87], v[36:39]\nv_mfma_f32_16x16x32_bf16 v[40:43], v[64:67], v[76:79], v[40:43]\n" \
    "buffer_load_dwordx4 v12, s[64:67], s85 offen offset:1024 sc1 lds\nv_mfma_f32_16x16x32_bf16 v[44:47], v[64:67], v[84:87], v[44:47]\ns_waitcnt lgkmcnt(0)\n" \
    "v_mfma_f32_16x16x32_bf16 v[48:51], a[156:159], v[88:91], v[48:51]\nv_mfma_f32_16x16x32_bf16 v[52:55], a[156:159], v[68:71], v[52:55]\n" \
    "ds_read_b128 v[64:67], v3 offset:14336\nv_mfma_f32_16x16x32_bf16 v[56:59], a[164:167], v[88:91], v[56:59]\nds_read_b128 v[84:87], v9 offset:25600\n" \
    "buffer_store_dwordx4 v[28:31], v2, s[64:67], s74 offen offset:0 sc1\nv_mfma_f32_16x16x32_bf16 v[60:63], a[164:167], v[68:71], v[60:63]\n" \
    "ds_read_b128 v[72:75], v9 offset:29696\nv_mfma_f32_16x16x32_bf16 v[32:35], a[148:151], v[88:91], v[32:35]\n" \
    "v_mfma_f32_16x16x32_bf16 v[36:39], a[148:151], v[68:71], v[36:39]\nv_mfma_f32_16x16x32_bf16 v[40:43], v[80:83], v[88:91], v[40:43]\n" \
    "v_mfma_f32_16x16x32_bf16 v[44:47], v[80:83], v[68:71], v[44:47]\ns_waitcnt lgkmcnt(0)\nv_mfma_f32_16x16x32_bf16 v[48:51], a[176:179], v[84:87], v[48:51]\n" \
    "buffer_store_dwordx4 v[28:31], v2, s[64:67], s74 offen offset:1024 sc1\ns_and_b32 s69, s35, 15\nv_mfma_f32_16x16x32_bf16 v[52:55], a[176:179], v[72:75], v[52:55]\n" \
    "ds_read_b128 v[68:71], v3 offset:15360\ns_lshl_b32 s69, s69, 18\nv_mfma_f32_16x16x32_bf16 v[56:59], a[184:187], v[84:87], v[56:59]\n" \
    "ds_read_b128 v[88:91], v10 offset:25600\ns_add_u32 s82, s38, s69\nv_mfma_f32_16x16x32_bf16 v[60:63], a[184:187], v[72:75], v[60:63]\n" \
    "ds_read_b128 v[76:79], v10 offset:29696\ns_lshr_b32 s69, s82, 8\nv_mfma_f32_16x16x32_bf16 v[32:35], a[168:171], v[84:87], v[32:35]\ns_and_b32 s69, s69, 0x2000\n" \
    "s_xor_b32 s82, s82, s69\nv_mfma_f32_16x16x32_bf16 v[36:39], a[168:171], v[72:75], v[36:39]\ns_and_b32 s69, s35, 15\ns_lshl_b32 s69, s69, 18\n" \
    "v_mfma_f32_16x16x32_bf16 v[40:43], v[64:67], v[84:87], v[40:43]\ns_add_u32 s83, s39, s69\nv_mfma_f32_16x16x32_bf16 v[44:47], v[64:67], v[72:75], v[44:47]\n" \
    "s_waitcnt lgkmcnt(0)\nv_mfma_f32_16x16x32_bf16 v[48:51], a[180:183], v[88:91], v[48:51]\nv_mfma_f32_16x16x32_bf16 v[52:55], a[180:183], v[76:79], v[52:55]\n" \
    "ds_read_b128 v[64:67], v3 offset:16384\nv_mfma_f32_16x16x32_bf16 v[56:59], a[188:191], v[88:91], v[56:59]\nds_read_b128 v[72:75], v9 offset:32768\n" \
    "v_mfma_f32_16x16x32_bf16 v[60:63], a[188:191], v[76:79], v[60:63]\nds_read_b128 v[80:83], v9 offset:36864\n" \
    "v_mfma_f32_16x16x32_bf16 v[32:35], a[172:175], v[88:91], v[32:35]\nv_mfma_f32_16x16x32_bf16 v[36:39], a[172:175], v[76:79], v[36:39]\n" \
    "v_mfma_f32_16x16x32_bf16 v[40:43], v[68:71], v[88:91], v[40:43]\nv_mfma_f32_16x16x32_bf16 v[44:47], v[68:71], v[76:79], v[44:47]\ns_waitcnt lgkmcnt(0)\n" \
    "v_mfma_f32_16x16x32_bf16 v[48:51], a[200:203], v[72:75], v[48:51]\nv_mfma_f32_16x16x32_bf16 v[52:55], a[200:203], v[80:83], v[52:55]\n" \
    "ds_read_b128 v[68:71], v3 offset:17408\nv_mfma_f32_16x16x32_bf16 v[56:59], a[208:211], v[72:75], v[56:59]\nds_read_b128 v[76:79], v10 offset:32768\n" \
    "v_mfma_f32_16x16x32_bf16 v[60:63], a[208:211], v[80:83], v[60:63]\nds_read_b128 v[84:87], v10 offset:36864\n" \
    "v_mfma_f32_16x16x32_bf16 v[32:35], a[192:195], v[72:75], v[32:35]\nv_mfma_f32_16x16x32_bf16 v[36:39], a[192:195], v[80:83], v[36:39]\n" \
    "v_mfma_f32_16x16x32_bf16 v[40:43], v[64:67], v[72:75], v[40:43]\nv_mfma_f32_16x16x32_bf16 v[44:47], v[64:67], v[80:83], v[44:47]\ns_waitcnt lgkmcnt(0)\n" \
    "v_mfma_f32_16x16x32_bf16 v[48:51], a[204:207], v[76:79], v[48:51]\nv_mfma_f32_16x16x32_bf16 v[52:55], a[204:207], v[84:87], v[52:55]\n" \
    "ds_read_b128 v[64:67], v3 offset:18432\nv_mfma_f32_16x16x32_bf16 v[56:59], a[212:215], v[76:79], v[56:59]\nds_read_b128 v[72:75], v9 offset:33792\n" \
    "v_mfma_f32_16x16x32_bf16 v[60:63], a[212:215], v[84:87], v[60:63]\nds_read_b128 v[80:83], v9 offset:37888\n" \
    "v_mfma_f32_16x16x32_bf16 v[32:35], a[196:199], v[76:79], v[32:35]\nv_mfma_f32_16x16x32_bf16 v[36:39], a[196:199], v[84:87], v[36:39]\n" \
    "v_mfma_f32_16x16x32_bf16 v[40:43], v[68:71], v[76:79], v[40:43]\nv_mfma_f32_16x16x32_bf16 v[44:47], v[68:71], v[84:87], v[44:47]\ns_waitcnt lgkmcnt(0)\n" \
    "v_mfma_f32_16x16x32_bf16 v[48:51], a[224:227], v[72:75], v[48:51]\nv_mfma_f32_16x16x32_bf16 v[52:55], a[224:227], v[80:83], v[52:55]\n" \
    "ds_read_b128 v[68:71], v3 offset:19456\nv_mfma_f32_16x16x32_bf16 v[56:59], a[232:235], v[72:75], v[56:59]\nds_read_b128 v[76:79], v10 offset:33792\n" \
    "v_mfma_f32_16x16x32_bf16 v[60:63], a[232:235], v[80:83], v[60:63]\nds_read_b128 v[84:87], v10 offset:37888\n" \
    "v_mfma_f32_16x16x32_bf16 v[32:35], a[216:219], v[72:75], v[32:35]\nv_mfma_f32_16x16x32_bf16 v[36:39], a[216:219], v[80:83], v[36:39]\n" \
    "v_mfma_f32_16x16x32_bf16 v[40:43], v[64:67], v[72:75], v[40:43]\nbuffer_load_dwordx2 v[18:19], off, s[64:67], s48 offset:768 sc1\n" \
    "v_mfma_f32_16x16x32_bf16 v[44:47], v[64:67], v[80:83], v[44:47]\ns_waitcnt lgkmcnt(0)\nv_mfma_f32_16x16x32_bf16 v[48:51], a[228:231], v[76:79], v[48:51]\n" \
    "v_mfma_f32_16x16x32_bf16 v[52:55], a[228:231], v[84:87], v[52:55]\nds_read_b128 v[64:67], v3 offset:20480\ns_waitcnt vmcnt(3)\ns_barrier\n" \
    "v_mfma_f32_16x16x32_bf16 v[56:59], a[236:239], v[76:79], v[56:59]\nds_read_b128 v[72:75], v9 offset:0\nv_mfma_f32_16x16x32_bf16 v[60:63], a[236:239], v[84:87], v[60:63]\n" \
    "ds_read_b128 v[80:83], v9 offset:4096\ns_add_u32 m0, s41, 16384\nv_mfma_f32_16x16x32_bf16 v[32:35], a[220:223], v[76:79], v[32:35]\ns_xor_b32 s85, s62, 1792\n" \
    "s_add_u32 s85, s85, s40\nv_mfma_f32_16x16x32_bf16 v[36:39], a[220:223], v[84:87], v[36:39]\nbuffer_load_dwordx4 v11, s[64:67], s85 offen sc1 lds\n" \
    "v_mfma_f32_16x16x32_bf16 v[40:43], v[68:71], v[76:79], v[40:43]\nv_mfma_f32_16x16x32_bf16 v[44:47], v[68:71], v[84:87], v[44:47]\ns_waitcnt lgkmcnt(0)\n" \
    "v_mfma_f32_16x16x32_bf16 v[48:51], a[248:251], v[72:75], v[48:51]\nbuffer_load_dwordx4 v12, s[64:67], s85 offen offset:1024 sc1 lds\n" \
    "v_mfma_f32_16x16x32_bf16 v[52:55], a[248:251], v[80:83], v[52:55]\nds_read_b128 v[68:71], v3 offset:21504\nv_mfma_f32_16x16x32_bf16 v[56:59], v[92:95], v[72:75], v[56:59]\n" \
    "ds_read_b128 v[76:79], v10 offset:0\nv_mfma_f32_16x16x32_bf16 v[60:63], v[92:95], v[80:83], v[60:63]\nds_read_b128 v[84:87], v10 offset:4096\n" \
    "v_mfma_f32_16x16x32_bf16 v[32:35], a[240:243], v[72:75], v[32:35]\nv_mfma_f32_16x16x32_bf16 v[36:39], a[240:243], v[80:83], v[36:39]\n" \
    "v_mfma_f32_16x16x32_bf16 v[40:43], v[64:67], v[72:75], v[40:43]\nv_mfma_f32_16x16x32_bf16 v[44:47], v[64:67], v[80:83], v[44:47]\ns_waitcnt lgkmcnt(0)\n" \
    "v_mfma_f32_16x16x32_bf16 v[48:51], a[252:255], v[76:79], v[48:51]\nv_mfma_f32_16x16x32_bf16 v[52:55], a[252:255], v[84:87], v[52:55]\n" \
    "ds_read_b128 v[64:67], v3 offset:22528\nv_mfma_f32_16x16x32_bf16 v[56:59], v[96:99], v[76:79], v[56:59]\nds_read_b128 v[72:75], v9 offset:1024\n" \
    "v_mfma_f32_16x16x32_bf16 v[60:63], v[96:99], v[84:87], v[60:63]\nds_read_b128 v[80:83], v9 offset:5120\nv_mfma_f32_16x16x32_bf16 v[32:35], a[244:247], v[76:79], v[32:35]\n" \
    "v_mfma_f32_16x16x32_bf16 v[36:39], a[244:247], v[84:87], v[36:39]\nv_mfma_f32_16x16x32_bf16 v[40:43], v[68:71], v[76:79], v[40:43]\n" \
    "v_mfma_f32_16x16x32_bf16 v[44:47], v[68:71], v[84:87], v[44:47]\ns_waitcnt lgkmcnt(0)\nv_mfma_f32_16x16x32_bf16 v[48:51], v[108:111], v[72:75], v[48:51]\n" \
    "v_mfma_f32_16x16x32_bf16 v[52:55], v[108:111], v[80:83], v[52:55]\nds_read_b128 v[68:71], v3 offset:23552\n" \
    "v_mfma_f32_16x16x32_bf16 v[56:59], v[116:119], v[72:75], v[56:59]\nds_read_b128 v[76:79], v10 offset:1024\n" \
    "v_mfma_f32_16x16x32_bf16 v[60:63], v[116:119], v[80:83], v[60:63]\nds_read_b128 v[84:87], v10 offset:5120\n" \
    "v_mfma_f32_16x16x32_bf16 v[32:35], v[100:103], v[72:75], v[32:35]\nv_mfma_f32_16x16x32_bf16 v[36:39], v[100:103], v[80:83], v[36:39]\n" \
    "v_mfma_f32_16x16x32_bf16 v[40:43], v[64:67], v[72:75], v[40:43]\nv_mfma_f32_16x16x32_bf16 v[44:47], v[64:67], v[80:83], v[44:47]\ns_waitcnt lgkmcnt(0)\n" \
    "v_mfma_f32_16x16x32_bf16 v[48:51], v[112:115], v[76:79], v[48:51]\nv_mfma_f32_16x16x32_bf16 v[52:55], v[112:115], v[84:87], v[52:55]\n" \
    "v_mfma_f32_16x16x32_bf16 v[56:59], v[120:123], v[76:79], v[56:59]\nds_read_b128 v[64:67], v9 offset:8192\n" \
    "v_mfma_f32_16x16x32_bf16 v[60:63], v[120:123], v[84:87], v[60:63]\nds_read_b128 v[72:75], v9 offset:12288\n" \
    "v_mfma_f32_16x16x32_bf16 v[32:35], v[104:107], v[76:79], v[32:35]\nv_mfma_f32_16x16x32_bf16 v[36:39], v[104:107], v[84:87], v[36:39]\n" \
    "v_mfma_f32_16x16x32_bf16 v[40:43], v[68:71], v[76:79], v[40:43]\ns_waitcnt vmcnt(2)\nv_and_b32 v18, 255, v18\nv_and_b32 v19, 255, v19\n" \
    "v_mfma_f32_16x16x32_bf16 v[44:47], v[68:71], v[84:87], v[44:47]\ns_waitcnt lgkmcnt(0)\nv_max_u32 v18, v18, v19\n" \
    "v_mfma_f32_16x16x32_bf16 v[48:51], v[140:143], v[64:67], v[48:51]\nv_readfirstlane_b32 s20, v18\nv_mfma_f32_16x16x32_bf16 v[52:55], v[140:143], v[72:75], v[52:55]\n" \
    "s_add_u32 s21, s20, s49\ns_andn2_b32 s21, s21, 3\nv_mfma_f32_16x16x32_bf16 v[56:59], v[148:151], v[64:67], v[56:59]\nds_read_b128 v[68:71], v10 offset:8192\n" \
    "s_add_u32 s22, s18, 4\nv_mfma_f32_16x16x32_bf16 v[60:63], v[148:151], v[72:75], v[60:63]\nds_read_b128 v[76:79], v10 offset:12288\ns_min_u32 s21, s21, s22\n" \
    "v_mfma_f32_16x16x32_bf16 v[32:35], v[124:127], v[64:67], v[32:35]\ns_max_u32 s21, s21, 128\ns_cmp_eq_u32 s20, 0\ns_cselect_b32 s18, s18, s21\n" \
    "v_mfma_f32_16x16x32_bf16 v[36:39], v[124:127], v[72:75], v[36:39]\ns_cmp_lt_u32 s35, 12\ns_cselect_b32 s20, s44, 0\n" \
    "v_mfma_f32_16x16x32_bf16 v[40:43], v[132:135], v[64:67], v[40:43]\ns_max_u32 s18, s18, s20\nv_mfma_f32_16x16x32_bf16 v[44:47], v[132:135], v[72:75], v[44:47]\n" \
    "s_waitcnt lgkmcnt(0)\nv_mfma_f32_16x16x32_bf16 v[48:51], v[144:147], v[68:71], v[48:51]\nbuffer_load_dword v13, off, s[64:67], s42 sc1\ns_and_b32 s69, s35, 15\n" \
    "v_mfma_f32_16x16x32_bf16 v[52:55], v[144:147], v[76:79], v[52:55]\ns_lshl_b32 s69, s69, 17\ns_add_u32 s81, s37, s69\n" \
    "v_mfma_f32_16x16x32_bf16 v[56:59], v[152:155], v[68:71], v[56:59]\nds_read_b128 v[64:67], v9 offset:9216\ns_lshr_b32 s69, s81, 8\n" \
    "v_mfma_f32_16x16x32_bf16 v[60:63], v[152:155], v[76:79], v[60:63]\nds_read_b128 v[72:75], v9 offset:13312\ns_and_b32 s69, s69, 0x2000\n" \
    "v_mfma_f32_16x16x32_bf16 v[32:35], v[128:131], v[68:71], v[32:35]\ns_xor_b32 s81, s81, s69\ns_mov_b32 s68, 0\n" \
    "v_mfma_f32_16x16x32_bf16 v[36:39], v[128:131], v[76:79], v[36:39]\nv_mfma_f32_16x16x32_bf16 v[40:43], v[136:139], v[68:71], v[40:43]\n" \
    "v_mfma_f32_16x16x32_bf16 v[44:47], v[136:139], v[76:79], v[44:47]\ns_waitcnt lgkmcnt(0)\nv_mfma_f32_16x16x32_bf16 v[48:51], v[172:175], v[64:67], v[48:51]\n" \
    "v_mfma_f32_16x16x32_bf16 v[52:55], v[172:175], v[72:75], v[52:55]\nv_mfma_f32_16x16x32_bf16 v[56:59], v[180:183], v[64:67], v[56:59]\n" \
    "ds_read_b128 v[68:71], v10 offset:9216\nv_mfma_f32_16x16x32_bf16 v[60:63], v[180:183], v[72:75], v[60:63]\nds_read_b128 v[76:79], v10 offset:13312\n" \
    "v_mfma_f32_16x16x32_bf16 v[32:35], v[156:159], v[64:67], v[32:35]\nv_mfma_f32_16x16x32_bf16 v[36:39], v[156:159], v[72:75], v[36:39]\n" \
    "v_mfma_f32_16x16x32_bf16 v[40:43], v[164:167], v[64:67], v[40:43]\nv_mfma_f32_16x16x32_bf16 v[44:47], v[164:167], v[72:75], v[44:47]\ns_waitcnt lgkmcnt(0)\n" \
    "v_mfma_f32_16x16x32_bf16 v[48:51], v[176:179], v[68:71], v[48:51]\nv_mfma_f32_16x16x32_bf16 v[52:55], v[176:179], v[76:79], v[52:55]\ns_waitcnt vmcnt(1)\ns_barrier\n" \
    "v_mfma_f32_16x16x32_bf16 v[56:59], v[184:187], v[68:71], v[56:59]\nds_read_b128 v[64:67], v9 offset:16384\n" \
    "v_mfma_f32_16x16x32_bf16 v[60:63], v[184:187], v[76:79], v[60:63]\nds_read_b128 v[72:75], v9 offset:20480\ns_add_u32 m0, s41, 0\n" \
    "v_mfma_f32_16x16x32_bf16 v[32:35], v[160:163], v[68:71], v[32:35]\ns_xor_b32 s85, s62, 0\ns_add_u32 s85, s85, s84\n" \
    "v_mfma_f32_16x16x32_bf16 v[36:39], v[160:163], v[76:79], v[36:39]\nbuffer_load_dwordx4 v11, s[64:67], s85 offen sc1 lds\n" \
    "v_mfma_f32_16x16x32_bf16 v[40:43], v[168:171], v[68:71], v[40:43]\nv_mfma_f32_16x16x32_bf16 v[44:47], v[168:171], v[76:79], v[44:47]\ns_waitcnt lgkmcnt(0)\n" \
    "v_mfma_f32_16x16x32_bf16 v[48:51], v[204:207], v[64:67], v[48:51]\nbuffer_load_dwordx4 v12, s[64:67], s85 offen offset:1024 sc1 lds\n" \
    "v_mfma_f32_16x16x32_bf16 v[52:55], v[204:207], v[72:75], v[52:55]\nv_mfma_f32_16x16x32_bf16 v[56:59], v[212:215], v[64:67], v[56:59]\n" \
    "ds_read_b128 v[68:71], v10 offset:16384\nv_mfma_f32_16x16x32_bf16 v[60:63], v[212:215], v[72:75], v[60:63]\nds_read_b128 v[76:79], v10 offset:20480\n" \
    "v_mfma_f32_16x16x32_bf16 v[32:35], v[188:191], v[64:67], v[32:35]\ns_waitcnt vmcnt(2)\nv_readfirstlane_b32 s74, v13\n" \
    "v_mfma_f32_16x16x32_bf16 v[36:39], v[188:191], v[72:75], v[36:39]\ns_and_b32 s75, s74, 127\ns_lshr_b32 s69, s74, 8\n" \
    "v_mfma_f32_16x16x32_bf16 v[40:43], v[196:199], v[64:67], v[40:43]\ns_sub_u32 s69, s69, s80\ns_bfe_i32 s69, s69, 0x180000\n" \
    "v_mfma_f32_16x16x32_bf16 v[44:47], v[196:199], v[72:75], v[44:47]\ns_cmp_eq_u32 s75, 0\ns_cselect_b32 s69, 0, s69\ns_waitcnt lgkmcnt(0)\n" \
    "v_mfma_f32_16x16x32_bf16 v[48:51], v[208:211], v[68:71], v[48:51]\ns_add_u32 s80, s80, s69\ns_lshl_b32 s74, s74, 5\n" \
    "v_mfma_f32_16x16x32_bf16 v[52:55], v[208:211], v[76:79], v[52:55]\ns_and_b32 s74, s74, 0x1000\ns_andn2_b32 s81, s81, 0x1000\n" \
    "v_mfma_f32_16x16x32_bf16 v[56:59], v[216:219], v[68:71], v[56:59]\nds_read_b128 v[64:67], v9 offset:17408\ns_or_b32 s81, s81, s74\n" \
    "v_mfma_f32_16x16x32_bf16 v[60:63], v[216:219], v[76:79], v[60:63]\nds_read_b128 v[72:75], v9 offset:21504\ns_lshl_b32 s74, s74, 2\n" \
    "v_mfma_f32_16x16x32_bf16 v[32:35], v[192:195], v[68:71], v[32:35]\ns_andn2_b32 s82, s82, 0x4000\ns_or_b32 s82, s82, s74\n" \
    "v_mfma_f32_16x16x32_bf16 v[36:39], v[192:195], v[76:79], v[36:39]\nv_mfma_f32_16x16x32_bf16 v[40:43], v[200:203], v[68:71], v[40:43]\n" \
    "ds_read_b128 v[68:71], v10 offset:17408\nv_mfma_f32_16x16x32_bf16 v[44:47], v[200:203], v[76:79], v[44:47]\nds_read_b128 v[76:79], v10 offset:21504\ns_waitcnt lgkmcnt(2)\n" \
    "v_mfma_f32_16x16x32_bf16 v[48:51], v[236:239], v[64:67], v[48:51]\nv_mfma_f32_16x16x32_bf16 v[52:55], v[236:239], v[72:75], v[52:55]\n" \
    "v_mfma_f32_16x16x32_bf16 v[56:59], v[244:247], v[64:67], v[56:59]\nv_mfma_f32_16x16x32_bf16 v[60:63], v[244:247], v[72:75], v[60:63]\ns_waitcnt lgkmcnt(0)\ns_barrier\n" \
    "v_mfma_f32_16x16x32_bf16 v[48:51], v[240:243], v[68:71], v[48:51]\ns_add_u32 m0, s41, 8192\ns_xor_b32 s85, s62, 256\n" \
    "v_mfma_f32_16x16x32_bf16 v[52:55], v[240:243], v[76:79], v[52:55]\ns_add_u32 s85, s85, s84\nbuffer_load_dwordx4 v11, s[64:67], s85 offen sc1 lds\n" \
    "v_mfma_f32_16x16x32_bf16 v[56:59], v[248:251], v[68:71], v[56:59]\nv_mfma_f32_16x16x32_bf16 v[60:63], v[248:251], v[76:79], v[60:63]\ns_nop 0\n" \
    "v_cvt_pk_bf16_f32 v16, v48, v49\nv_cvt_pk_bf16_f32 v17, v50, v51\nv_mfma_f32_16x16x32_bf16 v[32:35], v[220:223], v[64:67], v[32:35]\n" \
    "buffer_load_dwordx4 v12, s[64:67], s85 offen offset:1024 sc1 lds\ns_add_u32 m0, s41, 16384\nv_mfma_f32_16x16x32_bf16 v[36:39], v[220:223], v[72:75], v[36:39]\n" \
    "s_xor_b32 s85, s62, 512\ns_add_u32 s85, s85, s84\nv_cvt_pk_bf16_f32 v18, v56, v57\nv_mfma_f32_16x16x32_bf16 v[40:43], v[228:231], v[64:67], v[40:43]\n" \
    "buffer_load_dwordx4 v11, s[64:67], s85 offen sc1 lds\nv_cvt_pk_bf16_f32 v19, v58, v59\nv_mfma_f32_16x16x32_bf16 v[44:47], v[228:231], v[72:75], v[44:47]\n" \
    "ds_write_b128 v14, v[16:19] offset:0\nv_cvt_pk_bf16_f32 v20, v52, v53\nv_cvt_pk_bf16_f32 v21, v54, v55\nv_mfma_f32_16x16x32_bf16 v[32:35], v[224:227], v[68:71], v[32:35]\n" \
    "v_cvt_pk_bf16_f32 v22, v60, v61\nv_cvt_pk_bf16_f32 v23, v62, v63\nv_mfma_f32_16x16x32_bf16 v[36:39], v[224:227], v[76:79], v[36:39]\n" \
    "buffer_load_dwordx4 v12, s[64:67], s85 offen offset:1024 sc1 lds\nds_write_b128 v14, v[20:23] offset:4096\n" \
    "v_mfma_f32_16x16x32_bf16 v[40:43], v[232:235], v[68:71], v[40:43]\nv_mfma_f32_16x16x32_bf16 v[44:47], v[232:235], v[76:79], v[44:47]\n.Lq10_%=:\ns_cmp_lt_u32 s75, s35\n" \
    "s_cbranch_scc1 .Lq11_%=\ns_cmp_eq_u32 s36, 1\ns_cbranch_scc1 .Lq5_%=\ns_cmp_eq_u32 s36, 2\ns_cbranch_scc1 .Lq6_%=\ns_cmp_eq_u32 s36, 3\ns_cbranch_scc1 .Lq7_%=\n.Lq4_%=:\n" \
    ".Lq13_%=:\ns_memrealtime s[72:73]\ns_waitcnt lgkmcnt(0)\ns_mov_b32 s68, 0\ns_sub_u32 s76, s80, s45\ns_sub_u32 s69, s72, s76\ns_cmp_lt_i32 s69, -8\ns_cbranch_scc1 .Lq16_%=\n" \
    "s_xor_b32 s90, s81, 0x800\n.Lq18_%=:\ns_memrealtime s[72:73]\ns_waitcnt lgkmcnt(0)\ns_sub_u32 s72, s72, s80\ns_cmp_lt_i32 s72, 0\ns_cbranch_scc1 .Lq18_%=\n" \
    "buffer_load_dwordx4 v[72:75], v2, s[64:67], s81 offen offset:0 sc1\nv_cvt_pk_bf16_f32 v24, v32, v33\nv_cvt_pk_bf16_f32 v25, v34, v35\nv_cvt_pk_bf16_f32 v26, v40, v41\n" \
    "buffer_load_dwordx4 v[76:79], v2, s[64:67], s81 offen offset:1024 sc1\nv_cvt_pk_bf16_f32 v27, v42, v43\nds_write_b128 v14, v[24:27] offset:8192\n" \
    "v_cvt_pk_bf16_f32 v28, v36, v37\nbuffer_load_dwordx4 v[80:83], v2, s[64:67], s90 offen offset:0 sc1\nv_cvt_pk_bf16_f32 v29, v38, v39\nv_cvt_pk_bf16_f32 v30, v44, v45\n" \
    "v_cvt_pk_bf16_f32 v31, v46, v47\nbuffer_load_dwordx4 v[84:87], v2, s[64:67], s90 offen offset:1024 sc1\nds_write_b128 v14, v[28:31] offset:12288\n" \
    "ds_read_b128 v[32:35], v3 offset:0\nds_read_b128 v[36:39], v3 offset:1024\nds_read_b128 v[40:43], v3 offset:8192\nds_read_b128 v[44:47], v3 offset:9216\n" \
    "ds_read_b128 v[48:51], v3 offset:16384\nds_read_b128 v[52:55], v3 offset:17408\nds_read_b128 v[56:59], v3 offset:2048\nds_read_b128 v[60:63], v3 offset:3072\n" \
    "ds_read_b128 v[64:67], v3 offset:4096\nds_read_b128 v[68:71], v3 offset:5120\n.Lq17_%=:\nv_mov_b64 v[16:17], 0\nv_mov_b64 v[18:19], 0\nv_mov_b64 v[20:21], 0\n" \
    "v_mov_b64 v[22:23], 0\nv_mov_b64 v[24:25], 0\nv_mov_b64 v[26:27], 0\nv_mov_b64 v[28:29], 0\nv_mov_b64 v[30:31], 0\ns_waitcnt vmcnt(3) lgkmcnt(8)\n" \
    "v_smfmac_f32_16x16x64_bf16 v[20:23], v[72:75], v[32:39], v0\nds_write_b128 v1, v[72:75] offset:24576\nv_max3_u32 v252, v72, v73, v74\n" \
    "v_smfmac_f32_16x16x64_bf16 v[16:19], v[72:75], a[0:7], v0\nv_smfmac_f32_16x16x64_bf16 v[24:27], v[72:75], a[8:15], v0\n" \
    "v_smfmac_f32_16x16x64_bf16 v[28:31], v[72:75], a[16:23], v0\ns_waitcnt vmcnt(2)\nds_write_b128 v1, v[76:79] offset:28672\ns_waitcnt lgkmcnt(8)\n" \
    "v_max3_u32 v252, v252, v75, v76\nv_smfmac_f32_16x16x64_bf16 v[20:23], v[76:79], v[40:47], v0\nv_max3_u32 v252, v252, v77, v78\n" \
    "v_smfmac_f32_16x16x64_bf16 v[16:19], v[76:79], a[96:103], v0\nv_cmp_u_f32 vcc, v252, v79\nv_smfmac_f32_16x16x64_bf16 v[24:27], v[76:79], a[104:111], v0\n" \
    "s_cbranch_vccnz .Lq24_%=\n.Lq20_%=:\nv_smfmac_f32_16x16x64_bf16 v[28:31], v[76:79], a[112:119], v0\nds_read_b128 v[36:39], v3 offset:7168\ns_waitcnt vmcnt(1)\n" \
    "v_max3_u32 v252, v80, v81, v82\nv_cmp_u_f32 vcc, v252, v83\ns_nop 0\ns_cbranch_vccnz .Lq25_%=\n.Lq21_%=:\ns_waitcnt lgkmcnt(7)\n" \
    "v_smfmac_f32_16x16x64_bf16 v[20:23], v[80:83], v[48:55], v0\nds_write_b128 v1, v[80:83] offset:32768\nv_smfmac_f32_16x16x64_bf16 v[16:19], v[80:83], a[192:199], v0\n" \
    "ds_read_b128 v[32:35], v3 offset:6144\ns_waitcnt lgkmcnt(3)\ns_barrier\nds_read_b128 v[88:91], v1 offset:25600\n" \
    "v_smfmac_f32_16x16x64_bf16 v[24:27], v[80:83], a[200:207], v0\nds_read_b128 v[40:43], v3 offset:10240\nv_smfmac_f32_16x16x64_bf16 v[28:31], v[80:83], a[208:215], v0\n" \
    "ds_read_b128 v[48:51], v1 offset:26624\ns_waitcnt vmcnt(0)\nv_max3_u32 v252, v84, v85, v86\nv_cmp_u_f32 vcc, v252, v87\ns_nop 0\ns_cbranch_vccnz .Lq26_%=\n.Lq22_%=:\n" \
    "v_smfmac_f32_16x16x64_bf16 v[20:23], v[84:87], v[132:139], v0\nds_write_b128 v1, v[84:87] offset:36864\nv_smfmac_f32_16x16x64_bf16 v[16:19], v[84:87], v[124:131], v0\n" \
    "ds_read_b128 v[52:55], v1 offset:27648\nv_smfmac_f32_16x16x64_bf16 v[24:27], v[84:87], v[140:147], v0\nds_read_b128 v[72:75], v1 offset:29696\n" \
    "v_smfmac_f32_16x16x64_bf16 v[28:31], v[84:87], v[148:155], v0\nds_read_b128 v[44:47], v3 offset:11264\ns_waitcnt lgkmcnt(6)\n" \
    "v_smfmac_f32_16x16x64_bf16 v[20:23], v[88:91], v[56:63], v0\nds_read_b128 v[56:59], v1 offset:30720\nv_smfmac_f32_16x16x64_bf16 v[16:19], v[88:91], a[24:31], v0\n" \
    "ds_read_b128 v[76:79], v3 offset:12288\nv_smfmac_f32_16x16x64_bf16 v[24:27], v[88:91], a[32:39], v0\nds_read_b128 v[80:83], v3 offset:13312\n" \
    "v_smfmac_f32_16x16x64_bf16 v[28:31], v[88:91], a[40:47], v0\nds_read_b128 v[60:63], v1 offset:31744\ns_waitcnt lgkmcnt(7)\ns_barrier\n" \
    "v_smfmac_f32_16x16x64_bf16 v[20:23], v[48:51], v[64:71], v0\nds_read_b128 v[64:67], v3 offset:14336\nv_smfmac_f32_16x16x64_bf16 v[16:19], v[48:51], a[48:55], v0\n" \
    "ds_read_b128 v[68:71], v3 offset:15360\ns_waitcnt lgkmcnt(8)\nv_smfmac_f32_16x16x64_bf16 v[20:23], v[52:55], v[32:39], v0\nds_read_b128 v[32:35], v1 offset:33792\n" \
    "v_smfmac_f32_16x16x64_bf16 v[16:19], v[52:55], a[72:79], v0\nds_read_b128 v[84:87], v3 offset:18432\ns_waitcnt lgkmcnt(8)\n" \
    "v_smfmac_f32_16x16x64_bf16 v[20:23], v[72:75], v[40:47], v0\nds_read_b128 v[88:91], v3 offset:19456\nv_smfmac_f32_16x16x64_bf16 v[16:19], v[72:75], a[120:127], v0\n" \
    "ds_read_b128 v[36:39], v1 offset:34816\ns_waitcnt lgkmcnt(7)\nv_smfmac_f32_16x16x64_bf16 v[20:23], v[56:59], v[76:83], v0\nds_read_b128 v[40:43], v3 offset:20480\n" \
    "v_smfmac_f32_16x16x64_bf16 v[16:19], v[56:59], a[144:151], v0\nds_read_b128 v[44:47], v3 offset:21504\ns_waitcnt lgkmcnt(6)\n" \
    "v_smfmac_f32_16x16x64_bf16 v[20:23], v[60:63], v[64:71], v0\nds_read_b128 v[64:67], v1 offset:35840\nv_smfmac_f32_16x16x64_bf16 v[16:19], v[60:63], a[168:175], v0\n" \
    "ds_read_b128 v[76:79], v3 offset:22528\ns_waitcnt lgkmcnt(5)\nv_smfmac_f32_16x16x64_bf16 v[20:23], v[32:35], v[84:91], v0\nds_read_b128 v[80:83], v3 offset:23552\n" \
    "v_smfmac_f32_16x16x64_bf16 v[16:19], v[32:35], a[216:223], v0\nds_read_b128 v[68:71], v1 offset:37888\ns_waitcnt lgkmcnt(4)\n" \
    "v_smfmac_f32_16x16x64_bf16 v[20:23], v[36:39], v[40:47], v0\nds_read_b128 v[40:43], v1 offset:38912\nv_smfmac_f32_16x16x64_bf16 v[16:19], v[36:39], a[240:247], v0\n" \
    "ds_read_b128 v[44:47], v1 offset:39936\ns_waitcnt lgkmcnt(3)\nv_smfmac_f32_16x16x64_bf16 v[20:23], v[64:67], v[76:83], v0\n" \
    "v_smfmac_f32_16x16x64_bf16 v[16:19], v[64:67], v[100:107], v0\ns_waitcnt lgkmcnt(2)\nv_smfmac_f32_16x16x64_bf16 v[20:23], v[68:71], v[164:171], v0\ns_waitcnt lgkmcnt(1)\n" \
    "v_smfmac_f32_16x16x64_bf16 v[20:23], v[40:43], v[196:203], v0\ns_waitcnt lgkmcnt(0)\nv_smfmac_f32_16x16x64_bf16 v[20:23], v[44:47], v[228:235], v0\ns_nop 2\n" \
    "v_smfmac_f32_16x16x64_bf16 v[16:19], v[68:71], v[156:163], v0\ns_nop 2\nv_smfmac_f32_16x16x64_bf16 v[16:19], v[40:43], v[188:195], v0\nv_permlane32_swap_b32 v20, v22\n" \
    "v_smfmac_f32_16x16x64_bf16 v[16:19], v[44:47], v[220:227], v0\ns_nop 1\nv_permlane32_swap_b32 v21, v23\nv_smfmac_f32_16x16x64_bf16 v[24:27], v[48:51], a[56:63], v0\n" \
    "s_nop 1\nv_add_f32 v20, v20, v22\nv_smfmac_f32_16x16x64_bf16 v[28:31], v[48:51], a[64:71], v0\nv_add_f32 v21, v21, v23\n" \
    "v_smfmac_f32_16x16x64_bf16 v[24:27], v[52:55], a[80:87], v0\nv_mul_f32 v20, s32, v20\nv_smfmac_f32_16x16x64_bf16 v[28:31], v[52:55], a[88:95], v0\nv_mul_f32 v21, s32, v21\n" \
    "v_smfmac_f32_16x16x64_bf16 v[24:27], v[72:75], a[128:135], v0\nv_cvt_pk_bf16_f32 v20, v20, v21\nv_smfmac_f32_16x16x64_bf16 v[28:31], v[72:75], a[136:143], v0\n" \
    "ds_write_b16 v4, v20 offset:8\nv_permlane32_swap_b32 v16, v18\nv_smfmac_f32_16x16x64_bf16 v[24:27], v[56:59], a[152:159], v0\nds_write_b16_d16_hi v4, v20 offset:24\n" \
    "v_permlane32_swap_b32 v17, v19\nv_smfmac_f32_16x16x64_bf16 v[24:27], v[60:63], a[176:183], v0\nds_read_b128 v[48:51], v1 offset:40960\nv_add_f32 v16, v16, v18\n" \
    "v_smfmac_f32_16x16x64_bf16 v[24:27], v[32:35], a[224:231], v0\nds_read_b128 v[52:55], v1 offset:45056\nv_add_f32 v17, v17, v19\n" \
    "v_smfmac_f32_16x16x64_bf16 v[24:27], v[36:39], a[248:255], v0\nds_read_b128 v[72:75], v1 offset:41984\nv_mul_f32 v16, s32, v16\n" \
    "v_smfmac_f32_16x16x64_bf16 v[24:27], v[64:67], v[108:115], v0\nds_read_b128 v[76:79], v1 offset:46080\nv_mul_f32 v17, s32, v17\n" \
    "v_smfmac_f32_16x16x64_bf16 v[24:27], v[68:71], v[172:179], v0\nds_read_b128 v[80:83], v1 offset:43008\nv_cvt_pk_bf16_f32 v16, v16, v17\n" \
    "v_smfmac_f32_16x16x64_bf16 v[24:27], v[40:43], v[204:211], v0\nds_write_b16 v4, v16 offset:0\nv_smfmac_f32_16x16x64_bf16 v[24:27], v[44:47], v[236:243], v0\n" \
    "ds_write_b16_d16_hi v4, v16 offset:16\ns_nop 1\nv_smfmac_f32_16x16x64_bf16 v[28:31], v[56:59], a[160:167], v0\nds_read_b128 v[56:59], v1 offset:47104\ns_nop 1\n" \
    "v_smfmac_f32_16x16x64_bf16 v[28:31], v[60:63], a[184:191], v0\nds_read_b128 v[60:63], v1 offset:44032\nv_permlane32_swap_b32 v24, v26\n" \
    "v_smfmac_f32_16x16x64_bf16 v[28:31], v[32:35], a[232:239], v0\nds_read_b128 v[32:35], v1 offset:48128\nv_permlane32_swap_b32 v25, v27\n" \
    "v_smfmac_f32_16x16x64_bf16 v[28:31], v[36:39], v[92:99], v0\nds_read_b128 v[36:39], v6 offset:0\nv_add_f32 v24, v24, v26\n" \
    "v_smfmac_f32_16x16x64_bf16 v[28:31], v[64:67], v[116:123], v0\nds_read_b128 v[64:67], v6 offset:512\nv_add_f32 v25, v25, v27\n" \
    "v_smfmac_f32_16x16x64_bf16 v[28:31], v[68:71], v[180:187], v0\nds_read_b128 v[68:71], v6 offset:1024\nv_cvt_pk_bf16_f32 v24, v24, v25\n" \
    "v_smfmac_f32_16x16x64_bf16 v[28:31], v[40:43], v[212:219], v0\nds_read_b128 v[40:43], v6 offset:1536\nv_smfmac_f32_16x16x64_bf16 v[28:31], v[44:47], v[244:251], v0\n" \
    "ds_write_b16 v4, v24 offset:2048\nds_write_b16_d16_hi v4, v24 offset:2064\ns_waitcnt lgkmcnt(5)\nv_mfma_f32_16x16x32_bf16 v[16:19], v[48:51], v[36:39], 0\ns_nop 2\n" \
    "v_mfma_f32_16x16x32_bf16 v[20:23], v[52:55], v[36:39], 0\nv_permlane32_swap_b32 v28, v30\nv_permlane32_swap_b32 v29, v31\nv_add_f32 v28, v28, v30\nv_add_f32 v29, v29, v31\n" \
    "v_cvt_pk_bf16_f32 v28, v28, v29\nds_write_b16 v4, v28 offset:2056\nds_write_b16_d16_hi v4, v28 offset:2072\ns_waitcnt lgkmcnt(6)\n" \
    "v_mfma_f32_16x16x32_bf16 v[16:19], v[72:75], v[64:67], v[16:19]\nv_mfma_f32_16x16x32_bf16 v[20:23], v[76:79], v[64:67], v[20:23]\ns_waitcnt lgkmcnt(5)\n" \
    "v_mfma_f32_16x16x32_bf16 v[16:19], v[80:83], v[68:71], v[16:19]\nds_read_b128 v[44:47], v8 offset:0\nds_read_b128 v[84:87], v8 offset:512\n" \
    "v_mfma_f32_16x16x32_bf16 v[20:23], v[56:59], v[68:71], v[20:23]\nds_read_b128 v[88:91], v8 offset:1024\nds_read_b128 v[28:31], v8 offset:1536\ns_waitcnt lgkmcnt(8)\n" \
    "v_mfma_f32_16x16x32_bf16 v[16:19], v[60:63], v[40:43], v[16:19]\nv_mfma_f32_16x16x32_bf16 v[20:23], v[32:35], v[40:43], v[20:23]\nv_mov_b32 v15, v15\nv_mov_b32 v15, v15\n" \
    "s_waitcnt lgkmcnt(3)\nv_mfma_f32_16x16x32_bf16 v[24:27], v[44:47], v[36:39], 0\ns_nop 2\ns_waitcnt lgkmcnt(2)\n" \
    "v_mov_b32_dpp v16, v20 quad_perm:[0,1,2,3] row_mask:0xf bank_mask:0xc\nv_mov_b32_dpp v17, v21 quad_perm:[0,1,2,3] row_mask:0xf bank_mask:0xc\n" \
    "v_mfma_f32_16x16x32_bf16 v[24:27], v[84:87], v[64:67], v[24:27]\ns_waitcnt lgkmcnt(1)\nv_mov_b32_dpp v18, v22 quad_perm:[0,1,2,3] row_mask:0xf bank_mask:0xc\n" \
    "v_mov_b32_dpp v19, v23 quad_perm:[0,1,2,3] row_mask:0xf bank_mask:0xc\nv_mfma_f32_16x16x32_bf16 v[24:27], v[88:91], v[68:71], v[24:27]\ns_waitcnt lgkmcnt(0)\n" \
    "v_max3_f32 v254, v16, v17, v18\nv_max_f32 v254, v254, v19\nv_mfma_f32_16x16x32_bf16 v[24:27], v[28:31], v[40:43], v[24:27]\ns_nop 0\n" \
    "v_max_f32_dpp v252, v254, v254 row_ror:8 row_mask:0xf bank_mask:0xf\nv_max_f32_dpp v253, v254, v254 row_ror:8 row_mask:0xf bank_mask:0xf\ns_nop 1\n" \
    "v_permlane32_swap_b32 v252, v253\nv_max_f32 v252, v252, v253\ns_xor_b32 s74, s82, 0x4000\nv_max3_f32 v254, v252, v24, v25\nv_max3_f32 v252, v254, v26, v27\n" \
    "v_max3_f32 v253, v254, v26, v27\ns_nop 1\nv_permlane16_swap_b32 v252, v253\nv_max_f32 v252, v252, v253\n" \
    "v_pk_add_f32 v[16:17], v[16:17], v[252:253] op_sel_hi:[1,0] neg_lo:[0,1] neg_hi:[0,1]\n" \
    "v_pk_add_f32 v[18:19], v[18:19], v[252:253] op_sel_hi:[1,0] neg_lo:[0,1] neg_hi:[0,1]\n" \
    "v_pk_add_f32 v[24:25], v[24:25], v[252:253] op_sel_hi:[1,0] neg_lo:[0,1] neg_hi:[0,1]\n" \
    "v_pk_add_f32 v[26:27], v[26:27], v[252:253] op_sel_hi:[1,0] neg_lo:[0,1] neg_hi:[0,1]\nv_exp_f32 v16, v16\nv_exp_f32 v17, v17\nv_exp_f32 v18, v18\nv_exp_f32 v19, v19\n" \
    "v_exp_f32 v24, v24\nv_pk_add_f32 v[254:255], v[16:17], v[18:19]\nv_exp_f32 v25, v25\nv_add_f32 v254, v254, v255\nv_exp_f32 v26, v26\nv_exp_f32 v27, v27\n" \
    "v_add_f32_dpp v252, v254, v254 row_ror:8 row_mask:0xf bank_mask:0xf\nv_add_f32_dpp v253, v254, v254 row_ror:8 row_mask:0xf bank_mask:0xf\n" \
    "v_pk_add_f32 v[254:255], v[24:25], v[26:27]\ns_nop 0\nv_permlane32_swap_b32 v252, v253\nv_pk_add_f32 v[254:255], v[254:255], v[252:253]\n" \
    "v_pk_add_f32 v[252:253], v[254:255], v[254:255] op_sel:[0,1] op_sel_hi:[1,0]\ns_nop 1\nv_permlane16_swap_b32 v252, v253\nv_add_f32 v252, v252, v253\n" \
    "v_cmp_u_f32 vcc, v252, v252\nv_rcp_f32 v252, v252\ns_cbranch_vccnz .Lq12_%=\nv_pk_mul_f32 v[16:17], v[16:17], v[252:253] op_sel_hi:[1,0]\n" \
    "v_pk_mul_f32 v[24:25], v[24:25], v[252:253] op_sel_hi:[1,0]\nv_cvt_pk_bf16_f32 v20, v16, v17\nv_cvt_pk_bf16_f32 v22, v24, v25\n" \
    "v_pk_mul_f32 v[18:19], v[18:19], v[252:253] op_sel_hi:[1,0]\nv_pk_mul_f32 v[26:27], v[26:27], v[252:253] op_sel_hi:[1,0]\nv_cvt_pk_bf16_f32 v21, v18, v19\n" \
    "v_cvt_pk_bf16_f32 v23, v26, v27\nv_permlane32_swap_b32 v20, v22\ns_nop 0\nv_permlane32_swap_b32 v21, v23\nbuffer_store_dwordx4 v[20:23], v7, s[64:67], s82 offen sc1\n" \
    "buffer_store_dwordx4 v[20:23], v7, s[64:67], s74 offen sc1\ns_branch .Lq3_%=\n.Lq5_%=:\n.Lq32_%=:\ns_memrealtime s[72:73]\ns_waitcnt lgkmcnt(0)\ns_mov_b32 s68, 0\n" \
    "s_sub_u32 s76, s80, s45\ns_sub_u32 s69, s72, s76\ns_cmp_lt_i32 s69, -8\ns_cbranch_scc1 .Lq35_%=\ns_xor_b32 s90, s81, 0x800\n.Lq37_%=:\ns_memrealtime s[72:73]\n" \
    "s_waitcnt lgkmcnt(0)\ns_sub_u32 s72, s72, s80\ns_cmp_lt_i32 s72, 0\ns_cbranch_scc1 .Lq37_%=\nbuffer_load_dwordx4 v[72:75], v2, s[64:67], s81 offen offset:0 sc1\n" \
    "v_cvt_pk_bf16_f32 v24, v32, v33\nv_cvt_pk_bf16_f32 v25, v34, v35\nv_cvt_pk_bf16_f32 v26, v40, v41\nbuffer_load_dwordx4 v[76:79], v2, s[64:67], s81 offen offset:1024 sc1\n" \
    "v_cvt_pk_bf16_f32 v27, v42, v43\nds_write_b128 v14, v[24:27] offset:8192\nv_cvt_pk_bf16_f32 v28, v36, v37\n" \
    "buffer_load_dwordx4 v[80:83], v2, s[64:67], s90 offen offset:0 sc1\nv_cvt_pk_bf16_f32 v29, v38, v39\nv_cvt_pk_bf16_f32 v30, v44, v45\nv_cvt_pk_bf16_f32 v31, v46, v47\n" \
    "buffer_load_dwordx4 v[84:87], v2, s[64:67], s90 offen offset:1024 sc1\nds_write_b128 v14, v[28:31] offset:12288\nds_read_b128 v[32:35], v3 offset:2048\n" \
    "ds_read_b128 v[36:39], v3 offset:3072\nds_read_b128 v[40:43], v3 offset:10240\nds_read_b128 v[44:47], v3 offset:11264\nds_read_b128 v[48:51], v3 offset:18432\n" \
    "ds_read_b128 v[52:55], v3 offset:19456\nds_read_b128 v[56:59], v3 offset:0\nds_read_b128 v[60:63], v3 offset:1024\nds_read_b128 v[64:67], v3 offset:4096\n" \
    "ds_read_b128 v[68:71], v3 offset:5120\n.Lq36_%=:\nv_mov_b64 v[16:17], 0\nv_mov_b64 v[18:19], 0\nv_mov_b64 v[20:21], 0\nv_mov_b64 v[22:23], 0\nv_mov_b64 v[24:25], 0\n" \
    "v_mov_b64 v[26:27], 0\nv_mov_b64 v[28:29], 0\nv_mov_b64 v[30:31], 0\ns_waitcnt vmcnt(3) lgkmcnt(8)\nv_smfmac_f32_16x16x64_bf16 v[20:23], v[72:75], v[32:39], v0\n" \
    "ds_write_b128 v1, v[72:75] offset:25600\nv_max3_u32 v252, v72, v73, v74\nv_smfmac_f32_16x16x64_bf16 v[16:19], v[72:75], a[24:31], v0\n" \
    "v_smfmac_f32_16x16x64_bf16 v[24:27], v[72:75], a[32:39], v0\nv_smfmac_f32_16x16x64_bf16 v[28:31], v[72:75], a[40:47], v0\ns_waitcnt vmcnt(2)\n" \
    "ds_write_b128 v1, v[76:79] offset:29696\ns_waitcnt lgkmcnt(8)\nv_max3_u32 v252, v252, v75, v76\nv_smfmac_f32_16x16x64_bf16 v[20:23], v[76:79], v[40:47], v0\n" \
    "v_max3_u32 v252, v252, v77, v78\nv_smfmac_f32_16x16x64_bf16 v[16:19], v[76:79], a[120:127], v0\nv_cmp_u_f32 vcc, v252, v79\n" \
    "v_smfmac_f32_16x16x64_bf16 v[24:27], v[76:79], a[128:135], v0\ns_cbranch_vccnz .Lq43_%=\n.Lq39_%=:\nv_smfmac_f32_16x16x64_bf16 v[28:31], v[76:79], a[136:143], v0\n" \
    "ds_read_b128 v[36:39], v3 offset:7168\ns_waitcnt vmcnt(1)\nv_max3_u32 v252, v80, v81, v82\nv_cmp_u_f32 vcc, v252, v83\ns_nop 0\ns_cbranch_vccnz .Lq44_%=\n.Lq40_%=:\n" \
    "s_waitcnt lgkmcnt(7)\nv_smfmac_f32_16x16x64_bf16 v[20:23], v[80:83], v[48:55], v0\nds_write_b128 v1, v[80:83] offset:33792\n" \
    "v_smfmac_f32_16x16x64_bf16 v[16:19], v[80:83], a[216:223], v0\nds_read_b128 v[32:35], v3 offset:6144\ns_waitcnt lgkmcnt(3)\ns_barrier\n" \
    "ds_read_b128 v[88:91], v1 offset:24576\nv_smfmac_f32_16x16x64_bf16 v[24:27], v[80:83], a[224:231], v0\nds_read_b128 v[40:43], v3 offset:8192\n" \
    "v_smfmac_f32_16x16x64_bf16 v[28:31], v[80:83], a[232:239], v0\nds_read_b128 v[48:51], v1 offset:26624\ns_waitcnt vmcnt(0)\nv_max3_u32 v252, v84, v85, v86\n" \
    "v_cmp_u_f32 vcc, v252, v87\ns_nop 0\ns_cbranch_vccnz .Lq45_%=\n.Lq41_%=:\nv_smfmac_f32_16x16x64_bf16 v[20:23], v[84:87], v[164:171], v0\n" \
    "ds_write_b128 v1, v[84:87] offset:37888\nv_smfmac_f32_16x16x64_bf16 v[16:19], v[84:87], v[156:163], v0\nds_read_b128 v[52:55], v1 offset:27648\n" \
    "v_smfmac_f32_16x16x64_bf16 v[24:27], v[84:87], v[172:179], v0\nds_read_b128 v[72:75], v1 offset:28672\nv_smfmac_f32_16x16x64_bf16 v[28:31], v[84:87], v[180:187], v0\n" \
    "ds_read_b128 v[44:47], v3 offset:9216\ns_waitcnt lgkmcnt(6)\nv_smfmac_f32_16x16x64_bf16 v[20:23], v[88:91], v[56:63], v0\nds_read_b128 v[56:59], v1 offset:30720\n" \
    "v_smfmac_f32_16x16x64_bf16 v[16:19], v[88:91], a[0:7], v0\nds_read_b128 v[76:79], v3 offset:12288\nv_smfmac_f32_16x16x64_bf16 v[24:27], v[88:91], a[8:15], v0\n" \
    "ds_read_b128 v[80:83], v3 offset:13312\nv_smfmac_f32_16x16x64_bf16 v[28:31], v[88:91], a[16:23], v0\nds_read_b128 v[60:63], v1 offset:31744\ns_waitcnt lgkmcnt(7)\n" \
    "s_barrier\nv_smfmac_f32_16x16x64_bf16 v[20:23], v[48:51], v[64:71], v0\nds_read_b128 v[64:67], v3 offset:14336\n" \
    "v_smfmac_f32_16x16x64_bf16 v[16:19], v[48:51], a[48:55], v0\nds_read_b128 v[68:71], v3 offset:15360\ns_waitcnt lgkmcnt(8)\n" \
    "v_smfmac_f32_16x16x64_bf16 v[20:23], v[52:55], v[32:39], v0\nds_read_b128 v[32:35], v1 offset:32768\nv_smfmac_f32_16x16x64_bf16 v[16:19], v[52:55], a[72:79], v0\n" \
    "ds_read_b128 v[84:87], v3 offset:16384\ns_waitcnt lgkmcnt(8)\nv_smfmac_f32_16x16x64_bf16 v[20:23], v[72:75], v[40:47], v0\nds_read_b128 v[88:91], v3 offset:17408\n" \
    "v_smfmac_f32_16x16x64_bf16 v[16:19], v[72:75], a[96:103], v0\nds_read_b128 v[36:39], v1 offset:34816\ns_waitcnt lgkmcnt(7)\n" \
    "v_smfmac_f32_16x16x64_bf16 v[20:23], v[56:59], v[76:83], v0\nds_read_b128 v[40:43], v3 offset:20480\nv_smfmac_f32_16x16x64_bf16 v[16:19], v[56:59], a[144:151], v0\n" \
    "ds_read_b128 v[44:47], v3 offset:21504\ns_waitcnt lgkmcnt(6)\nv_smfmac_f32_16x16x64_bf16 v[20:23], v[60:63], v[64:71], v0\nds_read_b128 v[64:67], v1 offset:35840\n" \
    "v_smfmac_f32_16x16x64_bf16 v[16:19], v[60:63], a[168:175], v0\nds_read_b128 v[76:79], v3 offset:22528\ns_waitcnt lgkmcnt(5)\n" \
    "v_smfmac_f32_16x16x64_bf16 v[20:23], v[32:35], v[84:91], v0\nds_read_b128 v[80:83], v3 offset:23552\nv_smfmac_f32_16x16x64_bf16 v[16:19], v[32:35], a[192:199], v0\n" \
    "ds_read_b128 v[68:71], v1 offset:36864\ns_waitcnt lgkmcnt(4)\nv_smfmac_f32_16x16x64_bf16 v[20:23], v[36:39], v[40:47], v0\nds_read_b128 v[40:43], v1 offset:38912\n" \
    "v_smfmac_f32_16x16x64_bf16 v[16:19], v[36:39], a[240:247], v0\nds_read_b128 v[44:47], v1 offset:39936\ns_waitcnt lgkmcnt(3)\n" \
    "v_smfmac_f32_16x16x64_bf16 v[20:23], v[64:67], v[76:83], v0\nv_smfmac_f32_16x16x64_bf16 v[16:19], v[64:67], v[100:107], v0\ns_waitcnt lgkmcnt(2)\n" \
    "v_smfmac_f32_16x16x64_bf16 v[20:23], v[68:71], v[132:139], v0\ns_waitcnt lgkmcnt(1)\nv_smfmac_f32_16x16x64_bf16 v[20:23], v[40:43], v[196:203], v0\ns_waitcnt lgkmcnt(0)\n" \
    "v_smfmac_f32_16x16x64_bf16 v[20:23], v[44:47], v[228:235], v0\ns_nop 2\nv_smfmac_f32_16x16x64_bf16 v[16:19], v[68:71], v[124:131], v0\ns_nop 2\n" \
    "v_smfmac_f32_16x16x64_bf16 v[16:19], v[40:43], v[188:195], v0\nv_permlane32_swap_b32 v20, v22\nv_smfmac_f32_16x16x64_bf16 v[16:19], v[44:47], v[220:227], v0\ns_nop 1\n" \
    "v_permlane32_swap_b32 v21, v23\nv_smfmac_f32_16x16x64_bf16 v[24:27], v[48:51], a[56:63], v0\ns_nop 1\nv_add_f32 v20, v20, v22\n" \
    "v_smfmac_f32_16x16x64_bf16 v[28:31], v[48:51], a[64:71], v0\nv_add_f32 v21, v21, v23\nv_smfmac_f32_16x16x64_bf16 v[24:27], v[52:55], a[80:87], v0\nv_mul_f32 v20, s32, v20\n" \
    "v_smfmac_f32_16x16x64_bf16 v[28:31], v[52:55], a[88:95], v0\nv_mul_f32 v21, s32, v21\nv_smfmac_f32_16x16x64_bf16 v[24:27], v[72:75], a[104:111], v0\n" \
    "v_cvt_pk_bf16_f32 v20, v20, v21\nv_smfmac_f32_16x16x64_bf16 v[28:31], v[72:75], a[112:119], v0\nds_write_b16 v4, v20 offset:8\nv_permlane32_swap_b32 v16, v18\n" \
    "v_smfmac_f32_16x16x64_bf16 v[24:27], v[56:59], a[152:159], v0\nds_write_b16_d16_hi v4, v20 offset:24\nv_permlane32_swap_b32 v17, v19\n" \
    "v_smfmac_f32_16x16x64_bf16 v[24:27], v[60:63], a[176:183], v0\nds_read_b128 v[48:51], v1 offset:40960\nv_add_f32 v16, v16, v18\n" \
    "v_smfmac_f32_16x16x64_bf16 v[24:27], v[32:35], a[200:207], v0\nds_read_b128 v[52:55], v1 offset:45056\nv_add_f32 v17, v17, v19\n" \
    "v_smfmac_f32_16x16x64_bf16 v[24:27], v[36:39], a[248:255], v0\nds_read_b128 v[72:75], v1 offset:41984\nv_mul_f32 v16, s32, v16\n" \
    "v_smfmac_f32_16x16x64_bf16 v[24:27], v[64:67], v[108:115], v0\nds_read_b128 v[76:79], v1 offset:46080\nv_mul_f32 v17, s32, v17\n" \
    "v_smfmac_f32_16x16x64_bf16 v[24:27], v[68:71], v[140:147], v0\nds_read_b128 v[80:83], v1 offset:43008\nv_cvt_pk_bf16_f32 v16, v16, v17\n" \
    "v_smfmac_f32_16x16x64_bf16 v[24:27], v[40:43], v[204:211], v0\nds_write_b16 v4, v16 offset:0\nv_smfmac_f32_16x16x64_bf16 v[24:27], v[44:47], v[236:243], v0\n" \
    "ds_write_b16_d16_hi v4, v16 offset:16\ns_nop 1\nv_smfmac_f32_16x16x64_bf16 v[28:31], v[56:59], a[160:167], v0\nds_read_b128 v[56:59], v1 offset:47104\ns_nop 1\n" \
    "v_smfmac_f32_16x16x64_bf16 v[28:31], v[60:63], a[184:191], v0\nds_read_b128 v[60:63], v1 offset:44032\nv_permlane32_swap_b32 v24, v26\n" \
    "v_smfmac_f32_16x16x64_bf16 v[28:31], v[32:35], a[208:215], v0\nds_read_b128 v[32:35], v1 offset:48128\nv_permlane32_swap_b32 v25, v27\n" \
    "v_smfmac_f32_16x16x64_bf16 v[28:31], v[36:39], v[92:99], v0\nds_read_b128 v[36:39], v6 offset:0\nv_add_f32 v24, v24, v26\n" \
    "v_smfmac_f32_16x16x64_bf16 v[28:31], v[64:67], v[116:123], v0\nds_read_b128 v[64:67], v6 offset:512\nv_add_f32 v25, v25, v27\n" \
    "v_smfmac_f32_16x16x64_bf16 v[28:31], v[68:71], v[148:155], v0\nds_read_b128 v[68:71], v6 offset:1024\nv_cvt_pk_bf16_f32 v24, v24, v25\n" \
    "v_smfmac_f32_16x16x64_bf16 v[28:31], v[40:43], v[212:219], v0\nds_read_b128 v[40:43], v6 offset:1536\nv_smfmac_f32_16x16x64_bf16 v[28:31], v[44:47], v[244:251], v0\n" \
    "ds_write_b16 v4, v24 offset:2048\nds_write_b16_d16_hi v4, v24 offset:2064\ns_waitcnt lgkmcnt(5)\nv_mfma_f32_16x16x32_bf16 v[16:19], v[48:51], v[36:39], 0\ns_nop 2\n" \
    "v_mfma_f32_16x16x32_bf16 v[20:23], v[52:55], v[36:39], 0\nv_permlane32_swap_b32 v28, v30\nv_permlane32_swap_b32 v29, v31\nv_add_f32 v28, v28, v30\nv_add_f32 v29, v29, v31\n" \
    "v_cvt_pk_bf16_f32 v28, v28, v29\nds_write_b16 v4, v28 offset:2056\nds_write_b16_d16_hi v4, v28 offset:2072\ns_waitcnt lgkmcnt(6)\n" \
    "v_mfma_f32_16x16x32_bf16 v[16:19], v[72:75], v[64:67], v[16:19]\nv_mfma_f32_16x16x32_bf16 v[20:23], v[76:79], v[64:67], v[20:23]\ns_waitcnt lgkmcnt(5)\n" \
    "v_mfma_f32_16x16x32_bf16 v[16:19], v[80:83], v[68:71], v[16:19]\nds_read_b128 v[44:47], v8 offset:0\nds_read_b128 v[84:87], v8 offset:512\n" \
    "v_mfma_f32_16x16x32_bf16 v[20:23], v[56:59], v[68:71], v[20:23]\nds_read_b128 v[88:91], v8 offset:1024\nds_read_b128 v[28:31], v8 offset:1536\ns_waitcnt lgkmcnt(8)\n" \
    "v_mfma_f32_16x16x32_bf16 v[16:19], v[60:63], v[40:43], v[16:19]\nv_mfma_f32_16x16x32_bf16 v[20:23], v[32:35], v[40:43], v[20:23]\nv_mov_b32 v15, v15\nv_mov_b32 v15, v15\n" \
    "s_waitcnt lgkmcnt(3)\nv_mfma_f32_16x16x32_bf16 v[24:27], v[44:47], v[36:39], 0\ns_nop 2\ns_waitcnt lgkmcnt(2)\n" \
    "v_mov_b32_dpp v16, v20 quad_perm:[0,1,2,3] row_mask:0xf bank_mask:0xc\nv_mov_b32_dpp v17, v21 quad_perm:[0,1,2,3] row_mask:0xf bank_mask:0xc\n" \
    "v_mfma_f32_16x16x32_bf16 v[24:27], v[84:87], v[64:67], v[24:27]\ns_waitcnt lgkmcnt(1)\nv_mov_b32_dpp v18, v22 quad_perm:[0,1,2,3] row_mask:0xf bank_mask:0xc\n" \
    "v_mov_b32_dpp v19, v23 quad_perm:[0,1,2,3] row_mask:0xf bank_mask:0xc\nv_mfma_f32_16x16x32_bf16 v[24:27], v[88:91], v[68:71], v[24:27]\ns_waitcnt lgkmcnt(0)\n" \
    "v_max3_f32 v254, v16, v17, v18\nv_max_f32 v254, v254, v19\nv_mfma_f32_16x16x32_bf16 v[24:27], v[28:31], v[40:43], v[24:27]\ns_nop 0\n" \
    "v_max_f32_dpp v252, v254, v254 row_ror:8 row_mask:0xf bank_mask:0xf\nv_max_f32_dpp v253, v254, v254 row_ror:8 row_mask:0xf bank_mask:0xf\ns_nop 1\n" \
    "v_permlane32_swap_b32 v252, v253\nv_max_f32 v252, v252, v253\ns_xor_b32 s74, s82, 0x4000\nv_max3_f32 v254, v252, v24, v25\nv_max3_f32 v252, v254, v26, v27\n" \
    "v_max3_f32 v253, v254, v26, v27\ns_nop 1\nv_permlane16_swap_b32 v252, v253\nv_max_f32 v252, v252, v253\n" \
    "v_pk_add_f32 v[16:17], v[16:17], v[252:253] op_sel_hi:[1,0] neg_lo:[0,1] neg_hi:[0,1]\n" \
    "v_pk_add_f32 v[18:19], v[18:19], v[252:253] op_sel_hi:[1,0] neg_lo:[0,1] neg_hi:[0,1]\n" \
    "v_pk_add_f32 v[24:25], v[24:25], v[252:253] op_sel_hi:[1,0] neg_lo:[0,1] neg_hi:[0,1]\n" \
    "v_pk_add_f32 v[26:27], v[26:27], v[252:253] op_sel_hi:[1,0] neg_lo:[0,1] neg_hi:[0,1]\nv_exp_f32 v16, v16\nv_exp_f32 v17, v17\nv_exp_f32 v18, v18\nv_exp_f32 v19, v19\n" \
    "v_exp_f32 v24, v24\nv_pk_add_f32 v[254:255], v[16:17], v[18:19]\nv_exp_f32 v25, v25\nv_add_f32 v254, v254, v255\nv_exp_f32 v26, v26\nv_exp_f32 v27, v27\n" \
    "v_add_f32_dpp v252, v254, v254 row_ror:8 row_mask:0xf bank_mask:0xf\nv_add_f32_dpp v253, v254, v254 row_ror:8 row_mask:0xf bank_mask:0xf\n" \
    "v_pk_add_f32 v[254:255], v[24:25], v[26:27]\ns_nop 0\nv_permlane32_swap_b32 v252, v253\nv_pk_add_f32 v[254:255], v[254:255], v[252:253]\n" \
    "v_pk_add_f32 v[252:253], v[254:255], v[254:255] op_sel:[0,1] op_sel_hi:[1,0]\ns_nop 1\nv_permlane16_swap_b32 v252, v253\nv_add_f32 v252, v252, v253\n" \
    "v_cmp_u_f32 vcc, v252, v252\nv_rcp_f32 v252, v252\ns_cbranch_vccnz .Lq12_%=\nv_pk_mul_f32 v[16:17], v[16:17], v[252:253] op_sel_hi:[1,0]\n" \
    "v_pk_mul_f32 v[24:25], v[24:25], v[252:253] op_sel_hi:[1,0]\nv_cvt_pk_bf16_f32 v20, v16, v17\nv_cvt_pk_bf16_f32 v22, v24, v25\n" \
    "v_pk_mul_f32 v[18:19], v[18:19], v[252:253] op_sel_hi:[1,0]\nv_pk_mul_f32 v[26:27], v[26:27], v[252:253] op_sel_hi:[1,0]\nv_cvt_pk_bf16_f32 v21, v18, v19\n" \
    "v_cvt_pk_bf16_f32 v23, v26, v27\nv_permlane32_swap_b32 v20, v22\ns_nop 0\nv_permlane32_swap_b32 v21, v23\nbuffer_store_dwordx4 v[20:23], v7, s[64:67], s82 offen sc1\n" \
    "buffer_store_dwordx4 v[20:23], v7, s[64:67], s74 offen sc1\ns_branch .Lq3_%=\n.Lq6_%=:\n.Lq51_%=:\ns_memrealtime s[72:73]\ns_waitcnt lgkmcnt(0)\ns_mov_b32 s68, 0\n" \
    "s_sub_u32 s76, s80, s45\ns_sub_u32 s69, s72, s76\ns_cmp_lt_i32 s69, -8\ns_cbranch_scc1 .Lq54_%=\ns_xor_b32 s90, s81, 0x800\n.Lq56_%=:\ns_memrealtime s[72:73]\n" \
    "s_waitcnt lgkmcnt(0)\ns_sub_u32 s72, s72, s80\ns_cmp_lt_i32 s72, 0\ns_cbranch_scc1 .Lq56_%=\nbuffer_load_dwordx4 v[72:75], v2, s[64:67], s81 offen offset:0 sc1\n" \
    "v_cvt_pk_bf16_f32 v24, v32, v33\nv_cvt_pk_bf16_f32 v25, v34, v35\nv_cvt_pk_bf16_f32 v26, v40, v41\nbuffer_load_dwordx4 v[76:79], v2, s[64:67], s81 offen offset:1024 sc1\n" \
    "v_cvt_pk_bf16_f32 v27, v42, v43\nds_write_b128 v14, v[24:27] offset:8192\nv_cvt_pk_bf16_f32 v28, v36, v37\n" \
    "buffer_load_dwordx4 v[80:83], v2, s[64:67], s90 offen offset:0 sc1\nv_cvt_pk_bf16_f32 v29, v38, v39\nv_cvt_pk_bf16_f32 v30, v44, v45\nv_cvt_pk_bf16_f32 v31, v46, v47\n" \
    "buffer_load_dwordx4 v[84:87], v2, s[64:67], s90 offen offset:1024 sc1\nds_write_b128 v14, v[28:31] offset:12288\nds_read_b128 v[32:35], v3 offset:4096\n" \
    "ds_read_b128 v[36:39], v3 offset:5120\nds_read_b128 v[40:43], v3 offset:12288\nds_read_b128 v[44:47], v3 offset:13312\nds_read_b128 v[48:51], v3 offset:20480\n" \
    "ds_read_b128 v[52:55], v3 offset:21504\nds_read_b128 v[56:59], v3 offset:0\nds_read_b128 v[60:63], v3 offset:1024\nds_read_b128 v[64:67], v3 offset:2048\n" \
    "ds_read_b128 v[68:71], v3 offset:3072\n.Lq55_%=:\nv_mov_b64 v[16:17], 0\nv_mov_b64 v[18:19], 0\nv_mov_b64 v[20:21], 0\nv_mov_b64 v[22:23], 0\nv_mov_b64 v[24:25], 0\n" \
    "v_mov_b64 v[26:27], 0\nv_mov_b64 v[28:29], 0\nv_mov_b64 v[30:31], 0\ns_waitcnt vmcnt(3) lgkmcnt(8)\nv_smfmac_f32_16x16x64_bf16 v[20:23], v[72:75], v[32:39], v0\n" \
    "ds_write_b128 v1, v[72:75] offset:26624\nv_max3_u32 v252, v72, v73, v74\nv_smfmac_f32_16x16x64_bf16 v[16:19], v[72:75], a[48:55], v0\n" \
    "v_smfmac_f32_16x16x64_bf16 v[24:27], v[72:75], a[56:63], v0\nv_smfmac_f32_16x16x64_bf16 v[28:31], v[72:75], a[64:71], v0\ns_waitcnt vmcnt(2)\n" \
    "ds_write_b128 v1, v[76:79] offset:30720\ns_waitcnt lgkmcnt(8)\nv_max3_u32 v252, v252, v75, v76\nv_smfmac_f32_16x16x64_bf16 v[20:23], v[76:79], v[40:47], v0\n" \
    "v_max3_u32 v252, v252, v77, v78\nv_smfmac_f32_16x16x64_bf16 v[16:19], v[76:79], a[144:151], v0\nv_cmp_u_f32 vcc, v252, v79\n" \
    "v_smfmac_f32_16x16x64_bf16 v[24:27], v[76:79], a[152:159], v0\ns_cbranch_vccnz .Lq62_%=\n.Lq58_%=:\nv_smfmac_f32_16x16x64_bf16 v[28:31], v[76:79], a[160:167], v0\n" \
    "ds_read_b128 v[36:39], v3 offset:7168\ns_waitcnt vmcnt(1)\nv_max3_u32 v252, v80, v81, v82\nv_cmp_u_f32 vcc, v252, v83\ns_nop 0\ns_cbranch_vccnz .Lq63_%=\n.Lq59_%=:\n" \
    "s_waitcnt lgkmcnt(7)\nv_smfmac_f32_16x16x64_bf16 v[20:23], v[80:83], v[48:55], v0\nds_write_b128 v1, v[80:83] offset:34816\n" \
    "v_smfmac_f32_16x16x64_bf16 v[16:19], v[80:83], a[240:247], v0\nds_read_b128 v[32:35], v3 offset:6144\ns_waitcnt lgkmcnt(3)\ns_barrier\n" \
    "ds_read_b128 v[88:91], v1 offset:24576\nv_smfmac_f32_16x16x64_bf16 v[24:27], v[80:83], a[248:255], v0\nds_read_b128 v[40:43], v3 offset:8192\n" \
    "v_smfmac_f32_16x16x64_bf16 v[28:31], v[80:83], v[92:99], v0\nds_read_b128 v[48:51], v1 offset:25600\ns_waitcnt vmcnt(0)\nv_max3_u32 v252, v84, v85, v86\n" \
    "v_cmp_u_f32 vcc, v252, v87\ns_nop 0\ns_cbranch_vccnz .Lq64_%=\n.Lq60_%=:\nv_smfmac_f32_16x16x64_bf16 v[20:23], v[84:87], v[196:203], v0\n" \
    "ds_write_b128 v1, v[84:87] offset:38912\nv_smfmac_f32_16x16x64_bf16 v[16:19], v[84:87], v[188:195], v0\nds_read_b128 v[52:55], v1 offset:27648\n" \
    "v_smfmac_f32_16x16x64_bf16 v[24:27], v[84:87], v[204:211], v0\nds_read_b128 v[72:75], v1 offset:28672\nv_smfmac_f32_16x16x64_bf16 v[28:31], v[84:87], v[212:219], v0\n" \
    "ds_read_b128 v[44:47], v3 offset:9216\ns_waitcnt lgkmcnt(6)\nv_smfmac_f32_16x16x64_bf16 v[20:23], v[88:91], v[56:63], v0\nds_read_b128 v[56:59], v1 offset:29696\n" \
    "v_smfmac_f32_16x16x64_bf16 v[16:19], v[88:91], a[0:7], v0\nds_read_b128 v[76:79], v3 offset:10240\nv_smfmac_f32_16x16x64_bf16 v[24:27], v[88:91], a[8:15], v0\n" \
    "ds_read_b128 v[80:83], v3 offset:11264\nv_smfmac_f32_16x16x64_bf16 v[28:31], v[88:91], a[16:23], v0\nds_read_b128 v[60:63], v1 offset:31744\ns_waitcnt lgkmcnt(7)\n" \
    "s_barrier\nv_smfmac_f32_16x16x64_bf16 v[20:23], v[48:51], v[64:71], v0\nds_read_b128 v[64:67], v3 offset:14336\n" \
    "v_smfmac_f32_16x16x64_bf16 v[16:19], v[48:51], a[24:31], v0\nds_read_b128 v[68:71], v3 offset:15360\ns_waitcnt lgkmcnt(8)\n" \
    "v_smfmac_f32_16x16x64_bf16 v[20:23], v[52:55], v[32:39], v0\nds_read_b128 v[32:35], v1 offset:32768\nv_smfmac_f32_16x16x64_bf16 v[16:19], v[52:55], a[72:79], v0\n" \
    "ds_read_b128 v[84:87], v3 offset:16384\ns_waitcnt lgkmcnt(8)\nv_smfmac_f32_16x16x64_bf16 v[20:23], v[72:75], v[40:47], v0\nds_read_b128 v[88:91], v3 offset:17408\n" \
    "v_smfmac_f32_16x16x64_bf16 v[16:19], v[72:75], a[96:103], v0\nds_read_b128 v[36:39], v1 offset:33792\ns_waitcnt lgkmcnt(7)\n" \
    "v_smfmac_f32_16x16x64_bf16 v[20:23], v[56:59], v[76:83], v0\nds_read_b128 v[40:43], v3 offset:18432\nv_smfmac_f32_16x16x64_bf16 v[16:19], v[56:59], a[120:127], v0\n" \
    "ds_read_b128 v[44:47], v3 offset:19456\ns_waitcnt lgkmcnt(6)\nv_smfmac_f32_16x16x64_bf16 v[20:23], v[60:63], v[64:71], v0\nds_read_b128 v[64:67], v1 offset:35840\n" \
    "v_smfmac_f32_16x16x64_bf16 v[16:19], v[60:63], a[168:175], v0\nds_read_b128 v[76:79], v3 offset:22528\ns_waitcnt lgkmcnt(5)\n" \
    "v_smfmac_f32_16x16x64_bf16 v[20:23], v[32:35], v[84:91], v0\nds_read_b128 v[80:83], v3 offset:23552\nv_smfmac_f32_16x16x64_bf16 v[16:19], v[32:35], a[192:199], v0\n" \
    "ds_read_b128 v[68:71], v1 offset:36864\ns_waitcnt lgkmcnt(4)\nv_smfmac_f32_16x16x64_bf16 v[20:23], v[36:39], v[40:47], v0\nds_read_b128 v[40:43], v1 offset:37888\n" \
    "v_smfmac_f32_16x16x64_bf16 v[16:19], v[36:39], a[216:223], v0\nds_read_b128 v[44:47], v1 offset:39936\ns_waitcnt lgkmcnt(3)\n" \
    "v_smfmac_f32_16x16x64_bf16 v[20:23], v[64:67], v[76:83], v0\nv_smfmac_f32_16x16x64_bf16 v[16:19], v[64:67], v[100:107], v0\ns_waitcnt lgkmcnt(2)\n" \
    "v_smfmac_f32_16x16x64_bf16 v[20:23], v[68:71], v[132:139], v0\ns_waitcnt lgkmcnt(1)\nv_smfmac_f32_16x16x64_bf16 v[20:23], v[40:43], v[164:171], v0\ns_waitcnt lgkmcnt(0)\n" \
    "v_smfmac_f32_16x16x64_bf16 v[20:23], v[44:47], v[228:235], v0\ns_nop 2\nv_smfmac_f32_16x16x64_bf16 v[16:19], v[68:71], v[124:131], v0\ns_nop 2\n" \
    "v_smfmac_f32_16x16x64_bf16 v[16:19], v[40:43], v[156:163], v0\nv_permlane32_swap_b32 v20, v22\nv_smfmac_f32_16x16x64_bf16 v[16:19], v[44:47], v[220:227], v0\ns_nop 1\n" \
    "v_permlane32_swap_b32 v21, v23\nv_smfmac_f32_16x16x64_bf16 v[24:27], v[48:51], a[32:39], v0\ns_nop 1\nv_add_f32 v20, v20, v22\n" \
    "v_smfmac_f32_16x16x64_bf16 v[28:31], v[48:51], a[40:47], v0\nv_add_f32 v21, v21, v23\nv_smfmac_f32_16x16x64_bf16 v[24:27], v[52:55], a[80:87], v0\nv_mul_f32 v20, s32, v20\n" \
    "v_smfmac_f32_16x16x64_bf16 v[28:31], v[52:55], a[88:95], v0\nv_mul_f32 v21, s32, v21\nv_smfmac_f32_16x16x64_bf16 v[24:27], v[72:75], a[104:111], v0\n" \
    "v_cvt_pk_bf16_f32 v20, v20, v21\nv_smfmac_f32_16x16x64_bf16 v[28:31], v[72:75], a[112:119], v0\nds_write_b16 v4, v20 offset:8\nv_permlane32_swap_b32 v16, v18\n" \
    "v_smfmac_f32_16x16x64_bf16 v[24:27], v[56:59], a[128:135], v0\nds_write_b16_d16_hi v4, v20 offset:24\nv_permlane32_swap_b32 v17, v19\n" \
    "v_smfmac_f32_16x16x64_bf16 v[24:27], v[60:63], a[176:183], v0\nds_read_b128 v[48:51], v1 offset:40960\nv_add_f32 v16, v16, v18\n" \
    "v_smfmac_f32_16x16x64_bf16 v[24:27], v[32:35], a[200:207], v0\nds_read_b128 v[52:55], v1 offset:45056\nv_add_f32 v17, v17, v19\n" \
    "v_smfmac_f32_16x16x64_bf16 v[24:27], v[36:39], a[224:231], v0\nds_read_b128 v[72:75], v1 offset:41984\nv_mul_f32 v16, s32, v16\n" \
    "v_smfmac_f32_16x16x64_bf16 v[24:27], v[64:67], v[108:115], v0\nds_read_b128 v[76:79], v1 offset:46080\nv_mul_f32 v17, s32, v17\n" \
    "v_smfmac_f32_16x16x64_bf16 v[24:27], v[68:71], v[140:147], v0\nds_read_b128 v[80:83], v1 offset:43008\nv_cvt_pk_bf16_f32 v16, v16, v17\n" \
    "v_smfmac_f32_16x16x64_bf16 v[24:27], v[40:43], v[172:179], v0\nds_write_b16 v4, v16 offset:0\nv_smfmac_f32_16x16x64_bf16 v[24:27], v[44:47], v[236:243], v0\n" \
    "ds_write_b16_d16_hi v4, v16 offset:16\ns_nop 1\nv_smfmac_f32_16x16x64_bf16 v[28:31], v[56:59], a[136:143], v0\nds_read_b128 v[56:59], v1 offset:47104\ns_nop 1\n" \
    "v_smfmac_f32_16x16x64_bf16 v[28:31], v[60:63], a[184:191], v0\nds_read_b128 v[60:63], v1 offset:44032\nv_permlane32_swap_b32 v24, v26\n" \
    "v_smfmac_f32_16x16x64_bf16 v[28:31], v[32:35], a[208:215], v0\nds_read_b128 v[32:35], v1 offset:48128\nv_permlane32_swap_b32 v25, v27\n" \
    "v_smfmac_f32_16x16x64_bf16 v[28:31], v[36:39], a[232:239], v0\nds_read_b128 v[36:39], v6 offset:0\nv_add_f32 v24, v24, v26\n" \
    "v_smfmac_f32_16x16x64_bf16 v[28:31], v[64:67], v[116:123], v0\nds_read_b128 v[64:67], v6 offset:512\nv_add_f32 v25, v25, v27\n" \
    "v_smfmac_f32_16x16x64_bf16 v[28:31], v[68:71], v[148:155], v0\nds_read_b128 v[68:71], v6 offset:1024\nv_cvt_pk_bf16_f32 v24, v24, v25\n" \
    "v_smfmac_f32_16x16x64_bf16 v[28:31], v[40:43], v[180:187], v0\nds_read_b128 v[40:43], v6 offset:1536\nv_smfmac_f32_16x16x64_bf16 v[28:31], v[44:47], v[244:251], v0\n" \
    "ds_write_b16 v4, v24 offset:2048\nds_write_b16_d16_hi v4, v24 offset:2064\ns_waitcnt lgkmcnt(5)\nv_mfma_f32_16x16x32_bf16 v[16:19], v[48:51], v[36:39], 0\ns_nop 2\n" \
    "v_mfma_f32_16x16x32_bf16 v[20:23], v[52:55], v[36:39], 0\nv_permlane32_swap_b32 v28, v30\nv_permlane32_swap_b32 v29, v31\nv_add_f32 v28, v28, v30\nv_add_f32 v29, v29, v31\n" \
    "v_cvt_pk_bf16_f32 v28, v28, v29\nds_write_b16 v4, v28 offset:2056\nds_write_b16_d16_hi v4, v28 offset:2072\ns_waitcnt lgkmcnt(6)\n" \
    "v_mfma_f32_16x16x32_bf16 v[16:19], v[72:75], v[64:67], v[16:19]\nv_mfma_f32_16x16x32_bf16 v[20:23], v[76:79], v[64:67], v[20:23]\ns_waitcnt lgkmcnt(5)\n" \
    "v_mfma_f32_16x16x32_bf16 v[16:19], v[80:83], v[68:71], v[16:19]\nds_read_b128 v[44:47], v8 offset:0\nds_read_b128 v[84:87], v8 offset:512\n" \
    "v_mfma_f32_16x16x32_bf16 v[20:23], v[56:59], v[68:71], v[20:23]\nds_read_b128 v[88:91], v8 offset:1024\nds_read_b128 v[28:31], v8 offset:1536\ns_waitcnt lgkmcnt(8)\n" \
    "v_mfma_f32_16x16x32_bf16 v[16:19], v[60:63], v[40:43], v[16:19]\nv_mfma_f32_16x16x32_bf16 v[20:23], v[32:35], v[40:43], v[20:23]\nv_mov_b32 v15, v15\nv_mov_b32 v15, v15\n" \
    "s_waitcnt lgkmcnt(3)\nv_mfma_f32_16x16x32_bf16 v[24:27], v[44:47], v[36:39], 0\ns_nop 2\ns_waitcnt lgkmcnt(2)\n" \
    "v_mov_b32_dpp v16, v20 quad_perm:[0,1,2,3] row_mask:0xf bank_mask:0xc\nv_mov_b32_dpp v17, v21 quad_perm:[0,1,2,3] row_mask:0xf bank_mask:0xc\n" \
    "v_mfma_f32_16x16x32_bf16 v[24:27], v[84:87], v[64:67], v[24:27]\ns_waitcnt lgkmcnt(1)\nv_mov_b32_dpp v18, v22 quad_perm:[0,1,2,3] row_mask:0xf bank_mask:0xc\n" \
    "v_mov_b32_dpp v19, v23 quad_perm:[0,1,2,3] row_mask:0xf bank_mask:0xc\nv_mfma_f32_16x16x32_bf16 v[24:27], v[88:91], v[68:71], v[24:27]\ns_waitcnt lgkmcnt(0)\n" \
    "v_max3_f32 v254, v16, v17, v18\nv_max_f32 v254, v254, v19\nv_mfma_f32_16x16x32_bf16 v[24:27], v[28:31], v[40:43], v[24:27]\ns_nop 0\n" \
    "v_max_f32_dpp v252, v254, v254 row_ror:8 row_mask:0xf bank_mask:0xf\nv_max_f32_dpp v253, v254, v254 row_ror:8 row_mask:0xf bank_mask:0xf\ns_nop 1\n" \
    "v_permlane32_swap_b32 v252, v253\nv_max_f32 v252, v252, v253\ns_xor_b32 s74, s82, 0x4000\nv_max3_f32 v254, v252, v24, v25\nv_max3_f32 v252, v254, v26, v27\n" \
    "v_max3_f32 v253, v254, v26, v27\ns_nop 1\nv_permlane16_swap_b32 v252, v253\nv_max_f32 v252, v252, v253\n" \
    "v_pk_add_f32 v[16:17], v[16:17], v[252:253] op_sel_hi:[1,0] neg_lo:[0,1] neg_hi:[0,1]\n" \
    "v_pk_add_f32 v[18:19], v[18:19], v[252:253] op_sel_hi:[1,0] neg_lo:[0,1] neg_hi:[0,1]\n" \
    "v_pk_add_f32 v[24:25], v[24:25], v[252:253] op_sel_hi:[1,0] neg_lo:[0,1] neg_hi:[0,1]\n" \
    "v_pk_add_f32 v[26:27], v[26:27], v[252:253] op_sel_hi:[1,0] neg_lo:[0,1] neg_hi:[0,1]\nv_exp_f32 v16, v16\nv_exp_f32 v17, v17\nv_exp_f32 v18, v18\nv_exp_f32 v19, v19\n" \
    "v_exp_f32 v24, v24\nv_pk_add_f32 v[254:255], v[16:17], v[18:19]\nv_exp_f32 v25, v25\nv_add_f32 v254, v254, v255\nv_exp_f32 v26, v26\nv_exp_f32 v27, v27\n" \
    "v_add_f32_dpp v252, v254, v254 row_ror:8 row_mask:0xf bank_mask:0xf\nv_add_f32_dpp v253, v254, v254 row_ror:8 row_mask:0xf bank_mask:0xf\n" \
    "v_pk_add_f32 v[254:255], v[24:25], v[26:27]\ns_nop 0\nv_permlane32_swap_b32 v252, v253\nv_pk_add_f32 v[254:255], v[254:255], v[252:253]\n" \
    "v_pk_add_f32 v[252:253], v[254:255], v[254:255] op_sel:[0,1] op_sel_hi:[1,0]\ns_nop 1\nv_permlane16_swap_b32 v252, v253\nv_add_f32 v252, v252, v253\n" \
    "v_cmp_u_f32 vcc, v252, v252\nv_rcp_f32 v252, v252\ns_cbranch_vccnz .Lq12_%=\nv_pk_mul_f32 v[16:17], v[16:17], v[252:253] op_sel_hi:[1,0]\n" \
    "v_pk_mul_f32 v[24:25], v[24:25], v[252:253] op_sel_hi:[1,0]\nv_cvt_pk_bf16_f32 v20, v16, v17\nv_cvt_pk_bf16_f32 v22, v24, v25\n" \
    "v_pk_mul_f32 v[18:19], v[18:19], v[252:253] op_sel_hi:[1,0]\nv_pk_mul_f32 v[26:27], v[26:27], v[252:253] op_sel_hi:[1,0]\nv_cvt_pk_bf16_f32 v21, v18, v19\n" \
    "v_cvt_pk_bf16_f32 v23, v26, v27\nv_permlane32_swap_b32 v20, v22\ns_nop 0\nv_permlane32_swap_b32 v21, v23\nbuffer_store_dwordx4 v[20:23], v7, s[64:67], s82 offen sc1\n" \
    "buffer_store_dwordx4 v[20:23], v7, s[64:67], s74 offen sc1\ns_branch .Lq3_%=\n.Lq7_%=:\n.Lq70_%=:\ns_memrealtime s[72:73]\ns_waitcnt lgkmcnt(0)\ns_mov_b32 s68, 0\n" \
    "s_sub_u32 s76, s80, s45\ns_sub_u32 s69, s72, s76\ns_cmp_lt_i32 s69, -8\ns_cbranch_scc1 .Lq73_%=\ns_xor_b32 s90, s81, 0x800\n.Lq75_%=:\ns_memrealtime s[72:73]\n" \
    "s_waitcnt lgkmcnt(0)\ns_sub_u32 s72, s72, s80\ns_cmp_lt_i32 s72, 0\ns_cbranch_scc1 .Lq75_%=\nbuffer_load_dwordx4 v[72:75], v2, s[64:67], s81 offen offset:0 sc1\n" \
    "v_cvt_pk_bf16_f32 v24, v32, v33\nv_cvt_pk_bf16_f32 v25, v34, v35\nv_cvt_pk_bf16_f32 v26, v40, v41\nbuffer_load_dwordx4 v[76:79], v2, s[64:67], s81 offen offset:1024 sc1\n" \
    "v_cvt_pk_bf16_f32 v27, v42, v43\nds_write_b128 v14, v[24:27] offset:8192\nv_cvt_pk_bf16_f32 v28, v36, v37\n" \
    "buffer_load_dwordx4 v[80:83], v2, s[64:67], s90 offen offset:0 sc1\nv_cvt_pk_bf16_f32 v29, v38, v39\nv_cvt_pk_bf16_f32 v30, v44, v45\nv_cvt_pk_bf16_f32 v31, v46, v47\n" \
    "buffer_load_dwordx4 v[84:87], v2, s[64:67], s90 offen offset:1024 sc1\nds_write_b128 v14, v[28:31] offset:12288\nds_read_b128 v[32:35], v3 offset:6144\n" \
    "ds_read_b128 v[36:39], v3 offset:7168\nds_read_b128 v[40:43], v3 offset:14336\nds_read_b128 v[44:47], v3 offset:15360\nds_read_b128 v[48:51], v3 offset:22528\n" \
    "ds_read_b128 v[52:55], v3 offset:23552\nds_read_b128 v[56:59], v3 offset:0\nds_read_b128 v[60:63], v3 offset:1024\nds_read_b128 v[64:67], v3 offset:2048\n" \
    "ds_read_b128 v[68:71], v3 offset:3072\n.Lq74_%=:\nv_mov_b64 v[16:17], 0\nv_mov_b64 v[18:19], 0\nv_mov_b64 v[20:21], 0\nv_mov_b64 v[22:23], 0\nv_mov_b64 v[24:25], 0\n" \
    "v_mov_b64 v[26:27], 0\nv_mov_b64 v[28:29], 0\nv_mov_b64 v[30:31], 0\ns_waitcnt vmcnt(3) lgkmcnt(8)\nv_smfmac_f32_16x16x64_bf16 v[20:23], v[72:75], v[32:39], v0\n" \
    "ds_write_b128 v1, v[72:75] offset:27648\nv_max3_u32 v252, v72, v73, v74\nv_smfmac_f32_16x16x64_bf16 v[16:19], v[72:75], a[72:79], v0\n" \
    "v_smfmac_f32_16x16x64_bf16 v[24:27], v[72:75], a[80:87], v0\nv_smfmac_f32_16x16x64_bf16 v[28:31], v[72:75], a[88:95], v0\ns_waitcnt vmcnt(2)\n" \
    "ds_write_b128 v1, v[76:79] offset:31744\ns_waitcnt lgkmcnt(8)\nv_max3_u32 v252, v252, v75, v76\nv_smfmac_f32_16x16x64_bf16 v[20:23], v[76:79], v[40:47], v0\n" \
    "v_max3_u32 v252, v252, v77, v78\nv_smfmac_f32_16x16x64_bf16 v[16:19], v[76:79], a[168:175], v0\nv_cmp_u_f32 vcc, v252, v79\n" \
    "v_smfmac_f32_16x16x64_bf16 v[24:27], v[76:79], a[176:183], v0\ns_cbranch_vccnz .Lq81_%=\n.Lq77_%=:\nv_smfmac_f32_16x16x64_bf16 v[28:31], v[76:79], a[184:191], v0\n" \
    "ds_read_b128 v[36:39], v3 offset:5120\ns_waitcnt vmcnt(1)\nv_max3_u32 v252, v80, v81, v82\nv_cmp_u_f32 vcc, v252, v83\ns_nop 0\ns_cbranch_vccnz .Lq82_%=\n.Lq78_%=:\n" \
    "s_waitcnt lgkmcnt(7)\nv_smfmac_f32_16x16x64_bf16 v[20:23], v[80:83], v[48:55], v0\nds_write_b128 v1, v[80:83] offset:35840\n" \
    "v_smfmac_f32_16x16x64_bf16 v[16:19], v[80:83], v[100:107], v0\nds_read_b128 v[32:35], v3 offset:4096\ns_waitcnt lgkmcnt(3)\ns_barrier\n" \
    "ds_read_b128 v[88:91], v1 offset:24576\nv_smfmac_f32_16x16x64_bf16 v[24:27], v[80:83], v[108:115], v0\nds_read_b128 v[40:43], v3 offset:8192\n" \
    "v_smfmac_f32_16x16x64_bf16 v[28:31], v[80:83], v[116:123], v0\nds_read_b128 v[48:51], v1 offset:25600\ns_waitcnt vmcnt(0)\nv_max3_u32 v252, v84, v85, v86\n" \
    "v_cmp_u_f32 vcc, v252, v87\ns_nop 0\ns_cbranch_vccnz .Lq83_%=\n.Lq79_%=:\nv_smfmac_f32_16x16x64_bf16 v[20:23], v[84:87], v[228:235], v0\n" \
    "ds_write_b128 v1, v[84:87] offset:39936\nv_smfmac_f32_16x16x64_bf16 v[16:19], v[84:87], v[220:227], v0\nds_read_b128 v[52:55], v1 offset:26624\n" \
    "v_smfmac_f32_16x16x64_bf16 v[24:27], v[84:87], v[236:243], v0\nds_read_b128 v[72:75], v1 offset:28672\nv_smfmac_f32_16x16x64_bf16 v[28:31], v[84:87], v[244:251], v0\n" \
    "ds_read_b128 v[44:47], v3 offset:9216\ns_waitcnt lgkmcnt(6)\nv_smfmac_f32_16x16x64_bf16 v[20:23], v[88:91], v[56:63], v0\nds_read_b128 v[56:59], v1 offset:29696\n" \
    "v_smfmac_f32_16x16x64_bf16 v[16:19], v[88:91], a[0:7], v0\nds_read_b128 v[76:79], v3 offset:10240\nv_smfmac_f32_16x16x64_bf16 v[24:27], v[88:91], a[8:15], v0\n" \
    "ds_read_b128 v[80:83], v3 offset:11264\nv_smfmac_f32_16x16x64_bf16 v[28:31], v[88:91], a[16:23], v0\nds_read_b128 v[60:63], v1 offset:30720\ns_waitcnt lgkmcnt(7)\n" \
    "s_barrier\nv_smfmac_f32_16x16x64_bf16 v[20:23], v[48:51], v[64:71], v0\nds_read_b128 v[64:67], v3 offset:12288\n" \
    "v_smfmac_f32_16x16x64_bf16 v[16:19], v[48:51], a[24:31], v0\nds_read_b128 v[68:71], v3 offset:13312\ns_waitcnt lgkmcnt(8)\n" \
    "v_smfmac_f32_16x16x64_bf16 v[20:23], v[52:55], v[32:39], v0\nds_read_b128 v[32:35], v1 offset:32768\nv_smfmac_f32_16x16x64_bf16 v[16:19], v[52:55], a[48:55], v0\n" \
    "ds_read_b128 v[84:87], v3 offset:16384\ns_waitcnt lgkmcnt(8)\nv_smfmac_f32_16x16x64_bf16 v[20:23], v[72:75], v[40:47], v0\nds_read_b128 v[88:91], v3 offset:17408\n" \
    "v_smfmac_f32_16x16x64_bf16 v[16:19], v[72:75], a[96:103], v0\nds_read_b128 v[36:39], v1 offset:33792\ns_waitcnt lgkmcnt(7)\n" \
    "v_smfmac_f32_16x16x64_bf16 v[20:23], v[56:59], v[76:83], v0\nds_read_b128 v[40:43], v3 offset:18432\nv_smfmac_f32_16x16x64_bf16 v[16:19], v[56:59], a[120:127], v0\n" \
    "ds_read_b128 v[44:47], v3 offset:19456\ns_waitcnt lgkmcnt(6)\nv_smfmac_f32_16x16x64_bf16 v[20:23], v[60:63], v[64:71], v0\nds_read_b128 v[64:67], v1 offset:34816\n" \
    "v_smfmac_f32_16x16x64_bf16 v[16:19], v[60:63], a[144:151], v0\nds_read_b128 v[76:79], v3 offset:20480\ns_waitcnt lgkmcnt(5)\n" \
    "v_smfmac_f32_16x16x64_bf16 v[20:23], v[32:35], v[84:91], v0\nds_read_b128 v[80:83], v3 offset:21504\nv_smfmac_f32_16x16x64_bf16 v[16:19], v[32:35], a[192:199], v0\n" \
    "ds_read_b128 v[68:71], v1 offset:36864\ns_waitcnt lgkmcnt(4)\nv_smfmac_f32_16x16x64_bf16 v[20:23], v[36:39], v[40:47], v0\nds_read_b128 v[40:43], v1 offset:37888\n" \
    "v_smfmac_f32_16x16x64_bf16 v[16:19], v[36:39], a[216:223], v0\nds_read_b128 v[44:47], v1 offset:38912\ns_waitcnt lgkmcnt(3)\n" \
    "v_smfmac_f32_16x16x64_bf16 v[20:23], v[64:67], v[76:83], v0\nv_smfmac_f32_16x16x64_bf16 v[16:19], v[64:67], a[240:247], v0\ns_waitcnt lgkmcnt(2)\n" \
    "v_smfmac_f32_16x16x64_bf16 v[20:23], v[68:71], v[132:139], v0\ns_waitcnt lgkmcnt(1)\nv_smfmac_f32_16x16x64_bf16 v[20:23], v[40:43], v[164:171], v0\ns_waitcnt lgkmcnt(0)\n" \
    "v_smfmac_f32_16x16x64_bf16 v[20:23], v[44:47], v[196:203], v0\ns_nop 2\nv_smfmac_f32_16x16x64_bf16 v[16:19], v[68:71], v[124:131], v0\ns_nop 2\n" \
    "v_smfmac_f32_16x16x64_bf16 v[16:19], v[40:43], v[156:163], v0\nv_permlane32_swap_b32 v20, v22\nv_smfmac_f32_16x16x64_bf16 v[16:19], v[44:47], v[188:195], v0\ns_nop 1\n" \
    "v_permlane32_swap_b32 v21, v23\nv_smfmac_f32_16x16x64_bf16 v[24:27], v[48:51], a[32:39], v0\ns_nop 1\nv_add_f32 v20, v20, v22\n" \
    "v_smfmac_f32_16x16x64_bf16 v[28:31], v[48:51], a[40:47], v0\nv_add_f32 v21, v21, v23\nv_smfmac_f32_16x16x64_bf16 v[24:27], v[52:55], a[56:63], v0\nv_mul_f32 v20, s32, v20\n" \
    "v_smfmac_f32_16x16x64_bf16 v[28:31], v[52:55], a[64:71], v0\nv_mul_f32 v21, s32, v21\nv_smfmac_f32_16x16x64_bf16 v[24:27], v[72:75], a[104:111], v0\n" \
    "v_cvt_pk_bf16_f32 v20, v20, v21\nv_smfmac_f32_16x16x64_bf16 v[28:31], v[72:75], a[112:119], v0\nds_write_b16 v4, v20 offset:8\nv_permlane32_swap_b32 v16, v18\n" \
    "v_smfmac_f32_16x16x64_bf16 v[24:27], v[56:59], a[128:135], v0\nds_write_b16_d16_hi v4, v20 offset:24\nv_permlane32_swap_b32 v17, v19\n" \
    "v_smfmac_f32_16x16x64_bf16 v[24:27], v[60:63], a[152:159], v0\nds_read_b128 v[48:51], v1 offset:40960\nv_add_f32 v16, v16, v18\n" \
    "v_smfmac_f32_16x16x64_bf16 v[24:27], v[32:35], a[200:207], v0\nds_read_b128 v[52:55], v1 offset:45056\nv_add_f32 v17, v17, v19\n" \
    "v_smfmac_f32_16x16x64_bf16 v[24:27], v[36:39], a[224:231], v0\nds_read_b128 v[72:75], v1 offset:41984\nv_mul_f32 v16, s32, v16\n" \
    "v_smfmac_f32_16x16x64_bf16 v[24:27], v[64:67], a[248:255], v0\nds_read_b128 v[76:79], v1 offset:46080\nv_mul_f32 v17, s32, v17\n" \
    "v_smfmac_f32_16x16x64_bf16 v[24:27], v[68:71], v[140:147], v0\nds_read_b128 v[80:83], v1 offset:43008\nv_cvt_pk_bf16_f32 v16, v16, v17\n" \
    "v_smfmac_f32_16x16x64_bf16 v[24:27], v[40:43], v[172:179], v0\nds_write_b16 v4, v16 offset:0\nv_smfmac_f32_16x16x64_bf16 v[24:27], v[44:47], v[204:211], v0\n" \
    "ds_write_b16_d16_hi v4, v16 offset:16\ns_nop 1\nv_smfmac_f32_16x16x64_bf16 v[28:31], v[56:59], a[136:143], v0\nds_read_b128 v[56:59], v1 offset:47104\ns_nop 1\n" \
    "v_smfmac_f32_16x16x64_bf16 v[28:31], v[60:63], a[160:167], v0\nds_read_b128 v[60:63], v1 offset:44032\nv_permlane32_swap_b32 v24, v26\n" \
    "v_smfmac_f32_16x16x64_bf16 v[28:31], v[32:35], a[208:215], v0\nds_read_b128 v[32:35], v1 offset:48128\nv_permlane32_swap_b32 v25, v27\n" \
    "v_smfmac_f32_16x16x64_bf16 v[28:31], v[36:39], a[232:239], v0\nds_read_b128 v[36:39], v6 offset:0\nv_add_f32 v24, v24, v26\n" \
    "v_smfmac_f32_16x16x64_bf16 v[28:31], v[64:67], v[92:99], v0\nds_read_b128 v[64:67], v6 offset:512\nv_add_f32 v25, v25, v27\n" \
    "v_smfmac_f32_16x16x64_bf16 v[28:31], v[68:71], v[148:155], v0\nds_read_b128 v[68:71], v6 offset:1024\nv_cvt_pk_bf16_f32 v24, v24, v25\n" \
    "v_smfmac_f32_16x16x64_bf16 v[28:31], v[40:43], v[180:187], v0\nds_read_b128 v[40:43], v6 offset:1536\nv_smfmac_f32_16x16x64_bf16 v[28:31], v[44:47], v[212:219], v0\n" \
    "ds_write_b16 v4, v24 offset:2048\nds_write_b16_d16_hi v4, v24 offset:2064\ns_waitcnt lgkmcnt(5)\nv_mfma_f32_16x16x32_bf16 v[16:19], v[48:51], v[36:39], 0\ns_nop 2\n" \
    "v_mfma_f32_16x16x32_bf16 v[20:23], v[52:55], v[36:39], 0\nv_permlane32_swap_b32 v28, v30\nv_permlane32_swap_b32 v29, v31\nv_add_f32 v28, v28, v30\nv_add_f32 v29, v29, v31\n" \
    "v_cvt_pk_bf16_f32 v28, v28, v29\nds_write_b16 v4, v28 offset:2056\nds_write_b16_d16_hi v4, v28 offset:2072\ns_waitcnt lgkmcnt(6)\n" \
    "v_mfma_f32_16x16x32_bf16 v[16:19], v[72:75], v[64:67], v[16:19]\nv_mfma_f32_16x16x32_bf16 v[20:23], v[76:79], v[64:67], v[20:23]\ns_waitcnt lgkmcnt(5)\n" \
    "v_mfma_f32_16x16x32_bf16 v[16:19], v[80:83], v[68:71], v[16:19]\nds_read_b128 v[44:47], v8 offset:0\nds_read_b128 v[84:87], v8 offset:512\n" \
    "v_mfma_f32_16x16x32_bf16 v[20:23], v[56:59], v[68:71], v[20:23]\nds_read_b128 v[88:91], v8 offset:1024\nds_read_b128 v[28:31], v8 offset:1536\ns_waitcnt lgkmcnt(8)\n" \
    "v_mfma_f32_16x16x32_bf16 v[16:19], v[60:63], v[40:43], v[16:19]\nv_mfma_f32_16x16x32_bf16 v[20:23], v[32:35], v[40:43], v[20:23]\nv_mov_b32 v15, v15\nv_mov_b32 v15, v15\n" \
    "s_waitcnt lgkmcnt(3)\nv_mfma_f32_16x16x32_bf16 v[24:27], v[44:47], v[36:39], 0\ns_nop 2\ns_waitcnt lgkmcnt(2)\n" \
    "v_mov_b32_dpp v16, v20 quad_perm:[0,1,2,3] row_mask:0xf bank_mask:0xc\nv_mov_b32_dpp v17, v21 quad_perm:[0,1,2,3] row_mask:0xf bank_mask:0xc\n" \
    "v_mfma_f32_16x16x32_bf16 v[24:27], v[84:87], v[64:67], v[24:27]\ns_waitcnt lgkmcnt(1)\nv_mov_b32_dpp v18, v22 quad_perm:[0,1,2,3] row_mask:0xf bank_mask:0xc\n" \
    "v_mov_b32_dpp v19, v23 quad_perm:[0,1,2,3] row_mask:0xf bank_mask:0xc\nv_mfma_f32_16x16x32_bf16 v[24:27], v[88:91], v[68:71], v[24:27]\ns_waitcnt lgkmcnt(0)\n" \
    "v_max3_f32 v254, v16, v17, v18\nv_max_f32 v254, v254, v19\nv_mfma_f32_16x16x32_bf16 v[24:27], v[28:31], v[40:43], v[24:27]\ns_nop 0\n" \
    "v_max_f32_dpp v252, v254, v254 row_ror:8 row_mask:0xf bank_mask:0xf\nv_max_f32_dpp v253, v254, v254 row_ror:8 row_mask:0xf bank_mask:0xf\ns_nop 1\n" \
    "v_permlane32_swap_b32 v252, v253\nv_max_f32 v252, v252, v253\ns_xor_b32 s74, s82, 0x4000\nv_max3_f32 v254, v252, v24, v25\nv_max3_f32 v252, v254, v26, v27\n" \
    "v_max3_f32 v253, v254, v26, v27\ns_nop 1\nv_permlane16_swap_b32 v252, v253\nv_max_f32 v252, v252, v253\n" \
    "v_pk_add_f32 v[16:17], v[16:17], v[252:253] op_sel_hi:[1,0] neg_lo:[0,1] neg_hi:[0,1]\n" \
    "v_pk_add_f32 v[18:19], v[18:19], v[252:253] op_sel_hi:[1,0] neg_lo:[0,1] neg_hi:[0,1]\n" \
    "v_pk_add_f32 v[24:25], v[24:25], v[252:253] op_sel_hi:[1,0] neg_lo:[0,1] neg_hi:[0,1]\n" \
    "v_pk_add_f32 v[26:27], v[26:27], v[252:253] op_sel_hi:[1,0] neg_lo:[0,1] neg_hi:[0,1]\nv_exp_f32 v16, v16\nv_exp_f32 v17, v17\nv_exp_f32 v18, v18\nv_exp_f32 v19, v19\n" \
    "v_exp_f32 v24, v24\nv_pk_add_f32 v[254:255], v[16:17], v[18:19]\nv_exp_f32 v25, v25\nv_add_f32 v254, v254, v255\nv_exp_f32 v26, v26\nv_exp_f32 v27, v27\n" \
    "v_add_f32_dpp v252, v254, v254 row_ror:8 row_mask:0xf bank_mask:0xf\nv_add_f32_dpp v253, v254, v254 row_ror:8 row_mask:0xf bank_mask:0xf\n" \
    "v_pk_add_f32 v[254:255], v[24:25], v[26:27]\ns_nop 0\nv_permlane32_swap_b32 v252, v253\nv_pk_add_f32 v[254:255], v[254:255], v[252:253]\n" \
    "v_pk_add_f32 v[252:253], v[254:255], v[254:255] op_sel:[0,1] op_sel_hi:[1,0]\ns_nop 1\nv_permlane16_swap_b32 v252, v253\nv_add_f32 v252, v252, v253\n" \
    "v_cmp_u_f32 vcc, v252, v252\nv_rcp_f32 v252, v252\ns_cbranch_vccnz .Lq12_%=\nv_pk_mul_f32 v[16:17], v[16:17], v[252:253] op_sel_hi:[1,0]\n" \
    "v_pk_mul_f32 v[24:25], v[24:25], v[252:253] op_sel_hi:[1,0]\nv_cvt_pk_bf16_f32 v20, v16, v17\nv_cvt_pk_bf16_f32 v22, v24, v25\n" \
    "v_pk_mul_f32 v[18:19], v[18:19], v[252:253] op_sel_hi:[1,0]\nv_pk_mul_f32 v[26:27], v[26:27], v[252:253] op_sel_hi:[1,0]\nv_cvt_pk_bf16_f32 v21, v18, v19\n" \
    "v_cvt_pk_bf16_f32 v23, v26, v27\nv_permlane32_swap_b32 v20, v22\ns_nop 0\nv_permlane32_swap_b32 v21, v23\nbuffer_store_dwordx4 v[20:23], v7, s[64:67], s82 offen sc1\n" \
    "buffer_store_dwordx4 v[20:23], v7, s[64:67], s74 offen sc1\n.Lq3_%=:\nds_read_b128 v[36:39], v5 offset:0\nds_read_b128 v[64:67], v5 offset:1024\n" \
    "ds_read_b128 v[68:71], v5 offset:2048\nds_read_b128 v[40:43], v5 offset:3072\ns_mov_b32 s40, s84\ns_add_u32 s35, s35, 2\ns_barrier\ns_memrealtime s[72:73]\n" \
    "s_waitcnt lgkmcnt(0)\nv_mfma_f32_16x16x32_bf16 v[16:19], v[48:51], v[36:39], 0\nv_mfma_f32_16x16x32_bf16 v[20:23], v[52:55], v[36:39], 0\n" \
    "v_mfma_f32_16x16x32_bf16 v[16:19], v[72:75], v[64:67], v[16:19]\nv_mfma_f32_16x16x32_bf16 v[20:23], v[76:79], v[64:67], v[20:23]\n" \
    "v_mfma_f32_16x16x32_bf16 v[16:19], v[80:83], v[68:71], v[16:19]\nv_mfma_f32_16x16x32_bf16 v[20:23], v[56:59], v[68:71], v[20:23]\n" \
    "v_mfma_f32_16x16x32_bf16 v[16:19], v[60:63], v[40:43], v[16:19]\nv_mfma_f32_16x16x32_bf16 v[20:23], v[32:35], v[40:43], v[20:23]\n" \
    "v_mfma_f32_16x16x32_bf16 v[24:27], v[44:47], v[36:39], 0\nv_mfma_f32_16x16x32_bf16 v[24:27], v[84:87], v[64:67], v[24:27]\n" \
    "v_mfma_f32_16x16x32_bf16 v[24:27], v[88:91], v[68:71], v[24:27]\nv_mfma_f32_16x16x32_bf16 v[24:27], v[28:31], v[40:43], v[24:27]\ns_cmp_lt_u32 s35, 64\n" \
    "s_cbranch_scc1 .Lq2_%=\n.Lq8_%=:\ns_nop 1\nv_mov_b32_dpp v16, v20 quad_perm:[0,1,2,3] row_mask:0xf bank_mask:0xc\n" \
    "v_mov_b32_dpp v17, v21 quad_perm:[0,1,2,3] row_mask:0xf bank_mask:0xc\nv_mov_b32_dpp v18, v22 quad_perm:[0,1,2,3] row_mask:0xf bank_mask:0xc\n" \
    "v_mov_b32_dpp v19, v23 quad_perm:[0,1,2,3] row_mask:0xf bank_mask:0xc\nv_mov_b32 v254, 0xff800000\nv_cvt_pk_bf16_f32 v16, 0, v16\nv_cvt_pk_bf16_f32 v17, 0, v17\n" \
    "v_cvt_pk_bf16_f32 v18, 0, v18\nv_cvt_pk_bf16_f32 v19, 0, v19\nv_mul_f32 v16, s33, v16\nv_mul_f32 v17, s33, v17\nv_mul_f32 v18, s33, v18\nv_mul_f32 v19, s33, v19\n" \
    "v_cvt_pk_bf16_f32 v16, 0, v16\nv_cvt_pk_bf16_f32 v17, 0, v17\nv_cvt_pk_bf16_f32 v18, 0, v18\nv_cvt_pk_bf16_f32 v19, 0, v19\nv_cndmask_b32 v16, v254, v16, s[50:51]\n" \
    "v_cndmask_b32 v17, v254, v17, s[52:53]\nv_cndmask_b32 v18, v254, v18, s[54:55]\nv_cndmask_b32 v19, v254, v19, s[56:57]\nv_max3_f32 v254, v16, v17, v18\n" \
    "v_max_f32 v254, v254, v19\ns_nop 1\nv_max_f32_dpp v252, v254, v254 row_ror:8 row_mask:0xf bank_mask:0xf\n" \
    "v_max_f32_dpp v253, v254, v254 row_ror:8 row_mask:0xf bank_mask:0xf\ns_nop 1\nv_permlane32_swap_b32 v252, v253\nv_max_f32 v252, v252, v253\nv_cvt_pk_bf16_f32 v24, 0, v24\n" \
    "v_cvt_pk_bf16_f32 v25, 0, v25\nv_cvt_pk_bf16_f32 v26, 0, v26\nv_cvt_pk_bf16_f32 v27, 0, v27\nv_mul_f32 v24, s33, v24\nv_mul_f32 v25, s33, v25\nv_mul_f32 v26, s33, v26\n" \
    "v_mul_f32 v27, s33, v27\nv_cvt_pk_bf16_f32 v24, 0, v24\nv_cvt_pk_bf16_f32 v25, 0, v25\nv_cvt_pk_bf16_f32 v26, 0, v26\nv_cvt_pk_bf16_f32 v27, 0, v27\n" \
    "v_max3_f32 v254, v252, v24, v25\nv_max3_f32 v252, v254, v26, v27\nv_max3_f32 v253, v254, v26, v27\ns_nop 1\nv_permlane16_swap_b32 v252, v253\nv_max_f32 v252, v252, v253\n" \
    "v_pk_add_f32 v[16:17], v[16:17], v[252:253] op_sel_hi:[1,0] neg_lo:[0,1] neg_hi:[0,1]\n" \
    "v_pk_add_f32 v[18:19], v[18:19], v[252:253] op_sel_hi:[1,0] neg_lo:[0,1] neg_hi:[0,1]\n" \
    "v_pk_add_f32 v[24:25], v[24:25], v[252:253] op_sel_hi:[1,0] neg_lo:[0,1] neg_hi:[0,1]\n" \
    "v_pk_add_f32 v[26:27], v[26:27], v[252:253] op_sel_hi:[1,0] neg_lo:[0,1] neg_hi:[0,1]\nv_mul_f32 v16, s34, v16\nv_mul_f32 v17, s34, v17\nv_mul_f32 v18, s34, v18\n" \
    "v_mul_f32 v19, s34, v19\nv_mul_f32 v24, s34, v24\nv_mul_f32 v25, s34, v25\nv_mul_f32 v26, s34, v26\nv_mul_f32 v27, s34, v27\nv_exp_f32 v16, v16\nv_exp_f32 v17, v17\n" \
    "v_exp_f32 v18, v18\nv_exp_f32 v19, v19\nv_exp_f32 v24, v24\nv_pk_add_f32 v[254:255], v[16:17], v[18:19]\nv_exp_f32 v25, v25\nv_add_f32 v254, v254, v255\n" \
    "v_exp_f32 v26, v26\nv_exp_f32 v27, v27\nv_add_f32_dpp v252, v254, v254 row_ror:8 row_mask:0xf bank_mask:0xf\n" \
    "v_add_f32_dpp v253, v254, v254 row_ror:8 row_mask:0xf bank_mask:0xf\nv_pk_add_f32 v[254:255], v[24:25], v[26:27]\ns_nop 0\nv_permlane32_swap_b32 v252, v253\n" \
    "v_pk_add_f32 v[254:255], v[254:255], v[252:253]\nv_pk_add_f32 v[252:253], v[254:255], v[254:255] op_sel:[0,1] op_sel_hi:[1,0]\ns_nop 1\nv_permlane16_swap_b32 v252, v253\n" \
    "v_add_f32 v252, v252, v253\nv_rcp_f32 v252, v252\ns_nop 0\nv_pk_mul_f32 v[16:17], v[16:17], v[252:253] op_sel_hi:[1,0]\n" \
    "v_pk_mul_f32 v[24:25], v[24:25], v[252:253] op_sel_hi:[1,0]\nv_cvt_pk_bf16_f32 v20, v16, v17\nv_cvt_pk_bf16_f32 v22, v24, v25\n" \
    "v_pk_mul_f32 v[18:19], v[18:19], v[252:253] op_sel_hi:[1,0]\nv_pk_mul_f32 v[26:27], v[26:27], v[252:253] op_sel_hi:[1,0]\nv_cvt_pk_bf16_f32 v21, v18, v19\n" \
    "v_cvt_pk_bf16_f32 v23, v26, v27\nv_permlane32_swap_b32 v20, v22\ns_nop 0\nv_permlane32_swap_b32 v21, v23\nv_writelane_b32 v20, s18, 40\ns_xor_b32 s74, s83, 0x2000\n" \
    "buffer_store_dwordx4 v[20:23], v7, s[64:67], s83 offen sc1\nbuffer_store_dwordx4 v[20:23], v7, s[64:67], s74 offen sc1\ns_cmp_lt_u32 s35, 64\ns_cbranch_scc1 .Lq9_%=\n" \
    "s_endpgm\n.Lq11_%=:\ns_sleep 6\nbuffer_load_dword v13, off, s[64:67], s42 sc1\ns_waitcnt vmcnt(0)\nv_readfirstlane_b32 s74, v13\ns_nop 0\ns_and_b32 s75, s74, 127\n" \
    "s_lshr_b32 s69, s74, 8\ns_sub_u32 s69, s69, s80\ns_bfe_i32 s69, s69, 0x180000\ns_cmp_eq_u32 s75, 0\ns_cselect_b32 s69, 0, s69\ns_add_u32 s80, s80, s69\n" \
    "s_lshl_b32 s74, s74, 5\ns_and_b32 s74, s74, 0x1000\ns_andn2_b32 s81, s81, 0x1000\ns_or_b32 s81, s81, s74\ns_lshl_b32 s74, s74, 2\ns_andn2_b32 s82, s82, 0x4000\n" \
    "s_or_b32 s82, s82, s74\ns_add_u32 s68, s68, 1\ns_and_b32 s69, s68, 63\ns_cbranch_scc1 .Lq10_%=\ns_cmp_gt_u32 s68, 0x400000\ns_cbranch_scc1 .Lq1_%=\n" \
    "buffer_load_dword v253, off, s[64:67], 0 sc1\ns_waitcnt vmcnt(0)\nv_readfirstlane_b32 s69, v253\ns_nop 0\ns_cmp_eq_u32 s69, 1\ns_cbranch_scc0 .Lq10_%=\ns_endpgm\n" \
    ".Lq16_%=:\ns_memrealtime s[72:73]\ns_waitcnt lgkmcnt(0)\ns_sub_u32 s72, s72, s76\ns_cmp_lt_i32 s72, 0\ns_cbranch_scc1 .Lq16_%=\n" \
    "buffer_load_dword v13, off, s[64:67], s42 sc1\ns_waitcnt vmcnt(0)\nv_readfirstlane_b32 s74, v13\ns_nop 0\ns_and_b32 s75, s74, 127\ns_lshr_b32 s69, s74, 8\n" \
    "s_sub_u32 s69, s69, s80\ns_bfe_i32 s69, s69, 0x180000\ns_cmp_eq_u32 s75, 0\ns_cselect_b32 s69, 0, s69\ns_add_u32 s80, s80, s69\ns_lshl_b32 s74, s74, 5\n" \
    "s_and_b32 s74, s74, 0x1000\ns_andn2_b32 s81, s81, 0x1000\ns_or_b32 s81, s81, s74\ns_lshl_b32 s74, s74, 2\ns_andn2_b32 s82, s82, 0x4000\ns_or_b32 s82, s82, s74\n" \
    "s_branch .Lq13_%=\n.Lq24_%=:\nv_mov_b32 v252, 0\nv_max3_u32 v252, v252, v72, v73\nv_max3_u32 v252, v252, v74, v75\nv_max3_u32 v252, v252, v76, v77\n" \
    "v_max3_u32 v252, v252, v78, v79\nv_cmp_eq_u32 vcc, -1, v252\ns_nop 0\ns_cbranch_vccz .Lq20_%=\ns_add_u32 s80, s80, s43\ns_xor_b32 s81, s81, 0x1000\n" \
    "s_xor_b32 s90, s90, 0x1000\ns_xor_b32 s82, s82, 0x4000\ns_cmp_eq_u32 s61, 0\ns_cbranch_scc1 .Lq27_%=\ns_lshr_b32 s69, s81, 5\ns_and_b32 s69, s69, 128\n" \
    "s_lshl_b32 s74, s80, 8\ns_add_u32 s74, s74, s69\ns_add_u32 s74, s74, s35\nv_mov_b32 v253, s74\nv_lshlrev_b32 v252, 7, v2\ns_mov_b64 exec, 1\n" \
    "buffer_store_dword v253, v252, s[64:67], s61 offen sc1\ns_mov_b64 exec, -1\n.Lq27_%=:\ns_add_u32 s68, s68, 1\ns_and_b32 s69, s68, 63\ns_cbranch_scc1 .Lq14_%=\n" \
    "s_cmp_gt_u32 s68, 0x400000\ns_cbranch_scc1 .Lq1_%=\nbuffer_load_dword v253, off, s[64:67], 0 sc1\ns_waitcnt vmcnt(0)\nv_readfirstlane_b32 s69, v253\ns_nop 0\n" \
    "s_cmp_eq_u32 s69, 1\ns_cbranch_scc0 .Lq14_%=\ns_endpgm\n.Lq14_%=:\ns_memrealtime s[72:73]\ns_waitcnt lgkmcnt(0)\ns_sub_u32 s72, s72, s80\ns_cmp_lt_i32 s72, 0\n" \
    "s_cbranch_scc1 .Lq14_%=\nbuffer_load_dwordx4 v[72:75], v2, s[64:67], s81 offen offset:0 sc1\ns_nop 7\n" \
    "buffer_load_dwordx4 v[76:79], v2, s[64:67], s81 offen offset:1024 sc1\ns_nop 7\nbuffer_load_dwordx4 v[80:83], v2, s[64:67], s90 offen offset:0 sc1\ns_nop 7\n" \
    "buffer_load_dwordx4 v[84:87], v2, s[64:67], s90 offen offset:1024 sc1\ns_nop 7\ns_branch .Lq17_%=\n.Lq25_%=:\nv_mov_b32 v252, 0\nv_max3_u32 v252, v252, v80, v81\n" \
    "v_max3_u32 v252, v252, v82, v83\nv_cmp_eq_u32 vcc, -1, v252\ns_nop 0\ns_cbranch_vccz .Lq21_%=\ns_add_u32 s80, s80, s43\ns_xor_b32 s81, s81, 0x1000\n" \
    "s_xor_b32 s90, s90, 0x1000\ns_xor_b32 s82, s82, 0x4000\ns_cmp_eq_u32 s61, 0\ns_cbranch_scc1 .Lq29_%=\ns_lshr_b32 s69, s81, 5\ns_and_b32 s69, s69, 128\n" \
    "s_lshl_b32 s74, s80, 8\ns_add_u32 s74, s74, s69\ns_add_u32 s74, s74, s35\nv_mov_b32 v253, s74\nv_lshlrev_b32 v252, 7, v2\ns_mov_b64 exec, 1\n" \
    "buffer_store_dword v253, v252, s[64:67], s61 offen sc1\ns_mov_b64 exec, -1\n.Lq29_%=:\n.Lq28_%=:\ns_memrealtime s[72:73]\ns_waitcnt vmcnt(0) lgkmcnt(0)\n" \
    "s_sub_u32 s72, s72, s80\ns_cmp_lt_i32 s72, 0\ns_cbranch_scc1 .Lq28_%=\nbuffer_load_dwordx4 v[80:83], v2, s[64:67], s90 offen offset:0 sc1\n" \
    "buffer_load_dwordx4 v[84:87], v2, s[64:67], s90 offen offset:1024 sc1\ns_waitcnt vmcnt(0)\ns_add_u32 s68, s68, 1\ns_and_b32 s69, s68, 63\ns_cbranch_scc1 .Lq25_%=\n" \
    "s_cmp_gt_u32 s68, 0x400000\ns_cbranch_scc1 .Lq1_%=\nbuffer_load_dword v253, off, s[64:67], 0 sc1\ns_waitcnt vmcnt(0)\nv_readfirstlane_b32 s69, v253\ns_nop 0\n" \
    "s_cmp_eq_u32 s69, 1\ns_cbranch_scc0 .Lq25_%=\ns_endpgm\n.Lq26_%=:\nv_mov_b32 v252, 0\nv_max3_u32 v252, v252, v84, v85\nv_max3_u32 v252, v252, v86, v87\n" \
    "v_cmp_eq_u32 vcc, -1, v252\ns_nop 0\ns_cbranch_vccz .Lq22_%=\ns_add_u32 s80, s80, s43\ns_xor_b32 s81, s81, 0x1000\ns_xor_b32 s90, s90, 0x1000\ns_xor_b32 s82, s82, 0x4000\n" \
    "s_cmp_eq_u32 s61, 0\ns_cbranch_scc1 .Lq31_%=\ns_lshr_b32 s69, s81, 5\ns_and_b32 s69, s69, 128\ns_lshl_b32 s74, s80, 8\ns_add_u32 s74, s74, s69\ns_add_u32 s74, s74, s35\n" \
    "v_mov_b32 v253, s74\nv_lshlrev_b32 v252, 7, v2\ns_mov_b64 exec, 1\nbuffer_store_dword v253, v252, s[64:67], s61 offen sc1\ns_mov_b64 exec, -1\n.Lq31_%=:\n.Lq30_%=:\n" \
    "s_memrealtime s[72:73]\ns_waitcnt vmcnt(0) lgkmcnt(0)\ns_sub_u32 s72, s72, s80\ns_cmp_lt_i32 s72, 0\ns_cbranch_scc1 .Lq30_%=\n" \
    "buffer_load_dwordx4 v[84:87], v2, s[64:67], s90 offen offset:1024 sc1\ns_waitcnt vmcnt(0)\ns_add_u32 s68, s68, 1\ns_and_b32 s69, s68, 63\ns_cbranch_scc1 .Lq26_%=\n" \
    "s_cmp_gt_u32 s68, 0x400000\ns_cbranch_scc1 .Lq1_%=\nbuffer_load_dword v253, off, s[64:67], 0 sc1\ns_waitcnt vmcnt(0)\nv_readfirstlane_b32 s69, v253\ns_nop 0\n" \
    "s_cmp_eq_u32 s69, 1\ns_cbranch_scc0 .Lq26_%=\ns_endpgm\n.Lq35_%=:\ns_memrealtime s[72:73]\ns_waitcnt lgkmcnt(0)\ns_sub_u32 s72, s72, s76\ns_cmp_lt_i32 s72, 0\n" \
    "s_cbranch_scc1 .Lq35_%=\nbuffer_load_dword v13, off, s[64:67], s42 sc1\ns_waitcnt vmcnt(0)\nv_readfirstlane_b32 s74, v13\ns_nop 0\ns_and_b32 s75, s74, 127\n" \
    "s_lshr_b32 s69, s74, 8\ns_sub_u32 s69, s69, s80\ns_bfe_i32 s69, s69, 0x180000\ns_cmp_eq_u32 s75, 0\ns_cselect_b32 s69, 0, s69\ns_add_u32 s80, s80, s69\n" \
    "s_lshl_b32 s74, s74, 5\ns_and_b32 s74, s74, 0x1000\ns_andn2_b32 s81, s81, 0x1000\ns_or_b32 s81, s81, s74\ns_lshl_b32 s74, s74, 2\ns_andn2_b32 s82, s82, 0x4000\n" \
    "s_or_b32 s82, s82, s74\ns_branch .Lq32_%=\n.Lq43_%=:\nv_mov_b32 v252, 0\nv_max3_u32 v252, v252, v72, v73\nv_max3_u32 v252, v252, v74, v75\nv_max3_u32 v252, v252, v76, v77\n" \
    "v_max3_u32 v252, v252, v78, v79\nv_cmp_eq_u32 vcc, -1, v252\ns_nop 0\ns_cbranch_vccz .Lq39_%=\ns_add_u32 s80, s80, s43\ns_xor_b32 s81, s81, 0x1000\n" \
    "s_xor_b32 s90, s90, 0x1000\ns_xor_b32 s82, s82, 0x4000\ns_cmp_eq_u32 s61, 0\ns_cbranch_scc1 .Lq46_%=\ns_lshr_b32 s69, s81, 5\ns_and_b32 s69, s69, 128\n" \
    "s_lshl_b32 s74, s80, 8\ns_add_u32 s74, s74, s69\ns_add_u32 s74, s74, s35\nv_mov_b32 v253, s74\nv_lshlrev_b32 v252, 7, v2\ns_mov_b64 exec, 1\n" \
    "buffer_store_dword v253, v252, s[64:67], s61 offen sc1\ns_mov_b64 exec, -1\n.Lq46_%=:\ns_add_u32 s68, s68, 1\ns_and_b32 s69, s68, 63\ns_cbranch_scc1 .Lq33_%=\n" \
    "s_cmp_gt_u32 s68, 0x400000\ns_cbranch_scc1 .Lq1_%=\nbuffer_load_dword v253, off, s[64:67], 0 sc1\ns_waitcnt vmcnt(0)\nv_readfirstlane_b32 s69, v253\ns_nop 0\n" \
    "s_cmp_eq_u32 s69, 1\ns_cbranch_scc0 .Lq33_%=\ns_endpgm\n.Lq33_%=:\ns_memrealtime s[72:73]\ns_waitcnt lgkmcnt(0)\ns_sub_u32 s72, s72, s80\ns_cmp_lt_i32 s72, 0\n" \
    "s_cbranch_scc1 .Lq33_%=\nbuffer_load_dwordx4 v[72:75], v2, s[64:67], s81 offen offset:0 sc1\ns_nop 7\n" \
    "buffer_load_dwordx4 v[76:79], v2, s[64:67], s81 offen offset:1024 sc1\ns_nop 7\nbuffer_load_dwordx4 v[80:83], v2, s[64:67], s90 offen offset:0 sc1\ns_nop 7\n" \
    "buffer_load_dwordx4 v[84:87], v2, s[64:67], s90 offen offset:1024 sc1\ns_nop 7\ns_branch .Lq36_%=\n.Lq44_%=:\nv_mov_b32 v252, 0\nv_max3_u32 v252, v252, v80, v81\n" \
    "v_max3_u32 v252, v252, v82, v83\nv_cmp_eq_u32 vcc, -1, v252\ns_nop 0\ns_cbranch_vccz .Lq40_%=\ns_add_u32 s80, s80, s43\ns_xor_b32 s81, s81, 0x1000\n" \
    "s_xor_b32 s90, s90, 0x1000\ns_xor_b32 s82, s82, 0x4000\ns_cmp_eq_u32 s61, 0\ns_cbranch_scc1 .Lq48_%=\ns_lshr_b32 s69, s81, 5\ns_and_b32 s69, s69, 128\n" \
    "s_lshl_b32 s74, s80, 8\ns_add_u32 s74, s74, s69\ns_add_u32 s74, s74, s35\nv_mov_b32 v253, s74\nv_lshlrev_b32 v252, 7, v2\ns_mov_b64 exec, 1\n" \
    "buffer_store_dword v253, v252, s[64:67], s61 offen sc1\ns_mov_b64 exec, -1\n.Lq48_%=:\n.Lq47_%=:\ns_memrealtime s[72:73]\ns_waitcnt vmcnt(0) lgkmcnt(0)\n" \
    "s_sub_u32 s72, s72, s80\ns_cmp_lt_i32 s72, 0\ns_cbranch_scc1 .Lq47_%=\nbuffer_load_dwordx4 v[80:83], v2, s[64:67], s90 offen offset:0 sc1\n" \
    "buffer_load_dwordx4 v[84:87], v2, s[64:67], s90 offen offset:1024 sc1\ns_waitcnt vmcnt(0)\ns_add_u32 s68, s68, 1\ns_and_b32 s69, s68, 63\ns_cbranch_scc1 .Lq44_%=\n" \
    "s_cmp_gt_u32 s68, 0x400000\ns_cbranch_scc1 .Lq1_%=\nbuffer_load_dword v253, off, s[64:67], 0 sc1\ns_waitcnt vmcnt(0)\nv_readfirstlane_b32 s69, v253\ns_nop 0\n" \
    "s_cmp_eq_u32 s69, 1\ns_cbranch_scc0 .Lq44_%=\ns_endpgm\n.Lq45_%=:\nv_mov_b32 v252, 0\nv_max3_u32 v252, v252, v84, v85\nv_max3_u32 v252, v252, v86, v87\n" \
    "v_cmp_eq_u32 vcc, -1, v252\ns_nop 0\ns_cbranch_vccz .Lq41_%=\ns_add_u32 s80, s80, s43\ns_xor_b32 s81, s81, 0x1000\ns_xor_b32 s90, s90, 0x1000\ns_xor_b32 s82, s82, 0x4000\n" \
    "s_cmp_eq_u32 s61, 0\ns_cbranch_scc1 .Lq50_%=\ns_lshr_b32 s69, s81, 5\ns_and_b32 s69, s69, 128\ns_lshl_b32 s74, s80, 8\ns_add_u32 s74, s74, s69\ns_add_u32 s74, s74, s35\n" \
    "v_mov_b32 v253, s74\nv_lshlrev_b32 v252, 7, v2\ns_mov_b64 exec, 1\nbuffer_store_dword v253, v252, s[64:67], s61 offen sc1\ns_mov_b64 exec, -1\n.Lq50_%=:\n.Lq49_%=:\n" \
    "s_memrealtime s[72:73]\ns_waitcnt vmcnt(0) lgkmcnt(0)\ns_sub_u32 s72, s72, s80\ns_cmp_lt_i32 s72, 0\ns_cbranch_scc1 .Lq49_%=\n" \
    "buffer_load_dwordx4 v[84:87], v2, s[64:67], s90 offen offset:1024 sc1\ns_waitcnt vmcnt(0)\ns_add_u32 s68, s68, 1\ns_and_b32 s69, s68, 63\ns_cbranch_scc1 .Lq45_%=\n" \
    "s_cmp_gt_u32 s68, 0x400000\ns_cbranch_scc1 .Lq1_%=\nbuffer_load_dword v253, off, s[64:67], 0 sc1\ns_waitcnt vmcnt(0)\nv_readfirstlane_b32 s69, v253\ns_nop 0\n" \
    "s_cmp_eq_u32 s69, 1\ns_cbranch_scc0 .Lq45_%=\ns_endpgm\n.Lq54_%=:\ns_memrealtime s[72:73]\ns_waitcnt lgkmcnt(0)\ns_sub_u32 s72, s72, s76\ns_cmp_lt_i32 s72, 0\n" \
    "s_cbranch_scc1 .Lq54_%=\nbuffer_load_dword v13, off, s[64:67], s42 sc1\ns_waitcnt vmcnt(0)\nv_readfirstlane_b32 s74, v13\ns_nop 0\ns_and_b32 s75, s74, 127\n" \
    "s_lshr_b32 s69, s74, 8\ns_sub_u32 s69, s69, s80\ns_bfe_i32 s69, s69, 0x180000\ns_cmp_eq_u32 s75, 0\ns_cselect_b32 s69, 0, s69\ns_add_u32 s80, s80, s69\n" \
    "s_lshl_b32 s74, s74, 5\ns_and_b32 s74, s74, 0x1000\ns_andn2_b32 s81, s81, 0x1000\ns_or_b32 s81, s81, s74\ns_lshl_b32 s74, s74, 2\ns_andn2_b32 s82, s82, 0x4000\n" \
    "s_or_b32 s82, s82, s74\ns_branch .Lq51_%=\n.Lq62_%=:\nv_mov_b32 v252, 0\nv_max3_u32 v252, v252, v72, v73\nv_max3_u32 v252, v252, v74, v75\nv_max3_u32 v252, v252, v76, v77\n" \
    "v_max3_u32 v252, v252, v78, v79\nv_cmp_eq_u32 vcc, -1, v252\ns_nop 0\ns_cbranch_vccz .Lq58_%=\ns_add_u32 s80, s80, s43\ns_xor_b32 s81, s81, 0x1000\n" \
    "s_xor_b32 s90, s90, 0x1000\ns_xor_b32 s82, s82, 0x4000\ns_cmp_eq_u32 s61, 0\ns_cbranch_scc1 .Lq65_%=\ns_lshr_b32 s69, s81, 5\ns_and_b32 s69, s69, 128\n" \
    "s_lshl_b32 s74, s80, 8\ns_add_u32 s74, s74, s69\ns_add_u32 s74, s74, s35\nv_mov_b32 v253, s74\nv_lshlrev_b32 v252, 7, v2\ns_mov_b64 exec, 1\n" \
    "buffer_store_dword v253, v252, s[64:67], s61 offen sc1\ns_mov_b64 exec, -1\n.Lq65_%=:\ns_add_u32 s68, s68, 1\ns_and_b32 s69, s68, 63\ns_cbranch_scc1 .Lq52_%=\n" \
    "s_cmp_gt_u32 s68, 0x400000\ns_cbranch_scc1 .Lq1_%=\nbuffer_load_dword v253, off, s[64:67], 0 sc1\ns_waitcnt vmcnt(0)\nv_readfirstlane_b32 s69, v253\ns_nop 0\n" \
    "s_cmp_eq_u32 s69, 1\ns_cbranch_scc0 .Lq52_%=\ns_endpgm\n.Lq52_%=:\ns_memrealtime s[72:73]\ns_waitcnt lgkmcnt(0)\ns_sub_u32 s72, s72, s80\ns_cmp_lt_i32 s72, 0\n" \
    "s_cbranch_scc1 .Lq52_%=\nbuffer_load_dwordx4 v[72:75], v2, s[64:67], s81 offen offset:0 sc1\ns_nop 7\n" \
    "buffer_load_dwordx4 v[76:79], v2, s[64:67], s81 offen offset:1024 sc1\ns_nop 7\nbuffer_load_dwordx4 v[80:83], v2, s[64:67], s90 offen offset:0 sc1\ns_nop 7\n" \
    "buffer_load_dwordx4 v[84:87], v2, s[64:67], s90 offen offset:1024 sc1\ns_nop 7\ns_branch .Lq55_%=\n.Lq63_%=:\nv_mov_b32 v252, 0\nv_max3_u32 v252, v252, v80, v81\n" \
    "v_max3_u32 v252, v252, v82, v83\nv_cmp_eq_u32 vcc, -1, v252\ns_nop 0\ns_cbranch_vccz .Lq59_%=\ns_add_u32 s80, s80, s43\ns_xor_b32 s81, s81, 0x1000\n" \
    "s_xor_b32 s90, s90, 0x1000\ns_xor_b32 s82, s82, 0x4000\ns_cmp_eq_u32 s61, 0\ns_cbranch_scc1 .Lq67_%=\ns_lshr_b32 s69, s81, 5\ns_and_b32 s69, s69, 128\n" \
    "s_lshl_b32 s74, s80, 8\ns_add_u32 s74, s74, s69\ns_add_u32 s74, s74, s35\nv_mov_b32 v253, s74\nv_lshlrev_b32 v252, 7, v2\ns_mov_b64 exec, 1\n" \
    "buffer_store_dword v253, v252, s[64:67], s61 offen sc1\ns_mov_b64 exec, -1\n.Lq67_%=:\n.Lq66_%=:\ns_memrealtime s[72:73]\ns_waitcnt vmcnt(0) lgkmcnt(0)\n" \
    "s_sub_u32 s72, s72, s80\ns_cmp_lt_i32 s72, 0\ns_cbranch_scc1 .Lq66_%=\nbuffer_load_dwordx4 v[80:83], v2, s[64:67], s90 offen offset:0 sc1\n" \
    "buffer_load_dwordx4 v[84:87], v2, s[64:67], s90 offen offset:1024 sc1\ns_waitcnt vmcnt(0)\ns_add_u32 s68, s68, 1\ns_and_b32 s69, s68, 63\ns_cbranch_scc1 .Lq63_%=\n" \
    "s_cmp_gt_u32 s68, 0x400000\ns_cbranch_scc1 .Lq1_%=\nbuffer_load_dword v253, off, s[64:67], 0 sc1\ns_waitcnt vmcnt(0)\nv_readfirstlane_b32 s69, v253\ns_nop 0\n" \
    "s_cmp_eq_u32 s69, 1\ns_cbranch_scc0 .Lq63_%=\ns_endpgm\n.Lq64_%=:\nv_mov_b32 v252, 0\nv_max3_u32 v252, v252, v84, v85\nv_max3_u32 v252, v252, v86, v87\n" \
    "v_cmp_eq_u32 vcc, -1, v252\ns_nop 0\ns_cbranch_vccz .Lq60_%=\ns_add_u32 s80, s80, s43\ns_xor_b32 s81, s81, 0x1000\ns_xor_b32 s90, s90, 0x1000\ns_xor_b32 s82, s82, 0x4000\n" \
    "s_cmp_eq_u32 s61, 0\ns_cbranch_scc1 .Lq69_%=\ns_lshr_b32 s69, s81, 5\ns_and_b32 s69, s69, 128\ns_lshl_b32 s74, s80, 8\ns_add_u32 s74, s74, s69\ns_add_u32 s74, s74, s35\n" \
    "v_mov_b32 v253, s74\nv_lshlrev_b32 v252, 7, v2\ns_mov_b64 exec, 1\nbuffer_store_dword v253, v252, s[64:67], s61 offen sc1\ns_mov_b64 exec, -1\n.Lq69_%=:\n.Lq68_%=:\n" \
    "s_memrealtime s[72:73]\ns_waitcnt vmcnt(0) lgkmcnt(0)\ns_sub_u32 s72, s72, s80\ns_cmp_lt_i32 s72, 0\ns_cbranch_scc1 .Lq68_%=\n" \
    "buffer_load_dwordx4 v[84:87], v2, s[64:67], s90 offen offset:1024 sc1\ns_waitcnt vmcnt(0)\ns_add_u32 s68, s68, 1\ns_and_b32 s69, s68, 63\ns_cbranch_scc1 .Lq64_%=\n" \
    "s_cmp_gt_u32 s68, 0x400000\ns_cbranch_scc1 .Lq1_%=\nbuffer_load_dword v253, off, s[64:67], 0 sc1\ns_waitcnt vmcnt(0)\nv_readfirstlane_b32 s69, v253\ns_nop 0\n" \
    "s_cmp_eq_u32 s69, 1\ns_cbranch_scc0 .Lq64_%=\ns_endpgm\n.Lq73_%=:\ns_memrealtime s[72:73]\ns_waitcnt lgkmcnt(0)\ns_sub_u32 s72, s72, s76\ns_cmp_lt_i32 s72, 0\n" \
    "s_cbranch_scc1 .Lq73_%=\nbuffer_load_dword v13, off, s[64:67], s42 sc1\ns_waitcnt vmcnt(0)\nv_readfirstlane_b32 s74, v13\ns_nop 0\ns_and_b32 s75, s74, 127\n" \
    "s_lshr_b32 s69, s74, 8\ns_sub_u32 s69, s69, s80\ns_bfe_i32 s69, s69, 0x180000\ns_cmp_eq_u32 s75, 0\ns_cselect_b32 s69, 0, s69\ns_add_u32 s80, s80, s69\n" \
    "s_lshl_b32 s74, s74, 5\ns_and_b32 s74, s74, 0x1000\ns_andn2_b32 s81, s81, 0x1000\ns_or_b32 s81, s81, s74\ns_lshl_b32 s74, s74, 2\ns_andn2_b32 s82, s82, 0x4000\n" \
    "s_or_b32 s82, s82, s74\ns_branch .Lq70_%=\n.Lq81_%=:\nv_mov_b32 v252, 0\nv_max3_u32 v252, v252, v72, v73\nv_max3_u32 v252, v252, v74, v75\nv_max3_u32 v252, v252, v76, v77\n" \
    "v_max3_u32 v252, v252, v78, v79\nv_cmp_eq_u32 vcc, -1, v252\ns_nop 0\ns_cbranch_vccz .Lq77_%=\ns_add_u32 s80, s80, s43\ns_xor_b32 s81, s81, 0x1000\n" \
    "s_xor_b32 s90, s90, 0x1000\ns_xor_b32 s82, s82, 0x4000\ns_cmp_eq_u32 s61, 0\ns_cbranch_scc1 .Lq84_%=\ns_lshr_b32 s69, s81, 5\ns_and_b32 s69, s69, 128\n" \
    "s_lshl_b32 s74, s80, 8\ns_add_u32 s74, s74, s69\ns_add_u32 s74, s74, s35\nv_mov_b32 v253, s74\nv_lshlrev_b32 v252, 7, v2\ns_mov_b64 exec, 1\n" \
    "buffer_store_dword v253, v252, s[64:67], s61 offen sc1\ns_mov_b64 exec, -1\n.Lq84_%=:\ns_add_u32 s68, s68, 1\ns_and_b32 s69, s68, 63\ns_cbranch_scc1 .Lq71_%=\n" \
    "s_cmp_gt_u32 s68, 0x400000\ns_cbranch_scc1 .Lq1_%=\nbuffer_load_dword v253, off, s[64:67], 0 sc1\ns_waitcnt vmcnt(0)\nv_readfirstlane_b32 s69, v253\ns_nop 0\n" \
    "s_cmp_eq_u32 s69, 1\ns_cbranch_scc0 .Lq71_%=\ns_endpgm\n.Lq71_%=:\ns_memrealtime s[72:73]\ns_waitcnt lgkmcnt(0)\ns_sub_u32 s72, s72, s80\ns_cmp_lt_i32 s72, 0\n" \
    "s_cbranch_scc1 .Lq71_%=\nbuffer_load_dwordx4 v[72:75], v2, s[64:67], s81 offen offset:0 sc1\ns_nop 7\n" \
    "buffer_load_dwordx4 v[76:79], v2, s[64:67], s81 offen offset:1024 sc1\ns_nop 7\nbuffer_load_dwordx4 v[80:83], v2, s[64:67], s90 offen offset:0 sc1\ns_nop 7\n" \
    "buffer_load_dwordx4 v[84:87], v2, s[64:67], s90 offen offset:1024 sc1\ns_nop 7\ns_branch .Lq74_%=\n.Lq82_%=:\nv_mov_b32 v252, 0\nv_max3_u32 v252, v252, v80, v81\n" \
    "v_max3_u32 v252, v252, v82, v83\nv_cmp_eq_u32 vcc, -1, v252\ns_nop 0\ns_cbranch_vccz .Lq78_%=\ns_add_u32 s80, s80, s43\ns_xor_b32 s81, s81, 0x1000\n" \
    "s_xor_b32 s90, s90, 0x1000\ns_xor_b32 s82, s82, 0x4000\ns_cmp_eq_u32 s61, 0\ns_cbranch_scc1 .Lq86_%=\ns_lshr_b32 s69, s81, 5\ns_and_b32 s69, s69, 128\n" \
    "s_lshl_b32 s74, s80, 8\ns_add_u32 s74, s74, s69\ns_add_u32 s74, s74, s35\nv_mov_b32 v253, s74\nv_lshlrev_b32 v252, 7, v2\ns_mov_b64 exec, 1\n" \
    "buffer_store_dword v253, v252, s[64:67], s61 offen sc1\ns_mov_b64 exec, -1\n.Lq86_%=:\n.Lq85_%=:\ns_memrealtime s[72:73]\ns_waitcnt vmcnt(0) lgkmcnt(0)\n" \
    "s_sub_u32 s72, s72, s80\ns_cmp_lt_i32 s72, 0\ns_cbranch_scc1 .Lq85_%=\nbuffer_load_dwordx4 v[80:83], v2, s[64:67], s90 offen offset:0 sc1\n" \
    "buffer_load_dwordx4 v[84:87], v2, s[64:67], s90 offen offset:1024 sc1\ns_waitcnt vmcnt(0)\ns_add_u32 s68, s68, 1\ns_and_b32 s69, s68, 63\ns_cbranch_scc1 .Lq82_%=\n" \
    "s_cmp_gt_u32 s68, 0x400000\ns_cbranch_scc1 .Lq1_%=\nbuffer_load_dword v253, off, s[64:67], 0 sc1\ns_waitcnt vmcnt(0)\nv_readfirstlane_b32 s69, v253\ns_nop 0\n" \
    "s_cmp_eq_u32 s69, 1\ns_cbranch_scc0 .Lq82_%=\ns_endpgm\n.Lq83_%=:\nv_mov_b32 v252, 0\nv_max3_u32 v252, v252, v84, v85\nv_max3_u32 v252, v252, v86, v87\n" \
    "v_cmp_eq_u32 vcc, -1, v252\ns_nop 0\ns_cbranch_vccz .Lq79_%=\ns_add_u32 s80, s80, s43\ns_xor_b32 s81, s81, 0x1000\ns_xor_b32 s90, s90, 0x1000\ns_xor_b32 s82, s82, 0x4000\n" \
    "s_cmp_eq_u32 s61, 0\ns_cbranch_scc1 .Lq88_%=\ns_lshr_b32 s69, s81, 5\ns_and_b32 s69, s69, 128\ns_lshl_b32 s74, s80, 8\ns_add_u32 s74, s74, s69\ns_add_u32 s74, s74, s35\n" \
    "v_mov_b32 v253, s74\nv_lshlrev_b32 v252, 7, v2\ns_mov_b64 exec, 1\nbuffer_store_dword v253, v252, s[64:67], s61 offen sc1\ns_mov_b64 exec, -1\n.Lq88_%=:\n.Lq87_%=:\n" \
    "s_memrealtime s[72:73]\ns_waitcnt vmcnt(0) lgkmcnt(0)\ns_sub_u32 s72, s72, s80\ns_cmp_lt_i32 s72, 0\ns_cbranch_scc1 .Lq87_%=\n" \
    "buffer_load_dwordx4 v[84:87], v2, s[64:67], s90 offen offset:1024 sc1\ns_waitcnt vmcnt(0)\ns_add_u32 s68, s68, 1\ns_and_b32 s69, s68, 63\ns_cbranch_scc1 .Lq83_%=\n" \
    "s_cmp_gt_u32 s68, 0x400000\ns_cbranch_scc1 .Lq1_%=\nbuffer_load_dword v253, off, s[64:67], 0 sc1\ns_waitcnt vmcnt(0)\nv_readfirstlane_b32 s69, v253\ns_nop 0\n" \
    "s_cmp_eq_u32 s69, 1\ns_cbranch_scc0 .Lq83_%=\ns_endpgm\n.Lq12_%=:\ns_mov_b32 s68, 0\n.Lq89_%=:\nds_read_b128 v[36:39], v6 offset:0\nds_read_b128 v[44:47], v8 offset:0\n" \
    "ds_read_b128 v[64:67], v6 offset:512\nds_read_b128 v[84:87], v8 offset:512\nds_read_b128 v[68:71], v6 offset:1024\nds_read_b128 v[88:91], v8 offset:1024\n" \
    "ds_read_b128 v[40:43], v6 offset:1536\nds_read_b128 v[28:31], v8 offset:1536\nv_mov_b32 v252, 0\ns_waitcnt lgkmcnt(0)\nv_pk_max_u16 v252, v252, v36\n" \
    "v_pk_max_u16 v252, v252, v37\nv_pk_max_u16 v252, v252, v38\nv_pk_max_u16 v252, v252, v39\nv_pk_max_u16 v252, v252, v64\nv_pk_max_u16 v252, v252, v65\n" \
    "v_pk_max_u16 v252, v252, v66\nv_pk_max_u16 v252, v252, v67\nv_pk_max_u16 v252, v252, v68\nv_pk_max_u16 v252, v252, v69\nv_pk_max_u16 v252, v252, v70\n" \
    "v_pk_max_u16 v252, v252, v71\nv_pk_max_u16 v252, v252, v40\nv_pk_max_u16 v252, v252, v41\nv_pk_max_u16 v252, v252, v42\nv_pk_max_u16 v252, v252, v43\n" \
    "v_pk_max_u16 v252, v252, v44\nv_pk_max_u16 v252, v252, v45\nv_pk_max_u16 v252, v252, v46\nv_pk_max_u16 v252, v252, v47\nv_pk_max_u16 v252, v252, v84\n" \
    "v_pk_max_u16 v252, v252, v85\nv_pk_max_u16 v252, v252, v86\nv_pk_max_u16 v252, v252, v87\nv_pk_max_u16 v252, v252, v88\nv_pk_max_u16 v252, v252, v89\n" \
    "v_pk_max_u16 v252, v252, v90\nv_pk_max_u16 v252, v252, v91\nv_pk_max_u16 v252, v252, v28\nv_pk_max_u16 v252, v252, v29\nv_pk_max_u16 v252, v252, v30\n" \
    "v_pk_max_u16 v252, v252, v31\nv_pk_max_u16 v252, v252, v252 op_sel:[0,1] op_sel_hi:[1,0]\nv_cmp_eq_u32 vcc, -1, v252\ns_nop 0\ns_cbranch_vccnz .Lq90_%=\n" \
    "v_mfma_f32_16x16x32_bf16 v[16:19], v[48:51], v[36:39], 0\nv_mfma_f32_16x16x32_bf16 v[20:23], v[52:55], v[36:39], 0\n" \
    "v_mfma_f32_16x16x32_bf16 v[16:19], v[72:75], v[64:67], v[16:19]\nv_mfma_f32_16x16x32_bf16 v[20:23], v[76:79], v[64:67], v[20:23]\n" \
    "v_mfma_f32_16x16x32_bf16 v[16:19], v[80:83], v[68:71], v[16:19]\nv_mfma_f32_16x16x32_bf16 v[20:23], v[56:59], v[68:71], v[20:23]\n" \
    "v_mfma_f32_16x16x32_bf16 v[16:19], v[60:63], v[40:43], v[16:19]\nv_mfma_f32_16x16x32_bf16 v[20:23], v[32:35], v[40:43], v[20:23]\n" \
    "v_mfma_f32_16x16x32_bf16 v[24:27], v[44:47], v[36:39], 0\nv_mfma_f32_16x16x32_bf16 v[24:27], v[84:87], v[64:67], v[24:27]\n" \
    "v_mfma_f32_16x16x32_bf16 v[24:27], v[88:91], v[68:71], v[24:27]\nv_mfma_f32_16x16x32_bf16 v[24:27], v[28:31], v[40:43], v[24:27]\ns_nop 3\n" \
    "v_mov_b32_dpp v16, v20 quad_perm:[0,1,2,3] row_mask:0xf bank_mask:0xc\nv_mov_b32_dpp v17, v21 quad_perm:[0,1,2,3] row_mask:0xf bank_mask:0xc\n" \
    "v_mov_b32_dpp v18, v22 quad_perm:[0,1,2,3] row_mask:0xf bank_mask:0xc\nv_mov_b32_dpp v19, v23 quad_perm:[0,1,2,3] row_mask:0xf bank_mask:0xc\n" \
    "v_max3_f32 v254, v16, v17, v18\nv_max_f32 v254, v254, v19\ns_nop 1\nv_max_f32_dpp v252, v254, v254 row_ror:8 row_mask:0xf bank_mask:0xf\n" \
    "v_max_f32_dpp v253, v254, v254 row_ror:8 row_mask:0xf bank_mask:0xf\ns_nop 1\nv_permlane32_swap_b32 v252, v253\nv_max_f32 v252, v252, v253\n" \
    "v_max3_f32 v254, v252, v24, v25\nv_max3_f32 v252, v254, v26, v27\nv_max3_f32 v253, v254, v26, v27\ns_nop 1\nv_permlane16_swap_b32 v252, v253\nv_max_f32 v252, v252, v253\n" \
    "v_pk_add_f32 v[16:17], v[16:17], v[252:253] op_sel_hi:[1,0] neg_lo:[0,1] neg_hi:[0,1]\n" \
    "v_pk_add_f32 v[18:19], v[18:19], v[252:253] op_sel_hi:[1,0] neg_lo:[0,1] neg_hi:[0,1]\n" \
    "v_pk_add_f32 v[24:25], v[24:25], v[252:253] op_sel_hi:[1,0] neg_lo:[0,1] neg_hi:[0,1]\n" \
    "v_pk_add_f32 v[26:27], v[26:27], v[252:253] op_sel_hi:[1,0] neg_lo:[0,1] neg_hi:[0,1]\nv_exp_f32 v16, v16\nv_exp_f32 v17, v17\nv_exp_f32 v18, v18\nv_exp_f32 v19, v19\n" \
    "v_exp_f32 v24, v24\nv_pk_add_f32 v[254:255], v[16:17], v[18:19]\nv_exp_f32 v25, v25\nv_add_f32 v254, v254, v255\nv_exp_f32 v26, v26\nv_exp_f32 v27, v27\n" \
    "v_add_f32_dpp v252, v254, v254 row_ror:8 row_mask:0xf bank_mask:0xf\nv_add_f32_dpp v253, v254, v254 row_ror:8 row_mask:0xf bank_mask:0xf\n" \
    "v_pk_add_f32 v[254:255], v[24:25], v[26:27]\ns_nop 0\nv_permlane32_swap_b32 v252, v253\nv_pk_add_f32 v[254:255], v[254:255], v[252:253]\n" \
    "v_pk_add_f32 v[252:253], v[254:255], v[254:255] op_sel:[0,1] op_sel_hi:[1,0]\ns_nop 1\nv_permlane16_swap_b32 v252, v253\nv_add_f32 v252, v252, v253\nv_rcp_f32 v252, v252\n" \
    "s_nop 0\nv_pk_mul_f32 v[16:17], v[16:17], v[252:253] op_sel_hi:[1,0]\nv_pk_mul_f32 v[24:25], v[24:25], v[252:253] op_sel_hi:[1,0]\nv_cvt_pk_bf16_f32 v20, v16, v17\n" \
    "v_cvt_pk_bf16_f32 v22, v24, v25\nv_pk_mul_f32 v[18:19], v[18:19], v[252:253] op_sel_hi:[1,0]\nv_pk_mul_f32 v[26:27], v[26:27], v[252:253] op_sel_hi:[1,0]\n" \
    "v_cvt_pk_bf16_f32 v21, v18, v19\nv_cvt_pk_bf16_f32 v23, v26, v27\nv_permlane32_swap_b32 v20, v22\ns_nop 0\nv_permlane32_swap_b32 v21, v23\n" \
    "buffer_store_dwordx4 v[20:23], v7, s[64:67], s82 offen sc1\nbuffer_store_dwordx4 v[20:23], v7, s[64:67], s74 offen sc1\ns_branch .Lq3_%=\n.Lq90_%=:\ns_add_u32 s68, s68, 1\n" \
    "s_and_b32 s69, s68, 63\ns_cbranch_scc1 .Lq89_%=\ns_cmp_gt_u32 s68, 0x400000\ns_cbranch_scc1 .Lq1_%=\nbuffer_load_dword v253, off, s[64:67], 0 sc1\ns_waitcnt vmcnt(0)\n" \
    "v_readfirstlane_b32 s69, v253\ns_nop 0\ns_cmp_eq_u32 s69, 1\ns_cbranch_scc0 .Lq89_%=\ns_endpgm\n.Lq1_%=:\nv_mov_b32 v253, 1\nbuffer_store_dword v253, off, s[64:67], 0 sc1\n" \
    "s_endpgm\n"
#define ASM_V \
    "s_add_u32 m0, s41, 0\ns_xor_b32 s85, s62, 0\ns_add_u32 s85, s85, s40\nbuffer_load_dwordx4 v11, s[64:67], s85 offen sc1 lds\n" \
    "buffer_load_dwordx4 v12, s[64:67], s85 offen offset:1024 sc1 lds\ns_add_u32 m0, s41, 8192\ns_xor_b32 s85, s62, 256\ns_add_u32 s85, s85, s40\n" \
    "buffer_load_dwordx4 v11, s[64:67], s85 offen sc1 lds\nbuffer_load_dwordx4 v12, s[64:67], s85 offen offset:1024 sc1 lds\ns_add_u32 m0, s41, 16384\ns_xor_b32 s85, s62, 512\n" \
    "s_add_u32 s85, s85, s40\nbuffer_load_dwordx4 v11, s[64:67], s85 offen sc1 lds\nbuffer_load_dwordx4 v12, s[64:67], s85 offen offset:1024 sc1 lds\ns_add_u32 m0, s41, 40960\n" \
    "s_xor_b32 s85, s62, 768\ns_add_u32 s85, s85, s40\nbuffer_load_dwordx4 v11, s[64:67], s85 offen sc1 lds\nbuffer_load_dwordx4 v12, s[64:67], s85 offen offset:1024 sc1 lds\n" \
    "s_memrealtime s[72:73]\ns_waitcnt lgkmcnt(0)\ns_mov_b32 s80, s72\ns_waitcnt vmcnt(0)\ns_barrier\nds_read_b128 v[56:59], v3 offset:0\nds_read_b128 v[60:63], v9 offset:0\n" \
    "ds_read_b128 v[64:67], v9 offset:4096\nds_read_b128 v[68:71], v3 offset:1024\nds_read_b128 v[72:75], v10 offset:0\nds_read_b128 v[76:79], v10 offset:4096\n" \
    "s_waitcnt lgkmcnt(3)\nv_mfma_f32_16x16x32_bf16 v[16:19], a[0:3], v[60:63], 0\ns_add_u32 m0, s41, 24576\ns_xor_b32 s85, s62, 1024\n" \
    "v_mfma_f32_16x16x32_bf16 v[24:27], v[56:59], v[60:63], 0\nds_read_b128 v[60:63], v3 offset:2048\ns_add_u32 s85, s85, s40\n" \
    "v_mfma_f32_16x16x32_bf16 v[20:23], a[0:3], v[64:67], 0\nds_read_b128 v[80:83], v9 offset:1024\nbuffer_load_dwordx4 v11, s[64:67], s85 offen sc1 lds\n" \
    "v_mfma_f32_16x16x32_bf16 v[28:31], v[56:59], v[64:67], 0\nds_read_b128 v[56:59], v9 offset:5120\ns_waitcnt lgkmcnt(3)\n" \
    "v_mfma_f32_16x16x32_bf16 v[16:19], a[4:7], v[72:75], v[16:19]\nv_mfma_f32_16x16x32_bf16 v[24:27], v[68:71], v[72:75], v[24:27]\nds_read_b128 v[64:67], v3 offset:3072\n" \
    "buffer_load_dwordx4 v12, s[64:67], s85 offen offset:1024 sc1 lds\nv_mfma_f32_16x16x32_bf16 v[20:23], a[4:7], v[76:79], v[20:23]\nds_read_b128 v[72:75], v10 offset:1024\n" \
    "s_add_u32 m0, s41, 32768\nv_mfma_f32_16x16x32_bf16 v[28:31], v[68:71], v[76:79], v[28:31]\nds_read_b128 v[68:71], v10 offset:5120\ns_xor_b32 s85, s62, 1280\n" \
    "s_waitcnt lgkmcnt(3)\nv_mfma_f32_16x16x32_bf16 v[16:19], a[8:11], v[80:83], v[16:19]\ns_add_u32 s85, s85, s40\nbuffer_load_dwordx4 v11, s[64:67], s85 offen sc1 lds\n" \
    "v_mfma_f32_16x16x32_bf16 v[24:27], v[60:63], v[80:83], v[24:27]\nds_read_b128 v[76:79], v3 offset:4096\nv_mfma_f32_16x16x32_bf16 v[20:23], a[8:11], v[56:59], v[20:23]\n" \
    "ds_read_b128 v[80:83], v9 offset:8192\nv_mfma_f32_16x16x32_bf16 v[28:31], v[60:63], v[56:59], v[28:31]\nds_read_b128 v[56:59], v9 offset:12288\n" \
    "buffer_load_dwordx4 v12, s[64:67], s85 offen offset:1024 sc1 lds\ns_waitcnt lgkmcnt(3)\nv_mfma_f32_16x16x32_bf16 v[16:19], a[12:15], v[72:75], v[16:19]\n" \
    "s_add_u32 s84, s40, 0x20000\ns_cmp_lt_u32 s49, 62\ns_cselect_b32 s84, s84, s40\nv_mfma_f32_16x16x32_bf16 v[24:27], v[64:67], v[72:75], v[24:27]\n" \
    "ds_read_b128 v[60:63], v3 offset:5120\nv_mov_b64 v[252:253], -1\nv_mov_b64 v[254:255], -1\nv_mfma_f32_16x16x32_bf16 v[20:23], a[12:15], v[68:71], v[20:23]\n" \
    "ds_read_b128 v[72:75], v10 offset:8192\ns_and_b32 s69, s49, 15\nv_mfma_f32_16x16x32_bf16 v[28:31], v[64:67], v[68:71], v[28:31]\nds_read_b128 v[64:67], v10 offset:12288\n" \
    "s_lshl_b32 s69, s69, 19\ns_waitcnt lgkmcnt(3)\nv_mfma_f32_16x16x32_bf16 v[16:19], a[16:19], v[80:83], v[16:19]\ns_add_u32 s82, s38, s69\ns_lshr_b32 s69, s82, 8\n" \
    "v_mfma_f32_16x16x32_bf16 v[24:27], v[76:79], v[80:83], v[24:27]\nds_read_b128 v[68:71], v3 offset:6144\ns_and_b32 s69, s69, 0x2000\n" \
    "v_mfma_f32_16x16x32_bf16 v[20:23], a[16:19], v[56:59], v[20:23]\nds_read_b128 v[80:83], v9 offset:9216\ns_xor_b32 s82, s82, s69\n" \
    "v_mfma_f32_16x16x32_bf16 v[28:31], v[76:79], v[56:59], v[28:31]\nds_read_b128 v[56:59], v9 offset:13312\ns_and_b32 s69, s35, 15\ns_waitcnt lgkmcnt(3)\n" \
    "v_mfma_f32_16x16x32_bf16 v[16:19], a[20:23], v[72:75], v[16:19]\ns_lshl_b32 s69, s69, 19\ns_add_u32 s0, s38, s69\n" \
    "v_mfma_f32_16x16x32_bf16 v[24:27], v[60:63], v[72:75], v[24:27]\nds_read_b128 v[72:75], v3 offset:7168\ns_lshr_b32 s69, s0, 8\n" \
    "v_mfma_f32_16x16x32_bf16 v[20:23], a[20:23], v[64:67], v[20:23]\nds_read_b128 v[76:79], v10 offset:9216\ns_and_b32 s69, s69, 0x2000\n" \
    "v_mfma_f32_16x16x32_bf16 v[28:31], v[60:63], v[64:67], v[28:31]\nds_read_b128 v[60:63], v10 offset:13312\ns_xor_b32 s0, s0, s69\ns_waitcnt lgkmcnt(3)\n" \
    "v_mfma_f32_16x16x32_bf16 v[16:19], a[24:27], v[80:83], v[16:19]\ns_add_u32 s1, s0, 0x10000\ns_add_u32 s2, s0, 0x20000\n" \
    "v_mfma_f32_16x16x32_bf16 v[24:27], v[68:71], v[80:83], v[24:27]\nds_read_b128 v[64:67], v3 offset:8192\ns_add_u32 s3, s0, 0x30000\nv_lshrrev_b32 v15, 1, v2\n" \
    "v_mfma_f32_16x16x32_bf16 v[20:23], a[24:27], v[56:59], v[20:23]\nds_read_b128 v[80:83], v9 offset:16384\nv_add_u32 v15, s60, v15\n" \
    "v_mfma_f32_16x16x32_bf16 v[28:31], v[68:71], v[56:59], v[28:31]\nds_read_b128 v[56:59], v9 offset:20480\ns_waitcnt lgkmcnt(3)\n" \
    "v_mfma_f32_16x16x32_bf16 v[16:19], a[28:31], v[76:79], v[16:19]\nds_write_b64 v15, v[252:253]\nv_mfma_f32_16x16x32_bf16 v[24:27], v[72:75], v[76:79], v[24:27]\n" \
    "ds_read_b128 v[68:71], v3 offset:9216\nv_mfma_f32_16x16x32_bf16 v[20:23], a[28:31], v[60:63], v[20:23]\nds_read_b128 v[76:79], v10 offset:16384\n" \
    "v_mfma_f32_16x16x32_bf16 v[28:31], v[72:75], v[60:63], v[28:31]\nds_read_b128 v[60:63], v10 offset:20480\ns_waitcnt lgkmcnt(4)\n" \
    "v_mfma_f32_16x16x32_bf16 v[16:19], a[32:35], v[80:83], v[16:19]\nv_mfma_f32_16x16x32_bf16 v[24:27], v[64:67], v[80:83], v[24:27]\nds_read_b128 v[72:75], v3 offset:10240\n" \
    "v_mfma_f32_16x16x32_bf16 v[20:23], a[32:35], v[56:59], v[20:23]\nds_read_b128 v[80:83], v9 offset:17408\nbuffer_load_dword v13, off, s[64:67], s42 sc1\n" \
    "v_mfma_f32_16x16x32_bf16 v[28:31], v[64:67], v[56:59], v[28:31]\nds_read_b128 v[56:59], v9 offset:21504\ns_and_b32 s69, s49, 255\ns_waitcnt lgkmcnt(3)\n" \
    "v_mfma_f32_16x16x32_bf16 v[16:19], a[36:39], v[76:79], v[16:19]\ns_lshl_b32 s69, s69, 19\ns_add_u32 s74, s63, s69\n" \
    "v_mfma_f32_16x16x32_bf16 v[24:27], v[68:71], v[76:79], v[24:27]\nds_read_b128 v[64:67], v3 offset:11264\ns_cmp_lt_u32 s49, 64\ns_cselect_b32 s74, s74, 0x7fff0000\n" \
    "v_mfma_f32_16x16x32_bf16 v[20:23], a[36:39], v[60:63], v[20:23]\nds_read_b128 v[76:79], v10 offset:17408\nv_mfma_f32_16x16x32_bf16 v[28:31], v[68:71], v[60:63], v[28:31]\n" \
    "ds_read_b128 v[60:63], v10 offset:21504\ns_waitcnt lgkmcnt(3)\nv_mfma_f32_16x16x32_bf16 v[16:19], a[40:43], v[80:83], v[16:19]\n" \
    "v_mfma_f32_16x16x32_bf16 v[24:27], v[72:75], v[80:83], v[24:27]\nds_read_b128 v[68:71], v3 offset:12288\ns_barrier\n" \
    "v_mfma_f32_16x16x32_bf16 v[20:23], a[40:43], v[56:59], v[20:23]\nds_read_b128 v[80:83], v9 offset:40960\nv_mfma_f32_16x16x32_bf16 v[28:31], v[72:75], v[56:59], v[28:31]\n" \
    "ds_read_b128 v[56:59], v9 offset:45056\ns_add_u32 m0, s41, 0\ns_waitcnt lgkmcnt(3)\nv_mfma_f32_16x16x32_bf16 v[16:19], a[44:47], v[76:79], v[16:19]\n" \
    "s_xor_b32 s85, s62, 1536\ns_add_u32 s85, s85, s40\nv_mfma_f32_16x16x32_bf16 v[24:27], v[64:67], v[76:79], v[24:27]\nds_read_b128 v[72:75], v3 offset:13312\n" \
    "buffer_load_dwordx4 v11, s[64:67], s85 offen sc1 lds\nv_mfma_f32_16x16x32_bf16 v[20:23], a[44:47], v[60:63], v[20:23]\nds_read_b128 v[76:79], v10 offset:40960\n" \
    "buffer_store_dwordx4 v[252:255], v2, s[64:67], s74 offen offset:0 sc1\nv_mfma_f32_16x16x32_bf16 v[28:31], v[64:67], v[60:63], v[28:31]\n" \
    "ds_read_b128 v[60:63], v10 offset:45056\ns_waitcnt lgkmcnt(3)\nv_mfma_f32_16x16x32_bf16 v[16:19], a[48:51], v[80:83], v[16:19]\n" \
    "buffer_load_dwordx4 v12, s[64:67], s85 offen offset:1024 sc1 lds\ns_add_u32 m0, s41, 8192\nv_mfma_f32_16x16x32_bf16 v[24:27], v[68:71], v[80:83], v[24:27]\n" \
    "ds_read_b128 v[64:67], v3 offset:14336\ns_xor_b32 s85, s62, 1792\nv_mfma_f32_16x16x32_bf16 v[20:23], a[48:51], v[56:59], v[20:23]\nds_read_b128 v[80:83], v9 offset:41984\n" \
    "s_add_u32 s85, s85, s40\nv_mfma_f32_16x16x32_bf16 v[28:31], v[68:71], v[56:59], v[28:31]\nds_read_b128 v[56:59], v9 offset:46080\n" \
    "buffer_load_dwordx4 v11, s[64:67], s85 offen sc1 lds\ns_waitcnt lgkmcnt(3)\nv_mfma_f32_16x16x32_bf16 v[16:19], a[52:55], v[76:79], v[16:19]\n" \
    "buffer_store_dwordx4 v[252:255], v2, s[64:67], s74 offen offset:1024 sc1\nv_mfma_f32_16x16x32_bf16 v[24:27], v[72:75], v[76:79], v[24:27]\n" \
    "ds_read_b128 v[68:71], v3 offset:15360\nv_mfma_f32_16x16x32_bf16 v[20:23], a[52:55], v[60:63], v[20:23]\nds_read_b128 v[76:79], v10 offset:41984\n" \
    "buffer_load_dwordx4 v12, s[64:67], s85 offen offset:1024 sc1 lds\nv_mfma_f32_16x16x32_bf16 v[28:31], v[72:75], v[60:63], v[28:31]\nds_read_b128 v[60:63], v10 offset:46080\n" \
    "s_waitcnt lgkmcnt(3)\nv_mfma_f32_16x16x32_bf16 v[16:19], a[56:59], v[80:83], v[16:19]\nbuffer_store_dwordx4 v[252:255], v7, s[64:67], s82 offen sc1\n" \
    "s_add_u32 s82, s82, 0x10000\nv_mfma_f32_16x16x32_bf16 v[24:27], v[64:67], v[80:83], v[24:27]\nds_read_b128 v[72:75], v3 offset:16384\ns_waitcnt vmcnt(8)\ns_barrier\n" \
    "v_mfma_f32_16x16x32_bf16 v[20:23], a[56:59], v[56:59], v[20:23]\nds_read_b128 v[80:83], v9 offset:24576\nv_mfma_f32_16x16x32_bf16 v[28:31], v[64:67], v[56:59], v[28:31]\n" \
    "ds_read_b128 v[56:59], v9 offset:28672\ns_waitcnt lgkmcnt(3)\nv_mfma_f32_16x16x32_bf16 v[16:19], a[60:63], v[76:79], v[16:19]\n" \
    "buffer_store_dwordx4 v[252:255], v7, s[64:67], s82 offen sc1\ns_add_u32 s82, s82, 0x10000\nv_mfma_f32_16x16x32_bf16 v[24:27], v[68:71], v[76:79], v[24:27]\n" \
    "ds_read_b128 v[64:67], v3 offset:17408\nv_mfma_f32_16x16x32_bf16 v[20:23], a[60:63], v[60:63], v[20:23]\nds_read_b128 v[76:79], v10 offset:24576\n" \
    "v_mfma_f32_16x16x32_bf16 v[28:31], v[68:71], v[60:63], v[28:31]\nds_read_b128 v[60:63], v10 offset:28672\ns_waitcnt lgkmcnt(3)\n" \
    "v_mfma_f32_16x16x32_bf16 v[16:19], a[64:67], v[80:83], v[16:19]\nbuffer_store_dwordx4 v[252:255], v7, s[64:67], s82 offen sc1\ns_add_u32 s82, s82, 0x10000\n" \
    "v_mfma_f32_16x16x32_bf16 v[24:27], v[72:75], v[80:83], v[24:27]\nds_read_b128 v[68:71], v3 offset:18432\nv_mfma_f32_16x16x32_bf16 v[20:23], a[64:67], v[56:59], v[20:23]\n" \
    "ds_read_b128 v[80:83], v9 offset:25600\nv_mfma_f32_16x16x32_bf16 v[28:31], v[72:75], v[56:59], v[28:31]\nds_read_b128 v[56:59], v9 offset:29696\ns_waitcnt lgkmcnt(3)\n" \
    "v_mfma_f32_16x16x32_bf16 v[16:19], a[68:71], v[76:79], v[16:19]\nbuffer_store_dwordx4 v[252:255], v7, s[64:67], s82 offen sc1\ns_add_u32 s82, s82, 0x10000\n" \
    "v_mfma_f32_16x16x32_bf16 v[24:27], v[64:67], v[76:79], v[24:27]\nds_read_b128 v[72:75], v3 offset:19456\ns_add_u32 s49, s49, 2\n" \
    "v_mfma_f32_16x16x32_bf16 v[20:23], a[68:71], v[60:63], v[20:23]\nds_read_b128 v[76:79], v10 offset:25600\ns_and_b32 s69, s35, 15\n" \
    "v_mfma_f32_16x16x32_bf16 v[28:31], v[64:67], v[60:63], v[28:31]\nds_read_b128 v[60:63], v10 offset:29696\ns_lshl_b32 s69, s69, 17\ns_waitcnt lgkmcnt(3)\n" \
    "v_mfma_f32_16x16x32_bf16 v[16:19], a[72:75], v[80:83], v[16:19]\ns_add_u32 s81, s37, s69\ns_lshr_b32 s69, s81, 8\n" \
    "v_mfma_f32_16x16x32_bf16 v[24:27], v[68:71], v[80:83], v[24:27]\nds_read_b128 v[64:67], v3 offset:20480\ns_and_b32 s69, s69, 0x2000\n" \
    "v_mfma_f32_16x16x32_bf16 v[20:23], a[72:75], v[56:59], v[20:23]\nds_read_b128 v[80:83], v9 offset:32768\ns_xor_b32 s81, s81, s69\n" \
    "v_mfma_f32_16x16x32_bf16 v[28:31], v[68:71], v[56:59], v[28:31]\nds_read_b128 v[56:59], v9 offset:36864\ns_mov_b32 s68, 0\ns_waitcnt lgkmcnt(3)\n" \
    "v_mfma_f32_16x16x32_bf16 v[16:19], a[76:79], v[76:79], v[16:19]\nv_mfma_f32_16x16x32_bf16 v[24:27], v[72:75], v[76:79], v[24:27]\nds_read_b128 v[68:71], v3 offset:21504\n" \
    "v_mfma_f32_16x16x32_bf16 v[20:23], a[76:79], v[60:63], v[20:23]\nds_read_b128 v[76:79], v10 offset:32768\nv_mfma_f32_16x16x32_bf16 v[28:31], v[72:75], v[60:63], v[28:31]\n" \
    "ds_read_b128 v[60:63], v10 offset:36864\ns_waitcnt lgkmcnt(3)\nv_mfma_f32_16x16x32_bf16 v[16:19], a[80:83], v[80:83], v[16:19]\n" \
    "v_mfma_f32_16x16x32_bf16 v[24:27], v[64:67], v[80:83], v[24:27]\nds_read_b128 v[72:75], v3 offset:22528\nv_mfma_f32_16x16x32_bf16 v[20:23], a[80:83], v[56:59], v[20:23]\n" \
    "ds_read_b128 v[80:83], v9 offset:33792\nv_mfma_f32_16x16x32_bf16 v[28:31], v[64:67], v[56:59], v[28:31]\nds_read_b128 v[56:59], v9 offset:37888\ns_waitcnt lgkmcnt(3)\n" \
    "v_mfma_f32_16x16x32_bf16 v[16:19], a[84:87], v[76:79], v[16:19]\nv_mfma_f32_16x16x32_bf16 v[24:27], v[68:71], v[76:79], v[24:27]\nds_read_b128 v[64:67], v3 offset:23552\n" \
    "v_mfma_f32_16x16x32_bf16 v[20:23], a[84:87], v[60:63], v[20:23]\nds_read_b128 v[76:79], v10 offset:33792\nv_mfma_f32_16x16x32_bf16 v[28:31], v[68:71], v[60:63], v[28:31]\n" \
    "ds_read_b128 v[60:63], v10 offset:37888\ns_waitcnt lgkmcnt(3)\nv_mfma_f32_16x16x32_bf16 v[16:19], a[88:91], v[80:83], v[16:19]\n" \
    "v_mfma_f32_16x16x32_bf16 v[24:27], v[72:75], v[80:83], v[24:27]\ns_waitcnt vmcnt(4)\ns_barrier\nv_mfma_f32_16x16x32_bf16 v[20:23], a[88:91], v[56:59], v[20:23]\n" \
    "ds_read_b128 v[68:71], v9 offset:0\nv_mfma_f32_16x16x32_bf16 v[28:31], v[72:75], v[56:59], v[28:31]\nds_read_b128 v[56:59], v9 offset:4096\ns_add_u32 m0, s41, 16384\n" \
    "s_waitcnt lgkmcnt(2)\nv_mfma_f32_16x16x32_bf16 v[16:19], a[92:95], v[76:79], v[16:19]\ns_xor_b32 s85, s62, 512\ns_add_u32 s85, s85, s84\n" \
    "v_mfma_f32_16x16x32_bf16 v[24:27], v[64:67], v[76:79], v[24:27]\nbuffer_load_dwordx4 v11, s[64:67], s85 offen sc1 lds\n" \
    "v_mfma_f32_16x16x32_bf16 v[20:23], a[92:95], v[60:63], v[20:23]\nds_read_b128 v[72:75], v10 offset:0\nv_mfma_f32_16x16x32_bf16 v[28:31], v[64:67], v[60:63], v[28:31]\n" \
    "ds_read_b128 v[60:63], v10 offset:4096\ns_waitcnt lgkmcnt(2)\nv_mfma_f32_16x16x32_bf16 v[16:19], a[96:99], v[68:71], v[16:19]\n" \
    "buffer_load_dwordx4 v12, s[64:67], s85 offen offset:1024 sc1 lds\ns_add_u32 m0, s41, 40960\nv_mfma_f32_16x16x32_bf16 v[24:27], a[104:107], v[68:71], v[24:27]\n" \
    "s_xor_b32 s85, s62, 768\ns_add_u32 s85, s85, s84\nv_mfma_f32_16x16x32_bf16 v[20:23], a[96:99], v[56:59], v[20:23]\nds_read_b128 v[64:67], v9 offset:1024\n" \
    "buffer_load_dwordx4 v11, s[64:67], s85 offen sc1 lds\nv_mfma_f32_16x16x32_bf16 v[28:31], a[104:107], v[56:59], v[28:31]\nds_read_b128 v[56:59], v9 offset:5120\n" \
    "s_waitcnt lgkmcnt(2)\nv_mfma_f32_16x16x32_bf16 v[16:19], a[100:103], v[72:75], v[16:19]\nv_mfma_f32_16x16x32_bf16 v[24:27], a[108:111], v[72:75], v[24:27]\n" \
    "buffer_load_dwordx4 v12, s[64:67], s85 offen offset:1024 sc1 lds\nv_mfma_f32_16x16x32_bf16 v[20:23], a[100:103], v[60:63], v[20:23]\n" \
    "ds_read_b128 v[68:71], v10 offset:1024\nv_mfma_f32_16x16x32_bf16 v[28:31], a[108:111], v[60:63], v[28:31]\nds_read_b128 v[60:63], v10 offset:5120\ns_waitcnt lgkmcnt(2)\n" \
    "v_mfma_f32_16x16x32_bf16 v[16:19], a[112:115], v[64:67], v[16:19]\nv_mfma_f32_16x16x32_bf16 v[24:27], a[120:123], v[64:67], v[24:27]\n" \
    "v_mfma_f32_16x16x32_bf16 v[20:23], a[112:115], v[56:59], v[20:23]\nds_read_b128 v[64:67], v9 offset:8192\n" \
    "v_mfma_f32_16x16x32_bf16 v[28:31], a[120:123], v[56:59], v[28:31]\nds_read_b128 v[56:59], v9 offset:12288\ns_waitcnt lgkmcnt(2)\n" \
    "v_mfma_f32_16x16x32_bf16 v[16:19], a[116:119], v[68:71], v[16:19]\nv_mfma_f32_16x16x32_bf16 v[24:27], a[124:127], v[68:71], v[24:27]\n" \
    "v_mfma_f32_16x16x32_bf16 v[20:23], a[116:119], v[60:63], v[20:23]\nds_read_b128 v[68:71], v10 offset:8192\n" \
    "v_mfma_f32_16x16x32_bf16 v[28:31], a[124:127], v[60:63], v[28:31]\nds_read_b128 v[60:63], v10 offset:12288\ns_waitcnt lgkmcnt(2)\n" \
    "v_mfma_f32_16x16x32_bf16 v[16:19], a[128:131], v[64:67], v[16:19]\nv_readfirstlane_b32 s74, v13\nv_mfma_f32_16x16x32_bf16 v[24:27], a[136:139], v[64:67], v[24:27]\n" \
    "s_and_b32 s75, s74, 127\ns_lshr_b32 s69, s74, 8\nv_mfma_f32_16x16x32_bf16 v[20:23], a[128:131], v[56:59], v[20:23]\nds_read_b128 v[64:67], v9 offset:9216\n" \
    "s_sub_u32 s69, s69, s80\nv_mfma_f32_16x16x32_bf16 v[28:31], a[136:139], v[56:59], v[28:31]\nds_read_b128 v[56:59], v9 offset:13312\ns_bfe_i32 s69, s69, 0x180000\n" \
    "s_waitcnt lgkmcnt(2)\nv_mfma_f32_16x16x32_bf16 v[16:19], a[132:135], v[68:71], v[16:19]\nds_read_b128 v[72:75], v10 offset:9216\ns_cmp_eq_u32 s75, 0\n" \
    "s_cselect_b32 s69, 0, s69\nv_mfma_f32_16x16x32_bf16 v[24:27], a[140:143], v[68:71], v[24:27]\nds_read_b128 v[68:71], v10 offset:13312\ns_add_u32 s80, s80, s69\n" \
    "v_mfma_f32_16x16x32_bf16 v[20:23], a[132:135], v[60:63], v[20:23]\ns_lshl_b32 s74, s74, 5\ns_and_b32 s74, s74, 0x1000\n" \
    "v_mfma_f32_16x16x32_bf16 v[28:31], a[140:143], v[60:63], v[28:31]\ns_andn2_b32 s81, s81, 0x1000\ns_or_b32 s81, s81, s74\ns_waitcnt lgkmcnt(2)\n" \
    "v_mfma_f32_16x16x32_bf16 v[16:19], a[144:147], v[64:67], v[16:19]\nv_mfma_f32_16x16x32_bf16 v[24:27], a[152:155], v[64:67], v[24:27]\ns_waitcnt lgkmcnt(0)\ns_barrier\n" \
    "v_mfma_f32_16x16x32_bf16 v[16:19], a[148:151], v[72:75], v[16:19]\ns_add_u32 m0, s41, 0\ns_xor_b32 s85, s62, 0\n" \
    "v_mfma_f32_16x16x32_bf16 v[24:27], a[156:159], v[72:75], v[24:27]\ns_add_u32 s85, s85, s84\nbuffer_load_dwordx4 v11, s[64:67], s85 offen sc1 lds\n" \
    "v_mfma_f32_16x16x32_bf16 v[20:23], a[144:147], v[56:59], v[20:23]\ns_nop 1\nv_cvt_pk_bf16_f32 v32, v16, v17\nv_cvt_pk_bf16_f32 v33, v18, v19\n" \
    "v_mfma_f32_16x16x32_bf16 v[28:31], a[152:155], v[56:59], v[28:31]\nv_cvt_pk_bf16_f32 v34, v24, v25\nv_cvt_pk_bf16_f32 v35, v26, v27\n" \
    "v_mfma_f32_16x16x32_bf16 v[20:23], a[148:151], v[68:71], v[20:23]\nbuffer_load_dwordx4 v12, s[64:67], s85 offen offset:1024 sc1 lds\ns_add_u32 m0, s41, 8192\n" \
    "v_mfma_f32_16x16x32_bf16 v[28:31], a[156:159], v[68:71], v[28:31]\ns_xor_b32 s85, s62, 256\ns_add_u32 s85, s85, s84\nds_write_b128 v14, v[32:35] offset:0\n" \
    "buffer_load_dwordx4 v11, s[64:67], s85 offen sc1 lds\nbuffer_load_dwordx4 v12, s[64:67], s85 offen offset:1024 sc1 lds\nv_cvt_pk_bf16_f32 v36, v20, v21\n" \
    "v_cvt_pk_bf16_f32 v37, v22, v23\ns_mov_b32 s40, s84\nv_cvt_pk_bf16_f32 v38, v28, v29\nv_cvt_pk_bf16_f32 v39, v30, v31\nds_write_b128 v14, v[36:39] offset:4096\n" \
    "s_waitcnt vmcnt(0) lgkmcnt(0)\nv_mov_b32 v253, 1\ns_or_b32 s69, s35, s36\ns_cselect_b32 s69, 0x7fff0000, 0\ns_mov_b64 exec, 1\n" \
    "buffer_atomic_add v253, off, s[64:67], s69 offset:256\ns_mov_b64 exec, -1\ns_mov_b32 s16, 0\ns_cmp_eq_u32 s36, 0\ns_cselect_b32 s17, 511, -1\ns_mov_b32 s18, s44\n" \
    "s_mov_b32 s72, s80\ns_barrier\n.Lv5_%=:\nds_read_b128 v[56:59], v6 offset:0\nds_read_b128 v[60:63], v6 offset:1024\nds_read_b128 v[64:67], v6 offset:2048\n" \
    "v_mov_b64 v[32:33], 0\nv_mov_b64 v[34:35], 0\nv_mov_b64 v[36:37], 0\nv_mov_b64 v[38:39], 0\ns_and_b32 s69, s50, 7\ns_lshl_b32 s69, s69, 21\ns_add_u32 s86, s39, s69\n" \
    "s_lshr_b32 s69, s86, 8\ns_and_b32 s69, s69, 0x2000\ns_xor_b32 s86, s86, s69\ns_waitcnt lgkmcnt(2)\nv_smfmac_f32_16x16x64_bf16 v[32:35], v[56:59], a[160:167], v0\n" \
    "ds_read_b128 v[68:71], v6 offset:3072\nv_mov_b64 v[40:41], 0\nv_smfmac_f32_16x16x64_bf16 v[36:39], v[56:59], a[176:183], v0\nds_read_b128 v[72:75], v3 offset:0\n" \
    "s_waitcnt lgkmcnt(3)\nv_mov_b64 v[42:43], 0\nv_smfmac_f32_16x16x64_bf16 v[32:35], v[60:63], a[168:175], v0\nds_read_b128 v[76:79], v9 offset:0\nv_mov_b64 v[44:45], 0\n" \
    "v_smfmac_f32_16x16x64_bf16 v[36:39], v[60:63], a[184:191], v0\nds_read_b128 v[80:83], v9 offset:4096\ns_waitcnt lgkmcnt(4)\nv_mov_b64 v[46:47], 0\n" \
    "v_smfmac_f32_16x16x64_bf16 v[32:35], v[64:67], a[192:199], v0\ns_add_u32 s87, s86, 0x40000\ns_add_u32 s88, s86, 0x80000\n" \
    "v_smfmac_f32_16x16x64_bf16 v[36:39], v[64:67], a[208:215], v0\ns_add_u32 s89, s86, 0xc0000\ns_add_u32 s50, s50, 2\ns_waitcnt lgkmcnt(3)\n" \
    "v_smfmac_f32_16x16x64_bf16 v[32:35], v[68:71], a[200:207], v0\ns_add_u32 m0, s41, 24576\ns_xor_b32 s85, s62, 1024\n" \
    "v_smfmac_f32_16x16x64_bf16 v[36:39], v[68:71], a[216:223], v0\ns_add_u32 s85, s85, s40\nbuffer_load_dwordx4 v11, s[64:67], s85 offen sc1 lds\ns_waitcnt lgkmcnt(0)\n" \
    "v_mfma_f32_16x16x32_bf16 v[16:19], a[0:3], v[76:79], 0\ns_nop 0\nv_cvt_pk_bf16_f32 v48, v32, v33\nv_cvt_pk_bf16_f32 v50, v34, v35\n" \
    "v_mfma_f32_16x16x32_bf16 v[24:27], v[72:75], v[76:79], 0\nv_cvt_pk_bf16_f32 v49, v36, v37\nv_cvt_pk_bf16_f32 v51, v38, v39\n" \
    "v_smfmac_f32_16x16x64_bf16 v[40:43], v[56:59], a[224:231], v0\nbuffer_load_dwordx4 v12, s[64:67], s85 offen offset:1024 sc1 lds\nv_permlane16_swap_b32 v48, v49\n" \
    "v_smfmac_f32_16x16x64_bf16 v[44:47], v[56:59], a[240:247], v0\ns_add_u32 m0, s41, 32768\nv_permlane16_swap_b32 v50, v51\n" \
    "v_smfmac_f32_16x16x64_bf16 v[40:43], v[60:63], a[232:239], v0\nbuffer_store_dwordx4 v[48:51], v8, s[64:67], s86 offen offset:0 sc1\nv_mov_b64 v[32:33], 0\n" \
    "v_smfmac_f32_16x16x64_bf16 v[44:47], v[60:63], a[248:255], v0\ns_xor_b32 s85, s62, 1280\nv_mov_b64 v[34:35], 0\n" \
    "v_smfmac_f32_16x16x64_bf16 v[40:43], v[64:67], v[92:99], v0\ns_add_u32 s85, s85, s40\nv_mov_b64 v[36:37], 0\nv_smfmac_f32_16x16x64_bf16 v[44:47], v[64:67], v[108:115], v0\n" \
    "buffer_load_dwordx4 v11, s[64:67], s85 offen sc1 lds\nv_mov_b64 v[38:39], 0\nv_smfmac_f32_16x16x64_bf16 v[40:43], v[68:71], v[100:107], v0\n" \
    "v_smfmac_f32_16x16x64_bf16 v[44:47], v[68:71], v[116:123], v0\ns_nop 1\nv_mfma_f32_16x16x32_bf16 v[20:23], a[0:3], v[80:83], 0\n" \
    "buffer_load_dwordx4 v12, s[64:67], s85 offen offset:1024 sc1 lds\ns_sub_u32 s20, s72, s80\nv_mfma_f32_16x16x32_bf16 v[28:31], v[72:75], v[80:83], 0\n" \
    "s_add_u32 s20, s20, 10\nv_cvt_pk_bf16_f32 v52, v40, v41\nv_cvt_pk_bf16_f32 v54, v42, v43\nv_smfmac_f32_16x16x64_bf16 v[32:35], v[56:59], v[124:131], v0\n" \
    "s_min_u32 s21, s20, s16\nv_cvt_pk_bf16_f32 v53, v44, v45\nv_smfmac_f32_16x16x64_bf16 v[36:39], v[56:59], v[140:147], v0\nds_read_b128 v[72:75], v3 offset:1024\n" \
    "v_cvt_pk_bf16_f32 v55, v46, v47\nv_smfmac_f32_16x16x64_bf16 v[32:35], v[60:63], v[132:139], v0\nds_read_b128 v[76:79], v10 offset:0\nv_permlane16_swap_b32 v52, v53\n" \
    "v_smfmac_f32_16x16x64_bf16 v[36:39], v[60:63], v[148:155], v0\nds_read_b128 v[80:83], v10 offset:4096\nv_permlane16_swap_b32 v54, v55\n" \
    "v_smfmac_f32_16x16x64_bf16 v[32:35], v[64:67], v[156:163], v0\nbuffer_store_dwordx4 v[52:55], v8, s[64:67], s87 offen offset:0 sc1\nv_mov_b64 v[40:41], 0\n" \
    "v_smfmac_f32_16x16x64_bf16 v[36:39], v[64:67], v[172:179], v0\ns_mov_b32 s16, s20\nv_mov_b64 v[42:43], 0\nv_smfmac_f32_16x16x64_bf16 v[32:35], v[68:71], v[164:171], v0\n" \
    "s_cmp_gt_u32 s21, 156\ns_cselect_b32 s21, 0, s21\nv_mov_b64 v[44:45], 0\nv_smfmac_f32_16x16x64_bf16 v[36:39], v[68:71], v[180:187], v0\ns_lshr_b32 s20, s35, 2\n" \
    "s_waitcnt lgkmcnt(0)\nv_mov_b64 v[46:47], 0\nv_mfma_f32_16x16x32_bf16 v[16:19], a[4:7], v[76:79], v[16:19]\ns_lshl_b32 s22, s20, 8\nv_cvt_pk_bf16_f32 v48, v32, v33\n" \
    "v_cvt_pk_bf16_f32 v50, v34, v35\nv_mfma_f32_16x16x32_bf16 v[24:27], v[72:75], v[76:79], v[24:27]\ns_or_b32 s21, s21, s22\nv_cvt_pk_bf16_f32 v49, v36, v37\n" \
    "v_cvt_pk_bf16_f32 v51, v38, v39\nv_smfmac_f32_16x16x64_bf16 v[40:43], v[56:59], v[188:195], v0\ns_and_b32 s20, s20, 1\nv_permlane16_swap_b32 v48, v49\n" \
    "v_smfmac_f32_16x16x64_bf16 v[44:47], v[56:59], v[204:211], v0\ns_lshl_b32 s20, s20, 2\nv_permlane16_swap_b32 v50, v51\n" \
    "v_smfmac_f32_16x16x64_bf16 v[40:43], v[60:63], v[196:203], v0\nbuffer_store_dwordx4 v[48:51], v8, s[64:67], s88 offen offset:0 sc1\nv_mov_b64 v[32:33], 0\n" \
    "v_smfmac_f32_16x16x64_bf16 v[44:47], v[60:63], v[212:219], v0\ns_cmp_gt_u32 s21, s17\ns_cselect_b32 s20, s20, 0x7fff0000\nv_mov_b64 v[34:35], 0\n" \
    "v_smfmac_f32_16x16x64_bf16 v[40:43], v[64:67], v[220:227], v0\ns_max_u32 s17, s17, s21\nv_mov_b64 v[36:37], 0\n" \
    "v_smfmac_f32_16x16x64_bf16 v[44:47], v[64:67], v[236:243], v0\nds_read_b128 v[76:79], v6 offset:128\nv_mov_b64 v[38:39], 0\n" \
    "v_smfmac_f32_16x16x64_bf16 v[40:43], v[68:71], v[228:235], v0\nv_mov_b32 v15, s21\ns_mov_b64 exec, 1\nbuffer_atomic_umax v15, off, s[64:67], s20 offset:768\n" \
    "s_mov_b64 exec, -1\nv_smfmac_f32_16x16x64_bf16 v[44:47], v[68:71], v[244:251], v0\nds_read_b128 v[56:59], v6 offset:1152\ns_add_u32 s84, s40, 0x20000\n" \
    "v_mfma_f32_16x16x32_bf16 v[20:23], a[4:7], v[80:83], v[20:23]\ns_cmp_lt_u32 s49, 62\ns_cselect_b32 s84, s84, s40\nv_cvt_pk_bf16_f32 v52, v40, v41\n" \
    "v_mfma_f32_16x16x32_bf16 v[28:31], v[72:75], v[80:83], v[28:31]\nds_read_b128 v[60:63], v6 offset:2176\ns_waitcnt lgkmcnt(2)\nv_cvt_pk_bf16_f32 v54, v42, v43\n" \
    "v_smfmac_f32_16x16x64_bf16 v[32:35], v[76:79], a[160:167], v0\nds_read_b128 v[64:67], v6 offset:3200\nv_cvt_pk_bf16_f32 v53, v44, v45\n" \
    "v_smfmac_f32_16x16x64_bf16 v[36:39], v[76:79], a[176:183], v0\nds_read_b128 v[68:71], v3 offset:2048\ns_waitcnt lgkmcnt(3)\nv_cvt_pk_bf16_f32 v55, v46, v47\n" \
    "v_smfmac_f32_16x16x64_bf16 v[32:35], v[56:59], a[168:175], v0\nds_read_b128 v[72:75], v9 offset:1024\nv_permlane16_swap_b32 v52, v53\n" \
    "v_smfmac_f32_16x16x64_bf16 v[36:39], v[56:59], a[184:191], v0\nds_read_b128 v[80:83], v9 offset:5120\ns_waitcnt lgkmcnt(4)\nv_permlane16_swap_b32 v54, v55\n" \
    "v_smfmac_f32_16x16x64_bf16 v[32:35], v[60:63], a[192:199], v0\nbuffer_store_dwordx4 v[52:55], v8, s[64:67], s89 offen offset:0 sc1\nv_mov_b64 v[40:41], 0\n" \
    "v_smfmac_f32_16x16x64_bf16 v[36:39], v[60:63], a[208:215], v0\ns_waitcnt lgkmcnt(3)\nv_mov_b64 v[42:43], 0\nv_smfmac_f32_16x16x64_bf16 v[32:35], v[64:67], a[200:207], v0\n" \
    "v_mov_b64 v[44:45], 0\nv_smfmac_f32_16x16x64_bf16 v[36:39], v[64:67], a[216:223], v0\ns_waitcnt lgkmcnt(0)\nv_mov_b64 v[46:47], 0\n" \
    "v_mfma_f32_16x16x32_bf16 v[16:19], a[8:11], v[72:75], v[16:19]\ns_nop 0\nv_mov_b64 v[252:253], -1\nv_mov_b64 v[254:255], -1\n" \
    "v_mfma_f32_16x16x32_bf16 v[24:27], v[68:71], v[72:75], v[24:27]\ns_and_b32 s69, s49, 15\nv_cvt_pk_bf16_f32 v48, v32, v33\nv_cvt_pk_bf16_f32 v50, v34, v35\n" \
    "v_smfmac_f32_16x16x64_bf16 v[40:43], v[76:79], a[224:231], v0\ns_lshl_b32 s69, s69, 19\nv_cvt_pk_bf16_f32 v49, v36, v37\n" \
    "v_smfmac_f32_16x16x64_bf16 v[44:47], v[76:79], a[240:247], v0\ns_add_u32 s82, s38, s69\nv_cvt_pk_bf16_f32 v51, v38, v39\n" \
    "v_smfmac_f32_16x16x64_bf16 v[40:43], v[56:59], a[232:239], v0\ns_lshr_b32 s69, s82, 8\nv_permlane16_swap_b32 v48, v49\n" \
    "v_smfmac_f32_16x16x64_bf16 v[44:47], v[56:59], a[248:255], v0\ns_and_b32 s69, s69, 0x2000\nv_permlane16_swap_b32 v50, v51\n" \
    "v_smfmac_f32_16x16x64_bf16 v[40:43], v[60:63], v[92:99], v0\nbuffer_store_dwordx4 v[48:51], v8, s[64:67], s86 offen offset:256 sc1\nv_mov_b64 v[32:33], 0\n" \
    "v_smfmac_f32_16x16x64_bf16 v[44:47], v[60:63], v[108:115], v0\ns_xor_b32 s82, s82, s69\nv_mov_b64 v[34:35], 0\n" \
    "v_smfmac_f32_16x16x64_bf16 v[40:43], v[64:67], v[100:107], v0\ns_and_b32 s69, s35, 15\nv_mov_b64 v[36:37], 0\n" \
    "v_smfmac_f32_16x16x64_bf16 v[44:47], v[64:67], v[116:123], v0\ns_lshl_b32 s69, s69, 19\nv_mov_b64 v[38:39], 0\n" \
    "v_mfma_f32_16x16x32_bf16 v[20:23], a[8:11], v[80:83], v[20:23]\ns_add_u32 s0, s38, s69\ns_lshr_b32 s69, s0, 8\nv_cvt_pk_bf16_f32 v52, v40, v41\n" \
    "v_mfma_f32_16x16x32_bf16 v[28:31], v[68:71], v[80:83], v[28:31]\ns_and_b32 s69, s69, 0x2000\nv_cvt_pk_bf16_f32 v54, v42, v43\nv_cvt_pk_bf16_f32 v53, v44, v45\n" \
    "v_smfmac_f32_16x16x64_bf16 v[32:35], v[76:79], v[124:131], v0\ns_xor_b32 s0, s0, s69\nv_cvt_pk_bf16_f32 v55, v46, v47\n" \
    "v_smfmac_f32_16x16x64_bf16 v[36:39], v[76:79], v[140:147], v0\nds_read_b128 v[68:71], v3 offset:3072\nv_permlane16_swap_b32 v52, v53\n" \
    "v_smfmac_f32_16x16x64_bf16 v[32:35], v[56:59], v[132:139], v0\nds_read_b128 v[72:75], v10 offset:1024\nv_permlane16_swap_b32 v54, v55\n" \
    "v_smfmac_f32_16x16x64_bf16 v[36:39], v[56:59], v[148:155], v0\nds_read_b128 v[80:83], v10 offset:5120\n" \
    "buffer_store_dwordx4 v[52:55], v8, s[64:67], s87 offen offset:256 sc1\nv_mov_b64 v[40:41], 0\nv_smfmac_f32_16x16x64_bf16 v[32:35], v[60:63], v[156:163], v0\n" \
    "s_add_u32 s1, s0, 0x10000\nv_mov_b64 v[42:43], 0\nv_smfmac_f32_16x16x64_bf16 v[36:39], v[60:63], v[172:179], v0\ns_add_u32 s2, s0, 0x20000\nv_mov_b64 v[44:45], 0\n" \
    "v_smfmac_f32_16x16x64_bf16 v[32:35], v[64:67], v[164:171], v0\ns_add_u32 s3, s0, 0x30000\nv_mov_b64 v[46:47], 0\n" \
    "v_smfmac_f32_16x16x64_bf16 v[36:39], v[64:67], v[180:187], v0\ns_nop 0\ns_waitcnt lgkmcnt(0)\nv_lshrrev_b32 v15, 1, v2\n" \
    "v_mfma_f32_16x16x32_bf16 v[16:19], a[12:15], v[72:75], v[16:19]\ns_nop 0\nv_cvt_pk_bf16_f32 v48, v32, v33\nv_cvt_pk_bf16_f32 v50, v34, v35\n" \
    "v_mfma_f32_16x16x32_bf16 v[24:27], v[68:71], v[72:75], v[24:27]\nv_cvt_pk_bf16_f32 v49, v36, v37\nv_cvt_pk_bf16_f32 v51, v38, v39\n" \
    "v_smfmac_f32_16x16x64_bf16 v[40:43], v[76:79], v[188:195], v0\nv_permlane16_swap_b32 v48, v49\nv_smfmac_f32_16x16x64_bf16 v[44:47], v[76:79], v[204:211], v0\n" \
    "v_permlane16_swap_b32 v50, v51\nv_smfmac_f32_16x16x64_bf16 v[40:43], v[56:59], v[196:203], v0\nbuffer_store_dwordx4 v[48:51], v8, s[64:67], s88 offen offset:256 sc1\n" \
    "v_mov_b64 v[32:33], 0\nv_smfmac_f32_16x16x64_bf16 v[44:47], v[56:59], v[212:219], v0\nv_mov_b64 v[34:35], 0\nv_smfmac_f32_16x16x64_bf16 v[40:43], v[60:63], v[220:227], v0\n" \
    "v_mov_b64 v[36:37], 0\nv_smfmac_f32_16x16x64_bf16 v[44:47], v[60:63], v[236:243], v0\nds_read_b128 v[56:59], v6 offset:4096\nv_mov_b64 v[38:39], 0\n" \
    "v_smfmac_f32_16x16x64_bf16 v[40:43], v[64:67], v[228:235], v0\nv_add_u32 v15, s60, v15\nv_smfmac_f32_16x16x64_bf16 v[44:47], v[64:67], v[244:251], v0\n" \
    "ds_read_b128 v[60:63], v6 offset:5120\ns_nop 0\nv_mfma_f32_16x16x32_bf16 v[20:23], a[12:15], v[80:83], v[20:23]\nds_write_b64 v15, v[252:253]\ns_nop 0\n" \
    "v_mfma_f32_16x16x32_bf16 v[28:31], v[68:71], v[80:83], v[28:31]\nds_read_b128 v[64:67], v6 offset:6144\ns_waitcnt lgkmcnt(3)\nv_cvt_pk_bf16_f32 v52, v40, v41\n" \
    "v_smfmac_f32_16x16x64_bf16 v[32:35], v[56:59], a[160:167], v0\nds_read_b128 v[68:71], v6 offset:7168\nv_cvt_pk_bf16_f32 v54, v42, v43\n" \
    "v_smfmac_f32_16x16x64_bf16 v[36:39], v[56:59], a[176:183], v0\nds_read_b128 v[72:75], v3 offset:4096\ns_waitcnt lgkmcnt(4)\nv_cvt_pk_bf16_f32 v53, v44, v45\n" \
    "v_smfmac_f32_16x16x64_bf16 v[32:35], v[60:63], a[168:175], v0\nds_read_b128 v[76:79], v9 offset:8192\nv_cvt_pk_bf16_f32 v55, v46, v47\n" \
    "v_smfmac_f32_16x16x64_bf16 v[36:39], v[60:63], a[184:191], v0\nds_read_b128 v[80:83], v9 offset:12288\ns_waitcnt lgkmcnt(4)\nv_permlane16_swap_b32 v52, v53\n" \
    "v_smfmac_f32_16x16x64_bf16 v[32:35], v[64:67], a[192:199], v0\nv_permlane16_swap_b32 v54, v55\nv_smfmac_f32_16x16x64_bf16 v[36:39], v[64:67], a[208:215], v0\n" \
    "buffer_store_dwordx4 v[52:55], v8, s[64:67], s89 offen offset:256 sc1\ns_waitcnt lgkmcnt(3)\nv_mov_b64 v[40:41], 0\n" \
    "v_smfmac_f32_16x16x64_bf16 v[32:35], v[68:71], a[200:207], v0\nv_mov_b64 v[42:43], 0\nv_smfmac_f32_16x16x64_bf16 v[36:39], v[68:71], a[216:223], v0\ns_waitcnt lgkmcnt(0)\n" \
    "v_mov_b64 v[44:45], 0\nv_mfma_f32_16x16x32_bf16 v[16:19], a[16:19], v[76:79], v[16:19]\nv_mov_b64 v[46:47], 0\n" \
    "v_mfma_f32_16x16x32_bf16 v[24:27], v[72:75], v[76:79], v[24:27]\ns_nop 0\nv_cvt_pk_bf16_f32 v48, v32, v33\nv_cvt_pk_bf16_f32 v50, v34, v35\n" \
    "v_smfmac_f32_16x16x64_bf16 v[40:43], v[56:59], a[224:231], v0\nv_cvt_pk_bf16_f32 v49, v36, v37\nv_smfmac_f32_16x16x64_bf16 v[44:47], v[56:59], a[240:247], v0\n" \
    "v_cvt_pk_bf16_f32 v51, v38, v39\nv_smfmac_f32_16x16x64_bf16 v[40:43], v[60:63], a[232:239], v0\nv_permlane16_swap_b32 v48, v49\n" \
    "v_smfmac_f32_16x16x64_bf16 v[44:47], v[60:63], a[248:255], v0\nv_permlane16_swap_b32 v50, v51\nv_smfmac_f32_16x16x64_bf16 v[40:43], v[64:67], v[92:99], v0\n" \
    "buffer_store_dwordx4 v[48:51], v8, s[64:67], s86 offen offset:512 sc1\nv_mov_b64 v[32:33], 0\nv_smfmac_f32_16x16x64_bf16 v[44:47], v[64:67], v[108:115], v0\n" \
    "v_mov_b64 v[34:35], 0\nv_smfmac_f32_16x16x64_bf16 v[40:43], v[68:71], v[100:107], v0\nv_mov_b64 v[36:37], 0\nv_smfmac_f32_16x16x64_bf16 v[44:47], v[68:71], v[116:123], v0\n" \
    "v_mov_b64 v[38:39], 0\nv_mfma_f32_16x16x32_bf16 v[20:23], a[16:19], v[80:83], v[20:23]\ns_nop 1\nv_mfma_f32_16x16x32_bf16 v[28:31], v[72:75], v[80:83], v[28:31]\ns_nop 0\n" \
    "v_cvt_pk_bf16_f32 v52, v40, v41\nv_cvt_pk_bf16_f32 v54, v42, v43\nv_smfmac_f32_16x16x64_bf16 v[32:35], v[56:59], v[124:131], v0\nv_cvt_pk_bf16_f32 v53, v44, v45\n" \
    "v_smfmac_f32_16x16x64_bf16 v[36:39], v[56:59], v[140:147], v0\nds_read_b128 v[72:75], v3 offset:5120\nv_cvt_pk_bf16_f32 v55, v46, v47\n" \
    "v_smfmac_f32_16x16x64_bf16 v[32:35], v[60:63], v[132:139], v0\nds_read_b128 v[76:79], v10 offset:8192\nv_permlane16_swap_b32 v52, v53\n" \
    "v_smfmac_f32_16x16x64_bf16 v[36:39], v[60:63], v[148:155], v0\nds_read_b128 v[80:83], v10 offset:12288\nv_permlane16_swap_b32 v54, v55\n" \
    "v_smfmac_f32_16x16x64_bf16 v[32:35], v[64:67], v[156:163], v0\nbuffer_store_dwordx4 v[52:55], v8, s[64:67], s87 offen offset:512 sc1\nv_mov_b64 v[40:41], 0\n" \
    "v_smfmac_f32_16x16x64_bf16 v[36:39], v[64:67], v[172:179], v0\nv_mov_b64 v[42:43], 0\nv_smfmac_f32_16x16x64_bf16 v[32:35], v[68:71], v[164:171], v0\nv_mov_b64 v[44:45], 0\n" \
    "v_smfmac_f32_16x16x64_bf16 v[36:39], v[68:71], v[180:187], v0\ns_waitcnt lgkmcnt(0)\nv_mov_b64 v[46:47], 0\n" \
    "v_mfma_f32_16x16x32_bf16 v[16:19], a[20:23], v[76:79], v[16:19]\ns_nop 1\nv_mfma_f32_16x16x32_bf16 v[24:27], v[72:75], v[76:79], v[24:27]\nv_cvt_pk_bf16_f32 v48, v32, v33\n" \
    "v_cvt_pk_bf16_f32 v50, v34, v35\nv_smfmac_f32_16x16x64_bf16 v[40:43], v[56:59], v[188:195], v0\nv_cvt_pk_bf16_f32 v49, v36, v37\n" \
    "v_smfmac_f32_16x16x64_bf16 v[44:47], v[56:59], v[204:211], v0\nv_cvt_pk_bf16_f32 v51, v38, v39\nv_smfmac_f32_16x16x64_bf16 v[40:43], v[60:63], v[196:203], v0\n" \
    "v_permlane16_swap_b32 v48, v49\nv_smfmac_f32_16x16x64_bf16 v[44:47], v[60:63], v[212:219], v0\nv_permlane16_swap_b32 v50, v51\n" \
    "v_smfmac_f32_16x16x64_bf16 v[40:43], v[64:67], v[220:227], v0\nbuffer_store_dwordx4 v[48:51], v8, s[64:67], s88 offen offset:512 sc1\nv_mov_b64 v[32:33], 0\n" \
    "v_smfmac_f32_16x16x64_bf16 v[44:47], v[64:67], v[236:243], v0\nds_read_b128 v[76:79], v6 offset:4224\nv_mov_b64 v[34:35], 0\n" \
    "v_smfmac_f32_16x16x64_bf16 v[40:43], v[68:71], v[228:235], v0\nv_mov_b64 v[36:37], 0\nv_smfmac_f32_16x16x64_bf16 v[44:47], v[68:71], v[244:251], v0\n" \
    "ds_read_b128 v[56:59], v6 offset:5248\nv_mov_b64 v[38:39], 0\nv_mfma_f32_16x16x32_bf16 v[20:23], a[20:23], v[80:83], v[20:23]\ns_nop 1\n" \
    "v_mfma_f32_16x16x32_bf16 v[28:31], v[72:75], v[80:83], v[28:31]\nds_read_b128 v[60:63], v6 offset:6272\ns_waitcnt lgkmcnt(2)\nv_cvt_pk_bf16_f32 v52, v40, v41\n" \
    "v_smfmac_f32_16x16x64_bf16 v[32:35], v[76:79], a[160:167], v0\nds_read_b128 v[64:67], v6 offset:7296\nv_cvt_pk_bf16_f32 v54, v42, v43\n" \
    "v_smfmac_f32_16x16x64_bf16 v[36:39], v[76:79], a[176:183], v0\nds_read_b128 v[68:71], v3 offset:6144\ns_waitcnt lgkmcnt(3)\nv_cvt_pk_bf16_f32 v53, v44, v45\n" \
    "v_smfmac_f32_16x16x64_bf16 v[32:35], v[56:59], a[168:175], v0\nds_read_b128 v[72:75], v9 offset:9216\nv_cvt_pk_bf16_f32 v55, v46, v47\n" \
    "v_smfmac_f32_16x16x64_bf16 v[36:39], v[56:59], a[184:191], v0\nds_read_b128 v[80:83], v9 offset:13312\ns_waitcnt lgkmcnt(4)\nv_permlane16_swap_b32 v52, v53\n" \
    "v_smfmac_f32_16x16x64_bf16 v[32:35], v[60:63], a[192:199], v0\nv_permlane16_swap_b32 v54, v55\nv_smfmac_f32_16x16x64_bf16 v[36:39], v[60:63], a[208:215], v0\n" \
    "buffer_store_dwordx4 v[52:55], v8, s[64:67], s89 offen offset:512 sc1\ns_waitcnt lgkmcnt(3)\nv_mov_b64 v[40:41], 0\n" \
    "v_smfmac_f32_16x16x64_bf16 v[32:35], v[64:67], a[200:207], v0\nv_mov_b64 v[42:43], 0\nv_smfmac_f32_16x16x64_bf16 v[36:39], v[64:67], a[216:223], v0\ns_waitcnt lgkmcnt(0)\n" \
    "v_mov_b64 v[44:45], 0\nv_mfma_f32_16x16x32_bf16 v[16:19], a[24:27], v[72:75], v[16:19]\nv_mov_b64 v[46:47], 0\n" \
    "v_mfma_f32_16x16x32_bf16 v[24:27], v[68:71], v[72:75], v[24:27]\ns_nop 0\nv_cvt_pk_bf16_f32 v48, v32, v33\nv_cvt_pk_bf16_f32 v50, v34, v35\n" \
    "v_smfmac_f32_16x16x64_bf16 v[40:43], v[76:79], a[224:231], v0\nv_cvt_pk_bf16_f32 v49, v36, v37\nv_smfmac_f32_16x16x64_bf16 v[44:47], v[76:79], a[240:247], v0\n" \
    "v_cvt_pk_bf16_f32 v51, v38, v39\nv_smfmac_f32_16x16x64_bf16 v[40:43], v[56:59], a[232:239], v0\nv_permlane16_swap_b32 v48, v49\n" \
    "v_smfmac_f32_16x16x64_bf16 v[44:47], v[56:59], a[248:255], v0\nv_permlane16_swap_b32 v50, v51\nv_smfmac_f32_16x16x64_bf16 v[40:43], v[60:63], v[92:99], v0\n" \
    "buffer_store_dwordx4 v[48:51], v8, s[64:67], s86 offen offset:768 sc1\nv_mov_b64 v[32:33], 0\nv_smfmac_f32_16x16x64_bf16 v[44:47], v[60:63], v[108:115], v0\n" \
    "v_mov_b64 v[34:35], 0\nv_smfmac_f32_16x16x64_bf16 v[40:43], v[64:67], v[100:107], v0\nv_mov_b64 v[36:37], 0\nv_smfmac_f32_16x16x64_bf16 v[44:47], v[64:67], v[116:123], v0\n" \
    "v_mov_b64 v[38:39], 0\nv_mfma_f32_16x16x32_bf16 v[20:23], a[24:27], v[80:83], v[20:23]\ns_nop 1\nv_mfma_f32_16x16x32_bf16 v[28:31], v[68:71], v[80:83], v[28:31]\ns_nop 0\n" \
    "v_cvt_pk_bf16_f32 v52, v40, v41\nv_cvt_pk_bf16_f32 v54, v42, v43\nv_smfmac_f32_16x16x64_bf16 v[32:35], v[76:79], v[124:131], v0\nv_cvt_pk_bf16_f32 v53, v44, v45\n" \
    "v_smfmac_f32_16x16x64_bf16 v[36:39], v[76:79], v[140:147], v0\nds_read_b128 v[68:71], v3 offset:7168\nv_cvt_pk_bf16_f32 v55, v46, v47\n" \
    "v_smfmac_f32_16x16x64_bf16 v[32:35], v[56:59], v[132:139], v0\nds_read_b128 v[72:75], v10 offset:9216\nv_permlane16_swap_b32 v52, v53\n" \
    "v_smfmac_f32_16x16x64_bf16 v[36:39], v[56:59], v[148:155], v0\nds_read_b128 v[80:83], v10 offset:13312\nv_permlane16_swap_b32 v54, v55\n" \
    "v_smfmac_f32_16x16x64_bf16 v[32:35], v[60:63], v[156:163], v0\nbuffer_store_dwordx4 v[52:55], v8, s[64:67], s87 offen offset:768 sc1\nv_mov_b64 v[40:41], 0\n" \
    "v_smfmac_f32_16x16x64_bf16 v[36:39], v[60:63], v[172:179], v0\nv_mov_b64 v[42:43], 0\nv_smfmac_f32_16x16x64_bf16 v[32:35], v[64:67], v[164:171], v0\nv_mov_b64 v[44:45], 0\n" \
    "v_smfmac_f32_16x16x64_bf16 v[36:39], v[64:67], v[180:187], v0\ns_waitcnt lgkmcnt(0)\nv_mov_b64 v[46:47], 0\n" \
    "v_mfma_f32_16x16x32_bf16 v[16:19], a[28:31], v[72:75], v[16:19]\ns_nop 1\nv_mfma_f32_16x16x32_bf16 v[24:27], v[68:71], v[72:75], v[24:27]\nv_cvt_pk_bf16_f32 v48, v32, v33\n" \
    "v_cvt_pk_bf16_f32 v50, v34, v35\nv_smfmac_f32_16x16x64_bf16 v[40:43], v[76:79], v[188:195], v0\nv_cvt_pk_bf16_f32 v49, v36, v37\n" \
    "v_smfmac_f32_16x16x64_bf16 v[44:47], v[76:79], v[204:211], v0\nv_cvt_pk_bf16_f32 v51, v38, v39\nv_smfmac_f32_16x16x64_bf16 v[40:43], v[56:59], v[196:203], v0\n" \
    "v_permlane16_swap_b32 v48, v49\nv_smfmac_f32_16x16x64_bf16 v[44:47], v[56:59], v[212:219], v0\nds_read_b128 v[56:59], v3 offset:8192\nv_permlane16_swap_b32 v50, v51\n" \
    "v_smfmac_f32_16x16x64_bf16 v[40:43], v[60:63], v[220:227], v0\nds_read_b128 v[72:75], v9 offset:16384\n" \
    "buffer_store_dwordx4 v[48:51], v8, s[64:67], s88 offen offset:768 sc1\nv_smfmac_f32_16x16x64_bf16 v[44:47], v[60:63], v[236:243], v0\n" \
    "ds_read_b128 v[60:63], v9 offset:20480\nv_smfmac_f32_16x16x64_bf16 v[40:43], v[64:67], v[228:235], v0\nv_smfmac_f32_16x16x64_bf16 v[44:47], v[64:67], v[244:251], v0\n" \
    "ds_read_b128 v[64:67], v3 offset:9216\ns_nop 0\nv_mfma_f32_16x16x32_bf16 v[20:23], a[28:31], v[80:83], v[20:23]\nds_read_b128 v[76:79], v10 offset:16384\ns_nop 0\n" \
    "v_mfma_f32_16x16x32_bf16 v[28:31], v[68:71], v[80:83], v[28:31]\nds_read_b128 v[68:71], v10 offset:20480\ns_waitcnt lgkmcnt(3)\nv_cvt_pk_bf16_f32 v52, v40, v41\n" \
    "v_mfma_f32_16x16x32_bf16 v[16:19], a[32:35], v[72:75], v[16:19]\nv_cvt_pk_bf16_f32 v54, v42, v43\nv_cvt_pk_bf16_f32 v53, v44, v45\n" \
    "v_mfma_f32_16x16x32_bf16 v[24:27], v[56:59], v[72:75], v[24:27]\nds_read_b128 v[72:75], v3 offset:10240\nv_cvt_pk_bf16_f32 v55, v46, v47\nv_permlane16_swap_b32 v52, v53\n" \
    "v_mfma_f32_16x16x32_bf16 v[20:23], a[32:35], v[60:63], v[20:23]\nds_read_b128 v[80:83], v9 offset:17408\nv_permlane16_swap_b32 v54, v55\n" \
    "v_mfma_f32_16x16x32_bf16 v[28:31], v[56:59], v[60:63], v[28:31]\nds_read_b128 v[56:59], v9 offset:21504\n" \
    "buffer_store_dwordx4 v[52:55], v8, s[64:67], s89 offen offset:768 sc1\ns_waitcnt lgkmcnt(3)\nv_mfma_f32_16x16x32_bf16 v[16:19], a[36:39], v[76:79], v[16:19]\n" \
    "buffer_load_dword v13, off, s[64:67], s42 sc1\ns_and_b32 s69, s49, 255\nv_mfma_f32_16x16x32_bf16 v[24:27], v[64:67], v[76:79], v[24:27]\n" \
    "ds_read_b128 v[60:63], v3 offset:11264\ns_lshl_b32 s69, s69, 19\nv_mfma_f32_16x16x32_bf16 v[20:23], a[36:39], v[68:71], v[20:23]\nds_read_b128 v[76:79], v10 offset:17408\n" \
    "s_add_u32 s74, s63, s69\nv_mfma_f32_16x16x32_bf16 v[28:31], v[64:67], v[68:71], v[28:31]\nds_read_b128 v[64:67], v10 offset:21504\ns_cmp_lt_u32 s49, 64\n" \
    "s_cselect_b32 s74, s74, 0x7fff0000\ns_waitcnt lgkmcnt(3)\nv_mfma_f32_16x16x32_bf16 v[16:19], a[40:43], v[80:83], v[16:19]\n" \
    "v_mfma_f32_16x16x32_bf16 v[24:27], v[72:75], v[80:83], v[24:27]\nds_read_b128 v[68:71], v3 offset:12288\ns_waitcnt vmcnt(17)\ns_barrier\n" \
    "v_mfma_f32_16x16x32_bf16 v[20:23], a[40:43], v[56:59], v[20:23]\nds_read_b128 v[80:83], v9 offset:40960\nv_mfma_f32_16x16x32_bf16 v[28:31], v[72:75], v[56:59], v[28:31]\n" \
    "ds_read_b128 v[56:59], v9 offset:45056\ns_add_u32 m0, s41, 0\ns_waitcnt lgkmcnt(3)\nv_mfma_f32_16x16x32_bf16 v[16:19], a[44:47], v[76:79], v[16:19]\n" \
    "s_xor_b32 s85, s62, 1536\ns_add_u32 s85, s85, s40\nv_mfma_f32_16x16x32_bf16 v[24:27], v[60:63], v[76:79], v[24:27]\nds_read_b128 v[72:75], v3 offset:13312\n" \
    "buffer_load_dwordx4 v11, s[64:67], s85 offen sc1 lds\nv_mfma_f32_16x16x32_bf16 v[20:23], a[44:47], v[64:67], v[20:23]\nds_read_b128 v[76:79], v10 offset:40960\n" \
    "buffer_store_dwordx4 v[252:255], v2, s[64:67], s74 offen offset:0 sc1\nv_mfma_f32_16x16x32_bf16 v[28:31], v[60:63], v[64:67], v[28:31]\n" \
    "ds_read_b128 v[60:63], v10 offset:45056\ns_waitcnt lgkmcnt(3)\nv_mfma_f32_16x16x32_bf16 v[16:19], a[48:51], v[80:83], v[16:19]\n" \
    "buffer_load_dwordx4 v12, s[64:67], s85 offen offset:1024 sc1 lds\ns_add_u32 m0, s41, 8192\nv_mfma_f32_16x16x32_bf16 v[24:27], v[68:71], v[80:83], v[24:27]\n" \
    "ds_read_b128 v[64:67], v3 offset:14336\ns_xor_b32 s85, s62, 1792\nv_mfma_f32_16x16x32_bf16 v[20:23], a[48:51], v[56:59], v[20:23]\nds_read_b128 v[80:83], v9 offset:41984\n" \
    "s_add_u32 s85, s85, s40\nv_mfma_f32_16x16x32_bf16 v[28:31], v[68:71], v[56:59], v[28:31]\nds_read_b128 v[56:59], v9 offset:46080\n" \
    "buffer_load_dwordx4 v11, s[64:67], s85 offen sc1 lds\ns_waitcnt lgkmcnt(3)\nv_mfma_f32_16x16x32_bf16 v[16:19], a[52:55], v[76:79], v[16:19]\n" \
    "buffer_store_dwordx4 v[252:255], v2, s[64:67], s74 offen offset:1024 sc1\nv_mfma_f32_16x16x32_bf16 v[24:27], v[72:75], v[76:79], v[24:27]\n" \
    "ds_read_b128 v[68:71], v3 offset:15360\nv_mfma_f32_16x16x32_bf16 v[20:23], a[52:55], v[60:63], v[20:23]\nds_read_b128 v[76:79], v10 offset:41984\n" \
    "buffer_load_dwordx4 v12, s[64:67], s85 offen offset:1024 sc1 lds\nv_mfma_f32_16x16x32_bf16 v[28:31], v[72:75], v[60:63], v[28:31]\nds_read_b128 v[60:63], v10 offset:46080\n" \
    "s_waitcnt lgkmcnt(3)\nv_mfma_f32_16x16x32_bf16 v[16:19], a[56:59], v[80:83], v[16:19]\nbuffer_store_dwordx4 v[252:255], v7, s[64:67], s82 offen sc1\n" \
    "s_add_u32 s82, s82, 0x10000\nv_mfma_f32_16x16x32_bf16 v[24:27], v[64:67], v[80:83], v[24:27]\nds_read_b128 v[72:75], v3 offset:16384\n" \
    "v_mfma_f32_16x16x32_bf16 v[20:23], a[56:59], v[56:59], v[20:23]\nds_read_b128 v[80:83], v9 offset:24576\nv_mfma_f32_16x16x32_bf16 v[28:31], v[64:67], v[56:59], v[28:31]\n" \
    "ds_read_b128 v[56:59], v9 offset:28672\ns_waitcnt lgkmcnt(3)\nv_mfma_f32_16x16x32_bf16 v[16:19], a[60:63], v[76:79], v[16:19]\n" \
    "buffer_store_dwordx4 v[252:255], v7, s[64:67], s82 offen sc1\ns_add_u32 s82, s82, 0x10000\nv_mfma_f32_16x16x32_bf16 v[24:27], v[68:71], v[76:79], v[24:27]\n" \
    "ds_read_b128 v[64:67], v3 offset:17408\nv_mfma_f32_16x16x32_bf16 v[20:23], a[60:63], v[60:63], v[20:23]\nds_read_b128 v[76:79], v10 offset:24576\n" \
    "v_mfma_f32_16x16x32_bf16 v[28:31], v[68:71], v[60:63], v[28:31]\nds_read_b128 v[60:63], v10 offset:28672\ns_waitcnt lgkmcnt(3)\n" \
    "v_mfma_f32_16x16x32_bf16 v[16:19], a[64:67], v[80:83], v[16:19]\nbuffer_store_dwordx4 v[252:255], v7, s[64:67], s82 offen sc1\ns_add_u32 s82, s82, 0x10000\n" \
    "v_mfma_f32_16x16x32_bf16 v[24:27], v[72:75], v[80:83], v[24:27]\nds_read_b128 v[68:71], v3 offset:18432\nv_mfma_f32_16x16x32_bf16 v[20:23], a[64:67], v[56:59], v[20:23]\n" \
    "ds_read_b128 v[80:83], v9 offset:25600\nv_mfma_f32_16x16x32_bf16 v[28:31], v[72:75], v[56:59], v[28:31]\nds_read_b128 v[56:59], v9 offset:29696\ns_waitcnt lgkmcnt(3)\n" \
    "v_mfma_f32_16x16x32_bf16 v[16:19], a[68:71], v[76:79], v[16:19]\nbuffer_store_dwordx4 v[252:255], v7, s[64:67], s82 offen sc1\ns_add_u32 s82, s82, 0x10000\n" \
    "v_mfma_f32_16x16x32_bf16 v[24:27], v[64:67], v[76:79], v[24:27]\nds_read_b128 v[72:75], v3 offset:19456\ns_add_u32 s49, s49, 2\n" \
    "v_mfma_f32_16x16x32_bf16 v[20:23], a[68:71], v[60:63], v[20:23]\nds_read_b128 v[76:79], v10 offset:25600\ns_and_b32 s69, s35, 15\n" \
    "v_mfma_f32_16x16x32_bf16 v[28:31], v[64:67], v[60:63], v[28:31]\nds_read_b128 v[60:63], v10 offset:29696\ns_lshl_b32 s69, s69, 17\ns_waitcnt lgkmcnt(3)\n" \
    "v_mfma_f32_16x16x32_bf16 v[16:19], a[72:75], v[80:83], v[16:19]\ns_add_u32 s81, s37, s69\ns_lshr_b32 s69, s81, 8\n" \
    "v_mfma_f32_16x16x32_bf16 v[24:27], v[68:71], v[80:83], v[24:27]\nds_read_b128 v[64:67], v3 offset:20480\ns_and_b32 s69, s69, 0x2000\n" \
    "v_mfma_f32_16x16x32_bf16 v[20:23], a[72:75], v[56:59], v[20:23]\nds_read_b128 v[80:83], v9 offset:32768\ns_xor_b32 s81, s81, s69\n" \
    "v_mfma_f32_16x16x32_bf16 v[28:31], v[68:71], v[56:59], v[28:31]\nds_read_b128 v[56:59], v9 offset:36864\ns_mov_b32 s68, 0\ns_waitcnt lgkmcnt(3)\n" \
    "v_mfma_f32_16x16x32_bf16 v[16:19], a[76:79], v[76:79], v[16:19]\ns_waitcnt vmcnt(11)\nv_mov_b32 v15, s50\ns_mov_b64 exec, 1\n" \
    "buffer_store_dword v15, off, s[64:67], s48 sc1\ns_mov_b64 exec, -1\ns_xor_b32 s48, s48, 0x8000\nv_mfma_f32_16x16x32_bf16 v[24:27], v[72:75], v[76:79], v[24:27]\n" \
    "ds_read_b128 v[68:71], v3 offset:21504\nv_mfma_f32_16x16x32_bf16 v[20:23], a[76:79], v[60:63], v[20:23]\nds_read_b128 v[76:79], v10 offset:32768\n" \
    "v_mfma_f32_16x16x32_bf16 v[28:31], v[72:75], v[60:63], v[28:31]\nds_read_b128 v[60:63], v10 offset:36864\ns_waitcnt lgkmcnt(3)\n" \
    "v_mfma_f32_16x16x32_bf16 v[16:19], a[80:83], v[80:83], v[16:19]\nv_mfma_f32_16x16x32_bf16 v[24:27], v[64:67], v[80:83], v[24:27]\nds_read_b128 v[72:75], v3 offset:22528\n" \
    "v_mfma_f32_16x16x32_bf16 v[20:23], a[80:83], v[56:59], v[20:23]\nds_read_b128 v[80:83], v9 offset:33792\nv_mfma_f32_16x16x32_bf16 v[28:31], v[64:67], v[56:59], v[28:31]\n" \
    "ds_read_b128 v[56:59], v9 offset:37888\ns_waitcnt lgkmcnt(3)\nv_mfma_f32_16x16x32_bf16 v[16:19], a[84:87], v[76:79], v[16:19]\n" \
    "v_mfma_f32_16x16x32_bf16 v[24:27], v[68:71], v[76:79], v[24:27]\nds_read_b128 v[64:67], v3 offset:23552\nv_mfma_f32_16x16x32_bf16 v[20:23], a[84:87], v[60:63], v[20:23]\n" \
    "ds_read_b128 v[76:79], v10 offset:33792\nv_mfma_f32_16x16x32_bf16 v[28:31], v[68:71], v[60:63], v[28:31]\nds_read_b128 v[60:63], v10 offset:37888\ns_waitcnt lgkmcnt(3)\n" \
    "v_mfma_f32_16x16x32_bf16 v[16:19], a[88:91], v[80:83], v[16:19]\nv_mfma_f32_16x16x32_bf16 v[24:27], v[72:75], v[80:83], v[24:27]\ns_waitcnt vmcnt(5)\ns_barrier\n" \
    "v_mfma_f32_16x16x32_bf16 v[20:23], a[88:91], v[56:59], v[20:23]\nds_read_b128 v[68:71], v9 offset:0\nv_mfma_f32_16x16x32_bf16 v[28:31], v[72:75], v[56:59], v[28:31]\n" \
    "ds_read_b128 v[56:59], v9 offset:4096\ns_add_u32 m0, s41, 16384\ns_waitcnt lgkmcnt(2)\nv_mfma_f32_16x16x32_bf16 v[16:19], a[92:95], v[76:79], v[16:19]\n" \
    "s_xor_b32 s85, s62, 512\ns_add_u32 s85, s85, s84\nv_mfma_f32_16x16x32_bf16 v[24:27], v[64:67], v[76:79], v[24:27]\nbuffer_load_dwordx4 v11, s[64:67], s85 offen sc1 lds\n" \
    "v_mfma_f32_16x16x32_bf16 v[20:23], a[92:95], v[60:63], v[20:23]\nds_read_b128 v[72:75], v10 offset:0\nv_mfma_f32_16x16x32_bf16 v[28:31], v[64:67], v[60:63], v[28:31]\n" \
    "ds_read_b128 v[60:63], v10 offset:4096\ns_waitcnt lgkmcnt(2)\nv_mfma_f32_16x16x32_bf16 v[16:19], a[96:99], v[68:71], v[16:19]\n" \
    "buffer_load_dwordx4 v12, s[64:67], s85 offen offset:1024 sc1 lds\ns_add_u32 m0, s41, 40960\nv_mfma_f32_16x16x32_bf16 v[24:27], a[104:107], v[68:71], v[24:27]\n" \
    "s_xor_b32 s85, s62, 768\ns_add_u32 s85, s85, s84\nv_mfma_f32_16x16x32_bf16 v[20:23], a[96:99], v[56:59], v[20:23]\nds_read_b128 v[64:67], v9 offset:1024\n" \
    "buffer_load_dwordx4 v11, s[64:67], s85 offen sc1 lds\nv_mfma_f32_16x16x32_bf16 v[28:31], a[104:107], v[56:59], v[28:31]\nds_read_b128 v[56:59], v9 offset:5120\n" \
    "s_waitcnt lgkmcnt(2)\nv_mfma_f32_16x16x32_bf16 v[16:19], a[100:103], v[72:75], v[16:19]\nv_mfma_f32_16x16x32_bf16 v[24:27], a[108:111], v[72:75], v[24:27]\n" \
    "buffer_load_dwordx4 v12, s[64:67], s85 offen offset:1024 sc1 lds\nv_mfma_f32_16x16x32_bf16 v[20:23], a[100:103], v[60:63], v[20:23]\n" \
    "ds_read_b128 v[68:71], v10 offset:1024\nv_mfma_f32_16x16x32_bf16 v[28:31], a[108:111], v[60:63], v[28:31]\nds_read_b128 v[60:63], v10 offset:5120\ns_waitcnt lgkmcnt(2)\n" \
    "v_mfma_f32_16x16x32_bf16 v[16:19], a[112:115], v[64:67], v[16:19]\nv_mfma_f32_16x16x32_bf16 v[24:27], a[120:123], v[64:67], v[24:27]\n" \
    "v_mfma_f32_16x16x32_bf16 v[20:23], a[112:115], v[56:59], v[20:23]\nds_read_b128 v[64:67], v9 offset:8192\n" \
    "v_mfma_f32_16x16x32_bf16 v[28:31], a[120:123], v[56:59], v[28:31]\nds_read_b128 v[56:59], v9 offset:12288\ns_waitcnt lgkmcnt(2)\n" \
    "v_mfma_f32_16x16x32_bf16 v[16:19], a[116:119], v[68:71], v[16:19]\nv_mfma_f32_16x16x32_bf16 v[24:27], a[124:127], v[68:71], v[24:27]\n" \
    "v_mfma_f32_16x16x32_bf16 v[20:23], a[116:119], v[60:63], v[20:23]\nds_read_b128 v[68:71], v10 offset:8192\n" \
    "v_mfma_f32_16x16x32_bf16 v[28:31], a[124:127], v[60:63], v[28:31]\nds_read_b128 v[60:63], v10 offset:12288\ns_waitcnt lgkmcnt(2)\n" \
    "v_mfma_f32_16x16x32_bf16 v[16:19], a[128:131], v[64:67], v[16:19]\nv_readfirstlane_b32 s74, v13\nv_mfma_f32_16x16x32_bf16 v[24:27], a[136:139], v[64:67], v[24:27]\n" \
    "s_and_b32 s75, s74, 127\ns_lshr_b32 s69, s74, 8\nv_mfma_f32_16x16x32_bf16 v[20:23], a[128:131], v[56:59], v[20:23]\nds_read_b128 v[64:67], v9 offset:9216\n" \
    "s_sub_u32 s69, s69, s80\nv_mfma_f32_16x16x32_bf16 v[28:31], a[136:139], v[56:59], v[28:31]\nds_read_b128 v[56:59], v9 offset:13312\ns_bfe_i32 s69, s69, 0x180000\n" \
    "s_waitcnt lgkmcnt(2)\nv_mfma_f32_16x16x32_bf16 v[16:19], a[132:135], v[68:71], v[16:19]\nds_read_b128 v[72:75], v10 offset:9216\ns_cmp_eq_u32 s75, 0\n" \
    "s_cselect_b32 s69, 0, s69\nv_mfma_f32_16x16x32_bf16 v[24:27], a[140:143], v[68:71], v[24:27]\nds_read_b128 v[68:71], v10 offset:13312\ns_add_u32 s80, s80, s69\n" \
    "v_mfma_f32_16x16x32_bf16 v[20:23], a[132:135], v[60:63], v[20:23]\ns_lshl_b32 s74, s74, 5\ns_and_b32 s74, s74, 0x1000\n" \
    "v_mfma_f32_16x16x32_bf16 v[28:31], a[140:143], v[60:63], v[28:31]\ns_andn2_b32 s81, s81, 0x1000\ns_or_b32 s81, s81, s74\ns_waitcnt lgkmcnt(2)\n" \
    "v_mfma_f32_16x16x32_bf16 v[16:19], a[144:147], v[64:67], v[16:19]\nv_mfma_f32_16x16x32_bf16 v[24:27], a[152:155], v[64:67], v[24:27]\ns_waitcnt lgkmcnt(0)\ns_barrier\n" \
    "v_mfma_f32_16x16x32_bf16 v[16:19], a[148:151], v[72:75], v[16:19]\ns_add_u32 m0, s41, 0\ns_xor_b32 s85, s62, 0\n" \
    "v_mfma_f32_16x16x32_bf16 v[24:27], a[156:159], v[72:75], v[24:27]\ns_add_u32 s85, s85, s84\nbuffer_load_dwordx4 v11, s[64:67], s85 offen sc1 lds\n" \
    "v_mfma_f32_16x16x32_bf16 v[20:23], a[144:147], v[56:59], v[20:23]\ns_nop 1\nv_cvt_pk_bf16_f32 v32, v16, v17\nv_cvt_pk_bf16_f32 v33, v18, v19\n" \
    "v_mfma_f32_16x16x32_bf16 v[28:31], a[152:155], v[56:59], v[28:31]\nv_cvt_pk_bf16_f32 v34, v24, v25\nv_cvt_pk_bf16_f32 v35, v26, v27\n" \
    "v_mfma_f32_16x16x32_bf16 v[20:23], a[148:151], v[68:71], v[20:23]\nbuffer_load_dwordx4 v12, s[64:67], s85 offen offset:1024 sc1 lds\ns_add_u32 m0, s41, 8192\n" \
    "v_mfma_f32_16x16x32_bf16 v[28:31], a[156:159], v[68:71], v[28:31]\ns_xor_b32 s85, s62, 256\ns_add_u32 s85, s85, s84\nds_write_b128 v14, v[32:35] offset:0\n" \
    "buffer_load_dwordx4 v11, s[64:67], s85 offen sc1 lds\nbuffer_load_dwordx4 v12, s[64:67], s85 offen offset:1024 sc1 lds\ns_mov_b32 s40, s84\ns_sub_u32 s69, s50, s35\n" \
    "s_max_u32 s76, s35, 1\ns_add_u32 s69, s69, s76\ns_sub_u32 s69, s69, s35\ns_cmp_eq_u32 s69, 2\ns_cbranch_scc1 .Lv12_%=\n.Lv4_%=:\n.Lv13_%=:\ns_cmp_lt_u32 s75, s35\n" \
    "s_cbranch_scc1 .Lv14_%=\ns_cmp_eq_u32 s36, 1\ns_cbranch_scc1 .Lv9_%=\ns_cmp_eq_u32 s36, 2\ns_cbranch_scc1 .Lv10_%=\ns_cmp_eq_u32 s36, 3\ns_cbranch_scc1 .Lv11_%=\n.Lv8_%=:\n" \
    ".Lv15_%=:\ns_memrealtime s[72:73]\ns_waitcnt lgkmcnt(0)\ns_mov_b32 s68, 0\ns_sub_u32 s76, s80, s45\ns_sub_u32 s69, s72, s76\ns_cmp_lt_i32 s69, -8\ns_cbranch_scc1 .Lv18_%=\n" \
    "s_xor_b32 s90, s81, 0x800\n.Lv20_%=:\ns_memrealtime s[72:73]\ns_waitcnt lgkmcnt(0)\ns_sub_u32 s72, s72, s80\ns_cmp_lt_i32 s72, 0\ns_cbranch_scc1 .Lv20_%=\n" \
    "buffer_load_dwordx4 v[72:75], v2, s[64:67], s81 offen offset:0 sc1\nv_cvt_pk_bf16_f32 v36, v20, v21\nv_cvt_pk_bf16_f32 v37, v22, v23\nv_cvt_pk_bf16_f32 v38, v28, v29\n" \
    "buffer_load_dwordx4 v[76:79], v2, s[64:67], s81 offen offset:1024 sc1\nv_cvt_pk_bf16_f32 v39, v30, v31\nds_write_b128 v14, v[36:39] offset:4096\n" \
    "ds_read_b128 v[24:27], v3 offset:0\nbuffer_load_dwordx4 v[80:83], v2, s[64:67], s90 offen offset:0 sc1\nds_read_b128 v[28:31], v3 offset:1024\n" \
    "ds_read_b128 v[32:35], v3 offset:8192\nds_read_b128 v[36:39], v3 offset:9216\nbuffer_load_dwordx4 v[84:87], v2, s[64:67], s90 offen offset:1024 sc1\n" \
    "ds_read_b128 v[40:43], v3 offset:16384\nds_read_b128 v[44:47], v3 offset:17408\nds_read_b128 v[48:51], v3 offset:2048\nds_read_b128 v[52:55], v3 offset:3072\n" \
    "ds_read_b128 v[56:59], v3 offset:4096\nds_read_b128 v[60:63], v3 offset:5120\nds_read_b128 v[64:67], v3 offset:6144\nds_read_b128 v[68:71], v3 offset:7168\n.Lv19_%=:\n" \
    "v_mov_b64 v[16:17], 0\nv_mov_b64 v[18:19], 0\nv_mov_b64 v[20:21], 0\nv_mov_b64 v[22:23], 0\ns_waitcnt vmcnt(3) lgkmcnt(10)\ns_nop 0\n" \
    "v_smfmac_f32_16x16x64_bf16 v[20:23], v[72:75], v[24:31], v0\nds_write_b128 v1, v[72:75] offset:24576\nv_max3_u32 v252, v72, v73, v74\n" \
    "v_smfmac_f32_16x16x64_bf16 v[16:19], v[72:75], a[0:7], v0\ns_waitcnt vmcnt(2)\nds_write_b128 v1, v[76:79] offset:28672\ns_waitcnt lgkmcnt(10)\n" \
    "v_max3_u32 v252, v252, v75, v76\nv_max3_u32 v252, v252, v77, v78\nv_cmp_u_f32 vcc, v252, v79\nv_smfmac_f32_16x16x64_bf16 v[20:23], v[76:79], v[32:39], v0\n" \
    "s_cbranch_vccnz .Lv26_%=\n.Lv22_%=:\nv_smfmac_f32_16x16x64_bf16 v[16:19], v[76:79], a[32:39], v0\nds_read_b128 v[24:27], v3 offset:10240\n" \
    "ds_read_b128 v[28:31], v3 offset:11264\ns_waitcnt vmcnt(1)\nv_max3_u32 v252, v80, v81, v82\nv_cmp_u_f32 vcc, v252, v83\ns_nop 0\ns_cbranch_vccnz .Lv27_%=\n.Lv23_%=:\n" \
    "s_waitcnt lgkmcnt(10)\nv_smfmac_f32_16x16x64_bf16 v[20:23], v[80:83], v[40:47], v0\nds_write_b128 v1, v[80:83] offset:32768\nds_read_b128 v[36:39], v3 offset:13312\n" \
    "s_waitcnt lgkmcnt(4)\ns_barrier\nds_read_b128 v[88:91], v1 offset:25600\nds_read_b128 v[40:43], v1 offset:26624\n" \
    "v_smfmac_f32_16x16x64_bf16 v[16:19], v[80:83], a[64:71], v0\nds_read_b128 v[32:35], v3 offset:12288\nds_read_b128 v[44:47], v1 offset:27648\ns_waitcnt vmcnt(0)\n" \
    "v_max3_u32 v252, v84, v85, v86\nv_cmp_u_f32 vcc, v252, v87\ns_nop 0\ns_cbranch_vccnz .Lv28_%=\n.Lv24_%=:\nv_smfmac_f32_16x16x64_bf16 v[20:23], v[84:87], a[104:111], v0\n" \
    "ds_write_b128 v1, v[84:87] offset:36864\nv_smfmac_f32_16x16x64_bf16 v[16:19], v[84:87], a[96:103], v0\nds_read_b128 v[72:75], v1 offset:29696\ns_waitcnt lgkmcnt(5)\n" \
    "v_smfmac_f32_16x16x64_bf16 v[20:23], v[88:91], v[48:55], v0\nds_read_b128 v[48:51], v1 offset:30720\nv_smfmac_f32_16x16x64_bf16 v[16:19], v[88:91], a[8:15], v0\n" \
    "ds_read_b128 v[52:55], v1 offset:31744\ns_waitcnt lgkmcnt(6)\nv_smfmac_f32_16x16x64_bf16 v[20:23], v[40:43], v[56:63], v0\nds_read_b128 v[56:59], v3 offset:14336\n" \
    "s_waitcnt lgkmcnt(4)\ns_barrier\nv_smfmac_f32_16x16x64_bf16 v[16:19], v[40:43], a[16:23], v0\nds_read_b128 v[60:63], v3 offset:15360\n" \
    "v_smfmac_f32_16x16x64_bf16 v[20:23], v[44:47], v[64:71], v0\nds_read_b128 v[40:43], v1 offset:33792\nv_smfmac_f32_16x16x64_bf16 v[16:19], v[44:47], a[24:31], v0\n" \
    "ds_read_b128 v[64:67], v3 offset:18432\ns_waitcnt lgkmcnt(6)\nv_smfmac_f32_16x16x64_bf16 v[20:23], v[72:75], v[24:31], v0\nds_read_b128 v[68:71], v3 offset:19456\n" \
    "v_smfmac_f32_16x16x64_bf16 v[16:19], v[72:75], a[40:47], v0\nds_read_b128 v[44:47], v1 offset:34816\ns_waitcnt lgkmcnt(7)\n" \
    "v_smfmac_f32_16x16x64_bf16 v[20:23], v[48:51], v[32:39], v0\nds_read_b128 v[24:27], v3 offset:20480\nv_smfmac_f32_16x16x64_bf16 v[16:19], v[48:51], a[48:55], v0\n" \
    "ds_read_b128 v[28:31], v3 offset:21504\ns_waitcnt lgkmcnt(6)\nv_smfmac_f32_16x16x64_bf16 v[20:23], v[52:55], v[56:63], v0\nds_read_b128 v[48:51], v1 offset:35840\n" \
    "v_smfmac_f32_16x16x64_bf16 v[16:19], v[52:55], a[56:63], v0\nds_read_b128 v[32:35], v3 offset:22528\ns_waitcnt lgkmcnt(5)\n" \
    "v_smfmac_f32_16x16x64_bf16 v[20:23], v[40:43], v[64:71], v0\nds_read_b128 v[36:39], v3 offset:23552\nv_smfmac_f32_16x16x64_bf16 v[16:19], v[40:43], a[72:79], v0\n" \
    "ds_read_b128 v[40:43], v1 offset:37888\ns_waitcnt lgkmcnt(4)\nv_smfmac_f32_16x16x64_bf16 v[20:23], v[44:47], v[24:31], v0\nds_read_b128 v[24:27], v1 offset:38912\n" \
    "v_smfmac_f32_16x16x64_bf16 v[16:19], v[44:47], a[80:87], v0\nds_read_b128 v[28:31], v1 offset:39936\ns_waitcnt lgkmcnt(3)\n" \
    "v_smfmac_f32_16x16x64_bf16 v[20:23], v[48:51], v[32:39], v0\ns_waitcnt lgkmcnt(2)\nv_smfmac_f32_16x16x64_bf16 v[20:23], v[40:43], a[120:127], v0\ns_waitcnt lgkmcnt(1)\n" \
    "v_smfmac_f32_16x16x64_bf16 v[20:23], v[24:27], a[136:143], v0\ns_waitcnt lgkmcnt(0)\nv_smfmac_f32_16x16x64_bf16 v[20:23], v[28:31], a[152:159], v0\ns_nop 2\n" \
    "v_smfmac_f32_16x16x64_bf16 v[16:19], v[48:51], a[88:95], v0\ns_nop 2\nv_smfmac_f32_16x16x64_bf16 v[16:19], v[40:43], a[112:119], v0\nv_permlane32_swap_b32 v20, v22\n" \
    "v_smfmac_f32_16x16x64_bf16 v[16:19], v[24:27], a[128:135], v0\nv_permlane32_swap_b32 v21, v23\nv_smfmac_f32_16x16x64_bf16 v[16:19], v[28:31], a[144:151], v0\n" \
    "v_add_f32 v20, v20, v22\nv_add_f32 v21, v21, v23\nv_cvt_pk_bf16_f32 v20, v20, v21\nds_write_b16 v4, v20 offset:8\nds_write_b16_d16_hi v4, v20 offset:24\n" \
    "v_mov_b32 v15, v15\nv_mov_b32 v15, v15\nv_mov_b32 v15, v15\nv_permlane32_swap_b32 v16, v18\nv_permlane32_swap_b32 v17, v19\nv_pk_add_f32 v[16:17], v[16:17], v[18:19]\n" \
    "v_cvt_pk_bf16_f32 v16, v16, v17\nds_write_b16 v4, v16 offset:0\nds_write_b16_d16_hi v4, v16 offset:16\ns_branch .Lv7_%=\n.Lv9_%=:\n.Lv31_%=:\ns_memrealtime s[72:73]\n" \
    "s_waitcnt lgkmcnt(0)\ns_mov_b32 s68, 0\ns_sub_u32 s76, s80, s45\ns_sub_u32 s69, s72, s76\ns_cmp_lt_i32 s69, -8\ns_cbranch_scc1 .Lv34_%=\ns_xor_b32 s90, s81, 0x800\n" \
    ".Lv36_%=:\ns_memrealtime s[72:73]\ns_waitcnt lgkmcnt(0)\ns_sub_u32 s72, s72, s80\ns_cmp_lt_i32 s72, 0\ns_cbranch_scc1 .Lv36_%=\n" \
    "buffer_load_dwordx4 v[72:75], v2, s[64:67], s81 offen offset:0 sc1\nv_cvt_pk_bf16_f32 v36, v20, v21\nv_cvt_pk_bf16_f32 v37, v22, v23\nv_cvt_pk_bf16_f32 v38, v28, v29\n" \
    "buffer_load_dwordx4 v[76:79], v2, s[64:67], s81 offen offset:1024 sc1\nv_cvt_pk_bf16_f32 v39, v30, v31\nds_write_b128 v14, v[36:39] offset:4096\n" \
    "ds_read_b128 v[24:27], v3 offset:2048\nbuffer_load_dwordx4 v[80:83], v2, s[64:67], s90 offen offset:0 sc1\nds_read_b128 v[28:31], v3 offset:3072\n" \
    "ds_read_b128 v[32:35], v3 offset:10240\nds_read_b128 v[36:39], v3 offset:11264\nbuffer_load_dwordx4 v[84:87], v2, s[64:67], s90 offen offset:1024 sc1\n" \
    "ds_read_b128 v[40:43], v3 offset:18432\nds_read_b128 v[44:47], v3 offset:19456\nds_read_b128 v[48:51], v3 offset:0\nds_read_b128 v[52:55], v3 offset:1024\n" \
    "ds_read_b128 v[56:59], v3 offset:4096\nds_read_b128 v[60:63], v3 offset:5120\nds_read_b128 v[64:67], v3 offset:6144\nds_read_b128 v[68:71], v3 offset:7168\n.Lv35_%=:\n" \
    "v_mov_b64 v[16:17], 0\nv_mov_b64 v[18:19], 0\nv_mov_b64 v[20:21], 0\nv_mov_b64 v[22:23], 0\ns_waitcnt vmcnt(3) lgkmcnt(10)\ns_nop 0\n" \
    "v_smfmac_f32_16x16x64_bf16 v[20:23], v[72:75], v[24:31], v0\nds_write_b128 v1, v[72:75] offset:25600\nv_max3_u32 v252, v72, v73, v74\n" \
    "v_smfmac_f32_16x16x64_bf16 v[16:19], v[72:75], a[8:15], v0\ns_waitcnt vmcnt(2)\nds_write_b128 v1, v[76:79] offset:29696\ns_waitcnt lgkmcnt(10)\n" \
    "v_max3_u32 v252, v252, v75, v76\nv_max3_u32 v252, v252, v77, v78\nv_cmp_u_f32 vcc, v252, v79\nv_smfmac_f32_16x16x64_bf16 v[20:23], v[76:79], v[32:39], v0\n" \
    "s_cbranch_vccnz .Lv42_%=\n.Lv38_%=:\nv_smfmac_f32_16x16x64_bf16 v[16:19], v[76:79], a[40:47], v0\nds_read_b128 v[24:27], v3 offset:8192\n" \
    "ds_read_b128 v[28:31], v3 offset:9216\ns_waitcnt vmcnt(1)\nv_max3_u32 v252, v80, v81, v82\nv_cmp_u_f32 vcc, v252, v83\ns_nop 0\ns_cbranch_vccnz .Lv43_%=\n.Lv39_%=:\n" \
    "s_waitcnt lgkmcnt(10)\nv_smfmac_f32_16x16x64_bf16 v[20:23], v[80:83], v[40:47], v0\nds_write_b128 v1, v[80:83] offset:33792\nds_read_b128 v[36:39], v3 offset:13312\n" \
    "s_waitcnt lgkmcnt(4)\ns_barrier\nds_read_b128 v[88:91], v1 offset:24576\nds_read_b128 v[40:43], v1 offset:26624\n" \
    "v_smfmac_f32_16x16x64_bf16 v[16:19], v[80:83], a[72:79], v0\nds_read_b128 v[32:35], v3 offset:12288\nds_read_b128 v[44:47], v1 offset:27648\ns_waitcnt vmcnt(0)\n" \
    "v_max3_u32 v252, v84, v85, v86\nv_cmp_u_f32 vcc, v252, v87\ns_nop 0\ns_cbranch_vccnz .Lv44_%=\n.Lv40_%=:\nv_smfmac_f32_16x16x64_bf16 v[20:23], v[84:87], a[120:127], v0\n" \
    "ds_write_b128 v1, v[84:87] offset:37888\nv_smfmac_f32_16x16x64_bf16 v[16:19], v[84:87], a[112:119], v0\nds_read_b128 v[72:75], v1 offset:28672\ns_waitcnt lgkmcnt(5)\n" \
    "v_smfmac_f32_16x16x64_bf16 v[20:23], v[88:91], v[48:55], v0\nds_read_b128 v[48:51], v1 offset:30720\nv_smfmac_f32_16x16x64_bf16 v[16:19], v[88:91], a[0:7], v0\n" \
    "ds_read_b128 v[52:55], v1 offset:31744\ns_waitcnt lgkmcnt(6)\nv_smfmac_f32_16x16x64_bf16 v[20:23], v[40:43], v[56:63], v0\nds_read_b128 v[56:59], v3 offset:14336\n" \
    "s_waitcnt lgkmcnt(4)\ns_barrier\nv_smfmac_f32_16x16x64_bf16 v[16:19], v[40:43], a[16:23], v0\nds_read_b128 v[60:63], v3 offset:15360\n" \
    "v_smfmac_f32_16x16x64_bf16 v[20:23], v[44:47], v[64:71], v0\nds_read_b128 v[40:43], v1 offset:32768\nv_smfmac_f32_16x16x64_bf16 v[16:19], v[44:47], a[24:31], v0\n" \
    "ds_read_b128 v[64:67], v3 offset:16384\ns_waitcnt lgkmcnt(6)\nv_smfmac_f32_16x16x64_bf16 v[20:23], v[72:75], v[24:31], v0\nds_read_b128 v[68:71], v3 offset:17408\n" \
    "v_smfmac_f32_16x16x64_bf16 v[16:19], v[72:75], a[32:39], v0\nds_read_b128 v[44:47], v1 offset:34816\ns_waitcnt lgkmcnt(7)\n" \
    "v_smfmac_f32_16x16x64_bf16 v[20:23], v[48:51], v[32:39], v0\nds_read_b128 v[24:27], v3 offset:20480\nv_smfmac_f32_16x16x64_bf16 v[16:19], v[48:51], a[48:55], v0\n" \
    "ds_read_b128 v[28:31], v3 offset:21504\ns_waitcnt lgkmcnt(6)\nv_smfmac_f32_16x16x64_bf16 v[20:23], v[52:55], v[56:63], v0\nds_read_b128 v[48:51], v1 offset:35840\n" \
    "v_smfmac_f32_16x16x64_bf16 v[16:19], v[52:55], a[56:63], v0\nds_read_b128 v[32:35], v3 offset:22528\ns_waitcnt lgkmcnt(5)\n" \
    "v_smfmac_f32_16x16x64_bf16 v[20:23], v[40:43], v[64:71], v0\nds_read_b128 v[36:39], v3 offset:23552\nv_smfmac_f32_16x16x64_bf16 v[16:19], v[40:43], a[64:71], v0\n" \
    "ds_read_b128 v[40:43], v1 offset:36864\ns_waitcnt lgkmcnt(4)\nv_smfmac_f32_16x16x64_bf16 v[20:23], v[44:47], v[24:31], v0\nds_read_b128 v[24:27], v1 offset:38912\n" \
    "v_smfmac_f32_16x16x64_bf16 v[16:19], v[44:47], a[80:87], v0\nds_read_b128 v[28:31], v1 offset:39936\ns_waitcnt lgkmcnt(3)\n" \
    "v_smfmac_f32_16x16x64_bf16 v[20:23], v[48:51], v[32:39], v0\ns_waitcnt lgkmcnt(2)\nv_smfmac_f32_16x16x64_bf16 v[20:23], v[40:43], a[104:111], v0\ns_waitcnt lgkmcnt(1)\n" \
    "v_smfmac_f32_16x16x64_bf16 v[20:23], v[24:27], a[136:143], v0\ns_waitcnt lgkmcnt(0)\nv_smfmac_f32_16x16x64_bf16 v[20:23], v[28:31], a[152:159], v0\ns_nop 2\n" \
    "v_smfmac_f32_16x16x64_bf16 v[16:19], v[48:51], a[88:95], v0\ns_nop 2\nv_smfmac_f32_16x16x64_bf16 v[16:19], v[40:43], a[96:103], v0\nv_permlane32_swap_b32 v20, v22\n" \
    "v_smfmac_f32_16x16x64_bf16 v[16:19], v[24:27], a[128:135], v0\nv_permlane32_swap_b32 v21, v23\nv_smfmac_f32_16x16x64_bf16 v[16:19], v[28:31], a[144:151], v0\n" \
    "v_add_f32 v20, v20, v22\nv_add_f32 v21, v21, v23\nv_cvt_pk_bf16_f32 v20, v20, v21\nds_write_b16 v4, v20 offset:8\nds_write_b16_d16_hi v4, v20 offset:24\n" \
    "v_mov_b32 v15, v15\nv_mov_b32 v15, v15\nv_mov_b32 v15, v15\nv_permlane32_swap_b32 v16, v18\nv_permlane32_swap_b32 v17, v19\nv_pk_add_f32 v[16:17], v[16:17], v[18:19]\n" \
    "v_cvt_pk_bf16_f32 v16, v16, v17\nds_write_b16 v4, v16 offset:0\nds_write_b16_d16_hi v4, v16 offset:16\ns_branch .Lv7_%=\n.Lv10_%=:\n.Lv47_%=:\ns_memrealtime s[72:73]\n" \
    "s_waitcnt lgkmcnt(0)\ns_mov_b32 s68, 0\ns_sub_u32 s76, s80, s45\ns_sub_u32 s69, s72, s76\ns_cmp_lt_i32 s69, -8\ns_cbranch_scc1 .Lv50_%=\ns_xor_b32 s90, s81, 0x800\n" \
    ".Lv52_%=:\ns_memrealtime s[72:73]\ns_waitcnt lgkmcnt(0)\ns_sub_u32 s72, s72, s80\ns_cmp_lt_i32 s72, 0\ns_cbranch_scc1 .Lv52_%=\n" \
    "buffer_load_dwordx4 v[72:75], v2, s[64:67], s81 offen offset:0 sc1\nv_cvt_pk_bf16_f32 v36, v20, v21\nv_cvt_pk_bf16_f32 v37, v22, v23\nv_cvt_pk_bf16_f32 v38, v28, v29\n" \
    "buffer_load_dwordx4 v[76:79], v2, s[64:67], s81 offen offset:1024 sc1\nv_cvt_pk_bf16_f32 v39, v30, v31\nds_write_b128 v14, v[36:39] offset:4096\n" \
    "ds_read_b128 v[24:27], v3 offset:4096\nbuffer_load_dwordx4 v[80:83], v2, s[64:67], s90 offen offset:0 sc1\nds_read_b128 v[28:31], v3 offset:5120\n" \
    "ds_read_b128 v[32:35], v3 offset:12288\nds_read_b128 v[36:39], v3 offset:13312\nbuffer_load_dwordx4 v[84:87], v2, s[64:67], s90 offen offset:1024 sc1\n" \
    "ds_read_b128 v[40:43], v3 offset:20480\nds_read_b128 v[44:47], v3 offset:21504\nds_read_b128 v[48:51], v3 offset:0\nds_read_b128 v[52:55], v3 offset:1024\n" \
    "ds_read_b128 v[56:59], v3 offset:2048\nds_read_b128 v[60:63], v3 offset:3072\nds_read_b128 v[64:67], v3 offset:6144\nds_read_b128 v[68:71], v3 offset:7168\n.Lv51_%=:\n" \
    "v_mov_b64 v[16:17], 0\nv_mov_b64 v[18:19], 0\nv_mov_b64 v[20:21], 0\nv_mov_b64 v[22:23], 0\ns_waitcnt vmcnt(3) lgkmcnt(10)\ns_nop 0\n" \
    "v_smfmac_f32_16x16x64_bf16 v[20:23], v[72:75], v[24:31], v0\nds_write_b128 v1, v[72:75] offset:26624\nv_max3_u32 v252, v72, v73, v74\n" \
    "v_smfmac_f32_16x16x64_bf16 v[16:19], v[72:75], a[16:23], v0\ns_waitcnt vmcnt(2)\nds_write_b128 v1, v[76:79] offset:30720\ns_waitcnt lgkmcnt(10)\n" \
    "v_max3_u32 v252, v252, v75, v76\nv_max3_u32 v252, v252, v77, v78\nv_cmp_u_f32 vcc, v252, v79\nv_smfmac_f32_16x16x64_bf16 v[20:23], v[76:79], v[32:39], v0\n" \
    "s_cbranch_vccnz .Lv58_%=\n.Lv54_%=:\nv_smfmac_f32_16x16x64_bf16 v[16:19], v[76:79], a[48:55], v0\nds_read_b128 v[24:27], v3 offset:8192\n" \
    "ds_read_b128 v[28:31], v3 offset:9216\ns_waitcnt vmcnt(1)\nv_max3_u32 v252, v80, v81, v82\nv_cmp_u_f32 vcc, v252, v83\ns_nop 0\ns_cbranch_vccnz .Lv59_%=\n.Lv55_%=:\n" \
    "s_waitcnt lgkmcnt(10)\nv_smfmac_f32_16x16x64_bf16 v[20:23], v[80:83], v[40:47], v0\nds_write_b128 v1, v[80:83] offset:34816\nds_read_b128 v[36:39], v3 offset:11264\n" \
    "s_waitcnt lgkmcnt(4)\ns_barrier\nds_read_b128 v[88:91], v1 offset:24576\nds_read_b128 v[40:43], v1 offset:25600\n" \
    "v_smfmac_f32_16x16x64_bf16 v[16:19], v[80:83], a[80:87], v0\nds_read_b128 v[32:35], v3 offset:10240\nds_read_b128 v[44:47], v1 offset:27648\ns_waitcnt vmcnt(0)\n" \
    "v_max3_u32 v252, v84, v85, v86\nv_cmp_u_f32 vcc, v252, v87\ns_nop 0\ns_cbranch_vccnz .Lv60_%=\n.Lv56_%=:\nv_smfmac_f32_16x16x64_bf16 v[20:23], v[84:87], a[136:143], v0\n" \
    "ds_write_b128 v1, v[84:87] offset:38912\nv_smfmac_f32_16x16x64_bf16 v[16:19], v[84:87], a[128:135], v0\nds_read_b128 v[72:75], v1 offset:28672\ns_waitcnt lgkmcnt(5)\n" \
    "v_smfmac_f32_16x16x64_bf16 v[20:23], v[88:91], v[48:55], v0\nds_read_b128 v[48:51], v1 offset:29696\nv_smfmac_f32_16x16x64_bf16 v[16:19], v[88:91], a[0:7], v0\n" \
    "ds_read_b128 v[52:55], v1 offset:31744\ns_waitcnt lgkmcnt(6)\nv_smfmac_f32_16x16x64_bf16 v[20:23], v[40:43], v[56:63], v0\nds_read_b128 v[56:59], v3 offset:14336\n" \
    "s_waitcnt lgkmcnt(4)\ns_barrier\nv_smfmac_f32_16x16x64_bf16 v[16:19], v[40:43], a[8:15], v0\nds_read_b128 v[60:63], v3 offset:15360\n" \
    "v_smfmac_f32_16x16x64_bf16 v[20:23], v[44:47], v[64:71], v0\nds_read_b128 v[40:43], v1 offset:32768\nv_smfmac_f32_16x16x64_bf16 v[16:19], v[44:47], a[24:31], v0\n" \
    "ds_read_b128 v[64:67], v3 offset:16384\ns_waitcnt lgkmcnt(6)\nv_smfmac_f32_16x16x64_bf16 v[20:23], v[72:75], v[24:31], v0\nds_read_b128 v[68:71], v3 offset:17408\n" \
    "v_smfmac_f32_16x16x64_bf16 v[16:19], v[72:75], a[32:39], v0\nds_read_b128 v[44:47], v1 offset:33792\ns_waitcnt lgkmcnt(7)\n" \
    "v_smfmac_f32_16x16x64_bf16 v[20:23], v[48:51], v[32:39], v0\nds_read_b128 v[24:27], v3 offset:18432\nv_smfmac_f32_16x16x64_bf16 v[16:19], v[48:51], a[40:47], v0\n" \
    "ds_read_b128 v[28:31], v3 offset:19456\ns_waitcnt lgkmcnt(6)\nv_smfmac_f32_16x16x64_bf16 v[20:23], v[52:55], v[56:63], v0\nds_read_b128 v[48:51], v1 offset:35840\n" \
    "v_smfmac_f32_16x16x64_bf16 v[16:19], v[52:55], a[56:63], v0\nds_read_b128 v[32:35], v3 offset:22528\ns_waitcnt lgkmcnt(5)\n" \
    "v_smfmac_f32_16x16x64_bf16 v[20:23], v[40:43], v[64:71], v0\nds_read_b128 v[36:39], v3 offset:23552\nv_smfmac_f32_16x16x64_bf16 v[16:19], v[40:43], a[64:71], v0\n" \
    "ds_read_b128 v[40:43], v1 offset:36864\ns_waitcnt lgkmcnt(4)\nv_smfmac_f32_16x16x64_bf16 v[20:23], v[44:47], v[24:31], v0\nds_read_b128 v[24:27], v1 offset:37888\n" \
    "v_smfmac_f32_16x16x64_bf16 v[16:19], v[44:47], a[72:79], v0\nds_read_b128 v[28:31], v1 offset:39936\ns_waitcnt lgkmcnt(3)\n" \
    "v_smfmac_f32_16x16x64_bf16 v[20:23], v[48:51], v[32:39], v0\ns_waitcnt lgkmcnt(2)\nv_smfmac_f32_16x16x64_bf16 v[20:23], v[40:43], a[104:111], v0\ns_waitcnt lgkmcnt(1)\n" \
    "v_smfmac_f32_16x16x64_bf16 v[20:23], v[24:27], a[120:127], v0\ns_waitcnt lgkmcnt(0)\nv_smfmac_f32_16x16x64_bf16 v[20:23], v[28:31], a[152:159], v0\ns_nop 2\n" \
    "v_smfmac_f32_16x16x64_bf16 v[16:19], v[48:51], a[88:95], v0\ns_nop 2\nv_smfmac_f32_16x16x64_bf16 v[16:19], v[40:43], a[96:103], v0\nv_permlane32_swap_b32 v20, v22\n" \
    "v_smfmac_f32_16x16x64_bf16 v[16:19], v[24:27], a[112:119], v0\nv_permlane32_swap_b32 v21, v23\nv_smfmac_f32_16x16x64_bf16 v[16:19], v[28:31], a[144:151], v0\n" \
    "v_add_f32 v20, v20, v22\nv_add_f32 v21, v21, v23\nv_cvt_pk_bf16_f32 v20, v20, v21\nds_write_b16 v4, v20 offset:8\nds_write_b16_d16_hi v4, v20 offset:24\n" \
    "v_mov_b32 v15, v15\nv_mov_b32 v15, v15\nv_mov_b32 v15, v15\nv_permlane32_swap_b32 v16, v18\nv_permlane32_swap_b32 v17, v19\nv_pk_add_f32 v[16:17], v[16:17], v[18:19]\n" \
    "v_cvt_pk_bf16_f32 v16, v16, v17\nds_write_b16 v4, v16 offset:0\nds_write_b16_d16_hi v4, v16 offset:16\ns_branch .Lv7_%=\n.Lv11_%=:\n.Lv63_%=:\ns_memrealtime s[72:73]\n" \
    "s_waitcnt lgkmcnt(0)\ns_mov_b32 s68, 0\ns_sub_u32 s76, s80, s45\ns_sub_u32 s69, s72, s76\ns_cmp_lt_i32 s69, -8\ns_cbranch_scc1 .Lv66_%=\ns_xor_b32 s90, s81, 0x800\n" \
    ".Lv68_%=:\ns_memrealtime s[72:73]\ns_waitcnt lgkmcnt(0)\ns_sub_u32 s72, s72, s80\ns_cmp_lt_i32 s72, 0\ns_cbranch_scc1 .Lv68_%=\n" \
    "buffer_load_dwordx4 v[72:75], v2, s[64:67], s81 offen offset:0 sc1\nv_cvt_pk_bf16_f32 v36, v20, v21\nv_cvt_pk_bf16_f32 v37, v22, v23\nv_cvt_pk_bf16_f32 v38, v28, v29\n" \
    "buffer_load_dwordx4 v[76:79], v2, s[64:67], s81 offen offset:1024 sc1\nv_cvt_pk_bf16_f32 v39, v30, v31\nds_write_b128 v14, v[36:39] offset:4096\n" \
    "ds_read_b128 v[24:27], v3 offset:6144\nbuffer_load_dwordx4 v[80:83], v2, s[64:67], s90 offen offset:0 sc1\nds_read_b128 v[28:31], v3 offset:7168\n" \
    "ds_read_b128 v[32:35], v3 offset:14336\nds_read_b128 v[36:39], v3 offset:15360\nbuffer_load_dwordx4 v[84:87], v2, s[64:67], s90 offen offset:1024 sc1\n" \
    "ds_read_b128 v[40:43], v3 offset:22528\nds_read_b128 v[44:47], v3 offset:23552\nds_read_b128 v[48:51], v3 offset:0\nds_read_b128 v[52:55], v3 offset:1024\n" \
    "ds_read_b128 v[56:59], v3 offset:2048\nds_read_b128 v[60:63], v3 offset:3072\nds_read_b128 v[64:67], v3 offset:4096\nds_read_b128 v[68:71], v3 offset:5120\n.Lv67_%=:\n" \
    "v_mov_b64 v[16:17], 0\nv_mov_b64 v[18:19], 0\nv_mov_b64 v[20:21], 0\nv_mov_b64 v[22:23], 0\ns_waitcnt vmcnt(3) lgkmcnt(10)\ns_nop 0\n" \
    "v_smfmac_f32_16x16x64_bf16 v[20:23], v[72:75], v[24:31], v0\nds_write_b128 v1, v[72:75] offset:27648\nv_max3_u32 v252, v72, v73, v74\n" \
    "v_smfmac_f32_16x16x64_bf16 v[16:19], v[72:75], a[24:31], v0\ns_waitcnt vmcnt(2)\nds_write_b128 v1, v[76:79] offset:31744\ns_waitcnt lgkmcnt(10)\n" \
    "v_max3_u32 v252, v252, v75, v76\nv_max3_u32 v252, v252, v77, v78\nv_cmp_u_f32 vcc, v252, v79\nv_smfmac_f32_16x16x64_bf16 v[20:23], v[76:79], v[32:39], v0\n" \
    "s_cbranch_vccnz .Lv74_%=\n.Lv70_%=:\nv_smfmac_f32_16x16x64_bf16 v[16:19], v[76:79], a[56:63], v0\nds_read_b128 v[24:27], v3 offset:8192\n" \
    "ds_read_b128 v[28:31], v3 offset:9216\ns_waitcnt vmcnt(1)\nv_max3_u32 v252, v80, v81, v82\nv_cmp_u_f32 vcc, v252, v83\ns_nop 0\ns_cbranch_vccnz .Lv75_%=\n.Lv71_%=:\n" \
    "s_waitcnt lgkmcnt(10)\nv_smfmac_f32_16x16x64_bf16 v[20:23], v[80:83], v[40:47], v0\nds_write_b128 v1, v[80:83] offset:35840\nds_read_b128 v[36:39], v3 offset:11264\n" \
    "s_waitcnt lgkmcnt(4)\ns_barrier\nds_read_b128 v[88:91], v1 offset:24576\nds_read_b128 v[40:43], v1 offset:25600\n" \
    "v_smfmac_f32_16x16x64_bf16 v[16:19], v[80:83], a[88:95], v0\nds_read_b128 v[32:35], v3 offset:10240\nds_read_b128 v[44:47], v1 offset:26624\ns_waitcnt vmcnt(0)\n" \
    "v_max3_u32 v252, v84, v85, v86\nv_cmp_u_f32 vcc, v252, v87\ns_nop 0\ns_cbranch_vccnz .Lv76_%=\n.Lv72_%=:\nv_smfmac_f32_16x16x64_bf16 v[20:23], v[84:87], a[152:159], v0\n" \
    "ds_write_b128 v1, v[84:87] offset:39936\nv_smfmac_f32_16x16x64_bf16 v[16:19], v[84:87], a[144:151], v0\nds_read_b128 v[72:75], v1 offset:28672\ns_waitcnt lgkmcnt(5)\n" \
    "v_smfmac_f32_16x16x64_bf16 v[20:23], v[88:91], v[48:55], v0\nds_read_b128 v[48:51], v1 offset:29696\nv_smfmac_f32_16x16x64_bf16 v[16:19], v[88:91], a[0:7], v0\n" \
    "ds_read_b128 v[52:55], v1 offset:30720\ns_waitcnt lgkmcnt(6)\nv_smfmac_f32_16x16x64_bf16 v[20:23], v[40:43], v[56:63], v0\nds_read_b128 v[56:59], v3 offset:12288\n" \
    "s_waitcnt lgkmcnt(4)\ns_barrier\nv_smfmac_f32_16x16x64_bf16 v[16:19], v[40:43], a[8:15], v0\nds_read_b128 v[60:63], v3 offset:13312\n" \
    "v_smfmac_f32_16x16x64_bf16 v[20:23], v[44:47], v[64:71], v0\nds_read_b128 v[40:43], v1 offset:32768\nv_smfmac_f32_16x16x64_bf16 v[16:19], v[44:47], a[16:23], v0\n" \
    "ds_read_b128 v[64:67], v3 offset:16384\ns_waitcnt lgkmcnt(6)\nv_smfmac_f32_16x16x64_bf16 v[20:23], v[72:75], v[24:31], v0\nds_read_b128 v[68:71], v3 offset:17408\n" \
    "v_smfmac_f32_16x16x64_bf16 v[16:19], v[72:75], a[32:39], v0\nds_read_b128 v[44:47], v1 offset:33792\ns_waitcnt lgkmcnt(7)\n" \
    "v_smfmac_f32_16x16x64_bf16 v[20:23], v[48:51], v[32:39], v0\nds_read_b128 v[24:27], v3 offset:18432\nv_smfmac_f32_16x16x64_bf16 v[16:19], v[48:51], a[40:47], v0\n" \
    "ds_read_b128 v[28:31], v3 offset:19456\ns_waitcnt lgkmcnt(6)\nv_smfmac_f32_16x16x64_bf16 v[20:23], v[52:55], v[56:63], v0\nds_read_b128 v[48:51], v1 offset:34816\n" \
    "v_smfmac_f32_16x16x64_bf16 v[16:19], v[52:55], a[48:55], v0\nds_read_b128 v[32:35], v3 offset:20480\ns_waitcnt lgkmcnt(5)\n" \
    "v_smfmac_f32_16x16x64_bf16 v[20:23], v[40:43], v[64:71], v0\nds_read_b128 v[36:39], v3 offset:21504\nv_smfmac_f32_16x16x64_bf16 v[16:19], v[40:43], a[64:71], v0\n" \
    "ds_read_b128 v[40:43], v1 offset:36864\ns_waitcnt lgkmcnt(4)\nv_smfmac_f32_16x16x64_bf16 v[20:23], v[44:47], v[24:31], v0\nds_read_b128 v[24:27], v1 offset:37888\n" \
    "v_smfmac_f32_16x16x64_bf16 v[16:19], v[44:47], a[72:79], v0\nds_read_b128 v[28:31], v1 offset:38912\ns_waitcnt lgkmcnt(3)\n" \
    "v_smfmac_f32_16x16x64_bf16 v[20:23], v[48:51], v[32:39], v0\ns_waitcnt lgkmcnt(2)\nv_smfmac_f32_16x16x64_bf16 v[20:23], v[40:43], a[104:111], v0\ns_waitcnt lgkmcnt(1)\n" \
    "v_smfmac_f32_16x16x64_bf16 v[20:23], v[24:27], a[120:127], v0\ns_waitcnt lgkmcnt(0)\nv_smfmac_f32_16x16x64_bf16 v[20:23], v[28:31], a[136:143], v0\ns_nop 2\n" \
    "v_smfmac_f32_16x16x64_bf16 v[16:19], v[48:51], a[80:87], v0\ns_nop 2\nv_smfmac_f32_16x16x64_bf16 v[16:19], v[40:43], a[96:103], v0\nv_permlane32_swap_b32 v20, v22\n" \
    "v_smfmac_f32_16x16x64_bf16 v[16:19], v[24:27], a[112:119], v0\nv_permlane32_swap_b32 v21, v23\nv_smfmac_f32_16x16x64_bf16 v[16:19], v[28:31], a[128:135], v0\n" \
    "v_add_f32 v20, v20, v22\nv_add_f32 v21, v21, v23\nv_cvt_pk_bf16_f32 v20, v20, v21\nds_write_b16 v4, v20 offset:8\nds_write_b16_d16_hi v4, v20 offset:24\n" \
    "v_mov_b32 v15, v15\nv_mov_b32 v15, v15\nv_mov_b32 v15, v15\nv_permlane32_swap_b32 v16, v18\nv_permlane32_swap_b32 v17, v19\nv_pk_add_f32 v[16:17], v[16:17], v[18:19]\n" \
    "v_cvt_pk_bf16_f32 v16, v16, v17\nds_write_b16 v4, v16 offset:0\nds_write_b16_d16_hi v4, v16 offset:16\nv_mov_b32 v15, v15\nv_mov_b32 v15, v15\n.Lv7_%=:\n.Lv79_%=:\n" \
    "v_mov_b64 v[54:55], 0\nv_mov_b64 v[52:53], 0\nv_mov_b64 v[50:51], 0\nv_mov_b64 v[48:49], 0\nv_mov_b64 v[70:71], 0\nv_mov_b64 v[68:69], 0\nv_mov_b64 v[66:67], 0\n" \
    "v_mov_b64 v[64:65], 0\nds_read_b128 v[32:35], v5 offset:0\nds_read_b128 v[36:39], v5 offset:512\nds_read_b128 v[40:43], v5 offset:1024\n" \
    "ds_read_b128 v[44:47], v5 offset:1536\nv_mov_b64 v[30:31], 0\nv_mov_b64 v[28:29], 0\nv_mov_b64 v[26:27], 0\nv_mov_b64 v[24:25], 0\nv_mov_b64 v[22:23], 0\n" \
    "v_mov_b64 v[20:21], 0\nv_mov_b64 v[18:19], 0\nv_mov_b64 v[16:17], 0\ns_waitcnt lgkmcnt(3)\ns_nop 0\nv_smfmac_f32_16x16x64_bf16 v[16:19], v[32:35], a[160:167], v0\n" \
    "v_smfmac_f32_16x16x64_bf16 v[20:23], v[32:35], a[176:183], v0\ns_waitcnt lgkmcnt(2)\nv_smfmac_f32_16x16x64_bf16 v[16:19], v[36:39], a[168:175], v0\n" \
    "v_smfmac_f32_16x16x64_bf16 v[20:23], v[36:39], a[184:191], v0\ns_waitcnt lgkmcnt(1)\nv_smfmac_f32_16x16x64_bf16 v[16:19], v[40:43], a[192:199], v0\n" \
    "v_smfmac_f32_16x16x64_bf16 v[20:23], v[40:43], a[208:215], v0\ns_waitcnt lgkmcnt(0)\nv_smfmac_f32_16x16x64_bf16 v[16:19], v[44:47], a[200:207], v0\n" \
    "v_smfmac_f32_16x16x64_bf16 v[20:23], v[44:47], a[216:223], v0\ns_nop 2\nv_smfmac_f32_16x16x64_bf16 v[24:27], v[32:35], a[224:231], v0\ns_nop 2\n" \
    "v_cmp_u_f32 s[4:5], v16, v17\nv_smfmac_f32_16x16x64_bf16 v[28:31], v[32:35], a[240:247], v0\nv_cmp_u_f32 vcc, v18, v19\n" \
    "v_smfmac_f32_16x16x64_bf16 v[24:27], v[36:39], a[232:239], v0\nv_cvt_pk_bf16_f32 v72, v16, v17\nv_smfmac_f32_16x16x64_bf16 v[28:31], v[36:39], a[248:255], v0\n" \
    "v_cvt_pk_bf16_f32 v74, v18, v19\nv_smfmac_f32_16x16x64_bf16 v[24:27], v[40:43], v[92:99], v0\ns_or_b64 vcc, vcc, s[4:5]\ns_cbranch_scc1 .Lv80_%=\n.Lv81_%=:\n" \
    "v_cvt_pk_bf16_f32 v73, v20, v21\nv_smfmac_f32_16x16x64_bf16 v[28:31], v[40:43], v[108:115], v0\nv_cvt_pk_bf16_f32 v75, v22, v23\n" \
    "v_smfmac_f32_16x16x64_bf16 v[24:27], v[44:47], v[100:107], v0\nv_permlane16_swap_b32 v72, v73\nv_smfmac_f32_16x16x64_bf16 v[28:31], v[44:47], v[116:123], v0\n" \
    "v_permlane16_swap_b32 v74, v75\nv_smfmac_f32_16x16x64_bf16 v[64:67], v[32:35], v[124:131], v0\nbuffer_store_dwordx4 v[72:75], v7, s[64:67], s0 offen offset:0 sc1\ns_nop 1\n" \
    "v_smfmac_f32_16x16x64_bf16 v[68:71], v[32:35], v[140:147], v0\nv_cvt_pk_bf16_f32 v76, v24, v25\nv_smfmac_f32_16x16x64_bf16 v[64:67], v[36:39], v[132:139], v0\n" \
    "v_cvt_pk_bf16_f32 v78, v26, v27\nv_smfmac_f32_16x16x64_bf16 v[68:71], v[36:39], v[148:155], v0\nv_cvt_pk_bf16_f32 v77, v28, v29\n" \
    "v_smfmac_f32_16x16x64_bf16 v[64:67], v[40:43], v[156:163], v0\nv_cvt_pk_bf16_f32 v79, v30, v31\nv_smfmac_f32_16x16x64_bf16 v[68:71], v[40:43], v[172:179], v0\n" \
    "v_permlane16_swap_b32 v76, v77\nv_smfmac_f32_16x16x64_bf16 v[64:67], v[44:47], v[164:171], v0\nv_permlane16_swap_b32 v78, v79\n" \
    "v_smfmac_f32_16x16x64_bf16 v[68:71], v[44:47], v[180:187], v0\nbuffer_store_dwordx4 v[76:79], v7, s[64:67], s1 offen offset:0 sc1\ns_nop 1\n" \
    "v_smfmac_f32_16x16x64_bf16 v[48:51], v[32:35], v[188:195], v0\ns_nop 1\nv_cvt_pk_bf16_f32 v72, v64, v65\nv_smfmac_f32_16x16x64_bf16 v[52:55], v[32:35], v[204:211], v0\n" \
    "v_cvt_pk_bf16_f32 v74, v66, v67\nv_smfmac_f32_16x16x64_bf16 v[48:51], v[36:39], v[196:203], v0\nv_cvt_pk_bf16_f32 v73, v68, v69\n" \
    "v_smfmac_f32_16x16x64_bf16 v[52:55], v[36:39], v[212:219], v0\nv_cvt_pk_bf16_f32 v75, v70, v71\nv_smfmac_f32_16x16x64_bf16 v[48:51], v[40:43], v[220:227], v0\n" \
    "v_permlane16_swap_b32 v72, v73\nv_smfmac_f32_16x16x64_bf16 v[52:55], v[40:43], v[236:243], v0\nv_permlane16_swap_b32 v74, v75\n" \
    "v_smfmac_f32_16x16x64_bf16 v[48:51], v[44:47], v[228:235], v0\nbuffer_store_dwordx4 v[72:75], v7, s[64:67], s2 offen offset:0 sc1\n" \
    "v_smfmac_f32_16x16x64_bf16 v[52:55], v[44:47], v[244:251], v0\ns_nop 5\nv_cvt_pk_bf16_f32 v76, v48, v49\nv_cvt_pk_bf16_f32 v78, v50, v51\nv_cvt_pk_bf16_f32 v77, v52, v53\n" \
    "v_cvt_pk_bf16_f32 v79, v54, v55\ns_nop 0\nv_permlane16_swap_b32 v76, v77\nv_permlane16_swap_b32 v78, v79\n" \
    "buffer_store_dwordx4 v[76:79], v7, s[64:67], s3 offen offset:0 sc1\ns_barrier\ns_memrealtime s[72:73]\ns_waitcnt lgkmcnt(0)\ns_add_u32 s35, s35, 2\ns_cmp_lt_u32 s35, 62\n" \
    "s_cbranch_scc1 .Lv5_%=\ns_cmp_lt_u32 s35, 64\ns_cbranch_scc0 .Lv6_%=\nv_mov_b64 v[252:253], -1\ns_and_b32 s69, s35, 15\ns_lshl_b32 s69, s69, 19\ns_add_u32 s0, s38, s69\n" \
    "s_lshr_b32 s69, s0, 8\ns_and_b32 s69, s69, 0x2000\ns_xor_b32 s0, s0, s69\ns_add_u32 s1, s0, 0x10000\ns_add_u32 s2, s0, 0x20000\ns_add_u32 s3, s0, 0x30000\n" \
    "buffer_load_dword v13, off, s[64:67], s42 sc1\ns_and_b32 s69, s35, 15\ns_lshl_b32 s69, s69, 17\ns_add_u32 s81, s37, s69\ns_lshr_b32 s69, s81, 8\n" \
    "s_and_b32 s69, s69, 0x2000\ns_xor_b32 s81, s81, s69\ns_mov_b32 s68, 0\ns_waitcnt vmcnt(0)\nv_readfirstlane_b32 s74, v13\ns_nop 0\ns_and_b32 s75, s74, 127\n" \
    "s_lshr_b32 s69, s74, 8\ns_sub_u32 s69, s69, s80\ns_bfe_i32 s69, s69, 0x180000\ns_cmp_eq_u32 s75, 0\ns_cselect_b32 s69, 0, s69\ns_add_u32 s80, s80, s69\n" \
    "s_lshl_b32 s74, s74, 5\ns_and_b32 s74, s74, 0x1000\ns_andn2_b32 s81, s81, 0x1000\ns_or_b32 s81, s81, s74\nv_lshrrev_b32 v15, 1, v2\nv_add_u32 v15, s60, v15\n" \
    "ds_write_b64 v15, v[252:253]\ns_branch .Lv4_%=\n.Lv6_%=:\ns_endpgm\n.Lv12_%=:\ns_nop 7\nv_cvt_pk_bf16_f32 v36, v20, v21\nv_cvt_pk_bf16_f32 v37, v22, v23\n" \
    "v_cvt_pk_bf16_f32 v38, v28, v29\nv_cvt_pk_bf16_f32 v39, v30, v31\nds_write_b128 v14, v[36:39] offset:4096\ns_waitcnt vmcnt(0) lgkmcnt(0)\ns_barrier\ns_branch .Lv5_%=\n" \
    ".Lv14_%=:\ns_sleep 6\nbuffer_load_dword v13, off, s[64:67], s42 sc1\ns_waitcnt vmcnt(0)\nv_readfirstlane_b32 s74, v13\ns_nop 0\ns_and_b32 s75, s74, 127\n" \
    "s_lshr_b32 s69, s74, 8\ns_sub_u32 s69, s69, s80\ns_bfe_i32 s69, s69, 0x180000\ns_cmp_eq_u32 s75, 0\ns_cselect_b32 s69, 0, s69\ns_add_u32 s80, s80, s69\n" \
    "s_lshl_b32 s74, s74, 5\ns_and_b32 s74, s74, 0x1000\ns_andn2_b32 s81, s81, 0x1000\ns_or_b32 s81, s81, s74\ns_add_u32 s68, s68, 1\ns_and_b32 s69, s68, 63\n" \
    "s_cbranch_scc1 .Lv13_%=\ns_cmp_gt_u32 s68, 0x400000\ns_cbranch_scc1 .Lv1_%=\nbuffer_load_dword v253, off, s[64:67], 0 sc1\ns_waitcnt vmcnt(0)\n" \
    "v_readfirstlane_b32 s69, v253\ns_nop 0\ns_cmp_eq_u32 s69, 1\ns_cbranch_scc0 .Lv13_%=\ns_endpgm\n.Lv18_%=:\ns_memrealtime s[72:73]\ns_waitcnt lgkmcnt(0)\n" \
    "s_sub_u32 s72, s72, s76\ns_cmp_lt_i32 s72, 0\ns_cbranch_scc1 .Lv18_%=\nbuffer_load_dword v13, off, s[64:67], s42 sc1\ns_waitcnt vmcnt(0)\nv_readfirstlane_b32 s74, v13\n" \
    "s_nop 0\ns_and_b32 s75, s74, 127\ns_lshr_b32 s69, s74, 8\ns_sub_u32 s69, s69, s80\ns_bfe_i32 s69, s69, 0x180000\ns_cmp_eq_u32 s75, 0\ns_cselect_b32 s69, 0, s69\n" \
    "s_add_u32 s80, s80, s69\ns_lshl_b32 s74, s74, 5\ns_and_b32 s74, s74, 0x1000\ns_andn2_b32 s81, s81, 0x1000\ns_or_b32 s81, s81, s74\ns_branch .Lv15_%=\n.Lv26_%=:\n" \
    "v_mov_b32 v252, 0\nv_max3_u32 v252, v252, v72, v73\nv_max3_u32 v252, v252, v74, v75\nv_max3_u32 v252, v252, v76, v77\nv_max3_u32 v252, v252, v78, v79\n" \
    "v_cmp_eq_u32 vcc, -1, v252\ns_nop 0\ns_cbranch_vccz .Lv22_%=\ns_add_u32 s80, s80, s43\ns_xor_b32 s81, s81, 0x1000\ns_xor_b32 s90, s90, 0x1000\ns_add_u32 s68, s68, 1\n" \
    "s_and_b32 s69, s68, 63\ns_cbranch_scc1 .Lv16_%=\ns_cmp_gt_u32 s68, 0x400000\ns_cbranch_scc1 .Lv1_%=\nbuffer_load_dword v253, off, s[64:67], 0 sc1\ns_waitcnt vmcnt(0)\n" \
    "v_readfirstlane_b32 s69, v253\ns_nop 0\ns_cmp_eq_u32 s69, 1\ns_cbranch_scc0 .Lv16_%=\ns_endpgm\n.Lv16_%=:\ns_memrealtime s[72:73]\ns_waitcnt lgkmcnt(0)\n" \
    "s_sub_u32 s72, s72, s80\ns_cmp_lt_i32 s72, 0\ns_cbranch_scc1 .Lv16_%=\nbuffer_load_dwordx4 v[72:75], v2, s[64:67], s81 offen offset:0 sc1\ns_nop 7\n" \
    "buffer_load_dwordx4 v[76:79], v2, s[64:67], s81 offen offset:1024 sc1\ns_nop 7\nbuffer_load_dwordx4 v[80:83], v2, s[64:67], s90 offen offset:0 sc1\ns_nop 7\n" \
    "buffer_load_dwordx4 v[84:87], v2, s[64:67], s90 offen offset:1024 sc1\ns_nop 7\ns_branch .Lv19_%=\n.Lv27_%=:\nv_mov_b32 v252, 0\nv_max3_u32 v252, v252, v80, v81\n" \
    "v_max3_u32 v252, v252, v82, v83\nv_cmp_eq_u32 vcc, -1, v252\ns_nop 0\ns_cbranch_vccz .Lv23_%=\ns_add_u32 s80, s80, s43\ns_xor_b32 s81, s81, 0x1000\n" \
    "s_xor_b32 s90, s90, 0x1000\n.Lv29_%=:\ns_memrealtime s[72:73]\ns_waitcnt vmcnt(0) lgkmcnt(0)\ns_sub_u32 s72, s72, s80\ns_cmp_lt_i32 s72, 0\ns_cbranch_scc1 .Lv29_%=\n" \
    "buffer_load_dwordx4 v[80:83], v2, s[64:67], s90 offen offset:0 sc1\nbuffer_load_dwordx4 v[84:87], v2, s[64:67], s90 offen offset:1024 sc1\ns_waitcnt vmcnt(0)\n" \
    "s_add_u32 s68, s68, 1\ns_and_b32 s69, s68, 63\ns_cbranch_scc1 .Lv27_%=\ns_cmp_gt_u32 s68, 0x400000\ns_cbranch_scc1 .Lv1_%=\nbuffer_load_dword v253, off, s[64:67], 0 sc1\n" \
    "s_waitcnt vmcnt(0)\nv_readfirstlane_b32 s69, v253\ns_nop 0\ns_cmp_eq_u32 s69, 1\ns_cbranch_scc0 .Lv27_%=\ns_endpgm\n.Lv28_%=:\nv_mov_b32 v252, 0\n" \
    "v_max3_u32 v252, v252, v84, v85\nv_max3_u32 v252, v252, v86, v87\nv_cmp_eq_u32 vcc, -1, v252\ns_nop 0\ns_cbranch_vccz .Lv24_%=\ns_add_u32 s80, s80, s43\n" \
    "s_xor_b32 s81, s81, 0x1000\ns_xor_b32 s90, s90, 0x1000\n.Lv30_%=:\ns_memrealtime s[72:73]\ns_waitcnt vmcnt(0) lgkmcnt(0)\ns_sub_u32 s72, s72, s80\ns_cmp_lt_i32 s72, 0\n" \
    "s_cbranch_scc1 .Lv30_%=\nbuffer_load_dwordx4 v[84:87], v2, s[64:67], s90 offen offset:1024 sc1\ns_waitcnt vmcnt(0)\ns_add_u32 s68, s68, 1\ns_and_b32 s69, s68, 63\n" \
    "s_cbranch_scc1 .Lv28_%=\ns_cmp_gt_u32 s68, 0x400000\ns_cbranch_scc1 .Lv1_%=\nbuffer_load_dword v253, off, s[64:67], 0 sc1\ns_waitcnt vmcnt(0)\n" \
    "v_readfirstlane_b32 s69, v253\ns_nop 0\ns_cmp_eq_u32 s69, 1\ns_cbranch_scc0 .Lv28_%=\ns_endpgm\n.Lv34_%=:\ns_memrealtime s[72:73]\ns_waitcnt lgkmcnt(0)\n" \
    "s_sub_u32 s72, s72, s76\ns_cmp_lt_i32 s72, 0\ns_cbranch_scc1 .Lv34_%=\nbuffer_load_dword v13, off, s[64:67], s42 sc1\ns_waitcnt vmcnt(0)\nv_readfirstlane_b32 s74, v13\n" \
    "s_nop 0\ns_and_b32 s75, s74, 127\ns_lshr_b32 s69, s74, 8\ns_sub_u32 s69, s69, s80\ns_bfe_i32 s69, s69, 0x180000\ns_cmp_eq_u32 s75, 0\ns_cselect_b32 s69, 0, s69\n" \
    "s_add_u32 s80, s80, s69\ns_lshl_b32 s74, s74, 5\ns_and_b32 s74, s74, 0x1000\ns_andn2_b32 s81, s81, 0x1000\ns_or_b32 s81, s81, s74\ns_branch .Lv31_%=\n.Lv42_%=:\n" \
    "v_mov_b32 v252, 0\nv_max3_u32 v252, v252, v72, v73\nv_max3_u32 v252, v252, v74, v75\nv_max3_u32 v252, v252, v76, v77\nv_max3_u32 v252, v252, v78, v79\n" \
    "v_cmp_eq_u32 vcc, -1, v252\ns_nop 0\ns_cbranch_vccz .Lv38_%=\ns_add_u32 s80, s80, s43\ns_xor_b32 s81, s81, 0x1000\ns_xor_b32 s90, s90, 0x1000\ns_add_u32 s68, s68, 1\n" \
    "s_and_b32 s69, s68, 63\ns_cbranch_scc1 .Lv32_%=\ns_cmp_gt_u32 s68, 0x400000\ns_cbranch_scc1 .Lv1_%=\nbuffer_load_dword v253, off, s[64:67], 0 sc1\ns_waitcnt vmcnt(0)\n" \
    "v_readfirstlane_b32 s69, v253\ns_nop 0\ns_cmp_eq_u32 s69, 1\ns_cbranch_scc0 .Lv32_%=\ns_endpgm\n.Lv32_%=:\ns_memrealtime s[72:73]\ns_waitcnt lgkmcnt(0)\n" \
    "s_sub_u32 s72, s72, s80\ns_cmp_lt_i32 s72, 0\ns_cbranch_scc1 .Lv32_%=\nbuffer_load_dwordx4 v[72:75], v2, s[64:67], s81 offen offset:0 sc1\ns_nop 7\n" \
    "buffer_load_dwordx4 v[76:79], v2, s[64:67], s81 offen offset:1024 sc1\ns_nop 7\nbuffer_load_dwordx4 v[80:83], v2, s[64:67], s90 offen offset:0 sc1\ns_nop 7\n" \
    "buffer_load_dwordx4 v[84:87], v2, s[64:67], s90 offen offset:1024 sc1\ns_nop 7\ns_branch .Lv35_%=\n.Lv43_%=:\nv_mov_b32 v252, 0\nv_max3_u32 v252, v252, v80, v81\n" \
    "v_max3_u32 v252, v252, v82, v83\nv_cmp_eq_u32 vcc, -1, v252\ns_nop 0\ns_cbranch_vccz .Lv39_%=\ns_add_u32 s80, s80, s43\ns_xor_b32 s81, s81, 0x1000\n" \
    "s_xor_b32 s90, s90, 0x1000\n.Lv45_%=:\ns_memrealtime s[72:73]\ns_waitcnt vmcnt(0) lgkmcnt(0)\ns_sub_u32 s72, s72, s80\ns_cmp_lt_i32 s72, 0\ns_cbranch_scc1 .Lv45_%=\n" \
    "buffer_load_dwordx4 v[80:83], v2, s[64:67], s90 offen offset:0 sc1\nbuffer_load_dwordx4 v[84:87], v2, s[64:67], s90 offen offset:1024 sc1\ns_waitcnt vmcnt(0)\n" \
    "s_add_u32 s68, s68, 1\ns_and_b32 s69, s68, 63\ns_cbranch_scc1 .Lv43_%=\ns_cmp_gt_u32 s68, 0x400000\ns_cbranch_scc1 .Lv1_%=\nbuffer_load_dword v253, off, s[64:67], 0 sc1\n" \
    "s_waitcnt vmcnt(0)\nv_readfirstlane_b32 s69, v253\ns_nop 0\ns_cmp_eq_u32 s69, 1\ns_cbranch_scc0 .Lv43_%=\ns_endpgm\n.Lv44_%=:\nv_mov_b32 v252, 0\n" \
    "v_max3_u32 v252, v252, v84, v85\nv_max3_u32 v252, v252, v86, v87\nv_cmp_eq_u32 vcc, -1, v252\ns_nop 0\ns_cbranch_vccz .Lv40_%=\ns_add_u32 s80, s80, s43\n" \
    "s_xor_b32 s81, s81, 0x1000\ns_xor_b32 s90, s90, 0x1000\n.Lv46_%=:\ns_memrealtime s[72:73]\ns_waitcnt vmcnt(0) lgkmcnt(0)\ns_sub_u32 s72, s72, s80\ns_cmp_lt_i32 s72, 0\n" \
    "s_cbranch_scc1 .Lv46_%=\nbuffer_load_dwordx4 v[84:87], v2, s[64:67], s90 offen offset:1024 sc1\ns_waitcnt vmcnt(0)\ns_add_u32 s68, s68, 1\ns_and_b32 s69, s68, 63\n" \
    "s_cbranch_scc1 .Lv44_%=\ns_cmp_gt_u32 s68, 0x400000\ns_cbranch_scc1 .Lv1_%=\nbuffer_load_dword v253, off, s[64:67], 0 sc1\ns_waitcnt vmcnt(0)\n" \
    "v_readfirstlane_b32 s69, v253\ns_nop 0\ns_cmp_eq_u32 s69, 1\ns_cbranch_scc0 .Lv44_%=\ns_endpgm\n.Lv50_%=:\ns_memrealtime s[72:73]\ns_waitcnt lgkmcnt(0)\n" \
    "s_sub_u32 s72, s72, s76\ns_cmp_lt_i32 s72, 0\ns_cbranch_scc1 .Lv50_%=\nbuffer_load_dword v13, off, s[64:67], s42 sc1\ns_waitcnt vmcnt(0)\nv_readfirstlane_b32 s74, v13\n" \
    "s_nop 0\ns_and_b32 s75, s74, 127\ns_lshr_b32 s69, s74, 8\ns_sub_u32 s69, s69, s80\ns_bfe_i32 s69, s69, 0x180000\ns_cmp_eq_u32 s75, 0\ns_cselect_b32 s69, 0, s69\n" \
    "s_add_u32 s80, s80, s69\ns_lshl_b32 s74, s74, 5\ns_and_b32 s74, s74, 0x1000\ns_andn2_b32 s81, s81, 0x1000\ns_or_b32 s81, s81, s74\ns_branch .Lv47_%=\n.Lv58_%=:\n" \
    "v_mov_b32 v252, 0\nv_max3_u32 v252, v252, v72, v73\nv_max3_u32 v252, v252, v74, v75\nv_max3_u32 v252, v252, v76, v77\nv_max3_u32 v252, v252, v78, v79\n" \
    "v_cmp_eq_u32 vcc, -1, v252\ns_nop 0\ns_cbranch_vccz .Lv54_%=\ns_add_u32 s80, s80, s43\ns_xor_b32 s81, s81, 0x1000\ns_xor_b32 s90, s90, 0x1000\ns_add_u32 s68, s68, 1\n" \
    "s_and_b32 s69, s68, 63\ns_cbranch_scc1 .Lv48_%=\ns_cmp_gt_u32 s68, 0x400000\ns_cbranch_scc1 .Lv1_%=\nbuffer_load_dword v253, off, s[64:67], 0 sc1\ns_waitcnt vmcnt(0)\n" \
    "v_readfirstlane_b32 s69, v253\ns_nop 0\ns_cmp_eq_u32 s69, 1\ns_cbranch_scc0 .Lv48_%=\ns_endpgm\n.Lv48_%=:\ns_memrealtime s[72:73]\ns_waitcnt lgkmcnt(0)\n" \
    "s_sub_u32 s72, s72, s80\ns_cmp_lt_i32 s72, 0\ns_cbranch_scc1 .Lv48_%=\nbuffer_load_dwordx4 v[72:75], v2, s[64:67], s81 offen offset:0 sc1\ns_nop 7\n" \
    "buffer_load_dwordx4 v[76:79], v2, s[64:67], s81 offen offset:1024 sc1\ns_nop 7\nbuffer_load_dwordx4 v[80:83], v2, s[64:67], s90 offen offset:0 sc1\ns_nop 7\n" \
    "buffer_load_dwordx4 v[84:87], v2, s[64:67], s90 offen offset:1024 sc1\ns_nop 7\ns_branch .Lv51_%=\n.Lv59_%=:\nv_mov_b32 v252, 0\nv_max3_u32 v252, v252, v80, v81\n" \
    "v_max3_u32 v252, v252, v82, v83\nv_cmp_eq_u32 vcc, -1, v252\ns_nop 0\ns_cbranch_vccz .Lv55_%=\ns_add_u32 s80, s80, s43\ns_xor_b32 s81, s81, 0x1000\n" \
    "s_xor_b32 s90, s90, 0x1000\n.Lv61_%=:\ns_memrealtime s[72:73]\ns_waitcnt vmcnt(0) lgkmcnt(0)\ns_sub_u32 s72, s72, s80\ns_cmp_lt_i32 s72, 0\ns_cbranch_scc1 .Lv61_%=\n" \
    "buffer_load_dwordx4 v[80:83], v2, s[64:67], s90 offen offset:0 sc1\nbuffer_load_dwordx4 v[84:87], v2, s[64:67], s90 offen offset:1024 sc1\ns_waitcnt vmcnt(0)\n" \
    "s_add_u32 s68, s68, 1\ns_and_b32 s69, s68, 63\ns_cbranch_scc1 .Lv59_%=\ns_cmp_gt_u32 s68, 0x400000\ns_cbranch_scc1 .Lv1_%=\nbuffer_load_dword v253, off, s[64:67], 0 sc1\n" \
    "s_waitcnt vmcnt(0)\nv_readfirstlane_b32 s69, v253\ns_nop 0\ns_cmp_eq_u32 s69, 1\ns_cbranch_scc0 .Lv59_%=\ns_endpgm\n.Lv60_%=:\nv_mov_b32 v252, 0\n" \
    "v_max3_u32 v252, v252, v84, v85\nv_max3_u32 v252, v252, v86, v87\nv_cmp_eq_u32 vcc, -1, v252\ns_nop 0\ns_cbranch_vccz .Lv56_%=\ns_add_u32 s80, s80, s43\n" \
    "s_xor_b32 s81, s81, 0x1000\ns_xor_b32 s90, s90, 0x1000\n.Lv62_%=:\ns_memrealtime s[72:73]\ns_waitcnt vmcnt(0) lgkmcnt(0)\ns_sub_u32 s72, s72, s80\ns_cmp_lt_i32 s72, 0\n" \
    "s_cbranch_scc1 .Lv62_%=\nbuffer_load_dwordx4 v[84:87], v2, s[64:67], s90 offen offset:1024 sc1\ns_waitcnt vmcnt(0)\ns_add_u32 s68, s68, 1\ns_and_b32 s69, s68, 63\n" \
    "s_cbranch_scc1 .Lv60_%=\ns_cmp_gt_u32 s68, 0x400000\ns_cbranch_scc1 .Lv1_%=\nbuffer_load_dword v253, off, s[64:67], 0 sc1\ns_waitcnt vmcnt(0)\n" \
    "v_readfirstlane_b32 s69, v253\ns_nop 0\ns_cmp_eq_u32 s69, 1\ns_cbranch_scc0 .Lv60_%=\ns_endpgm\n.Lv66_%=:\ns_memrealtime s[72:73]\ns_waitcnt lgkmcnt(0)\n" \
    "s_sub_u32 s72, s72, s76\ns_cmp_lt_i32 s72, 0\ns_cbranch_scc1 .Lv66_%=\nbuffer_load_dword v13, off, s[64:67], s42 sc1\ns_waitcnt vmcnt(0)\nv_readfirstlane_b32 s74, v13\n" \
    "s_nop 0\ns_and_b32 s75, s74, 127\ns_lshr_b32 s69, s74, 8\ns_sub_u32 s69, s69, s80\ns_bfe_i32 s69, s69, 0x180000\ns_cmp_eq_u32 s75, 0\ns_cselect_b32 s69, 0, s69\n" \
    "s_add_u32 s80, s80, s69\ns_lshl_b32 s74, s74, 5\ns_and_b32 s74, s74, 0x1000\ns_andn2_b32 s81, s81, 0x1000\ns_or_b32 s81, s81, s74\ns_branch .Lv63_%=\n.Lv74_%=:\n" \
    "v_mov_b32 v252, 0\nv_max3_u32 v252, v252, v72, v73\nv_max3_u32 v252, v252, v74, v75\nv_max3_u32 v252, v252, v76, v77\nv_max3_u32 v252, v252, v78, v79\n" \
    "v_cmp_eq_u32 vcc, -1, v252\ns_nop 0\ns_cbranch_vccz .Lv70_%=\ns_add_u32 s80, s80, s43\ns_xor_b32 s81, s81, 0x1000\ns_xor_b32 s90, s90, 0x1000\ns_add_u32 s68, s68, 1\n" \
    "s_and_b32 s69, s68, 63\ns_cbranch_scc1 .Lv64_%=\ns_cmp_gt_u32 s68, 0x400000\ns_cbranch_scc1 .Lv1_%=\nbuffer_load_dword v253, off, s[64:67], 0 sc1\ns_waitcnt vmcnt(0)\n" \
    "v_readfirstlane_b32 s69, v253\ns_nop 0\ns_cmp_eq_u32 s69, 1\ns_cbranch_scc0 .Lv64_%=\ns_endpgm\n.Lv64_%=:\ns_memrealtime s[72:73]\ns_waitcnt lgkmcnt(0)\n" \
    "s_sub_u32 s72, s72, s80\ns_cmp_lt_i32 s72, 0\ns_cbranch_scc1 .Lv64_%=\nbuffer_load_dwordx4 v[72:75], v2, s[64:67], s81 offen offset:0 sc1\ns_nop 7\n" \
    "buffer_load_dwordx4 v[76:79], v2, s[64:67], s81 offen offset:1024 sc1\ns_nop 7\nbuffer_load_dwordx4 v[80:83], v2, s[64:67], s90 offen offset:0 sc1\ns_nop 7\n" \
    "buffer_load_dwordx4 v[84:87], v2, s[64:67], s90 offen offset:1024 sc1\ns_nop 7\ns_branch .Lv67_%=\n.Lv75_%=:\nv_mov_b32 v252, 0\nv_max3_u32 v252, v252, v80, v81\n" \
    "v_max3_u32 v252, v252, v82, v83\nv_cmp_eq_u32 vcc, -1, v252\ns_nop 0\ns_cbranch_vccz .Lv71_%=\ns_add_u32 s80, s80, s43\ns_xor_b32 s81, s81, 0x1000\n" \
    "s_xor_b32 s90, s90, 0x1000\n.Lv77_%=:\ns_memrealtime s[72:73]\ns_waitcnt vmcnt(0) lgkmcnt(0)\ns_sub_u32 s72, s72, s80\ns_cmp_lt_i32 s72, 0\ns_cbranch_scc1 .Lv77_%=\n" \
    "buffer_load_dwordx4 v[80:83], v2, s[64:67], s90 offen offset:0 sc1\nbuffer_load_dwordx4 v[84:87], v2, s[64:67], s90 offen offset:1024 sc1\ns_waitcnt vmcnt(0)\n" \
    "s_add_u32 s68, s68, 1\ns_and_b32 s69, s68, 63\ns_cbranch_scc1 .Lv75_%=\ns_cmp_gt_u32 s68, 0x400000\ns_cbranch_scc1 .Lv1_%=\nbuffer_load_dword v253, off, s[64:67], 0 sc1\n" \
    "s_waitcnt vmcnt(0)\nv_readfirstlane_b32 s69, v253\ns_nop 0\ns_cmp_eq_u32 s69, 1\ns_cbranch_scc0 .Lv75_%=\ns_endpgm\n.Lv76_%=:\nv_mov_b32 v252, 0\n" \
    "v_max3_u32 v252, v252, v84, v85\nv_max3_u32 v252, v252, v86, v87\nv_cmp_eq_u32 vcc, -1, v252\ns_nop 0\ns_cbranch_vccz .Lv72_%=\ns_add_u32 s80, s80, s43\n" \
    "s_xor_b32 s81, s81, 0x1000\ns_xor_b32 s90, s90, 0x1000\n.Lv78_%=:\ns_memrealtime s[72:73]\ns_waitcnt vmcnt(0) lgkmcnt(0)\ns_sub_u32 s72, s72, s80\ns_cmp_lt_i32 s72, 0\n" \
    "s_cbranch_scc1 .Lv78_%=\nbuffer_load_dwordx4 v[84:87], v2, s[64:67], s90 offen offset:1024 sc1\ns_waitcnt vmcnt(0)\ns_add_u32 s68, s68, 1\ns_and_b32 s69, s68, 63\n" \
    "s_cbranch_scc1 .Lv76_%=\ns_cmp_gt_u32 s68, 0x400000\ns_cbranch_scc1 .Lv1_%=\nbuffer_load_dword v253, off, s[64:67], 0 sc1\ns_waitcnt vmcnt(0)\n" \
    "v_readfirstlane_b32 s69, v253\ns_nop 0\ns_cmp_eq_u32 s69, 1\ns_cbranch_scc0 .Lv76_%=\ns_endpgm\n.Lv80_%=:\nv_mov_b32 v252, 0\nv_pk_max_u16 v252, v252, v32\n" \
    "v_pk_max_u16 v252, v252, v33\nv_pk_max_u16 v252, v252, v34\nv_pk_max_u16 v252, v252, v35\nv_pk_max_u16 v252, v252, v36\nv_pk_max_u16 v252, v252, v37\n" \
    "v_pk_max_u16 v252, v252, v38\nv_pk_max_u16 v252, v252, v39\nv_pk_max_u16 v252, v252, v40\nv_pk_max_u16 v252, v252, v41\nv_pk_max_u16 v252, v252, v42\n" \
    "v_pk_max_u16 v252, v252, v43\nv_pk_max_u16 v252, v252, v44\nv_pk_max_u16 v252, v252, v45\nv_pk_max_u16 v252, v252, v46\nv_pk_max_u16 v252, v252, v47\n" \
    "v_pk_max_u16 v252, v252, v252 op_sel:[0,1] op_sel_hi:[1,0]\nv_cmp_eq_u32 vcc, -1, v252\ns_nop 0\ns_cbranch_vccz .Lv81_%=\ns_add_u32 s68, s68, 1\ns_and_b32 s69, s68, 63\n" \
    "s_cbranch_scc1 .Lv79_%=\ns_cmp_gt_u32 s68, 0x400000\ns_cbranch_scc1 .Lv1_%=\nbuffer_load_dword v253, off, s[64:67], 0 sc1\ns_waitcnt vmcnt(0)\n" \
    "v_readfirstlane_b32 s69, v253\ns_nop 0\ns_cmp_eq_u32 s69, 1\ns_cbranch_scc0 .Lv79_%=\ns_endpgm\n.Lv1_%=:\nv_mov_b32 v253, 1\nbuffer_store_dword v253, off, s[64:67], 0 sc1\n" \
    "s_endpgm\n"
#define ASM_R \
    "s_mov_b32 s68, 0\ns_cmp_lg_u32 s56, 0\ns_cbranch_scc1 .Lr21_%=\n.Lr2_%=:\nglobal_load_dwordx4 v[224:227], v7, s[54:55]\ns_mov_b32 s68, 0\ns_cmp_eq_u32 s32, 7\n" \
    "s_cselect_b32 s79, 64, 0x7fff0000\ns_and_b32 s69, s32, 7\ns_lshl_b32 s69, s69, 21\ns_add_u32 s82, s42, s69\ns_lshr_b32 s69, s82, 8\ns_and_b32 s69, s69, 0x2000\n" \
    "s_xor_b32 s82, s82, s69\ns_and_b32 s69, s32, 15\ns_lshl_b32 s69, s69, 19\ns_add_u32 s83, s43, s69\ns_lshr_b32 s69, s83, 8\ns_and_b32 s69, s69, 0x2000\n" \
    "s_xor_b32 s83, s83, s69\ns_and_b32 s69, s32, 15\ns_lshl_b32 s69, s69, 18\ns_add_u32 s84, s44, s69\ns_lshr_b32 s69, s84, 8\ns_and_b32 s69, s69, 0x2000\n" \
    "s_xor_b32 s84, s84, s69\ns_and_b32 s69, s32, 15\ns_lshl_b32 s69, s69, 18\ns_add_u32 s94, s45, s69\ns_lshr_b32 s69, s94, 8\ns_and_b32 s69, s69, 0x2000\n" \
    "s_xor_b32 s94, s94, s69\ns_add_u32 s69, s32, 1\ns_and_b32 s69, s69, 15\ns_lshl_b32 s69, s69, 17\ns_add_u32 s92, s46, s69\ns_and_b32 s69, s32, 63\ns_lshl_b32 s69, s69, 19\n" \
    "s_add_u32 s93, s47, s69\ns_mov_b32 s68, 0\ns_cmp_eq_u32 s32, 0\ns_cbranch_scc1 .Lr6_%=\ns_sub_u32 s77, s32, 1\ns_max_u32 s77, s77, 1\n.Lr4_%=:\nds_read_b32 v253, v10\n" \
    "s_waitcnt lgkmcnt(0)\nv_readfirstlane_b32 s74, v253\ns_nop 0\ns_and_b32 s75, s74, 127\ns_cmp_lt_u32 s75, s77\ns_cbranch_scc1 .Lr5_%=\ns_cmp_ge_u32 s75, s32\n" \
    "s_cbranch_scc1 .Lr6_%=\ns_memrealtime s[72:73]\ns_waitcnt lgkmcnt(0)\ns_lshr_b32 s69, s74, 8\ns_sub_u32 s69, s69, s72\ns_bfe_i32 s69, s69, 0x180000\n" \
    "s_add_u32 s78, s72, s69\ns_sub_u32 s76, s78, 60\n.Lr28_%=:\ns_memrealtime s[72:73]\ns_waitcnt lgkmcnt(0)\ns_sub_u32 s72, s72, s76\ns_cmp_lt_i32 s72, 0\n" \
    "s_cbranch_scc1 .Lr28_%=\nbuffer_load_dword v254, v8, s[64:67], 0 offen sc1\ns_waitcnt vmcnt(0)\nv_cmp_ge_u32 vcc, s32, v254\ns_nop 0\ns_cmp_eq_u64 vcc, 0\n" \
    "s_cselect_b32 s76, 4, s58\ns_add_u32 s76, s76, s78\n.Lr29_%=:\ns_memrealtime s[72:73]\ns_waitcnt lgkmcnt(0)\ns_sub_u32 s72, s72, s76\ns_cmp_lt_i32 s72, 0\n" \
    "s_cbranch_scc1 .Lr29_%=\ns_cbranch_vccz .Lr15_%=\n.Lr6_%=:\nbuffer_load_dword v254, v8, s[64:67], 0 offen sc1\ns_waitcnt vmcnt(0)\nv_cmp_ge_u32 vcc, s32, v254\ns_nop 0\n" \
    "s_cbranch_vccnz .Lr7_%=\n.Lr15_%=:\nbuffer_load_dwordx4 a[0:3], v11, s[64:67], s82 offen offset:0 sc1\n" \
    "buffer_load_dwordx4 a[16:19], v11, s[64:67], s82 offen offset:512 sc1\nbuffer_load_dwordx4 a[4:7], v11, s[64:67], s82 offen offset:2048 sc1\n" \
    "buffer_load_dwordx4 a[20:23], v11, s[64:67], s82 offen offset:2560 sc1\ns_add_u32 s82, s82, 4096\nbuffer_load_dwordx4 a[8:11], v11, s[64:67], s82 offen offset:0 sc1\n" \
    "buffer_load_dwordx4 a[24:27], v11, s[64:67], s82 offen offset:512 sc1\nbuffer_load_dwordx4 a[12:15], v11, s[64:67], s82 offen offset:2048 sc1\n" \
    "buffer_load_dwordx4 a[28:31], v11, s[64:67], s82 offen offset:2560 sc1\ns_add_u32 s82, s82, 12288\nbuffer_load_dwordx4 a[32:35], v11, s[64:67], s82 offen offset:0 sc1\n" \
    "buffer_load_dwordx4 a[48:51], v11, s[64:67], s82 offen offset:512 sc1\nbuffer_load_dwordx4 a[36:39], v11, s[64:67], s82 offen offset:2048 sc1\n" \
    "buffer_load_dwordx4 a[52:55], v11, s[64:67], s82 offen offset:2560 sc1\ns_add_u32 s82, s82, 4096\nbuffer_load_dwordx4 a[40:43], v11, s[64:67], s82 offen offset:0 sc1\n" \
    "buffer_load_dwordx4 a[56:59], v11, s[64:67], s82 offen offset:512 sc1\nbuffer_load_dwordx4 a[44:47], v11, s[64:67], s82 offen offset:2048 sc1\n" \
    "buffer_load_dwordx4 a[60:63], v11, s[64:67], s82 offen offset:2560 sc1\ns_add_u32 s82, s82, 12288\nbuffer_load_dwordx4 a[64:67], v11, s[64:67], s82 offen offset:0 sc1\n" \
    "buffer_load_dwordx4 a[80:83], v11, s[64:67], s82 offen offset:512 sc1\nbuffer_load_dwordx4 a[68:71], v11, s[64:67], s82 offen offset:2048 sc1\n" \
    "buffer_load_dwordx4 a[84:87], v11, s[64:67], s82 offen offset:2560 sc1\ns_add_u32 s82, s82, 4096\nbuffer_load_dwordx4 a[72:75], v11, s[64:67], s82 offen offset:0 sc1\n" \
    "buffer_load_dwordx4 a[88:91], v11, s[64:67], s82 offen offset:512 sc1\nbuffer_load_dwordx4 a[76:79], v11, s[64:67], s82 offen offset:2048 sc1\n" \
    "buffer_load_dwordx4 a[92:95], v11, s[64:67], s82 offen offset:2560 sc1\ns_add_u32 s82, s82, 12288\nbuffer_load_dwordx4 a[96:99], v11, s[64:67], s82 offen offset:0 sc1\n" \
    "buffer_load_dwordx4 a[112:115], v11, s[64:67], s82 offen offset:512 sc1\nbuffer_load_dwordx4 a[100:103], v11, s[64:67], s82 offen offset:2048 sc1\n" \
    "buffer_load_dwordx4 a[116:119], v11, s[64:67], s82 offen offset:2560 sc1\ns_add_u32 s82, s82, 4096\nbuffer_load_dwordx4 a[104:107], v11, s[64:67], s82 offen offset:0 sc1\n" \
    "buffer_load_dwordx4 a[120:123], v11, s[64:67], s82 offen offset:512 sc1\nbuffer_load_dwordx4 a[108:111], v11, s[64:67], s82 offen offset:2048 sc1\n" \
    "buffer_load_dwordx4 a[124:127], v11, s[64:67], s82 offen offset:2560 sc1\nbuffer_load_dword v255, off, s[64:67], s79 offset:64 sc1\ns_waitcnt vmcnt(0)\n.Lr27_%=:\n" \
    "v_readfirstlane_b32 s74, v255\ns_lshr_b32 s69, s79, 16\ns_add_u32 s74, s74, s69\ns_cmp_lt_u32 s74, 64\ns_cbranch_scc1 .Lr26_%=\ns_cmp_eq_u32 s32, 0\n" \
    "s_cbranch_scc1 .Lr20_%=\n.Lr13_%=:\nds_read_b32 v253, v10\ns_waitcnt lgkmcnt(0)\nv_readfirstlane_b32 s74, v253\ns_nop 0\ns_and_b32 s75, s74, 127\ns_cmp_lt_u32 s75, s32\n" \
    "s_cbranch_scc1 .Lr14_%=\ns_and_b32 s57, s74, 128\ns_memrealtime s[72:73]\ns_waitcnt lgkmcnt(0)\ns_lshr_b32 s69, s74, 8\ns_sub_u32 s69, s69, s72\n" \
    "s_bfe_i32 s69, s69, 0x180000\ns_add_u32 s56, s72, s69\n.Lr3_%=:\ns_sub_u32 s76, s56, s59\n.Lr31_%=:\ns_memrealtime s[72:73]\ns_waitcnt lgkmcnt(0)\ns_sub_u32 s72, s72, s76\n" \
    "s_cmp_lt_i32 s72, 0\ns_cbranch_scc1 .Lr31_%=\ns_cmp_gt_i32 s72, 28\ns_cbranch_scc1 .Lr24_%=\nv_bfe_u32 v24, v1, 2, 7\nbuffer_load_dword v24, v24, s[64:67], s60 offen sc1\n" \
    "s_sub_u32 s76, s56, s34\ns_add_u32 s76, s76, 24\ns_lshl_b32 s76, s76, 8\ns_lshl_b32 s77, s38, 8\ns_waitcnt vmcnt(0)\nv_and_b32 v25, 127, v24\n" \
    "v_cmp_eq_u32 s[74:75], s32, v25\nv_subrev_u32 v25, s76, v24\nv_cmp_lt_i32 vcc, 0, v25\ns_nop 0\ns_and_b64 vcc, vcc, s[74:75]\ns_cbranch_scc0 .Lr24_%=\ns_cmp_eq_u32 s32, 0\n" \
    "s_cbranch_scc1 .Lr24_%=\n.Lr25_%=:\ns_add_u32 s56, s56, s38\ns_xor_b32 s57, s57, 128\nv_subrev_u32 v25, s77, v25\nv_cmp_lt_i32 vcc, 0, v25\ns_nop 0\n" \
    "s_and_b64 vcc, vcc, s[74:75]\ns_cbranch_scc1 .Lr25_%=\ns_add_u32 s76, s56, s35\ns_cmp_eq_u32 s48, 0\ns_cbranch_scc1 .Lr32_%=\ns_lshl_b32 s74, s76, 8\n" \
    "s_add_u32 s74, s74, s57\ns_add_u32 s74, s74, s32\ns_add_u32 s74, s74, 1\nv_mov_b32 v253, s74\ns_xor_b32 s69, s48, 512\ns_mov_b64 exec, 1\n" \
    "buffer_store_dword v253, off, s[64:67], s69 sc1\ns_mov_b64 exec, -1\n.Lr32_%=:\n.Lr24_%=:\ns_lshl_b32 s69, s57, 7\ns_xor_b32 s84, s84, s69\ns_lshl_b32 s69, s57, 5\n" \
    "s_xor_b32 s92, s92, s69\ns_add_u32 s75, s83, 4096\ns_mov_b32 s68, 0\n.Lr8_%=:\ns_add_u32 s85, s84, 0x8000\ns_add_u32 s86, s84, 0x10000\ns_add_u32 s87, s84, 0x18000\n" \
    "s_sub_u32 s76, s56, s36\n.Lr33_%=:\ns_memrealtime s[72:73]\ns_waitcnt lgkmcnt(0)\ns_sub_u32 s72, s72, s76\ns_cmp_lt_i32 s72, 0\ns_cbranch_scc1 .Lr33_%=\n" \
    "buffer_load_dwordx4 v[32:35], v1, s[64:67], s83 offen offset:0 sc1\nbuffer_load_dwordx4 v[36:39], v1, s[64:67], s83 offen offset:1024 sc1\n" \
    "buffer_load_dwordx4 v[40:43], v1, s[64:67], s83 offen offset:2048 sc1\nbuffer_load_dwordx4 v[44:47], v1, s[64:67], s83 offen offset:3072 sc1\n" \
    "buffer_load_dwordx4 v[48:51], v1, s[64:67], s75 offen offset:0 sc1\nbuffer_load_dwordx4 v[52:55], v1, s[64:67], s75 offen offset:1024 sc1\n" \
    "buffer_load_dwordx4 v[56:59], v1, s[64:67], s75 offen offset:2048 sc1\nbuffer_load_dwordx4 v[60:63], v1, s[64:67], s75 offen offset:3072 sc1\ns_cmp_eq_u32 s32, 63\n" \
    "s_cbranch_scc1 .Lr34_%=\n.Lr35_%=:\ns_memrealtime s[72:73]\ns_waitcnt lgkmcnt(0)\ns_sub_u32 s72, s72, s56\ns_cmp_lt_i32 s72, 0\ns_cbranch_scc1 .Lr35_%=\n" \
    "buffer_load_dwordx4 v[68:71], v1, s[64:67], s84 offen offset:0 sc1\nbuffer_load_dwordx4 v[72:75], v1, s[64:67], s84 offen offset:1024 sc1\n" \
    "buffer_load_dwordx4 v[76:79], v1, s[64:67], s84 offen offset:2048 sc1\nbuffer_load_dwordx4 v[80:83], v1, s[64:67], s84 offen offset:3072 sc1\n" \
    "buffer_load_dwordx4 v[88:91], v1, s[64:67], s85 offen offset:0 sc1\nbuffer_load_dwordx4 v[92:95], v1, s[64:67], s85 offen offset:1024 sc1\n" \
    "buffer_load_dwordx4 v[96:99], v1, s[64:67], s85 offen offset:2048 sc1\nbuffer_load_dwordx4 v[100:103], v1, s[64:67], s85 offen offset:3072 sc1\n" \
    "buffer_load_dwordx4 v[108:111], v1, s[64:67], s86 offen offset:0 sc1\nbuffer_load_dwordx4 v[112:115], v1, s[64:67], s86 offen offset:1024 sc1\n" \
    "buffer_load_dwordx4 v[116:119], v1, s[64:67], s86 offen offset:2048 sc1\nbuffer_load_dwordx4 v[120:123], v1, s[64:67], s86 offen offset:3072 sc1\n" \
    "buffer_load_dwordx4 v[128:131], v1, s[64:67], s87 offen offset:0 sc1\nbuffer_load_dwordx4 v[132:135], v1, s[64:67], s87 offen offset:1024 sc1\n" \
    "buffer_load_dwordx4 v[136:139], v1, s[64:67], s87 offen offset:2048 sc1\nbuffer_load_dwordx4 v[140:143], v1, s[64:67], s87 offen offset:3072 sc1\n" \
    "buffer_load_dwordx4 v[64:67], v2, s[64:67], s84 offen offset:0 sc1\nbuffer_load_dwordx4 v[84:87], v2, s[64:67], s85 offen offset:0 sc1\n" \
    "buffer_load_dwordx4 v[104:107], v2, s[64:67], s86 offen offset:0 sc1\nbuffer_load_dwordx4 v[124:127], v2, s[64:67], s87 offen offset:0 sc1\nv_mov_b64 v[24:25], 0\n" \
    "v_mov_b64 v[26:27], 0\ns_xor_b32 s77, s92, 0x1000\ns_waitcnt vmcnt(26)\nv_permlane16_swap_b32 v32, v33\nv_permlane16_swap_b32 v34, v35\nv_permlane16_swap_b32 v36, v37\n" \
    "v_permlane16_swap_b32 v38, v39\ns_waitcnt vmcnt(24)\nv_permlane16_swap_b32 v40, v41\nv_permlane16_swap_b32 v42, v43\nv_permlane16_swap_b32 v44, v45\n" \
    "v_permlane16_swap_b32 v46, v47\ns_waitcnt vmcnt(22)\nv_permlane16_swap_b32 v48, v49\nv_permlane16_swap_b32 v50, v51\nv_permlane16_swap_b32 v52, v53\n" \
    "v_permlane16_swap_b32 v54, v55\ns_waitcnt vmcnt(20)\nv_permlane16_swap_b32 v56, v57\nv_permlane16_swap_b32 v58, v59\nv_permlane16_swap_b32 v60, v61\n" \
    "v_permlane16_swap_b32 v62, v63\ns_waitcnt vmcnt(19)\nv_smfmac_f32_16x16x64_bf16 v[24:27], v[68:71], a[0:7], v0\ns_waitcnt vmcnt(18)\n" \
    "v_smfmac_f32_16x16x64_bf16 v[24:27], v[72:75], a[8:15], v0\ns_waitcnt vmcnt(17)\nv_smfmac_f32_16x16x64_bf16 v[24:27], v[76:79], a[16:23], v0\ns_waitcnt vmcnt(16)\n" \
    "v_smfmac_f32_16x16x64_bf16 v[24:27], v[80:83], a[24:31], v0\ns_waitcnt vmcnt(15)\nv_smfmac_f32_16x16x64_bf16 v[24:27], v[88:91], a[32:39], v0\ns_waitcnt vmcnt(14)\n" \
    "v_smfmac_f32_16x16x64_bf16 v[24:27], v[92:95], a[40:47], v0\ns_waitcnt vmcnt(13)\nv_smfmac_f32_16x16x64_bf16 v[24:27], v[96:99], a[48:55], v0\ns_waitcnt vmcnt(12)\n" \
    "v_smfmac_f32_16x16x64_bf16 v[24:27], v[100:103], a[56:63], v0\ns_waitcnt vmcnt(11)\nv_smfmac_f32_16x16x64_bf16 v[24:27], v[108:111], a[64:71], v0\ns_waitcnt vmcnt(10)\n" \
    "v_smfmac_f32_16x16x64_bf16 v[24:27], v[112:115], a[72:79], v0\ns_waitcnt vmcnt(9)\nv_smfmac_f32_16x16x64_bf16 v[24:27], v[116:119], a[80:87], v0\ns_waitcnt vmcnt(8)\n" \
    "v_smfmac_f32_16x16x64_bf16 v[24:27], v[120:123], a[88:95], v0\ns_waitcnt vmcnt(7)\nv_smfmac_f32_16x16x64_bf16 v[24:27], v[128:131], a[96:103], v0\ns_waitcnt vmcnt(6)\n" \
    "v_smfmac_f32_16x16x64_bf16 v[24:27], v[132:135], a[104:111], v0\ns_waitcnt vmcnt(5)\nv_smfmac_f32_16x16x64_bf16 v[24:27], v[136:139], a[112:119], v0\ns_waitcnt vmcnt(4)\n" \
    "v_smfmac_f32_16x16x64_bf16 v[24:27], v[140:143], a[120:127], v0\ns_waitcnt vmcnt(3)\nv_smfmac_f32_16x16x64_bf16 v[24:27], v[64:67], v[32:39], v0\ns_waitcnt vmcnt(2)\n" \
    "v_smfmac_f32_16x16x64_bf16 v[24:27], v[84:87], v[40:47], v0\ns_waitcnt vmcnt(1)\nv_smfmac_f32_16x16x64_bf16 v[24:27], v[104:107], v[48:55], v0\ns_waitcnt vmcnt(0)\n" \
    "v_smfmac_f32_16x16x64_bf16 v[24:27], v[124:127], v[56:63], v0\ns_nop 7\nv_permlane32_swap_b32 v24, v26\nv_permlane32_swap_b32 v25, v27\n" \
    "v_pk_add_f32 v[24:25], v[24:25], v[26:27]\nv_cvt_pk_bf16_f32 v26, v24, v25\nds_write_b32 v3, v26\nds_read_b64_tr_b16 v[28:29], v4\n" \
    "ds_read_b64_tr_b16 v[30:31], v4 offset:64\nv_cmp_u_f32 vcc, v24, v25\ns_nop 0\ns_cbranch_vccnz .Lr17_%=\n.Lr16_%=:\ns_waitcnt lgkmcnt(0)\n" \
    "buffer_store_dwordx4 v[28:31], v6, s[64:67], s92 offen sc1\nbuffer_store_dwordx4 v[28:31], v6, s[64:67], s77 offen sc1\n.Lr10_%=:\ns_memrealtime s[72:73]\n" \
    "s_waitcnt lgkmcnt(0)\ns_add_u32 s76, s56, s35\n.Lr37_%=:\ns_sub_u32 s69, s76, s72\ns_cmp_ge_i32 s69, s37\ns_cbranch_scc1 .Lr38_%=\ns_add_u32 s76, s76, s38\n" \
    "s_xor_b32 s57, s57, 128\ns_branch .Lr37_%=\n.Lr38_%=:\ns_add_u32 s76, s76, s34\ns_lshl_b32 s74, s76, 8\ns_add_u32 s74, s74, s57\ns_add_u32 s74, s74, s32\n" \
    "s_add_u32 s74, s74, 1\nv_mov_b32 v253, s74\nds_write_b32 v10, v253\ns_sub_u32 s78, s76, s40\ns_add_u32 s76, s76, s35\ns_cmp_eq_u32 s48, 0\ns_cbranch_scc1 .Lr39_%=\n" \
    "s_lshl_b32 s74, s76, 8\ns_add_u32 s74, s74, s57\ns_add_u32 s74, s74, s32\ns_add_u32 s74, s74, 2\nv_mov_b32 v253, s74\ns_xor_b32 s69, s48, 0\ns_mov_b64 exec, 1\n" \
    "buffer_store_dword v253, off, s[64:67], s69 sc1\ns_mov_b64 exec, -1\n.Lr39_%=:\ns_xor_b32 s69, s49, 0x20000\ns_add_u32 s0, s94, s69\ns_add_u32 s1, s0, 0x4000\n" \
    "s_add_u32 s2, s0, 0x8000\ns_add_u32 s3, s0, 0xc000\ns_xor_b32 s69, s49, 0x30000\ns_add_u32 s4, s94, s69\ns_add_u32 s5, s4, 0x4000\ns_add_u32 s6, s4, 0x8000\n" \
    "s_add_u32 s7, s4, 0xc000\ns_xor_b32 s69, s49, 0x0\ns_add_u32 s8, s94, s69\ns_add_u32 s9, s8, 0x4000\ns_add_u32 s10, s8, 0x8000\ns_add_u32 s11, s8, 0xc000\n" \
    "s_xor_b32 s69, s49, 0x10000\ns_add_u32 s12, s94, s69\ns_add_u32 s13, s12, 0x4000\ns_add_u32 s14, s12, 0x8000\ns_add_u32 s15, s12, 0xc000\ns_cmp_ge_u32 s32, 62\n" \
    "s_cselect_b32 s76, s61, s39\ns_add_u32 s76, s76, s56\ns_mov_b32 s68, 0\ns_mov_b32 s80, 0\n.Lr40_%=:\ns_memrealtime s[72:73]\ns_waitcnt lgkmcnt(0)\ns_sub_u32 s72, s72, s76\n" \
    "s_cmp_lt_i32 s72, 0\ns_cbranch_scc1 .Lr40_%=\n.Lr11_%=:\nv_mov_b64 v[24:25], 0\nv_mov_b64 v[26:27], 0\nv_mov_b64 v[28:29], 0\nv_mov_b64 v[30:31], 0\n" \
    "v_mov_b64 v[228:229], 0\nv_mov_b64 v[230:231], 0\nv_mov_b64 v[232:233], 0\nv_mov_b64 v[234:235], 0\nv_mov_b32 v252, 0\ns_add_u32 s76, s94, 5120\n" \
    "buffer_load_dword v250, off, s[64:67], s76 sc1\ns_lshr_b32 s69, s32, 2\ns_cmp_eq_u32 s69, 3\ns_cselect_b32 s35, s63, s35\n" \
    "buffer_load_dwordx4 v[64:67], v2, s[64:67], s0 offen offset:0 sc1\nbuffer_load_dwordx4 v[84:87], v2, s[64:67], s1 offen offset:0 sc1\n" \
    "buffer_load_dwordx4 v[104:107], v2, s[64:67], s2 offen offset:0 sc1\nbuffer_load_dwordx4 v[124:127], v2, s[64:67], s3 offen offset:0 sc1\n" \
    "buffer_load_dwordx4 v[144:147], v2, s[64:67], s4 offen offset:0 sc1\nbuffer_load_dwordx4 v[148:151], v2, s[64:67], s5 offen offset:0 sc1\n" \
    "buffer_load_dwordx4 v[152:155], v2, s[64:67], s6 offen offset:0 sc1\nbuffer_load_dwordx4 v[156:159], v2, s[64:67], s7 offen offset:0 sc1\n" \
    "buffer_load_dwordx4 v[160:163], v2, s[64:67], s8 offen offset:0 sc1\nbuffer_load_dwordx4 v[164:167], v2, s[64:67], s9 offen offset:0 sc1\n" \
    "buffer_load_dwordx4 v[168:171], v2, s[64:67], s10 offen offset:0 sc1\nbuffer_load_dwordx4 v[172:175], v2, s[64:67], s11 offen offset:0 sc1\n" \
    "buffer_load_dwordx4 v[176:179], v2, s[64:67], s12 offen offset:0 sc1\nbuffer_load_dwordx4 v[180:183], v2, s[64:67], s13 offen offset:0 sc1\n" \
    "buffer_load_dwordx4 v[184:187], v2, s[64:67], s14 offen offset:0 sc1\nbuffer_load_dwordx4 v[188:191], v2, s[64:67], s15 offen offset:0 sc1\n" \
    "buffer_load_dwordx4 v[68:71], v1, s[64:67], s0 offen offset:0 sc1\nbuffer_load_dwordx4 v[72:75], v1, s[64:67], s0 offen offset:1024 sc1\n" \
    "buffer_load_dwordx4 v[76:79], v1, s[64:67], s0 offen offset:2048 sc1\nbuffer_load_dwordx4 v[80:83], v1, s[64:67], s0 offen offset:3072 sc1\n" \
    "buffer_load_dwordx4 v[88:91], v1, s[64:67], s1 offen offset:0 sc1\nbuffer_load_dwordx4 v[92:95], v1, s[64:67], s1 offen offset:1024 sc1\n" \
    "buffer_load_dwordx4 v[96:99], v1, s[64:67], s1 offen offset:2048 sc1\nbuffer_load_dwordx4 v[100:103], v1, s[64:67], s1 offen offset:3072 sc1\n" \
    "buffer_load_dwordx4 v[108:111], v1, s[64:67], s2 offen offset:0 sc1\nbuffer_load_dwordx4 v[112:115], v1, s[64:67], s2 offen offset:1024 sc1\n" \
    "buffer_load_dwordx4 v[116:119], v1, s[64:67], s2 offen offset:2048 sc1\nbuffer_load_dwordx4 v[120:123], v1, s[64:67], s2 offen offset:3072 sc1\n" \
    "buffer_load_dwordx4 v[128:131], v1, s[64:67], s3 offen offset:0 sc1\nbuffer_load_dwordx4 v[132:135], v1, s[64:67], s3 offen offset:1024 sc1\n" \
    "buffer_load_dwordx4 v[136:139], v1, s[64:67], s3 offen offset:2048 sc1\nbuffer_load_dwordx4 v[140:143], v1, s[64:67], s3 offen offset:3072 sc1\ns_waitcnt vmcnt(30)\n" \
    "v_smfmac_f32_16x16x64_bf16 v[24:27], v[64:67], v[32:39], v0\nbuffer_load_dwordx4 a[128:131], v1, s[64:67], s4 offen offset:0 sc1\nv_readfirstlane_b32 s34, v250\n" \
    "v_smfmac_f32_16x16x64_bf16 v[24:27], v[84:87], v[40:47], v0\nbuffer_load_dwordx4 a[132:135], v1, s[64:67], s4 offen offset:1024 sc1\ns_waitcnt vmcnt(30)\n" \
    "v_smfmac_f32_16x16x64_bf16 v[24:27], v[104:107], v[48:55], v0\nbuffer_load_dwordx4 a[136:139], v1, s[64:67], s4 offen offset:2048 sc1\nv_max3_u32 v250, v250, v64, v84\n" \
    "v_smfmac_f32_16x16x64_bf16 v[24:27], v[124:127], v[56:63], v0\nbuffer_load_dwordx4 a[140:143], v1, s[64:67], s4 offen offset:3072 sc1\ns_waitcnt vmcnt(30)\n" \
    "v_max3_u32 v250, v250, v104, v124\nv_smfmac_f32_16x16x64_bf16 v[28:31], v[144:147], v[32:39], v0\nbuffer_load_dwordx4 a[144:147], v1, s[64:67], s5 offen offset:0 sc1\n" \
    "v_smfmac_f32_16x16x64_bf16 v[28:31], v[148:151], v[40:47], v0\nbuffer_load_dwordx4 a[148:151], v1, s[64:67], s5 offen offset:1024 sc1\ns_waitcnt vmcnt(30)\n" \
    "v_smfmac_f32_16x16x64_bf16 v[28:31], v[152:155], v[48:55], v0\nbuffer_load_dwordx4 a[152:155], v1, s[64:67], s5 offen offset:2048 sc1\nv_max3_u32 v250, v250, v144, v148\n" \
    "v_smfmac_f32_16x16x64_bf16 v[28:31], v[156:159], v[56:63], v0\nbuffer_load_dwordx4 a[156:159], v1, s[64:67], s5 offen offset:3072 sc1\ns_waitcnt vmcnt(30)\n" \
    "v_max3_u32 v250, v250, v152, v156\nv_smfmac_f32_16x16x64_bf16 v[228:231], v[160:163], v[32:39], v0\nbuffer_load_dwordx4 a[160:163], v1, s[64:67], s6 offen offset:0 sc1\n" \
    "v_smfmac_f32_16x16x64_bf16 v[228:231], v[164:167], v[40:47], v0\nbuffer_load_dwordx4 a[164:167], v1, s[64:67], s6 offen offset:1024 sc1\ns_waitcnt vmcnt(30)\n" \
    "v_smfmac_f32_16x16x64_bf16 v[228:231], v[168:171], v[48:55], v0\nbuffer_load_dwordx4 a[168:171], v1, s[64:67], s6 offen offset:2048 sc1\nv_max3_u32 v250, v250, v160, v164\n" \
    "v_smfmac_f32_16x16x64_bf16 v[228:231], v[172:175], v[56:63], v0\nbuffer_load_dwordx4 a[172:175], v1, s[64:67], s6 offen offset:3072 sc1\ns_waitcnt vmcnt(30)\n" \
    "v_max3_u32 v250, v250, v168, v172\nv_smfmac_f32_16x16x64_bf16 v[232:235], v[176:179], v[32:39], v0\nbuffer_load_dwordx4 a[176:179], v1, s[64:67], s7 offen offset:0 sc1\n" \
    "v_smfmac_f32_16x16x64_bf16 v[232:235], v[180:183], v[40:47], v0\nbuffer_load_dwordx4 a[180:183], v1, s[64:67], s7 offen offset:1024 sc1\ns_waitcnt vmcnt(30)\n" \
    "v_smfmac_f32_16x16x64_bf16 v[232:235], v[184:187], v[48:55], v0\nbuffer_load_dwordx4 a[184:187], v1, s[64:67], s7 offen offset:2048 sc1\nv_max3_u32 v250, v250, v176, v180\n" \
    "v_smfmac_f32_16x16x64_bf16 v[232:235], v[188:191], v[56:63], v0\nbuffer_load_dwordx4 a[188:191], v1, s[64:67], s7 offen offset:3072 sc1\ns_waitcnt vmcnt(30)\n" \
    "v_max3_u32 v250, v250, v184, v188\nv_smfmac_f32_16x16x64_bf16 v[24:27], v[68:71], a[0:7], v0\nbuffer_load_dwordx4 a[192:195], v1, s[64:67], s8 offen offset:0 sc1\n" \
    "v_cmp_eq_u32 vcc, -1, v250\nv_smfmac_f32_16x16x64_bf16 v[24:27], v[72:75], a[8:15], v0\nbuffer_load_dwordx4 a[196:199], v1, s[64:67], s8 offen offset:1024 sc1\n" \
    "s_waitcnt vmcnt(30)\nv_smfmac_f32_16x16x64_bf16 v[24:27], v[76:79], a[16:23], v0\nbuffer_load_dwordx4 a[200:203], v1, s[64:67], s9 offen offset:0 sc1\n" \
    "s_cbranch_vccnz .Lr41_%=\nv_smfmac_f32_16x16x64_bf16 v[24:27], v[80:83], a[24:31], v0\nbuffer_load_dwordx4 a[204:207], v1, s[64:67], s9 offen offset:1024 sc1\n" \
    "s_waitcnt vmcnt(30)\nv_smfmac_f32_16x16x64_bf16 v[24:27], v[88:91], a[32:39], v0\nbuffer_load_dwordx4 a[208:211], v1, s[64:67], s10 offen offset:0 sc1\n" \
    "v_smfmac_f32_16x16x64_bf16 v[24:27], v[92:95], a[40:47], v0\nbuffer_load_dwordx4 a[212:215], v1, s[64:67], s10 offen offset:1024 sc1\ns_waitcnt vmcnt(30)\n" \
    "v_smfmac_f32_16x16x64_bf16 v[24:27], v[96:99], a[48:55], v0\nbuffer_load_dwordx4 a[216:219], v1, s[64:67], s11 offen offset:0 sc1\n" \
    "v_smfmac_f32_16x16x64_bf16 v[24:27], v[100:103], a[56:63], v0\nbuffer_load_dwordx4 a[220:223], v1, s[64:67], s11 offen offset:1024 sc1\ns_waitcnt vmcnt(30)\n" \
    "v_smfmac_f32_16x16x64_bf16 v[24:27], v[108:111], a[64:71], v0\nbuffer_load_dwordx4 a[224:227], v1, s[64:67], s12 offen offset:0 sc1\n" \
    "v_smfmac_f32_16x16x64_bf16 v[24:27], v[112:115], a[72:79], v0\nbuffer_load_dwordx4 a[228:231], v1, s[64:67], s12 offen offset:1024 sc1\ns_waitcnt vmcnt(30)\n" \
    "v_smfmac_f32_16x16x64_bf16 v[24:27], v[116:119], a[80:87], v0\nbuffer_load_dwordx4 a[232:235], v1, s[64:67], s13 offen offset:0 sc1\n" \
    "v_smfmac_f32_16x16x64_bf16 v[24:27], v[120:123], a[88:95], v0\nbuffer_load_dwordx4 a[236:239], v1, s[64:67], s13 offen offset:1024 sc1\ns_waitcnt vmcnt(30)\n" \
    "v_smfmac_f32_16x16x64_bf16 v[24:27], v[128:131], a[96:103], v0\nbuffer_load_dwordx4 a[240:243], v1, s[64:67], s14 offen offset:0 sc1\n" \
    "v_smfmac_f32_16x16x64_bf16 v[24:27], v[132:135], a[104:111], v0\nbuffer_load_dwordx4 a[244:247], v1, s[64:67], s14 offen offset:1024 sc1\ns_waitcnt vmcnt(30)\n" \
    "v_smfmac_f32_16x16x64_bf16 v[24:27], v[136:139], a[112:119], v0\nbuffer_load_dwordx4 a[248:251], v1, s[64:67], s15 offen offset:0 sc1\n" \
    "v_smfmac_f32_16x16x64_bf16 v[24:27], v[140:143], a[120:127], v0\nbuffer_load_dwordx4 a[252:255], v1, s[64:67], s15 offen offset:1024 sc1\ns_nop 1\ns_waitcnt vmcnt(30)\n" \
    "v_smfmac_f32_16x16x64_bf16 v[28:31], a[128:131], a[0:7], v0\ns_nop 2\nv_permlane32_swap_b32 v24, v26\nv_smfmac_f32_16x16x64_bf16 v[28:31], a[132:135], a[8:15], v0\n" \
    "s_waitcnt vmcnt(28)\nv_permlane32_swap_b32 v25, v27\nv_smfmac_f32_16x16x64_bf16 v[28:31], a[136:139], a[16:23], v0\nv_add_f32 v24, v24, v26\n" \
    "v_smfmac_f32_16x16x64_bf16 v[28:31], a[140:143], a[24:31], v0\ns_waitcnt vmcnt(26)\nv_add_f32 v25, v25, v27\nv_smfmac_f32_16x16x64_bf16 v[28:31], a[144:147], a[32:39], v0\n" \
    "v_fma_f32 v252, v24, v25, v252\nv_smfmac_f32_16x16x64_bf16 v[28:31], a[148:151], a[40:47], v0\ns_waitcnt vmcnt(24)\nv_cvt_pk_bf16_f32 v24, v24, v25\n" \
    "v_smfmac_f32_16x16x64_bf16 v[28:31], a[152:155], a[48:55], v0\nds_write_b32 v3, v24 offset:512\nv_smfmac_f32_16x16x64_bf16 v[28:31], a[156:159], a[56:63], v0\n" \
    "s_waitcnt vmcnt(22)\nv_smfmac_f32_16x16x64_bf16 v[28:31], a[160:163], a[64:71], v0\nv_smfmac_f32_16x16x64_bf16 v[28:31], a[164:167], a[72:79], v0\ns_waitcnt vmcnt(20)\n" \
    "v_smfmac_f32_16x16x64_bf16 v[28:31], a[168:171], a[80:87], v0\nv_smfmac_f32_16x16x64_bf16 v[28:31], a[172:175], a[88:95], v0\ns_waitcnt vmcnt(18)\n" \
    "v_smfmac_f32_16x16x64_bf16 v[28:31], a[176:179], a[96:103], v0\nv_smfmac_f32_16x16x64_bf16 v[28:31], a[180:183], a[104:111], v0\ns_waitcnt vmcnt(16)\n" \
    "v_smfmac_f32_16x16x64_bf16 v[28:31], a[184:187], a[112:119], v0\nv_smfmac_f32_16x16x64_bf16 v[28:31], a[188:191], a[120:127], v0\ns_nop 2\ns_waitcnt vmcnt(14)\n" \
    "v_smfmac_f32_16x16x64_bf16 v[228:231], a[192:195], a[0:7], v0\ns_nop 2\nv_permlane32_swap_b32 v28, v30\nv_smfmac_f32_16x16x64_bf16 v[228:231], a[196:199], a[8:15], v0\n" \
    "s_waitcnt vmcnt(12)\nv_permlane32_swap_b32 v29, v31\nv_smfmac_f32_16x16x64_bf16 v[228:231], a[200:203], a[32:39], v0\nv_add_f32 v28, v28, v30\n" \
    "v_smfmac_f32_16x16x64_bf16 v[228:231], a[204:207], a[40:47], v0\ns_waitcnt vmcnt(10)\nv_add_f32 v29, v29, v31\n" \
    "v_smfmac_f32_16x16x64_bf16 v[228:231], a[208:211], a[64:71], v0\nv_fma_f32 v252, v28, v29, v252\nv_smfmac_f32_16x16x64_bf16 v[228:231], a[212:215], a[72:79], v0\n" \
    "s_waitcnt vmcnt(8)\nv_cvt_pk_bf16_f32 v28, v28, v29\nv_smfmac_f32_16x16x64_bf16 v[228:231], a[216:219], a[96:103], v0\nds_write_b32 v3, v28 offset:768\n" \
    "v_smfmac_f32_16x16x64_bf16 v[228:231], a[220:223], a[104:111], v0\ns_nop 2\ns_waitcnt vmcnt(6)\nv_smfmac_f32_16x16x64_bf16 v[232:235], a[224:227], a[0:7], v0\ns_nop 2\n" \
    "v_permlane32_swap_b32 v228, v230\nv_smfmac_f32_16x16x64_bf16 v[232:235], a[228:231], a[8:15], v0\ns_waitcnt vmcnt(4)\nv_permlane32_swap_b32 v229, v231\n" \
    "v_smfmac_f32_16x16x64_bf16 v[232:235], a[232:235], a[32:39], v0\nv_add_f32 v228, v228, v230\nv_smfmac_f32_16x16x64_bf16 v[232:235], a[236:239], a[40:47], v0\n" \
    "s_waitcnt vmcnt(2)\nv_add_f32 v229, v229, v231\nv_smfmac_f32_16x16x64_bf16 v[232:235], a[240:243], a[64:71], v0\nv_fma_f32 v252, v228, v229, v252\n" \
    "v_smfmac_f32_16x16x64_bf16 v[232:235], a[244:247], a[72:79], v0\ns_waitcnt vmcnt(0)\nv_cvt_pk_bf16_f32 v228, v228, v229\n" \
    "v_smfmac_f32_16x16x64_bf16 v[232:235], a[248:251], a[96:103], v0\nds_write_b32 v3, v228 offset:0\nv_smfmac_f32_16x16x64_bf16 v[232:235], a[252:255], a[104:111], v0\n" \
    "s_nop 7\nv_permlane32_swap_b32 v232, v234\nv_permlane32_swap_b32 v233, v235\nv_add_f32 v232, v232, v234\nv_add_f32 v233, v233, v235\nv_fma_f32 v252, v232, v233, v252\n" \
    "v_cvt_pk_bf16_f32 v232, v232, v233\nv_cmp_u_f32 vcc, v252, v252\nds_write_b32 v3, v232 offset:256\nds_read_b64_tr_b16 v[24:25], v5\n" \
    "ds_read_b64_tr_b16 v[26:27], v5 offset:64\ns_cbranch_vccnz .Lr12_%=\n.Lr18_%=:\ns_cmp_lt_u32 s32, 4\ns_cbranch_scc1 .Lr42_%=\n.Lr43_%=:\ns_memrealtime s[72:73]\n" \
    "v_lshlrev_b32 v72, 16, v224\nv_and_b32 v73, 0xffff0000, v224\nv_lshlrev_b32 v74, 16, v225\nv_and_b32 v75, 0xffff0000, v225\nv_lshlrev_b32 v76, 16, v226\n" \
    "v_and_b32 v77, 0xffff0000, v226\nv_lshlrev_b32 v78, 16, v227\nv_and_b32 v79, 0xffff0000, v227\ns_xor_b32 s77, s93, 0x2000\ns_waitcnt lgkmcnt(0)\ns_sub_u32 s78, s72, s78\n" \
    "s_cmp_ge_u32 s32, 62\ns_cselect_b32 s78, -1, s78\nv_lshlrev_b32 v64, 16, v24\nv_and_b32 v65, 0xffff0000, v24\nv_lshlrev_b32 v66, 16, v25\nv_and_b32 v67, 0xffff0000, v25\n" \
    "v_lshlrev_b32 v68, 16, v26\nv_and_b32 v69, 0xffff0000, v26\nv_lshlrev_b32 v70, 16, v27\nv_and_b32 v71, 0xffff0000, v27\nv_mul_f32 v64, v64, v16\nv_mul_f32 v65, v65, v17\n" \
    "v_mul_f32 v66, v66, v18\nv_mul_f32 v67, v67, v19\nv_mul_f32 v68, v68, v20\nv_mul_f32 v69, v69, v21\nv_mul_f32 v70, v70, v22\nv_mul_f32 v71, v71, v23\n" \
    "v_cvt_pk_bf16_f32 v64, 0, v64\nv_cvt_pk_bf16_f32 v65, 0, v65\nv_cvt_pk_bf16_f32 v66, 0, v66\nv_cvt_pk_bf16_f32 v67, 0, v67\nv_cvt_pk_bf16_f32 v68, 0, v68\n" \
    "v_cvt_pk_bf16_f32 v69, 0, v69\nv_cvt_pk_bf16_f32 v70, 0, v70\nv_cvt_pk_bf16_f32 v71, 0, v71\nv_add_f32 v64, v64, v72\nv_add_f32 v65, v65, v73\nv_add_f32 v66, v66, v74\n" \
    "v_add_f32 v67, v67, v75\nv_add_f32 v68, v68, v76\nv_add_f32 v69, v69, v77\nv_add_f32 v70, v70, v78\nv_add_f32 v71, v71, v79\nv_cvt_pk_bf16_f32 v24, v64, v65\n" \
    "v_cvt_pk_bf16_f32 v25, v66, v67\nv_cvt_pk_bf16_f32 v26, v68, v69\nv_cvt_pk_bf16_f32 v27, v70, v71\nv_min_u32 v28, -2, v24\nv_min_u32 v29, -2, v25\nv_min_u32 v30, -2, v26\n" \
    "v_min_u32 v31, -2, v27\ns_cmp_ge_i32 s78, 0\ns_cbranch_scc1 .Lr44_%=\nbuffer_store_dwordx4 v[28:31], v1, s[64:67], s93 offen sc1\n" \
    "buffer_store_dwordx4 v[28:31], v1, s[64:67], s77 offen sc1\n.Lr44_%=:\nv_lshlrev_b32 v64, 16, v24\nv_and_b32 v65, 0xffff0000, v24\nv_lshlrev_b32 v66, 16, v25\n" \
    "v_and_b32 v67, 0xffff0000, v25\nv_lshlrev_b32 v68, 16, v26\nv_and_b32 v69, 0xffff0000, v26\nv_lshlrev_b32 v70, 16, v27\nv_and_b32 v71, 0xffff0000, v27\n" \
    "v_add_f32 v72, v64, v65\nv_add_f32 v73, v66, v67\nv_add_f32 v74, v68, v69\nv_add_f32 v75, v70, v71\nv_add_f32 v72, v72, v73\nv_add_f32 v74, v74, v75\n" \
    "v_add_f32 v76, v72, v74\nv_add_f32 v77, v72, v74\ns_nop 1\nv_permlane32_swap_b32 v76, v77\nv_add_f32 v76, v76, v77\nv_mul_f32 v76, 0x3d800000, v76\n" \
    "v_sub_f32 v64, v64, v76\nv_sub_f32 v65, v65, v76\nv_sub_f32 v66, v66, v76\nv_sub_f32 v67, v67, v76\nv_sub_f32 v68, v68, v76\nv_sub_f32 v69, v69, v76\n" \
    "v_sub_f32 v70, v70, v76\nv_sub_f32 v71, v71, v76\nv_mul_f32 v72, v64, v64\nv_mul_f32 v73, v65, v65\nv_fmac_f32 v72, v66, v66\nv_fmac_f32 v73, v67, v67\n" \
    "v_fmac_f32 v72, v68, v68\nv_fmac_f32 v73, v69, v69\nv_fmac_f32 v72, v70, v70\nv_fmac_f32 v73, v71, v71\nv_add_f32 v74, v72, v73\nv_add_f32 v75, v72, v73\ns_nop 1\n" \
    "v_permlane32_swap_b32 v74, v75\nv_add_f32 v77, v74, v75\nv_min_u32 v76, -2, v76\nv_min_u32 v77, -2, v77\ns_cmp_lt_i32 s78, 0\ns_cbranch_scc1 .Lr45_%=\n" \
    "s_add_u32 s76, s32, 2\n.Lr47_%=:\nds_read_b32 v253, v10\ns_waitcnt lgkmcnt(0)\nv_readfirstlane_b32 s74, v253\ns_nop 0\ns_and_b32 s75, s74, 127\ns_cmp_lt_u32 s75, s76\n" \
    "s_cbranch_scc1 .Lr48_%=\nbuffer_store_dwordx4 v[28:31], v1, s[64:67], s93 offen sc1\nbuffer_store_dwordx4 v[28:31], v1, s[64:67], s77 offen sc1\n.Lr45_%=:\n" \
    "buffer_store_dwordx2 v[76:77], v9, s[64:67], s93 offen sc1\ns_cmp_ge_u32 s32, 60\ns_cbranch_scc1 .Lr49_%=\ns_add_u32 s76, s32, 2\n.Lr51_%=:\nds_read_b32 v253, v10\n" \
    "s_waitcnt lgkmcnt(0)\nv_readfirstlane_b32 s74, v253\ns_nop 0\ns_and_b32 s75, s74, 127\ns_cmp_lt_u32 s75, s76\ns_cbranch_scc1 .Lr52_%=\ns_cmp_ge_u32 s32, 56\n" \
    "s_cbranch_scc1 .Lr49_%=\nv_mov_b64 v[80:81], -1\nv_mov_b64 v[82:83], -1\ns_add_u32 s69, s32, 9\ns_and_b32 s69, s69, 15\ns_lshl_b32 s69, s69, 17\ns_add_u32 s92, s46, s69\n" \
    "s_xor_b32 s74, s92, 0x1000\nbuffer_store_dwordx4 v[80:83], v6, s[64:67], s92 offen sc1\nbuffer_store_dwordx4 v[80:83], v6, s[64:67], s74 offen sc1\n.Lr49_%=:\n" \
    "v_add_u32 v7, 0x40000, v7\ns_add_u32 s32, s32, 4\ns_cmp_lt_u32 s32, 64\ns_cbranch_scc1 .Lr2_%=\ns_endpgm\n.Lr26_%=:\ns_sleep 16\n" \
    "buffer_load_dword v255, off, s[64:67], s79 offset:64 sc1\ns_waitcnt vmcnt(0)\ns_add_u32 s68, s68, 1\ns_and_b32 s69, s68, 63\ns_cbranch_scc1 .Lr27_%=\n" \
    "s_cmp_gt_u32 s68, 0x400000\ns_cbranch_scc1 .Lr1_%=\nbuffer_load_dword v253, off, s[64:67], 0 sc1\ns_waitcnt vmcnt(0)\nv_readfirstlane_b32 s69, v253\ns_nop 0\n" \
    "s_cmp_eq_u32 s69, 1\ns_cbranch_scc0 .Lr27_%=\ns_endpgm\n.Lr5_%=:\ns_sleep 2\ns_add_u32 s68, s68, 1\ns_and_b32 s69, s68, 63\ns_cbranch_scc1 .Lr4_%=\n" \
    "s_cmp_gt_u32 s68, 0x400000\ns_cbranch_scc1 .Lr1_%=\nbuffer_load_dword v253, off, s[64:67], 0 sc1\ns_waitcnt vmcnt(0)\nv_readfirstlane_b32 s69, v253\ns_nop 0\n" \
    "s_cmp_eq_u32 s69, 1\ns_cbranch_scc0 .Lr4_%=\ns_endpgm\n.Lr7_%=:\ns_sleep 6\ns_add_u32 s68, s68, 1\ns_and_b32 s69, s68, 63\ns_cbranch_scc1 .Lr6_%=\n" \
    "s_cmp_gt_u32 s68, 0x400000\ns_cbranch_scc1 .Lr1_%=\nbuffer_load_dword v253, off, s[64:67], 0 sc1\ns_waitcnt vmcnt(0)\nv_readfirstlane_b32 s69, v253\ns_nop 0\n" \
    "s_cmp_eq_u32 s69, 1\ns_cbranch_scc0 .Lr6_%=\ns_endpgm\n.Lr14_%=:\ns_sleep 2\ns_add_u32 s68, s68, 1\ns_and_b32 s69, s68, 63\ns_cbranch_scc1 .Lr13_%=\n" \
    "s_cmp_gt_u32 s68, 0x400000\ns_cbranch_scc1 .Lr1_%=\nbuffer_load_dword v253, off, s[64:67], 0 sc1\ns_waitcnt vmcnt(0)\nv_readfirstlane_b32 s69, v253\ns_nop 0\n" \
    "s_cmp_eq_u32 s69, 1\ns_cbranch_scc0 .Lr13_%=\ns_endpgm\n.Lr21_%=:\nbuffer_load_dword v253, off, s[64:67], 0 offset:256 sc1\ns_waitcnt vmcnt(0)\n" \
    "v_readfirstlane_b32 s74, v253\ns_nop 0\ns_cmp_lt_u32 s74, 64\ns_cbranch_scc1 .Lr22_%=\ns_memrealtime s[72:73]\ns_waitcnt lgkmcnt(0)\ns_add_u32 s72, s72, s41\n" \
    "s_or_b32 s72, s72, 1\nv_mov_b32 v253, s72\nv_lshlrev_b32 v252, 7, v1\ns_mov_b64 exec, 0xff\nbuffer_store_dword v253, v252, s[64:67], s56 offen sc1\ns_mov_b64 exec, -1\n" \
    "s_branch .Lr2_%=\n.Lr20_%=:\nbuffer_load_dword v253, off, s[64:67], s60 offset:128 sc1\ns_waitcnt vmcnt(0)\nv_readfirstlane_b32 s74, v253\ns_nop 0\ns_cmp_eq_u32 s74, 0\n" \
    "s_cbranch_scc1 .Lr23_%=\ns_andn2_b32 s56, s74, 3\ns_add_u32 s76, s56, s35\ns_cmp_eq_u32 s48, 0\ns_cbranch_scc1 .Lr30_%=\ns_lshl_b32 s74, s76, 8\ns_add_u32 s74, s74, s57\n" \
    "s_add_u32 s74, s74, s32\ns_add_u32 s74, s74, 1\nv_mov_b32 v253, s74\ns_xor_b32 s69, s48, 512\ns_mov_b64 exec, 1\nbuffer_store_dword v253, off, s[64:67], s69 sc1\n" \
    "s_mov_b64 exec, -1\n.Lr30_%=:\ns_branch .Lr3_%=\n.Lr22_%=:\ns_sleep 4\ns_add_u32 s68, s68, 1\ns_and_b32 s69, s68, 63\ns_cbranch_scc1 .Lr21_%=\ns_cmp_gt_u32 s68, 0x400000\n" \
    "s_cbranch_scc1 .Lr1_%=\nbuffer_load_dword v253, off, s[64:67], 0 sc1\ns_waitcnt vmcnt(0)\nv_readfirstlane_b32 s69, v253\ns_nop 0\ns_cmp_eq_u32 s69, 1\n" \
    "s_cbranch_scc0 .Lr21_%=\ns_endpgm\n.Lr23_%=:\ns_sleep 4\ns_add_u32 s68, s68, 1\ns_and_b32 s69, s68, 63\ns_cbranch_scc1 .Lr20_%=\ns_cmp_gt_u32 s68, 0x400000\n" \
    "s_cbranch_scc1 .Lr1_%=\nbuffer_load_dword v253, off, s[64:67], 0 sc1\ns_waitcnt vmcnt(0)\nv_readfirstlane_b32 s69, v253\ns_nop 0\ns_cmp_eq_u32 s69, 1\n" \
    "s_cbranch_scc0 .Lr20_%=\ns_endpgm\n.Lr34_%=:\ns_waitcnt vmcnt(0)\nv_permlane16_swap_b32 v32, v33\nv_permlane16_swap_b32 v34, v35\nv_permlane16_swap_b32 v36, v37\n" \
    "v_permlane16_swap_b32 v38, v39\nv_permlane16_swap_b32 v40, v41\nv_permlane16_swap_b32 v42, v43\nv_permlane16_swap_b32 v44, v45\nv_permlane16_swap_b32 v46, v47\n" \
    "v_permlane16_swap_b32 v48, v49\nv_permlane16_swap_b32 v50, v51\nv_permlane16_swap_b32 v52, v53\nv_permlane16_swap_b32 v54, v55\nv_permlane16_swap_b32 v56, v57\n" \
    "v_permlane16_swap_b32 v58, v59\nv_permlane16_swap_b32 v60, v61\nv_permlane16_swap_b32 v62, v63\ns_branch .Lr10_%=\n.Lr17_%=:\ns_cmp_lt_u32 s68, 3\ns_cbranch_scc1 .Lr9_%=\n" \
    "v_max3_u32 v252, v32, v33, v34\nv_max3_u32 v253, v35, v36, v37\nv_max3_u32 v252, v252, v38, v39\nv_max3_u32 v253, v253, v40, v41\nv_max3_u32 v252, v252, v42, v43\n" \
    "v_max3_u32 v253, v253, v44, v45\nv_max3_u32 v252, v252, v46, v47\nv_max3_u32 v253, v253, v48, v49\nv_max3_u32 v252, v252, v50, v51\nv_max3_u32 v253, v253, v52, v53\n" \
    "v_max3_u32 v252, v252, v54, v55\nv_max3_u32 v253, v253, v56, v57\nv_max3_u32 v252, v252, v58, v59\nv_max3_u32 v253, v253, v60, v61\nv_max3_u32 v252, v252, v62, v63\n" \
    "v_max3_u32 v252, v252, v64, v65\nv_max3_u32 v253, v253, v66, v67\nv_max3_u32 v252, v252, v68, v69\nv_max3_u32 v253, v253, v70, v71\nv_max3_u32 v252, v252, v72, v73\n" \
    "v_max3_u32 v253, v253, v74, v75\nv_max3_u32 v252, v252, v76, v77\nv_max3_u32 v253, v253, v78, v79\nv_max3_u32 v252, v252, v80, v81\nv_max3_u32 v253, v253, v82, v83\n" \
    "v_max3_u32 v252, v252, v84, v85\nv_max3_u32 v253, v253, v86, v87\nv_max3_u32 v252, v252, v88, v89\nv_max3_u32 v253, v253, v90, v91\nv_max3_u32 v252, v252, v92, v93\n" \
    "v_max3_u32 v253, v253, v94, v95\nv_max3_u32 v252, v252, v96, v97\nv_max3_u32 v253, v253, v98, v99\nv_max3_u32 v252, v252, v100, v101\nv_max3_u32 v253, v253, v102, v103\n" \
    "v_max3_u32 v252, v252, v104, v105\nv_max3_u32 v253, v253, v106, v107\nv_max3_u32 v252, v252, v108, v109\nv_max3_u32 v253, v253, v110, v111\n" \
    "v_max3_u32 v252, v252, v112, v113\nv_max3_u32 v253, v253, v114, v115\nv_max3_u32 v252, v252, v116, v117\nv_max3_u32 v253, v253, v118, v119\n" \
    "v_max3_u32 v252, v252, v120, v121\nv_max3_u32 v253, v253, v122, v123\nv_max3_u32 v252, v252, v124, v125\nv_max3_u32 v253, v253, v126, v127\n" \
    "v_max3_u32 v252, v252, v128, v129\nv_max3_u32 v253, v253, v130, v131\nv_max3_u32 v252, v252, v132, v133\nv_max3_u32 v253, v253, v134, v135\n" \
    "v_max3_u32 v252, v252, v136, v137\nv_max3_u32 v253, v253, v138, v139\nv_max3_u32 v252, v252, v140, v141\nv_max3_u32 v253, v253, v142, v143\nv_max_u32 v252, v252, v253\n" \
    "v_cmp_eq_u32 vcc, -1, v252\ns_nop 0\ns_cbranch_vccz .Lr16_%=\n.Lr9_%=:\ns_add_u32 s56, s56, s38\ns_xor_b32 s57, s57, 128\ns_xor_b32 s84, s84, 0x4000\n" \
    "s_xor_b32 s92, s92, 0x1000\ns_add_u32 s76, s56, s35\ns_cmp_eq_u32 s48, 0\ns_cbranch_scc1 .Lr36_%=\ns_lshl_b32 s74, s76, 8\ns_add_u32 s74, s74, s57\n" \
    "s_add_u32 s74, s74, s32\ns_add_u32 s74, s74, 1\nv_mov_b32 v253, s74\ns_xor_b32 s69, s48, 512\ns_mov_b64 exec, 1\nbuffer_store_dword v253, off, s[64:67], s69 sc1\n" \
    "s_mov_b64 exec, -1\n.Lr36_%=:\ns_add_u32 s68, s68, 1\ns_and_b32 s69, s68, 63\ns_cbranch_scc1 .Lr8_%=\ns_cmp_gt_u32 s68, 0x400000\ns_cbranch_scc1 .Lr1_%=\n" \
    "buffer_load_dword v253, off, s[64:67], 0 sc1\ns_waitcnt vmcnt(0)\nv_readfirstlane_b32 s69, v253\ns_nop 0\ns_cmp_eq_u32 s69, 1\ns_cbranch_scc0 .Lr8_%=\ns_endpgm\n.Lr41_%=:\n" \
    "s_waitcnt vmcnt(0)\ns_add_u32 s68, s68, 1\ns_and_b32 s69, s68, 63\ns_cbranch_scc1 .Lr11_%=\ns_cmp_gt_u32 s68, 0x400000\ns_cbranch_scc1 .Lr1_%=\n" \
    "buffer_load_dword v253, off, s[64:67], 0 sc1\ns_waitcnt vmcnt(0)\nv_readfirstlane_b32 s69, v253\ns_nop 0\ns_cmp_eq_u32 s69, 1\ns_cbranch_scc0 .Lr11_%=\ns_endpgm\n" \
    ".Lr12_%=:\ns_cmp_lg_u32 s80, 0\ns_cbranch_scc1 .Lr18_%=\ns_sleep 4\nbuffer_load_dwordx4 v[32:35], v1, s[64:67], s83 offen offset:0 sc1\n" \
    "buffer_load_dwordx4 v[36:39], v1, s[64:67], s83 offen offset:1024 sc1\nbuffer_load_dwordx4 v[40:43], v1, s[64:67], s83 offen offset:2048 sc1\n" \
    "buffer_load_dwordx4 v[44:47], v1, s[64:67], s83 offen offset:3072 sc1\nbuffer_load_dwordx4 v[48:51], v1, s[64:67], s75 offen offset:0 sc1\n" \
    "buffer_load_dwordx4 v[52:55], v1, s[64:67], s75 offen offset:1024 sc1\nbuffer_load_dwordx4 v[56:59], v1, s[64:67], s75 offen offset:2048 sc1\n" \
    "buffer_load_dwordx4 v[60:63], v1, s[64:67], s75 offen offset:3072 sc1\ns_waitcnt vmcnt(0)\nv_permlane16_swap_b32 v32, v33\nv_permlane16_swap_b32 v34, v35\n" \
    "v_permlane16_swap_b32 v36, v37\nv_permlane16_swap_b32 v38, v39\nv_permlane16_swap_b32 v40, v41\nv_permlane16_swap_b32 v42, v43\nv_permlane16_swap_b32 v44, v45\n" \
    "v_permlane16_swap_b32 v46, v47\nv_permlane16_swap_b32 v48, v49\nv_permlane16_swap_b32 v50, v51\nv_permlane16_swap_b32 v52, v53\nv_permlane16_swap_b32 v54, v55\n" \
    "v_permlane16_swap_b32 v56, v57\nv_permlane16_swap_b32 v58, v59\nv_permlane16_swap_b32 v60, v61\nv_permlane16_swap_b32 v62, v63\nv_max3_u32 v252, v32, v33, v34\n" \
    "v_max3_u32 v253, v35, v36, v37\nv_max3_u32 v252, v252, v38, v39\nv_max3_u32 v253, v253, v40, v41\nv_max3_u32 v252, v252, v42, v43\nv_max3_u32 v253, v253, v44, v45\n" \
    "v_max3_u32 v252, v252, v46, v47\nv_max3_u32 v253, v253, v48, v49\nv_max3_u32 v252, v252, v50, v51\nv_max3_u32 v253, v253, v52, v53\nv_max3_u32 v252, v252, v54, v55\n" \
    "v_max3_u32 v253, v253, v56, v57\nv_max3_u32 v252, v252, v58, v59\nv_max3_u32 v253, v253, v60, v61\nv_max3_u32 v252, v252, v62, v63\ns_mov_b32 s76, s94\n.Lr19_%=:\n" \
    "s_add_u32 s84, s76, 0x0\ns_add_u32 s85, s76, 0x4000\ns_add_u32 s86, s76, 0x8000\ns_add_u32 s87, s76, 0xc000\n" \
    "buffer_load_dwordx4 v[64:67], v2, s[64:67], s84 offen offset:0 sc1\nbuffer_load_dwordx4 v[68:71], v1, s[64:67], s84 offen offset:0 sc1\n" \
    "buffer_load_dwordx4 v[72:75], v1, s[64:67], s84 offen offset:1024 sc1\nbuffer_load_dwordx4 v[76:79], v1, s[64:67], s84 offen offset:2048 sc1\n" \
    "buffer_load_dwordx4 v[80:83], v1, s[64:67], s84 offen offset:3072 sc1\nbuffer_load_dwordx4 v[84:87], v2, s[64:67], s85 offen offset:0 sc1\n" \
    "buffer_load_dwordx4 v[88:91], v1, s[64:67], s85 offen offset:0 sc1\nbuffer_load_dwordx4 v[92:95], v1, s[64:67], s85 offen offset:1024 sc1\n" \
    "buffer_load_dwordx4 v[96:99], v1, s[64:67], s85 offen offset:2048 sc1\nbuffer_load_dwordx4 v[100:103], v1, s[64:67], s85 offen offset:3072 sc1\n" \
    "buffer_load_dwordx4 v[104:107], v2, s[64:67], s86 offen offset:0 sc1\nbuffer_load_dwordx4 v[108:111], v1, s[64:67], s86 offen offset:0 sc1\n" \
    "buffer_load_dwordx4 v[112:115], v1, s[64:67], s86 offen offset:1024 sc1\nbuffer_load_dwordx4 v[116:119], v1, s[64:67], s86 offen offset:2048 sc1\n" \
    "buffer_load_dwordx4 v[120:123], v1, s[64:67], s86 offen offset:3072 sc1\nbuffer_load_dwordx4 v[124:127], v2, s[64:67], s87 offen offset:0 sc1\n" \
    "buffer_load_dwordx4 v[128:131], v1, s[64:67], s87 offen offset:0 sc1\nbuffer_load_dwordx4 v[132:135], v1, s[64:67], s87 offen offset:1024 sc1\n" \
    "buffer_load_dwordx4 v[136:139], v1, s[64:67], s87 offen offset:2048 sc1\nbuffer_load_dwordx4 v[140:143], v1, s[64:67], s87 offen offset:3072 sc1\ns_waitcnt vmcnt(0)\n" \
    "v_max3_u32 v252, v252, v64, v65\nv_max3_u32 v253, v253, v66, v67\nv_max3_u32 v252, v252, v68, v69\nv_max3_u32 v253, v253, v70, v71\nv_max3_u32 v252, v252, v72, v73\n" \
    "v_max3_u32 v253, v253, v74, v75\nv_max3_u32 v252, v252, v76, v77\nv_max3_u32 v253, v253, v78, v79\nv_max3_u32 v252, v252, v80, v81\nv_max3_u32 v253, v253, v82, v83\n" \
    "v_max3_u32 v252, v252, v84, v85\nv_max3_u32 v253, v253, v86, v87\nv_max3_u32 v252, v252, v88, v89\nv_max3_u32 v253, v253, v90, v91\nv_max3_u32 v252, v252, v92, v93\n" \
    "v_max3_u32 v253, v253, v94, v95\nv_max3_u32 v252, v252, v96, v97\nv_max3_u32 v253, v253, v98, v99\nv_max3_u32 v252, v252, v100, v101\nv_max3_u32 v253, v253, v102, v103\n" \
    "v_max3_u32 v252, v252, v104, v105\nv_max3_u32 v253, v253, v106, v107\nv_max3_u32 v252, v252, v108, v109\nv_max3_u32 v253, v253, v110, v111\n" \
    "v_max3_u32 v252, v252, v112, v113\nv_max3_u32 v253, v253, v114, v115\nv_max3_u32 v252, v252, v116, v117\nv_max3_u32 v253, v253, v118, v119\n" \
    "v_max3_u32 v252, v252, v120, v121\nv_max3_u32 v253, v253, v122, v123\nv_max3_u32 v252, v252, v124, v125\nv_max3_u32 v253, v253, v126, v127\n" \
    "v_max3_u32 v252, v252, v128, v129\nv_max3_u32 v253, v253, v130, v131\nv_max3_u32 v252, v252, v132, v133\nv_max3_u32 v253, v253, v134, v135\n" \
    "v_max3_u32 v252, v252, v136, v137\nv_max3_u32 v253, v253, v138, v139\nv_max3_u32 v252, v252, v140, v141\nv_max3_u32 v253, v253, v142, v143\ns_add_u32 s76, s76, 0x10000\n" \
    "s_sub_u32 s69, s76, s94\ns_cmp_lt_u32 s69, 0x40000\ns_cbranch_scc1 .Lr19_%=\nv_max_u32 v252, v252, v253\nv_cmp_eq_u32 vcc, -1, v252\ns_nop 0\ns_cmp_eq_u64 vcc, 0\n" \
    "s_cselect_b32 s80, 1, 0\ns_add_u32 s68, s68, 1\ns_and_b32 s69, s68, 63\ns_cbranch_scc1 .Lr11_%=\ns_cmp_gt_u32 s68, 0x400000\ns_cbranch_scc1 .Lr1_%=\n" \
    "buffer_load_dword v253, off, s[64:67], 0 sc1\ns_waitcnt vmcnt(0)\nv_readfirstlane_b32 s69, v253\ns_nop 0\ns_cmp_eq_u32 s69, 1\ns_cbranch_scc0 .Lr11_%=\ns_endpgm\n" \
    ".Lr42_%=:\nbuffer_load_dwordx4 v[16:19], v12, s[64:67], 0 offen offset:0 sc1\nbuffer_load_dwordx4 v[20:23], v12, s[64:67], 0 offen offset:16 sc1\ns_waitcnt vmcnt(0)\n" \
    "v_max3_u32 v252, v16, v17, v18\nv_max3_u32 v253, v19, v20, v21\nv_max3_u32 v252, v252, v22, v23\nv_max_u32 v252, v252, v253\nv_cmp_eq_u32 vcc, -1, v252\ns_nop 0\n" \
    "s_cbranch_vccz .Lr43_%=\ns_sleep 8\ns_add_u32 s68, s68, 1\ns_and_b32 s69, s68, 63\ns_cbranch_scc1 .Lr42_%=\ns_cmp_gt_u32 s68, 0x400000\ns_cbranch_scc1 .Lr1_%=\n" \
    "buffer_load_dword v253, off, s[64:67], 0 sc1\ns_waitcnt vmcnt(0)\nv_readfirstlane_b32 s69, v253\ns_nop 0\ns_cmp_eq_u32 s69, 1\ns_cbranch_scc0 .Lr42_%=\ns_endpgm\n" \
    ".Lr48_%=:\ns_sleep 2\ns_add_u32 s68, s68, 1\ns_and_b32 s69, s68, 63\ns_cbranch_scc1 .Lr47_%=\ns_cmp_gt_u32 s68, 0x400000\ns_cbranch_scc1 .Lr1_%=\n" \
    "buffer_load_dword v253, off, s[64:67], 0 sc1\ns_waitcnt vmcnt(0)\nv_readfirstlane_b32 s69, v253\ns_nop 0\ns_cmp_eq_u32 s69, 1\ns_cbranch_scc0 .Lr47_%=\ns_endpgm\n" \
    ".Lr52_%=:\ns_sleep 2\ns_add_u32 s68, s68, 1\ns_and_b32 s69, s68, 63\ns_cbranch_scc1 .Lr51_%=\ns_cmp_gt_u32 s68, 0x400000\ns_cbranch_scc1 .Lr1_%=\n" \
    "buffer_load_dword v253, off, s[64:67], 0 sc1\ns_waitcnt vmcnt(0)\nv_readfirstlane_b32 s69, v253\ns_nop 0\ns_cmp_eq_u32 s69, 1\ns_cbranch_scc0 .Lr51_%=\ns_endpgm\n.Lr1_%=:\n" \
    "v_mov_b32 v253, 1\nbuffer_store_dword v253, off, s[64:67], 0 sc1\ns_endpgm\n"
#define ASM_F1 \
    "v_mov_b32 v128, 0x3f803f80\nv_mov_b32 v129, 0x3f803f80\nv_mov_b32 v130, 0x3f803f80\nv_mov_b32 v131, 0x3f803f80\nds_read_b128 v[64:67], v12 offset:0\n" \
    "ds_read_b128 v[68:71], v12 offset:16\nds_read_b128 v[72:75], v13 offset:0\nds_read_b128 v[80:83], v12 offset:64\nds_read_b128 v[84:87], v12 offset:80\n" \
    "ds_read_b128 v[88:91], v13 offset:32\nds_read_b128 v[96:99], v12 offset:128\nds_read_b128 v[100:103], v12 offset:144\nds_read_b128 v[104:107], v13 offset:64\n" \
    "v_accvgpr_read_b32 v112, a0\nv_accvgpr_read_b32 v113, a1\nv_accvgpr_read_b32 v114, a2\nv_accvgpr_read_b32 v115, a3\nv_lshlrev_b32 v116, 16, v112\n" \
    "v_and_b32 v117, 0xffff0000, v112\nv_lshlrev_b32 v118, 16, v113\nv_and_b32 v119, 0xffff0000, v113\nv_lshlrev_b32 v120, 16, v114\nv_and_b32 v121, 0xffff0000, v114\n" \
    "v_lshlrev_b32 v122, 16, v115\nv_and_b32 v123, 0xffff0000, v115\ns_waitcnt lgkmcnt(6)\nv_mul_f32 v116, v116, v64\nv_mul_f32 v117, v117, v65\nv_mul_f32 v118, v118, v66\n" \
    "v_mul_f32 v119, v119, v67\nv_mul_f32 v120, v120, v68\nv_mul_f32 v121, v121, v69\nv_mul_f32 v122, v122, v70\nv_mul_f32 v123, v123, v71\n" \
    "v_mfma_f32_32x32x16_bf16 v[32:47], a[0:3], v[72:75], v[32:47]\nv_cvt_pk_bf16_f32 v132, v116, v117\nv_cvt_pk_bf16_f32 v133, v118, v119\nv_cvt_pk_bf16_f32 v134, v120, v121\n" \
    "v_cvt_pk_bf16_f32 v135, v122, v123\nv_accvgpr_write_b32 a0, v132\nv_accvgpr_write_b32 a1, v133\nv_accvgpr_write_b32 a2, v134\nv_accvgpr_write_b32 a3, v135\n" \
    "v_mfma_f32_32x32x16_bf16 v[16:31], v[132:135], v[128:131], 0\nds_read_b128 v[64:67], v12 offset:192\nds_read_b128 v[68:71], v12 offset:208\n" \
    "ds_read_b128 v[72:75], v13 offset:96\nv_accvgpr_read_b32 v112, a4\nv_accvgpr_read_b32 v113, a5\nv_accvgpr_read_b32 v114, a6\nv_accvgpr_read_b32 v115, a7\n" \
    "v_lshlrev_b32 v116, 16, v112\nv_and_b32 v117, 0xffff0000, v112\nv_lshlrev_b32 v118, 16, v113\nv_and_b32 v119, 0xffff0000, v113\nv_lshlrev_b32 v120, 16, v114\n" \
    "v_and_b32 v121, 0xffff0000, v114\nv_lshlrev_b32 v122, 16, v115\nv_and_b32 v123, 0xffff0000, v115\ns_waitcnt lgkmcnt(6)\nv_mul_f32 v116, v116, v80\n" \
    "v_mul_f32 v117, v117, v81\nv_mul_f32 v118, v118, v82\nv_mul_f32 v119, v119, v83\nv_mul_f32 v120, v120, v84\nv_mul_f32 v121, v121, v85\nv_mul_f32 v122, v122, v86\n" \
    "v_mul_f32 v123, v123, v87\nv_mfma_f32_32x32x16_bf16 v[32:47], a[4:7], v[88:91], v[32:47]\nv_cvt_pk_bf16_f32 v136, v116, v117\nv_cvt_pk_bf16_f32 v137, v118, v119\n" \
    "v_cvt_pk_bf16_f32 v138, v120, v121\nv_cvt_pk_bf16_f32 v139, v122, v123\nv_accvgpr_write_b32 a4, v136\nv_accvgpr_write_b32 a5, v137\nv_accvgpr_write_b32 a6, v138\n" \
    "v_accvgpr_write_b32 a7, v139\nv_mfma_f32_32x32x16_bf16 v[16:31], v[136:139], v[128:131], v[16:31]\nds_read_b128 v[80:83], v12 offset:256\n" \
    "ds_read_b128 v[84:87], v12 offset:272\nds_read_b128 v[88:91], v13 offset:128\nv_accvgpr_read_b32 v112, a8\nv_accvgpr_read_b32 v113, a9\nv_accvgpr_read_b32 v114, a10\n" \
    "v_accvgpr_read_b32 v115, a11\nv_lshlrev_b32 v116, 16, v112\nv_and_b32 v117, 0xffff0000, v112\nv_lshlrev_b32 v118, 16, v113\nv_and_b32 v119, 0xffff0000, v113\n" \
    "v_lshlrev_b32 v120, 16, v114\nv_and_b32 v121, 0xffff0000, v114\nv_lshlrev_b32 v122, 16, v115\nv_and_b32 v123, 0xffff0000, v115\ns_waitcnt lgkmcnt(6)\n" \
    "v_mul_f32 v116, v116, v96\nv_mul_f32 v117, v117, v97\nv_mul_f32 v118, v118, v98\nv_mul_f32 v119, v119, v99\nv_mul_f32 v120, v120, v100\nv_mul_f32 v121, v121, v101\n" \
    "v_mul_f32 v122, v122, v102\nv_mul_f32 v123, v123, v103\nv_mfma_f32_32x32x16_bf16 v[32:47], a[8:11], v[104:107], v[32:47]\nv_cvt_pk_bf16_f32 v132, v116, v117\n" \
    "v_cvt_pk_bf16_f32 v133, v118, v119\nv_cvt_pk_bf16_f32 v134, v120, v121\nv_cvt_pk_bf16_f32 v135, v122, v123\nv_accvgpr_write_b32 a8, v132\nv_accvgpr_write_b32 a9, v133\n" \
    "v_accvgpr_write_b32 a10, v134\nv_accvgpr_write_b32 a11, v135\nv_mfma_f32_32x32x16_bf16 v[16:31], v[132:135], v[128:131], v[16:31]\nds_read_b128 v[96:99], v12 offset:320\n" \
    "ds_read_b128 v[100:103], v12 offset:336\nds_read_b128 v[104:107], v13 offset:160\nv_accvgpr_read_b32 v112, a12\nv_accvgpr_read_b32 v113, a13\nv_accvgpr_read_b32 v114, a14\n" \
    "v_accvgpr_read_b32 v115, a15\nv_lshlrev_b32 v116, 16, v112\nv_and_b32 v117, 0xffff0000, v112\nv_lshlrev_b32 v118, 16, v113\nv_and_b32 v119, 0xffff0000, v113\n" \
    "v_lshlrev_b32 v120, 16, v114\nv_and_b32 v121, 0xffff0000, v114\nv_lshlrev_b32 v122, 16, v115\nv_and_b32 v123, 0xffff0000, v115\ns_waitcnt lgkmcnt(6)\n" \
    "v_mul_f32 v116, v116, v64\nv_mul_f32 v117, v117, v65\nv_mul_f32 v118, v118, v66\nv_mul_f32 v119, v119, v67\nv_mul_f32 v120, v120, v68\nv_mul_f32 v121, v121, v69\n" \
    "v_mul_f32 v122, v122, v70\nv_mul_f32 v123, v123, v71\nv_mfma_f32_32x32x16_bf16 v[32:47], a[12:15], v[72:75], v[32:47]\nv_cvt_pk_bf16_f32 v136, v116, v117\n" \
    "v_cvt_pk_bf16_f32 v137, v118, v119\nv_cvt_pk_bf16_f32 v138, v120, v121\nv_cvt_pk_bf16_f32 v139, v122, v123\nv_accvgpr_write_b32 a12, v136\nv_accvgpr_write_b32 a13, v137\n" \
    "v_accvgpr_write_b32 a14, v138\nv_accvgpr_write_b32 a15, v139\nv_mfma_f32_32x32x16_bf16 v[16:31], v[136:139], v[128:131], v[16:31]\nds_read_b128 v[64:67], v12 offset:384\n" \
    "ds_read_b128 v[68:71], v12 offset:400\nds_read_b128 v[72:75], v13 offset:192\nv_accvgpr_read_b32 v112, a16\nv_accvgpr_read_b32 v113, a17\nv_accvgpr_read_b32 v114, a18\n" \
    "v_accvgpr_read_b32 v115, a19\nv_lshlrev_b32 v116, 16, v112\nv_and_b32 v117, 0xffff0000, v112\nv_lshlrev_b32 v118, 16, v113\nv_and_b32 v119, 0xffff0000, v113\n" \
    "v_lshlrev_b32 v120, 16, v114\nv_and_b32 v121, 0xffff0000, v114\nv_lshlrev_b32 v122, 16, v115\nv_and_b32 v123, 0xffff0000, v115\ns_waitcnt lgkmcnt(6)\n" \
    "v_mul_f32 v116, v116, v80\nv_mul_f32 v117, v117, v81\nv_mul_f32 v118, v118, v82\nv_mul_f32 v119, v119, v83\nv_mul_f32 v120, v120, v84\nv_mul_f32 v121, v121, v85\n" \
    "v_mul_f32 v122, v122, v86\nv_mul_f32 v123, v123, v87\nv_mfma_f32_32x32x16_bf16 v[32:47], a[16:19], v[88:91], v[32:47]\nv_cvt_pk_bf16_f32 v132, v116, v117\n" \
    "v_cvt_pk_bf16_f32 v133, v118, v119\nv_cvt_pk_bf16_f32 v134, v120, v121\nv_cvt_pk_bf16_f32 v135, v122, v123\nv_accvgpr_write_b32 a16, v132\nv_accvgpr_write_b32 a17, v133\n" \
    "v_accvgpr_write_b32 a18, v134\nv_accvgpr_write_b32 a19, v135\nv_mfma_f32_32x32x16_bf16 v[16:31], v[132:135], v[128:131], v[16:31]\nds_read_b128 v[80:83], v12 offset:448\n" \
    "ds_read_b128 v[84:87], v12 offset:464\nds_read_b128 v[88:91], v13 offset:224\nv_accvgpr_read_b32 v112, a20\nv_accvgpr_read_b32 v113, a21\nv_accvgpr_read_b32 v114, a22\n" \
    "v_accvgpr_read_b32 v115, a23\nv_lshlrev_b32 v116, 16, v112\nv_and_b32 v117, 0xffff0000, v112\nv_lshlrev_b32 v118, 16, v113\nv_and_b32 v119, 0xffff0000, v113\n" \
    "v_lshlrev_b32 v120, 16, v114\nv_and_b32 v121, 0xffff0000, v114\nv_lshlrev_b32 v122, 16, v115\nv_and_b32 v123, 0xffff0000, v115\ns_waitcnt lgkmcnt(6)\n" \
    "v_mul_f32 v116, v116, v96\nv_mul_f32 v117, v117, v97\nv_mul_f32 v118, v118, v98\nv_mul_f32 v119, v119, v99\nv_mul_f32 v120, v120, v100\nv_mul_f32 v121, v121, v101\n" \
    "v_mul_f32 v122, v122, v102\nv_mul_f32 v123, v123, v103\nv_mfma_f32_32x32x16_bf16 v[32:47], a[20:23], v[104:107], v[32:47]\nv_cvt_pk_bf16_f32 v136, v116, v117\n" \
    "v_cvt_pk_bf16_f32 v137, v118, v119\nv_cvt_pk_bf16_f32 v138, v120, v121\nv_cvt_pk_bf16_f32 v139, v122, v123\nv_accvgpr_write_b32 a20, v136\nv_accvgpr_write_b32 a21, v137\n" \
    "v_accvgpr_write_b32 a22, v138\nv_accvgpr_write_b32 a23, v139\nv_mfma_f32_32x32x16_bf16 v[16:31], v[136:139], v[128:131], v[16:31]\nds_read_b128 v[96:99], v12 offset:512\n" \
    "ds_read_b128 v[100:103], v12 offset:528\nds_read_b128 v[104:107], v13 offset:256\nv_accvgpr_read_b32 v112, a24\nv_accvgpr_read_b32 v113, a25\nv_accvgpr_read_b32 v114, a26\n" \
    "v_accvgpr_read_b32 v115, a27\nv_lshlrev_b32 v116, 16, v112\nv_and_b32 v117, 0xffff0000, v112\nv_lshlrev_b32 v118, 16, v113\nv_and_b32 v119, 0xffff0000, v113\n" \
    "v_lshlrev_b32 v120, 16, v114\nv_and_b32 v121, 0xffff0000, v114\nv_lshlrev_b32 v122, 16, v115\nv_and_b32 v123, 0xffff0000, v115\ns_waitcnt lgkmcnt(6)\n" \
    "v_mul_f32 v116, v116, v64\nv_mul_f32 v117, v117, v65\nv_mul_f32 v118, v118, v66\nv_mul_f32 v119, v119, v67\nv_mul_f32 v120, v120, v68\nv_mul_f32 v121, v121, v69\n" \
    "v_mul_f32 v122, v122, v70\nv_mul_f32 v123, v123, v71\nv_mfma_f32_32x32x16_bf16 v[32:47], a[24:27], v[72:75], v[32:47]\nv_cvt_pk_bf16_f32 v132, v116, v117\n" \
    "v_cvt_pk_bf16_f32 v133, v118, v119\nv_cvt_pk_bf16_f32 v134, v120, v121\nv_cvt_pk_bf16_f32 v135, v122, v123\nv_accvgpr_write_b32 a24, v132\nv_accvgpr_write_b32 a25, v133\n" \
    "v_accvgpr_write_b32 a26, v134\nv_accvgpr_write_b32 a27, v135\nv_mfma_f32_32x32x16_bf16 v[16:31], v[132:135], v[128:131], v[16:31]\nds_read_b128 v[64:67], v12 offset:576\n" \
    "ds_read_b128 v[68:71], v12 offset:592\nds_read_b128 v[72:75], v13 offset:288\nv_accvgpr_read_b32 v112, a28\nv_accvgpr_read_b32 v113, a29\nv_accvgpr_read_b32 v114, a30\n" \
    "v_accvgpr_read_b32 v115, a31\nv_lshlrev_b32 v116, 16, v112\nv_and_b32 v117, 0xffff0000, v112\nv_lshlrev_b32 v118, 16, v113\nv_and_b32 v119, 0xffff0000, v113\n" \
    "v_lshlrev_b32 v120, 16, v114\nv_and_b32 v121, 0xffff0000, v114\nv_lshlrev_b32 v122, 16, v115\nv_and_b32 v123, 0xffff0000, v115\ns_waitcnt lgkmcnt(6)\n" \
    "v_mul_f32 v116, v116, v80\nv_mul_f32 v117, v117, v81\nv_mul_f32 v118, v118, v82\nv_mul_f32 v119, v119, v83\nv_mul_f32 v120, v120, v84\nv_mul_f32 v121, v121, v85\n" \
    "v_mul_f32 v122, v122, v86\nv_mul_f32 v123, v123, v87\nv_mfma_f32_32x32x16_bf16 v[32:47], a[28:31], v[88:91], v[32:47]\nv_cvt_pk_bf16_f32 v136, v116, v117\n" \
    "v_cvt_pk_bf16_f32 v137, v118, v119\nv_cvt_pk_bf16_f32 v138, v120, v121\nv_cvt_pk_bf16_f32 v139, v122, v123\nv_accvgpr_write_b32 a28, v136\nv_accvgpr_write_b32 a29, v137\n" \
    "v_accvgpr_write_b32 a30, v138\nv_accvgpr_write_b32 a31, v139\nv_mfma_f32_32x32x16_bf16 v[16:31], v[136:139], v[128:131], v[16:31]\nds_read_b128 v[80:83], v12 offset:640\n" \
    "ds_read_b128 v[84:87], v12 offset:656\nds_read_b128 v[88:91], v13 offset:320\nv_accvgpr_read_b32 v112, a32\nv_accvgpr_read_b32 v113, a33\nv_accvgpr_read_b32 v114, a34\n" \
    "v_accvgpr_read_b32 v115, a35\nv_lshlrev_b32 v116, 16, v112\nv_and_b32 v117, 0xffff0000, v112\nv_lshlrev_b32 v118, 16, v113\nv_and_b32 v119, 0xffff0000, v113\n" \
    "v_lshlrev_b32 v120, 16, v114\nv_and_b32 v121, 0xffff0000, v114\nv_lshlrev_b32 v122, 16, v115\nv_and_b32 v123, 0xffff0000, v115\ns_waitcnt lgkmcnt(6)\n" \
    "v_mul_f32 v116, v116, v96\nv_mul_f32 v117, v117, v97\nv_mul_f32 v118, v118, v98\nv_mul_f32 v119, v119, v99\nv_mul_f32 v120, v120, v100\nv_mul_f32 v121, v121, v101\n" \
    "v_mul_f32 v122, v122, v102\nv_mul_f32 v123, v123, v103\nv_mfma_f32_32x32x16_bf16 v[32:47], a[32:35], v[104:107], v[32:47]\nv_cvt_pk_bf16_f32 v132, v116, v117\n" \
    "v_cvt_pk_bf16_f32 v133, v118, v119\nv_cvt_pk_bf16_f32 v134, v120, v121\nv_cvt_pk_bf16_f32 v135, v122, v123\nv_accvgpr_write_b32 a32, v132\nv_accvgpr_write_b32 a33, v133\n" \
    "v_accvgpr_write_b32 a34, v134\nv_accvgpr_write_b32 a35, v135\nv_mfma_f32_32x32x16_bf16 v[16:31], v[132:135], v[128:131], v[16:31]\nds_read_b128 v[96:99], v12 offset:704\n" \
    "ds_read_b128 v[100:103], v12 offset:720\nds_read_b128 v[104:107], v13 offset:352\nv_accvgpr_read_b32 v112, a36\nv_accvgpr_read_b32 v113, a37\nv_accvgpr_read_b32 v114, a38\n" \
    "v_accvgpr_read_b32 v115, a39\nv_lshlrev_b32 v116, 16, v112\nv_and_b32 v117, 0xffff0000, v112\nv_lshlrev_b32 v118, 16, v113\nv_and_b32 v119, 0xffff0000, v113\n" \
    "v_lshlrev_b32 v120, 16, v114\nv_and_b32 v121, 0xffff0000, v114\nv_lshlrev_b32 v122, 16, v115\nv_and_b32 v123, 0xffff0000, v115\ns_waitcnt lgkmcnt(6)\n" \
    "v_mul_f32 v116, v116, v64\nv_mul_f32 v117, v117, v65\nv_mul_f32 v118, v118, v66\nv_mul_f32 v119, v119, v67\nv_mul_f32 v120, v120, v68\nv_mul_f32 v121, v121, v69\n" \
    "v_mul_f32 v122, v122, v70\nv_mul_f32 v123, v123, v71\nv_mfma_f32_32x32x16_bf16 v[32:47], a[36:39], v[72:75], v[32:47]\nv_cvt_pk_bf16_f32 v136, v116, v117\n" \
    "v_cvt_pk_bf16_f32 v137, v118, v119\nv_cvt_pk_bf16_f32 v138, v120, v121\nv_cvt_pk_bf16_f32 v139, v122, v123\nv_accvgpr_write_b32 a36, v136\nv_accvgpr_write_b32 a37, v137\n" \
    "v_accvgpr_write_b32 a38, v138\nv_accvgpr_write_b32 a39, v139\nv_mfma_f32_32x32x16_bf16 v[16:31], v[136:139], v[128:131], v[16:31]\nds_read_b128 v[64:67], v12 offset:768\n" \
    "ds_read_b128 v[68:71], v12 offset:784\nds_read_b128 v[72:75], v13 offset:384\nv_accvgpr_read_b32 v112, a40\nv_accvgpr_read_b32 v113, a41\nv_accvgpr_read_b32 v114, a42\n" \
    "v_accvgpr_read_b32 v115, a43\nv_lshlrev_b32 v116, 16, v112\nv_and_b32 v117, 0xffff0000, v112\nv_lshlrev_b32 v118, 16, v113\nv_and_b32 v119, 0xffff0000, v113\n" \
    "v_lshlrev_b32 v120, 16, v114\nv_and_b32 v121, 0xffff0000, v114\nv_lshlrev_b32 v122, 16, v115\nv_and_b32 v123, 0xffff0000, v115\ns_waitcnt lgkmcnt(6)\n" \
    "v_mul_f32 v116, v116, v80\nv_mul_f32 v117, v117, v81\nv_mul_f32 v118, v118, v82\nv_mul_f32 v119, v119, v83\nv_mul_f32 v120, v120, v84\nv_mul_f32 v121, v121, v85\n" \
    "v_mul_f32 v122, v122, v86\nv_mul_f32 v123, v123, v87\nv_mfma_f32_32x32x16_bf16 v[32:47], a[40:43], v[88:91], v[32:47]\nv_cvt_pk_bf16_f32 v132, v116, v117\n" \
    "v_cvt_pk_bf16_f32 v133, v118, v119\nv_cvt_pk_bf16_f32 v134, v120, v121\nv_cvt_pk_bf16_f32 v135, v122, v123\nv_accvgpr_write_b32 a40, v132\nv_accvgpr_write_b32 a41, v133\n" \
    "v_accvgpr_write_b32 a42, v134\nv_accvgpr_write_b32 a43, v135\nv_mfma_f32_32x32x16_bf16 v[16:31], v[132:135], v[128:131], v[16:31]\nds_read_b128 v[80:83], v12 offset:832\n" \
    "ds_read_b128 v[84:87], v12 offset:848\nds_read_b128 v[88:91], v13 offset:416\nv_accvgpr_read_b32 v112, a44\nv_accvgpr_read_b32 v113, a45\nv_accvgpr_read_b32 v114, a46\n" \
    "v_accvgpr_read_b32 v115, a47\nv_lshlrev_b32 v116, 16, v112\nv_and_b32 v117, 0xffff0000, v112\nv_lshlrev_b32 v118, 16, v113\nv_and_b32 v119, 0xffff0000, v113\n" \
    "v_lshlrev_b32 v120, 16, v114\nv_and_b32 v121, 0xffff0000, v114\nv_lshlrev_b32 v122, 16, v115\nv_and_b32 v123, 0xffff0000, v115\ns_waitcnt lgkmcnt(6)\n" \
    "v_mul_f32 v116, v116, v96\nv_mul_f32 v117, v117, v97\nv_mul_f32 v118, v118, v98\nv_mul_f32 v119, v119, v99\nv_mul_f32 v120, v120, v100\nv_mul_f32 v121, v121, v101\n" \
    "v_mul_f32 v122, v122, v102\nv_mul_f32 v123, v123, v103\nv_mfma_f32_32x32x16_bf16 v[32:47], a[44:47], v[104:107], v[32:47]\nv_cvt_pk_bf16_f32 v136, v116, v117\n" \
    "v_cvt_pk_bf16_f32 v137, v118, v119\nv_cvt_pk_bf16_f32 v138, v120, v121\nv_cvt_pk_bf16_f32 v139, v122, v123\nv_accvgpr_write_b32 a44, v136\nv_accvgpr_write_b32 a45, v137\n" \
    "v_accvgpr_write_b32 a46, v138\nv_accvgpr_write_b32 a47, v139\nv_mfma_f32_32x32x16_bf16 v[16:31], v[136:139], v[128:131], v[16:31]\nds_read_b128 v[96:99], v12 offset:896\n" \
    "ds_read_b128 v[100:103], v12 offset:912\nds_read_b128 v[104:107], v13 offset:448\nv_accvgpr_read_b32 v112, a48\nv_accvgpr_read_b32 v113, a49\nv_accvgpr_read_b32 v114, a50\n" \
    "v_accvgpr_read_b32 v115, a51\nv_lshlrev_b32 v116, 16, v112\nv_and_b32 v117, 0xffff0000, v112\nv_lshlrev_b32 v118, 16, v113\nv_and_b32 v119, 0xffff0000, v113\n" \
    "v_lshlrev_b32 v120, 16, v114\nv_and_b32 v121, 0xffff0000, v114\nv_lshlrev_b32 v122, 16, v115\nv_and_b32 v123, 0xffff0000, v115\ns_waitcnt lgkmcnt(6)\n" \
    "v_mul_f32 v116, v116, v64\nv_mul_f32 v117, v117, v65\nv_mul_f32 v118, v118, v66\nv_mul_f32 v119, v119, v67\nv_mul_f32 v120, v120, v68\nv_mul_f32 v121, v121, v69\n" \
    "v_mul_f32 v122, v122, v70\nv_mul_f32 v123, v123, v71\nv_mfma_f32_32x32x16_bf16 v[32:47], a[48:51], v[72:75], v[32:47]\nv_cvt_pk_bf16_f32 v132, v116, v117\n" \
    "v_cvt_pk_bf16_f32 v133, v118, v119\nv_cvt_pk_bf16_f32 v134, v120, v121\nv_cvt_pk_bf16_f32 v135, v122, v123\nv_accvgpr_write_b32 a48, v132\nv_accvgpr_write_b32 a49, v133\n" \
    "v_accvgpr_write_b32 a50, v134\nv_accvgpr_write_b32 a51, v135\nv_mfma_f32_32x32x16_bf16 v[16:31], v[132:135], v[128:131], v[16:31]\nds_read_b128 v[64:67], v12 offset:960\n" \
    "ds_read_b128 v[68:71], v12 offset:976\nds_read_b128 v[72:75], v13 offset:480\nv_accvgpr_read_b32 v112, a52\nv_accvgpr_read_b32 v113, a53\nv_accvgpr_read_b32 v114, a54\n" \
    "v_accvgpr_read_b32 v115, a55\nv_lshlrev_b32 v116, 16, v112\nv_and_b32 v117, 0xffff0000, v112\nv_lshlrev_b32 v118, 16, v113\nv_and_b32 v119, 0xffff0000, v113\n" \
    "v_lshlrev_b32 v120, 16, v114\nv_and_b32 v121, 0xffff0000, v114\nv_lshlrev_b32 v122, 16, v115\nv_and_b32 v123, 0xffff0000, v115\ns_waitcnt lgkmcnt(6)\n" \
    "v_mul_f32 v116, v116, v80\nv_mul_f32 v117, v117, v81\nv_mul_f32 v118, v118, v82\nv_mul_f32 v119, v119, v83\nv_mul_f32 v120, v120, v84\nv_mul_f32 v121, v121, v85\n" \
    "v_mul_f32 v122, v122, v86\nv_mul_f32 v123, v123, v87\nv_mfma_f32_32x32x16_bf16 v[32:47], a[52:55], v[88:91], v[32:47]\nv_cvt_pk_bf16_f32 v136, v116, v117\n" \
    "v_cvt_pk_bf16_f32 v137, v118, v119\nv_cvt_pk_bf16_f32 v138, v120, v121\nv_cvt_pk_bf16_f32 v139, v122, v123\nv_accvgpr_write_b32 a52, v136\nv_accvgpr_write_b32 a53, v137\n" \
    "v_accvgpr_write_b32 a54, v138\nv_accvgpr_write_b32 a55, v139\nv_mfma_f32_32x32x16_bf16 v[16:31], v[136:139], v[128:131], v[16:31]\nds_read_b128 v[80:83], v12 offset:1024\n" \
    "ds_read_b128 v[84:87], v12 offset:1040\nds_read_b128 v[88:91], v13 offset:512\nv_accvgpr_read_b32 v112, a56\nv_accvgpr_read_b32 v113, a57\nv_accvgpr_read_b32 v114, a58\n" \
    "v_accvgpr_read_b32 v115, a59\nv_lshlrev_b32 v116, 16, v112\nv_and_b32 v117, 0xffff0000, v112\nv_lshlrev_b32 v118, 16, v113\nv_and_b32 v119, 0xffff0000, v113\n" \
    "v_lshlrev_b32 v120, 16, v114\nv_and_b32 v121, 0xffff0000, v114\nv_lshlrev_b32 v122, 16, v115\nv_and_b32 v123, 0xffff0000, v115\ns_waitcnt lgkmcnt(6)\n" \
    "v_mul_f32 v116, v116, v96\nv_mul_f32 v117, v117, v97\nv_mul_f32 v118, v118, v98\nv_mul_f32 v119, v119, v99\nv_mul_f32 v120, v120, v100\nv_mul_f32 v121, v121, v101\n" \
    "v_mul_f32 v122, v122, v102\nv_mul_f32 v123, v123, v103\nv_mfma_f32_32x32x16_bf16 v[32:47], a[56:59], v[104:107], v[32:47]\nv_cvt_pk_bf16_f32 v132, v116, v117\n" \
    "v_cvt_pk_bf16_f32 v133, v118, v119\nv_cvt_pk_bf16_f32 v134, v120, v121\nv_cvt_pk_bf16_f32 v135, v122, v123\nv_accvgpr_write_b32 a56, v132\nv_accvgpr_write_b32 a57, v133\n" \
    "v_accvgpr_write_b32 a58, v134\nv_accvgpr_write_b32 a59, v135\nv_mfma_f32_32x32x16_bf16 v[16:31], v[132:135], v[128:131], v[16:31]\nds_read_b128 v[96:99], v12 offset:1088\n" \
    "ds_read_b128 v[100:103], v12 offset:1104\nds_read_b128 v[104:107], v13 offset:544\nv_accvgpr_read_b32 v112, a60\nv_accvgpr_read_b32 v113, a61\n" \
    "v_accvgpr_read_b32 v114, a62\nv_accvgpr_read_b32 v115, a63\nv_lshlrev_b32 v116, 16, v112\nv_and_b32 v117, 0xffff0000, v112\nv_lshlrev_b32 v118, 16, v113\n" \
    "v_and_b32 v119, 0xffff0000, v113\nv_lshlrev_b32 v120, 16, v114\nv_and_b32 v121, 0xffff0000, v114\nv_lshlrev_b32 v122, 16, v115\nv_and_b32 v123, 0xffff0000, v115\n" \
    "s_waitcnt lgkmcnt(6)\nv_mul_f32 v116, v116, v64\nv_mul_f32 v117, v117, v65\nv_mul_f32 v118, v118, v66\nv_mul_f32 v119, v119, v67\nv_mul_f32 v120, v120, v68\n" \
    "v_mul_f32 v121, v121, v69\nv_mul_f32 v122, v122, v70\nv_mul_f32 v123, v123, v71\nv_mfma_f32_32x32x16_bf16 v[32:47], a[60:63], v[72:75], v[32:47]\n" \
    "v_cvt_pk_bf16_f32 v136, v116, v117\nv_cvt_pk_bf16_f32 v137, v118, v119\nv_cvt_pk_bf16_f32 v138, v120, v121\nv_cvt_pk_bf16_f32 v139, v122, v123\n" \
    "v_accvgpr_write_b32 a60, v136\nv_accvgpr_write_b32 a61, v137\nv_accvgpr_write_b32 a62, v138\nv_accvgpr_write_b32 a63, v139\n" \
    "v_mfma_f32_32x32x16_bf16 v[16:31], v[136:139], v[128:131], v[16:31]\nds_read_b128 v[64:67], v12 offset:1152\nds_read_b128 v[68:71], v12 offset:1168\n" \
    "ds_read_b128 v[72:75], v13 offset:576\nv_accvgpr_read_b32 v112, a64\nv_accvgpr_read_b32 v113, a65\nv_accvgpr_read_b32 v114, a66\nv_accvgpr_read_b32 v115, a67\n" \
    "v_lshlrev_b32 v116, 16, v112\nv_and_b32 v117, 0xffff0000, v112\nv_lshlrev_b32 v118, 16, v113\nv_and_b32 v119, 0xffff0000, v113\nv_lshlrev_b32 v120, 16, v114\n" \
    "v_and_b32 v121, 0xffff0000, v114\nv_lshlrev_b32 v122, 16, v115\nv_and_b32 v123, 0xffff0000, v115\ns_waitcnt lgkmcnt(6)\nv_mul_f32 v116, v116, v80\n" \
    "v_mul_f32 v117, v117, v81\nv_mul_f32 v118, v118, v82\nv_mul_f32 v119, v119, v83\nv_mul_f32 v120, v120, v84\nv_mul_f32 v121, v121, v85\nv_mul_f32 v122, v122, v86\n" \
    "v_mul_f32 v123, v123, v87\nv_mfma_f32_32x32x16_bf16 v[32:47], a[64:67], v[88:91], v[32:47]\nv_cvt_pk_bf16_f32 v132, v116, v117\nv_cvt_pk_bf16_f32 v133, v118, v119\n" \
    "v_cvt_pk_bf16_f32 v134, v120, v121\nv_cvt_pk_bf16_f32 v135, v122, v123\nv_accvgpr_write_b32 a64, v132\nv_accvgpr_write_b32 a65, v133\nv_accvgpr_write_b32 a66, v134\n" \
    "v_accvgpr_write_b32 a67, v135\nv_mfma_f32_32x32x16_bf16 v[16:31], v[132:135], v[128:131], v[16:31]\nds_read_b128 v[80:83], v12 offset:1216\n" \
    "ds_read_b128 v[84:87], v12 offset:1232\nds_read_b128 v[88:91], v13 offset:608\nv_accvgpr_read_b32 v112, a68\nv_accvgpr_read_b32 v113, a69\nv_accvgpr_read_b32 v114, a70\n" \
    "v_accvgpr_read_b32 v115, a71\nv_lshlrev_b32 v116, 16, v112\nv_and_b32 v117, 0xffff0000, v112\nv_lshlrev_b32 v118, 16, v113\nv_and_b32 v119, 0xffff0000, v113\n" \
    "v_lshlrev_b32 v120, 16, v114\nv_and_b32 v121, 0xffff0000, v114\nv_lshlrev_b32 v122, 16, v115\nv_and_b32 v123, 0xffff0000, v115\ns_waitcnt lgkmcnt(6)\n" \
    "v_mul_f32 v116, v116, v96\nv_mul_f32 v117, v117, v97\nv_mul_f32 v118, v118, v98\nv_mul_f32 v119, v119, v99\nv_mul_f32 v120, v120, v100\nv_mul_f32 v121, v121, v101\n" \
    "v_mul_f32 v122, v122, v102\nv_mul_f32 v123, v123, v103\nv_mfma_f32_32x32x16_bf16 v[32:47], a[68:71], v[104:107], v[32:47]\nv_cvt_pk_bf16_f32 v136, v116, v117\n" \
    "v_cvt_pk_bf16_f32 v137, v118, v119\nv_cvt_pk_bf16_f32 v138, v120, v121\nv_cvt_pk_bf16_f32 v139, v122, v123\nv_accvgpr_write_b32 a68, v136\nv_accvgpr_write_b32 a69, v137\n" \
    "v_accvgpr_write_b32 a70, v138\nv_accvgpr_write_b32 a71, v139\nv_mfma_f32_32x32x16_bf16 v[16:31], v[136:139], v[128:131], v[16:31]\nds_read_b128 v[96:99], v12 offset:1280\n" \
    "ds_read_b128 v[100:103], v12 offset:1296\nds_read_b128 v[104:107], v13 offset:640\nv_accvgpr_read_b32 v112, a72\nv_accvgpr_read_b32 v113, a73\n" \
    "v_accvgpr_read_b32 v114, a74\nv_accvgpr_read_b32 v115, a75\nv_lshlrev_b32 v116, 16, v112\nv_and_b32 v117, 0xffff0000, v112\nv_lshlrev_b32 v118, 16, v113\n" \
    "v_and_b32 v119, 0xffff0000, v113\nv_lshlrev_b32 v120, 16, v114\nv_and_b32 v121, 0xffff0000, v114\nv_lshlrev_b32 v122, 16, v115\nv_and_b32 v123, 0xffff0000, v115\n" \
    "s_waitcnt lgkmcnt(6)\nv_mul_f32 v116, v116, v64\nv_mul_f32 v117, v117, v65\nv_mul_f32 v118, v118, v66\nv_mul_f32 v119, v119, v67\nv_mul_f32 v120, v120, v68\n" \
    "v_mul_f32 v121, v121, v69\nv_mul_f32 v122, v122, v70\nv_mul_f32 v123, v123, v71\nv_mfma_f32_32x32x16_bf16 v[32:47], a[72:75], v[72:75], v[32:47]\n" \
    "v_cvt_pk_bf16_f32 v132, v116, v117\nv_cvt_pk_bf16_f32 v133, v118, v119\nv_cvt_pk_bf16_f32 v134, v120, v121\nv_cvt_pk_bf16_f32 v135, v122, v123\n" \
    "v_accvgpr_write_b32 a72, v132\nv_accvgpr_write_b32 a73, v133\nv_accvgpr_write_b32 a74, v134\nv_accvgpr_write_b32 a75, v135\n" \
    "v_mfma_f32_32x32x16_bf16 v[16:31], v[132:135], v[128:131], v[16:31]\nds_read_b128 v[64:67], v12 offset:1344\nds_read_b128 v[68:71], v12 offset:1360\n" \
    "ds_read_b128 v[72:75], v13 offset:672\nv_accvgpr_read_b32 v112, a76\nv_accvgpr_read_b32 v113, a77\nv_accvgpr_read_b32 v114, a78\nv_accvgpr_read_b32 v115, a79\n" \
    "v_lshlrev_b32 v116, 16, v112\nv_and_b32 v117, 0xffff0000, v112\nv_lshlrev_b32 v118, 16, v113\nv_and_b32 v119, 0xffff0000, v113\nv_lshlrev_b32 v120, 16, v114\n" \
    "v_and_b32 v121, 0xffff0000, v114\nv_lshlrev_b32 v122, 16, v115\nv_and_b32 v123, 0xffff0000, v115\ns_waitcnt lgkmcnt(6)\nv_mul_f32 v116, v116, v80\n" \
    "v_mul_f32 v117, v117, v81\nv_mul_f32 v118, v118, v82\nv_mul_f32 v119, v119, v83\nv_mul_f32 v120, v120, v84\nv_mul_f32 v121, v121, v85\nv_mul_f32 v122, v122, v86\n" \
    "v_mul_f32 v123, v123, v87\nv_mfma_f32_32x32x16_bf16 v[32:47], a[76:79], v[88:91], v[32:47]\nv_cvt_pk_bf16_f32 v136, v116, v117\nv_cvt_pk_bf16_f32 v137, v118, v119\n" \
    "v_cvt_pk_bf16_f32 v138, v120, v121\nv_cvt_pk_bf16_f32 v139, v122, v123\nv_accvgpr_write_b32 a76, v136\nv_accvgpr_write_b32 a77, v137\nv_accvgpr_write_b32 a78, v138\n" \
    "v_accvgpr_write_b32 a79, v139\nv_mfma_f32_32x32x16_bf16 v[16:31], v[136:139], v[128:131], v[16:31]\nds_read_b128 v[80:83], v12 offset:1408\n" \
    "ds_read_b128 v[84:87], v12 offset:1424\nds_read_b128 v[88:91], v13 offset:704\nv_accvgpr_read_b32 v112, a80\nv_accvgpr_read_b32 v113, a81\nv_accvgpr_read_b32 v114, a82\n" \
    "v_accvgpr_read_b32 v115, a83\nv_lshlrev_b32 v116, 16, v112\nv_and_b32 v117, 0xffff0000, v112\nv_lshlrev_b32 v118, 16, v113\nv_and_b32 v119, 0xffff0000, v113\n" \
    "v_lshlrev_b32 v120, 16, v114\nv_and_b32 v121, 0xffff0000, v114\nv_lshlrev_b32 v122, 16, v115\nv_and_b32 v123, 0xffff0000, v115\ns_waitcnt lgkmcnt(6)\n" \
    "v_mul_f32 v116, v116, v96\nv_mul_f32 v117, v117, v97\nv_mul_f32 v118, v118, v98\nv_mul_f32 v119, v119, v99\nv_mul_f32 v120, v120, v100\nv_mul_f32 v121, v121, v101\n" \
    "v_mul_f32 v122, v122, v102\nv_mul_f32 v123, v123, v103\nv_mfma_f32_32x32x16_bf16 v[32:47], a[80:83], v[104:107], v[32:47]\nv_cvt_pk_bf16_f32 v132, v116, v117\n" \
    "v_cvt_pk_bf16_f32 v133, v118, v119\nv_cvt_pk_bf16_f32 v134, v120, v121\nv_cvt_pk_bf16_f32 v135, v122, v123\nv_accvgpr_write_b32 a80, v132\nv_accvgpr_write_b32 a81, v133\n" \
    "v_accvgpr_write_b32 a82, v134\nv_accvgpr_write_b32 a83, v135\nv_mfma_f32_32x32x16_bf16 v[16:31], v[132:135], v[128:131], v[16:31]\nds_read_b128 v[96:99], v12 offset:1472\n" \
    "ds_read_b128 v[100:103], v12 offset:1488\nds_read_b128 v[104:107], v13 offset:736\nv_accvgpr_read_b32 v112, a84\nv_accvgpr_read_b32 v113, a85\n" \
    "v_accvgpr_read_b32 v114, a86\nv_accvgpr_read_b32 v115, a87\nv_lshlrev_b32 v116, 16, v112\nv_and_b32 v117, 0xffff0000, v112\nv_lshlrev_b32 v118, 16, v113\n" \
    "v_and_b32 v119, 0xffff0000, v113\nv_lshlrev_b32 v120, 16, v114\nv_and_b32 v121, 0xffff0000, v114\nv_lshlrev_b32 v122, 16, v115\nv_and_b32 v123, 0xffff0000, v115\n" \
    "s_waitcnt lgkmcnt(6)\nv_mul_f32 v116, v116, v64\nv_mul_f32 v117, v117, v65\nv_mul_f32 v118, v118, v66\nv_mul_f32 v119, v119, v67\nv_mul_f32 v120, v120, v68\n" \
    "v_mul_f32 v121, v121, v69\nv_mul_f32 v122, v122, v70\nv_mul_f32 v123, v123, v71\nv_mfma_f32_32x32x16_bf16 v[32:47], a[84:87], v[72:75], v[32:47]\n" \
    "v_cvt_pk_bf16_f32 v136, v116, v117\nv_cvt_pk_bf16_f32 v137, v118, v119\nv_cvt_pk_bf16_f32 v138, v120, v121\nv_cvt_pk_bf16_f32 v139, v122, v123\n" \
    "v_accvgpr_write_b32 a84, v136\nv_accvgpr_write_b32 a85, v137\nv_accvgpr_write_b32 a86, v138\nv_accvgpr_write_b32 a87, v139\n" \
    "v_mfma_f32_32x32x16_bf16 v[16:31], v[136:139], v[128:131], v[16:31]\nds_read_b128 v[64:67], v12 offset:1536\nds_read_b128 v[68:71], v12 offset:1552\n" \
    "ds_read_b128 v[72:75], v13 offset:768\nv_accvgpr_read_b32 v112, a88\nv_accvgpr_read_b32 v113, a89\nv_accvgpr_read_b32 v114, a90\nv_accvgpr_read_b32 v115, a91\n" \
    "v_lshlrev_b32 v116, 16, v112\nv_and_b32 v117, 0xffff0000, v112\nv_lshlrev_b32 v118, 16, v113\nv_and_b32 v119, 0xffff0000, v113\nv_lshlrev_b32 v120, 16, v114\n" \
    "v_and_b32 v121, 0xffff0000, v114\nv_lshlrev_b32 v122, 16, v115\nv_and_b32 v123, 0xffff0000, v115\ns_waitcnt lgkmcnt(6)\nv_mul_f32 v116, v116, v80\n" \
    "v_mul_f32 v117, v117, v81\nv_mul_f32 v118, v118, v82\nv_mul_f32 v119, v119, v83\nv_mul_f32 v120, v120, v84\nv_mul_f32 v121, v121, v85\nv_mul_f32 v122, v122, v86\n" \
    "v_mul_f32 v123, v123, v87\nv_mfma_f32_32x32x16_bf16 v[32:47], a[88:91], v[88:91], v[32:47]\nv_cvt_pk_bf16_f32 v132, v116, v117\nv_cvt_pk_bf16_f32 v133, v118, v119\n" \
    "v_cvt_pk_bf16_f32 v134, v120, v121\nv_cvt_pk_bf16_f32 v135, v122, v123\nv_accvgpr_write_b32 a88, v132\nv_accvgpr_write_b32 a89, v133\nv_accvgpr_write_b32 a90, v134\n" \
    "v_accvgpr_write_b32 a91, v135\nv_mfma_f32_32x32x16_bf16 v[16:31], v[132:135], v[128:131], v[16:31]\nds_read_b128 v[80:83], v12 offset:1600\n" \
    "ds_read_b128 v[84:87], v12 offset:1616\nds_read_b128 v[88:91], v13 offset:800\nv_accvgpr_read_b32 v112, a92\nv_accvgpr_read_b32 v113, a93\nv_accvgpr_read_b32 v114, a94\n" \
    "v_accvgpr_read_b32 v115, a95\nv_lshlrev_b32 v116, 16, v112\nv_and_b32 v117, 0xffff0000, v112\nv_lshlrev_b32 v118, 16, v113\nv_and_b32 v119, 0xffff0000, v113\n" \
    "v_lshlrev_b32 v120, 16, v114\nv_and_b32 v121, 0xffff0000, v114\nv_lshlrev_b32 v122, 16, v115\nv_and_b32 v123, 0xffff0000, v115\ns_waitcnt lgkmcnt(6)\n" \
    "v_mul_f32 v116, v116, v96\nv_mul_f32 v117, v117, v97\nv_mul_f32 v118, v118, v98\nv_mul_f32 v119, v119, v99\nv_mul_f32 v120, v120, v100\nv_mul_f32 v121, v121, v101\n" \
    "v_mul_f32 v122, v122, v102\nv_mul_f32 v123, v123, v103\nv_mfma_f32_32x32x16_bf16 v[32:47], a[92:95], v[104:107], v[32:47]\nv_cvt_pk_bf16_f32 v136, v116, v117\n" \
    "v_cvt_pk_bf16_f32 v137, v118, v119\nv_cvt_pk_bf16_f32 v138, v120, v121\nv_cvt_pk_bf16_f32 v139, v122, v123\nv_accvgpr_write_b32 a92, v136\nv_accvgpr_write_b32 a93, v137\n" \
    "v_accvgpr_write_b32 a94, v138\nv_accvgpr_write_b32 a95, v139\nv_mfma_f32_32x32x16_bf16 v[16:31], v[136:139], v[128:131], v[16:31]\nds_read_b128 v[96:99], v12 offset:1664\n" \
    "ds_read_b128 v[100:103], v12 offset:1680\nds_read_b128 v[104:107], v13 offset:832\nv_accvgpr_read_b32 v112, a96\nv_accvgpr_read_b32 v113, a97\n" \
    "v_accvgpr_read_b32 v114, a98\nv_accvgpr_read_b32 v115, a99\nv_lshlrev_b32 v116, 16, v112\nv_and_b32 v117, 0xffff0000, v112\nv_lshlrev_b32 v118, 16, v113\n" \
    "v_and_b32 v119, 0xffff0000, v113\nv_lshlrev_b32 v120, 16, v114\nv_and_b32 v121, 0xffff0000, v114\nv_lshlrev_b32 v122, 16, v115\nv_and_b32 v123, 0xffff0000, v115\n" \
    "s_waitcnt lgkmcnt(6)\nv_mul_f32 v116, v116, v64\nv_mul_f32 v117, v117, v65\nv_mul_f32 v118, v118, v66\nv_mul_f32 v119, v119, v67\nv_mul_f32 v120, v120, v68\n" \
    "v_mul_f32 v121, v121, v69\nv_mul_f32 v122, v122, v70\nv_mul_f32 v123, v123, v71\nv_mfma_f32_32x32x16_bf16 v[32:47], a[96:99], v[72:75], v[32:47]\n" \
    "v_cvt_pk_bf16_f32 v132, v116, v117\nv_cvt_pk_bf16_f32 v133, v118, v119\nv_cvt_pk_bf16_f32 v134, v120, v121\nv_cvt_pk_bf16_f32 v135, v122, v123\n" \
    "v_accvgpr_write_b32 a96, v132\nv_accvgpr_write_b32 a97, v133\nv_accvgpr_write_b32 a98, v134\nv_accvgpr_write_b32 a99, v135\n" \
    "v_mfma_f32_32x32x16_bf16 v[16:31], v[132:135], v[128:131], v[16:31]\nds_read_b128 v[64:67], v12 offset:1728\nds_read_b128 v[68:71], v12 offset:1744\n" \
    "ds_read_b128 v[72:75], v13 offset:864\nv_accvgpr_read_b32 v112, a100\nv_accvgpr_read_b32 v113, a101\nv_accvgpr_read_b32 v114, a102\nv_accvgpr_read_b32 v115, a103\n" \
    "v_lshlrev_b32 v116, 16, v112\nv_and_b32 v117, 0xffff0000, v112\nv_lshlrev_b32 v118, 16, v113\nv_and_b32 v119, 0xffff0000, v113\nv_lshlrev_b32 v120, 16, v114\n" \
    "v_and_b32 v121, 0xffff0000, v114\nv_lshlrev_b32 v122, 16, v115\nv_and_b32 v123, 0xffff0000, v115\ns_waitcnt lgkmcnt(6)\nv_mul_f32 v116, v116, v80\n" \
    "v_mul_f32 v117, v117, v81\nv_mul_f32 v118, v118, v82\nv_mul_f32 v119, v119, v83\nv_mul_f32 v120, v120, v84\nv_mul_f32 v121, v121, v85\nv_mul_f32 v122, v122, v86\n" \
    "v_mul_f32 v123, v123, v87\nv_mfma_f32_32x32x16_bf16 v[32:47], a[100:103], v[88:91], v[32:47]\nv_cvt_pk_bf16_f32 v136, v116, v117\nv_cvt_pk_bf16_f32 v137, v118, v119\n" \
    "v_cvt_pk_bf16_f32 v138, v120, v121\nv_cvt_pk_bf16_f32 v139, v122, v123\nv_accvgpr_write_b32 a100, v136\nv_accvgpr_write_b32 a101, v137\nv_accvgpr_write_b32 a102, v138\n" \
    "v_accvgpr_write_b32 a103, v139\nv_mfma_f32_32x32x16_bf16 v[16:31], v[136:139], v[128:131], v[16:31]\nds_read_b128 v[80:83], v12 offset:1792\n" \
    "ds_read_b128 v[84:87], v12 offset:1808\nds_read_b128 v[88:91], v13 offset:896\nv_accvgpr_read_b32 v112, a104\nv_accvgpr_read_b32 v113, a105\nv_accvgpr_read_b32 v114, a106\n" \
    "v_accvgpr_read_b32 v115, a107\nv_lshlrev_b32 v116, 16, v112\nv_and_b32 v117, 0xffff0000, v112\nv_lshlrev_b32 v118, 16, v113\nv_and_b32 v119, 0xffff0000, v113\n" \
    "v_lshlrev_b32 v120, 16, v114\nv_and_b32 v121, 0xffff0000, v114\nv_lshlrev_b32 v122, 16, v115\nv_and_b32 v123, 0xffff0000, v115\ns_waitcnt lgkmcnt(6)\n" \
    "v_mul_f32 v116, v116, v96\nv_mul_f32 v117, v117, v97\nv_mul_f32 v118, v118, v98\nv_mul_f32 v119, v119, v99\nv_mul_f32 v120, v120, v100\nv_mul_f32 v121, v121, v101\n" \
    "v_mul_f32 v122, v122, v102\nv_mul_f32 v123, v123, v103\nv_mfma_f32_32x32x16_bf16 v[32:47], a[104:107], v[104:107], v[32:47]\nv_cvt_pk_bf16_f32 v132, v116, v117\n" \
    "v_cvt_pk_bf16_f32 v133, v118, v119\nv_cvt_pk_bf16_f32 v134, v120, v121\nv_cvt_pk_bf16_f32 v135, v122, v123\nv_accvgpr_write_b32 a104, v132\nv_accvgpr_write_b32 a105, v133\n" \
    "v_accvgpr_write_b32 a106, v134\nv_accvgpr_write_b32 a107, v135\nv_mfma_f32_32x32x16_bf16 v[16:31], v[132:135], v[128:131], v[16:31]\n" \
    "ds_read_b128 v[96:99], v12 offset:1856\nds_read_b128 v[100:103], v12 offset:1872\nds_read_b128 v[104:107], v13 offset:928\nv_accvgpr_read_b32 v112, a108\n" \
    "v_accvgpr_read_b32 v113, a109\nv_accvgpr_read_b32 v114, a110\nv_accvgpr_read_b32 v115, a111\nv_lshlrev_b32 v116, 16, v112\nv_and_b32 v117, 0xffff0000, v112\n" \
    "v_lshlrev_b32 v118, 16, v113\nv_and_b32 v119, 0xffff0000, v113\nv_lshlrev_b32 v120, 16, v114\nv_and_b32 v121, 0xffff0000, v114\nv_lshlrev_b32 v122, 16, v115\n" \
    "v_and_b32 v123, 0xffff0000, v115\ns_waitcnt lgkmcnt(6)\nv_mul_f32 v116, v116, v64\nv_mul_f32 v117, v117, v65\nv_mul_f32 v118, v118, v66\nv_mul_f32 v119, v119, v67\n" \
    "v_mul_f32 v120, v120, v68\nv_mul_f32 v121, v121, v69\nv_mul_f32 v122, v122, v70\nv_mul_f32 v123, v123, v71\n" \
    "v_mfma_f32_32x32x16_bf16 v[32:47], a[108:111], v[72:75], v[32:47]\nv_cvt_pk_bf16_f32 v136, v116, v117\nv_cvt_pk_bf16_f32 v137, v118, v119\n" \
    "v_cvt_pk_bf16_f32 v138, v120, v121\nv_cvt_pk_bf16_f32 v139, v122, v123\nv_accvgpr_write_b32 a108, v136\nv_accvgpr_write_b32 a109, v137\nv_accvgpr_write_b32 a110, v138\n" \
    "v_accvgpr_write_b32 a111, v139\nv_mfma_f32_32x32x16_bf16 v[16:31], v[136:139], v[128:131], v[16:31]\nds_read_b128 v[64:67], v12 offset:1920\n" \
    "ds_read_b128 v[68:71], v12 offset:1936\nds_read_b128 v[72:75], v13 offset:960\nv_accvgpr_read_b32 v112, a112\nv_accvgpr_read_b32 v113, a113\nv_accvgpr_read_b32 v114, a114\n" \
    "v_accvgpr_read_b32 v115, a115\nv_lshlrev_b32 v116, 16, v112\nv_and_b32 v117, 0xffff0000, v112\nv_lshlrev_b32 v118, 16, v113\nv_and_b32 v119, 0xffff0000, v113\n" \
    "v_lshlrev_b32 v120, 16, v114\nv_and_b32 v121, 0xffff0000, v114\nv_lshlrev_b32 v122, 16, v115\nv_and_b32 v123, 0xffff0000, v115\ns_waitcnt lgkmcnt(6)\n" \
    "v_mul_f32 v116, v116, v80\nv_mul_f32 v117, v117, v81\nv_mul_f32 v118, v118, v82\nv_mul_f32 v119, v119, v83\nv_mul_f32 v120, v120, v84\nv_mul_f32 v121, v121, v85\n" \
    "v_mul_f32 v122, v122, v86\nv_mul_f32 v123, v123, v87\nv_mfma_f32_32x32x16_bf16 v[32:47], a[112:115], v[88:91], v[32:47]\nv_cvt_pk_bf16_f32 v132, v116, v117\n" \
    "v_cvt_pk_bf16_f32 v133, v118, v119\nv_cvt_pk_bf16_f32 v134, v120, v121\nv_cvt_pk_bf16_f32 v135, v122, v123\nv_accvgpr_write_b32 a112, v132\nv_accvgpr_write_b32 a113, v133\n" \
    "v_accvgpr_write_b32 a114, v134\nv_accvgpr_write_b32 a115, v135\nv_mfma_f32_32x32x16_bf16 v[16:31], v[132:135], v[128:131], v[16:31]\n" \
    "ds_read_b128 v[80:83], v12 offset:1984\nds_read_b128 v[84:87], v12 offset:2000\nds_read_b128 v[88:91], v13 offset:992\nv_accvgpr_read_b32 v112, a116\n" \
    "v_accvgpr_read_b32 v113, a117\nv_accvgpr_read_b32 v114, a118\nv_accvgpr_read_b32 v115, a119\nv_lshlrev_b32 v116, 16, v112\nv_and_b32 v117, 0xffff0000, v112\n" \
    "v_lshlrev_b32 v118, 16, v113\nv_and_b32 v119, 0xffff0000, v113\nv_lshlrev_b32 v120, 16, v114\nv_and_b32 v121, 0xffff0000, v114\nv_lshlrev_b32 v122, 16, v115\n" \
    "v_and_b32 v123, 0xffff0000, v115\ns_waitcnt lgkmcnt(6)\nv_mul_f32 v116, v116, v96\nv_mul_f32 v117, v117, v97\nv_mul_f32 v118, v118, v98\nv_mul_f32 v119, v119, v99\n" \
    "v_mul_f32 v120, v120, v100\nv_mul_f32 v121, v121, v101\nv_mul_f32 v122, v122, v102\nv_mul_f32 v123, v123, v103\n" \
    "v_mfma_f32_32x32x16_bf16 v[32:47], a[116:119], v[104:107], v[32:47]\nv_cvt_pk_bf16_f32 v136, v116, v117\nv_cvt_pk_bf16_f32 v137, v118, v119\n" \
    "v_cvt_pk_bf16_f32 v138, v120, v121\nv_cvt_pk_bf16_f32 v139, v122, v123\nv_accvgpr_write_b32 a116, v136\nv_accvgpr_write_b32 a117, v137\nv_accvgpr_write_b32 a118, v138\n" \
    "v_accvgpr_write_b32 a119, v139\nv_mfma_f32_32x32x16_bf16 v[16:31], v[136:139], v[128:131], v[16:31]\nds_read_b128 v[96:99], v12 offset:2048\n" \
    "ds_read_b128 v[100:103], v12 offset:2064\nds_read_b128 v[104:107], v13 offset:1024\nv_accvgpr_read_b32 v112, a120\nv_accvgpr_read_b32 v113, a121\n" \
    "v_accvgpr_read_b32 v114, a122\nv_accvgpr_read_b32 v115, a123\nv_lshlrev_b32 v116, 16, v112\nv_and_b32 v117, 0xffff0000, v112\nv_lshlrev_b32 v118, 16, v113\n" \
    "v_and_b32 v119, 0xffff0000, v113\nv_lshlrev_b32 v120, 16, v114\nv_and_b32 v121, 0xffff0000, v114\nv_lshlrev_b32 v122, 16, v115\nv_and_b32 v123, 0xffff0000, v115\n" \
    "s_waitcnt lgkmcnt(6)\nv_mul_f32 v116, v116, v64\nv_mul_f32 v117, v117, v65\nv_mul_f32 v118, v118, v66\nv_mul_f32 v119, v119, v67\nv_mul_f32 v120, v120, v68\n" \
    "v_mul_f32 v121, v121, v69\nv_mul_f32 v122, v122, v70\nv_mul_f32 v123, v123, v71\nv_mfma_f32_32x32x16_bf16 v[32:47], a[120:123], v[72:75], v[32:47]\n" \
    "v_cvt_pk_bf16_f32 v132, v116, v117\nv_cvt_pk_bf16_f32 v133, v118, v119\nv_cvt_pk_bf16_f32 v134, v120, v121\nv_cvt_pk_bf16_f32 v135, v122, v123\n" \
    "v_accvgpr_write_b32 a120, v132\nv_accvgpr_write_b32 a121, v133\nv_accvgpr_write_b32 a122, v134\nv_accvgpr_write_b32 a123, v135\n" \
    "v_mfma_f32_32x32x16_bf16 v[16:31], v[132:135], v[128:131], v[16:31]\nds_read_b128 v[64:67], v12 offset:2112\nds_read_b128 v[68:71], v12 offset:2128\n" \
    "ds_read_b128 v[72:75], v13 offset:1056\nv_accvgpr_read_b32 v112, a124\nv_accvgpr_read_b32 v113, a125\nv_accvgpr_read_b32 v114, a126\nv_accvgpr_read_b32 v115, a127\n" \
    "v_lshlrev_b32 v116, 16, v112\nv_and_b32 v117, 0xffff0000, v112\nv_lshlrev_b32 v118, 16, v113\nv_and_b32 v119, 0xffff0000, v113\nv_lshlrev_b32 v120, 16, v114\n" \
    "v_and_b32 v121, 0xffff0000, v114\nv_lshlrev_b32 v122, 16, v115\nv_and_b32 v123, 0xffff0000, v115\ns_waitcnt lgkmcnt(6)\nv_mul_f32 v116, v116, v80\n" \
    "v_mul_f32 v117, v117, v81\nv_mul_f32 v118, v118, v82\nv_mul_f32 v119, v119, v83\nv_mul_f32 v120, v120, v84\nv_mul_f32 v121, v121, v85\nv_mul_f32 v122, v122, v86\n" \
    "v_mul_f32 v123, v123, v87\nv_mfma_f32_32x32x16_bf16 v[32:47], a[124:127], v[88:91], v[32:47]\nv_cvt_pk_bf16_f32 v136, v116, v117\nv_cvt_pk_bf16_f32 v137, v118, v119\n" \
    "v_cvt_pk_bf16_f32 v138, v120, v121\nv_cvt_pk_bf16_f32 v139, v122, v123\nv_accvgpr_write_b32 a124, v136\nv_accvgpr_write_b32 a125, v137\nv_accvgpr_write_b32 a126, v138\n" \
    "v_accvgpr_write_b32 a127, v139\nv_mfma_f32_32x32x16_bf16 v[16:31], v[136:139], v[128:131], v[16:31]\nds_read_b128 v[80:83], v12 offset:2176\n" \
    "ds_read_b128 v[84:87], v12 offset:2192\nds_read_b128 v[88:91], v13 offset:1088\nv_accvgpr_read_b32 v112, a128\nv_accvgpr_read_b32 v113, a129\n" \
    "v_accvgpr_read_b32 v114, a130\nv_accvgpr_read_b32 v115, a131\nv_lshlrev_b32 v116, 16, v112\nv_and_b32 v117, 0xffff0000, v112\nv_lshlrev_b32 v118, 16, v113\n" \
    "v_and_b32 v119, 0xffff0000, v113\nv_lshlrev_b32 v120, 16, v114\nv_and_b32 v121, 0xffff0000, v114\nv_lshlrev_b32 v122, 16, v115\nv_and_b32 v123, 0xffff0000, v115\n" \
    "s_waitcnt lgkmcnt(6)\nv_mul_f32 v116, v116, v96\nv_mul_f32 v117, v117, v97\nv_mul_f32 v118, v118, v98\nv_mul_f32 v119, v119, v99\nv_mul_f32 v120, v120, v100\n" \
    "v_mul_f32 v121, v121, v101\nv_mul_f32 v122, v122, v102\nv_mul_f32 v123, v123, v103\nv_mfma_f32_32x32x16_bf16 v[32:47], a[128:131], v[104:107], v[32:47]\n" \
    "v_cvt_pk_bf16_f32 v132, v116, v117\nv_cvt_pk_bf16_f32 v133, v118, v119\nv_cvt_pk_bf16_f32 v134, v120, v121\nv_cvt_pk_bf16_f32 v135, v122, v123\n" \
    "v_accvgpr_write_b32 a128, v132\nv_accvgpr_write_b32 a129, v133\nv_accvgpr_write_b32 a130, v134\nv_accvgpr_write_b32 a131, v135\n" \
    "v_mfma_f32_32x32x16_bf16 v[16:31], v[132:135], v[128:131], v[16:31]\nds_read_b128 v[96:99], v12 offset:2240\nds_read_b128 v[100:103], v12 offset:2256\n" \
    "ds_read_b128 v[104:107], v13 offset:1120\nv_accvgpr_read_b32 v112, a132\nv_accvgpr_read_b32 v113, a133\nv_accvgpr_read_b32 v114, a134\nv_accvgpr_read_b32 v115, a135\n" \
    "v_lshlrev_b32 v116, 16, v112\nv_and_b32 v117, 0xffff0000, v112\nv_lshlrev_b32 v118, 16, v113\nv_and_b32 v119, 0xffff0000, v113\nv_lshlrev_b32 v120, 16, v114\n" \
    "v_and_b32 v121, 0xffff0000, v114\nv_lshlrev_b32 v122, 16, v115\nv_and_b32 v123, 0xffff0000, v115\ns_waitcnt lgkmcnt(6)\nv_mul_f32 v116, v116, v64\n" \
    "v_mul_f32 v117, v117, v65\nv_mul_f32 v118, v118, v66\nv_mul_f32 v119, v119, v67\nv_mul_f32 v120, v120, v68\nv_mul_f32 v121, v121, v69\nv_mul_f32 v122, v122, v70\n" \
    "v_mul_f32 v123, v123, v71\nv_mfma_f32_32x32x16_bf16 v[32:47], a[132:135], v[72:75], v[32:47]\nv_cvt_pk_bf16_f32 v136, v116, v117\nv_cvt_pk_bf16_f32 v137, v118, v119\n" \
    "v_cvt_pk_bf16_f32 v138, v120, v121\nv_cvt_pk_bf16_f32 v139, v122, v123\nv_accvgpr_write_b32 a132, v136\nv_accvgpr_write_b32 a133, v137\nv_accvgpr_write_b32 a134, v138\n" \
    "v_accvgpr_write_b32 a135, v139\nv_mfma_f32_32x32x16_bf16 v[16:31], v[136:139], v[128:131], v[16:31]\nds_read_b128 v[64:67], v12 offset:2304\n" \
    "ds_read_b128 v[68:71], v12 offset:2320\nds_read_b128 v[72:75], v13 offset:1152\nv_accvgpr_read_b32 v112, a136\nv_accvgpr_read_b32 v113, a137\n" \
    "v_accvgpr_read_b32 v114, a138\nv_accvgpr_read_b32 v115, a139\nv_lshlrev_b32 v116, 16, v112\nv_and_b32 v117, 0xffff0000, v112\nv_lshlrev_b32 v118, 16, v113\n" \
    "v_and_b32 v119, 0xffff0000, v113\nv_lshlrev_b32 v120, 16, v114\nv_and_b32 v121, 0xffff0000, v114\nv_lshlrev_b32 v122, 16, v115\nv_and_b32 v123, 0xffff0000, v115\n" \
    "s_waitcnt lgkmcnt(6)\nv_mul_f32 v116, v116, v80\nv_mul_f32 v117, v117, v81\nv_mul_f32 v118, v118, v82\nv_mul_f32 v119, v119, v83\nv_mul_f32 v120, v120, v84\n" \
    "v_mul_f32 v121, v121, v85\nv_mul_f32 v122, v122, v86\nv_mul_f32 v123, v123, v87\nv_mfma_f32_32x32x16_bf16 v[32:47], a[136:139], v[88:91], v[32:47]\n" \
    "v_cvt_pk_bf16_f32 v132, v116, v117\nv_cvt_pk_bf16_f32 v133, v118, v119\nv_cvt_pk_bf16_f32 v134, v120, v121\nv_cvt_pk_bf16_f32 v135, v122, v123\n" \
    "v_accvgpr_write_b32 a136, v132\nv_accvgpr_write_b32 a137, v133\nv_accvgpr_write_b32 a138, v134\nv_accvgpr_write_b32 a139, v135\n" \
    "v_mfma_f32_32x32x16_bf16 v[16:31], v[132:135], v[128:131], v[16:31]\nds_read_b128 v[80:83], v12 offset:2368\nds_read_b128 v[84:87], v12 offset:2384\n" \
    "ds_read_b128 v[88:91], v13 offset:1184\nv_accvgpr_read_b32 v112, a140\nv_accvgpr_read_b32 v113, a141\nv_accvgpr_read_b32 v114, a142\nv_accvgpr_read_b32 v115, a143\n" \
    "v_lshlrev_b32 v116, 16, v112\nv_and_b32 v117, 0xffff0000, v112\nv_lshlrev_b32 v118, 16, v113\nv_and_b32 v119, 0xffff0000, v113\nv_lshlrev_b32 v120, 16, v114\n" \
    "v_and_b32 v121, 0xffff0000, v114\nv_lshlrev_b32 v122, 16, v115\nv_and_b32 v123, 0xffff0000, v115\ns_waitcnt lgkmcnt(6)\nv_mul_f32 v116, v116, v96\n" \
    "v_mul_f32 v117, v117, v97\nv_mul_f32 v118, v118, v98\nv_mul_f32 v119, v119, v99\nv_mul_f32 v120, v120, v100\nv_mul_f32 v121, v121, v101\nv_mul_f32 v122, v122, v102\n" \
    "v_mul_f32 v123, v123, v103\nv_mfma_f32_32x32x16_bf16 v[32:47], a[140:143], v[104:107], v[32:47]\nv_cvt_pk_bf16_f32 v136, v116, v117\nv_cvt_pk_bf16_f32 v137, v118, v119\n" \
    "v_cvt_pk_bf16_f32 v138, v120, v121\nv_cvt_pk_bf16_f32 v139, v122, v123\nv_accvgpr_write_b32 a140, v136\nv_accvgpr_write_b32 a141, v137\nv_accvgpr_write_b32 a142, v138\n" \
    "v_accvgpr_write_b32 a143, v139\nv_mfma_f32_32x32x16_bf16 v[16:31], v[136:139], v[128:131], v[16:31]\nds_read_b128 v[96:99], v12 offset:2432\n" \
    "ds_read_b128 v[100:103], v12 offset:2448\nds_read_b128 v[104:107], v13 offset:1216\nv_accvgpr_read_b32 v112, a144\nv_accvgpr_read_b32 v113, a145\n" \
    "v_accvgpr_read_b32 v114, a146\nv_accvgpr_read_b32 v115, a147\nv_lshlrev_b32 v116, 16, v112\nv_and_b32 v117, 0xffff0000, v112\nv_lshlrev_b32 v118, 16, v113\n" \
    "v_and_b32 v119, 0xffff0000, v113\nv_lshlrev_b32 v120, 16, v114\nv_and_b32 v121, 0xffff0000, v114\nv_lshlrev_b32 v122, 16, v115\nv_and_b32 v123, 0xffff0000, v115\n" \
    "s_waitcnt lgkmcnt(6)\nv_mul_f32 v116, v116, v64\nv_mul_f32 v117, v117, v65\nv_mul_f32 v118, v118, v66\nv_mul_f32 v119, v119, v67\nv_mul_f32 v120, v120, v68\n" \
    "v_mul_f32 v121, v121, v69\nv_mul_f32 v122, v122, v70\nv_mul_f32 v123, v123, v71\nv_mfma_f32_32x32x16_bf16 v[32:47], a[144:147], v[72:75], v[32:47]\n" \
    "v_cvt_pk_bf16_f32 v132, v116, v117\nv_cvt_pk_bf16_f32 v133, v118, v119\nv_cvt_pk_bf16_f32 v134, v120, v121\nv_cvt_pk_bf16_f32 v135, v122, v123\n" \
    "v_accvgpr_write_b32 a144, v132\nv_accvgpr_write_b32 a145, v133\nv_accvgpr_write_b32 a146, v134\nv_accvgpr_write_b32 a147, v135\n" \
    "v_mfma_f32_32x32x16_bf16 v[16:31], v[132:135], v[128:131], v[16:31]\nds_read_b128 v[64:67], v12 offset:2496\nds_read_b128 v[68:71], v12 offset:2512\n" \
    "ds_read_b128 v[72:75], v13 offset:1248\nv_accvgpr_read_b32 v112, a148\nv_accvgpr_read_b32 v113, a149\nv_accvgpr_read_b32 v114, a150\nv_accvgpr_read_b32 v115, a151\n" \
    "v_lshlrev_b32 v116, 16, v112\nv_and_b32 v117, 0xffff0000, v112\nv_lshlrev_b32 v118, 16, v113\nv_and_b32 v119, 0xffff0000, v113\nv_lshlrev_b32 v120, 16, v114\n" \
    "v_and_b32 v121, 0xffff0000, v114\nv_lshlrev_b32 v122, 16, v115\nv_and_b32 v123, 0xffff0000, v115\ns_waitcnt lgkmcnt(6)\nv_mul_f32 v116, v116, v80\n" \
    "v_mul_f32 v117, v117, v81\nv_mul_f32 v118, v118, v82\nv_mul_f32 v119, v119, v83\nv_mul_f32 v120, v120, v84\nv_mul_f32 v121, v121, v85\nv_mul_f32 v122, v122, v86\n" \
    "v_mul_f32 v123, v123, v87\nv_mfma_f32_32x32x16_bf16 v[32:47], a[148:151], v[88:91], v[32:47]\nv_cvt_pk_bf16_f32 v136, v116, v117\nv_cvt_pk_bf16_f32 v137, v118, v119\n" \
    "v_cvt_pk_bf16_f32 v138, v120, v121\nv_cvt_pk_bf16_f32 v139, v122, v123\nv_accvgpr_write_b32 a148, v136\nv_accvgpr_write_b32 a149, v137\nv_accvgpr_write_b32 a150, v138\n" \
    "v_accvgpr_write_b32 a151, v139\nv_mfma_f32_32x32x16_bf16 v[16:31], v[136:139], v[128:131], v[16:31]\nds_read_b128 v[80:83], v12 offset:2560\n" \
    "ds_read_b128 v[84:87], v12 offset:2576\nds_read_b128 v[88:91], v13 offset:1280\nv_accvgpr_read_b32 v112, a152\nv_accvgpr_read_b32 v113, a153\n" \
    "v_accvgpr_read_b32 v114, a154\nv_accvgpr_read_b32 v115, a155\nv_lshlrev_b32 v116, 16, v112\nv_and_b32 v117, 0xffff0000, v112\nv_lshlrev_b32 v118, 16, v113\n" \
    "v_and_b32 v119, 0xffff0000, v113\nv_lshlrev_b32 v120, 16, v114\nv_and_b32 v121, 0xffff0000, v114\nv_lshlrev_b32 v122, 16, v115\nv_and_b32 v123, 0xffff0000, v115\n" \
    "s_waitcnt lgkmcnt(6)\nv_mul_f32 v116, v116, v96\nv_mul_f32 v117, v117, v97\nv_mul_f32 v118, v118, v98\nv_mul_f32 v119, v119, v99\nv_mul_f32 v120, v120, v100\n" \
    "v_mul_f32 v121, v121, v101\nv_mul_f32 v122, v122, v102\nv_mul_f32 v123, v123, v103\nv_mfma_f32_32x32x16_bf16 v[32:47], a[152:155], v[104:107], v[32:47]\n" \
    "v_cvt_pk_bf16_f32 v132, v116, v117\nv_cvt_pk_bf16_f32 v133, v118, v119\nv_cvt_pk_bf16_f32 v134, v120, v121\nv_cvt_pk_bf16_f32 v135, v122, v123\n" \
    "v_accvgpr_write_b32 a152, v132\nv_accvgpr_write_b32 a153, v133\nv_accvgpr_write_b32 a154, v134\nv_accvgpr_write_b32 a155, v135\n" \
    "v_mfma_f32_32x32x16_bf16 v[16:31], v[132:135], v[128:131], v[16:31]\nds_read_b128 v[96:99], v12 offset:2624\nds_read_b128 v[100:103], v12 offset:2640\n" \
    "ds_read_b128 v[104:107], v13 offset:1312\nv_accvgpr_read_b32 v112, a156\nv_accvgpr_read_b32 v113, a157\nv_accvgpr_read_b32 v114, a158\nv_accvgpr_read_b32 v115, a159\n" \
    "v_lshlrev_b32 v116, 16, v112\nv_and_b32 v117, 0xffff0000, v112\nv_lshlrev_b32 v118, 16, v113\nv_and_b32 v119, 0xffff0000, v113\nv_lshlrev_b32 v120, 16, v114\n" \
    "v_and_b32 v121, 0xffff0000, v114\nv_lshlrev_b32 v122, 16, v115\nv_and_b32 v123, 0xffff0000, v115\ns_waitcnt lgkmcnt(6)\nv_mul_f32 v116, v116, v64\n" \
    "v_mul_f32 v117, v117, v65\nv_mul_f32 v118, v118, v66\nv_mul_f32 v119, v119, v67\nv_mul_f32 v120, v120, v68\nv_mul_f32 v121, v121, v69\nv_mul_f32 v122, v122, v70\n" \
    "v_mul_f32 v123, v123, v71\nv_mfma_f32_32x32x16_bf16 v[32:47], a[156:159], v[72:75], v[32:47]\nv_cvt_pk_bf16_f32 v136, v116, v117\nv_cvt_pk_bf16_f32 v137, v118, v119\n" \
    "v_cvt_pk_bf16_f32 v138, v120, v121\nv_cvt_pk_bf16_f32 v139, v122, v123\nv_accvgpr_write_b32 a156, v136\nv_accvgpr_write_b32 a157, v137\nv_accvgpr_write_b32 a158, v138\n" \
    "v_accvgpr_write_b32 a159, v139\nv_mfma_f32_32x32x16_bf16 v[16:31], v[136:139], v[128:131], v[16:31]\nds_read_b128 v[64:67], v12 offset:2688\n" \
    "ds_read_b128 v[68:71], v12 offset:2704\nds_read_b128 v[72:75], v13 offset:1344\nv_accvgpr_read_b32 v112, a160\nv_accvgpr_read_b32 v113, a161\n" \
    "v_accvgpr_read_b32 v114, a162\nv_accvgpr_read_b32 v115, a163\nv_lshlrev_b32 v116, 16, v112\nv_and_b32 v117, 0xffff0000, v112\nv_lshlrev_b32 v118, 16, v113\n" \
    "v_and_b32 v119, 0xffff0000, v113\nv_lshlrev_b32 v120, 16, v114\nv_and_b32 v121, 0xffff0000, v114\nv_lshlrev_b32 v122, 16, v115\nv_and_b32 v123, 0xffff0000, v115\n" \
    "s_waitcnt lgkmcnt(6)\nv_mul_f32 v116, v116, v80\nv_mul_f32 v117, v117, v81\nv_mul_f32 v118, v118, v82\nv_mul_f32 v119, v119, v83\nv_mul_f32 v120, v120, v84\n" \
    "v_mul_f32 v121, v121, v85\nv_mul_f32 v122, v122, v86\nv_mul_f32 v123, v123, v87\nv_mfma_f32_32x32x16_bf16 v[32:47], a[160:163], v[88:91], v[32:47]\n" \
    "v_cvt_pk_bf16_f32 v132, v116, v117\nv_cvt_pk_bf16_f32 v133, v118, v119\nv_cvt_pk_bf16_f32 v134, v120, v121\nv_cvt_pk_bf16_f32 v135, v122, v123\n" \
    "v_accvgpr_write_b32 a160, v132\nv_accvgpr_write_b32 a161, v133\nv_accvgpr_write_b32 a162, v134\nv_accvgpr_write_b32 a163, v135\n" \
    "v_mfma_f32_32x32x16_bf16 v[16:31], v[132:135], v[128:131], v[16:31]\nds_read_b128 v[80:83], v12 offset:2752\nds_read_b128 v[84:87], v12 offset:2768\n" \
    "ds_read_b128 v[88:91], v13 offset:1376\nv_accvgpr_read_b32 v112, a164\nv_accvgpr_read_b32 v113, a165\nv_accvgpr_read_b32 v114, a166\nv_accvgpr_read_b32 v115, a167\n" \
    "v_lshlrev_b32 v116, 16, v112\nv_and_b32 v117, 0xffff0000, v112\nv_lshlrev_b32 v118, 16, v113\nv_and_b32 v119, 0xffff0000, v113\nv_lshlrev_b32 v120, 16, v114\n" \
    "v_and_b32 v121, 0xffff0000, v114\nv_lshlrev_b32 v122, 16, v115\nv_and_b32 v123, 0xffff0000, v115\ns_waitcnt lgkmcnt(6)\nv_mul_f32 v116, v116, v96\n" \
    "v_mul_f32 v117, v117, v97\nv_mul_f32 v118, v118, v98\nv_mul_f32 v119, v119, v99\nv_mul_f32 v120, v120, v100\nv_mul_f32 v121, v121, v101\nv_mul_f32 v122, v122, v102\n" \
    "v_mul_f32 v123, v123, v103\nv_mfma_f32_32x32x16_bf16 v[32:47], a[164:167], v[104:107], v[32:47]\nv_cvt_pk_bf16_f32 v136, v116, v117\nv_cvt_pk_bf16_f32 v137, v118, v119\n" \
    "v_cvt_pk_bf16_f32 v138, v120, v121\nv_cvt_pk_bf16_f32 v139, v122, v123\nv_accvgpr_write_b32 a164, v136\nv_accvgpr_write_b32 a165, v137\nv_accvgpr_write_b32 a166, v138\n" \
    "v_accvgpr_write_b32 a167, v139\nv_mfma_f32_32x32x16_bf16 v[16:31], v[136:139], v[128:131], v[16:31]\nds_read_b128 v[96:99], v12 offset:2816\n" \
    "ds_read_b128 v[100:103], v12 offset:2832\nds_read_b128 v[104:107], v13 offset:1408\nv_accvgpr_read_b32 v112, a168\nv_accvgpr_read_b32 v113, a169\n" \
    "v_accvgpr_read_b32 v114, a170\nv_accvgpr_read_b32 v115, a171\nv_lshlrev_b32 v116, 16, v112\nv_and_b32 v117, 0xffff0000, v112\nv_lshlrev_b32 v118, 16, v113\n" \
    "v_and_b32 v119, 0xffff0000, v113\nv_lshlrev_b32 v120, 16, v114\nv_and_b32 v121, 0xffff0000, v114\nv_lshlrev_b32 v122, 16, v115\nv_and_b32 v123, 0xffff0000, v115\n" \
    "s_waitcnt lgkmcnt(6)\nv_mul_f32 v116, v116, v64\nv_mul_f32 v117, v117, v65\nv_mul_f32 v118, v118, v66\nv_mul_f32 v119, v119, v67\nv_mul_f32 v120, v120, v68\n" \
    "v_mul_f32 v121, v121, v69\nv_mul_f32 v122, v122, v70\nv_mul_f32 v123, v123, v71\nv_mfma_f32_32x32x16_bf16 v[32:47], a[168:171], v[72:75], v[32:47]\n" \
    "v_cvt_pk_bf16_f32 v132, v116, v117\nv_cvt_pk_bf16_f32 v133, v118, v119\nv_cvt_pk_bf16_f32 v134, v120, v121\nv_cvt_pk_bf16_f32 v135, v122, v123\n" \
    "v_accvgpr_write_b32 a168, v132\nv_accvgpr_write_b32 a169, v133\nv_accvgpr_write_b32 a170, v134\nv_accvgpr_write_b32 a171, v135\n" \
    "v_mfma_f32_32x32x16_bf16 v[16:31], v[132:135], v[128:131], v[16:31]\nds_read_b128 v[64:67], v12 offset:2880\nds_read_b128 v[68:71], v12 offset:2896\n" \
    "ds_read_b128 v[72:75], v13 offset:1440\nv_accvgpr_read_b32 v112, a172\nv_accvgpr_read_b32 v113, a173\nv_accvgpr_read_b32 v114, a174\nv_accvgpr_read_b32 v115, a175\n" \
    "v_lshlrev_b32 v116, 16, v112\nv_and_b32 v117, 0xffff0000, v112\nv_lshlrev_b32 v118, 16, v113\nv_and_b32 v119, 0xffff0000, v113\nv_lshlrev_b32 v120, 16, v114\n" \
    "v_and_b32 v121, 0xffff0000, v114\nv_lshlrev_b32 v122, 16, v115\nv_and_b32 v123, 0xffff0000, v115\ns_waitcnt lgkmcnt(6)\nv_mul_f32 v116, v116, v80\n" \
    "v_mul_f32 v117, v117, v81\nv_mul_f32 v118, v118, v82\nv_mul_f32 v119, v119, v83\nv_mul_f32 v120, v120, v84\nv_mul_f32 v121, v121, v85\nv_mul_f32 v122, v122, v86\n" \
    "v_mul_f32 v123, v123, v87\nv_mfma_f32_32x32x16_bf16 v[32:47], a[172:175], v[88:91], v[32:47]\nv_cvt_pk_bf16_f32 v136, v116, v117\nv_cvt_pk_bf16_f32 v137, v118, v119\n" \
    "v_cvt_pk_bf16_f32 v138, v120, v121\nv_cvt_pk_bf16_f32 v139, v122, v123\nv_accvgpr_write_b32 a172, v136\nv_accvgpr_write_b32 a173, v137\nv_accvgpr_write_b32 a174, v138\n" \
    "v_accvgpr_write_b32 a175, v139\nv_mfma_f32_32x32x16_bf16 v[16:31], v[136:139], v[128:131], v[16:31]\nds_read_b128 v[80:83], v12 offset:2944\n" \
    "ds_read_b128 v[84:87], v12 offset:2960\nds_read_b128 v[88:91], v13 offset:1472\nv_accvgpr_read_b32 v112, a176\nv_accvgpr_read_b32 v113, a177\n" \
    "v_accvgpr_read_b32 v114, a178\nv_accvgpr_read_b32 v115, a179\nv_lshlrev_b32 v116, 16, v112\nv_and_b32 v117, 0xffff0000, v112\nv_lshlrev_b32 v118, 16, v113\n" \
    "v_and_b32 v119, 0xffff0000, v113\nv_lshlrev_b32 v120, 16, v114\nv_and_b32 v121, 0xffff0000, v114\nv_lshlrev_b32 v122, 16, v115\nv_and_b32 v123, 0xffff0000, v115\n" \
    "s_waitcnt lgkmcnt(6)\nv_mul_f32 v116, v116, v96\nv_mul_f32 v117, v117, v97\nv_mul_f32 v118, v118, v98\nv_mul_f32 v119, v119, v99\nv_mul_f32 v120, v120, v100\n" \
    "v_mul_f32 v121, v121, v101\nv_mul_f32 v122, v122, v102\nv_mul_f32 v123, v123, v103\nv_mfma_f32_32x32x16_bf16 v[32:47], a[176:179], v[104:107], v[32:47]\n" \
    "v_cvt_pk_bf16_f32 v132, v116, v117\nv_cvt_pk_bf16_f32 v133, v118, v119\nv_cvt_pk_bf16_f32 v134, v120, v121\nv_cvt_pk_bf16_f32 v135, v122, v123\n" \
    "v_accvgpr_write_b32 a176, v132\nv_accvgpr_write_b32 a177, v133\nv_accvgpr_write_b32 a178, v134\nv_accvgpr_write_b32 a179, v135\n" \
    "v_mfma_f32_32x32x16_bf16 v[16:31], v[132:135], v[128:131], v[16:31]\nds_read_b128 v[96:99], v12 offset:3008\nds_read_b128 v[100:103], v12 offset:3024\n" \
    "ds_read_b128 v[104:107], v13 offset:1504\nv_accvgpr_read_b32 v112, a180\nv_accvgpr_read_b32 v113, a181\nv_accvgpr_read_b32 v114, a182\nv_accvgpr_read_b32 v115, a183\n" \
    "v_lshlrev_b32 v116, 16, v112\nv_and_b32 v117, 0xffff0000, v112\nv_lshlrev_b32 v118, 16, v113\nv_and_b32 v119, 0xffff0000, v113\nv_lshlrev_b32 v120, 16, v114\n" \
    "v_and_b32 v121, 0xffff0000, v114\nv_lshlrev_b32 v122, 16, v115\nv_and_b32 v123, 0xffff0000, v115\ns_waitcnt lgkmcnt(6)\nv_mul_f32 v116, v116, v64\n" \
    "v_mul_f32 v117, v117, v65\nv_mul_f32 v118, v118, v66\nv_mul_f32 v119, v119, v67\nv_mul_f32 v120, v120, v68\nv_mul_f32 v121, v121, v69\nv_mul_f32 v122, v122, v70\n" \
    "v_mul_f32 v123, v123, v71\nv_mfma_f32_32x32x16_bf16 v[32:47], a[180:183], v[72:75], v[32:47]\nv_cvt_pk_bf16_f32 v136, v116, v117\nv_cvt_pk_bf16_f32 v137, v118, v119\n" \
    "v_cvt_pk_bf16_f32 v138, v120, v121\nv_cvt_pk_bf16_f32 v139, v122, v123\nv_accvgpr_write_b32 a180, v136\nv_accvgpr_write_b32 a181, v137\nv_accvgpr_write_b32 a182, v138\n" \
    "v_accvgpr_write_b32 a183, v139\nv_mfma_f32_32x32x16_bf16 v[16:31], v[136:139], v[128:131], v[16:31]\nds_read_b128 v[64:67], v12 offset:3072\n" \
    "ds_read_b128 v[68:71], v12 offset:3088\nds_read_b128 v[72:75], v13 offset:1536\nv_accvgpr_read_b32 v112, a184\nv_accvgpr_read_b32 v113, a185\n" \
    "v_accvgpr_read_b32 v114, a186\nv_accvgpr_read_b32 v115, a187\nv_lshlrev_b32 v116, 16, v112\nv_and_b32 v117, 0xffff0000, v112\nv_lshlrev_b32 v118, 16, v113\n" \
    "v_and_b32 v119, 0xffff0000, v113\nv_lshlrev_b32 v120, 16, v114\nv_and_b32 v121, 0xffff0000, v114\nv_lshlrev_b32 v122, 16, v115\nv_and_b32 v123, 0xffff0000, v115\n" \
    "s_waitcnt lgkmcnt(6)\nv_mul_f32 v116, v116, v80\nv_mul_f32 v117, v117, v81\nv_mul_f32 v118, v118, v82\nv_mul_f32 v119, v119, v83\nv_mul_f32 v120, v120, v84\n" \
    "v_mul_f32 v121, v121, v85\nv_mul_f32 v122, v122, v86\nv_mul_f32 v123, v123, v87\nv_mfma_f32_32x32x16_bf16 v[32:47], a[184:187], v[88:91], v[32:47]\n" \
    "v_cvt_pk_bf16_f32 v132, v116, v117\nv_cvt_pk_bf16_f32 v133, v118, v119\nv_cvt_pk_bf16_f32 v134, v120, v121\nv_cvt_pk_bf16_f32 v135, v122, v123\n" \
    "v_accvgpr_write_b32 a184, v132\nv_accvgpr_write_b32 a185, v133\nv_accvgpr_write_b32 a186, v134\nv_accvgpr_write_b32 a187, v135\n" \
    "v_mfma_f32_32x32x16_bf16 v[16:31], v[132:135], v[128:131], v[16:31]\nds_read_b128 v[80:83], v12 offset:3136\nds_read_b128 v[84:87], v12 offset:3152\n" \
    "ds_read_b128 v[88:91], v13 offset:1568\nv_accvgpr_read_b32 v112, a188\nv_accvgpr_read_b32 v113, a189\nv_accvgpr_read_b32 v114, a190\nv_accvgpr_read_b32 v115, a191\n" \
    "v_lshlrev_b32 v116, 16, v112\nv_and_b32 v117, 0xffff0000, v112\nv_lshlrev_b32 v118, 16, v113\nv_and_b32 v119, 0xffff0000, v113\nv_lshlrev_b32 v120, 16, v114\n" \
    "v_and_b32 v121, 0xffff0000, v114\nv_lshlrev_b32 v122, 16, v115\nv_and_b32 v123, 0xffff0000, v115\ns_waitcnt lgkmcnt(6)\nv_mul_f32 v116, v116, v96\n" \
    "v_mul_f32 v117, v117, v97\nv_mul_f32 v118, v118, v98\nv_mul_f32 v119, v119, v99\nv_mul_f32 v120, v120, v100\nv_mul_f32 v121, v121, v101\nv_mul_f32 v122, v122, v102\n" \
    "v_mul_f32 v123, v123, v103\nv_mfma_f32_32x32x16_bf16 v[32:47], a[188:191], v[104:107], v[32:47]\nv_cvt_pk_bf16_f32 v136, v116, v117\nv_cvt_pk_bf16_f32 v137, v118, v119\n" \
    "v_cvt_pk_bf16_f32 v138, v120, v121\nv_cvt_pk_bf16_f32 v139, v122, v123\nv_accvgpr_write_b32 a188, v136\nv_accvgpr_write_b32 a189, v137\nv_accvgpr_write_b32 a190, v138\n" \
    "v_accvgpr_write_b32 a191, v139\nv_mfma_f32_32x32x16_bf16 v[16:31], v[136:139], v[128:131], v[16:31]\nds_read_b128 v[96:99], v12 offset:3200\n" \
    "ds_read_b128 v[100:103], v12 offset:3216\nds_read_b128 v[104:107], v13 offset:1600\nv_accvgpr_read_b32 v112, a192\nv_accvgpr_read_b32 v113, a193\n" \
    "v_accvgpr_read_b32 v114, a194\nv_accvgpr_read_b32 v115, a195\nv_lshlrev_b32 v116, 16, v112\nv_and_b32 v117, 0xffff0000, v112\nv_lshlrev_b32 v118, 16, v113\n" \
    "v_and_b32 v119, 0xffff0000, v113\nv_lshlrev_b32 v120, 16, v114\nv_and_b32 v121, 0xffff0000, v114\nv_lshlrev_b32 v122, 16, v115\nv_and_b32 v123, 0xffff0000, v115\n" \
    "s_waitcnt lgkmcnt(6)\nv_mul_f32 v116, v116, v64\nv_mul_f32 v117, v117, v65\nv_mul_f32 v118, v118, v66\nv_mul_f32 v119, v119, v67\nv_mul_f32 v120, v120, v68\n" \
    "v_mul_f32 v121, v121, v69\nv_mul_f32 v122, v122, v70\nv_mul_f32 v123, v123, v71\nv_mfma_f32_32x32x16_bf16 v[32:47], a[192:195], v[72:75], v[32:47]\n" \
    "v_cvt_pk_bf16_f32 v132, v116, v117\nv_cvt_pk_bf16_f32 v133, v118, v119\nv_cvt_pk_bf16_f32 v134, v120, v121\nv_cvt_pk_bf16_f32 v135, v122, v123\n" \
    "v_accvgpr_write_b32 a192, v132\nv_accvgpr_write_b32 a193, v133\nv_accvgpr_write_b32 a194, v134\nv_accvgpr_write_b32 a195, v135\n" \
    "v_mfma_f32_32x32x16_bf16 v[16:31], v[132:135], v[128:131], v[16:31]\nds_read_b128 v[64:67], v12 offset:3264\nds_read_b128 v[68:71], v12 offset:3280\n" \
    "ds_read_b128 v[72:75], v13 offset:1632\nv_accvgpr_read_b32 v112, a196\nv_accvgpr_read_b32 v113, a197\nv_accvgpr_read_b32 v114, a198\nv_accvgpr_read_b32 v115, a199\n" \
    "v_lshlrev_b32 v116, 16, v112\nv_and_b32 v117, 0xffff0000, v112\nv_lshlrev_b32 v118, 16, v113\nv_and_b32 v119, 0xffff0000, v113\nv_lshlrev_b32 v120, 16, v114\n" \
    "v_and_b32 v121, 0xffff0000, v114\nv_lshlrev_b32 v122, 16, v115\nv_and_b32 v123, 0xffff0000, v115\ns_waitcnt lgkmcnt(6)\nv_mul_f32 v116, v116, v80\n" \
    "v_mul_f32 v117, v117, v81\nv_mul_f32 v118, v118, v82\nv_mul_f32 v119, v119, v83\nv_mul_f32 v120, v120, v84\nv_mul_f32 v121, v121, v85\nv_mul_f32 v122, v122, v86\n" \
    "v_mul_f32 v123, v123, v87\nv_mfma_f32_32x32x16_bf16 v[32:47], a[196:199], v[88:91], v[32:47]\nv_cvt_pk_bf16_f32 v136, v116, v117\nv_cvt_pk_bf16_f32 v137, v118, v119\n" \
    "v_cvt_pk_bf16_f32 v138, v120, v121\nv_cvt_pk_bf16_f32 v139, v122, v123\nv_accvgpr_write_b32 a196, v136\nv_accvgpr_write_b32 a197, v137\nv_accvgpr_write_b32 a198, v138\n" \
    "v_accvgpr_write_b32 a199, v139\nv_mfma_f32_32x32x16_bf16 v[16:31], v[136:139], v[128:131], v[16:31]\nds_read_b128 v[80:83], v12 offset:3328\n" \
    "ds_read_b128 v[84:87], v12 offset:3344\nds_read_b128 v[88:91], v13 offset:1664\nv_accvgpr_read_b32 v112, a200\nv_accvgpr_read_b32 v113, a201\n" \
    "v_accvgpr_read_b32 v114, a202\nv_accvgpr_read_b32 v115, a203\nv_lshlrev_b32 v116, 16, v112\nv_and_b32 v117, 0xffff0000, v112\nv_lshlrev_b32 v118, 16, v113\n" \
    "v_and_b32 v119, 0xffff0000, v113\nv_lshlrev_b32 v120, 16, v114\nv_and_b32 v121, 0xffff0000, v114\nv_lshlrev_b32 v122, 16, v115\nv_and_b32 v123, 0xffff0000, v115\n" \
    "s_waitcnt lgkmcnt(6)\nv_mul_f32 v116, v116, v96\nv_mul_f32 v117, v117, v97\nv_mul_f32 v118, v118, v98\nv_mul_f32 v119, v119, v99\nv_mul_f32 v120, v120, v100\n" \
    "v_mul_f32 v121, v121, v101\nv_mul_f32 v122, v122, v102\nv_mul_f32 v123, v123, v103\nv_mfma_f32_32x32x16_bf16 v[32:47], a[200:203], v[104:107], v[32:47]\n" \
    "v_cvt_pk_bf16_f32 v132, v116, v117\nv_cvt_pk_bf16_f32 v133, v118, v119\nv_cvt_pk_bf16_f32 v134, v120, v121\nv_cvt_pk_bf16_f32 v135, v122, v123\n" \
    "v_accvgpr_write_b32 a200, v132\nv_accvgpr_write_b32 a201, v133\nv_accvgpr_write_b32 a202, v134\nv_accvgpr_write_b32 a203, v135\n" \
    "v_mfma_f32_32x32x16_bf16 v[16:31], v[132:135], v[128:131], v[16:31]\nds_read_b128 v[96:99], v12 offset:3392\nds_read_b128 v[100:103], v12 offset:3408\n" \
    "ds_read_b128 v[104:107], v13 offset:1696\nv_accvgpr_read_b32 v112, a204\nv_accvgpr_read_b32 v113, a205\nv_accvgpr_read_b32 v114, a206\nv_accvgpr_read_b32 v115, a207\n" \
    "v_lshlrev_b32 v116, 16, v112\nv_and_b32 v117, 0xffff0000, v112\nv_lshlrev_b32 v118, 16, v113\nv_and_b32 v119, 0xffff0000, v113\nv_lshlrev_b32 v120, 16, v114\n" \
    "v_and_b32 v121, 0xffff0000, v114\nv_lshlrev_b32 v122, 16, v115\nv_and_b32 v123, 0xffff0000, v115\ns_waitcnt lgkmcnt(6)\nv_mul_f32 v116, v116, v64\n" \
    "v_mul_f32 v117, v117, v65\nv_mul_f32 v118, v118, v66\nv_mul_f32 v119, v119, v67\nv_mul_f32 v120, v120, v68\nv_mul_f32 v121, v121, v69\nv_mul_f32 v122, v122, v70\n" \
    "v_mul_f32 v123, v123, v71\nv_mfma_f32_32x32x16_bf16 v[32:47], a[204:207], v[72:75], v[32:47]\nv_cvt_pk_bf16_f32 v136, v116, v117\nv_cvt_pk_bf16_f32 v137, v118, v119\n" \
    "v_cvt_pk_bf16_f32 v138, v120, v121\nv_cvt_pk_bf16_f32 v139, v122, v123\nv_accvgpr_write_b32 a204, v136\nv_accvgpr_write_b32 a205, v137\nv_accvgpr_write_b32 a206, v138\n" \
    "v_accvgpr_write_b32 a207, v139\nv_mfma_f32_32x32x16_bf16 v[16:31], v[136:139], v[128:131], v[16:31]\nds_read_b128 v[64:67], v12 offset:3456\n" \
    "ds_read_b128 v[68:71], v12 offset:3472\nds_read_b128 v[72:75], v13 offset:1728\nv_accvgpr_read_b32 v112, a208\nv_accvgpr_read_b32 v113, a209\n" \
    "v_accvgpr_read_b32 v114, a210\nv_accvgpr_read_b32 v115, a211\nv_lshlrev_b32 v116, 16, v112\nv_and_b32 v117, 0xffff0000, v112\nv_lshlrev_b32 v118, 16, v113\n" \
    "v_and_b32 v119, 0xffff0000, v113\nv_lshlrev_b32 v120, 16, v114\nv_and_b32 v121, 0xffff0000, v114\nv_lshlrev_b32 v122, 16, v115\nv_and_b32 v123, 0xffff0000, v115\n" \
    "s_waitcnt lgkmcnt(6)\nv_mul_f32 v116, v116, v80\nv_mul_f32 v117, v117, v81\nv_mul_f32 v118, v118, v82\nv_mul_f32 v119, v119, v83\nv_mul_f32 v120, v120, v84\n" \
    "v_mul_f32 v121, v121, v85\nv_mul_f32 v122, v122, v86\nv_mul_f32 v123, v123, v87\nv_mfma_f32_32x32x16_bf16 v[32:47], a[208:211], v[88:91], v[32:47]\n" \
    "v_cvt_pk_bf16_f32 v132, v116, v117\nv_cvt_pk_bf16_f32 v133, v118, v119\nv_cvt_pk_bf16_f32 v134, v120, v121\nv_cvt_pk_bf16_f32 v135, v122, v123\n" \
    "v_accvgpr_write_b32 a208, v132\nv_accvgpr_write_b32 a209, v133\nv_accvgpr_write_b32 a210, v134\nv_accvgpr_write_b32 a211, v135\n" \
    "v_mfma_f32_32x32x16_bf16 v[16:31], v[132:135], v[128:131], v[16:31]\nds_read_b128 v[80:83], v12 offset:3520\nds_read_b128 v[84:87], v12 offset:3536\n" \
    "ds_read_b128 v[88:91], v13 offset:1760\nv_accvgpr_read_b32 v112, a212\nv_accvgpr_read_b32 v113, a213\nv_accvgpr_read_b32 v114, a214\nv_accvgpr_read_b32 v115, a215\n" \
    "v_lshlrev_b32 v116, 16, v112\nv_and_b32 v117, 0xffff0000, v112\nv_lshlrev_b32 v118, 16, v113\nv_and_b32 v119, 0xffff0000, v113\nv_lshlrev_b32 v120, 16, v114\n" \
    "v_and_b32 v121, 0xffff0000, v114\nv_lshlrev_b32 v122, 16, v115\nv_and_b32 v123, 0xffff0000, v115\ns_waitcnt lgkmcnt(6)\nv_mul_f32 v116, v116, v96\n" \
    "v_mul_f32 v117, v117, v97\nv_mul_f32 v118, v118, v98\nv_mul_f32 v119, v119, v99\nv_mul_f32 v120, v120, v100\nv_mul_f32 v121, v121, v101\nv_mul_f32 v122, v122, v102\n" \
    "v_mul_f32 v123, v123, v103\nv_mfma_f32_32x32x16_bf16 v[32:47], a[212:215], v[104:107], v[32:47]\nv_cvt_pk_bf16_f32 v136, v116, v117\nv_cvt_pk_bf16_f32 v137, v118, v119\n" \
    "v_cvt_pk_bf16_f32 v138, v120, v121\nv_cvt_pk_bf16_f32 v139, v122, v123\nv_accvgpr_write_b32 a212, v136\nv_accvgpr_write_b32 a213, v137\nv_accvgpr_write_b32 a214, v138\n" \
    "v_accvgpr_write_b32 a215, v139\nv_mfma_f32_32x32x16_bf16 v[16:31], v[136:139], v[128:131], v[16:31]\nds_read_b128 v[96:99], v12 offset:3584\n" \
    "ds_read_b128 v[100:103], v12 offset:3600\nds_read_b128 v[104:107], v13 offset:1792\nv_accvgpr_read_b32 v112, a216\nv_accvgpr_read_b32 v113, a217\n" \
    "v_accvgpr_read_b32 v114, a218\nv_accvgpr_read_b32 v115, a219\nv_lshlrev_b32 v116, 16, v112\nv_and_b32 v117, 0xffff0000, v112\nv_lshlrev_b32 v118, 16, v113\n" \
    "v_and_b32 v119, 0xffff0000, v113\nv_lshlrev_b32 v120, 16, v114\nv_and_b32 v121, 0xffff0000, v114\nv_lshlrev_b32 v122, 16, v115\nv_and_b32 v123, 0xffff0000, v115\n" \
    "s_waitcnt lgkmcnt(6)\nv_mul_f32 v116, v116, v64\nv_mul_f32 v117, v117, v65\nv_mul_f32 v118, v118, v66\nv_mul_f32 v119, v119, v67\nv_mul_f32 v120, v120, v68\n" \
    "v_mul_f32 v121, v121, v69\nv_mul_f32 v122, v122, v70\nv_mul_f32 v123, v123, v71\nv_mfma_f32_32x32x16_bf16 v[32:47], a[216:219], v[72:75], v[32:47]\n" \
    "v_cvt_pk_bf16_f32 v132, v116, v117\nv_cvt_pk_bf16_f32 v133, v118, v119\nv_cvt_pk_bf16_f32 v134, v120, v121\nv_cvt_pk_bf16_f32 v135, v122, v123\n" \
    "v_accvgpr_write_b32 a216, v132\nv_accvgpr_write_b32 a217, v133\nv_accvgpr_write_b32 a218, v134\nv_accvgpr_write_b32 a219, v135\n" \
    "v_mfma_f32_32x32x16_bf16 v[16:31], v[132:135], v[128:131], v[16:31]\nds_read_b128 v[64:67], v12 offset:3648\nds_read_b128 v[68:71], v12 offset:3664\n" \
    "ds_read_b128 v[72:75], v13 offset:1824\nv_accvgpr_read_b32 v112, a220\nv_accvgpr_read_b32 v113, a221\nv_accvgpr_read_b32 v114, a222\nv_accvgpr_read_b32 v115, a223\n" \
    "v_lshlrev_b32 v116, 16, v112\nv_and_b32 v117, 0xffff0000, v112\nv_lshlrev_b32 v118, 16, v113\nv_and_b32 v119, 0xffff0000, v113\nv_lshlrev_b32 v120, 16, v114\n" \
    "v_and_b32 v121, 0xffff0000, v114\nv_lshlrev_b32 v122, 16, v115\nv_and_b32 v123, 0xffff0000, v115\ns_waitcnt lgkmcnt(6)\nv_mul_f32 v116, v116, v80\n" \
    "v_mul_f32 v117, v117, v81\nv_mul_f32 v118, v118, v82\nv_mul_f32 v119, v119, v83\nv_mul_f32 v120, v120, v84\nv_mul_f32 v121, v121, v85\nv_mul_f32 v122, v122, v86\n" \
    "v_mul_f32 v123, v123, v87\nv_mfma_f32_32x32x16_bf16 v[32:47], a[220:223], v[88:91], v[32:47]\nv_cvt_pk_bf16_f32 v136, v116, v117\nv_cvt_pk_bf16_f32 v137, v118, v119\n" \
    "v_cvt_pk_bf16_f32 v138, v120, v121\nv_cvt_pk_bf16_f32 v139, v122, v123\nv_accvgpr_write_b32 a220, v136\nv_accvgpr_write_b32 a221, v137\nv_accvgpr_write_b32 a222, v138\n" \
    "v_accvgpr_write_b32 a223, v139\nv_mfma_f32_32x32x16_bf16 v[16:31], v[136:139], v[128:131], v[16:31]\nds_read_b128 v[80:83], v12 offset:3712\n" \
    "ds_read_b128 v[84:87], v12 offset:3728\nds_read_b128 v[88:91], v13 offset:1856\nv_accvgpr_read_b32 v112, a224\nv_accvgpr_read_b32 v113, a225\n" \
    "v_accvgpr_read_b32 v114, a226\nv_accvgpr_read_b32 v115, a227\nv_lshlrev_b32 v116, 16, v112\nv_and_b32 v117, 0xffff0000, v112\nv_lshlrev_b32 v118, 16, v113\n" \
    "v_and_b32 v119, 0xffff0000, v113\nv_lshlrev_b32 v120, 16, v114\nv_and_b32 v121, 0xffff0000, v114\nv_lshlrev_b32 v122, 16, v115\nv_and_b32 v123, 0xffff0000, v115\n" \
    "s_waitcnt lgkmcnt(6)\nv_mul_f32 v116, v116, v96\nv_mul_f32 v117, v117, v97\nv_mul_f32 v118, v118, v98\nv_mul_f32 v119, v119, v99\nv_mul_f32 v120, v120, v100\n" \
    "v_mul_f32 v121, v121, v101\nv_mul_f32 v122, v122, v102\nv_mul_f32 v123, v123, v103\nv_mfma_f32_32x32x16_bf16 v[32:47], a[224:227], v[104:107], v[32:47]\n" \
    "v_cvt_pk_bf16_f32 v132, v116, v117\nv_cvt_pk_bf16_f32 v133, v118, v119\nv_cvt_pk_bf16_f32 v134, v120, v121\nv_cvt_pk_bf16_f32 v135, v122, v123\n" \
    "v_accvgpr_write_b32 a224, v132\nv_accvgpr_write_b32 a225, v133\nv_accvgpr_write_b32 a226, v134\nv_accvgpr_write_b32 a227, v135\n" \
    "v_mfma_f32_32x32x16_bf16 v[16:31], v[132:135], v[128:131], v[16:31]\nds_read_b128 v[96:99], v12 offset:3776\nds_read_b128 v[100:103], v12 offset:3792\n" \
    "ds_read_b128 v[104:107], v13 offset:1888\nv_accvgpr_read_b32 v112, a228\nv_accvgpr_read_b32 v113, a229\nv_accvgpr_read_b32 v114, a230\nv_accvgpr_read_b32 v115, a231\n" \
    "v_lshlrev_b32 v116, 16, v112\nv_and_b32 v117, 0xffff0000, v112\nv_lshlrev_b32 v118, 16, v113\nv_and_b32 v119, 0xffff0000, v113\nv_lshlrev_b32 v120, 16, v114\n" \
    "v_and_b32 v121, 0xffff0000, v114\nv_lshlrev_b32 v122, 16, v115\nv_and_b32 v123, 0xffff0000, v115\ns_waitcnt lgkmcnt(6)\nv_mul_f32 v116, v116, v64\n" \
    "v_mul_f32 v117, v117, v65\nv_mul_f32 v118, v118, v66\nv_mul_f32 v119, v119, v67\nv_mul_f32 v120, v120, v68\nv_mul_f32 v121, v121, v69\nv_mul_f32 v122, v122, v70\n" \
    "v_mul_f32 v123, v123, v71\nv_mfma_f32_32x32x16_bf16 v[32:47], a[228:231], v[72:75], v[32:47]\nv_cvt_pk_bf16_f32 v136, v116, v117\nv_cvt_pk_bf16_f32 v137, v118, v119\n" \
    "v_cvt_pk_bf16_f32 v138, v120, v121\nv_cvt_pk_bf16_f32 v139, v122, v123\nv_accvgpr_write_b32 a228, v136\nv_accvgpr_write_b32 a229, v137\nv_accvgpr_write_b32 a230, v138\n" \
    "v_accvgpr_write_b32 a231, v139\nv_mfma_f32_32x32x16_bf16 v[16:31], v[136:139], v[128:131], v[16:31]\nds_read_b128 v[64:67], v12 offset:3840\n" \
    "ds_read_b128 v[68:71], v12 offset:3856\nds_read_b128 v[72:75], v13 offset:1920\nv_accvgpr_read_b32 v112, a232\nv_accvgpr_read_b32 v113, a233\n" \
    "v_accvgpr_read_b32 v114, a234\nv_accvgpr_read_b32 v115, a235\nv_lshlrev_b32 v116, 16, v112\nv_and_b32 v117, 0xffff0000, v112\nv_lshlrev_b32 v118, 16, v113\n" \
    "v_and_b32 v119, 0xffff0000, v113\nv_lshlrev_b32 v120, 16, v114\nv_and_b32 v121, 0xffff0000, v114\nv_lshlrev_b32 v122, 16, v115\nv_and_b32 v123, 0xffff0000, v115\n" \
    "s_waitcnt lgkmcnt(6)\nv_mul_f32 v116, v116, v80\nv_mul_f32 v117, v117, v81\nv_mul_f32 v118, v118, v82\nv_mul_f32 v119, v119, v83\nv_mul_f32 v120, v120, v84\n" \
    "v_mul_f32 v121, v121, v85\nv_mul_f32 v122, v122, v86\nv_mul_f32 v123, v123, v87\nv_mfma_f32_32x32x16_bf16 v[32:47], a[232:235], v[88:91], v[32:47]\n" \
    "v_cvt_pk_bf16_f32 v132, v116, v117\nv_cvt_pk_bf16_f32 v133, v118, v119\nv_cvt_pk_bf16_f32 v134, v120, v121\nv_cvt_pk_bf16_f32 v135, v122, v123\n" \
    "v_accvgpr_write_b32 a232, v132\nv_accvgpr_write_b32 a233, v133\nv_accvgpr_write_b32 a234, v134\nv_accvgpr_write_b32 a235, v135\n" \
    "v_mfma_f32_32x32x16_bf16 v[16:31], v[132:135], v[128:131], v[16:31]\nds_read_b128 v[80:83], v12 offset:3904\nds_read_b128 v[84:87], v12 offset:3920\n" \
    "ds_read_b128 v[88:91], v13 offset:1952\nv_accvgpr_read_b32 v112, a236\nv_accvgpr_read_b32 v113, a237\nv_accvgpr_read_b32 v114, a238\nv_accvgpr_read_b32 v115, a239\n" \
    "v_lshlrev_b32 v116, 16, v112\nv_and_b32 v117, 0xffff0000, v112\nv_lshlrev_b32 v118, 16, v113\nv_and_b32 v119, 0xffff0000, v113\nv_lshlrev_b32 v120, 16, v114\n" \
    "v_and_b32 v121, 0xffff0000, v114\nv_lshlrev_b32 v122, 16, v115\nv_and_b32 v123, 0xffff0000, v115\ns_waitcnt lgkmcnt(6)\nv_mul_f32 v116, v116, v96\n" \
    "v_mul_f32 v117, v117, v97\nv_mul_f32 v118, v118, v98\nv_mul_f32 v119, v119, v99\nv_mul_f32 v120, v120, v100\nv_mul_f32 v121, v121, v101\nv_mul_f32 v122, v122, v102\n" \
    "v_mul_f32 v123, v123, v103\nv_mfma_f32_32x32x16_bf16 v[32:47], a[236:239], v[104:107], v[32:47]\nv_cvt_pk_bf16_f32 v136, v116, v117\nv_cvt_pk_bf16_f32 v137, v118, v119\n" \
    "v_cvt_pk_bf16_f32 v138, v120, v121\nv_cvt_pk_bf16_f32 v139, v122, v123\nv_accvgpr_write_b32 a236, v136\nv_accvgpr_write_b32 a237, v137\nv_accvgpr_write_b32 a238, v138\n" \
    "v_accvgpr_write_b32 a239, v139\nv_mfma_f32_32x32x16_bf16 v[16:31], v[136:139], v[128:131], v[16:31]\nds_read_b128 v[96:99], v12 offset:3968\n" \
    "ds_read_b128 v[100:103], v12 offset:3984\nds_read_b128 v[104:107], v13 offset:1984\nv_accvgpr_read_b32 v112, a240\nv_accvgpr_read_b32 v113, a241\n" \
    "v_accvgpr_read_b32 v114, a242\nv_accvgpr_read_b32 v115, a243\nv_lshlrev_b32 v116, 16, v112\nv_and_b32 v117, 0xffff0000, v112\nv_lshlrev_b32 v118, 16, v113\n" \
    "v_and_b32 v119, 0xffff0000, v113\nv_lshlrev_b32 v120, 16, v114\nv_and_b32 v121, 0xffff0000, v114\nv_lshlrev_b32 v122, 16, v115\nv_and_b32 v123, 0xffff0000, v115\n" \
    "s_waitcnt lgkmcnt(6)\nv_mul_f32 v116, v116, v64\nv_mul_f32 v117, v117, v65\nv_mul_f32 v118, v118, v66\nv_mul_f32 v119, v119, v67\nv_mul_f32 v120, v120, v68\n" \
    "v_mul_f32 v121, v121, v69\nv_mul_f32 v122, v122, v70\nv_mul_f32 v123, v123, v71\nv_mfma_f32_32x32x16_bf16 v[32:47], a[240:243], v[72:75], v[32:47]\n" \
    "v_cvt_pk_bf16_f32 v132, v116, v117\nv_cvt_pk_bf16_f32 v133, v118, v119\nv_cvt_pk_bf16_f32 v134, v120, v121\nv_cvt_pk_bf16_f32 v135, v122, v123\n" \
    "v_accvgpr_write_b32 a240, v132\nv_accvgpr_write_b32 a241, v133\nv_accvgpr_write_b32 a242, v134\nv_accvgpr_write_b32 a243, v135\n" \
    "v_mfma_f32_32x32x16_bf16 v[16:31], v[132:135], v[128:131], v[16:31]\nds_read_b128 v[64:67], v12 offset:4032\nds_read_b128 v[68:71], v12 offset:4048\n" \
    "ds_read_b128 v[72:75], v13 offset:2016\nv_accvgpr_read_b32 v112, a244\nv_accvgpr_read_b32 v113, a245\nv_accvgpr_read_b32 v114, a246\nv_accvgpr_read_b32 v115, a247\n" \
    "v_lshlrev_b32 v116, 16, v112\nv_and_b32 v117, 0xffff0000, v112\nv_lshlrev_b32 v118, 16, v113\nv_and_b32 v119, 0xffff0000, v113\nv_lshlrev_b32 v120, 16, v114\n" \
    "v_and_b32 v121, 0xffff0000, v114\nv_lshlrev_b32 v122, 16, v115\nv_and_b32 v123, 0xffff0000, v115\ns_waitcnt lgkmcnt(6)\nv_mul_f32 v116, v116, v80\n" \
    "v_mul_f32 v117, v117, v81\nv_mul_f32 v118, v118, v82\nv_mul_f32 v119, v119, v83\nv_mul_f32 v120, v120, v84\nv_mul_f32 v121, v121, v85\nv_mul_f32 v122, v122, v86\n" \
    "v_mul_f32 v123, v123, v87\nv_mfma_f32_32x32x16_bf16 v[32:47], a[244:247], v[88:91], v[32:47]\nv_cvt_pk_bf16_f32 v136, v116, v117\nv_cvt_pk_bf16_f32 v137, v118, v119\n" \
    "v_cvt_pk_bf16_f32 v138, v120, v121\nv_cvt_pk_bf16_f32 v139, v122, v123\nv_accvgpr_write_b32 a244, v136\nv_accvgpr_write_b32 a245, v137\nv_accvgpr_write_b32 a246, v138\n" \
    "v_accvgpr_write_b32 a247, v139\nv_mfma_f32_32x32x16_bf16 v[16:31], v[136:139], v[128:131], v[16:31]\nv_accvgpr_read_b32 v112, a248\nv_accvgpr_read_b32 v113, a249\n" \
    "v_accvgpr_read_b32 v114, a250\nv_accvgpr_read_b32 v115, a251\nv_lshlrev_b32 v116, 16, v112\nv_and_b32 v117, 0xffff0000, v112\nv_lshlrev_b32 v118, 16, v113\n" \
    "v_and_b32 v119, 0xffff0000, v113\nv_lshlrev_b32 v120, 16, v114\nv_and_b32 v121, 0xffff0000, v114\nv_lshlrev_b32 v122, 16, v115\nv_and_b32 v123, 0xffff0000, v115\n" \
    "s_waitcnt lgkmcnt(3)\nv_mul_f32 v116, v116, v96\nv_mul_f32 v117, v117, v97\nv_mul_f32 v118, v118, v98\nv_mul_f32 v119, v119, v99\nv_mul_f32 v120, v120, v100\n" \
    "v_mul_f32 v121, v121, v101\nv_mul_f32 v122, v122, v102\nv_mul_f32 v123, v123, v103\nv_mfma_f32_32x32x16_bf16 v[32:47], a[248:251], v[104:107], v[32:47]\n" \
    "v_cvt_pk_bf16_f32 v132, v116, v117\nv_cvt_pk_bf16_f32 v133, v118, v119\nv_cvt_pk_bf16_f32 v134, v120, v121\nv_cvt_pk_bf16_f32 v135, v122, v123\n" \
    "v_accvgpr_write_b32 a248, v132\nv_accvgpr_write_b32 a249, v133\nv_accvgpr_write_b32 a250, v134\nv_accvgpr_write_b32 a251, v135\n" \
    "v_mfma_f32_32x32x16_bf16 v[16:31], v[132:135], v[128:131], v[16:31]\nv_accvgpr_read_b32 v112, a252\nv_accvgpr_read_b32 v113, a253\nv_accvgpr_read_b32 v114, a254\n" \
    "v_accvgpr_read_b32 v115, a255\nv_lshlrev_b32 v116, 16, v112\nv_and_b32 v117, 0xffff0000, v112\nv_lshlrev_b32 v118, 16, v113\nv_and_b32 v119, 0xffff0000, v113\n" \
    "v_lshlrev_b32 v120, 16, v114\nv_and_b32 v121, 0xffff0000, v114\nv_lshlrev_b32 v122, 16, v115\nv_and_b32 v123, 0xffff0000, v115\ns_waitcnt lgkmcnt(0)\n" \
    "v_mul_f32 v116, v116, v64\nv_mul_f32 v117, v117, v65\nv_mul_f32 v118, v118, v66\nv_mul_f32 v119, v119, v67\nv_mul_f32 v120, v120, v68\nv_mul_f32 v121, v121, v69\n" \
    "v_mul_f32 v122, v122, v70\nv_mul_f32 v123, v123, v71\nv_mfma_f32_32x32x16_bf16 v[32:47], a[252:255], v[72:75], v[32:47]\nv_cvt_pk_bf16_f32 v136, v116, v117\n" \
    "v_cvt_pk_bf16_f32 v137, v118, v119\nv_cvt_pk_bf16_f32 v138, v120, v121\nv_cvt_pk_bf16_f32 v139, v122, v123\nv_accvgpr_write_b32 a252, v136\nv_accvgpr_write_b32 a253, v137\n" \
    "v_accvgpr_write_b32 a254, v138\nv_accvgpr_write_b32 a255, v139\nv_mfma_f32_32x32x16_bf16 v[16:31], v[136:139], v[128:131], v[16:31]\ns_mov_b32 s90, 0x7fff0000\n" \
    "s_mov_b32 s91, 0x7fff0000\ns_mov_b32 s92, 0x7fff0000\ns_mov_b32 s93, 0x7fff0000\ns_mov_b32 s80, 0\ns_mov_b32 s81, 0\ns_mov_b32 s68, 0\n.Lf2_%=:\ns_xor_b32 s85, s38, 512\n" \
    "s_and_b32 s69, s32, 63\ns_lshl_b32 s69, s69, 19\ns_add_u32 s82, s36, s69\ns_lshr_b32 s69, s82, 8\ns_and_b32 s69, s69, 0x2000\ns_xor_b32 s82, s82, s69\n" \
    "s_add_u32 s69, s32, 1\ns_and_b32 s69, s69, 63\ns_lshl_b32 s69, s69, 19\ns_add_u32 s83, s36, s69\ns_lshr_b32 s69, s83, 8\ns_and_b32 s69, s69, 0x2000\n" \
    "s_xor_b32 s83, s83, s69\ns_add_u32 s84, s37, s80\ns_cmp_eq_u32 s81, 1\ns_cbranch_scc1 .Lf6_%=\ns_add_u32 s77, s32, 2\ns_cmp_eq_u32 s81, 2\ns_cbranch_scc1 .Lf5_%=\n" \
    ".Lf3_%=:\nbuffer_load_dword v150, off, s[64:67], s38 sc1\ns_waitcnt vmcnt(0)\nv_and_b32 v112, 127, v150\nv_cmp_gt_u32 vcc, s77, v112\ns_nop 0\ns_cbranch_vccnz .Lf4_%=\n" \
    ".Lf5_%=:\nv_and_b32 v112, 127, v150\n.Lf11_%=:\ns_memrealtime s[72:73]\ns_waitcnt lgkmcnt(0)\ns_cmp_ge_u32 s77, 64\ns_cselect_b32 s69, s60, s34\ns_sub_u32 s72, s72, s69\n" \
    "s_lshl_b32 s72, s72, 8\nv_sub_u32 v113, s72, v150\nv_cmp_gt_i32 s[74:75], 0, v113\nv_cmp_eq_u32 vcc, s77, v112\ns_nop 0\ns_and_b64 vcc, vcc, s[74:75]\n" \
    "s_cbranch_scc1 .Lf11_%=\n.Lf12_%=:\nbuffer_load_dword v151, v2, s[64:67], s82 offen sc1\ns_add_u32 m0, s84, 0\ns_add_u32 s69, s82, s48\n" \
    "buffer_load_dwordx4 v0, s[64:67], s69 offen offset:0 sc1 lds\nbuffer_load_dwordx4 v0, s[64:67], s69 offen offset:1024 sc1 lds\ns_add_u32 m0, s84, 8192\n" \
    "s_add_u32 s69, s82, s49\nbuffer_load_dwordx4 v0, s[64:67], s69 offen offset:0 sc1 lds\nbuffer_load_dwordx4 v0, s[64:67], s69 offen offset:1024 sc1 lds\n" \
    "s_add_u32 m0, s84, 16384\ns_add_u32 s69, s82, s50\nbuffer_load_dwordx4 v0, s[64:67], s69 offen offset:0 sc1 lds\n" \
    "buffer_load_dwordx4 v0, s[64:67], s69 offen offset:1024 sc1 lds\n.Lf6_%=:\ns_waitcnt vmcnt(6)\nv_cmp_eq_u32 vcc, -1, v151\ns_nop 0\ns_cbranch_vccnz .Lf13_%=\n" \
    "s_cmp_lg_u32 s33, 0\ns_cbranch_scc1 .Lf15_%=\ns_memrealtime s[72:73]\ns_waitcnt lgkmcnt(0)\ns_lshl_b32 s72, s72, 8\ns_add_u32 s72, s72, s32\ns_add_u32 s72, s72, 1\n" \
    "v_mov_b32 v112, s72\ns_mov_b64 exec, 1\nbuffer_store_dword v112, off, s[64:67], s39 sc1\ns_mov_b64 exec, -1\n.Lf15_%=:\ns_add_u32 s77, s32, 3\ns_add_u32 m0, s84, 24576\n" \
    "s_add_u32 s69, s82, s51\nbuffer_load_dwordx4 v0, s[64:67], s69 offen offset:0 sc1 lds\nbuffer_load_dwordx4 v0, s[64:67], s69 offen offset:1024 sc1 lds\ns_waitcnt vmcnt(6)\n" \
    "s_barrier\nds_read_b128 v[64:67], v1 offset:0\nds_read_b128 v[68:71], v1 offset:1024\nds_read_b128 v[72:75], v1 offset:2048\nds_read_b128 v[76:79], v1 offset:3072\n" \
    "s_waitcnt lgkmcnt(3)\nv_mfma_f32_32x32x16_bf16 v[48:63], a[0:3], v[64:67], 0\nds_read_b128 v[80:83], v1 offset:4096\ns_add_u32 s69, s82, s41\n" \
    "buffer_load_dwordx4 v[96:99], v0, s[64:67], s69 offen offset:0 sc1\ns_waitcnt lgkmcnt(3)\nv_mfma_f32_32x32x16_bf16 v[48:63], a[4:7], v[68:71], v[48:63]\n" \
    "ds_read_b128 v[84:87], v1 offset:5120\nbuffer_load_dwordx4 v[100:103], v0, s[64:67], s69 offen offset:1024 sc1\ns_waitcnt lgkmcnt(3)\n" \
    "v_mfma_f32_32x32x16_bf16 v[48:63], a[8:11], v[72:75], v[48:63]\nds_read_b128 v[88:91], v1 offset:6144\n" \
    "buffer_load_dwordx4 v[104:107], v0, s[64:67], s69 offen offset:2048 sc1\ns_waitcnt lgkmcnt(3)\nv_mfma_f32_32x32x16_bf16 v[48:63], a[12:15], v[76:79], v[48:63]\n" \
    "ds_read_b128 v[92:95], v1 offset:7168\nbuffer_load_dwordx4 v[108:111], v0, s[64:67], s69 offen offset:3072 sc1\ns_waitcnt vmcnt(8) lgkmcnt(3)\ns_barrier\n" \
    "v_mfma_f32_32x32x16_bf16 v[48:63], a[16:19], v[80:83], v[48:63]\nds_read_b128 v[64:67], v1 offset:8192\ns_add_u32 m0, s84, 32768\ns_add_u32 s69, s82, s52\n" \
    "buffer_load_dwordx4 v0, s[64:67], s69 offen offset:0 sc1 lds\ns_waitcnt lgkmcnt(3)\nv_mfma_f32_32x32x16_bf16 v[48:63], a[20:23], v[84:87], v[48:63]\n" \
    "ds_read_b128 v[68:71], v1 offset:9216\ns_waitcnt lgkmcnt(3)\nv_mfma_f32_32x32x16_bf16 v[48:63], a[24:27], v[88:91], v[48:63]\nds_read_b128 v[72:75], v1 offset:10240\n" \
    "buffer_load_dwordx4 v0, s[64:67], s69 offen offset:1024 sc1 lds\ns_waitcnt lgkmcnt(3)\nv_mfma_f32_32x32x16_bf16 v[48:63], a[28:31], v[92:95], v[48:63]\n" \
    "ds_read_b128 v[76:79], v1 offset:11264\ns_add_u32 m0, s84, 40960\ns_add_u32 s69, s82, s53\nbuffer_load_dwordx4 v0, s[64:67], s69 offen offset:0 sc1 lds\n" \
    "s_waitcnt lgkmcnt(3)\nv_mfma_f32_32x32x16_bf16 v[48:63], a[32:35], v[64:67], v[48:63]\nds_read_b128 v[80:83], v1 offset:12288\ns_waitcnt lgkmcnt(3)\n" \
    "v_mfma_f32_32x32x16_bf16 v[48:63], a[36:39], v[68:71], v[48:63]\nds_read_b128 v[84:87], v1 offset:13312\nbuffer_load_dwordx4 v0, s[64:67], s69 offen offset:1024 sc1 lds\n" \
    "s_waitcnt lgkmcnt(3)\nv_mfma_f32_32x32x16_bf16 v[48:63], a[40:43], v[72:75], v[48:63]\nds_read_b128 v[88:91], v1 offset:14336\ns_add_u32 m0, s84, 49152\n" \
    "s_add_u32 s69, s82, s54\nbuffer_load_dwordx4 v0, s[64:67], s69 offen offset:0 sc1 lds\ns_waitcnt lgkmcnt(3)\n" \
    "v_mfma_f32_32x32x16_bf16 v[48:63], a[44:47], v[76:79], v[48:63]\nds_read_b128 v[92:95], v1 offset:15360\ns_waitcnt vmcnt(9) lgkmcnt(3)\ns_barrier\n" \
    "v_mfma_f32_32x32x16_bf16 v[48:63], a[48:51], v[80:83], v[48:63]\nds_read_b128 v[64:67], v1 offset:16384\nbuffer_load_dwordx4 v0, s[64:67], s69 offen offset:1024 sc1 lds\n" \
    "s_waitcnt lgkmcnt(3)\nv_mfma_f32_32x32x16_bf16 v[48:63], a[52:55], v[84:87], v[48:63]\nds_read_b128 v[68:71], v1 offset:17408\ns_add_u32 m0, s84, 57344\n" \
    "s_add_u32 s69, s82, s55\nbuffer_load_dwordx4 v0, s[64:67], s69 offen offset:0 sc1 lds\ns_waitcnt lgkmcnt(3)\n" \
    "v_mfma_f32_32x32x16_bf16 v[48:63], a[56:59], v[88:91], v[48:63]\nds_read_b128 v[72:75], v1 offset:18432\ns_waitcnt lgkmcnt(3)\n" \
    "v_mfma_f32_32x32x16_bf16 v[48:63], a[60:63], v[92:95], v[48:63]\nds_read_b128 v[76:79], v1 offset:19456\nbuffer_load_dwordx4 v0, s[64:67], s69 offen offset:1024 sc1 lds\n" \
    "s_waitcnt lgkmcnt(3)\nv_mfma_f32_32x32x16_bf16 v[48:63], a[64:67], v[64:67], v[48:63]\nds_read_b128 v[80:83], v1 offset:20480\ns_and_b32 s69, s32, 63\n" \
    "s_lshl_b32 s69, s69, 19\ns_add_u32 s82, s40, s69\ns_lshr_b32 s69, s82, 8\ns_and_b32 s69, s69, 0x2000\ns_waitcnt lgkmcnt(3)\n" \
    "v_mfma_f32_32x32x16_bf16 v[48:63], a[68:71], v[68:71], v[48:63]\nds_read_b128 v[84:87], v1 offset:21504\ns_xor_b32 s82, s82, s69\ns_waitcnt vmcnt(8)\ns_waitcnt lgkmcnt(3)\n" \
    "v_readfirstlane_b32 s76, v150\nv_max3_u32 v116, v96, v97, v98\nv_max3_u32 v116, v116, v99, v100\nv_mfma_f32_32x32x16_bf16 v[48:63], a[72:75], v[72:75], v[48:63]\n" \
    "ds_read_b128 v[88:91], v1 offset:22528\ns_and_b32 s76, s76, 127\ns_waitcnt lgkmcnt(3)\nv_max3_u32 v116, v116, v101, v102\nv_max3_u32 v116, v116, v103, v104\n" \
    "v_max3_u32 v116, v116, v105, v106\nv_mfma_f32_32x32x16_bf16 v[48:63], a[76:79], v[76:79], v[48:63]\nds_read_b128 v[92:95], v1 offset:23552\ns_waitcnt lgkmcnt(3)\n" \
    "v_max3_u32 v116, v116, v107, v108\nv_max3_u32 v116, v116, v109, v110\nv_max_u32 v116, v116, v111\nv_mfma_f32_32x32x16_bf16 v[48:63], a[80:83], v[80:83], v[48:63]\n" \
    "ds_read_b128 v[64:67], v1 offset:24576\ns_waitcnt lgkmcnt(3)\nv_cmp_eq_u32 s[70:71], -1, v116\nv_sub_f32 v114, v96, v100\nv_add_f32 v96, v96, v100\n" \
    "v_mfma_f32_32x32x16_bf16 v[48:63], a[84:87], v[84:87], v[48:63]\nds_read_b128 v[68:71], v1 offset:25600\ns_waitcnt lgkmcnt(3)\nv_add_f32 v97, v97, v101\n" \
    "v_mul_f32 v114, v114, v114\nv_mul_f32 v96, 0.5, v96\nv_mfma_f32_32x32x16_bf16 v[48:63], a[88:91], v[88:91], v[48:63]\nds_read_b128 v[72:75], v1 offset:26624\n" \
    "s_waitcnt lgkmcnt(3)\nv_fmac_f32 v97, 0x41000000, v114\nv_sub_f32 v114, v104, v108\nv_add_f32 v104, v104, v108\n" \
    "v_mfma_f32_32x32x16_bf16 v[48:63], a[92:95], v[92:95], v[48:63]\nds_read_b128 v[76:79], v1 offset:27648\ns_waitcnt lgkmcnt(3)\nv_add_f32 v105, v105, v109\n" \
    "v_mul_f32 v114, v114, v114\nv_mul_f32 v104, 0.5, v104\nv_mfma_f32_32x32x16_bf16 v[48:63], a[96:99], v[64:67], v[48:63]\nds_read_b128 v[80:83], v1 offset:28672\n" \
    "s_waitcnt lgkmcnt(3)\nv_fmac_f32 v105, 0x41000000, v114\nv_sub_f32 v114, v96, v104\nv_add_f32 v96, v96, v104\n" \
    "v_mfma_f32_32x32x16_bf16 v[48:63], a[100:103], v[68:71], v[48:63]\nds_read_b128 v[84:87], v1 offset:29696\ns_waitcnt lgkmcnt(3)\nv_add_f32 v97, v97, v105\n" \
    "v_mul_f32 v114, v114, v114\nv_mul_f32 v96, 0.5, v96\nv_mfma_f32_32x32x16_bf16 v[48:63], a[104:107], v[72:75], v[48:63]\nds_read_b128 v[88:91], v1 offset:30720\n" \
    "s_waitcnt lgkmcnt(3)\nv_fmac_f32 v97, 0x41800000, v114\nv_sub_f32 v114, v98, v102\nv_add_f32 v98, v98, v102\n" \
    "v_mfma_f32_32x32x16_bf16 v[48:63], a[108:111], v[76:79], v[48:63]\nds_read_b128 v[92:95], v1 offset:31744\ns_waitcnt vmcnt(4) lgkmcnt(3)\ns_barrier\n" \
    "v_add_f32 v99, v99, v103\nv_mul_f32 v114, v114, v114\nv_mul_f32 v98, 0.5, v98\nv_mfma_f32_32x32x16_bf16 v[48:63], a[112:115], v[80:83], v[48:63]\n" \
    "ds_read_b128 v[64:67], v1 offset:32768\ns_waitcnt lgkmcnt(3)\nv_fmac_f32 v99, 0x41000000, v114\nv_sub_f32 v114, v106, v110\nv_add_f32 v106, v106, v110\n" \
    "v_mfma_f32_32x32x16_bf16 v[48:63], a[116:119], v[84:87], v[48:63]\nds_read_b128 v[68:71], v1 offset:33792\ns_waitcnt lgkmcnt(3)\nv_add_f32 v107, v107, v111\n" \
    "v_mul_f32 v114, v114, v114\nv_mul_f32 v106, 0.5, v106\nv_mfma_f32_32x32x16_bf16 v[48:63], a[120:123], v[88:91], v[48:63]\nds_read_b128 v[72:75], v1 offset:34816\n" \
    "s_waitcnt lgkmcnt(3)\nv_fmac_f32 v107, 0x41000000, v114\nv_sub_f32 v114, v98, v106\nv_add_f32 v98, v98, v106\n" \
    "v_mfma_f32_32x32x16_bf16 v[48:63], a[124:127], v[92:95], v[48:63]\nds_read_b128 v[76:79], v1 offset:35840\ns_waitcnt lgkmcnt(3)\nv_add_f32 v99, v99, v107\n" \
    "v_mul_f32 v114, v114, v114\nv_mul_f32 v98, 0.5, v98\nv_mfma_f32_32x32x16_bf16 v[48:63], a[128:131], v[64:67], v[48:63]\nds_read_b128 v[80:83], v1 offset:36864\n" \
    "s_waitcnt lgkmcnt(3)\nv_fmac_f32 v99, 0x41800000, v114\nv_mov_b64 v[100:101], v[96:97]\nv_mov_b64 v[102:103], v[98:99]\n" \
    "v_mfma_f32_32x32x16_bf16 v[48:63], a[132:135], v[68:71], v[48:63]\nds_read_b128 v[84:87], v1 offset:37888\ns_waitcnt lgkmcnt(3)\nv_permlane16_swap_b32 v96, v100\n" \
    "v_permlane16_swap_b32 v97, v101\nv_mfma_f32_32x32x16_bf16 v[48:63], a[136:139], v[72:75], v[48:63]\nds_read_b128 v[88:91], v1 offset:38912\ns_waitcnt lgkmcnt(3)\n" \
    "v_permlane16_swap_b32 v98, v102\nv_permlane16_swap_b32 v99, v103\nv_mfma_f32_32x32x16_bf16 v[48:63], a[140:143], v[76:79], v[48:63]\n" \
    "ds_read_b128 v[92:95], v1 offset:39936\ns_waitcnt lgkmcnt(3)\nv_sub_f32 v114, v96, v100\nv_add_f32 v96, v96, v100\nv_add_f32 v97, v97, v101\n" \
    "v_mfma_f32_32x32x16_bf16 v[48:63], a[144:147], v[80:83], v[48:63]\nds_read_b128 v[64:67], v1 offset:40960\ns_waitcnt lgkmcnt(3)\nv_mul_f32 v114, v114, v114\n" \
    "v_mul_f32 v96, 0.5, v96\nv_fmac_f32 v97, 0x42000000, v114\nv_mfma_f32_32x32x16_bf16 v[48:63], a[148:151], v[84:87], v[48:63]\nds_read_b128 v[68:71], v1 offset:41984\n" \
    "s_waitcnt lgkmcnt(3)\nv_sub_f32 v114, v98, v102\nv_add_f32 v98, v98, v102\nv_add_f32 v99, v99, v103\nv_mfma_f32_32x32x16_bf16 v[48:63], a[152:155], v[88:91], v[48:63]\n" \
    "ds_read_b128 v[72:75], v1 offset:43008\ns_waitcnt lgkmcnt(3)\nv_mul_f32 v114, v114, v114\nv_mul_f32 v98, 0.5, v98\nv_fmac_f32 v99, 0x42000000, v114\n" \
    "v_mfma_f32_32x32x16_bf16 v[48:63], a[156:159], v[92:95], v[48:63]\nds_read_b128 v[76:79], v1 offset:44032\ns_waitcnt lgkmcnt(3)\nv_mov_b64 v[100:101], v[96:97]\n" \
    "v_mov_b64 v[102:103], v[98:99]\nv_mfma_f32_32x32x16_bf16 v[48:63], a[160:163], v[64:67], v[48:63]\nds_read_b128 v[80:83], v1 offset:45056\ns_waitcnt lgkmcnt(3)\n" \
    "v_permlane32_swap_b32 v96, v100\nv_permlane32_swap_b32 v97, v101\nv_mfma_f32_32x32x16_bf16 v[48:63], a[164:167], v[68:71], v[48:63]\n" \
    "ds_read_b128 v[84:87], v1 offset:46080\ns_waitcnt lgkmcnt(3)\nv_permlane32_swap_b32 v98, v102\nv_permlane32_swap_b32 v99, v103\n" \
    "v_mfma_f32_32x32x16_bf16 v[48:63], a[168:171], v[72:75], v[48:63]\nds_read_b128 v[88:91], v1 offset:47104\ns_waitcnt lgkmcnt(3)\nv_sub_f32 v114, v96, v100\n" \
    "v_add_f32 v96, v96, v100\nv_add_f32 v97, v97, v101\nv_mfma_f32_32x32x16_bf16 v[48:63], a[172:175], v[76:79], v[48:63]\nds_read_b128 v[92:95], v1 offset:48128\n" \
    "s_waitcnt vmcnt(0) lgkmcnt(3)\ns_barrier\nv_mul_f32 v114, v114, v114\nv_mul_f32 v96, 0.5, v96\nv_fmac_f32 v97, 0x42800000, v114\n" \
    "v_mfma_f32_32x32x16_bf16 v[48:63], a[176:179], v[80:83], v[48:63]\nds_read_b128 v[64:67], v1 offset:49152\ns_waitcnt lgkmcnt(3)\nv_sub_f32 v114, v98, v102\n" \
    "v_add_f32 v98, v98, v102\nv_add_f32 v99, v99, v103\nv_mfma_f32_32x32x16_bf16 v[48:63], a[180:183], v[84:87], v[48:63]\nds_read_b128 v[68:71], v1 offset:50176\n" \
    "s_waitcnt lgkmcnt(3)\nv_mul_f32 v114, v114, v114\nv_mul_f32 v98, 0.5, v98\nv_fmac_f32 v99, 0x42800000, v114\n" \
    "v_mfma_f32_32x32x16_bf16 v[48:63], a[184:187], v[88:91], v[48:63]\nds_read_b128 v[72:75], v1 offset:51200\ns_cmp_gt_u32 s76, s77\ns_cbranch_scc1 .Lf16_%=\n" \
    "buffer_load_dword v150, off, s[64:67], s85 sc1\n.Lf16_%=:\ns_cmp_lg_u64 s[70:71], 0\ns_cselect_b32 s78, -1, 0\ns_waitcnt lgkmcnt(3)\nv_min_u32 v96, -2, v96\n" \
    "v_min_u32 v98, -2, v98\nv_or_b32 v96, s78, v96\nv_mfma_f32_32x32x16_bf16 v[48:63], a[188:191], v[92:95], v[48:63]\nds_read_b128 v[76:79], v1 offset:52224\n" \
    "s_waitcnt lgkmcnt(3)\nv_or_b32 v98, s78, v98\nv_mfma_f32_32x32x16_bf16 v[48:63], a[192:195], v[64:67], v[48:63]\nds_read_b128 v[80:83], v1 offset:53248\n" \
    "ds_write_b128 v3, v[96:99]\ns_waitcnt lgkmcnt(4)\nv_mfma_f32_32x32x16_bf16 v[48:63], a[196:199], v[68:71], v[48:63]\nds_read_b128 v[84:87], v1 offset:54272\n" \
    "s_waitcnt lgkmcnt(4)\nv_mfma_f32_32x32x16_bf16 v[48:63], a[200:203], v[72:75], v[48:63]\nds_read_b128 v[88:91], v1 offset:55296\ns_waitcnt lgkmcnt(2)\ns_barrier\n" \
    "ds_read_b64 v[96:97], v4 offset:0\nds_read_b64 v[98:99], v4 offset:256\nds_read_b64 v[100:101], v4 offset:512\n" \
    "v_mfma_f32_32x32x16_bf16 v[48:63], a[204:207], v[76:79], v[48:63]\nds_read_b128 v[92:95], v1 offset:56320\nds_read_b64 v[102:103], v4 offset:768\n" \
    "v_mfma_f32_32x32x16_bf16 v[48:63], a[208:211], v[80:83], v[48:63]\nds_read_b128 v[64:67], v1 offset:57344\ns_waitcnt lgkmcnt(7)\n" \
    "v_mfma_f32_32x32x16_bf16 v[48:63], a[212:215], v[84:87], v[48:63]\nds_read_b128 v[68:71], v1 offset:58368\ns_waitcnt lgkmcnt(2)\nv_max3_u32 v116, v96, v98, v100\n" \
    "v_max_u32 v116, v116, v102\nv_cmp_eq_u32 s[70:71], -1, v116\nv_mfma_f32_32x32x16_bf16 v[48:63], a[216:219], v[88:91], v[48:63]\nds_read_b128 v[72:75], v1 offset:59392\n" \
    "v_sub_f32 v114, v96, v98\nv_add_f32 v96, v96, v98\nv_add_f32 v97, v97, v99\nv_mfma_f32_32x32x16_bf16 v[48:63], a[220:223], v[92:95], v[48:63]\n" \
    "ds_read_b128 v[76:79], v1 offset:60416\ns_waitcnt lgkmcnt(3)\nv_mul_f32 v114, v114, v114\nv_mul_f32 v96, 0.5, v96\nv_fmac_f32 v97, 0x43000000, v114\n" \
    "v_mfma_f32_32x32x16_bf16 v[48:63], a[224:227], v[64:67], v[48:63]\nds_read_b128 v[80:83], v1 offset:61440\ns_waitcnt lgkmcnt(3)\nv_sub_f32 v114, v100, v102\n" \
    "v_add_f32 v100, v100, v102\nv_add_f32 v101, v101, v103\nv_mfma_f32_32x32x16_bf16 v[48:63], a[228:231], v[68:71], v[48:63]\nds_read_b128 v[84:87], v1 offset:62464\n" \
    "s_waitcnt lgkmcnt(3)\nv_mul_f32 v114, v114, v114\nv_mul_f32 v100, 0.5, v100\nv_fmac_f32 v101, 0x43000000, v114\n" \
    "v_mfma_f32_32x32x16_bf16 v[48:63], a[232:235], v[72:75], v[48:63]\nds_read_b128 v[88:91], v1 offset:63488\ns_waitcnt vmcnt(0)\ns_waitcnt lgkmcnt(3)\n" \
    "v_and_b32 v112, 127, v150\nv_cmp_gt_u32 vcc, s77, v112\nv_sub_f32 v114, v96, v100\nv_mfma_f32_32x32x16_bf16 v[48:63], a[236:239], v[76:79], v[48:63]\n" \
    "ds_read_b128 v[92:95], v1 offset:64512\ns_cmp_eq_u64 vcc, 0\ns_cselect_b32 s81, 1, 0\ns_waitcnt lgkmcnt(3)\nv_add_f32 v96, v96, v100\nv_add_f32 v97, v97, v101\n" \
    "v_mul_f32 v114, v114, v114\nv_mfma_f32_32x32x16_bf16 v[48:63], a[240:243], v[80:83], v[48:63]\ns_waitcnt lgkmcnt(2)\nv_mul_f32 v96, 0.5, v96\n" \
    "v_fmac_f32 v97, 0x43800000, v114\nv_mul_f32 v98, 0x3a800000, v97\nv_mfma_f32_32x32x16_bf16 v[48:63], a[244:247], v[84:87], v[48:63]\ns_waitcnt lgkmcnt(0)\n" \
    "s_memrealtime s[72:73]\nv_cvt_pk_bf16_f32 v96, 0, v96\nv_cvt_pk_bf16_f32 v98, 0, v98\nv_add_f32 v98, 0x3727c5ac, v98\n" \
    "v_mfma_f32_32x32x16_bf16 v[48:63], a[248:251], v[88:91], v[48:63]\nv_cvt_pk_bf16_f32 v98, 0, v98\nv_sqrt_f32 v98, v98\n" \
    "v_mfma_f32_32x32x16_bf16 v[48:63], a[252:255], v[92:95], v[48:63]\ns_waitcnt lgkmcnt(0)\ns_cmp_ge_u32 s77, 64\ns_cselect_b32 s69, s60, s34\ns_sub_u32 s72, s72, s69\n" \
    "s_lshl_b32 s72, s72, 8\nv_sub_u32 v113, s72, v150\nv_cmp_gt_i32 s[74:75], 0, v113\nv_cmp_eq_u32 vcc, s77, v112\ns_nop 0\ns_and_b64 vcc, vcc, s[74:75]\n" \
    "s_cselect_b32 s69, 1, 0\ns_lshl_b32 s81, s81, s69\nv_cvt_pk_bf16_f32 v98, 0, v98\nv_rcp_f32 v98, v98\ns_cmp_lg_u64 s[70:71], 0\ns_cbranch_scc1 .Lf9_%=\n" \
    "v_cmp_u_f32 vcc, v48, v48\ns_nop 0\ns_cbranch_vccnz .Lf7_%=\n.Lf8_%=:\ns_cmp_eq_u32 s81, 1\ns_cbranch_scc1 .Lf17_%=\n" \
    "v_pk_fma_f32 v[48:49], v[16:17], v[96:97], v[48:49] op_sel_hi:[1,0,1] neg_lo:[0,1,0] neg_hi:[0,1,0]\n" \
    "v_pk_fma_f32 v[50:51], v[18:19], v[96:97], v[50:51] op_sel_hi:[1,0,1] neg_lo:[0,1,0] neg_hi:[0,1,0]\n" \
    "v_pk_fma_f32 v[52:53], v[20:21], v[96:97], v[52:53] op_sel_hi:[1,0,1] neg_lo:[0,1,0] neg_hi:[0,1,0]\n" \
    "v_pk_fma_f32 v[54:55], v[22:23], v[96:97], v[54:55] op_sel_hi:[1,0,1] neg_lo:[0,1,0] neg_hi:[0,1,0]\nv_pk_fma_f32 v[48:49], v[48:49], v[98:99], v[32:33] op_sel_hi:[1,0,1]\n" \
    "v_pk_fma_f32 v[50:51], v[50:51], v[98:99], v[34:35] op_sel_hi:[1,0,1]\nv_pk_fma_f32 v[52:53], v[52:53], v[98:99], v[36:37] op_sel_hi:[1,0,1]\n" \
    "v_pk_fma_f32 v[54:55], v[54:55], v[98:99], v[38:39] op_sel_hi:[1,0,1]\nv_pk_mul_f32 v[72:73], v[48:49], v[48:49]\nv_pk_mul_f32 v[74:75], v[50:51], v[50:51]\n" \
    "v_pk_mul_f32 v[76:77], v[52:53], v[52:53]\nv_pk_mul_f32 v[78:79], v[54:55], v[54:55]\nv_pk_fma_f32 v[72:73], v[72:73], v[6:7], v[8:9]\n" \
    "v_pk_fma_f32 v[74:75], v[74:75], v[6:7], v[8:9]\nv_pk_fma_f32 v[76:77], v[76:77], v[6:7], v[8:9]\nv_pk_fma_f32 v[78:79], v[78:79], v[6:7], v[8:9]\n" \
    "v_pk_mul_f32 v[72:73], v[72:73], v[48:49]\nv_pk_mul_f32 v[74:75], v[74:75], v[50:51]\nv_pk_mul_f32 v[76:77], v[76:77], v[52:53]\nv_pk_mul_f32 v[78:79], v[78:79], v[54:55]\n" \
    "v_exp_f32 v72, v72\nv_exp_f32 v73, v73\nv_exp_f32 v74, v74\nv_exp_f32 v75, v75\nv_exp_f32 v76, v76\nv_exp_f32 v77, v77\nv_exp_f32 v78, v78\nv_exp_f32 v79, v79\n" \
    "v_pk_add_f32 v[72:73], v[72:73], v[10:11]\nv_pk_add_f32 v[74:75], v[74:75], v[10:11]\nv_pk_add_f32 v[76:77], v[76:77], v[10:11]\nv_pk_add_f32 v[78:79], v[78:79], v[10:11]\n" \
    "v_rcp_f32 v72, v72\nv_rcp_f32 v73, v73\nv_rcp_f32 v74, v74\nv_rcp_f32 v75, v75\nv_rcp_f32 v76, v76\nv_rcp_f32 v77, v77\nv_rcp_f32 v78, v78\nv_rcp_f32 v79, v79\n" \
    "v_pk_mul_f32 v[72:73], v[72:73], v[48:49]\nv_pk_mul_f32 v[74:75], v[74:75], v[50:51]\nv_pk_mul_f32 v[76:77], v[76:77], v[52:53]\nv_pk_mul_f32 v[78:79], v[78:79], v[54:55]\n" \
    "v_cvt_pk_bf16_f32 v64, v72, v73\nv_cvt_pk_bf16_f32 v65, v74, v75\nv_cvt_pk_bf16_f32 v66, v76, v77\nv_cvt_pk_bf16_f32 v67, v78, v79\nv_min_u32 v64, -2, v64\n" \
    "v_min_u32 v65, -2, v65\nv_min_u32 v66, -2, v66\nv_min_u32 v67, -2, v67\nbuffer_store_dwordx4 v[64:67], v0, s[64:67], s82 offen offset:0 sc1\n" \
    "v_pk_fma_f32 v[56:57], v[24:25], v[96:97], v[56:57] op_sel_hi:[1,0,1] neg_lo:[0,1,0] neg_hi:[0,1,0]\n" \
    "v_pk_fma_f32 v[58:59], v[26:27], v[96:97], v[58:59] op_sel_hi:[1,0,1] neg_lo:[0,1,0] neg_hi:[0,1,0]\n" \
    "v_pk_fma_f32 v[60:61], v[28:29], v[96:97], v[60:61] op_sel_hi:[1,0,1] neg_lo:[0,1,0] neg_hi:[0,1,0]\n" \
    "v_pk_fma_f32 v[62:63], v[30:31], v[96:97], v[62:63] op_sel_hi:[1,0,1] neg_lo:[0,1,0] neg_hi:[0,1,0]\nv_pk_fma_f32 v[56:57], v[56:57], v[98:99], v[40:41] op_sel_hi:[1,0,1]\n" \
    "v_pk_fma_f32 v[58:59], v[58:59], v[98:99], v[42:43] op_sel_hi:[1,0,1]\nv_pk_fma_f32 v[60:61], v[60:61], v[98:99], v[44:45] op_sel_hi:[1,0,1]\n" \
    "v_pk_fma_f32 v[62:63], v[62:63], v[98:99], v[46:47] op_sel_hi:[1,0,1]\nv_pk_mul_f32 v[72:73], v[56:57], v[56:57]\nv_pk_mul_f32 v[74:75], v[58:59], v[58:59]\n" \
    "v_pk_mul_f32 v[76:77], v[60:61], v[60:61]\nv_pk_mul_f32 v[78:79], v[62:63], v[62:63]\nv_pk_fma_f32 v[72:73], v[72:73], v[6:7], v[8:9]\n" \
    "v_pk_fma_f32 v[74:75], v[74:75], v[6:7], v[8:9]\nv_pk_fma_f32 v[76:77], v[76:77], v[6:7], v[8:9]\nv_pk_fma_f32 v[78:79], v[78:79], v[6:7], v[8:9]\n" \
    "v_pk_mul_f32 v[72:73], v[72:73], v[56:57]\nv_pk_mul_f32 v[74:75], v[74:75], v[58:59]\nv_pk_mul_f32 v[76:77], v[76:77], v[60:61]\nv_pk_mul_f32 v[78:79], v[78:79], v[62:63]\n" \
    "v_exp_f32 v72, v72\nv_exp_f32 v73, v73\nv_exp_f32 v74, v74\nv_exp_f32 v75, v75\nv_exp_f32 v76, v76\nv_exp_f32 v77, v77\nv_exp_f32 v78, v78\nv_exp_f32 v79, v79\n" \
    "v_pk_add_f32 v[72:73], v[72:73], v[10:11]\nv_pk_add_f32 v[74:75], v[74:75], v[10:11]\nv_pk_add_f32 v[76:77], v[76:77], v[10:11]\nv_pk_add_f32 v[78:79], v[78:79], v[10:11]\n" \
    "v_rcp_f32 v72, v72\nv_rcp_f32 v73, v73\nv_rcp_f32 v74, v74\nv_rcp_f32 v75, v75\nv_rcp_f32 v76, v76\nv_rcp_f32 v77, v77\nv_rcp_f32 v78, v78\nv_rcp_f32 v79, v79\n" \
    "v_pk_mul_f32 v[72:73], v[72:73], v[56:57]\nv_pk_mul_f32 v[74:75], v[74:75], v[58:59]\nv_pk_mul_f32 v[76:77], v[76:77], v[60:61]\nv_pk_mul_f32 v[78:79], v[78:79], v[62:63]\n" \
    "v_cvt_pk_bf16_f32 v68, v72, v73\nv_cvt_pk_bf16_f32 v69, v74, v75\nv_cvt_pk_bf16_f32 v70, v76, v77\nv_cvt_pk_bf16_f32 v71, v78, v79\nv_min_u32 v68, -2, v68\n" \
    "v_min_u32 v69, -2, v69\nv_min_u32 v70, -2, v70\nv_min_u32 v71, -2, v71\nbuffer_store_dwordx4 v[68:71], v0, s[64:67], s82 offen offset:1024 sc1\n.Lf18_%=:\n" \
    "s_mov_b32 s38, s85\ns_xor_b32 s39, s39, 128\ns_xor_b32 s80, s80, 0x10000\nv_xor_b32 v1, 0x10000, v1\ns_mov_b32 s68, 0\ns_add_u32 s32, s32, 1\ns_cmp_lt_u32 s32, 64\n" \
    "s_cbranch_scc1 .Lf2_%=\ns_endpgm\n.Lf4_%=:\ns_sleep 8\ns_add_u32 s68, s68, 1\ns_and_b32 s69, s68, 63\ns_cbranch_scc1 .Lf3_%=\ns_cmp_gt_u32 s68, 0x400000\n" \
    "s_cbranch_scc1 .Lf1_%=\nbuffer_load_dword v253, off, s[64:67], 0 sc1\ns_waitcnt vmcnt(0)\nv_readfirstlane_b32 s69, v253\ns_nop 0\ns_cmp_eq_u32 s69, 1\n" \
    "s_cbranch_scc0 .Lf3_%=\ns_endpgm\n.Lf13_%=:\ns_cmp_ge_u32 s32, 62\ns_cbranch_scc1 .Lf14_%=\ns_sleep 10\n.Lf14_%=:\ns_sleep 3\n" \
    "buffer_load_dword v151, v2, s[64:67], s82 offen sc1\ns_waitcnt vmcnt(0)\nv_cmp_eq_u32 vcc, -1, v151\ns_nop 0\ns_cbranch_vccz .Lf12_%=\ns_add_u32 s68, s68, 1\n" \
    "s_and_b32 s69, s68, 63\ns_cbranch_scc1 .Lf13_%=\ns_cmp_gt_u32 s68, 0x400000\ns_cbranch_scc1 .Lf1_%=\nbuffer_load_dword v253, off, s[64:67], 0 sc1\ns_waitcnt vmcnt(0)\n" \
    "v_readfirstlane_b32 s69, v253\ns_nop 0\ns_cmp_eq_u32 s69, 1\ns_cbranch_scc0 .Lf13_%=\ns_endpgm\n.Lf17_%=:\ns_xor_b32 s84, s84, 0x10000\n" \
    "buffer_load_dword v151, v2, s[64:67], s83 offen sc1\nv_pk_fma_f32 v[48:49], v[16:17], v[96:97], v[48:49] op_sel_hi:[1,0,1] neg_lo:[0,1,0] neg_hi:[0,1,0]\n" \
    "v_pk_fma_f32 v[50:51], v[18:19], v[96:97], v[50:51] op_sel_hi:[1,0,1] neg_lo:[0,1,0] neg_hi:[0,1,0]\n" \
    "v_pk_fma_f32 v[52:53], v[20:21], v[96:97], v[52:53] op_sel_hi:[1,0,1] neg_lo:[0,1,0] neg_hi:[0,1,0]\n" \
    "v_pk_fma_f32 v[54:55], v[22:23], v[96:97], v[54:55] op_sel_hi:[1,0,1] neg_lo:[0,1,0] neg_hi:[0,1,0]\nv_pk_fma_f32 v[48:49], v[48:49], v[98:99], v[32:33] op_sel_hi:[1,0,1]\n" \
    "v_pk_fma_f32 v[50:51], v[50:51], v[98:99], v[34:35] op_sel_hi:[1,0,1]\nv_pk_fma_f32 v[52:53], v[52:53], v[98:99], v[36:37] op_sel_hi:[1,0,1]\n" \
    "v_pk_fma_f32 v[54:55], v[54:55], v[98:99], v[38:39] op_sel_hi:[1,0,1]\nv_pk_mul_f32 v[72:73], v[48:49], v[48:49]\nv_pk_mul_f32 v[74:75], v[50:51], v[50:51]\n" \
    "v_pk_mul_f32 v[76:77], v[52:53], v[52:53]\nv_pk_mul_f32 v[78:79], v[54:55], v[54:55]\ns_add_u32 m0, s84, 0\ns_add_u32 s69, s83, s48\n" \
    "buffer_load_dwordx4 v0, s[64:67], s69 offen offset:0 sc1 lds\nv_pk_fma_f32 v[72:73], v[72:73], v[6:7], v[8:9]\nv_pk_fma_f32 v[74:75], v[74:75], v[6:7], v[8:9]\n" \
    "v_pk_fma_f32 v[76:77], v[76:77], v[6:7], v[8:9]\nv_pk_fma_f32 v[78:79], v[78:79], v[6:7], v[8:9]\nv_pk_mul_f32 v[72:73], v[72:73], v[48:49]\n" \
    "v_pk_mul_f32 v[74:75], v[74:75], v[50:51]\nv_pk_mul_f32 v[76:77], v[76:77], v[52:53]\nv_pk_mul_f32 v[78:79], v[78:79], v[54:55]\nv_exp_f32 v72, v72\nv_exp_f32 v73, v73\n" \
    "v_exp_f32 v74, v74\nv_exp_f32 v75, v75\nbuffer_load_dwordx4 v0, s[64:67], s69 offen offset:1024 sc1 lds\nv_exp_f32 v76, v76\nv_exp_f32 v77, v77\nv_exp_f32 v78, v78\n" \
    "v_exp_f32 v79, v79\nv_pk_add_f32 v[72:73], v[72:73], v[10:11]\nv_pk_add_f32 v[74:75], v[74:75], v[10:11]\nv_pk_add_f32 v[76:77], v[76:77], v[10:11]\n" \
    "v_pk_add_f32 v[78:79], v[78:79], v[10:11]\nv_rcp_f32 v72, v72\nv_rcp_f32 v73, v73\nv_rcp_f32 v74, v74\nv_rcp_f32 v75, v75\ns_add_u32 m0, s84, 8192\n" \
    "s_add_u32 s69, s83, s49\nbuffer_load_dwordx4 v0, s[64:67], s69 offen offset:0 sc1 lds\nv_rcp_f32 v76, v76\nv_rcp_f32 v77, v77\nv_rcp_f32 v78, v78\nv_rcp_f32 v79, v79\n" \
    "v_pk_mul_f32 v[72:73], v[72:73], v[48:49]\nv_pk_mul_f32 v[74:75], v[74:75], v[50:51]\nv_pk_mul_f32 v[76:77], v[76:77], v[52:53]\nv_pk_mul_f32 v[78:79], v[78:79], v[54:55]\n" \
    "v_cvt_pk_bf16_f32 v64, v72, v73\nv_cvt_pk_bf16_f32 v65, v74, v75\nv_cvt_pk_bf16_f32 v66, v76, v77\nv_cvt_pk_bf16_f32 v67, v78, v79\n" \
    "buffer_load_dwordx4 v0, s[64:67], s69 offen offset:1024 sc1 lds\nv_min_u32 v64, -2, v64\nv_min_u32 v65, -2, v65\nv_min_u32 v66, -2, v66\nv_min_u32 v67, -2, v67\n" \
    "buffer_store_dwordx4 v[64:67], v0, s[64:67], s82 offen offset:0 sc1\nv_pk_fma_f32 v[56:57], v[24:25], v[96:97], v[56:57] op_sel_hi:[1,0,1] neg_lo:[0,1,0] neg_hi:[0,1,0]\n" \
    "v_pk_fma_f32 v[58:59], v[26:27], v[96:97], v[58:59] op_sel_hi:[1,0,1] neg_lo:[0,1,0] neg_hi:[0,1,0]\n" \
    "v_pk_fma_f32 v[60:61], v[28:29], v[96:97], v[60:61] op_sel_hi:[1,0,1] neg_lo:[0,1,0] neg_hi:[0,1,0]\n" \
    "v_pk_fma_f32 v[62:63], v[30:31], v[96:97], v[62:63] op_sel_hi:[1,0,1] neg_lo:[0,1,0] neg_hi:[0,1,0]\nv_pk_fma_f32 v[56:57], v[56:57], v[98:99], v[40:41] op_sel_hi:[1,0,1]\n" \
    "v_pk_fma_f32 v[58:59], v[58:59], v[98:99], v[42:43] op_sel_hi:[1,0,1]\nv_pk_fma_f32 v[60:61], v[60:61], v[98:99], v[44:45] op_sel_hi:[1,0,1]\ns_add_u32 m0, s84, 16384\n" \
    "s_add_u32 s69, s83, s50\nbuffer_load_dwordx4 v0, s[64:67], s69 offen offset:0 sc1 lds\nv_pk_fma_f32 v[62:63], v[62:63], v[98:99], v[46:47] op_sel_hi:[1,0,1]\n" \
    "v_pk_mul_f32 v[72:73], v[56:57], v[56:57]\nv_pk_mul_f32 v[74:75], v[58:59], v[58:59]\nv_pk_mul_f32 v[76:77], v[60:61], v[60:61]\nv_pk_mul_f32 v[78:79], v[62:63], v[62:63]\n" \
    "v_pk_fma_f32 v[72:73], v[72:73], v[6:7], v[8:9]\nv_pk_fma_f32 v[74:75], v[74:75], v[6:7], v[8:9]\nv_pk_fma_f32 v[76:77], v[76:77], v[6:7], v[8:9]\n" \
    "v_pk_fma_f32 v[78:79], v[78:79], v[6:7], v[8:9]\nv_pk_mul_f32 v[72:73], v[72:73], v[56:57]\nv_pk_mul_f32 v[74:75], v[74:75], v[58:59]\n" \
    "v_pk_mul_f32 v[76:77], v[76:77], v[60:61]\nbuffer_load_dwordx4 v0, s[64:67], s69 offen offset:1024 sc1 lds\nv_pk_mul_f32 v[78:79], v[78:79], v[62:63]\nv_exp_f32 v72, v72\n" \
    "v_exp_f32 v73, v73\nv_exp_f32 v74, v74\nv_exp_f32 v75, v75\nv_exp_f32 v76, v76\nv_exp_f32 v77, v77\nv_exp_f32 v78, v78\nv_exp_f32 v79, v79\n" \
    "v_pk_add_f32 v[72:73], v[72:73], v[10:11]\nv_pk_add_f32 v[74:75], v[74:75], v[10:11]\nv_pk_add_f32 v[76:77], v[76:77], v[10:11]\nv_pk_add_f32 v[78:79], v[78:79], v[10:11]\n" \
    "v_rcp_f32 v72, v72\nv_rcp_f32 v73, v73\nv_rcp_f32 v74, v74\nv_rcp_f32 v75, v75\nv_rcp_f32 v76, v76\nv_rcp_f32 v77, v77\nv_rcp_f32 v78, v78\nv_rcp_f32 v79, v79\n" \
    "v_pk_mul_f32 v[72:73], v[72:73], v[56:57]\nv_pk_mul_f32 v[74:75], v[74:75], v[58:59]\nv_pk_mul_f32 v[76:77], v[76:77], v[60:61]\nv_pk_mul_f32 v[78:79], v[78:79], v[62:63]\n" \
    "v_cvt_pk_bf16_f32 v68, v72, v73\nv_cvt_pk_bf16_f32 v69, v74, v75\nv_cvt_pk_bf16_f32 v70, v76, v77\nv_cvt_pk_bf16_f32 v71, v78, v79\nv_min_u32 v68, -2, v68\n" \
    "v_min_u32 v69, -2, v69\nv_min_u32 v70, -2, v70\nv_min_u32 v71, -2, v71\nbuffer_store_dwordx4 v[68:71], v0, s[64:67], s82 offen offset:1024 sc1\ns_branch .Lf18_%=\n" \
    ".Lf7_%=:\nv_mov_b32 v120, 0\nv_mov_b32 v121, v1\ns_mov_b32 s76, 16\n.Lf10_%=:\nds_read_b128 v[124:127], v121 offset:0\nds_read_b128 v[128:131], v121 offset:1024\n" \
    "ds_read_b128 v[132:135], v121 offset:2048\nds_read_b128 v[136:139], v121 offset:3072\nv_add_u32 v121, 0x1000, v121\ns_waitcnt lgkmcnt(0)\n" \
    "v_max3_u32 v120, v120, v124, v125\nv_max3_u32 v120, v120, v126, v127\nv_max3_u32 v120, v120, v128, v129\nv_max3_u32 v120, v120, v130, v131\n" \
    "v_max3_u32 v120, v120, v132, v133\nv_max3_u32 v120, v120, v134, v135\nv_max3_u32 v120, v120, v136, v137\nv_max3_u32 v120, v120, v138, v139\ns_sub_u32 s76, s76, 1\n" \
    "s_cmp_lg_u32 s76, 0\ns_cbranch_scc1 .Lf10_%=\nv_cmp_eq_u32 vcc, -1, v120\ns_nop 0\ns_cbranch_vccz .Lf8_%=\n.Lf9_%=:\ns_mov_b32 s81, 0\ns_barrier\ns_sleep 4\n" \
    "s_add_u32 s68, s68, 1\ns_and_b32 s69, s68, 63\ns_cbranch_scc1 .Lf2_%=\ns_cmp_gt_u32 s68, 0x400000\ns_cbranch_scc1 .Lf1_%=\nbuffer_load_dword v253, off, s[64:67], 0 sc1\n" \
    "s_waitcnt vmcnt(0)\nv_readfirstlane_b32 s69, v253\ns_nop 0\ns_cmp_eq_u32 s69, 1\ns_cbranch_scc0 .Lf2_%=\ns_endpgm\n.Lf1_%=:\nv_mov_b32 v253, 1\n" \
    "buffer_store_dword v253, off, s[64:67], 0 sc1\ns_endpgm\n"
#define ASM_F2 \
    "s_mov_b32 s90, 0x7fff0000\ns_mov_b32 s91, 0x7fff0000\ns_mov_b32 s92, 0x7fff0000\ns_mov_b32 s93, 0x7fff0000\ns_mov_b32 s80, 0\ns_mov_b32 s81, 0\ns_mov_b32 s68, 0\n.Lg2_%=:\n" \
    "s_xor_b32 s85, s38, 128\ns_and_b32 s69, s32, 63\ns_lshl_b32 s69, s69, 19\ns_add_u32 s82, s36, s69\ns_lshr_b32 s69, s82, 8\ns_and_b32 s69, s69, 0x2000\n" \
    "s_xor_b32 s82, s82, s69\ns_add_u32 s69, s32, 1\ns_and_b32 s69, s69, 63\ns_lshl_b32 s69, s69, 19\ns_add_u32 s83, s36, s69\ns_lshr_b32 s69, s83, 8\n" \
    "s_and_b32 s69, s69, 0x2000\ns_xor_b32 s83, s83, s69\ns_add_u32 s84, s37, s80\ns_cmp_eq_u32 s81, 1\ns_cbranch_scc1 .Lg6_%=\ns_add_u32 s77, s32, 1\ns_cmp_eq_u32 s81, 2\n" \
    "s_cbranch_scc1 .Lg5_%=\n.Lg3_%=:\nbuffer_load_dword v150, v3, s[64:67], s38 offen sc1\ns_waitcnt vmcnt(0)\nv_and_b32 v112, 127, v150\nv_cmp_gt_u32 vcc, s77, v112\ns_nop 0\n" \
    "s_cbranch_vccnz .Lg4_%=\n.Lg5_%=:\nv_and_b32 v112, 127, v150\n.Lg11_%=:\ns_memrealtime s[72:73]\ns_waitcnt lgkmcnt(0)\ns_cmp_ge_u32 s77, 64\ns_cselect_b32 s69, s60, s34\n" \
    "s_sub_u32 s72, s72, s69\ns_lshl_b32 s72, s72, 8\nv_sub_u32 v113, s72, v150\nv_cmp_gt_i32 s[74:75], 0, v113\nv_cmp_eq_u32 vcc, s77, v112\ns_nop 0\n" \
    "s_and_b64 vcc, vcc, s[74:75]\ns_cbranch_scc1 .Lg11_%=\n.Lg12_%=:\nbuffer_load_dword v151, v2, s[64:67], s82 offen sc1\ns_add_u32 m0, s84, 0\ns_add_u32 s69, s82, s48\n" \
    "buffer_load_dwordx4 v0, s[64:67], s69 offen offset:0 sc1 lds\nbuffer_load_dwordx4 v0, s[64:67], s69 offen offset:1024 sc1 lds\ns_add_u32 m0, s84, 8192\n" \
    "s_add_u32 s69, s82, s49\nbuffer_load_dwordx4 v0, s[64:67], s69 offen offset:0 sc1 lds\nbuffer_load_dwordx4 v0, s[64:67], s69 offen offset:1024 sc1 lds\n" \
    "s_add_u32 m0, s84, 16384\ns_add_u32 s69, s82, s50\nbuffer_load_dwordx4 v0, s[64:67], s69 offen offset:0 sc1 lds\n" \
    "buffer_load_dwordx4 v0, s[64:67], s69 offen offset:1024 sc1 lds\n.Lg6_%=:\ns_waitcnt vmcnt(6)\nv_cmp_eq_u32 vcc, -1, v151\ns_nop 0\ns_cbranch_vccnz .Lg13_%=\n" \
    "s_add_u32 s77, s32, 2\ns_add_u32 m0, s84, 24576\ns_add_u32 s69, s82, s51\nbuffer_load_dwordx4 v0, s[64:67], s69 offen offset:0 sc1 lds\n" \
    "buffer_load_dwordx4 v0, s[64:67], s69 offen offset:1024 sc1 lds\ns_waitcnt vmcnt(6)\ns_barrier\nds_read_b128 v[64:67], v1 offset:0\nds_read_b128 v[68:71], v1 offset:1024\n" \
    "ds_read_b128 v[72:75], v1 offset:2048\nds_read_b128 v[76:79], v1 offset:3072\ns_waitcnt lgkmcnt(3)\nv_mfma_f32_32x32x16_bf16 v[48:63], a[0:3], v[64:67], 0\n" \
    "ds_read_b128 v[80:83], v1 offset:4096\ns_add_u32 m0, s84, 32768\ns_add_u32 s69, s82, s52\nbuffer_load_dwordx4 v0, s[64:67], s69 offen offset:0 sc1 lds\n" \
    "s_waitcnt lgkmcnt(3)\nv_mfma_f32_32x32x16_bf16 v[48:63], a[4:7], v[68:71], v[48:63]\nds_read_b128 v[84:87], v1 offset:5120\ns_waitcnt lgkmcnt(3)\n" \
    "v_mfma_f32_32x32x16_bf16 v[48:63], a[8:11], v[72:75], v[48:63]\nds_read_b128 v[88:91], v1 offset:6144\nbuffer_load_dwordx4 v0, s[64:67], s69 offen offset:1024 sc1 lds\n" \
    "s_waitcnt lgkmcnt(3)\nv_mfma_f32_32x32x16_bf16 v[48:63], a[12:15], v[76:79], v[48:63]\nds_read_b128 v[92:95], v1 offset:7168\ns_waitcnt vmcnt(6) lgkmcnt(3)\ns_barrier\n" \
    "v_mfma_f32_32x32x16_bf16 v[48:63], a[16:19], v[80:83], v[48:63]\nds_read_b128 v[64:67], v1 offset:8192\ns_add_u32 m0, s84, 40960\ns_add_u32 s69, s82, s53\n" \
    "buffer_load_dwordx4 v0, s[64:67], s69 offen offset:0 sc1 lds\ns_waitcnt lgkmcnt(3)\nv_mfma_f32_32x32x16_bf16 v[48:63], a[20:23], v[84:87], v[48:63]\n" \
    "ds_read_b128 v[68:71], v1 offset:9216\ns_waitcnt lgkmcnt(3)\nv_mfma_f32_32x32x16_bf16 v[48:63], a[24:27], v[88:91], v[48:63]\nds_read_b128 v[72:75], v1 offset:10240\n" \
    "buffer_load_dwordx4 v0, s[64:67], s69 offen offset:1024 sc1 lds\ns_waitcnt lgkmcnt(3)\nv_mfma_f32_32x32x16_bf16 v[48:63], a[28:31], v[92:95], v[48:63]\n" \
    "ds_read_b128 v[76:79], v1 offset:11264\ns_waitcnt lgkmcnt(3)\nv_mfma_f32_32x32x16_bf16 v[48:63], a[32:35], v[64:67], v[48:63]\nds_read_b128 v[80:83], v1 offset:12288\n" \
    "s_add_u32 m0, s84, 49152\ns_add_u32 s69, s82, s54\nbuffer_load_dwordx4 v0, s[64:67], s69 offen offset:0 sc1 lds\ns_waitcnt lgkmcnt(3)\n" \
    "v_mfma_f32_32x32x16_bf16 v[48:63], a[36:39], v[68:71], v[48:63]\nds_read_b128 v[84:87], v1 offset:13312\ns_waitcnt lgkmcnt(3)\n" \
    "v_mfma_f32_32x32x16_bf16 v[48:63], a[40:43], v[72:75], v[48:63]\nds_read_b128 v[88:91], v1 offset:14336\nbuffer_load_dwordx4 v0, s[64:67], s69 offen offset:1024 sc1 lds\n" \
    "s_waitcnt lgkmcnt(3)\nv_mfma_f32_32x32x16_bf16 v[48:63], a[44:47], v[76:79], v[48:63]\nds_read_b128 v[92:95], v1 offset:15360\ns_waitcnt vmcnt(6) lgkmcnt(3)\ns_barrier\n" \
    "v_mfma_f32_32x32x16_bf16 v[48:63], a[48:51], v[80:83], v[48:63]\nds_read_b128 v[64:67], v1 offset:16384\ns_and_b32 s69, s32, 3\ns_lshl_b32 s69, s69, 19\n" \
    "s_add_u32 s86, s56, s69\ns_lshr_b32 s69, s86, 8\ns_and_b32 s69, s69, 0x2000\ns_waitcnt lgkmcnt(3)\nv_mfma_f32_32x32x16_bf16 v[48:63], a[52:55], v[84:87], v[48:63]\n" \
    "ds_read_b128 v[68:71], v1 offset:17408\ns_xor_b32 s86, s86, s69\ns_and_b32 s69, s32, 3\ns_lshl_b32 s69, s69, 19\ns_add_u32 s87, s57, s69\ns_lshr_b32 s69, s87, 8\n" \
    "s_waitcnt lgkmcnt(3)\nv_mfma_f32_32x32x16_bf16 v[48:63], a[56:59], v[88:91], v[48:63]\nds_read_b128 v[72:75], v1 offset:18432\ns_and_b32 s69, s69, 0x2000\n" \
    "s_xor_b32 s87, s87, s69\ns_and_b32 s69, s32, 3\ns_lshl_b32 s69, s69, 19\ns_add_u32 s88, s58, s69\ns_waitcnt lgkmcnt(3)\n" \
    "v_mfma_f32_32x32x16_bf16 v[48:63], a[60:63], v[92:95], v[48:63]\nds_read_b128 v[76:79], v1 offset:19456\ns_lshr_b32 s69, s88, 8\ns_and_b32 s69, s69, 0x2000\n" \
    "s_xor_b32 s88, s88, s69\ns_and_b32 s69, s32, 3\ns_lshl_b32 s69, s69, 19\ns_waitcnt lgkmcnt(3)\nv_mfma_f32_32x32x16_bf16 v[48:63], a[64:67], v[64:67], v[48:63]\n" \
    "ds_read_b128 v[80:83], v1 offset:20480\ns_add_u32 s89, s59, s69\ns_lshr_b32 s69, s89, 8\ns_and_b32 s69, s69, 0x2000\ns_xor_b32 s89, s89, s69\ns_bfe_u32 s78, s32, 0x10002\n" \
    "s_waitcnt lgkmcnt(3)\nv_mfma_f32_32x32x16_bf16 v[48:63], a[68:71], v[68:71], v[48:63]\nds_read_b128 v[84:87], v1 offset:21504\ns_waitcnt lgkmcnt(3)\n" \
    "v_mfma_f32_32x32x16_bf16 v[48:63], a[72:75], v[72:75], v[48:63]\nds_read_b128 v[88:91], v1 offset:22528\nbuffer_load_dwordx2 v[108:109], v4, s[64:67], s93 offen sc1\n" \
    "s_waitcnt lgkmcnt(3)\nv_mfma_f32_32x32x16_bf16 v[48:63], a[76:79], v[76:79], v[48:63]\nds_read_b128 v[92:95], v1 offset:23552\n" \
    "buffer_load_dwordx4 v[96:99], v0, s[64:67], s90 offen sc1\ns_waitcnt lgkmcnt(3)\nv_mfma_f32_32x32x16_bf16 v[48:63], a[80:83], v[80:83], v[48:63]\n" \
    "ds_read_b128 v[64:67], v1 offset:24576\nbuffer_load_dwordx4 v[100:103], v0, s[64:67], s91 offen sc1\ns_waitcnt lgkmcnt(3)\n" \
    "v_mfma_f32_32x32x16_bf16 v[48:63], a[84:87], v[84:87], v[48:63]\nds_read_b128 v[68:71], v1 offset:25600\nbuffer_load_dwordx4 v[104:107], v0, s[64:67], s92 offen sc1\n" \
    "s_waitcnt lgkmcnt(3)\nv_mfma_f32_32x32x16_bf16 v[48:63], a[88:91], v[88:91], v[48:63]\nds_read_b128 v[72:75], v1 offset:26624\ns_waitcnt lgkmcnt(3)\n" \
    "v_mfma_f32_32x32x16_bf16 v[48:63], a[92:95], v[92:95], v[48:63]\nds_read_b128 v[76:79], v1 offset:27648\ns_waitcnt lgkmcnt(3)\n" \
    "v_mfma_f32_32x32x16_bf16 v[48:63], a[96:99], v[64:67], v[48:63]\nds_read_b128 v[80:83], v1 offset:28672\ns_waitcnt lgkmcnt(3)\n" \
    "v_mfma_f32_32x32x16_bf16 v[48:63], a[100:103], v[68:71], v[48:63]\nds_read_b128 v[84:87], v1 offset:29696\ns_waitcnt lgkmcnt(3)\n" \
    "v_mfma_f32_32x32x16_bf16 v[48:63], a[104:107], v[72:75], v[48:63]\nds_read_b128 v[88:91], v1 offset:30720\ns_waitcnt lgkmcnt(3)\n" \
    "v_mfma_f32_32x32x16_bf16 v[48:63], a[108:111], v[76:79], v[48:63]\nds_read_b128 v[92:95], v1 offset:31744\ns_waitcnt vmcnt(6) lgkmcnt(3)\ns_barrier\n" \
    "v_mfma_f32_32x32x16_bf16 v[48:63], a[112:115], v[80:83], v[48:63]\nds_read_b128 v[64:67], v1 offset:32768\ns_waitcnt lgkmcnt(3)\n" \
    "v_mfma_f32_32x32x16_bf16 v[48:63], a[116:119], v[84:87], v[48:63]\nds_read_b128 v[68:71], v1 offset:33792\ns_waitcnt lgkmcnt(3)\n" \
    "v_mfma_f32_32x32x16_bf16 v[48:63], a[120:123], v[88:91], v[48:63]\nds_read_b128 v[72:75], v1 offset:34816\ns_waitcnt lgkmcnt(3)\n" \
    "v_mfma_f32_32x32x16_bf16 v[48:63], a[124:127], v[92:95], v[48:63]\nds_read_b128 v[76:79], v1 offset:35840\ns_waitcnt lgkmcnt(3)\n" \
    "v_mfma_f32_32x32x16_bf16 v[48:63], a[128:131], v[64:67], v[48:63]\nds_read_b128 v[80:83], v1 offset:36864\nbuffer_load_dword v150, v3, s[64:67], s85 offen sc1\n" \
    "s_waitcnt lgkmcnt(3)\nv_mfma_f32_32x32x16_bf16 v[48:63], a[132:135], v[68:71], v[48:63]\nds_read_b128 v[84:87], v1 offset:37888\ns_waitcnt lgkmcnt(3)\n" \
    "v_mfma_f32_32x32x16_bf16 v[48:63], a[136:139], v[72:75], v[48:63]\nds_read_b128 v[88:91], v1 offset:38912\ns_waitcnt lgkmcnt(3)\n" \
    "v_mfma_f32_32x32x16_bf16 v[48:63], a[140:143], v[76:79], v[48:63]\nds_read_b128 v[92:95], v1 offset:39936\ns_waitcnt lgkmcnt(3)\n" \
    "v_mfma_f32_32x32x16_bf16 v[48:63], a[144:147], v[80:83], v[48:63]\nds_read_b128 v[64:67], v1 offset:40960\ns_waitcnt lgkmcnt(3)\n" \
    "v_mfma_f32_32x32x16_bf16 v[48:63], a[148:151], v[84:87], v[48:63]\nds_read_b128 v[68:71], v1 offset:41984\ns_waitcnt vmcnt(1)\ns_waitcnt lgkmcnt(3)\n" \
    "v_max_u32 v116, v108, v109\nv_cmp_eq_u32 s[70:71], -1, v116\nv_mfma_f32_32x32x16_bf16 v[48:63], a[152:155], v[88:91], v[48:63]\nds_read_b128 v[72:75], v1 offset:43008\n" \
    "s_waitcnt lgkmcnt(3)\nv_cndmask_b32 v116, 0, 1, s[70:71]\nv_xor_b32 v96, s79, v96\nv_xor_b32 v97, s79, v97\nv_xor_b32 v98, s79, v98\n" \
    "v_mfma_f32_32x32x16_bf16 v[48:63], a[156:159], v[92:95], v[48:63]\nds_read_b128 v[76:79], v1 offset:44032\ns_waitcnt lgkmcnt(3)\nv_xor_b32 v99, s79, v99\n" \
    "v_xor_b32 v100, s79, v100\nv_xor_b32 v101, s79, v101\nv_xor_b32 v102, s79, v102\nv_mfma_f32_32x32x16_bf16 v[48:63], a[160:163], v[64:67], v[48:63]\n" \
    "ds_read_b128 v[80:83], v1 offset:45056\ns_waitcnt lgkmcnt(3)\nv_xor_b32 v103, s79, v103\nv_xor_b32 v104, s79, v104\nv_xor_b32 v105, s79, v105\nv_xor_b32 v106, s79, v106\n" \
    "v_mfma_f32_32x32x16_bf16 v[48:63], a[164:167], v[68:71], v[48:63]\nds_read_b128 v[84:87], v1 offset:46080\ns_waitcnt lgkmcnt(3)\nv_xor_b32 v107, s79, v107\n" \
    "v_or3_b32 v116, v116, v96, v97\nv_or3_b32 v116, v116, v98, v99\nv_or3_b32 v116, v116, v100, v101\nv_mfma_f32_32x32x16_bf16 v[48:63], a[168:171], v[72:75], v[48:63]\n" \
    "ds_read_b128 v[88:91], v1 offset:47104\ns_waitcnt lgkmcnt(3)\nv_or3_b32 v116, v116, v102, v103\nv_or3_b32 v116, v116, v104, v105\nv_or3_b32 v116, v116, v106, v107\n" \
    "v_and_b32 v116, 1, v116\nv_mfma_f32_32x32x16_bf16 v[48:63], a[172:175], v[76:79], v[48:63]\nds_read_b128 v[92:95], v1 offset:48128\ns_waitcnt lgkmcnt(3)\ns_barrier\n" \
    "v_cmp_eq_u32 s[70:71], 1, v116\nv_add_f32 v96, v96, v16\nv_add_f32 v97, v97, v17\nv_add_f32 v98, v98, v18\n" \
    "v_mfma_f32_32x32x16_bf16 v[48:63], a[176:179], v[80:83], v[48:63]\nds_read_b128 v[64:67], v1 offset:49152\ns_waitcnt lgkmcnt(3)\nv_add_f32 v99, v99, v19\n" \
    "v_add_f32 v100, v100, v104\nv_add_f32 v101, v101, v105\nv_add_f32 v102, v102, v106\nv_mfma_f32_32x32x16_bf16 v[48:63], a[180:183], v[84:87], v[48:63]\n" \
    "ds_read_b128 v[68:71], v1 offset:50176\ns_waitcnt lgkmcnt(3)\nv_add_f32 v103, v103, v107\nv_add_f32 v96, v96, v100\nv_add_f32 v97, v97, v101\nv_add_f32 v98, v98, v102\n" \
    "v_mfma_f32_32x32x16_bf16 v[48:63], a[184:187], v[88:91], v[48:63]\nds_read_b128 v[72:75], v1 offset:51200\ns_waitcnt lgkmcnt(3)\nv_add_f32 v99, v99, v103\n" \
    "v_cvt_pk_bf16_f32 v96, 0, v96\nv_cvt_pk_bf16_f32 v97, 0, v97\nv_cvt_pk_bf16_f32 v98, 0, v98\nv_mfma_f32_32x32x16_bf16 v[48:63], a[188:191], v[92:95], v[48:63]\n" \
    "ds_read_b128 v[76:79], v1 offset:52224\ns_waitcnt lgkmcnt(3)\nv_cvt_pk_bf16_f32 v99, 0, v99\nv_add_f32 v96, v96, v8\nv_add_f32 v97, v97, v9\nv_add_f32 v98, v98, v10\n" \
    "v_mfma_f32_32x32x16_bf16 v[48:63], a[192:195], v[64:67], v[48:63]\nds_read_b128 v[80:83], v1 offset:53248\ns_waitcnt lgkmcnt(3)\nv_add_f32 v99, v99, v11\n" \
    "v_cvt_pk_bf16_f32 v96, 0, v96\nv_cvt_pk_bf16_f32 v97, 0, v97\nv_cvt_pk_bf16_f32 v98, 0, v98\nv_mfma_f32_32x32x16_bf16 v[48:63], a[196:199], v[68:71], v[48:63]\n" \
    "ds_read_b128 v[84:87], v1 offset:54272\ns_waitcnt lgkmcnt(3)\nv_cvt_pk_bf16_f32 v99, 0, v99\nv_mul_f32 v96, v96, v12\nv_mul_f32 v97, v97, v13\nv_mul_f32 v98, v98, v14\n" \
    "v_mfma_f32_32x32x16_bf16 v[48:63], a[200:203], v[72:75], v[48:63]\nds_read_b128 v[88:91], v1 offset:55296\ns_waitcnt vmcnt(0)\ns_waitcnt lgkmcnt(3)\n" \
    "v_and_b32 v112, 127, v150\nv_cmp_gt_u32 vcc, s77, v112\nv_mul_f32 v99, v99, v15\nv_cvt_pk_bf16_f32 v96, 0, v96\n" \
    "v_mfma_f32_32x32x16_bf16 v[48:63], a[204:207], v[76:79], v[48:63]\nds_read_b128 v[92:95], v1 offset:56320\ns_cmp_eq_u64 vcc, 0\ns_cselect_b32 s81, 1, 0\n" \
    "s_waitcnt lgkmcnt(3)\nv_cvt_pk_bf16_f32 v97, 0, v97\nv_cvt_pk_bf16_f32 v98, 0, v98\nv_cvt_pk_bf16_f32 v99, 0, v99\nv_lshlrev_b32 v100, 16, v108\n" \
    "v_mfma_f32_32x32x16_bf16 v[48:63], a[208:211], v[80:83], v[48:63]\ns_waitcnt lgkmcnt(2)\nv_and_b32 v101, 0xffff0000, v108\nv_lshlrev_b32 v102, 16, v109\n" \
    "v_and_b32 v103, 0xffff0000, v109\nv_add_f32 v96, v96, v100\nv_mfma_f32_32x32x16_bf16 v[48:63], a[212:215], v[84:87], v[48:63]\ns_waitcnt lgkmcnt(0)\n" \
    "s_memrealtime s[72:73]\nv_add_f32 v97, v97, v101\nv_add_f32 v98, v98, v102\nv_add_f32 v99, v99, v103\nv_cvt_pk_bf16_f32 v116, v96, v97\n" \
    "v_mfma_f32_32x32x16_bf16 v[48:63], a[216:219], v[88:91], v[48:63]\nv_cvt_pk_bf16_f32 v117, v98, v99\nv_mfma_f32_32x32x16_bf16 v[48:63], a[220:223], v[92:95], v[48:63]\n" \
    "s_waitcnt lgkmcnt(0)\ns_cmp_ge_u32 s77, 64\ns_cselect_b32 s69, s60, s34\ns_sub_u32 s72, s72, s69\ns_lshl_b32 s72, s72, 8\nv_sub_u32 v113, s72, v150\n" \
    "v_cmp_gt_i32 s[74:75], 0, v113\nv_cmp_eq_u32 vcc, s77, v112\ns_nop 0\ns_and_b64 vcc, vcc, s[74:75]\ns_cselect_b32 s69, 1, 0\ns_lshl_b32 s81, s81, s69\ns_nop 7\n" \
    "s_cmp_eq_u32 s90, 0x7fff0000\ns_cbranch_scc1 .Lg16_%=\ns_cmp_lg_u64 s[70:71], 0\ns_cbranch_scc1 .Lg17_%=\n.Lg18_%=:\nglobal_store_dwordx2 v5, v[116:117], s[46:47]\n" \
    "v_add_u32 v5, 0x10000, v5\ns_mov_b32 s90, 0x7fff0000\ns_mov_b32 s91, 0x7fff0000\ns_mov_b32 s92, 0x7fff0000\ns_mov_b32 s93, 0x7fff0000\n.Lg16_%=:\ns_nop 1\n" \
    "v_cmp_u_f32 vcc, v48, v48\ns_nop 0\ns_cbranch_vccnz .Lg7_%=\n.Lg8_%=:\nv_and_or_b32 v52, v52, -2, s78\nv_and_or_b32 v53, v53, -2, s78\nv_and_or_b32 v54, v54, -2, s78\n" \
    "v_and_or_b32 v55, v55, -2, s78\nbuffer_store_dwordx4 v[52:55], v0, s[64:67], s87 offen sc1\nv_and_or_b32 v56, v56, -2, s78\nv_and_or_b32 v57, v57, -2, s78\n" \
    "v_and_or_b32 v58, v58, -2, s78\nv_and_or_b32 v59, v59, -2, s78\nbuffer_store_dwordx4 v[56:59], v0, s[64:67], s88 offen sc1\nv_and_or_b32 v60, v60, -2, s78\n" \
    "v_and_or_b32 v61, v61, -2, s78\nv_and_or_b32 v62, v62, -2, s78\nv_and_or_b32 v63, v63, -2, s78\nbuffer_store_dwordx4 v[60:63], v0, s[64:67], s89 offen sc1\n" \
    "s_cmp_lg_u32 s81, 1\ns_cbranch_scc1 .Lg21_%=\ns_xor_b32 s84, s84, 0x10000\nbuffer_load_dword v151, v2, s[64:67], s83 offen sc1\ns_add_u32 m0, s84, 0\n" \
    "s_add_u32 s69, s83, s48\nbuffer_load_dwordx4 v0, s[64:67], s69 offen offset:0 sc1 lds\nbuffer_load_dwordx4 v0, s[64:67], s69 offen offset:1024 sc1 lds\n" \
    "s_add_u32 m0, s84, 8192\ns_add_u32 s69, s83, s49\nbuffer_load_dwordx4 v0, s[64:67], s69 offen offset:0 sc1 lds\n" \
    "buffer_load_dwordx4 v0, s[64:67], s69 offen offset:1024 sc1 lds\ns_add_u32 m0, s84, 16384\ns_add_u32 s69, s83, s50\n" \
    "buffer_load_dwordx4 v0, s[64:67], s69 offen offset:0 sc1 lds\nbuffer_load_dwordx4 v0, s[64:67], s69 offen offset:1024 sc1 lds\n.Lg21_%=:\nv_mov_b64 v[16:17], v[48:49]\n" \
    "v_mov_b64 v[18:19], v[50:51]\ns_add_u32 s90, s86, 0x1000\ns_add_u32 s91, s86, 0x4000\ns_add_u32 s92, s86, 0x5000\ns_mov_b32 s93, s82\ns_mov_b32 s79, s78\n" \
    "s_cmp_lt_u32 s32, 63\ns_cbranch_scc1 .Lg15_%=\ns_memrealtime s[72:73]\ns_waitcnt lgkmcnt(0)\ns_add_u32 s76, s72, s35\n.Lg22_%=:\ns_memrealtime s[72:73]\n" \
    "s_waitcnt lgkmcnt(0)\ns_sub_u32 s72, s72, s76\ns_cmp_lt_i32 s72, 0\ns_cbranch_scc1 .Lg22_%=\n.Lg19_%=:\nbuffer_load_dwordx2 v[108:109], v4, s[64:67], s93 offen sc1\n" \
    "buffer_load_dwordx4 v[96:99], v0, s[64:67], s90 offen sc1\nbuffer_load_dwordx4 v[100:103], v0, s[64:67], s91 offen sc1\n" \
    "buffer_load_dwordx4 v[104:107], v0, s[64:67], s92 offen sc1\ns_waitcnt vmcnt(0)\nv_max_u32 v116, v108, v109\nv_cmp_eq_u32 s[70:71], -1, v116\ns_nop 1\n" \
    "v_cndmask_b32 v116, 0, 1, s[70:71]\nv_xor_b32 v96, s79, v96\nv_xor_b32 v97, s79, v97\nv_xor_b32 v98, s79, v98\nv_xor_b32 v99, s79, v99\nv_xor_b32 v100, s79, v100\n" \
    "v_xor_b32 v101, s79, v101\nv_xor_b32 v102, s79, v102\nv_xor_b32 v103, s79, v103\nv_xor_b32 v104, s79, v104\nv_xor_b32 v105, s79, v105\nv_xor_b32 v106, s79, v106\n" \
    "v_xor_b32 v107, s79, v107\nv_or3_b32 v116, v116, v96, v97\nv_or3_b32 v116, v116, v98, v99\nv_or3_b32 v116, v116, v100, v101\nv_or3_b32 v116, v116, v102, v103\n" \
    "v_or3_b32 v116, v116, v104, v105\nv_or3_b32 v116, v116, v106, v107\nv_and_b32 v116, 1, v116\nv_cmp_eq_u32 s[70:71], 1, v116\nv_pk_add_f32 v[96:97], v[96:97], v[16:17]\n" \
    "v_pk_add_f32 v[98:99], v[98:99], v[18:19]\nv_pk_add_f32 v[100:101], v[100:101], v[104:105]\nv_pk_add_f32 v[102:103], v[102:103], v[106:107]\n" \
    "v_pk_add_f32 v[96:97], v[96:97], v[100:101]\nv_pk_add_f32 v[98:99], v[98:99], v[102:103]\nv_cvt_pk_bf16_f32 v96, 0, v96\nv_cvt_pk_bf16_f32 v97, 0, v97\n" \
    "v_cvt_pk_bf16_f32 v98, 0, v98\nv_cvt_pk_bf16_f32 v99, 0, v99\nv_pk_add_f32 v[96:97], v[96:97], v[8:9]\nv_pk_add_f32 v[98:99], v[98:99], v[10:11]\n" \
    "v_cvt_pk_bf16_f32 v96, 0, v96\nv_cvt_pk_bf16_f32 v97, 0, v97\nv_cvt_pk_bf16_f32 v98, 0, v98\nv_cvt_pk_bf16_f32 v99, 0, v99\nv_pk_mul_f32 v[96:97], v[96:97], v[12:13]\n" \
    "v_pk_mul_f32 v[98:99], v[98:99], v[14:15]\nv_cvt_pk_bf16_f32 v96, 0, v96\nv_cvt_pk_bf16_f32 v97, 0, v97\nv_cvt_pk_bf16_f32 v98, 0, v98\nv_cvt_pk_bf16_f32 v99, 0, v99\n" \
    "v_lshlrev_b32 v100, 16, v108\nv_and_b32 v101, 0xffff0000, v108\nv_lshlrev_b32 v102, 16, v109\nv_and_b32 v103, 0xffff0000, v109\n" \
    "v_pk_add_f32 v[96:97], v[96:97], v[100:101]\nv_pk_add_f32 v[98:99], v[98:99], v[102:103]\nv_cvt_pk_bf16_f32 v116, v96, v97\nv_cvt_pk_bf16_f32 v117, v98, v99\n" \
    "s_cmp_lg_u64 s[70:71], 0\ns_cbranch_scc1 .Lg20_%=\nglobal_store_dwordx2 v5, v[116:117], s[46:47]\nv_add_u32 v5, 0x10000, v5\ns_mov_b32 s90, 0x7fff0000\n" \
    "s_mov_b32 s91, 0x7fff0000\ns_mov_b32 s92, 0x7fff0000\ns_mov_b32 s93, 0x7fff0000\n.Lg15_%=:\ns_mov_b32 s38, s85\ns_xor_b32 s39, s39, 128\ns_xor_b32 s80, s80, 0x10000\n" \
    "v_xor_b32 v1, 0x10000, v1\ns_mov_b32 s68, 0\ns_add_u32 s32, s32, 1\ns_cmp_lt_u32 s32, 64\ns_cbranch_scc1 .Lg2_%=\ns_endpgm\n.Lg4_%=:\ns_sleep 8\ns_add_u32 s68, s68, 1\n" \
    "s_and_b32 s69, s68, 63\ns_cbranch_scc1 .Lg3_%=\ns_cmp_gt_u32 s68, 0x400000\ns_cbranch_scc1 .Lg1_%=\nbuffer_load_dword v253, off, s[64:67], 0 sc1\ns_waitcnt vmcnt(0)\n" \
    "v_readfirstlane_b32 s69, v253\ns_nop 0\ns_cmp_eq_u32 s69, 1\ns_cbranch_scc0 .Lg3_%=\ns_endpgm\n.Lg13_%=:\ns_cmp_ge_u32 s32, 62\ns_cbranch_scc1 .Lg14_%=\ns_sleep 10\n" \
    ".Lg14_%=:\ns_sleep 3\nbuffer_load_dword v151, v2, s[64:67], s82 offen sc1\ns_waitcnt vmcnt(0)\nv_cmp_eq_u32 vcc, -1, v151\ns_nop 0\ns_cbranch_vccz .Lg12_%=\n" \
    "s_add_u32 s68, s68, 1\ns_and_b32 s69, s68, 63\ns_cbranch_scc1 .Lg13_%=\ns_cmp_gt_u32 s68, 0x400000\ns_cbranch_scc1 .Lg1_%=\nbuffer_load_dword v253, off, s[64:67], 0 sc1\n" \
    "s_waitcnt vmcnt(0)\nv_readfirstlane_b32 s69, v253\ns_nop 0\ns_cmp_eq_u32 s69, 1\ns_cbranch_scc0 .Lg13_%=\ns_endpgm\n.Lg17_%=:\ns_sleep 4\n" \
    "buffer_load_dwordx2 v[108:109], v4, s[64:67], s93 offen sc1\nbuffer_load_dwordx4 v[96:99], v0, s[64:67], s90 offen sc1\n" \
    "buffer_load_dwordx4 v[100:103], v0, s[64:67], s91 offen sc1\nbuffer_load_dwordx4 v[104:107], v0, s[64:67], s92 offen sc1\ns_waitcnt vmcnt(0)\nv_max_u32 v116, v108, v109\n" \
    "v_cmp_eq_u32 s[70:71], -1, v116\ns_nop 1\nv_cndmask_b32 v116, 0, 1, s[70:71]\nv_xor_b32 v96, s79, v96\nv_xor_b32 v97, s79, v97\nv_xor_b32 v98, s79, v98\n" \
    "v_xor_b32 v99, s79, v99\nv_xor_b32 v100, s79, v100\nv_xor_b32 v101, s79, v101\nv_xor_b32 v102, s79, v102\nv_xor_b32 v103, s79, v103\nv_xor_b32 v104, s79, v104\n" \
    "v_xor_b32 v105, s79, v105\nv_xor_b32 v106, s79, v106\nv_xor_b32 v107, s79, v107\nv_or3_b32 v116, v116, v96, v97\nv_or3_b32 v116, v116, v98, v99\n" \
    "v_or3_b32 v116, v116, v100, v101\nv_or3_b32 v116, v116, v102, v103\nv_or3_b32 v116, v116, v104, v105\nv_or3_b32 v116, v116, v106, v107\nv_and_b32 v116, 1, v116\n" \
    "v_cmp_eq_u32 s[70:71], 1, v116\nv_pk_add_f32 v[96:97], v[96:97], v[16:17]\nv_pk_add_f32 v[98:99], v[98:99], v[18:19]\nv_pk_add_f32 v[100:101], v[100:101], v[104:105]\n" \
    "v_pk_add_f32 v[102:103], v[102:103], v[106:107]\nv_pk_add_f32 v[96:97], v[96:97], v[100:101]\nv_pk_add_f32 v[98:99], v[98:99], v[102:103]\nv_cvt_pk_bf16_f32 v96, 0, v96\n" \
    "v_cvt_pk_bf16_f32 v97, 0, v97\nv_cvt_pk_bf16_f32 v98, 0, v98\nv_cvt_pk_bf16_f32 v99, 0, v99\nv_pk_add_f32 v[96:97], v[96:97], v[8:9]\n" \
    "v_pk_add_f32 v[98:99], v[98:99], v[10:11]\nv_cvt_pk_bf16_f32 v96, 0, v96\nv_cvt_pk_bf16_f32 v97, 0, v97\nv_cvt_pk_bf16_f32 v98, 0, v98\nv_cvt_pk_bf16_f32 v99, 0, v99\n" \
    "v_pk_mul_f32 v[96:97], v[96:97], v[12:13]\nv_pk_mul_f32 v[98:99], v[98:99], v[14:15]\nv_cvt_pk_bf16_f32 v96, 0, v96\nv_cvt_pk_bf16_f32 v97, 0, v97\n" \
    "v_cvt_pk_bf16_f32 v98, 0, v98\nv_cvt_pk_bf16_f32 v99, 0, v99\nv_lshlrev_b32 v100, 16, v108\nv_and_b32 v101, 0xffff0000, v108\nv_lshlrev_b32 v102, 16, v109\n" \
    "v_and_b32 v103, 0xffff0000, v109\nv_pk_add_f32 v[96:97], v[96:97], v[100:101]\nv_pk_add_f32 v[98:99], v[98:99], v[102:103]\nv_cvt_pk_bf16_f32 v116, v96, v97\n" \
    "v_cvt_pk_bf16_f32 v117, v98, v99\ns_cmp_lg_u64 s[70:71], 0\ns_cbranch_scc0 .Lg18_%=\ns_add_u32 s68, s68, 1\ns_and_b32 s69, s68, 63\ns_cbranch_scc1 .Lg17_%=\n" \
    "s_cmp_gt_u32 s68, 0x400000\ns_cbranch_scc1 .Lg1_%=\nbuffer_load_dword v253, off, s[64:67], 0 sc1\ns_waitcnt vmcnt(0)\nv_readfirstlane_b32 s69, v253\ns_nop 0\n" \
    "s_cmp_eq_u32 s69, 1\ns_cbranch_scc0 .Lg17_%=\ns_endpgm\n.Lg20_%=:\ns_sleep 4\ns_add_u32 s68, s68, 1\ns_and_b32 s69, s68, 63\ns_cbranch_scc1 .Lg19_%=\n" \
    "s_cmp_gt_u32 s68, 0x400000\ns_cbranch_scc1 .Lg1_%=\nbuffer_load_dword v253, off, s[64:67], 0 sc1\ns_waitcnt vmcnt(0)\nv_readfirstlane_b32 s69, v253\ns_nop 0\n" \
    "s_cmp_eq_u32 s69, 1\ns_cbranch_scc0 .Lg19_%=\ns_endpgm\n.Lg7_%=:\nv_mov_b32 v120, 0\nv_mov_b32 v121, v1\ns_mov_b32 s76, 14\n.Lg10_%=:\n" \
    "ds_read_b128 v[124:127], v121 offset:0\nds_read_b128 v[128:131], v121 offset:1024\nds_read_b128 v[132:135], v121 offset:2048\nds_read_b128 v[136:139], v121 offset:3072\n" \
    "v_add_u32 v121, 0x1000, v121\ns_waitcnt lgkmcnt(0)\nv_max3_u32 v120, v120, v124, v125\nv_max3_u32 v120, v120, v126, v127\nv_max3_u32 v120, v120, v128, v129\n" \
    "v_max3_u32 v120, v120, v130, v131\nv_max3_u32 v120, v120, v132, v133\nv_max3_u32 v120, v120, v134, v135\nv_max3_u32 v120, v120, v136, v137\n" \
    "v_max3_u32 v120, v120, v138, v139\ns_sub_u32 s76, s76, 1\ns_cmp_lg_u32 s76, 0\ns_cbranch_scc1 .Lg10_%=\nv_cmp_eq_u32 vcc, -1, v120\ns_nop 0\ns_cbranch_vccz .Lg8_%=\n" \
    ".Lg9_%=:\ns_mov_b32 s81, 0\ns_barrier\ns_sleep 4\ns_add_u32 s68, s68, 1\ns_and_b32 s69, s68, 63\ns_cbranch_scc1 .Lg2_%=\ns_cmp_gt_u32 s68, 0x400000\n" \
    "s_cbranch_scc1 .Lg1_%=\nbuffer_load_dword v253, off, s[64:67], 0 sc1\ns_waitcnt vmcnt(0)\nv_readfirstlane_b32 s69, v253\ns_nop 0\ns_cmp_eq_u32 s69, 1\n" \
    "s_cbranch_scc0 .Lg2_%=\ns_endpgm\n.Lg1_%=:\nv_mov_b32 v253, 1\nbuffer_store_dword v253, off, s[64:67], 0 sc1\ns_endpgm\n"
typedef unsigned v32u __attribute__((ext_vector_type(32)));
typedef unsigned v16u __attribute__((ext_vector_type(16)));
#define ORD 1
template <bool isV>
__device__ __forceinline__ void s1_main(const Args& a, unsigned char* lds, const int xg, const int ii) {
    const int wave = a.pad0, head = xg * 4 + (ii >> 2), set = (ii >> 1) & 1;
    char* const ws = a.ws;
    unsigned* const ctl = (unsigned*)(ws + O_CTL);
    const unsigned pubb = isV ? O_UM + (wave & 1) * 262144 + head * 256 : O_EP + ((wave & 1) * 4 + (head >> 3)) * 32768;
    const unsigned psb = isV ? O_UX + (wave & 1) * MiB + (head >> 3) * 16384 + (head & 7) * 1024 : O_PS + (wave * 4 + (head >> 3)) * 16384;
    v16bf w[52], pend[12];
    v4u rX = mkrs(isV ? a.Wv : wave < 2 ? a.Wq : a.Wk, DM * HD * 2), rO = mkrs(a.Wo, DM * HD * 2);
    unsigned bo[4];
#pragma unroll
    for (int i = 0; i < 4; ++i) {
        const int lane = lane_id(), row = lane >> 3, ch = (lane & 7) ^ ((2 * (i & 1) + (row >> 2)) << 1);
        bo[i] = i < 2 ? (((isV ? 64 * (wave >> 1) : 0) + row) * HD + head * DH + 64 * (wave & 1) + 8 * ch) * 2 : ((head * DH + row) * DM + 256 * wave + 8 * ch) * 2;
    }
    asm volatile("s_nop 4" : "+s"(rX), "+s"(rO));
    const int kx = ORD * set;
    unsigned go;
    {
        const int lane = lane_id(), l15 = lane & 15, gb = lane >> 4;
        auto bufp = [&](int u, int wv) { const int b = (u + 4) % 5; return lds + (b < 3 ? L_W + wv * 24576 + b * 8192 : L_H + wv * 16384 + (b - 3) * 8192); };
        auto issue = [&](auto uu) {
            constexpr int u = decltype(uu)::v, c = u;
            constexpr bool wo = isV && c >= 8;
            const unsigned d = (unsigned)(size_t)(LAS unsigned char*)bufp(u, wave);
#pragma unroll
            for (int r8 = 0; r8 < 8; ++r8)
                bdma<(NTW & (u < 4 ? 1 : u < 8 ? 2 : 64))>(wo ? rO : rX, wo ? bo[2 + (r8 & 1)] : bo[r8 & 1], wo ? ((64 * (c & 1) + 8 * r8) * DM + 64 * ((c - 8) >> 1)) * 2 : (((isV ? 128 : 64) * c + 8 * r8) ^ (512 * kx)) * HD * 2, d + r8 * 1024);
        };
        const unsigned q = l15 >> 2, p = lane & 3;
        const unsigned row0 = 16 * (gb >> 1) + 4 * (gb & 1) + (q & 1) + 8 * (q >> 1);
        const unsigned sw = ((gb & 1) | ((q >> 1) << 1)) << 1;
        auto flush = [&](int j) {
            const v8u v = __builtin_bit_cast(v8u, pend[j]);
            unsigned char* d = lds + L_W + wave * 24576 + j * 2048 + lane * 16;
            *(LAS v4u*)d = (v4u){v[0], v[1], v[2], v[3]};
            *(LAS v4u*)(d + 1024) = (v4u){v[4], v[5], v[6], v[7]};
        };
        sfor<0, 16>([&](auto cc) {
            constexpr int u = decltype(cc)::v, c = u;
            asm volatile("s_waitcnt vmcnt(%0)\n\ts_barrier" :: "n"(u == 0 ? 32 : u < 13 ? 24 : 8 * (15 - u) + (u == 15)) : "memory");
            if constexpr (u == 14) asm volatile("s_nop 4\n\tglobal_load_dword %0, %1, %2 sc1" : "=&v"(go) : "v"(O_GO + blockIdx.x * 128), "s"(ws));
            if constexpr (u > 0 && u + 4 < 16) issue(IC<u + 4>{});
            if (u >= 12 && u < 15) { flush(4 * u - 48); flush(4 * u - 47); flush(4 * u - 46); flush(4 * u - 45); }
            v16bf t[4];
#pragma unroll
            for (int n = 0; n < 4; ++n) {
                constexpr bool pk = isV && c >= 8;
                const unsigned char* buf = bufp(u, pk ? wave : (n & 2) + (wave >> 1));
                const unsigned o = pk ? (32 * (n & 1) + 16 * (gb & 1) + 4 * (gb >> 1) + (q & 1)) * 128 + (((4 * (n >> 1) + 2 * (q >> 1) + (p >> 1)) ^ (gb & 2)) << 4) + (p & 1) * 8
                                      : row0 * 128 + (((4 * (wave & 1) + 2 * (n & 1) + (p >> 1)) ^ sw) << 4) + (p & 1) * 8;
                const unsigned o2 = pk ? (o ^ 64) + 8 * 128 : o + 32 * 128;
                const v2u r0 = tr_rd(buf, o), r1 = tr_rd(buf, o + 2 * 128), r2 = tr_rd(buf, o2), r3 = tr_rd(buf, o2 + 2 * 128);
                const v8u v = {r0.x, r0.y, r1.x, r1.y, r2.x, r2.y, r3.x, r3.y};
                t[n] = __builtin_bit_cast(v16bf, v);
            }
            asm volatile("s_waitcnt lgkmcnt(0)" ::: "memory");
            sfor<0, 4>([&](auto nn) {
                constexpr int n = decltype(nn)::v, j = lsl(isV, c, n), r = ridx(isV, c, n);
                if constexpr (j >= 0) { pend[j] = t[n]; asm volatile("" : "+v"(pend[j])); }
                else {
                    w[r] = t[n];
                    if constexpr (r < 32) asm volatile("" : "+a"(w[r])); else asm volatile("" : "+v"(w[r]));
                }
            });
        });
    }
    RSTAMP(74, 0);
    asm volatile("s_waitcnt vmcnt(0)\n\tv_readfirstlane_b32 %0, %1" : "=s"(go) : "v"(go));
    const unsigned pol = (go ? go : wait_go(ws)) & 1;
    lds_barrier();
    RSTAMP(74, 1);
    const unsigned lb0 = (unsigned)(size_t)(LAS unsigned char*)lds;
    const unsigned lane = lane_id(), l15 = lane & 15, g = lane >> 4, q = l15 >> 2, p = lane & 3, l16 = lane * 16, to = g * 128 + (l15 & 7) * 16, xb = ((g & 1) << 1) | (g >> 1);
    const unsigned r = 8 * wave + (lane >> 3), ao = r * 2048 + 16 * ((lane & 7) ^ ((r >> 1) & 7)) + 1024, ra = lb0 + L_H + (l15 >> 3) * 2048 + (l15 & 7) * 128;
    const v16u c0 = {(lane & 8) ? 0xEEEEu : 0x4444u, lb0 + L_H + l16, l16, lb0 + L_W + wave * 24576 + l16, lb0 + L_T + wave * 512 + q * 128 + (g & 1) * 64 + (g >> 1) * 32 + p * 2,
                     lb0 + (isV ? L_T + to : L_QX + (wave >> 1) * 4096 + (wave & 1) * 128 + g * 128 + to), isV ? lb0 + L_QX + g * 128 + to : lb0 + L_T + to,
                     isV ? xb * 16384 + l15 * 16 : g < 2 ? (l15 >> 3) * 2048 + (head & 7) * 256 + to : lane < 40 ? 4096 + (head & 7) * 128 + l15 * 16 : lane == 40 && head + wave == 0 ? 5120 : 0x7FFF0000u, isV ? xb * 65536 + l15 * 16 : lb0 + L_T + 2048 + (to ^ (l15 >> 3) * 64),
                     ra + 16 * (g ^ (l15 >> 1)), ra + 16 * ((4 + g) ^ (l15 >> 1)), ao, ao - 896,
                     0u, lb0 + (isV ? L_QX : L_KX) + wave * 1024 + l16, 0u};
    const unsigned sd = ((pol ^ (wave >> 1)) & 1) * 8192;
    PARS;
    const v16u s0 = {0x3e0293eeu, 0x3db504f3u, 0x3fb8aa3bu, (unsigned)set, (unsigned)wave, O_E1 + (((xg >> 1) & 1) * 4 + wave) * 16384 + ((pol ^ (xg >> 2)) & 1) * 8192 + 2048 * kx, pubb + sd, psb + (isV ? sd : 0),
                     O_H1 - 1024 + set * 65536, lb0 + L_H + wave * 2048, hw(pol, xg) + H_C + 512 * set, par(P_R), par(P_A), par(P_X), lb0 + L_ST + (wave == STAMP_WAVE ? 0 : MiB), lb0 + L_ST};
    u64 cm[4];
#pragma unroll
    for (int i = 0; i < 4; ++i) cm[i] = __ballot(16 * (l15 >> 3) + 4 * g + i <= 8 * wave + (l15 & 7));
    const u64 dbg = DBG(a, blockIdx.x);
    const v16u s1 = {isV ? O_FL + set * 16384 + sd + (wave & 1) * 4096 + head * 128 : head + wave ? 0x7FFF0000u : 0u, isV ? set : par(P_M), isV ? set : (unsigned)cm[0], (unsigned)(cm[0] >> 32), (unsigned)cm[1], (unsigned)(cm[1] >> 32),
                     (unsigned)cm[2], (unsigned)(cm[2] >> 32), (unsigned)cm[3], (unsigned)(cm[3] >> 32), (unsigned)dbg, (unsigned)(dbg >> 32), lb0 + L_T + wave * 512, ii < 4 ? hw(pol, xg) + H_D + 4 * wave : 0u, 1024u * kx,
                     head < (isV ? 20 : 28) ? O_D + (isV ? D_X2 : 0) + head * 8192 + wave * 2048 : 0x70000000u};
    const v4u rws = mkrs(ws, WS_END);
    v32u ww[13];
#pragma unroll
    for (int i = 0; i < 13; ++i) {
        const v8u x0 = __builtin_bit_cast(v8u, w[4 * i]), x1 = __builtin_bit_cast(v8u, w[4 * i + 1]), x2 = __builtin_bit_cast(v8u, w[4 * i + 2]), x3 = __builtin_bit_cast(v8u, w[4 * i + 3]);
        ww[i] = __builtin_shufflevector(__builtin_shufflevector(x0, x1, 0, 1, 2, 3, 4, 5, 6, 7, 8, 9, 10, 11, 12, 13, 14, 15), __builtin_shufflevector(x2, x3, 0, 1, 2, 3, 4, 5, 6, 7, 8, 9, 10, 11, 12, 13, 14, 15),
                                        0, 1, 2, 3, 4, 5, 6, 7, 8, 9, 10, 11, 12, 13, 14, 15, 16, 17, 18, 19, 20, 21, 22, 23, 24, 25, 26, 27, 28, 29, 30, 31);
    }
#define S1_CALL(TXT) asm volatile("s_nop 4\n" TXT :: "{v[0:15]}"(c0), "{s[32:47]}"(s0), "{s[48:63]}"(s1), "{s[64:67]}"(rws), \
              "{a[0:31]}"(ww[0]), "{a[32:63]}"(ww[1]), "{a[64:95]}"(ww[2]), "{a[96:127]}"(ww[3]), "{a[128:159]}"(ww[4]), "{a[160:191]}"(ww[5]), "{a[192:223]}"(ww[6]), "{a[224:255]}"(ww[7]), \
              "{v[92:123]}"(ww[8]), "{v[124:155]}"(ww[9]), "{v[156:187]}"(ww[10]), "{v[188:219]}"(ww[11]), "{v[220:251]}"(ww[12]) : "memory", "s95", "v253")
    if constexpr (isV) S1_CALL(ASM_V); else S1_CALL(ASM_Q);
}

__device__ __forceinline__ float gelu_tanh(float v) {
    const float e = v * (-2.302208198f - 0.1029432397f * (v * v));
    return v * __builtin_amdgcn_rcpf(1.f + __builtin_amdgcn_exp2f(e));
}
__device__ __forceinline__ v4u unsent4(v4u v) { return (v4u){unsent(v.x), unsent(v.y), unsent(v.z), unsent(v.w)}; }
__device__ __forceinline__ unsigned rt_now() { return (unsigned)__builtin_amdgcn_s_memrealtime(); }
__device__ __forceinline__ void rt_delay(unsigned ticks  , unsigned t0 = rt_now()) {
    while ((unsigned)__builtin_amdgcn_s_memrealtime() - t0 < ticks) __builtin_amdgcn_s_sleep(2);
}
__device__ __forceinline__ float wave_sum(float x) {
    x += __uint_as_float(__builtin_amdgcn_update_dpp(0, __float_as_uint(x), 0xB1, 0xF, 0xF, false));
    x += __uint_as_float(__builtin_amdgcn_update_dpp(0, __float_as_uint(x), 0x4E, 0xF, 0xF, false));
    x += __uint_as_float(__builtin_amdgcn_update_dpp(0, __float_as_uint(x), 0x124, 0xF, 0xF, false));
    x += __uint_as_float(__builtin_amdgcn_update_dpp(0, __float_as_uint(x), 0x128, 0xF, 0xF, false));
    return red32_add(red16_add(x));
}
__device__ __forceinline__ void ln_row(const v4u (&xr)[2], const f32x4 (&c1)[4], const f32x4 (&sh)[4], v4u (&o)[2]) {
    f32x2 v[8];
    f32x2 s2 = {0.f, 0.f};
#pragma unroll
    for (int j = 0; j < 8; ++j) { v[j] = (f32x2){lo16f(xr[j >> 2][j & 3]), hi16f(xr[j >> 2][j & 3])}; s2 += v[j]; }
    const float mean = wave_sum(s2[0] + s2[1]) * (1.f / DM);
    f32x2 q2 = {0.f, 0.f};
#pragma unroll
    for (int j = 0; j < 8; ++j) { const f32x2 d = v[j] - mean; q2 += d * d; }
    const float vs = wave_sum(q2[0] + q2[1]);
    const float mb = rbf(mean), vb = rbf(vs * (1.f / DM));
    const float rsd = 1.f / rbf(sqrtf(rbf(vb + 1e-5f)));
    auto r2 = [](f32x2 z) { const unsigned u = pack2(z[0], z[1]); return (f32x2){lo16f(u), hi16f(u)}; };
#pragma unroll
    for (int j = 0; j < 8; ++j) {
        const f32x2 cc = {c1[j >> 1][2 * (j & 1)], c1[j >> 1][2 * (j & 1) + 1]}, ss = {sh[j >> 1][2 * (j & 1)], sh[j >> 1][2 * (j & 1) + 1]};
        const f32x2 y = r2(r2(r2(v[j] - mb) * rsd) * cc) + ss;
        o[j >> 2][j & 3] = pack2(y[0], y[1]);
    }
}
template <int N> __device__ __forceinline__ void ld_mod(rsrc_t rs, unsigned* ctl, const int (&col)[N], f32x4* m) {
    for (unsigned spins = 0;;) {
        unsigned mx = 0;
#pragma unroll
        for (int i = 0; i < N; ++i) { const v4u v = bld16(rs, O_MOD + col[i] * 4); mx = max(mx, umax4(v)); m[i] = __builtin_bit_cast(f32x4, v); }
        if (!__any(mx == SENT)) break;
        __builtin_amdgcn_s_sleep(6);
        if (spin_fail(spins, ctl + C_ABORT)) asm volatile("s_endpgm");
    }
}
__device__ __forceinline__ void set_m0(unsigned v) { asm volatile("s_mov_b32 m0, %0" :: "s"(v) : "m0"); }
__device__ __forceinline__ void mod8(rsrc_t rs, int col, f32x4 (&m)[2]) {
    f32x4 p[4][2];
#pragma unroll
    for (int k = 0; k < 8; ++k) p[k >> 1][k & 1] = __builtin_bit_cast(f32x4, bld16(rs, O_MODP + ((k >> 1) * 2048 + col + 4 * (k & 1)) * 4));
#pragma unroll
    for (int h = 0; h < 2; ++h) {
        const f32x4 t = (p[0][h] + p[1][h]) + (p[2][h] + p[3][h]);
#pragma unroll
        for (int i = 0; i < 4; ++i) { float v = rbf(t[i]); if (col >= DM) v = rbf(1.f + v); m[h][i] = v; }
    }
}
template <int N, class F> __device__ __forceinline__ void mod_cols(const Args& a, rsrc_t rs, unsigned char* lds, int cg, int k0, F&& mid) {
    const int tid = otid();
    v4u wv[N];
    const v2u cr = *(const v2u*)(a.c + 4 * tid);
#pragma unroll
    for (int i = 0; i < N; ++i) {
        const v4u* p = (const v4u*)(a.w_mod + (size_t)(k0 + (tid >> 3) + 32 * i) * 6144 + cg * 64 + 8 * (tid & 7));
        wv[i] = NTW & 4 ? __builtin_nontemporal_load(p) : *p;
    }
    SB(); mid(); SB();
    f32x4 sv = {lo16f(cr.x), hi16f(cr.x), lo16f(cr.y), hi16f(cr.y)};
#pragma unroll
    for (int i = 0; i < 4; ++i) sv[i] = rbf(sv[i] / (1.f + __expf(-sv[i])));
    *(LAS f32x4*)(lds + 16384 + tid * 16) = sv;
    lds_barrier();
    f32x2 acc[4] = {{0.f, 0.f}, {0.f, 0.f}, {0.f, 0.f}, {0.f, 0.f}};
#pragma unroll
    for (int i = 0; i < N; ++i) {
        const float sv = *(const LAS float*)(lds + 16384 + (k0 + (tid >> 3) + 32 * i) * 4);
#pragma unroll
        for (int j = 0; j < 4; ++j) acc[j] += sv * (f32x2){lo16f(wv[i][j]), hi16f(wv[i][j])};
    }
    unsigned char* red = lds + 20480;
    *(LAS f32x4*)(red + (tid >> 3) * 272 + (tid & 7) * 32) = (f32x4){acc[0][0], acc[0][1], acc[1][0], acc[1][1]};
    *(LAS f32x4*)(red + (tid >> 3) * 272 + (tid & 7) * 32 + 16) = (f32x4){acc[2][0], acc[2][1], acc[3][0], acc[3][1]};
    lds_barrier();
    if (tid < 64) {
        f32x4 t = {0.f, 0.f, 0.f, 0.f};
#pragma unroll
        for (int i = 0; i < 8; ++i) t += *(const LAS f32x4*)(red + ((tid >> 4) * 8 + i) * 272 + (tid & 15) * 16);
#pragma unroll
        for (int i = 0; i < 4; ++i) { float v = red32_add(red16_add(t[i])); if (N == 32) { v = rbf(v); if ((cg >> 4) == 4) v = rbf(1.f + v); } t[i] = v; }
        if (tid < 16) bst16(rs, (N == 32 ? O_MOD : O_MODP + k0 * 32) + (cg * 64 + 4 * tid) * 4, unsent4(__builtin_bit_cast(v4u, t)));
    }
}
#define TD_A 300
#define TD_L 1000
#define TD_M 850
#define TD_G 120
constexpr int NARM0 = 2216, NARM1 = 4712;
__device__ __forceinline__ unsigned arm0(unsigned v) {
    if (v < 104) return (v < 64 ? O_FL : O_HW - 65536) + v * 1024;
    if ((v -= 104) < 768) return (v < 384 ? O_EP : O_PS - MiB / 2) + v / 6 * 8192 + v % 6 * 1024;
    if ((v -= 768) < 1024) return O_E1 + 131072 + v * 1024;
    if ((v -= 1024) < 320) return O_D + D_X2 + v / 160 * D_SLOT + v % 160 * 1024;
    if ((v -= 320) < 448) return O_D + v / 224 * D_SLOT + v % 224 * 1024;
    return O_SL + (v - 448) * 1024;
}
__device__ __forceinline__ void f_start(const Args& a, rsrc_t rs, unsigned char* lds, int idx  , unsigned t0) {
    const int tid = otid(), lane = tid & 63, wave = a.pad0;
    unsigned* const ctl = (unsigned*)(a.ws + O_CTL);
    const int r0 = 4 * idx + wave;
    const bool boss = idx == 63;
    unsigned gv = wave >> 1;
    auto serve = [&](bool all) { if (gv && (all || !ldg4_sc1(a.ws, O_GO + tid * 128))) __hip_atomic_store((gu32*)(a.ws + O_GO + tid * 128), gv, RLX_AGENT); };
    if (boss) bst16(rs, tid * 16, (v4u){0u, 0u, 0u, 0u});
    v4u xr[6][2];
    auto ldx = [&](int k, int row) { for (int c = 0; c < 2; ++c) xr[k][c] = *(const v4u*)(a.x + (size_t)row * DM + 512 * c + 8 * lane); };
    SB();
    mod_cols<8>(a, rs, lds, idx & 31, (idx >> 5) * 256, [&]() {
        if (boss) { asm volatile("s_waitcnt vmcnt(8)\n\ts_barrier" ::: "memory"); rt_delay(TD_G, t0); serve(true); }
        ldx(0, r0); if (idx == 55) ldx(1, r0 + 16);
    });
    RSTAMP(73, 1);
    if (boss) serve(false); else if (wave == 0) { wait_vm0(); wait_go(a.ws); }
    drained(ctl + C_M + 32 * (idx & 7));
    RSTAMP(73, 2);
    if (idx >= 64) {
        for (unsigned v = r0 - 256; v < NARM0; v += 256) bst16(rs, arm0(v) + lane * 16, v < 80 ? (v4u){0u, 0u, 0u, 0u} : sentv());
        if (idx < 80) {
            const unsigned u = wave * 256 + ((idx >> 1) & 3) * 64 + lane;
            const v4u m = unsent4(*(const v4u*)(a.mem0 + (u & 7) * DM + (u >> 4) * 16 + ((u >> 3) & 1) * 8));
            bst16(rs, O_E1 + (idx - 64) * 8192 + tid * 16, m); bst16(rs, O_E1 + (idx - 64) * 8192 + 4096 + tid * 16, m);
        }
    }
    if (idx == 59) {
        unsigned best[2] = {~0u, ~0u};
        for (int r = 0; r < NPR; ++r) {
            const u64 t0 = __builtin_readcyclecounter();
            asm volatile("s_mov_b64 exec, 1\n\ts_nop 4\n\tglobal_store_dword %0, %1, %2 sc1\n\ts_waitcnt vmcnt(0)\n\ts_mov_b64 exec, -1" :: "v"(O_POL + 256 + ((r ^ wave) & 1) * 8192 + wave * 1280), "v"(0u), "s"(a.ws) : "memory");
            const unsigned d = (unsigned)(__builtin_readcyclecounter() - t0);
            if ((r ^ wave) & 1) best[1] = min(best[1], d); else best[0] = min(best[0], d);
        }
        *(LAS v2u*)(lds + 40960 + wave * 8) = (v2u){best[0], best[1]};
        lds_barrier();
        const v4u p = lds16(lds + 40960), q = lds16(lds + 40976);
        const bool second = min(min(p.y, p.w), min(q.y, q.w)) < min(min(p.x, p.z), min(q.x, q.z));
        if (tid == 0) __hip_atomic_store((gu32*)(a.ws + O_POL), (second ^ (__builtin_amdgcn_s_getreg((3 << 11) | 20) >> 2)) & 1, RLX_AGENT);
    }
    RSTAMP(72, 0);
    for (unsigned spins = 0; idx != 59 && __any(lane < 8 && __hip_atomic_load((gu32*)(ctl + C_M + 32 * (lane & 7)), RLX_AGENT) < 16u);) {
        if (boss) serve(false);
        __builtin_amdgcn_s_sleep(8);
        if (spin_fail(spins, ctl + C_ABORT)) asm volatile("s_endpgm");
    }
    RSTAMP(73, 0);
    f32x4 c1[4], sh[4];
    auto prep = [&]() {
        f32x4 m[2];
        mod8(rs, 8 * tid, m);
        *(LAS f32x4*)(lds + 32 * tid) = m[0]; *(LAS f32x4*)(lds + 32 * tid + 16) = m[1];
        lds_barrier();
#pragma unroll
        for (int i = 0; i < 4; ++i) { const int col = 512 * (i >> 1) + 8 * lane + 4 * (i & 1); sh[i] = *(const LAS f32x4*)(lds + col * 4); c1[i] = *(const LAS f32x4*)(lds + (DM + col) * 4); }
    };
    prep();
    auto row = [&](int k, int r) {
        v4u o[2];
        ln_row(xr[k], c1, sh, o);
#pragma unroll
        for (int c = 0; c < 2; ++c) {
            const bool od = lane & 1;
            const unsigned y0 = __builtin_amdgcn_update_dpp(0, od ? o[c].x : o[c].z, 0xB1, 0xF, 0xF, false), y1 = __builtin_amdgcn_update_dpp(0, od ? o[c].y : o[c].w, 0xB1, 0xF, 0xF, false);
            bst16(rs, O_H1 + (unsigned)(r * DM + 512 * c + 8 * lane) * 2, od ? (v4u){y0, o[c].z, y1, o[c].w} : (v4u){o[c].x, y0, o[c].y, y1});
        }
    };
    if (idx != 59) row(0, r0);
    if (idx == 55) row(1, r0 + 16);
    drained(ctl + C_A + 32 * (idx & 7));
    RSTAMP(75, 2);
    if (boss) { gv = 2 + wait_set<4>(a.ws, ctl); serve(true); }
    rt_delay(idx >= 64 ? TD_M : TD_L, t0);
    if (idx >= 64) {
#pragma unroll
        for (int k = 0; k < 6; ++k) ldx(k, 6 * r0 - 1024 + k);
        for (unsigned v = NARM0 + r0 - 256; v < NARM1; v += 256) bst16(rs, arm0(v) + lane * 16, sentv());
#pragma unroll 1
        for (int k = 0; k < 6; ++k) {
            row(0, 6 * r0 - 1024 + k);
#pragma unroll
            for (int i = 0; i < 10; ++i) xr[i >> 1][i & 1] = xr[(i >> 1) + 1][i & 1];
        }
        drained(ctl + C_H);
        return;
    }
    const unsigned pol = wait_set(a.ws, ctl);
    if (idx < 32) {
#pragma unroll 1
        for (int h = 0; h < 2; ++h) { mod_cols<32>(a, rs, lds, h ? (idx < 16 ? idx + 32 : idx + 64) : idx + 48, 0, []() {}); lds_barrier(); }
    }
    if (boss) {
        for (unsigned spins = 0; (__builtin_amdgcn_readfirstlane(ldg4_sc1(a.ws, hw(pol, 7) + H_C + 512)) & 127) < 3;) {
            serve(false);
            __builtin_amdgcn_s_sleep(64);
            if (spin_fail(spins, ctl + C_ABORT)) break;
        }
    }
}
#define TD_W 1500
__device__ __forceinline__ unsigned r_ew(unsigned pol, unsigned lane) { return ((lane >> 4) & 1) * 65536 + (((O_E1 >> 21) ^ pol ^ (lane >> 5)) & 1) * 8192 + (lane & 15) * 16; }
__device__ __forceinline__ unsigned r_e1(int cb) { return O_E1 + ((cb >> 2) & 3) * 16384 + (cb >> 4) * 1024 + (cb & 3) * 256; }
__device__ __forceinline__ unsigned r_db(int cb) { return D_X2 + (cb >> 3) * 16384 + (cb & 7) * 1024; }
__device__ __forceinline__ unsigned r_st(int cb, unsigned lane) { return D_ST + (cb >> 5) * 16384 + (lane >> 5) * 8192 + (cb & 31) * 256 + (lane & 31) * 8; }
__device__ __forceinline__ void r_main(const Args& a, rsrc_t rs, unsigned char* lds, int xg, int i8, unsigned* ctl, unsigned t0) {
    const unsigned lane = lane_id(), wave = a.pad0, l15 = lane & 15, g = lane >> 4, q = l15 >> 2, p = lane & 3, cb = 8 * xg + i8;
    const unsigned pol = wait_set(a.ws, ctl), lb = (unsigned)(size_t)(LAS unsigned char*)lds;
    *(volatile LAS unsigned*)(lds + 4096) = 0u;
    lds_barrier();
    RSTAMP(74, 0);
    const v16u c0 = {(lane & 8) ? 0xEEEEu : 0x4444u, lane * 16, 4096 + lane * 16, lb + wave * 1024 + l15 * 16 + (g & 1) * 8 + (g >> 1) * 4,
                     lb + wave * 1024 + (8 * (p >> 1) + q) * 16 + (p & 1) * 8, lb + wave * 1024 + ((2 * (g & 1) + (p >> 1)) ^ (cb & 1)) * 256 + (8 * (g >> 1) + q) * 16 + (p & 1) * 8,
                     r_ew(pol, lane), (wave * 32 + (lane & 31)) * 2048 + cb * 32 + (lane >> 5) * 16, nearg(pol, O_FL + wave * 16384, xg >> 2) + ((cb >> 4) & 1) * 4096 + (lane & 31) * 128, r_st(cb, lane) - r_db(cb), lb + 4096, (g >> 1) * 1024 + (g & 1) * 256 + l15 * 16,
                     O_MOD + (2 * DM + 16 * cb + 8 * (lane >> 5)) * 4, (lane >> 3) * 16384 + 4096 + (lane & 7) * 128, 0u, 0u};
    const unsigned sd = ((pol ^ (xg >> 2)) & 1) * 8192;
    PARS;
    const v16u s0 = {wave, wave, par(P_A) + (wave == 0 ? par(P_A0) : wave == 1 ? par(P_A1) : 0), par(P_B), par(P_EU), par(P_DMIN), par(P_R), par(P_TP), par(P_TU), par(P_T0),
                     O_UX + (cb & 31) * 65536 + sd, O_UM + (cb & 31) * 16384 + sd, O_EP + ((xg >> 1) & 1) * 131072 + sd, O_PS + sd, r_e1(cb), O_D + r_db(cb)};
    const u64 dbg = DBG(a, blockIdx.x);
    const v8u s1 = {i8 ? 0u : hw(pol, xg) + H_C + 512 * (wave & 1), (cb & 1) * 65536, lb + 8192 + wave * 32, lb + 8192, (unsigned)dbg, (unsigned)(dbg >> 32), (unsigned)(u64)a.x, (unsigned)((u64)a.x >> 32)};
    const v4u rws = mkrs(a.ws, WS_END);
    const v8u s2 = {cb || wave != 3 ? 0u : O_HW + H_D + 128, 0u, par(P_TF), par(P_X2), hw(pol, xg) + H_D, par(P_TT), t0, par(P_B2)};
    asm volatile("s_nop 4\n" ASM_R :: "{v[0:15]}"(c0), "{s[32:47]}"(s0), "{s[48:55]}"(s1), "{s[56:63]}"(s2), "{s[64:67]}"(rws) : "memory", "s95", "v253", "a255");
}

typedef __attribute__((ext_vector_type(16))) float f32x16;
template <bool is2> __device__ __forceinline__ void f_main(const Args& a, rsrc_t rs, unsigned char* lds, int xg, int kq, int cg, unsigned* ctl) {
    constexpr int L_C = 131072, L_S = L_C + 8192, NG = is2 ? 7 : 8, LD = is2 ? DM : FF;
    const int tid = otid(), lane = tid & 63, wave = a.pad0, l15 = lane & 15, g = lane >> 4;
    const unsigned lb = (unsigned)(size_t)(LAS unsigned char*)lds, q = l15 >> 2, p = lane & 3, l16 = lane * 16;
    const unsigned pol = __builtin_amdgcn_readfirstlane(ldg4_sc1(a.ws, O_POL));
    auto gr = [&](int G) { const int x = G + 2 * ((blockIdx.x >> 3) & 3); return x >= NG ? x - NG : x; };
    lds_barrier();
    v4u w[64];
    f32x16 u2;
    {
        v4u rW = mkrs(is2 ? a.w2 : a.w1, DM * FF * 2);
        unsigned bo[2];
#pragma unroll
        for (int i = 0; i < 2; ++i) bo[i] = (g * LD + 16 * ((l15 >> 1) ^ (g | i << 2)) + 8 * (lane & 1)) * 2;
        asm volatile("s_nop 4" : "+s"(rW));
        auto issue = [&](int G) {
            const unsigned r0 = 896 * kq + 128 * gr(G) + 32 * wave;
#pragma unroll
            for (int j = 0; j < 8; ++j) bdma<(NTW & 8)>(rW, bo[(j >> 1) & 1], ((r0 + 4 * j) * LD + 128 * cg) * 2, lb + (G & 3) * 32768 + wave * 8192 + j * 1024);
        };
        issue(0); issue(1); issue(2); issue(3);
        const unsigned ox = is2 ? (4 * (g >> 1) + q) * 256 + 32 * ((2 * (kq ^ (2 * (g & 1) + (p >> 1))) + (wave >> 1)) ^ q) + 16 * (wave & 1) + 8 * (p & 1)
                                : (8 * (g >> 1) + q) * 256 + 32 * ((2 * wave + (g & 1)) ^ (q | (g >> 1) << 2)) + 8 * p;
        sfor<0, NG>([&](auto gg) {
            constexpr int G = decltype(gg)::v;
            asm volatile("s_waitcnt vmcnt(%0)\n\ts_barrier" :: "n"(G < NG - 3 ? 24 : 8 * (NG - 1 - G)) : "memory");
#pragma unroll
            for (int k = 0; k < 8; ++k) {
                const unsigned o = (G & 3) * 32768 + k * 4096;
                const v2u lo = tr_rd(lds, o + ox), hi = tr_rd(lds, o + (is2 ? (ox ^ 128) + 2048 : ox + 1024));
                w[8 * G + k] = (v4u){lo.x, lo.y, hi.x, hi.y};
            }
#pragma unroll
            for (int k = 8 * G; k < 8 * G + 8; ++k) asm volatile("" : "+a"(w[k]));
            lds_barrier();
            if (G + 4 < NG) issue(G + 4);
        });
    }
    const unsigned sd = ((pol ^ (xg >> 2)) & 1) * 8192;
    v16u c0 = {l16, lb + l16, is2 ? min(lane, 6) * 16384 + wave * 2048 + 2044 : D_ST - D_X2 + (l15 >> 3) * 16384 + ((l15 & 6) * 4 + 2 * wave + (lane & 1)) * 256, lb + L_S + wave * 256 + l15 * 16, lb + L_S + (lane & 31) * 8, 0u, 0xbdd2d3e8u, 0xbdd2d3e8u, 0xc0135761u, 0xc0135761u, 0x3f800000u, 0x3f800000u, 0u, 0u, 0u, 0u};
    if (is2) {
        const int col = 128 * cg + 32 * kq + 8 * wave + 4 * (lane >> 5);
        const v2u br = *(const v2u*)(a.b2 + col);
        f32x4 gv;
        ld_mod<1>(rs, ctl, {5 * DM + col}, &gv);
        const int f1 = 7 * (kq & 1) + min(lane, 6);
        c0[3] = hw(pol, (xg & 4) + (f1 >> 2)) + H_P + 256 * (f1 & 3); c0[4] = r_db(col >> 4) - (kq & 1) * 7 * 16384 + (wave & 1) * 512 + (lane & 31) * 16 + (lane >> 5) * 8; c0[5] = ((lane & 31) * DM + col) * 2;
        c0[8] = br.x << 16; c0[9] = br.x & 0xFFFF0000u; c0[10] = br.y << 16; c0[11] = br.y & 0xFFFF0000u;
#pragma unroll
        for (int i = 0; i < 4; ++i) c0[12 + i] = __float_as_uint(gv[i]);
    } else {
#pragma unroll
        for (int h = 0; h < 4; ++h) {
            const v2u br = *(const v2u*)(a.b1 + 128 * cg + 32 * wave + 8 * h + 4 * (g >> 1));
            u2[4 * h] = lo16f(br.x); u2[4 * h + 1] = hi16f(br.x); u2[4 * h + 2] = lo16f(br.y); u2[4 * h + 3] = hi16f(br.y);
        }
        f32x4 md[2];
        const int k0 = 128 * gr(tid >> 5) + 4 * (tid & 31);
        ld_mod<2>(rs, ctl, {4 * DM + k0, 3 * DM + k0}, md);
        *(LAS f32x4*)(lds + L_C + 16 * tid) = md[0]; *(LAS v2u*)(lds + L_C + 4096 + 8 * tid) = (v2u){pack2(md[1][0], md[1][1]), pack2(md[1][2], md[1][3])};
        lds_barrier();
        c0[12] = lb + L_C + 32 * (g >> 1); c0[13] = lb + L_C + 4096 + 16 * (g >> 1);
    }
    if (!wave_wait<16>(ctl + C_H, 1, 1, 64, ctl + C_ABORT)) asm volatile("s_endpgm");
    RSTAMP(74, 0);
    const u64 dbg = DBG(a, blockIdx.x);
    PARS;
    const v16u s0 = {0u, (unsigned)wave, is2 ? par(P_T1) : -par(P_TZ), par(P_TS), O_D + (is2 ? (kq & 1) * 7 * 16384 : D_X2) + sd, lb + wave * 2048, is2 ? 0u : hw(pol, xg) + H_C, hw(pol, xg) + H_P + 256 * ((blockIdx.x >> 3) & 3),
                     O_D + (cg >= 14 ? cg - 14 : cg) * 16384 + wave * 2048 + sd, D_ST - D_X2 + (wave >> 1) * 16384 + (wave & 1) * 4096, lb + L_S + 2048 + (wave == STAMP_WAVE ? 0 : MiB), lb + L_S + 2048,
                     (unsigned)dbg, (unsigned)(dbg >> 32), (unsigned)(u64)a.out, (unsigned)((u64)a.out >> 32)};
    v16u s1;
#pragma unroll
    for (int i = 0; i < 16; ++i) {
        const int o = kq ^ (i & 3);
        s1[i] = i < 8 ? gr(i) * 16384 + wave * 2048 : O_SL + ((cg * 2 + (o & 1)) * 2 + ((i >> 1) & 1)) * 16384 + ((pol ^ (o >> 1)) & 1) * 8192 + (i & 1) * 4096 + wave * 1024;
    }
    s1[12] = is2 ? par(P_T2) : -par(P_TY);
    const v4u rws = mkrs(a.ws, WS_END);
    v32u ww[8];
#pragma unroll
    for (int i = 0; i < NG; ++i) {
        const v16u x0 = __builtin_shufflevector(__builtin_shufflevector(w[8 * i], w[8 * i + 1], 0, 1, 2, 3, 4, 5, 6, 7), __builtin_shufflevector(w[8 * i + 2], w[8 * i + 3], 0, 1, 2, 3, 4, 5, 6, 7), 0, 1, 2, 3, 4, 5, 6, 7, 8, 9, 10, 11, 12, 13, 14, 15);
        const v16u x1 = __builtin_shufflevector(__builtin_shufflevector(w[8 * i + 4], w[8 * i + 5], 0, 1, 2, 3, 4, 5, 6, 7), __builtin_shufflevector(w[8 * i + 6], w[8 * i + 7], 0, 1, 2, 3, 4, 5, 6, 7), 0, 1, 2, 3, 4, 5, 6, 7, 8, 9, 10, 11, 12, 13, 14, 15);
        ww[i] = __builtin_shufflevector(x0, x1, 0, 1, 2, 3, 4, 5, 6, 7, 8, 9, 10, 11, 12, 13, 14, 15, 16, 17, 18, 19, 20, 21, 22, 23, 24, 25, 26, 27, 28, 29, 30, 31);
    }
#define F_IN "{v[0:15]}"(c0), "{s[32:47]}"(s0), "{s[48:63]}"(s1), "{s[64:67]}"(rws), "{a[0:31]}"(ww[0]), "{a[32:63]}"(ww[1]), "{a[64:95]}"(ww[2]), "{a[96:127]}"(ww[3]), "{a[128:159]}"(ww[4]), "{a[160:191]}"(ww[5]), "{a[192:223]}"(ww[6])
    if constexpr (is2) asm volatile("s_nop 4\n" ASM_F2 :: F_IN : "memory", "s95");
    else asm volatile("s_nop 7\ns_nop 7\n" ASM_F1 :: F_IN, "{a[224:255]}"(ww[7]), "{v[32:47]}"(u2) : "memory", "s95");
}

#define NTS ""
static_assert(ORD == 1, "see below");
__global__ void __launch_bounds__(256, 1) k_main(Args) {
    extern __shared__ __attribute__((aligned(16))) unsigned char lds[];
    const int xg = blockIdx.x & 7, ii = blockIdx.x >> 3;
    int wv = __builtin_amdgcn_readfirstlane(threadIdx.x >> 6); asm volatile("" : "+s"(wv));
    asm volatile("s_load_dwordx8 s[8:15], %0, 0\n s_load_dword s16, %0, 0x68\n s_getpc_b64 s[4:5]\n s_bfe_u32 s3, %1, 0x20003\n s_lshl_b32 s3, s3, 15\n v_lshl_add_u32 v1, %2, 7, s3\n v_min_u32 v1, %4, v1\n"
                 "s_mul_i32 s17, %3, 24576\n s_mov_b32 m0, s17\n s_cmp_gt_u32 %1, 127\n global_load_lds_dword v1, s[4:5]\n s_cbranch_scc1 3f\n"
                 "v_and_b32 v1, 63, %2\n v_lshrrev_b32 v2, 3, v1\n v_lshrrev_b32 v3, 4, v1\n v_and_b32 v3, 2, v3\n v_xor_b32 v1, v1, v3\n v_and_b32 v1, 7, v1\n v_lshlrev_b32 v1, 4, v1\n v_lshl_add_u32 v1, v2, 13, v1\n"
                 "s_and_b32 s3, %1, 7\n s_lshl_b32 s3, s3, 2\n s_lshr_b32 s18, %1, 5\n s_add_u32 s3, s3, s18\n s_lshl_b32 s3, s3, 1\n s_and_b32 s18, %3, 1\n s_add_u32 s3, s3, s18\n v_lshl_add_u32 v1, s3, 7, v1\n v_xor_b32 v2, 64, v1\n"
                 "s_bfe_u32 s19, %1, 0x10004\n s_lshl_b32 s19, s19, 22\n s_lshl_b32 s20, %3, 14\n s_add_u32 s20, s20, %5\n s_add_u32 m0, s20, 0x2000\n s_bfe_u32 s21, %1, 0x10003\n s_lshl_b32 s21, s21, 19\n s_lshr_b32 s18, %3, 1\n s_mul_i32 s22, s18, s21\n"
                 "s_mov_b32 s6, 0x800000\n s_mov_b32 s7, 0x20000\n s_mov_b32 s23, 0\n s_waitcnt lgkmcnt(0)\n s_cmp_lg_u32 s18, 0\n s_cselect_b64 s[4:5], s[10:11], s[8:9]\n s_cmp_lg_u32 s21, 0\n s_cselect_b64 s[4:5], s[12:13], s[4:5]\n s_and_b32 s5, s5, 0xffff\n"
                 "1: s_xor_b32 s3, s22, s19\n buffer_load_dwordx4 v1, s[4:7], s3 offen " NTS "lds\n s_add_u32 m0, m0, 0x400\n s_add_u32 s3, s3, 0x10000\n buffer_load_dwordx4 v2, s[4:7], s3 offen " NTS "lds\n s_add_u32 m0, m0, 0x400\n s_add_u32 s22, s22, 0x20000\n"
                 "s_add_u32 s23, s23, 1\n s_and_b32 s3, s23, 3\n s_cbranch_scc1 1b\n s_add_u32 s22, s22, s21\n s_cmp_eq_u32 s23, 4\n s_cselect_b32 m0, s17, m0\n s_cmp_eq_u32 s23, 16\n s_cselect_b32 m0, s20, m0\n s_cmp_lt_u32 s23, 20\n s_cbranch_scc1 1b\n"
                 "3: s_waitcnt lgkmcnt(0)"
                 :: "s"(__builtin_amdgcn_kernarg_segment_ptr()), "s"(blockIdx.x), "v"(threadIdx.x), "s"(wv), "n"(CODE_KB * 1024 - 4), "n"(L_H)
                 : "memory", "scc", "m0", "v1", "v2", "v3", "s3", "s4", "s5", "s6", "s7", "s8", "s9", "s10", "s11", "s12", "s13", "s14", "s15", "s16", "s17", "s18", "s19", "s20", "s21", "s22", "s23");
    const unsigned t0 = rt_now();
#define MODE_ blockIdx.x
    auto args = [&]() { PARS; Args r = *(const Args*)pp_; r.pad0 = wv; r.mode = MODE_; return r; };
    const Args a = args();
    if (threadIdx.x == 0) __hip_atomic_store((gu32*)(a.ws + O_GO + blockIdx.x * 128), 0u, RLX_AGENT);
    RSTAMP(75, 0);
    if (ii < 16) {
        if (ii & 1) s1_main<true>(args(), lds, xg, ii); else s1_main<false>(args(), lds, xg, ii);
        return;
    }
    const rsrc_t rs = __builtin_amdgcn_make_buffer_rsrc((void*)a.ws, (short)0, (int)WS_END, 0x00020000);
    unsigned* const ctl = (unsigned*)(a.ws + O_CTL);
    const int i4 = ii & 3, cg = 4 * (xg & 3) + i4;
    f_start(args(), rs, lds, (blockIdx.x - 64) & 127, t0);
    if (ii < 24) {
        rt_delay(TD_W - 200, t0);
        if (wv < 2) {
            v4u r = wv ? mkrs(a.x, S_TOK * DM * 2) : mkrs(a.ws, WS_END);
            asm volatile("s_mov_b32 m0, 0x8000\n\ts_nop 4\n\tbuffer_load_dword %0, %1, 0 offen sc1 lds" :: "v"(wv ? lane_id() * 65536 : lane_id() * 2 * MiB + (ii - 16) * 262144 + 7168), "s"(r) : "m0");
        }
        rt_delay(TD_W, t0);
        r_main(args(), rs, lds, xg, ii - 16, ctl, t0);
        return;
    }
    RSTAMP(75, 1);
    if (ii >= 28 && cg >= 14) return;
    if (ii < 28) f_main<true>(args(), rs, lds, xg, xg >> 1, 4 * (xg & 1) + i4, ctl);
    else f_main<false>(args(), rs, lds, xg, 0, 14 * (xg >> 2) + cg, ctl);
}

extern "C" void kernel_launch(void* const* d_in, const int* in_sizes, int n_in,
                              void* d_out, int out_size, void* d_ws, size_t ws_size,
                              hipStream_t stream) {
    Args a;
    a.x = (const u16*)d_in[0]; a.mem0 = (const u16*)d_in[1]; a.Wq = (const u16*)d_in[2]; a.Wk = (const u16*)d_in[3];
    a.Wv = (const u16*)d_in[4]; a.Wo = (const u16*)d_in[5];
    a.w1 = (const u16*)d_in[8]; a.b1 = (const u16*)d_in[9]; a.w2 = (const u16*)d_in[10]; a.b2 = (const u16*)d_in[11];
    a.out = (u16*)d_out; a.ws = (char*)d_ws + (size_t)WSB * MiB; a.mode = 0; a.pad0 = 0;
    a.c = (const u16*)d_in[12]; a.w_mod = (const u16*)d_in[13];
    hipFuncSetAttribute((const void*)k_main, hipFuncAttributeMaxDynamicSharedMemorySize, LDS_BYTES);
    k_main<<<256, 256, LDS_BYTES, stream>>>(a);
}
```

```cpp
#include <hip/hip_runtime.h>
#include <hip/hip_bf16.h>
#include <stdint.h>

typedef unsigned short u16;
typedef unsigned long long u64;
typedef __attribute__((ext_vector_type(8))) short bf16x8;
typedef __attribute__((ext_vector_type(4))) short bf16x4;
typedef __attribute__((ext_vector_type(4))) __bf16 bf16x4_t;
typedef __attribute__((ext_vector_type(8))) __bf16 v8bf;
typedef __attribute__((ext_vector_type(16))) __bf16 v16bf;
typedef __attribute__((ext_vector_type(4))) float f32x4;
typedef __attribute__((ext_vector_type(2))) float f32x2;
typedef unsigned v8u __attribute__((ext_vector_type(8)));
typedef unsigned v4u __attribute__((ext_vector_type(4)));
typedef unsigned v2u __attribute__((ext_vector_type(2)));
#define GAS __attribute__((address_space(1)))
#define LAS __attribute__((address_space(3)))
typedef GAS unsigned gu32;
#define RLX_AGENT __ATOMIC_RELAXED, __HIP_MEMORY_SCOPE_AGENT

constexpr int S_TOK = 2048, DM = 1024, NH = 32, DH = 128, MEMR = 8, SEG = 32, NSEG = 64;
constexpr int HD = NH * DH;
constexpr int QKN = 2 * HD;
constexpr int FF = 3584;

#define NTW 12
constexpr int RG = 16;
#define RGX 8
constexpr unsigned MiB = 1u << 20;
constexpr unsigned O_CTL = 0;
constexpr unsigned O_MOD = 65536;
constexpr unsigned O_POL = O_MOD + 32768;
constexpr unsigned O_MODP = 131072;
constexpr unsigned O_GO = 196608;
constexpr unsigned O_FL = 262144;
constexpr unsigned O_EP = 2 * MiB;
constexpr unsigned O_PS = O_EP + RG * 262144;
constexpr unsigned O_UM = O_PS + RG * 262144;
constexpr unsigned O_E1 = O_UM + RG * 524288;
constexpr unsigned O_H1 = O_E1 + RG * 131072;
constexpr unsigned O_UX = O_H1 + 4 * MiB;
constexpr unsigned O_D = O_UX + RGX * 2 * MiB, D_SLOT = 524288, D_X2 = 14 * 16384, D_ST = 22 * 16384;
constexpr unsigned O_SL = O_D + 32 * MiB;
constexpr unsigned WS_END = O_SL + 2 * MiB;
#define WSB 0
static_assert(WSB * MiB + WS_END <= 256 * MiB && O_UX % (2 * MiB) == 0 && O_E1 % (2 * MiB) == 0, "ws");
__device__ __forceinline__ unsigned nearg(unsigned pol, unsigned off, unsigned h) { return off + (((off >> 21) ^ pol ^ h) & 1) * 8192; }
constexpr int C_ABORT = 0;
constexpr int C_H = 32;
constexpr int C_Q = 64;
constexpr int C_T = 192;
constexpr int C_M = 256;
constexpr int C_A = 512;
constexpr unsigned O_HW = 49152;
constexpr int H_C = 0;
constexpr int H_D = 256;
constexpr int H_P = 1024;
__device__ __forceinline__ unsigned hw(unsigned pol, int x) { return nearg(pol, O_HW, x >> 2) + (x & 3) * 2048; }

__device__ __forceinline__ float bf2f(u16 v) { return __uint_as_float(((unsigned)v) << 16); }
__device__ __forceinline__ u16 f2bf(float f) {
    __hip_bfloat16 h = __float2bfloat16(f);
    return __builtin_bit_cast(u16, h);
}
__device__ __forceinline__ float rbf(float f) { return bf2f(f2bf(f)); }

#define CODE_KB 96
#define NPR 8
enum { P_A, P_B, P_EU, P_DMIN, P_R, P_TP, P_TU, P_T0, P_X, P_TZ, P_TF, P_X2, P_T1, P_TS, P_TT, P_TY, P_T2, P_A0, P_A1, P_M, P_B2, P_TW, NPAR };
#define CH_PAR 152, 76, 8, 4, 60, 100, 36, 440, 36, -30u, 40, 72, 190, 28, 12, 128, 144, 40, 110, 8, 72, 84
struct Args {
    const u16* Wq; const u16* Wk; const u16* Wv; const u16* Wo;
    const u16* c; const u16* w_mod; const u16* mem0;
    const u16* x; const u16* w1; const u16* b1; const u16* w2; const u16* b2;
    u16* out; char* ws;
    unsigned mode, pad0;
};
#define PARS auto pp_ = (__attribute__((address_space(4))) const Args*)__builtin_amdgcn_kernarg_segment_ptr(); asm volatile("" : "+s"(pp_))
constexpr unsigned PARV[NPAR] = {CH_PAR};
#define par(i) PARV[i]
#define RSTAMP(sg, i) do { } while (0)
#define STAMP_WAVE 0
#define DBG(a, i) 0ull
constexpr unsigned SPIN_CAP = 1u << 22;
constexpr unsigned SENT = 0xFFFFFFFFu;

__device__ __forceinline__ void lds_barrier() { asm volatile("s_waitcnt lgkmcnt(0)\n\ts_barrier" ::: "memory"); }
__device__ __forceinline__ void wait_vm0() { asm volatile("s_waitcnt vmcnt(0)" ::: "memory"); }
typedef __bf16 bf16x2_t __attribute__((ext_vector_type(2)));
__device__ __forceinline__ unsigned pack2(float a, float b) { const f32x2 v = {a, b}; return __builtin_bit_cast(unsigned, __builtin_convertvector(v, bf16x2_t)); }
__device__ __forceinline__ float lo16f(unsigned u) { return __uint_as_float(u << 16); }
__device__ __forceinline__ float hi16f(unsigned u) { return __uint_as_float(u & 0xFFFF0000u); }
__device__ __forceinline__ unsigned umax4(v4u x) { return max(max(x.x, x.y), max(x.z, x.w)); }
__device__ __forceinline__ unsigned unsent(unsigned x) { return min(x, 0xFFFFFFFEu); }

__device__ __forceinline__ unsigned ldg4_sc1(const void* sb, unsigned vo) {
    unsigned r;
    asm volatile("global_load_dword %0, %1, %2 sc1\n\ts_waitcnt vmcnt(0)" : "=&v"(r) : "v"(vo), "s"(sb) : "memory");
    return r;
}
__device__ __forceinline__ bool spin_fail(unsigned& spins, unsigned* abortw) {
    if (++spins > SPIN_CAP) { __hip_atomic_store((gu32*)abortw, 1u, RLX_AGENT); return true; }
    if ((spins & 63u) == 0 && __builtin_amdgcn_readfirstlane(__hip_atomic_load((gu32*)abortw, RLX_AGENT)) == 1u) return true;
    return false;
}
__device__ __forceinline__ float red16_add(float x) { auto r = __builtin_amdgcn_permlane16_swap(__float_as_uint(x), __float_as_uint(x), false, false); return __uint_as_float(r[0]) + __uint_as_float(r[1]); }
__device__ __forceinline__ float red32_add(float x) { auto r = __builtin_amdgcn_permlane32_swap(__float_as_uint(x), __float_as_uint(x), false, false); return __uint_as_float(r[0]) + __uint_as_float(r[1]); }
__device__ __forceinline__ v2u tr_rd(const unsigned char* lds_base, unsigned off) {
    return __builtin_bit_cast(v2u, __builtin_amdgcn_ds_read_tr16_b64_v4bf16((LAS bf16x4_t*)(lds_base + off)));
}
__device__ __forceinline__ v4u lds16(const unsigned char* p) { return *(const LAS v4u*)p; }
__device__ __forceinline__ void dma16(const void* src, unsigned char* dst, int aux) {
    if (aux) __builtin_amdgcn_global_load_lds((const unsigned*)src, (unsigned*)dst, 16, 0, 2);
    else __builtin_amdgcn_global_load_lds((const unsigned*)src, (unsigned*)dst, 16, 0, 0);
}

__device__ __forceinline__ v4u mkrs(const void* p, unsigned bytes) { return (v4u){(unsigned)(u64)p, (unsigned)((u64)p >> 32) & 0xFFFFu, bytes, 0x00020000u}; }
template <int AUX> __device__ __forceinline__ void bdma(v4u r, unsigned vo, unsigned so, unsigned ldsaddr) {
    if (AUX == 16) asm volatile("s_mov_b32 m0, %0\n\ts_nop 0\n\tbuffer_load_dwordx4 %1, %2, %3 offen sc1 lds" :: "s"(ldsaddr), "v"(vo), "s"(r), "s"(so) : "m0");
    else if (AUX) asm volatile("s_mov_b32 m0, %0\n\ts_nop 0\n\tbuffer_load_dwordx4 %1, %2, %3 offen nt lds" :: "s"(ldsaddr), "v"(vo), "s"(r), "s"(so) : "m0");
    else asm volatile("s_mov_b32 m0, %0\n\ts_nop 0\n\tbuffer_load_dwordx4 %1, %2, %3 offen lds" :: "s"(ldsaddr), "v"(vo), "s"(r), "s"(so) : "m0");
}

template <int I> struct IC { static constexpr int v = I; };
template <int B, int E, class F> __device__ __forceinline__ void sfor(F&& f) { if constexpr (B < E) { f(IC<B>{}); sfor<B + 1, E>(f); } }
#define SB() __builtin_amdgcn_sched_barrier(0)

__device__ __forceinline__ int lane_id() { int l; asm volatile("v_mbcnt_lo_u32_b32 %0, -1, 0\n\tv_mbcnt_hi_u32_b32 %0, -1, %0" : "=v"(l)); return l; }
constexpr int LDS_BYTES = 163840;
typedef __amdgpu_buffer_rsrc_t rsrc_t;
__device__ __forceinline__ v4u bld16(rsrc_t r, unsigned off) { return __builtin_bit_cast(v4u, __builtin_amdgcn_raw_buffer_load_b128(r, off, 0, 16)); }
__device__ __forceinline__ v2u bld8(rsrc_t r, unsigned off) { return __builtin_bit_cast(v2u, __builtin_amdgcn_raw_buffer_load_b64(r, off, 0, 16)); }
__device__ __forceinline__ void bst16(rsrc_t r, unsigned off, v4u v) { __builtin_amdgcn_raw_buffer_store_b128(__builtin_bit_cast(__attribute__((ext_vector_type(4))) unsigned, v), r, off, 0, 16); }
__device__ __forceinline__ void bst8(rsrc_t r, unsigned off, v2u v) { __builtin_amdgcn_raw_buffer_store_b64(__builtin_bit_cast(__attribute__((ext_vector_type(2))) unsigned, v), r, off, 0, 16); }

constexpr int L_FLAG = LDS_BYTES - 64;
#define otid() (a.pad0 * 64 + lane_id())
__device__ __forceinline__ v4u sld16(rsrc_t r, unsigned vo, unsigned so) { return __builtin_bit_cast(v4u, __builtin_amdgcn_raw_buffer_load_b128(r, vo, so, 16)); }
__device__ __forceinline__ v4u sentv() { unsigned x = SENT; asm volatile("" : "+v"(x)); return (v4u){x, x, x, x}; }
#define BW_SLEEP 16
template <int SLP = BW_SLEEP> __device__ __forceinline__ bool wave_wait(const unsigned* p, int stride, int n, unsigned target, unsigned* abortw, unsigned char* lds = nullptr) {
    const int lane = lane_id();
    for (unsigned spins = 0;;) {
        const unsigned v = lane < n ? __hip_atomic_load((gu32*)(p + lane * stride), RLX_AGENT) : target;
        if (!__any(v < target)) { if (lds && lane == 0) *(volatile LAS unsigned*)(lds + L_FLAG + 32) = v; return true; }
        __builtin_amdgcn_s_sleep(SLP);
        if (++spins > (SPIN_CAP << 1)) { __hip_atomic_store((gu32*)abortw, 1u, RLX_AGENT); return false; }
        if ((spins & 63u) == 0 && __builtin_amdgcn_readfirstlane(__hip_atomic_load((gu32*)abortw, RLX_AGENT)) == 1u) return false;
    }
}
__device__ __forceinline__ unsigned wait_go(const char* ws) {
    for (unsigned spins = 0;;) {
        const unsigned v = __builtin_amdgcn_readfirstlane(ldg4_sc1(ws, O_GO + blockIdx.x * 128));
        if (v) return v;
        __builtin_amdgcn_s_sleep(8);
        if (++spins > SPIN_CAP) asm volatile("s_endpgm");
    }
}
template <int SLP = 16> __device__ __forceinline__ unsigned wait_set(const char* ws, unsigned* ctl) {
    if (!wave_wait<SLP>(ctl + C_A, 32, 8, 16, ctl + C_ABORT)) asm volatile("s_endpgm");
    return __builtin_amdgcn_readfirstlane(ldg4_sc1(ws, O_POL));
}
__device__ __forceinline__ void drained(unsigned* cnt) {
    asm volatile("s_waitcnt vmcnt(0)\n\ts_barrier" ::: "memory");
    if (threadIdx.x == 0) __hip_atomic_fetch_add((gu32*)cnt, 1u, RLX_AGENT);
}

constexpr int L_W = 0;
constexpr int L_H = 98304;
constexpr int L_KX = L_H + 40960;
constexpr int L_QX = L_KX + 8192;
constexpr int L_T = L_QX + 8192;
constexpr int L_ST = L_T + 4096;
constexpr int lsl(bool isV, int c, int n) { return isV ? (c < 6 && (n & 1) ? 2 * c + (n >> 1) : -1) : (c < 12 && n == 1 ? c : -1); }
constexpr int ridx(bool isV, int c, int n) { int r = 0; for (int i = 0; i < 4 * c + n; ++i) r += lsl(isV, i >> 2, i & 3) < 0; return r; }
#define ASM_Q \
    "s_add_u32 m0, s41, 0\ns_xor_b32 s85, s62, 0\ns_add_u32 s85, s85, s40\nbuffer_load_dwordx4 v11, s[64:67], s85 offen sc1 lds\n" \
    "buffer_load_dwordx4 v12, s[64:67], s85 offen offset:1024 sc1 lds\ns_add_u32 m0, s41, 8192\ns_xor_b32 s85, s62, 256\ns_add_u32 s85, s85, s40\n" \
    "buffer_load_dwordx4 v11, s[64:67], s85 offen sc1 lds\nbuffer_load_dwordx4 v12, s[64:67], s85 offen offset:1024 sc1 lds\ns_add_u32 m0, s41, 16384\ns_xor_b32 s85, s62, 512\n" \
    "s_add_u32 s85, s85, s40\nbuffer_load_dwordx4 v11, s[64:67], s85 offen sc1 lds\nbuffer_load_dwordx4 v12, s[64:67], s85 offen offset:1024 sc1 lds\ns_memrealtime s[72:73]\n" \
    "s_waitcnt lgkmcnt(0)\ns_mov_b32 s80, s72\ns_mov_b32 s83, 0x7fff0000\ns_mov_b32 s19, s35\ns_branch .Lq8_%=\n.Lq9_%=:\ns_mov_b32 s35, s19\ns_mov_b32 s19, 64\n" \
    "s_waitcnt vmcnt(0)\ns_memrealtime s[72:73]\ns_waitcnt lgkmcnt(0)\nv_mov_b32 v253, 1\nv_mov_b32 v252, s72\ns_or_b32 s69, s35, s36\ns_cselect_b32 s69, 0x7fff0000, 0\n" \
    "s_mov_b64 exec, 1\nbuffer_atomic_umax v252, off, s[64:67], s69 offset:260\nbuffer_atomic_add v253, off, s[64:67], s69 offset:256\ns_mov_b64 exec, -1\ns_mov_b32 s16, 0\n" \
    "s_cmp_eq_u32 s36, 0\ns_cselect_b32 s17, 511, -1\ns_mov_b32 s18, s44\ns_mov_b32 s72, s80\ns_barrier\n.Lq2_%=:\nds_read_b128 v[64:67], v3 offset:0\n" \
    "ds_read_b128 v[68:71], v9 offset:0\nds_read_b128 v[72:75], v9 offset:4096\nv_mov_b32_dpp v16, v20 quad_perm:[0,1,2,3] row_mask:0xf bank_mask:0xc\n" \
    "v_mov_b32_dpp v17, v21 quad_perm:[0,1,2,3] row_mask:0xf bank_mask:0xc\nv_mov_b32_dpp v18, v22 quad_perm:[0,1,2,3] row_mask:0xf bank_mask:0xc\n" \
    "v_mov_b32_dpp v19, v23 quad_perm:[0,1,2,3] row_mask:0xf bank_mask:0xc\nv_mov_b32 v254, 0xff800000\nv_cvt_pk_bf16_f32 v16, 0, v16\nv_cvt_pk_bf16_f32 v17, 0, v17\n" \
    "v_cvt_pk_bf16_f32 v18, 0, v18\nv_cvt_pk_bf16_f32 v19, 0, v19\nv_mul_f32 v16, s33, v16\nv_mul_f32 v17, s33, v17\nv_mul_f32 v18, s33, v18\ns_waitcnt lgkmcnt(0)\n" \
    "v_mfma_f32_16x16x32_bf16 v[48:51], a[8:11], v[68:71], 0\ns_add_u32 m0, s41, 24576\nv_mul_f32 v19, s33, v19\nv_cvt_pk_bf16_f32 v16, 0, v16\n" \
    "v_mfma_f32_16x16x32_bf16 v[52:55], a[8:11], v[72:75], 0\nds_read_b128 v[76:79], v3 offset:1024\nv_cvt_pk_bf16_f32 v17, 0, v17\nv_cvt_pk_bf16_f32 v18, 0, v18\n" \
    "v_mfma_f32_16x16x32_bf16 v[56:59], a[16:19], v[68:71], 0\nds_read_b128 v[80:83], v10 offset:0\nv_cvt_pk_bf16_f32 v19, 0, v19\nv_cndmask_b32 v16, v254, v16, s[50:51]\n" \
    "v_mfma_f32_16x16x32_bf16 v[60:63], a[16:19], v[72:75], 0\nds_read_b128 v[84:87], v10 offset:4096\nv_cndmask_b32 v17, v254, v17, s[52:53]\n" \
    "v_cndmask_b32 v18, v254, v18, s[54:55]\nv_mfma_f32_16x16x32_bf16 v[32:35], a[0:3], v[68:71], 0\ns_xor_b32 s85, s62, 768\nv_cndmask_b32 v19, v254, v19, s[56:57]\n" \
    "v_max3_f32 v254, v16, v17, v18\nv_mfma_f32_16x16x32_bf16 v[36:39], a[0:3], v[72:75], 0\ns_add_u32 s85, s85, s40\nv_max_f32 v254, v254, v19\n" \
    "v_mfma_f32_16x16x32_bf16 v[40:43], v[64:67], v[68:71], 0\nbuffer_load_dwordx4 v11, s[64:67], s85 offen sc1 lds\n" \
    "v_max_f32_dpp v252, v254, v254 row_ror:8 row_mask:0xf bank_mask:0xf\nv_max_f32_dpp v253, v254, v254 row_ror:8 row_mask:0xf bank_mask:0xf\n" \
    "v_mfma_f32_16x16x32_bf16 v[44:47], v[64:67], v[72:75], 0\ns_nop 0\ns_waitcnt lgkmcnt(0)\nv_permlane32_swap_b32 v252, v253\n" \
    "v_mfma_f32_16x16x32_bf16 v[48:51], a[12:15], v[80:83], v[48:51]\nv_max_f32 v252, v252, v253\nv_cvt_pk_bf16_f32 v24, 0, v24\n" \
    "v_mfma_f32_16x16x32_bf16 v[52:55], a[12:15], v[84:87], v[52:55]\nds_read_b128 v[88:91], v3 offset:2048\nv_cvt_pk_bf16_f32 v25, 0, v25\nv_cvt_pk_bf16_f32 v26, 0, v26\n" \
    "v_mfma_f32_16x16x32_bf16 v[56:59], a[20:23], v[80:83], v[56:59]\nds_read_b128 v[64:67], v9 offset:1024\nv_cvt_pk_bf16_f32 v27, 0, v27\nv_mul_f32 v24, s33, v24\n" \
    "v_mfma_f32_16x16x32_bf16 v[60:63], a[20:23], v[84:87], v[60:63]\nds_read_b128 v[68:71], v9 offset:5120\nv_mul_f32 v25, s33, v25\nv_mul_f32 v26, s33, v26\n" \
    "v_mfma_f32_16x16x32_bf16 v[32:35], a[4:7], v[80:83], v[32:35]\nbuffer_load_dwordx4 v12, s[64:67], s85 offen offset:1024 sc1 lds\nv_mul_f32 v27, s33, v27\n" \
    "v_cvt_pk_bf16_f32 v24, 0, v24\nv_mfma_f32_16x16x32_bf16 v[36:39], a[4:7], v[84:87], v[36:39]\ns_add_u32 m0, s41, 32768\nv_cvt_pk_bf16_f32 v25, 0, v25\n" \
    "v_cvt_pk_bf16_f32 v26, 0, v26\nv_mfma_f32_16x16x32_bf16 v[40:43], v[76:79], v[80:83], v[40:43]\ns_xor_b32 s85, s62, 1024\nv_cvt_pk_bf16_f32 v27, 0, v27\n" \
    "v_max3_f32 v254, v252, v24, v25\nv_mfma_f32_16x16x32_bf16 v[44:47], v[76:79], v[84:87], v[44:47]\ns_add_u32 s85, s85, s40\ns_waitcnt lgkmcnt(0)\n" \
    "v_max3_f32 v252, v254, v26, v27\nv_mfma_f32_16x16x32_bf16 v[48:51], a[32:35], v[64:67], v[48:51]\nbuffer_load_dwordx4 v11, s[64:67], s85 offen sc1 lds\n" \
    "v_max3_f32 v253, v254, v26, v27\nv_mfma_f32_16x16x32_bf16 v[52:55], a[32:35], v[68:71], v[52:55]\nds_read_b128 v[72:75], v3 offset:3072\nv_permlane16_swap_b32 v252, v253\n" \
    "v_mfma_f32_16x16x32_bf16 v[56:59], a[40:43], v[64:67], v[56:59]\nds_read_b128 v[76:79], v10 offset:1024\nv_max_f32 v252, v252, v253\nv_sub_f32 v16, v16, v252\n" \
    "v_mfma_f32_16x16x32_bf16 v[60:63], a[40:43], v[68:71], v[60:63]\nds_read_b128 v[80:83], v10 offset:5120\nv_sub_f32 v17, v17, v252\nv_sub_f32 v18, v18, v252\n" \
    "v_mfma_f32_16x16x32_bf16 v[32:35], a[24:27], v[64:67], v[32:35]\nbuffer_load_dwordx4 v12, s[64:67], s85 offen offset:1024 sc1 lds\nv_sub_f32 v19, v19, v252\n" \
    "v_sub_f32 v24, v24, v252\nv_mfma_f32_16x16x32_bf16 v[36:39], a[24:27], v[68:71], v[36:39]\ns_add_u32 s84, s40, 0x20000\nv_sub_f32 v25, v25, v252\nv_sub_f32 v26, v26, v252\n" \
    "v_mfma_f32_16x16x32_bf16 v[40:43], v[88:91], v[64:67], v[40:43]\ns_cmp_lt_u32 s35, 62\ns_cselect_b32 s84, s84, s40\nv_sub_f32 v27, v27, v252\n" \
    "v_mfma_f32_16x16x32_bf16 v[44:47], v[88:91], v[68:71], v[44:47]\ns_sub_u32 s20, s72, s80\ns_waitcnt lgkmcnt(0)\nv_mul_f32 v16, s34, v16\n" \
    "v_mfma_f32_16x16x32_bf16 v[48:51], a[36:39], v[76:79], v[48:51]\ns_add_u32 s20, s20, 0\nv_mul_f32 v17, s34, v17\nv_mul_f32 v18, s34, v18\n" \
    "v_mfma_f32_16x16x32_bf16 v[52:55], a[36:39], v[80:83], v[52:55]\nds_read_b128 v[64:67], v3 offset:4096\nv_mul_f32 v19, s34, v19\nv_mul_f32 v24, s34, v24\n" \
    "v_mfma_f32_16x16x32_bf16 v[56:59], a[44:47], v[76:79], v[56:59]\nds_read_b128 v[68:71], v9 offset:8192\nv_mul_f32 v25, s34, v25\nv_mul_f32 v26, s34, v26\n" \
    "v_mfma_f32_16x16x32_bf16 v[60:63], a[44:47], v[80:83], v[60:63]\nds_read_b128 v[84:87], v9 offset:12288\nv_mul_f32 v27, s34, v27\nv_exp_f32 v16, v16\n" \
    "v_mfma_f32_16x16x32_bf16 v[32:35], a[28:31], v[76:79], v[32:35]\ns_min_u32 s21, s20, s16\nv_exp_f32 v17, v17\n" \
    "v_mfma_f32_16x16x32_bf16 v[36:39], a[28:31], v[80:83], v[36:39]\ns_mov_b32 s16, s20\nv_exp_f32 v18, v18\nv_mfma_f32_16x16x32_bf16 v[40:43], v[72:75], v[76:79], v[40:43]\n" \
    "s_cmp_gt_u32 s21, 156\ns_cselect_b32 s21, 0, s21\nv_exp_f32 v19, v19\nv_mfma_f32_16x16x32_bf16 v[44:47], v[72:75], v[80:83], v[44:47]\ns_lshr_b32 s20, s35, 2\n" \
    "s_waitcnt lgkmcnt(0)\nv_exp_f32 v24, v24\nv_mfma_f32_16x16x32_bf16 v[48:51], a[56:59], v[68:71], v[48:51]\ns_lshl_b32 s22, s20, 8\nv_add_f32 v254, v16, v18\n" \
    "v_add_f32 v255, v17, v19\nv_mfma_f32_16x16x32_bf16 v[52:55], a[56:59], v[84:87], v[52:55]\nds_read_b128 v[88:91], v3 offset:5120\nv_exp_f32 v25, v25\n" \
    "v_mfma_f32_16x16x32_bf16 v[56:59], a[64:67], v[68:71], v[56:59]\nds_read_b128 v[72:75], v10 offset:8192\nv_add_f32 v254, v254, v255\nv_exp_f32 v26, v26\n" \
    "v_mfma_f32_16x16x32_bf16 v[60:63], a[64:67], v[84:87], v[60:63]\nds_read_b128 v[76:79], v10 offset:12288\nv_exp_f32 v27, v27\n" \
    "v_mfma_f32_16x16x32_bf16 v[32:35], a[48:51], v[68:71], v[32:35]\ns_or_b32 s21, s21, s22\nv_add_f32_dpp v252, v254, v254 row_ror:8 row_mask:0xf bank_mask:0xf\n" \
    "v_add_f32_dpp v253, v254, v254 row_ror:8 row_mask:0xf bank_mask:0xf\nv_mfma_f32_16x16x32_bf16 v[36:39], a[48:51], v[84:87], v[36:39]\ns_and_b32 s20, s20, 1\n" \
    "v_add_f32 v254, v24, v26\nv_add_f32 v255, v25, v27\nv_mfma_f32_16x16x32_bf16 v[40:43], v[64:67], v[68:71], v[40:43]\ns_lshl_b32 s20, s20, 2\n" \
    "v_permlane32_swap_b32 v252, v253\nv_mfma_f32_16x16x32_bf16 v[44:47], v[64:67], v[84:87], v[44:47]\ns_cmp_gt_u32 s21, s17\ns_cselect_b32 s20, s20, 0x7fff0000\n" \
    "s_waitcnt lgkmcnt(0)\nv_mfma_f32_16x16x32_bf16 v[48:51], a[60:63], v[72:75], v[48:51]\ns_max_u32 s17, s17, s21\nv_add_f32 v254, v254, v252\nv_add_f32 v255, v255, v253\n" \
    "v_mfma_f32_16x16x32_bf16 v[52:55], a[60:63], v[76:79], v[52:55]\nds_read_b128 v[64:67], v3 offset:6144\nv_add_f32 v252, v254, v255\nv_add_f32 v253, v255, v254\n" \
    "v_mfma_f32_16x16x32_bf16 v[56:59], a[68:71], v[72:75], v[56:59]\nds_read_b128 v[68:71], v9 offset:9216\nv_permlane16_swap_b32 v252, v253\n" \
    "v_mfma_f32_16x16x32_bf16 v[60:63], a[68:71], v[76:79], v[60:63]\nds_read_b128 v[80:83], v9 offset:13312\nv_add_f32 v252, v252, v253\nv_rcp_f32 v252, v252\n" \
    "v_mfma_f32_16x16x32_bf16 v[32:35], a[52:55], v[72:75], v[32:35]\nv_mov_b32 v15, s21\ns_mov_b64 exec, 1\nbuffer_atomic_umax v15, off, s[64:67], s20 offset:768\n" \
    "s_mov_b64 exec, -1\nv_mfma_f32_16x16x32_bf16 v[36:39], a[52:55], v[76:79], v[36:39]\nv_mul_f32 v16, v16, v252\nv_mul_f32 v17, v17, v252\n" \
    "v_mfma_f32_16x16x32_bf16 v[40:43], v[88:91], v[72:75], v[40:43]\nv_mul_f32 v24, v24, v252\nv_mul_f32 v25, v25, v252\n" \
    "v_mfma_f32_16x16x32_bf16 v[44:47], v[88:91], v[76:79], v[44:47]\ns_waitcnt lgkmcnt(0)\nv_cvt_pk_bf16_f32 v20, v16, v17\nv_cvt_pk_bf16_f32 v22, v24, v25\n" \
    "v_mfma_f32_16x16x32_bf16 v[48:51], a[80:83], v[68:71], v[48:51]\nv_mul_f32 v18, v18, v252\nv_mul_f32 v19, v19, v252\n" \
    "v_mfma_f32_16x16x32_bf16 v[52:55], a[80:83], v[80:83], v[52:55]\nds_read_b128 v[72:75], v3 offset:7168\nv_mul_f32 v26, v26, v252\nv_mul_f32 v27, v27, v252\n" \
    "v_mfma_f32_16x16x32_bf16 v[56:59], a[88:91], v[68:71], v[56:59]\nds_read_b128 v[76:79], v10 offset:9216\nv_cvt_pk_bf16_f32 v21, v18, v19\nv_cvt_pk_bf16_f32 v23, v26, v27\n" \
    "v_mfma_f32_16x16x32_bf16 v[60:63], a[88:91], v[80:83], v[60:63]\nds_read_b128 v[84:87], v10 offset:13312\nv_permlane32_swap_b32 v20, v22\n" \
    "v_mfma_f32_16x16x32_bf16 v[32:35], a[72:75], v[68:71], v[32:35]\nv_permlane32_swap_b32 v21, v23\nv_mfma_f32_16x16x32_bf16 v[36:39], a[72:75], v[80:83], v[36:39]\n" \
    "v_writelane_b32 v20, s18, 40\nv_mfma_f32_16x16x32_bf16 v[40:43], v[64:67], v[68:71], v[40:43]\ns_xor_b32 s74, s83, 0x2000\n" \
    "buffer_store_dwordx4 v[20:23], v7, s[64:67], s83 offen sc1\nv_mfma_f32_16x16x32_bf16 v[44:47], v[64:67], v[80:83], v[44:47]\n" \
    "buffer_store_dwordx4 v[20:23], v7, s[64:67], s74 offen sc1\ns_waitcnt lgkmcnt(0)\nv_mov_b64 v[28:29], -1\nv_mfma_f32_16x16x32_bf16 v[48:51], a[84:87], v[76:79], v[48:51]\n" \
    "v_mov_b64 v[30:31], -1\nv_mfma_f32_16x16x32_bf16 v[52:55], a[84:87], v[84:87], v[52:55]\nds_read_b128 v[80:83], v3 offset:8192\ns_add_u32 s69, s35, 2\n" \
    "v_mfma_f32_16x16x32_bf16 v[56:59], a[92:95], v[76:79], v[56:59]\nds_read_b128 v[88:91], v9 offset:16384\ns_and_b32 s69, s69, 15\n" \
    "v_mfma_f32_16x16x32_bf16 v[60:63], a[92:95], v[84:87], v[60:63]\nds_read_b128 v[64:67], v9 offset:20480\ns_lshl_b32 s69, s69, 18\n" \
    "v_mfma_f32_16x16x32_bf16 v[32:35], a[76:79], v[76:79], v[32:35]\ns_add_u32 s82, s38, s69\ns_lshr_b32 s69, s82, 8\n" \
    "v_mfma_f32_16x16x32_bf16 v[36:39], a[76:79], v[84:87], v[36:39]\ns_and_b32 s69, s69, 0x2000\ns_xor_b32 s82, s82, s69\n" \
    "v_mfma_f32_16x16x32_bf16 v[40:43], v[72:75], v[76:79], v[40:43]\ns_add_u32 s69, s35, 2\ns_and_b32 s69, s69, 15\n" \
    "v_mfma_f32_16x16x32_bf16 v[44:47], v[72:75], v[84:87], v[44:47]\ns_lshl_b32 s69, s69, 18\ns_add_u32 s83, s39, s69\ns_waitcnt lgkmcnt(0)\n" \
    "v_mfma_f32_16x16x32_bf16 v[48:51], a[104:107], v[88:91], v[48:51]\ns_xor_b32 s74, s82, 0x4000\ns_xor_b32 s75, s83, 0x2000\n" \
    "v_mfma_f32_16x16x32_bf16 v[52:55], a[104:107], v[64:67], v[52:55]\nds_read_b128 v[84:87], v3 offset:9216\n" \
    "v_mfma_f32_16x16x32_bf16 v[56:59], a[112:115], v[88:91], v[56:59]\nds_read_b128 v[68:71], v10 offset:16384\n" \
    "v_mfma_f32_16x16x32_bf16 v[60:63], a[112:115], v[64:67], v[60:63]\nds_read_b128 v[72:75], v10 offset:20480\n" \
    "v_mfma_f32_16x16x32_bf16 v[32:35], a[96:99], v[88:91], v[32:35]\nv_mfma_f32_16x16x32_bf16 v[36:39], a[96:99], v[64:67], v[36:39]\n" \
    "v_mfma_f32_16x16x32_bf16 v[40:43], v[80:83], v[88:91], v[40:43]\nbuffer_store_dwordx4 v[28:31], v7, s[64:67], s82 offen sc1\n" \
    "v_mfma_f32_16x16x32_bf16 v[44:47], v[80:83], v[64:67], v[44:47]\ns_waitcnt lgkmcnt(0)\nv_mfma_f32_16x16x32_bf16 v[48:51], a[108:111], v[68:71], v[48:51]\n" \
    "v_mfma_f32_16x16x32_bf16 v[52:55], a[108:111], v[72:75], v[52:55]\nds_read_b128 v[64:67], v3 offset:10240\n" \
    "v_mfma_f32_16x16x32_bf16 v[56:59], a[116:119], v[68:71], v[56:59]\nds_read_b128 v[88:91], v9 offset:17408\n" \
    "v_mfma_f32_16x16x32_bf16 v[60:63], a[116:119], v[72:75], v[60:63]\nds_read_b128 v[76:79], v9 offset:21504\n" \
    "v_mfma_f32_16x16x32_bf16 v[32:35], a[100:103], v[68:71], v[32:35]\nbuffer_store_dwordx4 v[28:31], v7, s[64:67], s74 offen sc1\n" \
    "v_mfma_f32_16x16x32_bf16 v[36:39], a[100:103], v[72:75], v[36:39]\nv_mfma_f32_16x16x32_bf16 v[40:43], v[84:87], v[68:71], v[40:43]\n" \
    "v_mfma_f32_16x16x32_bf16 v[44:47], v[84:87], v[72:75], v[44:47]\ns_waitcnt lgkmcnt(0)\nv_mfma_f32_16x16x32_bf16 v[48:51], a[128:131], v[88:91], v[48:51]\n" \
    "v_mfma_f32_16x16x32_bf16 v[52:55], a[128:131], v[76:79], v[52:55]\nds_read_b128 v[68:71], v3 offset:11264\n" \
    "v_mfma_f32_16x16x32_bf16 v[56:59], a[136:139], v[88:91], v[56:59]\nds_read_b128 v[72:75], v10 offset:17408\nbuffer_store_dwordx4 v[28:31], v7, s[64:67], s83 offen sc1\n" \
    "v_mfma_f32_16x16x32_bf16 v[60:63], a[136:139], v[76:79], v[60:63]\nds_read_b128 v[80:83], v10 offset:21504\n" \
    "v_mfma_f32_16x16x32_bf16 v[32:35], a[120:123], v[88:91], v[32:35]\nv_mfma_f32_16x16x32_bf16 v[36:39], a[120:123], v[76:79], v[36:39]\n" \
    "v_mfma_f32_16x16x32_bf16 v[40:43], v[64:67], v[88:91], v[40:43]\nv_mfma_f32_16x16x32_bf16 v[44:47], v[64:67], v[76:79], v[44:47]\ns_waitcnt lgkmcnt(0)\n" \
    "v_mfma_f32_16x16x32_bf16 v[48:51], a[132:135], v[72:75], v[48:51]\nbuffer_store_dwordx4 v[28:31], v7, s[64:67], s75 offen sc1\nv_lshrrev_b32 v16, 1, v2\n" \
    "v_add_u32 v16, s60, v16\nv_mfma_f32_16x16x32_bf16 v[52:55], a[132:135], v[80:83], v[52:55]\nds_read_b128 v[64:67], v3 offset:12288\ns_waitcnt vmcnt(7)\ns_barrier\n" \
    "v_mfma_f32_16x16x32_bf16 v[56:59], a[140:143], v[72:75], v[56:59]\nds_read_b128 v[76:79], v9 offset:24576\n" \
    "v_mfma_f32_16x16x32_bf16 v[60:63], a[140:143], v[80:83], v[60:63]\nds_read_b128 v[84:87], v9 offset:28672\ns_add_u32 m0, s41, 0\n" \
    "v_mfma_f32_16x16x32_bf16 v[32:35], a[124:127], v[72:75], v[32:35]\ns_xor_b32 s85, s62, 1280\ns_add_u32 s85, s85, s40\n" \
    "v_mfma_f32_16x16x32_bf16 v[36:39], a[124:127], v[80:83], v[36:39]\nbuffer_load_dwordx4 v11, s[64:67], s85 offen sc1 lds\nds_write_b64 v16, v[28:29]\n" \
    "v_mfma_f32_16x16x32_bf16 v[40:43], v[68:71], v[72:75], v[40:43]\nds_write_b64 v16, v[28:29] offset:2048\ns_add_u32 s69, s35, 2\n" \
    "v_mfma_f32_16x16x32_bf16 v[44:47], v[68:71], v[80:83], v[44:47]\ns_and_b32 s69, s69, 255\ns_lshl_b32 s69, s69, 19\ns_waitcnt lgkmcnt(2)\n" \
    "v_mfma_f32_16x16x32_bf16 v[48:51], a[152:155], v[76:79], v[48:51]\nbuffer_load_dwordx4 v12, s[64:67], s85 offen offset:1024 sc1 lds\ns_add_u32 m0, s41, 8192\n" \
    "v_mfma_f32_16x16x32_bf16 v[52:55], a[152:155], v[84:87], v[52:55]\nds_read_b128 v[80:83], v3 offset:13312\ns_xor_b32 s85, s62, 1536\n" \
    "v_mfma_f32_16x16x32_bf16 v[56:59], a[160:163], v[76:79], v[56:59]\nds_read_b128 v[88:91], v10 offset:24576\ns_add_u32 s85, s85, s40\n" \
    "v_mfma_f32_16x16x32_bf16 v[60:63], a[160:163], v[84:87], v[60:63]\nds_read_b128 v[68:71], v10 offset:28672\nbuffer_load_dwordx4 v11, s[64:67], s85 offen sc1 lds\n" \
    "v_mfma_f32_16x16x32_bf16 v[32:35], a[144:147], v[76:79], v[32:35]\ns_add_u32 s74, s63, s69\ns_cmp_lt_u32 s35, 62\ns_cselect_b32 s74, s74, 0x7fff0000\n" \
    "v_mfma_f32_16x16x32_bf16 v[36:39], a[144:147], v[84:87], v[36:39]\nv_mfma_f32_16x16x32_bf16 v[40:43], v[64:67], v[76:79], v[40:43]\n" \
    "buffer_load_dwordx4 v12, s[64:67], s85 offen offset:1024 sc1 lds\nv_mfma_f32_16x16x32_bf16 v[44:47], v[64:67], v[84:87], v[44:47]\ns_waitcnt lgkmcnt(0)\n" \
    "v_mfma_f32_16x16x32_bf16 v[48:51], a[156:159], v[88:91], v[48:51]\nv_mfma_f32_16x16x32_bf16 v[52:55], a[156:159], v[68:71], v[52:55]\n" \
    "ds_read_b128 v[64:67], v3 offset:14336\nv_mfma_f32_16x16x32_bf16 v[56:59], a[164:167], v[88:91], v[56:59]\nds_read_b128 v[84:87], v9 offset:25600\n" \
    "buffer_store_dwordx4 v[28:31], v2, s[64:67], s74 offen offset:0 sc1\nv_mfma_f32_16x16x32_bf16 v[60:63], a[164:167], v[68:71], v[60:63]\n" \
    "ds_read_b128 v[72:75], v9 offset:29696\nv_mfma_f32_16x16x32_bf16 v[32:35], a[148:151], v[88:91], v[32:35]\n" \
    "v_mfma_f32_16x16x32_bf16 v[36:39], a[148:151], v[68:71], v[36:39]\nv_mfma_f32_16x16x32_bf16 v[40:43], v[80:83], v[88:91], v[40:43]\n" \
    "v_mfma_f32_16x16x32_bf16 v[44:47], v[80:83], v[68:71], v[44:47]\ns_waitcnt lgkmcnt(0)\nv_mfma_f32_16x16x32_bf16 v[48:51], a[176:179], v[84:87], v[48:51]\n" \
    "buffer_store_dwordx4 v[28:31], v2, s[64:67], s74 offen offset:1024 sc1\ns_and_b32 s69, s35, 15\nv_mfma_f32_16x16x32_bf16 v[52:55], a[176:179], v[72:75], v[52:55]\n" \
    "ds_read_b128 v[68:71], v3 offset:15360\ns_lshl_b32 s69, s69, 18\nv_mfma_f32_16x16x32_bf16 v[56:59], a[184:187], v[84:87], v[56:59]\n" \
    "ds_read_b128 v[88:91], v10 offset:25600\ns_add_u32 s82, s38, s69\nv_mfma_f32_16x16x32_bf16 v[60:63], a[184:187], v[72:75], v[60:63]\n" \
    "ds_read_b128 v[76:79], v10 offset:29696\ns_lshr_b32 s69, s82, 8\nv_mfma_f32_16x16x32_bf16 v[32:35], a[168:171], v[84:87], v[32:35]\ns_and_b32 s69, s69, 0x2000\n" \
    "s_xor_b32 s82, s82, s69\nv_mfma_f32_16x16x32_bf16 v[36:39], a[168:171], v[72:75], v[36:39]\ns_and_b32 s69, s35, 15\ns_lshl_b32 s69, s69, 18\n" \
    "v_mfma_f32_16x16x32_bf16 v[40:43], v[64:67], v[84:87], v[40:43]\ns_add_u32 s83, s39, s69\nv_mfma_f32_16x16x32_bf16 v[44:47], v[64:67], v[72:75], v[44:47]\n" \
    "s_waitcnt lgkmcnt(0)\nv_mfma_f32_16x16x32_bf16 v[48:51], a[180:183], v[88:91], v[48:51]\nv_mfma_f32_16x16x32_bf16 v[52:55], a[180:183], v[76:79], v[52:55]\n" \
    "ds_read_b128 v[64:67], v3 offset:16384\nv_mfma_f32_16x16x32_bf16 v[56:59], a[188:191], v[88:91], v[56:59]\nds_read_b128 v[72:75], v9 offset:32768\n" \
    "v_mfma_f32_16x16x32_bf16 v[60:63], a[188:191], v[76:79], v[60:63]\nds_read_b128 v[80:83], v9 offset:36864\n" \
    "v_mfma_f32_16x16x32_bf16 v[32:35], a[172:175], v[88:91], v[32:35]\nv_mfma_f32_16x16x32_bf16 v[36:39], a[172:175], v[76:79], v[36:39]\n" \
    "v_mfma_f32_16x16x32_bf16 v[40:43], v[68:71], v[88:91], v[40:43]\nv_mfma_f32_16x16x32_bf16 v[44:47], v[68:71], v[76:79], v[44:47]\ns_waitcnt lgkmcnt(0)\n" \
    "v_mfma_f32_16x16x32_bf16 v[48:51], a[200:203], v[72:75], v[48:51]\nv_mfma_f32_16x16x32_bf16 v[52:55], a[200:203], v[80:83], v[52:55]\n" \
    "ds_read_b128 v[68:71], v3 offset:17408\nv_mfma_f32_16x16x32_bf16 v[56:59], a[208:211], v[72:75], v[56:59]\nds_read_b128 v[76:79], v10 offset:32768\n" \
    "v_mfma_f32_16x16x32_bf16 v[60:63], a[208:211], v[80:83], v[60:63]\nds_read_b128 v[84:87], v10 offset:36864\n" \
    "v_mfma_f32_16x16x32_bf16 v[32:35], a[192:195], v[72:75], v[32:35]\nv_mfma_f32_16x16x32_bf16 v[36:39], a[192:195], v[80:83], v[36:39]\n" \
    "v_mfma_f32_16x16x32_bf16 v[40:43], v[64:67], v[72:75], v[40:43]\nv_mfma_f32_16x16x32_bf16 v[44:47], v[64:67], v[80:83], v[44:47]\ns_waitcnt lgkmcnt(0)\n" \
    "v_mfma_f32_16x16x32_bf16 v[48:51], a[204:207], v[76:79], v[48:51]\nv_mfma_f32_16x16x32_bf16 v[52:55], a[204:207], v[84:87], v[52:55]\n" \
    "ds_read_b128 v[64:67], v3 offset:18432\nv_mfma_f32_16x16x32_bf16 v[56:59], a[212:215], v[76:79], v[56:59]\nds_read_b128 v[72:75], v9 offset:33792\n" \
    "v_mfma_f32_16x16x32_bf16 v[60:63], a[212:215], v[84:87], v[60:63]\nds_read_b128 v[80:83], v9 offset:37888\n" \
    "v_mfma_f32_16x16x32_bf16 v[32:35], a[196:199], v[76:79], v[32:35]\nv_mfma_f32_16x16x32_bf16 v[36:39], a[196:199], v[84:87], v[36:39]\n" \
    "v_mfma_f32_16x16x32_bf16 v[40:43], v[68:71], v[76:79], v[40:43]\nv_mfma_f32_16x16x32_bf16 v[44:47], v[68:71], v[84:87], v[44:47]\ns_waitcnt lgkmcnt(0)\n" \
    "v_mfma_f32_16x16x32_bf16 v[48:51], a[224:227], v[72:75], v[48:51]\nv_mfma_f32_16x16x32_bf16 v[52:55], a[224:227], v[80:83], v[52:55]\n" \
    "ds_read_b128 v[68:71], v3 offset:19456\nv_mfma_f32_16x16x32_bf16 v[56:59], a[232:235], v[72:75], v[56:59]\nds_read_b128 v[76:79], v10 offset:33792\n" \
    "v_mfma_f32_16x16x32_bf16 v[60:63], a[232:235], v[80:83], v[60:63]\nds_read_b128 v[84:87], v10 offset:37888\n" \
    "v_mfma_f32_16x16x32_bf16 v[32:35], a[216:219], v[72:75], v[32:35]\nv_mfma_f32_16x16x32_bf16 v[36:39], a[216:219], v[80:83], v[36:39]\n" \
    "v_mfma_f32_16x16x32_bf16 v[40:43], v[64:67], v[72:75], v[40:43]\nbuffer_load_dwordx2 v[18:19], off, s[64:67], s48 offset:768 sc1\n" \
    "v_mfma_f32_16x16x32_bf16 v[44:47], v[64:67], v[80:83], v[44:47]\ns_waitcnt lgkmcnt(0)\nv_mfma_f32_16x16x32_bf16 v[48:51], a[228:231], v[76:79], v[48:51]\n" \
    "v_mfma_f32_16x16x32_bf16 v[52:55], a[228:231], v[84:87], v[52:55]\nds_read_b128 v[64:67], v3 offset:20480\ns_waitcnt vmcnt(3)\ns_barrier\n" \
    "v_mfma_f32_16x16x32_bf16 v[56:59], a[236:239], v[76:79], v[56:59]\nds_read_b128 v[72:75], v9 offset:0\nv_mfma_f32_16x16x32_bf16 v[60:63], a[236:239], v[84:87], v[60:63]\n" \
    "ds_read_b128 v[80:83], v9 offset:4096\ns_add_u32 m0, s41, 16384\nv_mfma_f32_16x16x32_bf16 v[32:35], a[220:223], v[76:79], v[32:35]\ns_xor_b32 s85, s62, 1792\n" \
    "s_add_u32 s85, s85, s40\nv_mfma_f32_16x16x32_bf16 v[36:39], a[220:223], v[84:87], v[36:39]\nbuffer_load_dwordx4 v11, s[64:67], s85 offen sc1 lds\n" \
    "v_mfma_f32_16x16x32_bf16 v[40:43], v[68:71], v[76:79], v[40:43]\nv_mfma_f32_16x16x32_bf16 v[44:47], v[68:71], v[84:87], v[44:47]\ns_waitcnt lgkmcnt(0)\n" \
    "v_mfma_f32_16x16x32_bf16 v[48:51], a[248:251], v[72:75], v[48:51]\nbuffer_load_dwordx4 v12, s[64:67], s85 offen offset:1024 sc1 lds\n" \
    "v_mfma_f32_16x16x32_bf16 v[52:55], a[248:251], v[80:83], v[52:55]\nds_read_b128 v[68:71], v3 offset:21504\nv_mfma_f32_16x16x32_bf16 v[56:59], v[92:95], v[72:75], v[56:59]\n" \
    "ds_read_b128 v[76:79], v10 offset:0\nv_mfma_f32_16x16x32_bf16 v[60:63], v[92:95], v[80:83], v[60:63]\nds_read_b128 v[84:87], v10 offset:4096\n" \
    "v_mfma_f32_16x16x32_bf16 v[32:35], a[240:243], v[72:75], v[32:35]\nv_mfma_f32_16x16x32_bf16 v[36:39], a[240:243], v[80:83], v[36:39]\n" \
    "v_mfma_f32_16x16x32_bf16 v[40:43], v[64:67], v[72:75], v[40:43]\nv_mfma_f32_16x16x32_bf16 v[44:47], v[64:67], v[80:83], v[44:47]\ns_waitcnt lgkmcnt(0)\n" \
    "v_mfma_f32_16x16x32_bf16 v[48:51], a[252:255], v[76:79], v[48:51]\nv_mfma_f32_16x16x32_bf16 v[52:55], a[252:255], v[84:87], v[52:55]\n" \
    "ds_read_b128 v[64:67], v3 offset:22528\nv_mfma_f32_16x16x32_bf16 v[56:59], v[96:99], v[76:79], v[56:59]\nds_read_b128 v[72:75], v9 offset:1024\n" \
    "v_mfma_f32_16x16x32_bf16 v[60:63], v[96:99], v[84:87], v[60:63]\nds_read_b128 v[80:83], v9 offset:5120\nv_mfma_f32_16x16x32_bf16 v[32:35], a[244:247], v[76:79], v[32:35]\n" \
    "v_mfma_f32_16x16x32_bf16 v[36:39], a[244:247], v[84:87], v[36:39]\nv_mfma_f32_16x16x32_bf16 v[40:43], v[68:71], v[76:79], v[40:43]\n" \
    "v_mfma_f32_16x16x32_bf16 v[44:47], v[68:71], v[84:87], v[44:47]\ns_waitcnt lgkmcnt(0)\nv_mfma_f32_16x16x32_bf16 v[48:51], v[108:111], v[72:75], v[48:51]\n" \
    "v_mfma_f32_16x16x32_bf16 v[52:55], v[108:111], v[80:83], v[52:55]\nds_read_b128 v[68:71], v3 offset:23552\n" \
    "v_mfma_f32_16x16x32_bf16 v[56:59], v[116:119], v[72:75], v[56:59]\nds_read_b128 v[76:79], v10 offset:1024\n" \
    "v_mfma_f32_16x16x32_bf16 v[60:63], v[116:119], v[80:83], v[60:63]\nds_read_b128 v[84:87], v10 offset:5120\n" \
    "v_mfma_f32_16x16x32_bf16 v[32:35], v[100:103], v[72:75], v[32:35]\nv_mfma_f32_16x16x32_bf16 v[36:39], v[100:103], v[80:83], v[36:39]\n" \
    "v_mfma_f32_16x16x32_bf16 v[40:43], v[64:67], v[72:75], v[40:43]\nv_mfma_f32_16x16x32_bf16 v[44:47], v[64:67], v[80:83], v[44:47]\ns_waitcnt lgkmcnt(0)\n" \
    "v_mfma_f32_16x16x32_bf16 v[48:51], v[112:115], v[76:79], v[48:51]\nv_mfma_f32_16x16x32_bf16 v[52:55], v[112:115], v[84:87], v[52:55]\n" \
    "v_mfma_f32_16x16x32_bf16 v[56:59], v[120:123], v[76:79], v[56:59]\nds_read_b128 v[64:67], v9 offset:8192\n" \
    "v_mfma_f32_16x16x32_bf16 v[60:63], v[120:123], v[84:87], v[60:63]\nds_read_b128 v[72:75], v9 offset:12288\n" \
    "v_mfma_f32_16x16x32_bf16 v[32:35], v[104:107], v[76:79], v[32:35]\nv_mfma_f32_16x16x32_bf16 v[36:39], v[104:107], v[84:87], v[36:39]\n" \
    "v_mfma_f32_16x16x32_bf16 v[40:43], v[68:71], v[76:79], v[40:43]\ns_waitcnt vmcnt(2)\nv_and_b32 v18, 255, v18\nv_and_b32 v19, 255, v19\n" \
    "v_mfma_f32_16x16x32_bf16 v[44:47], v[68:71], v[84:87], v[44:47]\ns_waitcnt lgkmcnt(0)\nv_max_u32 v18, v18, v19\n" \
    "v_mfma_f32_16x16x32_bf16 v[48:51], v[140:143], v[64:67], v[48:51]\nv_readfirstlane_b32 s20, v18\nv_mfma_f32_16x16x32_bf16 v[52:55], v[140:143], v[72:75], v[52:55]\n" \
    "s_add_u32 s21, s20, s49\ns_andn2_b32 s21, s21, 3\nv_mfma_f32_16x16x32_bf16 v[56:59], v[148:151], v[64:67], v[56:59]\nds_read_b128 v[68:71], v10 offset:8192\n" \
    "s_add_u32 s22, s18, 4\nv_mfma_f32_16x16x32_bf16 v[60:63], v[148:151], v[72:75], v[60:63]\nds_read_b128 v[76:79], v10 offset:12288\ns_min_u32 s21, s21, s22\n" \
    "v_mfma_f32_16x16x32_bf16 v[32:35], v[124:127], v[64:67], v[32:35]\ns_max_u32 s21, s21, 128\ns_cmp_eq_u32 s20, 0\ns_cselect_b32 s18, s18, s21\n" \
    "v_mfma_f32_16x16x32_bf16 v[36:39], v[124:127], v[72:75], v[36:39]\ns_cmp_lt_u32 s35, 4\ns_cselect_b32 s20, s44, 0\n" \
    "v_mfma_f32_16x16x32_bf16 v[40:43], v[132:135], v[64:67], v[40:43]\ns_max_u32 s18, s18, s20\nv_mfma_f32_16x16x32_bf16 v[44:47], v[132:135], v[72:75], v[44:47]\n" \
    "s_waitcnt lgkmcnt(0)\nv_mfma_f32_16x16x32_bf16 v[48:51], v[144:147], v[68:71], v[48:51]\nbuffer_load_dword v13, off, s[64:67], s42 sc1\ns_and_b32 s69, s35, 15\n" \
    "v_mfma_f32_16x16x32_bf16 v[52:55], v[144:147], v[76:79], v[52:55]\ns_lshl_b32 s69, s69, 17\ns_add_u32 s81, s37, s69\n" \
    "v_mfma_f32_16x16x32_bf16 v[56:59], v[152:155], v[68:71], v[56:59]\nds_read_b128 v[64:67], v9 offset:9216\ns_lshr_b32 s69, s81, 8\n" \
    "v_mfma_f32_16x16x32_bf16 v[60:63], v[152:155], v[76:79], v[60:63]\nds_read_b128 v[72:75], v9 offset:13312\ns_and_b32 s69, s69, 0x2000\n" \
    "v_mfma_f32_16x16x32_bf16 v[32:35], v[128:131], v[68:71], v[32:35]\ns_xor_b32 s81, s81, s69\ns_mov_b32 s68, 0\n" \
    "v_mfma_f32_16x16x32_bf16 v[36:39], v[128:131], v[76:79], v[36:39]\nv_mfma_f32_16x16x32_bf16 v[40:43], v[136:139], v[68:71], v[40:43]\n" \
    "v_mfma_f32_16x16x32_bf16 v[44:47], v[136:139], v[76:79], v[44:47]\ns_waitcnt lgkmcnt(0)\nv_mfma_f32_16x16x32_bf16 v[48:51], v[172:175], v[64:67], v[48:51]\n" \
    "v_mfma_f32_16x16x32_bf16 v[52:55], v[172:175], v[72:75], v[52:55]\nv_mfma_f32_16x16x32_bf16 v[56:59], v[180:183], v[64:67], v[56:59]\n" \
    "ds_read_b128 v[68:71], v10 offset:9216\nv_mfma_f32_16x16x32_bf16 v[60:63], v[180:183], v[72:75], v[60:63]\nds_read_b128 v[76:79], v10 offset:13312\n" \
    "v_mfma_f32_16x16x32_bf16 v[32:35], v[156:159], v[64:67], v[32:35]\nv_mfma_f32_16x16x32_bf16 v[36:39], v[156:159], v[72:75], v[36:39]\n" \
    "v_mfma_f32_16x16x32_bf16 v[40:43], v[164:167], v[64:67], v[40:43]\nv_mfma_f32_16x16x32_bf16 v[44:47], v[164:167], v[72:75], v[44:47]\ns_waitcnt lgkmcnt(0)\n" \
    "v_mfma_f32_16x16x32_bf16 v[48:51], v[176:179], v[68:71], v[48:51]\nv_mfma_f32_16x16x32_bf16 v[52:55], v[176:179], v[76:79], v[52:55]\ns_waitcnt vmcnt(1)\ns_barrier\n" \
    "v_mfma_f32_16x16x32_bf16 v[56:59], v[184:187], v[68:71], v[56:59]\nds_read_b128 v[64:67], v9 offset:16384\n" \
    "v_mfma_f32_16x16x32_bf16 v[60:63], v[184:187], v[76:79], v[60:63]\nds_read_b128 v[72:75], v9 offset:20480\ns_add_u32 m0, s41, 0\n" \
    "v_mfma_f32_16x16x32_bf16 v[32:35], v[160:163], v[68:71], v[32:35]\ns_xor_b32 s85, s62, 0\ns_add_u32 s85, s85, s84\n" \
    "v_mfma_f32_16x16x32_bf16 v[36:39], v[160:163], v[76:79], v[36:39]\nbuffer_load_dwordx4 v11, s[64:67], s85 offen sc1 lds\n" \
    "v_mfma_f32_16x16x32_bf16 v[40:43], v[168:171], v[68:71], v[40:43]\nv_mfma_f32_16x16x32_bf16 v[44:47], v[168:171], v[76:79], v[44:47]\ns_waitcnt lgkmcnt(0)\n" \
    "v_mfma_f32_16x16x32_bf16 v[48:51], v[204:207], v[64:67], v[48:51]\nbuffer_load_dwordx4 v12, s[64:67], s85 offen offset:1024 sc1 lds\n" \
    "v_mfma_f32_16x16x32_bf16 v[52:55], v[204:207], v[72:75], v[52:55]\nv_mfma_f32_16x16x32_bf16 v[56:59], v[212:215], v[64:67], v[56:59]\n" \
    "ds_read_b128 v[68:71], v10 offset:16384\nv_mfma_f32_16x16x32_bf16 v[60:63], v[212:215], v[72:75], v[60:63]\nds_read_b128 v[76:79], v10 offset:20480\n" \
    "v_mfma_f32_16x16x32_bf16 v[32:35], v[188:191], v[64:67], v[32:35]\ns_waitcnt vmcnt(2)\nv_readfirstlane_b32 s74, v13\n" \
    "v_mfma_f32_16x16x32_bf16 v[36:39], v[188:191], v[72:75], v[36:39]\ns_and_b32 s75, s74, 127\ns_lshr_b32 s69, s74, 8\n" \
    "v_mfma_f32_16x16x32_bf16 v[40:43], v[196:199], v[64:67], v[40:43]\ns_sub_u32 s69, s69, s80\ns_bfe_i32 s69, s69, 0x180000\n" \
    "v_mfma_f32_16x16x32_bf16 v[44:47], v[196:199], v[72:75], v[44:47]\ns_cmp_eq_u32 s75, 0\ns_cselect_b32 s69, 0, s69\ns_waitcnt lgkmcnt(0)\n" \
    "v_mfma_f32_16x16x32_bf16 v[48:51], v[208:211], v[68:71], v[48:51]\ns_add_u32 s80, s80, s69\ns_lshl_b32 s74, s74, 5\n" \
    "v_mfma_f32_16x16x32_bf16 v[52:55], v[208:211], v[76:79], v[52:55]\ns_and_b32 s74, s74, 0x1000\ns_andn2_b32 s81, s81, 0x1000\n" \
    "v_mfma_f32_16x16x32_bf16 v[56:59], v[216:219], v[68:71], v[56:59]\nds_read_b128 v[64:67], v9 offset:17408\ns_or_b32 s81, s81, s74\n" \
    "v_mfma_f32_16x16x32_bf16 v[60:63], v[216:219], v[76:79], v[60:63]\nds_read_b128 v[72:75], v9 offset:21504\ns_lshl_b32 s74, s74, 2\n" \
    "v_mfma_f32_16x16x32_bf16 v[32:35], v[192:195], v[68:71], v[32:35]\ns_andn2_b32 s82, s82, 0x4000\ns_or_b32 s82, s82, s74\n" \
    "v_mfma_f32_16x16x32_bf16 v[36:39], v[192:195], v[76:79], v[36:39]\nv_mfma_f32_16x16x32_bf16 v[40:43], v[200:203], v[68:71], v[40:43]\n" \
    "ds_read_b128 v[68:71], v10 offset:17408\nv_mfma_f32_16x16x32_bf16 v[44:47], v[200:203], v[76:79], v[44:47]\nds_read_b128 v[76:79], v10 offset:21504\ns_waitcnt lgkmcnt(2)\n" \
    "v_mfma_f32_16x16x32_bf16 v[48:51], v[236:239], v[64:67], v[48:51]\nv_mfma_f32_16x16x32_bf16 v[52:55], v[236:239], v[72:75], v[52:55]\n" \
    "v_mfma_f32_16x16x32_bf16 v[56:59], v[244:247], v[64:67], v[56:59]\nv_mfma_f32_16x16x32_bf16 v[60:63], v[244:247], v[72:75], v[60:63]\ns_waitcnt lgkmcnt(0)\ns_barrier\n" \
    "v_mfma_f32_16x16x32_bf16 v[48:51], v[240:243], v[68:71], v[48:51]\ns_add_u32 m0, s41, 8192\ns_xor_b32 s85, s62, 256\n" \
    "v_mfma_f32_16x16x32_bf16 v[52:55], v[240:243], v[76:79], v[52:55]\ns_add_u32 s85, s85, s84\nbuffer_load_dwordx4 v11, s[64:67], s85 offen sc1 lds\n" \
    "v_mfma_f32_16x16x32_bf16 v[56:59], v[248:251], v[68:71], v[56:59]\nv_mfma_f32_16x16x32_bf16 v[60:63], v[248:251], v[76:79], v[60:63]\ns_nop 0\n" \
    "v_cvt_pk_bf16_f32 v16, v48, v49\nv_cvt_pk_bf16_f32 v17, v50, v51\nv_mfma_f32_16x16x32_bf16 v[32:35], v[220:223], v[64:67], v[32:35]\n" \
    "buffer_load_dwordx4 v12, s[64:67], s85 offen offset:1024 sc1 lds\ns_add_u32 m0, s41, 16384\nv_mfma_f32_16x16x32_bf16 v[36:39], v[220:223], v[72:75], v[36:39]\n" \
    "s_xor_b32 s85, s62, 512\ns_add_u32 s85, s85, s84\nv_cvt_pk_bf16_f32 v18, v56, v57\nv_mfma_f32_16x16x32_bf16 v[40:43], v[228:231], v[64:67], v[40:43]\n" \
    "buffer_load_dwordx4 v11, s[64:67], s85 offen sc1 lds\nv_cvt_pk_bf16_f32 v19, v58, v59\nv_mfma_f32_16x16x32_bf16 v[44:47], v[228:231], v[72:75], v[44:47]\n" \
    "ds_write_b128 v14, v[16:19] offset:0\nv_cvt_pk_bf16_f32 v20, v52, v53\nv_cvt_pk_bf16_f32 v21, v54, v55\nv_mfma_f32_16x16x32_bf16 v[32:35], v[224:227], v[68:71], v[32:35]\n" \
    "v_cvt_pk_bf16_f32 v22, v60, v61\nv_cvt_pk_bf16_f32 v23, v62, v63\nv_mfma_f32_16x16x32_bf16 v[36:39], v[224:227], v[76:79], v[36:39]\n" \
    "buffer_load_dwordx4 v12, s[64:67], s85 offen offset:1024 sc1 lds\nds_write_b128 v14, v[20:23] offset:4096\n" \
    "v_mfma_f32_16x16x32_bf16 v[40:43], v[232:235], v[68:71], v[40:43]\nv_mfma_f32_16x16x32_bf16 v[44:47], v[232:235], v[76:79], v[44:47]\n.Lq10_%=:\ns_cmp_lt_u32 s75, s35\n" \
    "s_cbranch_scc1 .Lq11_%=\ns_cmp_eq_u32 s36, 1\ns_cbranch_scc1 .Lq5_%=\ns_cmp_eq_u32 s36, 2\ns_cbranch_scc1 .Lq6_%=\ns_cmp_eq_u32 s36, 3\ns_cbranch_scc1 .Lq7_%=\n.Lq4_%=:\n" \
    ".Lq13_%=:\ns_memrealtime s[72:73]\ns_waitcnt lgkmcnt(0)\ns_mov_b32 s68, 0\ns_sub_u32 s76, s80, s45\ns_sub_u32 s69, s72, s76\ns_cmp_lt_i32 s69, -8\ns_cbranch_scc1 .Lq16_%=\n" \
    "s_xor_b32 s90, s81, 2048\n.Lq18_%=:\ns_memrealtime s[72:73]\ns_waitcnt lgkmcnt(0)\ns_sub_u32 s72, s72, s80\ns_cmp_lt_i32 s72, 0\ns_cbranch_scc1 .Lq18_%=\n" \
    "buffer_load_dwordx4 v[72:75], v2, s[64:67], s81 offen offset:0 sc1\nv_cvt_pk_bf16_f32 v24, v32, v33\nv_cvt_pk_bf16_f32 v25, v34, v35\nv_cvt_pk_bf16_f32 v26, v40, v41\n" \
    "buffer_load_dwordx4 v[76:79], v2, s[64:67], s81 offen offset:1024 sc1\nv_cvt_pk_bf16_f32 v27, v42, v43\nds_write_b128 v14, v[24:27] offset:8192\n" \
    "v_cvt_pk_bf16_f32 v28, v36, v37\nbuffer_load_dwordx4 v[80:83], v2, s[64:67], s90 offen offset:0 sc1\nv_cvt_pk_bf16_f32 v29, v38, v39\nv_cvt_pk_bf16_f32 v30, v44, v45\n" \
    "v_cvt_pk_bf16_f32 v31, v46, v47\nbuffer_load_dwordx4 v[84:87], v2, s[64:67], s90 offen offset:1024 sc1\nds_write_b128 v14, v[28:31] offset:12288\n" \
    "ds_read_b128 v[32:35], v3 offset:0\nds_read_b128 v[36:39], v3 offset:1024\nds_read_b128 v[40:43], v3 offset:8192\nds_read_b128 v[44:47], v3 offset:9216\n" \
    "ds_read_b128 v[48:51], v3 offset:16384\nds_read_b128 v[52:55], v3 offset:17408\nds_read_b128 v[56:59], v3 offset:2048\nds_read_b128 v[60:63], v3 offset:3072\n" \
    "ds_read_b128 v[64:67], v3 offset:4096\nds_read_b128 v[68:71], v3 offset:5120\n.Lq17_%=:\nv_mov_b64 v[16:17], 0\nv_mov_b64 v[18:19], 0\nv_mov_b64 v[20:21], 0\n" \
    "v_mov_b64 v[22:23], 0\nv_mov_b64 v[24:25], 0\nv_mov_b64 v[26:27], 0\nv_mov_b64 v[28:29], 0\nv_mov_b64 v[30:31], 0\ns_waitcnt vmcnt(3) lgkmcnt(8)\n" \
    "v_smfmac_f32_16x16x64_bf16 v[20:23], v[72:75], v[32:39], v0\nds_write_b128 v1, v[72:75] offset:24576\nv_max3_u32 v252, v72, v73, v74\n" \
    "v_smfmac_f32_16x16x64_bf16 v[16:19], v[72:75], a[0:7], v0\nv_smfmac_f32_16x16x64_bf16 v[24:27], v[72:75], a[8:15], v0\n" \
    "v_smfmac_f32_16x16x64_bf16 v[28:31], v[72:75], a[16:23], v0\ns_waitcnt vmcnt(2)\nds_write_b128 v1, v[76:79] offset:28672\ns_waitcnt lgkmcnt(8)\n" \
    "v_max3_u32 v252, v252, v75, v76\nv_smfmac_f32_16x16x64_bf16 v[20:23], v[76:79], v[40:47], v0\nv_max3_u32 v252, v252, v77, v78\n" \
    "v_smfmac_f32_16x16x64_bf16 v[16:19], v[76:79], a[96:103], v0\nv_cmp_u_f32 vcc, v252, v79\nv_smfmac_f32_16x16x64_bf16 v[24:27], v[76:79], a[104:111], v0\n" \
    "s_cbranch_vccnz .Lq24_%=\n.Lq20_%=:\nv_smfmac_f32_16x16x64_bf16 v[28:31], v[76:79], a[112:119], v0\nds_read_b128 v[36:39], v3 offset:7168\ns_waitcnt vmcnt(1)\n" \
    "v_max3_u32 v252, v80, v81, v82\nv_cmp_u_f32 vcc, v252, v83\ns_nop 0\ns_cbranch_vccnz .Lq25_%=\n.Lq21_%=:\ns_waitcnt lgkmcnt(7)\n" \
    "v_smfmac_f32_16x16x64_bf16 v[20:23], v[80:83], v[48:55], v0\nds_write_b128 v1, v[80:83] offset:32768\nv_smfmac_f32_16x16x64_bf16 v[16:19], v[80:83], a[192:199], v0\n" \
    "ds_read_b128 v[32:35], v3 offset:6144\ns_waitcnt lgkmcnt(3)\ns_barrier\nds_read_b128 v[88:91], v1 offset:25600\n" \
    "v_smfmac_f32_16x16x64_bf16 v[24:27], v[80:83], a[200:207], v0\nds_read_b128 v[40:43], v3 offset:10240\nv_smfmac_f32_16x16x64_bf16 v[28:31], v[80:83], a[208:215], v0\n" \
    "ds_read_b128 v[48:51], v1 offset:26624\ns_waitcnt vmcnt(0)\nv_max3_u32 v252, v84, v85, v86\nv_cmp_u_f32 vcc, v252, v87\ns_nop 0\ns_cbranch_vccnz .Lq26_%=\n.Lq22_%=:\n" \
    "v_smfmac_f32_16x16x64_bf16 v[20:23], v[84:87], v[132:139], v0\nds_write_b128 v1, v[84:87] offset:36864\nv_smfmac_f32_16x16x64_bf16 v[16:19], v[84:87], v[124:131], v0\n" \
    "ds_read_b128 v[52:55], v1 offset:27648\nv_smfmac_f32_16x16x64_bf16 v[24:27], v[84:87], v[140:147], v0\nds_read_b128 v[72:75], v1 offset:29696\n" \
    "v_smfmac_f32_16x16x64_bf16 v[28:31], v[84:87], v[148:155], v0\nds_read_b128 v[44:47], v3 offset:11264\ns_waitcnt lgkmcnt(6)\n" \
    "v_smfmac_f32_16x16x64_bf16 v[20:23], v[88:91], v[56:63], v0\nds_read_b128 v[56:59], v1 offset:30720\nv_smfmac_f32_16x16x64_bf16 v[16:19], v[88:91], a[24:31], v0\n" \
    "ds_read_b128 v[76:79], v3 offset:12288\nv_smfmac_f32_16x16x64_bf16 v[24:27], v[88:91], a[32:39], v0\nds_read_b128 v[80:83], v3 offset:13312\n" \
    "v_smfmac_f32_16x16x64_bf16 v[28:31], v[88:91], a[40:47], v0\nds_read_b128 v[60:63], v1 offset:31744\ns_waitcnt lgkmcnt(7)\ns_barrier\n" \
    "v_smfmac_f32_16x16x64_bf16 v[20:23], v[48:51], v[64:71], v0\nds_read_b128 v[64:67], v3 offset:14336\nv_smfmac_f32_16x16x64_bf16 v[16:19], v[48:51], a[48:55], v0\n" \
    "ds_read_b128 v[68:71], v3 offset:15360\ns_waitcnt lgkmcnt(8)\nv_smfmac_f32_16x16x64_bf16 v[20:23], v[52:55], v[32:39], v0\nds_read_b128 v[32:35], v1 offset:33792\n" \
    "v_smfmac_f32_16x16x64_bf16 v[16:19], v[52:55], a[72:79], v0\nds_read_b128 v[84:87], v3 offset:18432\ns_waitcnt lgkmcnt(8)\n" \
    "v_smfmac_f32_16x16x64_bf16 v[20:23], v[72:75], v[40:47], v0\nds_read_b128 v[88:91], v3 offset:19456\nv_smfmac_f32_16x16x64_bf16 v[16:19], v[72:75], a[120:127], v0\n" \
    "ds_read_b128 v[36:39], v1 offset:34816\ns_waitcnt lgkmcnt(7)\nv_smfmac_f32_16x16x64_bf16 v[20:23], v[56:59], v[76:83], v0\nds_read_b128 v[40:43], v3 offset:20480\n" \
    "v_smfmac_f32_16x16x64_bf16 v[16:19], v[56:59], a[144:151], v0\nds_read_b128 v[44:47], v3 offset:21504\ns_waitcnt lgkmcnt(6)\n" \
    "v_smfmac_f32_16x16x64_bf16 v[20:23], v[60:63], v[64:71], v0\nds_read_b128 v[64:67], v1 offset:35840\nv_smfmac_f32_16x16x64_bf16 v[16:19], v[60:63], a[168:175], v0\n" \
    "ds_read_b128 v[76:79], v3 offset:22528\ns_waitcnt lgkmcnt(5)\nv_smfmac_f32_16x16x64_bf16 v[20:23], v[32:35], v[84:91], v0\nds_read_b128 v[80:83], v3 offset:23552\n" \
    "v_smfmac_f32_16x16x64_bf16 v[16:19], v[32:35], a[216:223], v0\nds_read_b128 v[68:71], v1 offset:37888\ns_waitcnt lgkmcnt(4)\n" \
    "v_smfmac_f32_16x16x64_bf16 v[20:23], v[36:39], v[40:47], v0\nds_read_b128 v[40:43], v1 offset:38912\nv_smfmac_f32_16x16x64_bf16 v[16:19], v[36:39], a[240:247], v0\n" \
    "ds_read_b128 v[44:47], v1 offset:39936\ns_waitcnt lgkmcnt(3)\nv_smfmac_f32_16x16x64_bf16 v[20:23], v[64:67], v[76:83], v0\n" \
    "v_smfmac_f32_16x16x64_bf16 v[16:19], v[64:67], v[100:107], v0\ns_waitcnt lgkmcnt(2)\nv_smfmac_f32_16x16x64_bf16 v[20:23], v[68:71], v[164:171], v0\ns_waitcnt lgkmcnt(1)\n" \
    "v_smfmac_f32_16x16x64_bf16 v[20:23], v[40:43], v[196:203], v0\ns_waitcnt lgkmcnt(0)\nv_smfmac_f32_16x16x64_bf16 v[20:23], v[44:47], v[228:235], v0\ns_nop 2\n" \
    "v_smfmac_f32_16x16x64_bf16 v[16:19], v[68:71], v[156:163], v0\ns_nop 2\nv_smfmac_f32_16x16x64_bf16 v[16:19], v[40:43], v[188:195], v0\nv_permlane32_swap_b32 v20, v22\n" \
    "v_smfmac_f32_16x16x64_bf16 v[16:19], v[44:47], v[220:227], v0\ns_nop 1\nv_permlane32_swap_b32 v21, v23\nv_smfmac_f32_16x16x64_bf16 v[24:27], v[48:51], a[56:63], v0\n" \
    "s_nop 1\nv_add_f32 v20, v20, v22\nv_smfmac_f32_16x16x64_bf16 v[28:31], v[48:51], a[64:71], v0\nv_add_f32 v21, v21, v23\n" \
    "v_smfmac_f32_16x16x64_bf16 v[24:27], v[52:55], a[80:87], v0\nv_mul_f32 v20, s32, v20\nv_smfmac_f32_16x16x64_bf16 v[28:31], v[52:55], a[88:95], v0\nv_mul_f32 v21, s32, v21\n" \
    "v_smfmac_f32_16x16x64_bf16 v[24:27], v[72:75], a[128:135], v0\nv_cvt_pk_bf16_f32 v20, v20, v21\nv_smfmac_f32_16x16x64_bf16 v[28:31], v[72:75], a[136:143], v0\n" \
    "ds_write_b16 v4, v20 offset:8\nv_permlane32_swap_b32 v16, v18\nv_smfmac_f32_16x16x64_bf16 v[24:27], v[56:59], a[152:159], v0\nds_write_b16_d16_hi v4, v20 offset:24\n" \
    "v_permlane32_swap_b32 v17, v19\nv_smfmac_f32_16x16x64_bf16 v[24:27], v[60:63], a[176:183], v0\nds_read_b128 v[48:51], v1 offset:40960\nv_add_f32 v16, v16, v18\n" \
    "v_smfmac_f32_16x16x64_bf16 v[24:27], v[32:35], a[224:231], v0\nds_read_b128 v[52:55], v1 offset:45056\nv_add_f32 v17, v17, v19\n" \
    "v_smfmac_f32_16x16x64_bf16 v[24:27], v[36:39], a[248:255], v0\nds_read_b128 v[72:75], v1 offset:41984\nv_mul_f32 v16, s32, v16\n" \
    "v_smfmac_f32_16x16x64_bf16 v[24:27], v[64:67], v[108:115], v0\nds_read_b128 v[76:79], v1 offset:46080\nv_mul_f32 v17, s32, v17\n" \
    "v_smfmac_f32_16x16x64_bf16 v[24:27], v[68:71], v[172:179], v0\nds_read_b128 v[80:83], v1 offset:43008\nv_cvt_pk_bf16_f32 v16, v16, v17\n" \
    "v_smfmac_f32_16x16x64_bf16 v[24:27], v[40:43], v[204:211], v0\nds_write_b16 v4, v16 offset:0\nv_smfmac_f32_16x16x64_bf16 v[24:27], v[44:47], v[236:243], v0\n" \
    "ds_write_b16_d16_hi v4, v16 offset:16\ns_nop 1\nv_smfmac_f32_16x16x64_bf16 v[28:31], v[56:59], a[160:167], v0\nds_read_b128 v[56:59], v1 offset:47104\ns_nop 1\n" \
    "v_smfmac_f32_16x16x64_bf16 v[28:31], v[60:63], a[184:191], v0\nds_read_b128 v[60:63], v1 offset:44032\nv_permlane32_swap_b32 v24, v26\n" \
    "v_smfmac_f32_16x16x64_bf16 v[28:31], v[32:35], a[232:239], v0\nds_read_b128 v[32:35], v1 offset:48128\nv_permlane32_swap_b32 v25, v27\n" \
    "v_smfmac_f32_16x16x64_bf16 v[28:31], v[36:39], v[92:99], v0\nds_read_b128 v[36:39], v6 offset:0\nv_add_f32 v24, v24, v26\n" \
    "v_smfmac_f32_16x16x64_bf16 v[28:31], v[64:67], v[116:123], v0\nds_read_b128 v[64:67], v6 offset:512\nv_add_f32 v25, v25, v27\n" \
    "v_smfmac_f32_16x16x64_bf16 v[28:31], v[68:71], v[180:187], v0\nds_read_b128 v[68:71], v6 offset:1024\nv_cvt_pk_bf16_f32 v24, v24, v25\n" \
    "v_smfmac_f32_16x16x64_bf16 v[28:31], v[40:43], v[212:219], v0\nds_read_b128 v[40:43], v6 offset:1536\nv_smfmac_f32_16x16x64_bf16 v[28:31], v[44:47], v[244:251], v0\n" \
    "ds_write_b16 v4, v24 offset:2048\nds_write_b16_d16_hi v4, v24 offset:2064\ns_waitcnt lgkmcnt(5)\nv_mfma_f32_16x16x32_bf16 v[16:19], v[48:51], v[36:39], 0\ns_nop 2\n" \
    "v_mfma_f32_16x16x32_bf16 v[20:23], v[52:55], v[36:39], 0\nv_permlane32_swap_b32 v28, v30\nv_permlane32_swap_b32 v29, v31\nv_add_f32 v28, v28, v30\nv_add_f32 v29, v29, v31\n" \
    "v_cvt_pk_bf16_f32 v28, v28, v29\nds_write_b16 v4, v28 offset:2056\nds_write_b16_d16_hi v4, v28 offset:2072\ns_waitcnt lgkmcnt(6)\n" \
    "v_mfma_f32_16x16x32_bf16 v[16:19], v[72:75], v[64:67], v[16:19]\ns_cmp_eq_u32 s35, 63\ns_cbranch_scc1 .Lq8_%=\n" \
    "v_mfma_f32_16x16x32_bf16 v[20:23], v[76:79], v[64:67], v[20:23]\ns_waitcnt lgkmcnt(5)\nv_mfma_f32_16x16x32_bf16 v[16:19], v[80:83], v[68:71], v[16:19]\n" \
    "ds_read_b128 v[44:47], v8 offset:0\nds_read_b128 v[84:87], v8 offset:512\nv_mfma_f32_16x16x32_bf16 v[20:23], v[56:59], v[68:71], v[20:23]\n" \
    "ds_read_b128 v[88:91], v8 offset:1024\nds_read_b128 v[28:31], v8 offset:1536\ns_waitcnt lgkmcnt(8)\nv_mfma_f32_16x16x32_bf16 v[16:19], v[60:63], v[40:43], v[16:19]\n" \
    "v_mfma_f32_16x16x32_bf16 v[20:23], v[32:35], v[40:43], v[20:23]\nv_mov_b32 v15, v15\nv_mov_b32 v15, v15\ns_waitcnt lgkmcnt(3)\n" \
    "v_mfma_f32_16x16x32_bf16 v[24:27], v[44:47], v[36:39], 0\ns_nop 2\ns_waitcnt lgkmcnt(2)\nv_mov_b32_dpp v16, v20 quad_perm:[0,1,2,3] row_mask:0xf bank_mask:0xc\n" \
    "v_mov_b32_dpp v17, v21 quad_perm:[0,1,2,3] row_mask:0xf bank_mask:0xc\nv_mfma_f32_16x16x32_bf16 v[24:27], v[84:87], v[64:67], v[24:27]\ns_waitcnt lgkmcnt(1)\n" \
    "v_mov_b32_dpp v18, v22 quad_perm:[0,1,2,3] row_mask:0xf bank_mask:0xc\nv_mov_b32_dpp v19, v23 quad_perm:[0,1,2,3] row_mask:0xf bank_mask:0xc\n" \
    "v_mfma_f32_16x16x32_bf16 v[24:27], v[88:91], v[68:71], v[24:27]\ns_waitcnt lgkmcnt(0)\nv_max3_f32 v254, v16, v17, v18\nv_max_f32 v254, v254, v19\n" \
    "v_mfma_f32_16x16x32_bf16 v[24:27], v[28:31], v[40:43], v[24:27]\ns_nop 0\nv_max_f32_dpp v252, v254, v254 row_ror:8 row_mask:0xf bank_mask:0xf\n" \
    "v_max_f32_dpp v253, v254, v254 row_ror:8 row_mask:0xf bank_mask:0xf\ns_nop 1\nv_permlane32_swap_b32 v252, v253\nv_max_f32 v252, v252, v253\ns_xor_b32 s74, s82, 0x4000\n" \
    "v_max3_f32 v254, v252, v24, v25\nv_max3_f32 v252, v254, v26, v27\nv_max3_f32 v253, v254, v26, v27\ns_nop 1\nv_permlane16_swap_b32 v252, v253\nv_max_f32 v252, v252, v253\n" \
    "v_pk_add_f32 v[16:17], v[16:17], v[252:253] op_sel_hi:[1,0] neg_lo:[0,1] neg_hi:[0,1]\n" \
    "v_pk_add_f32 v[18:19], v[18:19], v[252:253] op_sel_hi:[1,0] neg_lo:[0,1] neg_hi:[0,1]\n" \
    "v_pk_add_f32 v[24:25], v[24:25], v[252:253] op_sel_hi:[1,0] neg_lo:[0,1] neg_hi:[0,1]\n" \
    "v_pk_add_f32 v[26:27], v[26:27], v[252:253] op_sel_hi:[1,0] neg_lo:[0,1] neg_hi:[0,1]\nv_exp_f32 v16, v16\nv_exp_f32 v17, v17\nv_exp_f32 v18, v18\nv_exp_f32 v19, v19\n" \
    "v_exp_f32 v24, v24\nv_pk_add_f32 v[254:255], v[16:17], v[18:19]\nv_exp_f32 v25, v25\nv_add_f32 v254, v254, v255\nv_exp_f32 v26, v26\nv_exp_f32 v27, v27\n" \
    "v_add_f32_dpp v252, v254, v254 row_ror:8 row_mask:0xf bank_mask:0xf\nv_add_f32_dpp v253, v254, v254 row_ror:8 row_mask:0xf bank_mask:0xf\n" \
    "v_pk_add_f32 v[254:255], v[24:25], v[26:27]\ns_nop 0\nv_permlane32_swap_b32 v252, v253\nv_pk_add_f32 v[254:255], v[254:255], v[252:253]\n" \
    "v_pk_add_f32 v[252:253], v[254:255], v[254:255] op_sel:[0,1] op_sel_hi:[1,0]\ns_nop 1\nv_permlane16_swap_b32 v252, v253\nv_add_f32 v252, v252, v253\n" \
    "v_cmp_u_f32 vcc, v252, v252\nv_rcp_f32 v252, v252\ns_cbranch_vccnz .Lq12_%=\nv_pk_mul_f32 v[16:17], v[16:17], v[252:253] op_sel_hi:[1,0]\n" \
    "v_pk_mul_f32 v[24:25], v[24:25], v[252:253] op_sel_hi:[1,0]\nv_cvt_pk_bf16_f32 v20, v16, v17\nv_cvt_pk_bf16_f32 v22, v24, v25\n" \
    "v_pk_mul_f32 v[18:19], v[18:19], v[252:253] op_sel_hi:[1,0]\nv_pk_mul_f32 v[26:27], v[26:27], v[252:253] op_sel_hi:[1,0]\nv_cvt_pk_bf16_f32 v21, v18, v19\n" \
    "v_cvt_pk_bf16_f32 v23, v26, v27\nv_permlane32_swap_b32 v20, v22\ns_nop 0\nv_permlane32_swap_b32 v21, v23\nbuffer_store_dwordx4 v[20:23], v7, s[64:67], s82 offen sc1\n" \
    "buffer_store_dwordx4 v[20:23], v7, s[64:67], s74 offen sc1\ns_branch .Lq3_%=\n.Lq5_%=:\n.Lq32_%=:\ns_memrealtime s[72:73]\ns_waitcnt lgkmcnt(0)\ns_mov_b32 s68, 0\n" \
    "s_sub_u32 s76, s80, s45\ns_sub_u32 s69, s72, s76\ns_cmp_lt_i32 s69, -8\ns_cbranch_scc1 .Lq35_%=\ns_xor_b32 s90, s81, 2048\n.Lq37_%=:\ns_memrealtime s[72:73]\n" \
    "s_waitcnt lgkmcnt(0)\ns_sub_u32 s72, s72, s80\ns_cmp_lt_i32 s72, 0\ns_cbranch_scc1 .Lq37_%=\nbuffer_load_dwordx4 v[72:75], v2, s[64:67], s81 offen offset:0 sc1\n" \
    "v_cvt_pk_bf16_f32 v24, v32, v33\nv_cvt_pk_bf16_f32 v25, v34, v35\nv_cvt_pk_bf16_f32 v26, v40, v41\nbuffer_load_dwordx4 v[76:79], v2, s[64:67], s81 offen offset:1024 sc1\n" \
    "v_cvt_pk_bf16_f32 v27, v42, v43\nds_write_b128 v14, v[24:27] offset:8192\nv_cvt_pk_bf16_f32 v28, v36, v37\n" \
    "buffer_load_dwordx4 v[80:83], v2, s[64:67], s90 offen offset:0 sc1\nv_cvt_pk_bf16_f32 v29, v38, v39\nv_cvt_pk_bf16_f32 v30, v44, v45\nv_cvt_pk_bf16_f32 v31, v46, v47\n" \
    "buffer_load_dwordx4 v[84:87], v2, s[64:67], s90 offen offset:1024 sc1\nds_write_b128 v14, v[28:31] offset:12288\nds_read_b128 v[32:35], v3 offset:2048\n" \
    "ds_read_b128 v[36:39], v3 offset:3072\nds_read_b128 v[40:43], v3 offset:10240\nds_read_b128 v[44:47], v3 offset:11264\nds_read_b128 v[48:51], v3 offset:18432\n" \
    "ds_read_b128 v[52:55], v3 offset:19456\nds_read_b128 v[56:59], v3 offset:0\nds_read_b128 v[60:63], v3 offset:1024\nds_read_b128 v[64:67], v3 offset:4096\n" \
    "ds_read_b128 v[68:71], v3 offset:5120\n.Lq36_%=:\nv_mov_b64 v[16:17], 0\nv_mov_b64 v[18:19], 0\nv_mov_b64 v[20:21], 0\nv_mov_b64 v[22:23], 0\nv_mov_b64 v[24:25], 0\n" \
    "v_mov_b64 v[26:27], 0\nv_mov_b64 v[28:29], 0\nv_mov_b64 v[30:31], 0\ns_waitcnt vmcnt(3) lgkmcnt(8)\nv_smfmac_f32_16x16x64_bf16 v[20:23], v[72:75], v[32:39], v0\n" \
    "ds_write_b128 v1, v[72:75] offset:25600\nv_max3_u32 v252, v72, v73, v74\nv_smfmac_f32_16x16x64_bf16 v[16:19], v[72:75], a[24:31], v0\n" \
    "v_smfmac_f32_16x16x64_bf16 v[24:27], v[72:75], a[32:39], v0\nv_smfmac_f32_16x16x64_bf16 v[28:31], v[72:75], a[40:47], v0\ns_waitcnt vmcnt(2)\n" \
    "ds_write_b128 v1, v[76:79] offset:29696\ns_waitcnt lgkmcnt(8)\nv_max3_u32 v252, v252, v75, v76\nv_smfmac_f32_16x16x64_bf16 v[20:23], v[76:79], v[40:47], v0\n" \
    "v_max3_u32 v252, v252, v77, v78\nv_smfmac_f32_16x16x64_bf16 v[16:19], v[76:79], a[120:127], v0\nv_cmp_u_f32 vcc, v252, v79\n" \
    "v_smfmac_f32_16x16x64_bf16 v[24:27], v[76:79], a[128:135], v0\ns_cbranch_vccnz .Lq43_%=\n.Lq39_%=:\nv_smfmac_f32_16x16x64_bf16 v[28:31], v[76:79], a[136:143], v0\n" \
    "ds_read_b128 v[36:39], v3 offset:7168\ns_waitcnt vmcnt(1)\nv_max3_u32 v252, v80, v81, v82\nv_cmp_u_f32 vcc, v252, v83\ns_nop 0\ns_cbranch_vccnz .Lq44_%=\n.Lq40_%=:\n" \
    "s_waitcnt lgkmcnt(7)\nv_smfmac_f32_16x16x64_bf16 v[20:23], v[80:83], v[48:55], v0\nds_write_b128 v1, v[80:83] offset:33792\n" \
    "v_smfmac_f32_16x16x64_bf16 v[16:19], v[80:83], a[216:223], v0\nds_read_b128 v[32:35], v3 offset:6144\ns_waitcnt lgkmcnt(3)\ns_barrier\n" \
    "ds_read_b128 v[88:91], v1 offset:24576\nv_smfmac_f32_16x16x64_bf16 v[24:27], v[80:83], a[224:231], v0\nds_read_b128 v[40:43], v3 offset:8192\n" \
    "v_smfmac_f32_16x16x64_bf16 v[28:31], v[80:83], a[232:239], v0\nds_read_b128 v[48:51], v1 offset:26624\ns_waitcnt vmcnt(0)\nv_max3_u32 v252, v84, v85, v86\n" \
    "v_cmp_u_f32 vcc, v252, v87\ns_nop 0\ns_cbranch_vccnz .Lq45_%=\n.Lq41_%=:\nv_smfmac_f32_16x16x64_bf16 v[20:23], v[84:87], v[164:171], v0\n" \
    "ds_write_b128 v1, v[84:87] offset:37888\nv_smfmac_f32_16x16x64_bf16 v[16:19], v[84:87], v[156:163], v0\nds_read_b128 v[52:55], v1 offset:27648\n" \
    "v_smfmac_f32_16x16x64_bf16 v[24:27], v[84:87], v[172:179], v0\nds_read_b128 v[72:75], v1 offset:28672\nv_smfmac_f32_16x16x64_bf16 v[28:31], v[84:87], v[180:187], v0\n" \
    "ds_read_b128 v[44:47], v3 offset:9216\ns_waitcnt lgkmcnt(6)\nv_smfmac_f32_16x16x64_bf16 v[20:23], v[88:91], v[56:63], v0\nds_read_b128 v[56:59], v1 offset:30720\n" \
    "v_smfmac_f32_16x16x64_bf16 v[16:19], v[88:91], a[0:7], v0\nds_read_b128 v[76:79], v3 offset:12288\nv_smfmac_f32_16x16x64_bf16 v[24:27], v[88:91], a[8:15], v0\n" \
    "ds_read_b128 v[80:83], v3 offset:13312\nv_smfmac_f32_16x16x64_bf16 v[28:31], v[88:91], a[16:23], v0\nds_read_b128 v[60:63], v1 offset:31744\ns_waitcnt lgkmcnt(7)\n" \
    "s_barrier\nv_smfmac_f32_16x16x64_bf16 v[20:23], v[48:51], v[64:71], v0\nds_read_b128 v[64:67], v3 offset:14336\n" \
    "v_smfmac_f32_16x16x64_bf16 v[16:19], v[48:51], a[48:55], v0\nds_read_b128 v[68:71], v3 offset:15360\ns_waitcnt lgkmcnt(8)\n" \
    "v_smfmac_f32_16x16x64_bf16 v[20:23], v[52:55], v[32:39], v0\nds_read_b128 v[32:35], v1 offset:32768\nv_smfmac_f32_16x16x64_bf16 v[16:19], v[52:55], a[72:79], v0\n" \
    "ds_read_b128 v[84:87], v3 offset:16384\ns_waitcnt lgkmcnt(8)\nv_smfmac_f32_16x16x64_bf16 v[20:23], v[72:75], v[40:47], v0\nds_read_b128 v[88:91], v3 offset:17408\n" \
    "v_smfmac_f32_16x16x64_bf16 v[16:19], v[72:75], a[96:103], v0\nds_read_b128 v[36:39], v1 offset:34816\ns_waitcnt lgkmcnt(7)\n" \
    "v_smfmac_f32_16x16x64_bf16 v[20:23], v[56:59], v[76:83], v0\nds_read_b128 v[40:43], v3 offset:20480\nv_smfmac_f32_16x16x64_bf16 v[16:19], v[56:59], a[144:151], v0\n" \
    "ds_read_b128 v[44:47], v3 offset:21504\ns_waitcnt lgkmcnt(6)\nv_smfmac_f32_16x16x64_bf16 v[20:23], v[60:63], v[64:71], v0\nds_read_b128 v[64:67], v1 offset:35840\n" \
    "v_smfmac_f32_16x16x64_bf16 v[16:19], v[60:63], a[168:175], v0\nds_read_b128 v[76:79], v3 offset:22528\ns_waitcnt lgkmcnt(5)\n" \
    "v_smfmac_f32_16x16x64_bf16 v[20:23], v[32:35], v[84:91], v0\nds_read_b128 v[80:83], v3 offset:23552\nv_smfmac_f32_16x16x64_bf16 v[16:19], v[32:35], a[192:199], v0\n" \
    "ds_read_b128 v[68:71], v1 offset:36864\ns_waitcnt lgkmcnt(4)\nv_smfmac_f32_16x16x64_bf16 v[20:23], v[36:39], v[40:47], v0\nds_read_b128 v[40:43], v1 offset:38912\n" \
    "v_smfmac_f32_16x16x64_bf16 v[16:19], v[36:39], a[240:247], v0\nds_read_b128 v[44:47], v1 offset:39936\ns_waitcnt lgkmcnt(3)\n" \
    "v_smfmac_f32_16x16x64_bf16 v[20:23], v[64:67], v[76:83], v0\nv_smfmac_f32_16x16x64_bf16 v[16:19], v[64:67], v[100:107], v0\ns_waitcnt lgkmcnt(2)\n" \
    "v_smfmac_f32_16x16x64_bf16 v[20:23], v[68:71], v[132:139], v0\ns_waitcnt lgkmcnt(1)\nv_smfmac_f32_16x16x64_bf16 v[20:23], v[40:43], v[196:203], v0\ns_waitcnt lgkmcnt(0)\n" \
    "v_smfmac_f32_16x16x64_bf16 v[20:23], v[44:47], v[228:235], v0\ns_nop 2\nv_smfmac_f32_16x16x64_bf16 v[16:19], v[68:71], v[124:131], v0\ns_nop 2\n" \
    "v_smfmac_f32_16x16x64_bf16 v[16:19], v[40:43], v[188:195], v0\nv_permlane32_swap_b32 v20, v22\nv_smfmac_f32_16x16x64_bf16 v[16:19], v[44:47], v[220:227], v0\ns_nop 1\n" \
    "v_permlane32_swap_b32 v21, v23\nv_smfmac_f32_16x16x64_bf16 v[24:27], v[48:51], a[56:63], v0\ns_nop 1\nv_add_f32 v20, v20, v22\n" \
    "v_smfmac_f32_16x16x64_bf16 v[28:31], v[48:51], a[64:71], v0\nv_add_f32 v21, v21, v23\nv_smfmac_f32_16x16x64_bf16 v[24:27], v[52:55], a[80:87], v0\nv_mul_f32 v20, s32, v20\n" \
    "v_smfmac_f32_16x16x64_bf16 v[28:31], v[52:55], a[88:95], v0\nv_mul_f32 v21, s32, v21\nv_smfmac_f32_16x16x64_bf16 v[24:27], v[72:75], a[104:111], v0\n" \
    "v_cvt_pk_bf16_f32 v20, v20, v21\nv_smfmac_f32_16x16x64_bf16 v[28:31], v[72:75], a[112:119], v0\nds_write_b16 v4, v20 offset:8\nv_permlane32_swap_b32 v16, v18\n" \
    "v_smfmac_f32_16x16x64_bf16 v[24:27], v[56:59], a[152:159], v0\nds_write_b16_d16_hi v4, v20 offset:24\nv_permlane32_swap_b32 v17, v19\n" \
    "v_smfmac_f32_16x16x64_bf16 v[24:27], v[60:63], a[176:183], v0\nds_read_b128 v[48:51], v1 offset:40960\nv_add_f32 v16, v16, v18\n" \
    "v_smfmac_f32_16x16x64_bf16 v[24:27], v[32:35], a[200:207], v0\nds_read_b128 v[52:55], v1 offset:45056\nv_add_f32 v17, v17, v19\n" \
    "v_smfmac_f32_16x16x64_bf16 v[24:27], v[36:39], a[248:255], v0\nds_read_b128 v[72:75], v1 offset:41984\nv_mul_f32 v16, s32, v16\n" \
    "v_smfmac_f32_16x16x64_bf16 v[24:27], v[64:67], v[108:115], v0\nds_read_b128 v[76:79], v1 offset:46080\nv_mul_f32 v17, s32, v17\n" \
    "v_smfmac_f32_16x16x64_bf16 v[24:27], v[68:71], v[140:147], v0\nds_read_b128 v[80:83], v1 offset:43008\nv_cvt_pk_bf16_f32 v16, v16, v17\n" \
    "v_smfmac_f32_16x16x64_bf16 v[24:27], v[40:43], v[204:211], v0\nds_write_b16 v4, v16 offset:0\nv_smfmac_f32_16x16x64_bf16 v[24:27], v[44:47], v[236:243], v0\n" \
    "ds_write_b16_d16_hi v4, v16 offset:16\ns_nop 1\nv_smfmac_f32_16x16x64_bf16 v[28:31], v[56:59], a[160:167], v0\nds_read_b128 v[56:59], v1 offset:47104\ns_nop 1\n" \
    "v_smfmac_f32_16x16x64_bf16 v[28:31], v[60:63], a[184:191], v0\nds_read_b128 v[60:63], v1 offset:44032\nv_permlane32_swap_b32 v24, v26\n" \
    "v_smfmac_f32_16x16x64_bf16 v[28:31], v[32:35], a[208:215], v0\nds_read_b128 v[32:35], v1 offset:48128\nv_permlane32_swap_b32 v25, v27\n" \
    "v_smfmac_f32_16x16x64_bf16 v[28:31], v[36:39], v[92:99], v0\nds_read_b128 v[36:39], v6 offset:0\nv_add_f32 v24, v24, v26\n" \
    "v_smfmac_f32_16x16x64_bf16 v[28:31], v[64:67], v[116:123], v0\nds_read_b128 v[64:67], v6 offset:512\nv_add_f32 v25, v25, v27\n" \
    "v_smfmac_f32_16x16x64_bf16 v[28:31], v[68:71], v[148:155], v0\nds_read_b128 v[68:71], v6 offset:1024\nv_cvt_pk_bf16_f32 v24, v24, v25\n" \
    "v_smfmac_f32_16x16x64_bf16 v[28:31], v[40:43], v[212:219], v0\nds_read_b128 v[40:43], v6 offset:1536\nv_smfmac_f32_16x16x64_bf16 v[28:31], v[44:47], v[244:251], v0\n" \
    "ds_write_b16 v4, v24 offset:2048\nds_write_b16_d16_hi v4, v24 offset:2064\ns_waitcnt lgkmcnt(5)\nv_mfma_f32_16x16x32_bf16 v[16:19], v[48:51], v[36:39], 0\ns_nop 2\n" \
    "v_mfma_f32_16x16x32_bf16 v[20:23], v[52:55], v[36:39], 0\nv_permlane32_swap_b32 v28, v30\nv_permlane32_swap_b32 v29, v31\nv_add_f32 v28, v28, v30\nv_add_f32 v29, v29, v31\n" \
    "v_cvt_pk_bf16_f32 v28, v28, v29\nds_write_b16 v4, v28 offset:2056\nds_write_b16_d16_hi v4, v28 offset:2072\ns_waitcnt lgkmcnt(6)\n" \
    "v_mfma_f32_16x16x32_bf16 v[16:19], v[72:75], v[64:67], v[16:19]\ns_cmp_eq_u32 s35, 63\ns_cbranch_scc1 .Lq8_%=\n" \
    "v_mfma_f32_16x16x32_bf16 v[20:23], v[76:79], v[64:67], v[20:23]\ns_waitcnt lgkmcnt(5)\nv_mfma_f32_16x16x32_bf16 v[16:19], v[80:83], v[68:71], v[16:19]\n" \
    "ds_read_b128 v[44:47], v8 offset:0\nds_read_b128 v[84:87], v8 offset:512\nv_mfma_f32_16x16x32_bf16 v[20:23], v[56:59], v[68:71], v[20:23]\n" \
    "ds_read_b128 v[88:91], v8 offset:1024\nds_read_b128 v[28:31], v8 offset:1536\ns_waitcnt lgkmcnt(8)\nv_mfma_f32_16x16x32_bf16 v[16:19], v[60:63], v[40:43], v[16:19]\n" \
    "v_mfma_f32_16x16x32_bf16 v[20:23], v[32:35], v[40:43], v[20:23]\nv_mov_b32 v15, v15\nv_mov_b32 v15, v15\ns_waitcnt lgkmcnt(3)\n" \
    "v_mfma_f32_16x16x32_bf16 v[24:27], v[44:47], v[36:39], 0\ns_nop 2\ns_waitcnt lgkmcnt(2)\nv_mov_b32_dpp v16, v20 quad_perm:[0,1,2,3] row_mask:0xf bank_mask:0xc\n" \
    "v_mov_b32_dpp v17, v21 quad_perm:[0,1,2,3] row_mask:0xf bank_mask:0xc\nv_mfma_f32_16x16x32_bf16 v[24:27], v[84:87], v[64:67], v[24:27]\ns_waitcnt lgkmcnt(1)\n" \
    "v_mov_b32_dpp v18, v22 quad_perm:[0,1,2,3] row_mask:0xf bank_mask:0xc\nv_mov_b32_dpp v19, v23 quad_perm:[0,1,2,3] row_mask:0xf bank_mask:0xc\n" \
    "v_mfma_f32_16x16x32_bf16 v[24:27], v[88:91], v[68:71], v[24:27]\ns_waitcnt lgkmcnt(0)\nv_max3_f32 v254, v16, v17, v18\nv_max_f32 v254, v254, v19\n" \
    "v_mfma_f32_16x16x32_bf16 v[24:27], v[28:31], v[40:43], v[24:27]\ns_nop 0\nv_max_f32_dpp v252, v254, v254 row_ror:8 row_mask:0xf bank_mask:0xf\n" \
    "v_max_f32_dpp v253, v254, v254 row_ror:8 row_mask:0xf bank_mask:0xf\ns_nop 1\nv_permlane32_swap_b32 v252, v253\nv_max_f32 v252, v252, v253\ns_xor_b32 s74, s82, 0x4000\n" \
    "v_max3_f32 v254, v252, v24, v25\nv_max3_f32 v252, v254, v26, v27\nv_max3_f32 v253, v254, v26, v27\ns_nop 1\nv_permlane16_swap_b32 v252, v253\nv_max_f32 v252, v252, v253\n" \
    "v_pk_add_f32 v[16:17], v[16:17], v[252:253] op_sel_hi:[1,0] neg_lo:[0,1] neg_hi:[0,1]\n" \
    "v_pk_add_f32 v[18:19], v[18:19], v[252:253] op_sel_hi:[1,0] neg_lo:[0,1] neg_hi:[0,1]\n" \
    "v_pk_add_f32 v[24:25], v[24:25], v[252:253] op_sel_hi:[1,0] neg_lo:[0,1] neg_hi:[0,1]\n" \
    "v_pk_add_f32 v[26:27], v[26:27], v[252:253] op_sel_hi:[1,0] neg_lo:[0,1] neg_hi:[0,1]\nv_exp_f32 v16, v16\nv_exp_f32 v17, v17\nv_exp_f32 v18, v18\nv_exp_f32 v19, v19\n" \
    "v_exp_f32 v24, v24\nv_pk_add_f32 v[254:255], v[16:17], v[18:19]\nv_exp_f32 v25, v25\nv_add_f32 v254, v254, v255\nv_exp_f32 v26, v26\nv_exp_f32 v27, v27\n" \
    "v_add_f32_dpp v252, v254, v254 row_ror:8 row_mask:0xf bank_mask:0xf\nv_add_f32_dpp v253, v254, v254 row_ror:8 row_mask:0xf bank_mask:0xf\n" \
    "v_pk_add_f32 v[254:255], v[24:25], v[26:27]\ns_nop 0\nv_permlane32_swap_b32 v252, v253\nv_pk_add_f32 v[254:255], v[254:255], v[252:253]\n" \
    "v_pk_add_f32 v[252:253], v[254:255], v[254:255] op_sel:[0,1] op_sel_hi:[1,0]\ns_nop 1\nv_permlane16_swap_b32 v252, v253\nv_add_f32 v252, v252, v253\n" \
    "v_cmp_u_f32 vcc, v252, v252\nv_rcp_f32 v252, v252\ns_cbranch_vccnz .Lq12_%=\nv_pk_mul_f32 v[16:17], v[16:17], v[252:253] op_sel_hi:[1,0]\n" \
    "v_pk_mul_f32 v[24:25], v[24:25], v[252:253] op_sel_hi:[1,0]\nv_cvt_pk_bf16_f32 v20, v16, v17\nv_cvt_pk_bf16_f32 v22, v24, v25\n" \
    "v_pk_mul_f32 v[18:19], v[18:19], v[252:253] op_sel_hi:[1,0]\nv_pk_mul_f32 v[26:27], v[26:27], v[252:253] op_sel_hi:[1,0]\nv_cvt_pk_bf16_f32 v21, v18, v19\n" \
    "v_cvt_pk_bf16_f32 v23, v26, v27\nv_permlane32_swap_b32 v20, v22\ns_nop 0\nv_permlane32_swap_b32 v21, v23\nbuffer_store_dwordx4 v[20:23], v7, s[64:67], s82 offen sc1\n" \
    "buffer_store_dwordx4 v[20:23], v7, s[64:67], s74 offen sc1\ns_branch .Lq3_%=\n.Lq6_%=:\n.Lq51_%=:\ns_memrealtime s[72:73]\ns_waitcnt lgkmcnt(0)\ns_mov_b32 s68, 0\n" \
    "s_sub_u32 s76, s80, s45\ns_sub_u32 s69, s72, s76\ns_cmp_lt_i32 s69, -8\ns_cbranch_scc1 .Lq54_%=\ns_xor_b32 s90, s81, 2048\n.Lq56_%=:\ns_memrealtime s[72:73]\n" \
    "s_waitcnt lgkmcnt(0)\ns_sub_u32 s72, s72, s80\ns_cmp_lt_i32 s72, 0\ns_cbranch_scc1 .Lq56_%=\nbuffer_load_dwordx4 v[72:75], v2, s[64:67], s81 offen offset:0 sc1\n" \
    "v_cvt_pk_bf16_f32 v24, v32, v33\nv_cvt_pk_bf16_f32 v25, v34, v35\nv_cvt_pk_bf16_f32 v26, v40, v41\nbuffer_load_dwordx4 v[76:79], v2, s[64:67], s81 offen offset:1024 sc1\n" \
    "v_cvt_pk_bf16_f32 v27, v42, v43\nds_write_b128 v14, v[24:27] offset:8192\nv_cvt_pk_bf16_f32 v28, v36, v37\n" \
    "buffer_load_dwordx4 v[80:83], v2, s[64:67], s90 offen offset:0 sc1\nv_cvt_pk_bf16_f32 v29, v38, v39\nv_cvt_pk_bf16_f32 v30, v44, v45\nv_cvt_pk_bf16_f32 v31, v46, v47\n" \
    "buffer_load_dwordx4 v[84:87], v2, s[64:67], s90 offen offset:1024 sc1\nds_write_b128 v14, v[28:31] offset:12288\nds_read_b128 v[32:35], v3 offset:4096\n" \
    "ds_read_b128 v[36:39], v3 offset:5120\nds_read_b128 v[40:43], v3 offset:12288\nds_read_b128 v[44:47], v3 offset:13312\nds_read_b128 v[48:51], v3 offset:20480\n" \
    "ds_read_b128 v[52:55], v3 offset:21504\nds_read_b128 v[56:59], v3 offset:0\nds_read_b128 v[60:63], v3 offset:1024\nds_read_b128 v[64:67], v3 offset:2048\n" \
    "ds_read_b128 v[68:71], v3 offset:3072\n.Lq55_%=:\nv_mov_b64 v[16:17], 0\nv_mov_b64 v[18:19], 0\nv_mov_b64 v[20:21], 0\nv_mov_b64 v[22:23], 0\nv_mov_b64 v[24:25], 0\n" \
    "v_mov_b64 v[26:27], 0\nv_mov_b64 v[28:29], 0\nv_mov_b64 v[30:31], 0\ns_waitcnt vmcnt(3) lgkmcnt(8)\nv_smfmac_f32_16x16x64_bf16 v[20:23], v[72:75], v[32:39], v0\n" \
    "ds_write_b128 v1, v[72:75] offset:26624\nv_max3_u32 v252, v72, v73, v74\nv_smfmac_f32_16x16x64_bf16 v[16:19], v[72:75], a[48:55], v0\n" \
    "v_smfmac_f32_16x16x64_bf16 v[24:27], v[72:75], a[56:63], v0\nv_smfmac_f32_16x16x64_bf16 v[28:31], v[72:75], a[64:71], v0\ns_waitcnt vmcnt(2)\n" \
    "ds_write_b128 v1, v[76:79] offset:30720\ns_waitcnt lgkmcnt(8)\nv_max3_u32 v252, v252, v75, v76\nv_smfmac_f32_16x16x64_bf16 v[20:23], v[76:79], v[40:47], v0\n" \
    "v_max3_u32 v252, v252, v77, v78\nv_smfmac_f32_16x16x64_bf16 v[16:19], v[76:79], a[144:151], v0\nv_cmp_u_f32 vcc, v252, v79\n" \
    "v_smfmac_f32_16x16x64_bf16 v[24:27], v[76:79], a[152:159], v0\ns_cbranch_vccnz .Lq62_%=\n.Lq58_%=:\nv_smfmac_f32_16x16x64_bf16 v[28:31], v[76:79], a[160:167], v0\n" \
    "ds_read_b128 v[36:39], v3 offset:7168\ns_waitcnt vmcnt(1)\nv_max3_u32 v252, v80, v81, v82\nv_cmp_u_f32 vcc, v252, v83\ns_nop 0\ns_cbranch_vccnz .Lq63_%=\n.Lq59_%=:\n" \
    "s_waitcnt lgkmcnt(7)\nv_smfmac_f32_16x16x64_bf16 v[20:23], v[80:83], v[48:55], v0\nds_write_b128 v1, v[80:83] offset:34816\n" \
    "v_smfmac_f32_16x16x64_bf16 v[16:19], v[80:83], a[240:247], v0\nds_read_b128 v[32:35], v3 offset:6144\ns_waitcnt lgkmcnt(3)\ns_barrier\n" \
    "ds_read_b128 v[88:91], v1 offset:24576\nv_smfmac_f32_16x16x64_bf16 v[24:27], v[80:83], a[248:255], v0\nds_read_b128 v[40:43], v3 offset:8192\n" \
    "v_smfmac_f32_16x16x64_bf16 v[28:31], v[80:83], v[92:99], v0\nds_read_b128 v[48:51], v1 offset:25600\ns_waitcnt vmcnt(0)\nv_max3_u32 v252, v84, v85, v86\n" \
    "v_cmp_u_f32 vcc, v252, v87\ns_nop 0\ns_cbranch_vccnz .Lq64_%=\n.Lq60_%=:\nv_smfmac_f32_16x16x64_bf16 v[20:23], v[84:87], v[196:203], v0\n" \
    "ds_write_b128 v1, v[84:87] offset:38912\nv_smfmac_f32_16x16x64_bf16 v[16:19], v[84:87], v[188:195], v0\nds_read_b128 v[52:55], v1 offset:27648\n" \
    "v_smfmac_f32_16x16x64_bf16 v[24:27], v[84:87], v[204:211], v0\nds_read_b128 v[72:75], v1 offset:28672\nv_smfmac_f32_16x16x64_bf16 v[28:31], v[84:87], v[212:219], v0\n" \
    "ds_read_b128 v[44:47], v3 offset:9216\ns_waitcnt lgkmcnt(6)\nv_smfmac_f32_16x16x64_bf16 v[20:23], v[88:91], v[56:63], v0\nds_read_b128 v[56:59], v1 offset:29696\n" \
    "v_smfmac_f32_16x16x64_bf16 v[16:19], v[88:91], a[0:7], v0\nds_read_b128 v[76:79], v3 offset:10240\nv_smfmac_f32_16x16x64_bf16 v[24:27], v[88:91], a[8:15], v0\n" \
    "ds_read_b128 v[80:83], v3 offset:11264\nv_smfmac_f32_16x16x64_bf16 v[28:31], v[88:91], a[16:23], v0\nds_read_b128 v[60:63], v1 offset:31744\ns_waitcnt lgkmcnt(7)\n" \
    "s_barrier\nv_smfmac_f32_16x16x64_bf16 v[20:23], v[48:51], v[64:71], v0\nds_read_b128 v[64:67], v3 offset:14336\n" \
    "v_smfmac_f32_16x16x64_bf16 v[16:19], v[48:51], a[24:31], v0\nds_read_b128 v[68:71], v3 offset:15360\ns_waitcnt lgkmcnt(8)\n" \
    "v_smfmac_f32_16x16x64_bf16 v[20:23], v[52:55], v[32:39], v0\nds_read_b128 v[32:35], v1 offset:32768\nv_smfmac_f32_16x16x64_bf16 v[16:19], v[52:55], a[72:79], v0\n" \
    "ds_read_b128 v[84:87], v3 offset:16384\ns_waitcnt lgkmcnt(8)\nv_smfmac_f32_16x16x64_bf16 v[20:23], v[72:75], v[40:47], v0\nds_read_b128 v[88:91], v3 offset:17408\n" \
    "v_smfmac_f32_16x16x64_bf16 v[16:19], v[72:75], a[96:103], v0\nds_read_b128 v[36:39], v1 offset:33792\ns_waitcnt lgkmcnt(7)\n" \
    "v_smfmac_f32_16x16x64_bf16 v[20:23], v[56:59], v[76:83], v0\nds_read_b128 v[40:43], v3 offset:18432\nv_smfmac_f32_16x16x64_bf16 v[16:19], v[56:59], a[120:127], v0\n" \
    "ds_read_b128 v[44:47], v3 offset:19456\ns_waitcnt lgkmcnt(6)\nv_smfmac_f32_16x16x64_bf16 v[20:23], v[60:63], v[64:71], v0\nds_read_b128 v[64:67], v1 offset:35840\n" \
    "v_smfmac_f32_16x16x64_bf16 v[16:19], v[60:63], a[168:175], v0\nds_read_b128 v[76:79], v3 offset:22528\ns_waitcnt lgkmcnt(5)\n" \
    "v_smfmac_f32_16x16x64_bf16 v[20:23], v[32:35], v[84:91], v0\nds_read_b128 v[80:83], v3 offset:23552\nv_smfmac_f32_16x16x64_bf16 v[16:19], v[32:35], a[192:199], v0\n" \
    "ds_read_b128 v[68:71], v1 offset:36864\ns_waitcnt lgkmcnt(4)\nv_smfmac_f32_16x16x64_bf16 v[20:23], v[36:39], v[40:47], v0\nds_read_b128 v[40:43], v1 offset:37888\n" \
    "v_smfmac_f32_16x16x64_bf16 v[16:19], v[36:39], a[216:223], v0\nds_read_b128 v[44:47], v1 offset:39936\ns_waitcnt lgkmcnt(3)\n" \
    "v_smfmac_f32_16x16x64_bf16 v[20:23], v[64:67], v[76:83], v0\nv_smfmac_f32_16x16x64_bf16 v[16:19], v[64:67], v[100:107], v0\ns_waitcnt lgkmcnt(2)\n" \
    "v_smfmac_f32_16x16x64_bf16 v[20:23], v[68:71], v[132:139], v0\ns_waitcnt lgkmcnt(1)\nv_smfmac_f32_16x16x64_bf16 v[20:23], v[40:43], v[164:171], v0\ns_waitcnt lgkmcnt(0)\n" \
    "v_smfmac_f32_16x16x64_bf16 v[20:23], v[44:47], v[228:235], v0\ns_nop 2\nv_smfmac_f32_16x16x64_bf16 v[16:19], v[68:71], v[124:131], v0\ns_nop 2\n" \
    "v_smfmac_f32_16x16x64_bf16 v[16:19], v[40:43], v[156:163], v0\nv_permlane32_swap_b32 v20, v22\nv_smfmac_f32_16x16x64_bf16 v[16:19], v[44:47], v[220:227], v0\ns_nop 1\n" \
    "v_permlane32_swap_b32 v21, v23\nv_smfmac_f32_16x16x64_bf16 v[24:27], v[48:51], a[32:39], v0\ns_nop 1\nv_add_f32 v20, v20, v22\n" \
    "v_smfmac_f32_16x16x64_bf16 v[28:31], v[48:51], a[40:47], v0\nv_add_f32 v21, v21, v23\nv_smfmac_f32_16x16x64_bf16 v[24:27], v[52:55], a[80:87], v0\nv_mul_f32 v20, s32, v20\n" \
    "v_smfmac_f32_16x16x64_bf16 v[28:31], v[52:55], a[88:95], v0\nv_mul_f32 v21, s32, v21\nv_smfmac_f32_16x16x64_bf16 v[24:27], v[72:75], a[104:111], v0\n" \
    "v_cvt_pk_bf16_f32 v20, v20, v21\nv_smfmac_f32_16x16x64_bf16 v[28:31], v[72:75], a[112:119], v0\nds_write_b16 v4, v20 offset:8\nv_permlane32_swap_b32 v16, v18\n" \
    "v_smfmac_f32_16x16x64_bf16 v[24:27], v[56:59], a[128:135], v0\nds_write_b16_d16_hi v4, v20 offset:24\nv_permlane32_swap_b32 v17, v19\n" \
    "v_smfmac_f32_16x16x64_bf16 v[24:27], v[60:63], a[176:183], v0\nds_read_b128 v[48:51], v1 offset:40960\nv_add_f32 v16, v16, v18\n" \
    "v_smfmac_f32_16x16x64_bf16 v[24:27], v[32:35], a[200:207], v0\nds_read_b128 v[52:55], v1 offset:45056\nv_add_f32 v17, v17, v19\n" \
    "v_smfmac_f32_16x16x64_bf16 v[24:27], v[36:39], a[224:231], v0\nds_read_b128 v[72:75], v1 offset:41984\nv_mul_f32 v16, s32, v16\n" \
    "v_smfmac_f32_16x16x64_bf16 v[24:27], v[64:67], v[108:115], v0\nds_read_b128 v[76:79], v1 offset:46080\nv_mul_f32 v17, s32, v17\n" \
    "v_smfmac_f32_16x16x64_bf16 v[24:27], v[68:71], v[140:147], v0\nds_read_b128 v[80:83], v1 offset:43008\nv_cvt_pk_bf16_f32 v16, v16, v17\n" \
    "v_smfmac_f32_16x16x64_bf16 v[24:27], v[40:43], v[172:179], v0\nds_write_b16 v4, v16 offset:0\nv_smfmac_f32_16x16x64_bf16 v[24:27], v[44:47], v[236:243], v0\n" \
    "ds_write_b16_d16_hi v4, v16 offset:16\ns_nop 1\nv_smfmac_f32_16x16x64_bf16 v[28:31], v[56:59], a[136:143], v0\nds_read_b128 v[56:59], v1 offset:47104\ns_nop 1\n" \
    "v_smfmac_f32_16x16x64_bf16 v[28:31], v[60:63], a[184:191], v0\nds_read_b128 v[60:63], v1 offset:44032\nv_permlane32_swap_b32 v24, v26\n" \
    "v_smfmac_f32_16x16x64_bf16 v[28:31], v[32:35], a[208:215], v0\nds_read_b128 v[32:35], v1 offset:48128\nv_permlane32_swap_b32 v25, v27\n" \
    "v_smfmac_f32_16x16x64_bf16 v[28:31], v[36:39], a[232:239], v0\nds_read_b128 v[36:39], v6 offset:0\nv_add_f32 v24, v24, v26\n" \
    "v_smfmac_f32_16x16x64_bf16 v[28:31], v[64:67], v[116:123], v0\nds_read_b128 v[64:67], v6 offset:512\nv_add_f32 v25, v25, v27\n" \
    "v_smfmac_f32_16x16x64_bf16 v[28:31], v[68:71], v[148:155], v0\nds_read_b128 v[68:71], v6 offset:1024\nv_cvt_pk_bf16_f32 v24, v24, v25\n" \
    "v_smfmac_f32_16x16x64_bf16 v[28:31], v[40:43], v[180:187], v0\nds_read_b128 v[40:43], v6 offset:1536\nv_smfmac_f32_16x16x64_bf16 v[28:31], v[44:47], v[244:251], v0\n" \
    "ds_write_b16 v4, v24 offset:2048\nds_write_b16_d16_hi v4, v24 offset:2064\ns_waitcnt lgkmcnt(5)\nv_mfma_f32_16x16x32_bf16 v[16:19], v[48:51], v[36:39], 0\ns_nop 2\n" \
    "v_mfma_f32_16x16x32_bf16 v[20:23], v[52:55], v[36:39], 0\nv_permlane32_swap_b32 v28, v30\nv_permlane32_swap_b32 v29, v31\nv_add_f32 v28, v28, v30\nv_add_f32 v29, v29, v31\n" \
    "v_cvt_pk_bf16_f32 v28, v28, v29\nds_write_b16 v4, v28 offset:2056\nds_write_b16_d16_hi v4, v28 offset:2072\ns_waitcnt lgkmcnt(6)\n" \
    "v_mfma_f32_16x16x32_bf16 v[16:19], v[72:75], v[64:67], v[16:19]\ns_cmp_eq_u32 s35, 63\ns_cbranch_scc1 .Lq8_%=\n" \
    "v_mfma_f32_16x16x32_bf16 v[20:23], v[76:79], v[64:67], v[20:23]\ns_waitcnt lgkmcnt(5)\nv_mfma_f32_16x16x32_bf16 v[16:19], v[80:83], v[68:71], v[16:19]\n" \
    "ds_read_b128 v[44:47], v8 offset:0\nds_read_b128 v[84:87], v8 offset:512\nv_mfma_f32_16x16x32_bf16 v[20:23], v[56:59], v[68:71], v[20:23]\n" \
    "ds_read_b128 v[88:91], v8 offset:1024\nds_read_b128 v[28:31], v8 offset:1536\ns_waitcnt lgkmcnt(8)\nv_mfma_f32_16x16x32_bf16 v[16:19], v[60:63], v[40:43], v[16:19]\n" \
    "v_mfma_f32_16x16x32_bf16 v[20:23], v[32:35], v[40:43], v[20:23]\nv_mov_b32 v15, v15\nv_mov_b32 v15, v15\ns_waitcnt lgkmcnt(3)\n" \
    "v_mfma_f32_16x16x32_bf16 v[24:27], v[44:47], v[36:39], 0\ns_nop 2\ns_waitcnt lgkmcnt(2)\nv_mov_b32_dpp v16, v20 quad_perm:[0,1,2,3] row_mask:0xf bank_mask:0xc\n" \
    "v_mov_b32_dpp v17, v21 quad_perm:[0,1,2,3] row_mask:0xf bank_mask:0xc\nv_mfma_f32_16x16x32_bf16 v[24:27], v[84:87], v[64:67], v[24:27]\ns_waitcnt lgkmcnt(1)\n" \
    "v_mov_b32_dpp v18, v22 quad_perm:[0,1,2,3] row_mask:0xf bank_mask:0xc\nv_mov_b32_dpp v19, v23 quad_perm:[0,1,2,3] row_mask:0xf bank_mask:0xc\n" \
    "v_mfma_f32_16x16x32_bf16 v[24:27], v[88:91], v[68:71], v[24:27]\ns_waitcnt lgkmcnt(0)\nv_max3_f32 v254, v16, v17, v18\nv_max_f32 v254, v254, v19\n" \
    "v_mfma_f32_16x16x32_bf16 v[24:27], v[28:31], v[40:43], v[24:27]\ns_nop 0\nv_max_f32_dpp v252, v254, v254 row_ror:8 row_mask:0xf bank_mask:0xf\n" \
    "v_max_f32_dpp v253, v254, v254 row_ror:8 row_mask:0xf bank_mask:0xf\ns_nop 1\nv_permlane32_swap_b32 v252, v253\nv_max_f32 v252, v252, v253\ns_xor_b32 s74, s82, 0x4000\n" \
    "v_max3_f32 v254, v252, v24, v25\nv_max3_f32 v252, v254, v26, v27\nv_max3_f32 v253, v254, v26, v27\ns_nop 1\nv_permlane16_swap_b32 v252, v253\nv_max_f32 v252, v252, v253\n" \
    "v_pk_add_f32 v[16:17], v[16:17], v[252:253] op_sel_hi:[1,0] neg_lo:[0,1] neg_hi:[0,1]\n" \
    "v_pk_add_f32 v[18:19], v[18:19], v[252:253] op_sel_hi:[1,0] neg_lo:[0,1] neg_hi:[0,1]\n" \
    "v_pk_add_f32 v[24:25], v[24:25], v[252:253] op_sel_hi:[1,0] neg_lo:[0,1] neg_hi:[0,1]\n" \
    "v_pk_add_f32 v[26:27], v[26:27], v[252:253] op_sel_hi:[1,0] neg_lo:[0,1] neg_hi:[0,1]\nv_exp_f32 v16, v16\nv_exp_f32 v17, v17\nv_exp_f32 v18, v18\nv_exp_f32 v19, v19\n" \
    "v_exp_f32 v24, v24\nv_pk_add_f32 v[254:255], v[16:17], v[18:19]\nv_exp_f32 v25, v25\nv_add_f32 v254, v254, v255\nv_exp_f32 v26, v26\nv_exp_f32 v27, v27\n" \
    "v_add_f32_dpp v252, v254, v254 row_ror:8 row_mask:0xf bank_mask:0xf\nv_add_f32_dpp v253, v254, v254 row_ror:8 row_mask:0xf bank_mask:0xf\n" \
    "v_pk_add_f32 v[254:255], v[24:25], v[26:27]\ns_nop 0\nv_permlane32_swap_b32 v252, v253\nv_pk_add_f32 v[254:255], v[254:255], v[252:253]\n" \
    "v_pk_add_f32 v[252:253], v[254:255], v[254:255] op_sel:[0,1] op_sel_hi:[1,0]\ns_nop 1\nv_permlane16_swap_b32 v252, v253\nv_add_f32 v252, v252, v253\n" \
    "v_cmp_u_f32 vcc, v252, v252\nv_rcp_f32 v252, v252\ns_cbranch_vccnz .Lq12_%=\nv_pk_mul_f32 v[16:17], v[16:17], v[252:253] op_sel_hi:[1,0]\n" \
    "v_pk_mul_f32 v[24:25], v[24:25], v[252:253] op_sel_hi:[1,0]\nv_cvt_pk_bf16_f32 v20, v16, v17\nv_cvt_pk_bf16_f32 v22, v24, v25\n" \
    "v_pk_mul_f32 v[18:19], v[18:19], v[252:253] op_sel_hi:[1,0]\nv_pk_mul_f32 v[26:27], v[26:27], v[252:253] op_sel_hi:[1,0]\nv_cvt_pk_bf16_f32 v21, v18, v19\n" \
    "v_cvt_pk_bf16_f32 v23, v26, v27\nv_permlane32_swap_b32 v20, v22\ns_nop 0\nv_permlane32_swap_b32 v21, v23\nbuffer_store_dwordx4 v[20:23], v7, s[64:67], s82 offen sc1\n" \
    "buffer_store_dwordx4 v[20:23], v7, s[64:67], s74 offen sc1\ns_branch .Lq3_%=\n.Lq7_%=:\n.Lq70_%=:\ns_memrealtime s[72:73]\ns_waitcnt lgkmcnt(0)\ns_mov_b32 s68, 0\n" \
    "s_sub_u32 s76, s80, s45\ns_sub_u32 s69, s72, s76\ns_cmp_lt_i32 s69, -8\ns_cbranch_scc1 .Lq73_%=\ns_xor_b32 s90, s81, 2048\n.Lq75_%=:\ns_memrealtime s[72:73]\n" \
    "s_waitcnt lgkmcnt(0)\ns_sub_u32 s72, s72, s80\ns_cmp_lt_i32 s72, 0\ns_cbranch_scc1 .Lq75_%=\nbuffer_load_dwordx4 v[72:75], v2, s[64:67], s81 offen offset:0 sc1\n" \
    "v_cvt_pk_bf16_f32 v24, v32, v33\nv_cvt_pk_bf16_f32 v25, v34, v35\nv_cvt_pk_bf16_f32 v26, v40, v41\nbuffer_load_dwordx4 v[76:79], v2, s[64:67], s81 offen offset:1024 sc1\n" \
    "v_cvt_pk_bf16_f32 v27, v42, v43\nds_write_b128 v14, v[24:27] offset:8192\nv_cvt_pk_bf16_f32 v28, v36, v37\n" \
    "buffer_load_dwordx4 v[80:83], v2, s[64:67], s90 offen offset:0 sc1\nv_cvt_pk_bf16_f32 v29, v38, v39\nv_cvt_pk_bf16_f32 v30, v44, v45\nv_cvt_pk_bf16_f32 v31, v46, v47\n" \
    "buffer_load_dwordx4 v[84:87], v2, s[64:67], s90 offen offset:1024 sc1\nds_write_b128 v14, v[28:31] offset:12288\nds_read_b128 v[32:35], v3 offset:6144\n" \
    "ds_read_b128 v[36:39], v3 offset:7168\nds_read_b128 v[40:43], v3 offset:14336\nds_read_b128 v[44:47], v3 offset:15360\nds_read_b128 v[48:51], v3 offset:22528\n" \
    "ds_read_b128 v[52:55], v3 offset:23552\nds_read_b128 v[56:59], v3 offset:0\nds_read_b128 v[60:63], v3 offset:1024\nds_read_b128 v[64:67], v3 offset:2048\n" \
    "ds_read_b128 v[68:71], v3 offset:3072\n.Lq74_%=:\nv_mov_b64 v[16:17], 0\nv_mov_b64 v[18:19], 0\nv_mov_b64 v[20:21], 0\nv_mov_b64 v[22:23], 0\nv_mov_b64 v[24:25], 0\n" \
    "v_mov_b64 v[26:27], 0\nv_mov_b64 v[28:29], 0\nv_mov_b64 v[30:31], 0\ns_waitcnt vmcnt(3) lgkmcnt(8)\nv_smfmac_f32_16x16x64_bf16 v[20:23], v[72:75], v[32:39], v0\n" \
    "ds_write_b128 v1, v[72:75] offset:27648\nv_max3_u32 v252, v72, v73, v74\nv_smfmac_f32_16x16x64_bf16 v[16:19], v[72:75], a[72:79], v0\n" \
    "v_smfmac_f32_16x16x64_bf16 v[24:27], v[72:75], a[80:87], v0\nv_smfmac_f32_16x16x64_bf16 v[28:31], v[72:75], a[88:95], v0\ns_waitcnt vmcnt(2)\n" \
    "ds_write_b128 v1, v[76:79] offset:31744\ns_waitcnt lgkmcnt(8)\nv_max3_u32 v252, v252, v75, v76\nv_smfmac_f32_16x16x64_bf16 v[20:23], v[76:79], v[40:47], v0\n" \
    "v_max3_u32 v252, v252, v77, v78\nv_smfmac_f32_16x16x64_bf16 v[16:19], v[76:79], a[168:175], v0\nv_cmp_u_f32 vcc, v252, v79\n" \
    "v_smfmac_f32_16x16x64_bf16 v[24:27], v[76:79], a[176:183], v0\ns_cbranch_vccnz .Lq81_%=\n.Lq77_%=:\nv_smfmac_f32_16x16x64_bf16 v[28:31], v[76:79], a[184:191], v0\n" \
    "ds_read_b128 v[36:39], v3 offset:5120\ns_waitcnt vmcnt(1)\nv_max3_u32 v252, v80, v81, v82\nv_cmp_u_f32 vcc, v252, v83\ns_nop 0\ns_cbranch_vccnz .Lq82_%=\n.Lq78_%=:\n" \
    "s_waitcnt lgkmcnt(7)\nv_smfmac_f32_16x16x64_bf16 v[20:23], v[80:83], v[48:55], v0\nds_write_b128 v1, v[80:83] offset:35840\n" \
    "v_smfmac_f32_16x16x64_bf16 v[16:19], v[80:83], v[100:107], v0\nds_read_b128 v[32:35], v3 offset:4096\ns_waitcnt lgkmcnt(3)\ns_barrier\n" \
    "ds_read_b128 v[88:91], v1 offset:24576\nv_smfmac_f32_16x16x64_bf16 v[24:27], v[80:83], v[108:115], v0\nds_read_b128 v[40:43], v3 offset:8192\n" \
    "v_smfmac_f32_16x16x64_bf16 v[28:31], v[80:83], v[116:123], v0\nds_read_b128 v[48:51], v1 offset:25600\ns_waitcnt vmcnt(0)\nv_max3_u32 v252, v84, v85, v86\n" \
    "v_cmp_u_f32 vcc, v252, v87\ns_nop 0\ns_cbranch_vccnz .Lq83_%=\n.Lq79_%=:\nv_smfmac_f32_16x16x64_bf16 v[20:23], v[84:87], v[228:235], v0\n" \
    "ds_write_b128 v1, v[84:87] offset:39936\nv_smfmac_f32_16x16x64_bf16 v[16:19], v[84:87], v[220:227], v0\nds_read_b128 v[52:55], v1 offset:26624\n" \
    "v_smfmac_f32_16x16x64_bf16 v[24:27], v[84:87], v[236:243], v0\nds_read_b128 v[72:75], v1 offset:28672\nv_smfmac_f32_16x16x64_bf16 v[28:31], v[84:87], v[244:251], v0\n" \
    "ds_read_b128 v[44:47], v3 offset:9216\ns_waitcnt lgkmcnt(6)\nv_smfmac_f32_16x16x64_bf16 v[20:23], v[88:91], v[56:63], v0\nds_read_b128 v[56:59], v1 offset:29696\n" \
    "v_smfmac_f32_16x16x64_bf16 v[16:19], v[88:91], a[0:7], v0\nds_read_b128 v[76:79], v3 offset:10240\nv_smfmac_f32_16x16x64_bf16 v[24:27], v[88:91], a[8:15], v0\n" \
    "ds_read_b128 v[80:83], v3 offset:11264\nv_smfmac_f32_16x16x64_bf16 v[28:31], v[88:91], a[16:23], v0\nds_read_b128 v[60:63], v1 offset:30720\ns_waitcnt lgkmcnt(7)\n" \
    "s_barrier\nv_smfmac_f32_16x16x64_bf16 v[20:23], v[48:51], v[64:71], v0\nds_read_b128 v[64:67], v3 offset:12288\n" \
    "v_smfmac_f32_16x16x64_bf16 v[16:19], v[48:51], a[24:31], v0\nds_read_b128 v[68:71], v3 offset:13312\ns_waitcnt lgkmcnt(8)\n" \
    "v_smfmac_f32_16x16x64_bf16 v[20:23], v[52:55], v[32:39], v0\nds_read_b128 v[32:35], v1 offset:32768\nv_smfmac_f32_16x16x64_bf16 v[16:19], v[52:55], a[48:55], v0\n" \
    "ds_read_b128 v[84:87], v3 offset:16384\ns_waitcnt lgkmcnt(8)\nv_smfmac_f32_16x16x64_bf16 v[20:23], v[72:75], v[40:47], v0\nds_read_b128 v[88:91], v3 offset:17408\n" \
    "v_smfmac_f32_16x16x64_bf16 v[16:19], v[72:75], a[96:103], v0\nds_read_b128 v[36:39], v1 offset:33792\ns_waitcnt lgkmcnt(7)\n" \
    "v_smfmac_f32_16x16x64_bf16 v[20:23], v[56:59], v[76:83], v0\nds_read_b128 v[40:43], v3 offset:18432\nv_smfmac_f32_16x16x64_bf16 v[16:19], v[56:59], a[120:127], v0\n" \
    "ds_read_b128 v[44:47], v3 offset:19456\ns_waitcnt lgkmcnt(6)\nv_smfmac_f32_16x16x64_bf16 v[20:23], v[60:63], v[64:71], v0\nds_read_b128 v[64:67], v1 offset:34816\n" \
    "v_smfmac_f32_16x16x64_bf16 v[16:19], v[60:63], a[144:151], v0\nds_read_b128 v[76:79], v3 offset:20480\ns_waitcnt lgkmcnt(5)\n" \
    "v_smfmac_f32_16x16x64_bf16 v[20:23], v[32:35], v[84:91], v0\nds_read_b128 v[80:83], v3 offset:21504\nv_smfmac_f32_16x16x64_bf16 v[16:19], v[32:35], a[192:199], v0\n" \
    "ds_read_b128 v[68:71], v1 offset:36864\ns_waitcnt lgkmcnt(4)\nv_smfmac_f32_16x16x64_bf16 v[20:23], v[36:39], v[40:47], v0\nds_read_b128 v[40:43], v1 offset:37888\n" \
    "v_smfmac_f32_16x16x64_bf16 v[16:19], v[36:39], a[216:223], v0\nds_read_b128 v[44:47], v1 offset:38912\ns_waitcnt lgkmcnt(3)\n" \
    "v_smfmac_f32_16x16x64_bf16 v[20:23], v[64:67], v[76:83], v0\nv_smfmac_f32_16x16x64_bf16 v[16:19], v[64:67], a[240:247], v0\ns_waitcnt lgkmcnt(2)\n" \
    "v_smfmac_f32_16x16x64_bf16 v[20:23], v[68:71], v[132:139], v0\ns_waitcnt lgkmcnt(1)\nv_smfmac_f32_16x16x64_bf16 v[20:23], v[40:43], v[164:171], v0\ns_waitcnt lgkmcnt(0)\n" \
    "v_smfmac_f32_16x16x64_bf16 v[20:23], v[44:47], v[196:203], v0\ns_nop 2\nv_smfmac_f32_16x16x64_bf16 v[16:19], v[68:71], v[124:131], v0\ns_nop 2\n" \
    "v_smfmac_f32_16x16x64_bf16 v[16:19], v[40:43], v[156:163], v0\nv_permlane32_swap_b32 v20, v22\nv_smfmac_f32_16x16x64_bf16 v[16:19], v[44:47], v[188:195], v0\ns_nop 1\n" \
    "v_permlane32_swap_b32 v21, v23\nv_smfmac_f32_16x16x64_bf16 v[24:27], v[48:51], a[32:39], v0\ns_nop 1\nv_add_f32 v20, v20, v22\n" \
    "v_smfmac_f32_16x16x64_bf16 v[28:31], v[48:51], a[40:47], v0\nv_add_f32 v21, v21, v23\nv_smfmac_f32_16x16x64_bf16 v[24:27], v[52:55], a[56:63], v0\nv_mul_f32 v20, s32, v20\n" \
    "v_smfmac_f32_16x16x64_bf16 v[28:31], v[52:55], a[64:71], v0\nv_mul_f32 v21, s32, v21\nv_smfmac_f32_16x16x64_bf16 v[24:27], v[72:75], a[104:111], v0\n" \
    "v_cvt_pk_bf16_f32 v20, v20, v21\nv_smfmac_f32_16x16x64_bf16 v[28:31], v[72:75], a[112:119], v0\nds_write_b16 v4, v20 offset:8\nv_permlane32_swap_b32 v16, v18\n" \
    "v_smfmac_f32_16x16x64_bf16 v[24:27], v[56:59], a[128:135], v0\nds_write_b16_d16_hi v4, v20 offset:24\nv_permlane32_swap_b32 v17, v19\n" \
    "v_smfmac_f32_16x16x64_bf16 v[24:27], v[60:63], a[152:159], v0\nds_read_b128 v[48:51], v1 offset:40960\nv_add_f32 v16, v16, v18\n" \
    "v_smfmac_f32_16x16x64_bf16 v[24:27], v[32:35], a[200:207], v0\nds_read_b128 v[52:55], v1 offset:45056\nv_add_f32 v17, v17, v19\n" \
    "v_smfmac_f32_16x16x64_bf16 v[24:27], v[36:39], a[224:231], v0\nds_read_b128 v[72:75], v1 offset:41984\nv_mul_f32 v16, s32, v16\n" \
    "v_smfmac_f32_16x16x64_bf16 v[24:27], v[64:67], a[248:255], v0\nds_read_b128 v[76:79], v1 offset:46080\nv_mul_f32 v17, s32, v17\n" \
    "v_smfmac_f32_16x16x64_bf16 v[24:27], v[68:71], v[140:147], v0\nds_read_b128 v[80:83], v1 offset:43008\nv_cvt_pk_bf16_f32 v16, v16, v17\n" \
    "v_smfmac_f32_16x16x64_bf16 v[24:27], v[40:43], v[172:179], v0\nds_write_b16 v4, v16 offset:0\nv_smfmac_f32_16x16x64_bf16 v[24:27], v[44:47], v[204:211], v0\n" \
    "ds_write_b16_d16_hi v4, v16 offset:16\ns_nop 1\nv_smfmac_f32_16x16x64_bf16 v[28:31], v[56:59], a[136:143], v0\nds_read_b128 v[56:59], v1 offset:47104\ns_nop 1\n" \
    "v_smfmac_f32_16x16x64_bf16 v[28:31], v[60:63], a[160:167], v0\nds_read_b128 v[60:63], v1 offset:44032\nv_permlane32_swap_b32 v24, v26\n" \
    "v_smfmac_f32_16x16x64_bf16 v[28:31], v[32:35], a[208:215], v0\nds_read_b128 v[32:35], v1 offset:48128\nv_permlane32_swap_b32 v25, v27\n" \
    "v_smfmac_f32_16x16x64_bf16 v[28:31], v[36:39], a[232:239], v0\nds_read_b128 v[36:39], v6 offset:0\nv_add_f32 v24, v24, v26\n" \
    "v_smfmac_f32_16x16x64_bf16 v[28:31], v[64:67], v[92:99], v0\nds_read_b128 v[64:67], v6 offset:512\nv_add_f32 v25, v25, v27\n" \
    "v_smfmac_f32_16x16x64_bf16 v[28:31], v[68:71], v[148:155], v0\nds_read_b128 v[68:71], v6 offset:1024\nv_cvt_pk_bf16_f32 v24, v24, v25\n" \
    "v_smfmac_f32_16x16x64_bf16 v[28:31], v[40:43], v[180:187], v0\nds_read_b128 v[40:43], v6 offset:1536\nv_smfmac_f32_16x16x64_bf16 v[28:31], v[44:47], v[212:219], v0\n" \
    "ds_write_b16 v4, v24 offset:2048\nds_write_b16_d16_hi v4, v24 offset:2064\ns_waitcnt lgkmcnt(5)\nv_mfma_f32_16x16x32_bf16 v[16:19], v[48:51], v[36:39], 0\ns_nop 2\n" \
    "v_mfma_f32_16x16x32_bf16 v[20:23], v[52:55], v[36:39], 0\nv_permlane32_swap_b32 v28, v30\nv_permlane32_swap_b32 v29, v31\nv_add_f32 v28, v28, v30\nv_add_f32 v29, v29, v31\n" \
    "v_cvt_pk_bf16_f32 v28, v28, v29\nds_write_b16 v4, v28 offset:2056\nds_write_b16_d16_hi v4, v28 offset:2072\ns_waitcnt lgkmcnt(6)\n" \
    "v_mfma_f32_16x16x32_bf16 v[16:19], v[72:75], v[64:67], v[16:19]\ns_cmp_eq_u32 s35, 63\ns_cbranch_scc1 .Lq8_%=\n" \
    "v_mfma_f32_16x16x32_bf16 v[20:23], v[76:79], v[64:67], v[20:23]\ns_waitcnt lgkmcnt(5)\nv_mfma_f32_16x16x32_bf16 v[16:19], v[80:83], v[68:71], v[16:19]\n" \
    "ds_read_b128 v[44:47], v8 offset:0\nds_read_b128 v[84:87], v8 offset:512\nv_mfma_f32_16x16x32_bf16 v[20:23], v[56:59], v[68:71], v[20:23]\n" \
    "ds_read_b128 v[88:91], v8 offset:1024\nds_read_b128 v[28:31], v8 offset:1536\ns_waitcnt lgkmcnt(8)\nv_mfma_f32_16x16x32_bf16 v[16:19], v[60:63], v[40:43], v[16:19]\n" \
    "v_mfma_f32_16x16x32_bf16 v[20:23], v[32:35], v[40:43], v[20:23]\nv_mov_b32 v15, v15\nv_mov_b32 v15, v15\ns_waitcnt lgkmcnt(3)\n" \
    "v_mfma_f32_16x16x32_bf16 v[24:27], v[44:47], v[36:39], 0\ns_nop 2\ns_waitcnt lgkmcnt(2)\nv_mov_b32_dpp v16, v20 quad_perm:[0,1,2,3] row_mask:0xf bank_mask:0xc\n" \
    "v_mov_b32_dpp v17, v21 quad_perm:[0,1,2,3] row_mask:0xf bank_mask:0xc\nv_mfma_f32_16x16x32_bf16 v[24:27], v[84:87], v[64:67], v[24:27]\ns_waitcnt lgkmcnt(1)\n" \
    "v_mov_b32_dpp v18, v22 quad_perm:[0,1,2,3] row_mask:0xf bank_mask:0xc\nv_mov_b32_dpp v19, v23 quad_perm:[0,1,2,3] row_mask:0xf bank_mask:0xc\n" \
    "v_mfma_f32_16x16x32_bf16 v[24:27], v[88:91], v[68:71], v[24:27]\ns_waitcnt lgkmcnt(0)\nv_max3_f32 v254, v16, v17, v18\nv_max_f32 v254, v254, v19\n" \
    "v_mfma_f32_16x16x32_bf16 v[24:27], v[28:31], v[40:43], v[24:27]\ns_nop 0\nv_max_f32_dpp v252, v254, v254 row_ror:8 row_mask:0xf bank_mask:0xf\n" \
    "v_max_f32_dpp v253, v254, v254 row_ror:8 row_mask:0xf bank_mask:0xf\ns_nop 1\nv_permlane32_swap_b32 v252, v253\nv_max_f32 v252, v252, v253\ns_xor_b32 s74, s82, 0x4000\n" \
    "v_max3_f32 v254, v252, v24, v25\nv_max3_f32 v252, v254, v26, v27\nv_max3_f32 v253, v254, v26, v27\ns_nop 1\nv_permlane16_swap_b32 v252, v253\nv_max_f32 v252, v252, v253\n" \
    "v_pk_add_f32 v[16:17], v[16:17], v[252:253] op_sel_hi:[1,0] neg_lo:[0,1] neg_hi:[0,1]\n" \
    "v_pk_add_f32 v[18:19], v[18:19], v[252:253] op_sel_hi:[1,0] neg_lo:[0,1] neg_hi:[0,1]\n" \
    "v_pk_add_f32 v[24:25], v[24:25], v[252:253] op_sel_hi:[1,0] neg_lo:[0,1] neg_hi:[0,1]\n" \
    "v_pk_add_f32 v[26:27], v[26:27], v[252:253] op_sel_hi:[1,0] neg_lo:[0,1] neg_hi:[0,1]\nv_exp_f32 v16, v16\nv_exp_f32 v17, v17\nv_exp_f32 v18, v18\nv_exp_f32 v19, v19\n" \
    "v_exp_f32 v24, v24\nv_pk_add_f32 v[254:255], v[16:17], v[18:19]\nv_exp_f32 v25, v25\nv_add_f32 v254, v254, v255\nv_exp_f32 v26, v26\nv_exp_f32 v27, v27\n" \
    "v_add_f32_dpp v252, v254, v254 row_ror:8 row_mask:0xf bank_mask:0xf\nv_add_f32_dpp v253, v254, v254 row_ror:8 row_mask:0xf bank_mask:0xf\n" \
    "v_pk_add_f32 v[254:255], v[24:25], v[26:27]\ns_nop 0\nv_permlane32_swap_b32 v252, v253\nv_pk_add_f32 v[254:255], v[254:255], v[252:253]\n" \
    "v_pk_add_f32 v[252:253], v[254:255], v[254:255] op_sel:[0,1] op_sel_hi:[1,0]\ns_nop 1\nv_permlane16_swap_b32 v252, v253\nv_add_f32 v252, v252, v253\n" \
    "v_cmp_u_f32 vcc, v252, v252\nv_rcp_f32 v252, v252\ns_cbranch_vccnz .Lq12_%=\nv_pk_mul_f32 v[16:17], v[16:17], v[252:253] op_sel_hi:[1,0]\n" \
    "v_pk_mul_f32 v[24:25], v[24:25], v[252:253] op_sel_hi:[1,0]\nv_cvt_pk_bf16_f32 v20, v16, v17\nv_cvt_pk_bf16_f32 v22, v24, v25\n" \
    "v_pk_mul_f32 v[18:19], v[18:19], v[252:253] op_sel_hi:[1,0]\nv_pk_mul_f32 v[26:27], v[26:27], v[252:253] op_sel_hi:[1,0]\nv_cvt_pk_bf16_f32 v21, v18, v19\n" \
    "v_cvt_pk_bf16_f32 v23, v26, v27\nv_permlane32_swap_b32 v20, v22\ns_nop 0\nv_permlane32_swap_b32 v21, v23\nbuffer_store_dwordx4 v[20:23], v7, s[64:67], s82 offen sc1\n" \
    "buffer_store_dwordx4 v[20:23], v7, s[64:67], s74 offen sc1\n.Lq3_%=:\nds_read_b128 v[36:39], v5 offset:0\nds_read_b128 v[64:67], v5 offset:1024\n" \
    "ds_read_b128 v[68:71], v5 offset:2048\nds_read_b128 v[40:43], v5 offset:3072\ns_mov_b32 s40, s84\ns_add_u32 s35, s35, 2\ns_barrier\ns_memrealtime s[72:73]\n" \
    "s_waitcnt lgkmcnt(0)\n.Lq91_%=:\nv_mfma_f32_16x16x32_bf16 v[16:19], v[48:51], v[36:39], 0\nv_mfma_f32_16x16x32_bf16 v[20:23], v[52:55], v[36:39], 0\n" \
    "v_mfma_f32_16x16x32_bf16 v[16:19], v[72:75], v[64:67], v[16:19]\nv_mfma_f32_16x16x32_bf16 v[20:23], v[76:79], v[64:67], v[20:23]\n" \
    "v_mfma_f32_16x16x32_bf16 v[16:19], v[80:83], v[68:71], v[16:19]\nv_mfma_f32_16x16x32_bf16 v[20:23], v[56:59], v[68:71], v[20:23]\n" \
    "v_mfma_f32_16x16x32_bf16 v[16:19], v[60:63], v[40:43], v[16:19]\nv_mfma_f32_16x16x32_bf16 v[20:23], v[32:35], v[40:43], v[20:23]\n" \
    "v_mfma_f32_16x16x32_bf16 v[24:27], v[44:47], v[36:39], 0\nv_mfma_f32_16x16x32_bf16 v[24:27], v[84:87], v[64:67], v[24:27]\n" \
    "v_mfma_f32_16x16x32_bf16 v[24:27], v[88:91], v[68:71], v[24:27]\nv_mfma_f32_16x16x32_bf16 v[24:27], v[28:31], v[40:43], v[24:27]\ns_cmp_lt_u32 s35, 62\n" \
    "s_cbranch_scc1 .Lq2_%=\ns_nop 1\nv_mov_b32_dpp v16, v20 quad_perm:[0,1,2,3] row_mask:0xf bank_mask:0xc\n" \
    "v_mov_b32_dpp v17, v21 quad_perm:[0,1,2,3] row_mask:0xf bank_mask:0xc\nv_mov_b32_dpp v18, v22 quad_perm:[0,1,2,3] row_mask:0xf bank_mask:0xc\n" \
    "v_mov_b32_dpp v19, v23 quad_perm:[0,1,2,3] row_mask:0xf bank_mask:0xc\nv_mov_b32 v254, 0xff800000\nv_cvt_pk_bf16_f32 v16, 0, v16\nv_cvt_pk_bf16_f32 v17, 0, v17\n" \
    "v_cvt_pk_bf16_f32 v18, 0, v18\nv_cvt_pk_bf16_f32 v19, 0, v19\nv_mul_f32 v16, s33, v16\nv_mul_f32 v17, s33, v17\nv_mul_f32 v18, s33, v18\nv_mul_f32 v19, s33, v19\n" \
    "v_cvt_pk_bf16_f32 v16, 0, v16\nv_cvt_pk_bf16_f32 v17, 0, v17\nv_cvt_pk_bf16_f32 v18, 0, v18\nv_cvt_pk_bf16_f32 v19, 0, v19\nv_cndmask_b32 v16, v254, v16, s[50:51]\n" \
    "v_cndmask_b32 v17, v254, v17, s[52:53]\nv_cndmask_b32 v18, v254, v18, s[54:55]\nv_cndmask_b32 v19, v254, v19, s[56:57]\nv_max3_f32 v254, v16, v17, v18\n" \
    "v_max_f32 v254, v254, v19\ns_nop 1\nv_max_f32_dpp v252, v254, v254 row_ror:8 row_mask:0xf bank_mask:0xf\n" \
    "v_max_f32_dpp v253, v254, v254 row_ror:8 row_mask:0xf bank_mask:0xf\ns_nop 1\nv_permlane32_swap_b32 v252, v253\nv_max_f32 v252, v252, v253\nv_cvt_pk_bf16_f32 v24, 0, v24\n" \
    "v_cvt_pk_bf16_f32 v25, 0, v25\nv_cvt_pk_bf16_f32 v26, 0, v26\nv_cvt_pk_bf16_f32 v27, 0, v27\nv_mul_f32 v24, s33, v24\nv_mul_f32 v25, s33, v25\nv_mul_f32 v26, s33, v26\n" \
    "v_mul_f32 v27, s33, v27\nv_cvt_pk_bf16_f32 v24, 0, v24\nv_cvt_pk_bf16_f32 v25, 0, v25\nv_cvt_pk_bf16_f32 v26, 0, v26\nv_cvt_pk_bf16_f32 v27, 0, v27\n" \
    "v_max3_f32 v254, v252, v24, v25\nv_max3_f32 v252, v254, v26, v27\nv_max3_f32 v253, v254, v26, v27\ns_nop 1\nv_permlane16_swap_b32 v252, v253\nv_max_f32 v252, v252, v253\n" \
    "v_pk_add_f32 v[16:17], v[16:17], v[252:253] op_sel_hi:[1,0] neg_lo:[0,1] neg_hi:[0,1]\n" \
    "v_pk_add_f32 v[18:19], v[18:19], v[252:253] op_sel_hi:[1,0] neg_lo:[0,1] neg_hi:[0,1]\n" \
    "v_pk_add_f32 v[24:25], v[24:25], v[252:253] op_sel_hi:[1,0] neg_lo:[0,1] neg_hi:[0,1]\n" \
    "v_pk_add_f32 v[26:27], v[26:27], v[252:253] op_sel_hi:[1,0] neg_lo:[0,1] neg_hi:[0,1]\nv_mul_f32 v16, s34, v16\nv_mul_f32 v17, s34, v17\nv_mul_f32 v18, s34, v18\n" \
    "v_mul_f32 v19, s34, v19\nv_mul_f32 v24, s34, v24\nv_mul_f32 v25, s34, v25\nv_mul_f32 v26, s34, v26\nv_mul_f32 v27, s34, v27\nv_exp_f32 v16, v16\nv_exp_f32 v17, v17\n" \
    "v_exp_f32 v18, v18\nv_exp_f32 v19, v19\nv_exp_f32 v24, v24\nv_pk_add_f32 v[254:255], v[16:17], v[18:19]\nv_exp_f32 v25, v25\nv_add_f32 v254, v254, v255\n" \
    "v_exp_f32 v26, v26\nv_exp_f32 v27, v27\nv_add_f32_dpp v252, v254, v254 row_ror:8 row_mask:0xf bank_mask:0xf\n" \
    "v_add_f32_dpp v253, v254, v254 row_ror:8 row_mask:0xf bank_mask:0xf\nv_pk_add_f32 v[254:255], v[24:25], v[26:27]\ns_nop 0\nv_permlane32_swap_b32 v252, v253\n" \
    "v_pk_add_f32 v[254:255], v[254:255], v[252:253]\nv_pk_add_f32 v[252:253], v[254:255], v[254:255] op_sel:[0,1] op_sel_hi:[1,0]\ns_nop 1\nv_permlane16_swap_b32 v252, v253\n" \
    "v_add_f32 v252, v252, v253\nv_rcp_f32 v252, v252\ns_nop 0\nv_pk_mul_f32 v[16:17], v[16:17], v[252:253] op_sel_hi:[1,0]\n" \
    "v_pk_mul_f32 v[24:25], v[24:25], v[252:253] op_sel_hi:[1,0]\nv_cvt_pk_bf16_f32 v20, v16, v17\nv_cvt_pk_bf16_f32 v22, v24, v25\n" \
    "v_pk_mul_f32 v[18:19], v[18:19], v[252:253] op_sel_hi:[1,0]\nv_pk_mul_f32 v[26:27], v[26:27], v[252:253] op_sel_hi:[1,0]\nv_cvt_pk_bf16_f32 v21, v18, v19\n" \
    "v_cvt_pk_bf16_f32 v23, v26, v27\nv_permlane32_swap_b32 v20, v22\ns_nop 0\nv_permlane32_swap_b32 v21, v23\nv_writelane_b32 v20, s18, 40\ns_xor_b32 s74, s83, 0x2000\n" \
    "buffer_store_dwordx4 v[20:23], v7, s[64:67], s83 offen sc1\nbuffer_store_dwordx4 v[20:23], v7, s[64:67], s74 offen sc1\ns_mov_b32 s83, 0x7fff0000\ns_cmp_lt_u32 s35, 64\n" \
    "s_cbranch_scc1 .Lq2_%=\ns_cmp_lt_u32 s19, 64\ns_cbranch_scc1 .Lq9_%=\ns_endpgm\n.Lq8_%=:\ns_mov_b32 s35, 65\nds_read_b128 v[36:39], v5 offset:0\n" \
    "ds_read_b128 v[64:67], v5 offset:1024\nds_read_b128 v[68:71], v5 offset:2048\nds_read_b128 v[40:43], v5 offset:3072\ns_waitcnt lgkmcnt(0)\ns_barrier\n" \
    "ds_read_b128 v[44:47], v8 offset:0\nds_read_b128 v[84:87], v8 offset:512\nds_read_b128 v[88:91], v8 offset:1024\nds_read_b128 v[28:31], v8 offset:1536\n" \
    "s_waitcnt lgkmcnt(0)\ns_branch .Lq91_%=\n.Lq11_%=:\ns_sleep 6\nbuffer_load_dword v13, off, s[64:67], s42 sc1\ns_waitcnt vmcnt(0)\nv_readfirstlane_b32 s74, v13\ns_nop 0\n" \
    "s_and_b32 s75, s74, 127\ns_lshr_b32 s69, s74, 8\ns_sub_u32 s69, s69, s80\ns_bfe_i32 s69, s69, 0x180000\ns_cmp_eq_u32 s75, 0\ns_cselect_b32 s69, 0, s69\n" \
    "s_add_u32 s80, s80, s69\ns_lshl_b32 s74, s74, 5\ns_and_b32 s74, s74, 0x1000\ns_andn2_b32 s81, s81, 0x1000\ns_or_b32 s81, s81, s74\ns_lshl_b32 s74, s74, 2\n" \
    "s_andn2_b32 s82, s82, 0x4000\ns_or_b32 s82, s82, s74\ns_add_u32 s68, s68, 1\ns_and_b32 s69, s68, 63\ns_cbranch_scc1 .Lq10_%=\ns_cmp_gt_u32 s68, 0x400000\n" \
    "s_cbranch_scc1 .Lq1_%=\nbuffer_load_dword v253, off, s[64:67], 0 sc1\ns_waitcnt vmcnt(0)\nv_readfirstlane_b32 s69, v253\ns_nop 0\ns_cmp_eq_u32 s69, 1\n" \
    "s_cbranch_scc0 .Lq10_%=\ns_endpgm\n.Lq16_%=:\ns_memrealtime s[72:73]\ns_waitcnt lgkmcnt(0)\ns_sub_u32 s72, s72, s76\ns_cmp_lt_i32 s72, 0\ns_cbranch_scc1 .Lq16_%=\n" \
    "buffer_load_dword v13, off, s[64:67], s42 sc1\ns_waitcnt vmcnt(0)\nv_readfirstlane_b32 s74, v13\ns_nop 0\ns_and_b32 s75, s74, 127\ns_lshr_b32 s69, s74, 8\n" \
    "s_sub_u32 s69, s69, s80\ns_bfe_i32 s69, s69, 0x180000\ns_cmp_eq_u32 s75, 0\ns_cselect_b32 s69, 0, s69\ns_add_u32 s80, s80, s69\ns_lshl_b32 s74, s74, 5\n" \
    "s_and_b32 s74, s74, 0x1000\ns_andn2_b32 s81, s81, 0x1000\ns_or_b32 s81, s81, s74\ns_lshl_b32 s74, s74, 2\ns_andn2_b32 s82, s82, 0x4000\ns_or_b32 s82, s82, s74\n" \
    "s_branch .Lq13_%=\n.Lq24_%=:\nv_mov_b32 v252, 0\nv_max3_u32 v252, v252, v72, v73\nv_max3_u32 v252, v252, v74, v75\nv_max3_u32 v252, v252, v76, v77\n" \
    "v_max3_u32 v252, v252, v78, v79\nv_cmp_eq_u32 vcc, -1, v252\ns_nop 0\ns_cbranch_vccz .Lq20_%=\ns_add_u32 s80, s80, s43\ns_xor_b32 s81, s81, 0x1000\n" \
    "s_xor_b32 s90, s90, 0x1000\ns_xor_b32 s82, s82, 0x4000\ns_cmp_eq_u32 s61, 0\ns_cbranch_scc1 .Lq27_%=\ns_lshr_b32 s69, s81, 5\ns_and_b32 s69, s69, 128\n" \
    "s_lshl_b32 s74, s80, 8\ns_add_u32 s74, s74, s69\ns_add_u32 s74, s74, s35\nv_mov_b32 v253, s74\nv_lshlrev_b32 v252, 7, v2\ns_mov_b64 exec, 1\n" \
    "buffer_store_dword v253, v252, s[64:67], s61 offen sc1\ns_mov_b64 exec, -1\n.Lq27_%=:\ns_add_u32 s68, s68, 1\ns_and_b32 s69, s68, 63\ns_cbranch_scc1 .Lq14_%=\n" \
    "s_cmp_gt_u32 s68, 0x400000\ns_cbranch_scc1 .Lq1_%=\nbuffer_load_dword v253, off, s[64:67], 0 sc1\ns_waitcnt vmcnt(0)\nv_readfirstlane_b32 s69, v253\ns_nop 0\n" \
    "s_cmp_eq_u32 s69, 1\ns_cbranch_scc0 .Lq14_%=\ns_endpgm\n.Lq14_%=:\ns_memrealtime s[72:73]\ns_waitcnt lgkmcnt(0)\ns_sub_u32 s72, s72, s80\ns_cmp_lt_i32 s72, 0\n" \
    "s_cbranch_scc1 .Lq14_%=\nbuffer_load_dwordx4 v[72:75], v2, s[64:67], s81 offen offset:0 sc1\ns_nop 7\n" \
    "buffer_load_dwordx4 v[76:79], v2, s[64:67], s81 offen offset:1024 sc1\ns_nop 7\nbuffer_load_dwordx4 v[80:83], v2, s[64:67], s90 offen offset:0 sc1\ns_nop 7\n" \
    "buffer_load_dwordx4 v[84:87], v2, s[64:67], s90 offen offset:1024 sc1\ns_nop 7\ns_branch .Lq17_%=\n.Lq25_%=:\nv_mov_b32 v252, 0\nv_max3_u32 v252, v252, v80, v81\n" \
    "v_max3_u32 v252, v252, v82, v83\nv_cmp_eq_u32 vcc, -1, v252\ns_nop 0\ns_cbranch_vccz .Lq21_%=\ns_add_u32 s80, s80, s43\ns_xor_b32 s81, s81, 0x1000\n" \
    "s_xor_b32 s90, s90, 0x1000\ns_xor_b32 s82, s82, 0x4000\ns_cmp_eq_u32 s61, 0\ns_cbranch_scc1 .Lq29_%=\ns_lshr_b32 s69, s81, 5\ns_and_b32 s69, s69, 128\n" \
    "s_lshl_b32 s74, s80, 8\ns_add_u32 s74, s74, s69\ns_add_u32 s74, s74, s35\nv_mov_b32 v253, s74\nv_lshlrev_b32 v252, 7, v2\ns_mov_b64 exec, 1\n" \
    "buffer_store_dword v253, v252, s[64:67], s61 offen sc1\ns_mov_b64 exec, -1\n.Lq29_%=:\n.Lq28_%=:\ns_memrealtime s[72:73]\ns_waitcnt vmcnt(0) lgkmcnt(0)\n" \
    "s_sub_u32 s72, s72, s80\ns_cmp_lt_i32 s72, 0\ns_cbranch_scc1 .Lq28_%=\nbuffer_load_dwordx4 v[80:83], v2, s[64:67], s90 offen offset:0 sc1\n" \
    "buffer_load_dwordx4 v[84:87], v2, s[64:67], s90 offen offset:1024 sc1\ns_waitcnt vmcnt(0)\ns_add_u32 s68, s68, 1\ns_and_b32 s69, s68, 63\ns_cbranch_scc1 .Lq25_%=\n" \
    "s_cmp_gt_u32 s68, 0x400000\ns_cbranch_scc1 .Lq1_%=\nbuffer_load_dword v253, off, s[64:67], 0 sc1\ns_waitcnt vmcnt(0)\nv_readfirstlane_b32 s69, v253\ns_nop 0\n" \
    "s_cmp_eq_u32 s69, 1\ns_cbranch_scc0 .Lq25_%=\ns_endpgm\n.Lq26_%=:\nv_mov_b32 v252, 0\nv_max3_u32 v252, v252, v84, v85\nv_max3_u32 v252, v252, v86, v87\n" \
    "v_cmp_eq_u32 vcc, -1, v252\ns_nop 0\ns_cbranch_vccz .Lq22_%=\ns_add_u32 s80, s80, s43\ns_xor_b32 s81, s81, 0x1000\ns_xor_b32 s90, s90, 0x1000\ns_xor_b32 s82, s82, 0x4000\n" \
    "s_cmp_eq_u32 s61, 0\ns_cbranch_scc1 .Lq31_%=\ns_lshr_b32 s69, s81, 5\ns_and_b32 s69, s69, 128\ns_lshl_b32 s74, s80, 8\ns_add_u32 s74, s74, s69\ns_add_u32 s74, s74, s35\n" \
    "v_mov_b32 v253, s74\nv_lshlrev_b32 v252, 7, v2\ns_mov_b64 exec, 1\nbuffer_store_dword v253, v252, s[64:67], s61 offen sc1\ns_mov_b64 exec, -1\n.Lq31_%=:\n.Lq30_%=:\n" \
    "s_memrealtime s[72:73]\ns_waitcnt vmcnt(0) lgkmcnt(0)\ns_sub_u32 s72, s72, s80\ns_cmp_lt_i32 s72, 0\ns_cbranch_scc1 .Lq30_%=\n" \
    "buffer_load_dwordx4 v[84:87], v2, s[64:67], s90 offen offset:1024 sc1\ns_waitcnt vmcnt(0)\ns_add_u32 s68, s68, 1\ns_and_b32 s69, s68, 63\ns_cbranch_scc1 .Lq26_%=\n" \
    "s_cmp_gt_u32 s68, 0x400000\ns_cbranch_scc1 .Lq1_%=\nbuffer_load_dword v253, off, s[64:67], 0 sc1\ns_waitcnt vmcnt(0)\nv_readfirstlane_b32 s69, v253\ns_nop 0\n" \
    "s_cmp_eq_u32 s69, 1\ns_cbranch_scc0 .Lq26_%=\ns_endpgm\n.Lq35_%=:\ns_memrealtime s[72:73]\ns_waitcnt lgkmcnt(0)\ns_sub_u32 s72, s72, s76\ns_cmp_lt_i32 s72, 0\n" \
    "s_cbranch_scc1 .Lq35_%=\nbuffer_load_dword v13, off, s[64:67], s42 sc1\ns_waitcnt vmcnt(0)\nv_readfirstlane_b32 s74, v13\ns_nop 0\ns_and_b32 s75, s74, 127\n" \
    "s_lshr_b32 s69, s74, 8\ns_sub_u32 s69, s69, s80\ns_bfe_i32 s69, s69, 0x180000\ns_cmp_eq_u32 s75, 0\ns_cselect_b32 s69, 0, s69\ns_add_u32 s80, s80, s69\n" \
    "s_lshl_b32 s74, s74, 5\ns_and_b32 s74, s74, 0x1000\ns_andn2_b32 s81, s81, 0x1000\ns_or_b32 s81, s81, s74\ns_lshl_b32 s74, s74, 2\ns_andn2_b32 s82, s82, 0x4000\n" \
    "s_or_b32 s82, s82, s74\ns_branch .Lq32_%=\n.Lq43_%=:\nv_mov_b32 v252, 0\nv_max3_u32 v252, v252, v72, v73\nv_max3_u32 v252, v252, v74, v75\nv_max3_u32 v252, v252, v76, v77\n" \
    "v_max3_u32 v252, v252, v78, v79\nv_cmp_eq_u32 vcc, -1, v252\ns_nop 0\ns_cbranch_vccz .Lq39_%=\ns_add_u32 s80, s80, s43\ns_xor_b32 s81, s81, 0x1000\n" \
    "s_xor_b32 s90, s90, 0x1000\ns_xor_b32 s82, s82, 0x4000\ns_cmp_eq_u32 s61, 0\ns_cbranch_scc1 .Lq46_%=\ns_lshr_b32 s69, s81, 5\ns_and_b32 s69, s69, 128\n" \
    "s_lshl_b32 s74, s80, 8\ns_add_u32 s74, s74, s69\ns_add_u32 s74, s74, s35\nv_mov_b32 v253, s74\nv_lshlrev_b32 v252, 7, v2\ns_mov_b64 exec, 1\n" \
    "buffer_store_dword v253, v252, s[64:67], s61 offen sc1\ns_mov_b64 exec, -1\n.Lq46_%=:\ns_add_u32 s68, s68, 1\ns_and_b32 s69, s68, 63\ns_cbranch_scc1 .Lq33_%=\n" \
    "s_cmp_gt_u32 s68, 0x400000\ns_cbranch_scc1 .Lq1_%=\nbuffer_load_dword v253, off, s[64:67], 0 sc1\ns_waitcnt vmcnt(0)\nv_readfirstlane_b32 s69, v253\ns_nop 0\n" \
    "s_cmp_eq_u32 s69, 1\ns_cbranch_scc0 .Lq33_%=\ns_endpgm\n.Lq33_%=:\ns_memrealtime s[72:73]\ns_waitcnt lgkmcnt(0)\ns_sub_u32 s72, s72, s80\ns_cmp_lt_i32 s72, 0\n" \
    "s_cbranch_scc1 .Lq33_%=\nbuffer_load_dwordx4 v[72:75], v2, s[64:67], s81 offen offset:0 sc1\ns_nop 7\n" \
    "buffer_load_dwordx4 v[76:79], v2, s[64:67], s81 offen offset:1024 sc1\ns_nop 7\nbuffer_load_dwordx4 v[80:83], v2, s[64:67], s90 offen offset:0 sc1\ns_nop 7\n" \
    "buffer_load_dwordx4 v[84:87], v2, s[64:67], s90 offen offset:1024 sc1\ns_nop 7\ns_branch .Lq36_%=\n.Lq44_%=:\nv_mov_b32 v252, 0\nv_max3_u32 v252, v252, v80, v81\n" \
    "v_max3_u32 v252, v252, v82, v83\nv_cmp_eq_u32 vcc, -1, v252\ns_nop 0\ns_cbranch_vccz .Lq40_%=\ns_add_u32 s80, s80, s43\ns_xor_b32 s81, s81, 0x1000\n" \
    "s_xor_b32 s90, s90, 0x1000\ns_xor_b32 s82, s82, 0x4000\ns_cmp_eq_u32 s61, 0\ns_cbranch_scc1 .Lq48_%=\ns_lshr_b32 s69, s81, 5\ns_and_b32 s69, s69, 128\n" \
    "s_lshl_b32 s74, s80, 8\ns_add_u32 s74, s74, s69\ns_add_u32 s74, s74, s35\nv_mov_b32 v253, s74\nv_lshlrev_b32 v252, 7, v2\ns_mov_b64 exec, 1\n" \
    "buffer_store_dword v253, v252, s[64:67], s61 offen sc1\ns_mov_b64 exec, -1\n.Lq48_%=:\n.Lq47_%=:\ns_memrealtime s[72:73]\ns_waitcnt vmcnt(0) lgkmcnt(0)\n" \
    "s_sub_u32 s72, s72, s80\ns_cmp_lt_i32 s72, 0\ns_cbranch_scc1 .Lq47_%=\nbuffer_load_dwordx4 v[80:83], v2, s[64:67], s90 offen offset:0 sc1\n" \
    "buffer_load_dwordx4 v[84:87], v2, s[64:67], s90 offen offset:1024 sc1\ns_waitcnt vmcnt(0)\ns_add_u32 s68, s68, 1\ns_and_b32 s69, s68, 63\ns_cbranch_scc1 .Lq44_%=\n" \
    "s_cmp_gt_u32 s68, 0x400000\ns_cbranch_scc1 .Lq1_%=\nbuffer_load_dword v253, off, s[64:67], 0 sc1\ns_waitcnt vmcnt(0)\nv_readfirstlane_b32 s69, v253\ns_nop 0\n" \
    "s_cmp_eq_u32 s69, 1\ns_cbranch_scc0 .Lq44_%=\ns_endpgm\n.Lq45_%=:\nv_mov_b32 v252, 0\nv_max3_u32 v252, v252, v84, v85\nv_max3_u32 v252, v252, v86, v87\n" \
    "v_cmp_eq_u32 vcc, -1, v252\ns_nop 0\ns_cbranch_vccz .Lq41_%=\ns_add_u32 s80, s80, s43\ns_xor_b32 s81, s81, 0x1000\ns_xor_b32 s90, s90, 0x1000\ns_xor_b32 s82, s82, 0x4000\n" \
    "s_cmp_eq_u32 s61, 0\ns_cbranch_scc1 .Lq50_%=\ns_lshr_b32 s69, s81, 5\ns_and_b32 s69, s69, 128\ns_lshl_b32 s74, s80, 8\ns_add_u32 s74, s74, s69\ns_add_u32 s74, s74, s35\n" \
    "v_mov_b32 v253, s74\nv_lshlrev_b32 v252, 7, v2\ns_mov_b64 exec, 1\nbuffer_store_dword v253, v252, s[64:67], s61 offen sc1\ns_mov_b64 exec, -1\n.Lq50_%=:\n.Lq49_%=:\n" \
    "s_memrealtime s[72:73]\ns_waitcnt vmcnt(0) lgkmcnt(0)\ns_sub_u32 s72, s72, s80\ns_cmp_lt_i32 s72, 0\ns_cbranch_scc1 .Lq49_%=\n" \
    "buffer_load_dwordx4 v[84:87], v2, s[64:67], s90 offen offset:1024 sc1\ns_waitcnt vmcnt(0)\ns_add_u32 s68, s68, 1\ns_and_b32 s69, s68, 63\ns_cbranch_scc1 .Lq45_%=\n" \
    "s_cmp_gt_u32 s68, 0x400000\ns_cbranch_scc1 .Lq1_%=\nbuffer_load_dword v253, off, s[64:67], 0 sc1\ns_waitcnt vmcnt(0)\nv_readfirstlane_b32 s69, v253\ns_nop 0\n" \
    "s_cmp_eq_u32 s69, 1\ns_cbranch_scc0 .Lq45_%=\ns_endpgm\n.Lq54_%=:\ns_memrealtime s[72:73]\ns_waitcnt lgkmcnt(0)\ns_sub_u32 s72, s72, s76\ns_cmp_lt_i32 s72, 0\n" \
    "s_cbranch_scc1 .Lq54_%=\nbuffer_load_dword v13, off, s[64:67], s42 sc1\ns_waitcnt vmcnt(0)\nv_readfirstlane_b32 s74, v13\ns_nop 0\ns_and_b32 s75, s74, 127\n" \
    "s_lshr_b32 s69, s74, 8\ns_sub_u32 s69, s69, s80\ns_bfe_i32 s69, s69, 0x180000\ns_cmp_eq_u32 s75, 0\ns_cselect_b32 s69, 0, s69\ns_add_u32 s80, s80, s69\n" \
    "s_lshl_b32 s74, s74, 5\ns_and_b32 s74, s74, 0x1000\ns_andn2_b32 s81, s81, 0x1000\ns_or_b32 s81, s81, s74\ns_lshl_b32 s74, s74, 2\ns_andn2_b32 s82, s82, 0x4000\n" \
    "s_or_b32 s82, s82, s74\ns_branch .Lq51_%=\n.Lq62_%=:\nv_mov_b32 v252, 0\nv_max3_u32 v252, v252, v72, v73\nv_max3_u32 v252, v252, v74, v75\nv_max3_u32 v252, v252, v76, v77\n" \
    "v_max3_u32 v252, v252, v78, v79\nv_cmp_eq_u32 vcc, -1, v252\ns_nop 0\ns_cbranch_vccz .Lq58_%=\ns_add_u32 s80, s80, s43\ns_xor_b32 s81, s81, 0x1000\n" \
    "s_xor_b32 s90, s90, 0x1000\ns_xor_b32 s82, s82, 0x4000\ns_cmp_eq_u32 s61, 0\ns_cbranch_scc1 .Lq65_%=\ns_lshr_b32 s69, s81, 5\ns_and_b32 s69, s69, 128\n" \
    "s_lshl_b32 s74, s80, 8\ns_add_u32 s74, s74, s69\ns_add_u32 s74, s74, s35\nv_mov_b32 v253, s74\nv_lshlrev_b32 v252, 7, v2\ns_mov_b64 exec, 1\n" \
    "buffer_store_dword v253, v252, s[64:67], s61 offen sc1\ns_mov_b64 exec, -1\n.Lq65_%=:\ns_add_u32 s68, s68, 1\ns_and_b32 s69, s68, 63\ns_cbranch_scc1 .Lq52_%=\n" \
    "s_cmp_gt_u32 s68, 0x400000\ns_cbranch_scc1 .Lq1_%=\nbuffer_load_dword v253, off, s[64:67], 0 sc1\ns_waitcnt vmcnt(0)\nv_readfirstlane_b32 s69, v253\ns_nop 0\n" \
    "s_cmp_eq_u32 s69, 1\ns_cbranch_scc0 .Lq52_%=\ns_endpgm\n.Lq52_%=:\ns_memrealtime s[72:73]\ns_waitcnt lgkmcnt(0)\ns_sub_u32 s72, s72, s80\ns_cmp_lt_i32 s72, 0\n" \
    "s_cbranch_scc1 .Lq52_%=\nbuffer_load_dwordx4 v[72:75], v2, s[64:67], s81 offen offset:0 sc1\ns_nop 7\n" \
    "buffer_load_dwordx4 v[76:79], v2, s[64:67], s81 offen offset:1024 sc1\ns_nop 7\nbuffer_load_dwordx4 v[80:83], v2, s[64:67], s90 offen offset:0 sc1\ns_nop 7\n" \
    "buffer_load_dwordx4 v[84:87], v2, s[64:67], s90 offen offset:1024 sc1\ns_nop 7\ns_branch .Lq55_%=\n.Lq63_%=:\nv_mov_b32 v252, 0\nv_max3_u32 v252, v252, v80, v81\n" \
    "v_max3_u32 v252, v252, v82, v83\nv_cmp_eq_u32 vcc, -1, v252\ns_nop 0\ns_cbranch_vccz .Lq59_%=\ns_add_u32 s80, s80, s43\ns_xor_b32 s81, s81, 0x1000\n" \
    "s_xor_b32 s90, s90, 0x1000\ns_xor_b32 s82, s82, 0x4000\ns_cmp_eq_u32 s61, 0\ns_cbranch_scc1 .Lq67_%=\ns_lshr_b32 s69, s81, 5\ns_and_b32 s69, s69, 128\n" \
    "s_lshl_b32 s74, s80, 8\ns_add_u32 s74, s74, s69\ns_add_u32 s74, s74, s35\nv_mov_b32 v253, s74\nv_lshlrev_b32 v252, 7, v2\ns_mov_b64 exec, 1\n" \
    "buffer_store_dword v253, v252, s[64:67], s61 offen sc1\ns_mov_b64 exec, -1\n.Lq67_%=:\n.Lq66_%=:\ns_memrealtime s[72:73]\ns_waitcnt vmcnt(0) lgkmcnt(0)\n" \
    "s_sub_u32 s72, s72, s80\ns_cmp_lt_i32 s72, 0\ns_cbranch_scc1 .Lq66_%=\nbuffer_load_dwordx4 v[80:83], v2, s[64:67], s90 offen offset:0 sc1\n" \
    "buffer_load_dwordx4 v[84:87], v2, s[64:67], s90 offen offset:1024 sc1\ns_waitcnt vmcnt(0)\ns_add_u32 s68, s68, 1\ns_and_b32 s69, s68, 63\ns_cbranch_scc1 .Lq63_%=\n" \
    "s_cmp_gt_u32 s68, 0x400000\ns_cbranch_scc1 .Lq1_%=\nbuffer_load_dword v253, off, s[64:67], 0 sc1\ns_waitcnt vmcnt(0)\nv_readfirstlane_b32 s69, v253\ns_nop 0\n" \
    "s_cmp_eq_u32 s69, 1\ns_cbranch_scc0 .Lq63_%=\ns_endpgm\n.Lq64_%=:\nv_mov_b32 v252, 0\nv_max3_u32 v252, v252, v84, v85\nv_max3_u32 v252, v252, v86, v87\n" \
    "v_cmp_eq_u32 vcc, -1, v252\ns_nop 0\ns_cbranch_vccz .Lq60_%=\ns_add_u32 s80, s80, s43\ns_xor_b32 s81, s81, 0x1000\ns_xor_b32 s90, s90, 0x1000\ns_xor_b32 s82, s82, 0x4000\n" \
    "s_cmp_eq_u32 s61, 0\ns_cbranch_scc1 .Lq69_%=\ns_lshr_b32 s69, s81, 5\ns_and_b32 s69, s69, 128\ns_lshl_b32 s74, s80, 8\ns_add_u32 s74, s74, s69\ns_add_u32 s74, s74, s35\n" \
    "v_mov_b32 v253, s74\nv_lshlrev_b32 v252, 7, v2\ns_mov_b64 exec, 1\nbuffer_store_dword v253, v252, s[64:67], s61 offen sc1\ns_mov_b64 exec, -1\n.Lq69_%=:\n.Lq68_%=:\n" \
    "s_memrealtime s[72:73]\ns_waitcnt vmcnt(0) lgkmcnt(0)\ns_sub_u32 s72, s72, s80\ns_cmp_lt_i32 s72, 0\ns_cbranch_scc1 .Lq68_%=\n" \
    "buffer_load_dwordx4 v[84:87], v2, s[64:67], s90 offen offset:1024 sc1\ns_waitcnt vmcnt(0)\ns_add_u32 s68, s68, 1\ns_and_b32 s69, s68, 63\ns_cbranch_scc1 .Lq64_%=\n" \
    "s_cmp_gt_u32 s68, 0x400000\ns_cbranch_scc1 .Lq1_%=\nbuffer_load_dword v253, off, s[64:67], 0 sc1\ns_waitcnt vmcnt(0)\nv_readfirstlane_b32 s69, v253\ns_nop 0\n" \
    "s_cmp_eq_u32 s69, 1\ns_cbranch_scc0 .Lq64_%=\ns_endpgm\n.Lq73_%=:\ns_memrealtime s[72:73]\ns_waitcnt lgkmcnt(0)\ns_sub_u32 s72, s72, s76\ns_cmp_lt_i32 s72, 0\n" \
    "s_cbranch_scc1 .Lq73_%=\nbuffer_load_dword v13, off, s[64:67], s42 sc1\ns_waitcnt vmcnt(0)\nv_readfirstlane_b32 s74, v13\ns_nop 0\ns_and_b32 s75, s74, 127\n" \
    "s_lshr_b32 s69, s74, 8\ns_sub_u32 s69, s69, s80\ns_bfe_i32 s69, s69, 0x180000\ns_cmp_eq_u32 s75, 0\ns_cselect_b32 s69, 0, s69\ns_add_u32 s80, s80, s69\n" \
    "s_lshl_b32 s74, s74, 5\ns_and_b32 s74, s74, 0x1000\ns_andn2_b32 s81, s81, 0x1000\ns_or_b32 s81, s81, s74\ns_lshl_b32 s74, s74, 2\ns_andn2_b32 s82, s82, 0x4000\n" \
    "s_or_b32 s82, s82, s74\ns_branch .Lq70_%=\n.Lq81_%=:\nv_mov_b32 v252, 0\nv_max3_u32 v252, v252, v72, v73\nv_max3_u32 v252, v252, v74, v75\nv_max3_u32 v252, v252, v76, v77\n" \
    "v_max3_u32 v252, v252, v78, v79\nv_cmp_eq_u32 vcc, -1, v252\ns_nop 0\ns_cbranch_vccz .Lq77_%=\ns_add_u32 s80, s80, s43\ns_xor_b32 s81, s81, 0x1000\n" \
    "s_xor_b32 s90, s90, 0x1000\ns_xor_b32 s82, s82, 0x4000\ns_cmp_eq_u32 s61, 0\ns_cbranch_scc1 .Lq84_%=\ns_lshr_b32 s69, s81, 5\ns_and_b32 s69, s69, 128\n" \
    "s_lshl_b32 s74, s80, 8\ns_add_u32 s74, s74, s69\ns_add_u32 s74, s74, s35\nv_mov_b32 v253, s74\nv_lshlrev_b32 v252, 7, v2\ns_mov_b64 exec, 1\n" \
    "buffer_store_dword v253, v252, s[64:67], s61 offen sc1\ns_mov_b64 exec, -1\n.Lq84_%=:\ns_add_u32 s68, s68, 1\ns_and_b32 s69, s68, 63\ns_cbranch_scc1 .Lq71_%=\n" \
    "s_cmp_gt_u32 s68, 0x400000\ns_cbranch_scc1 .Lq1_%=\nbuffer_load_dword v253, off, s[64:67], 0 sc1\ns_waitcnt vmcnt(0)\nv_readfirstlane_b32 s69, v253\ns_nop 0\n" \
    "s_cmp_eq_u32 s69, 1\ns_cbranch_scc0 .Lq71_%=\ns_endpgm\n.Lq71_%=:\ns_memrealtime s[72:73]\ns_waitcnt lgkmcnt(0)\ns_sub_u32 s72, s72, s80\ns_cmp_lt_i32 s72, 0\n" \
    "s_cbranch_scc1 .Lq71_%=\nbuffer_load_dwordx4 v[72:75], v2, s[64:67], s81 offen offset:0 sc1\ns_nop 7\n" \
    "buffer_load_dwordx4 v[76:79], v2, s[64:67], s81 offen offset:1024 sc1\ns_nop 7\nbuffer_load_dwordx4 v[80:83], v2, s[64:67], s90 offen offset:0 sc1\ns_nop 7\n" \
    "buffer_load_dwordx4 v[84:87], v2, s[64:67], s90 offen offset:1024 sc1\ns_nop 7\ns_branch .Lq74_%=\n.Lq82_%=:\nv_mov_b32 v252, 0\nv_max3_u32 v252, v252, v80, v81\n" \
    "v_max3_u32 v252, v252, v82, v83\nv_cmp_eq_u32 vcc, -1, v252\ns_nop 0\ns_cbranch_vccz .Lq78_%=\ns_add_u32 s80, s80, s43\ns_xor_b32 s81, s81, 0x1000\n" \
    "s_xor_b32 s90, s90, 0x1000\ns_xor_b32 s82, s82, 0x4000\ns_cmp_eq_u32 s61, 0\ns_cbranch_scc1 .Lq86_%=\ns_lshr_b32 s69, s81, 5\ns_and_b32 s69, s69, 128\n" \
    "s_lshl_b32 s74, s80, 8\ns_add_u32 s74, s74, s69\ns_add_u32 s74, s74, s35\nv_mov_b32 v253, s74\nv_lshlrev_b32 v252, 7, v2\ns_mov_b64 exec, 1\n" \
    "buffer_store_dword v253, v252, s[64:67], s61 offen sc1\ns_mov_b64 exec, -1\n.Lq86_%=:\n.Lq85_%=:\ns_memrealtime s[72:73]\ns_waitcnt vmcnt(0) lgkmcnt(0)\n" \
    "s_sub_u32 s72, s72, s80\ns_cmp_lt_i32 s72, 0\ns_cbranch_scc1 .Lq85_%=\nbuffer_load_dwordx4 v[80:83], v2, s[64:67], s90 offen offset:0 sc1\n" \
    "buffer_load_dwordx4 v[84:87], v2, s[64:67], s90 offen offset:1024 sc1\ns_waitcnt vmcnt(0)\ns_add_u32 s68, s68, 1\ns_and_b32 s69, s68, 63\ns_cbranch_scc1 .Lq82_%=\n" \
    "s_cmp_gt_u32 s68, 0x400000\ns_cbranch_scc1 .Lq1_%=\nbuffer_load_dword v253, off, s[64:67], 0 sc1\ns_waitcnt vmcnt(0)\nv_readfirstlane_b32 s69, v253\ns_nop 0\n" \
    "s_cmp_eq_u32 s69, 1\ns_cbranch_scc0 .Lq82_%=\ns_endpgm\n.Lq83_%=:\nv_mov_b32 v252, 0\nv_max3_u32 v252, v252, v84, v85\nv_max3_u32 v252, v252, v86, v87\n" \
    "v_cmp_eq_u32 vcc, -1, v252\ns_nop 0\ns_cbranch_vccz .Lq79_%=\ns_add_u32 s80, s80, s43\ns_xor_b32 s81, s81, 0x1000\ns_xor_b32 s90, s90, 0x1000\ns_xor_b32 s82, s82, 0x4000\n" \
    "s_cmp_eq_u32 s61, 0\ns_cbranch_scc1 .Lq88_%=\ns_lshr_b32 s69, s81, 5\ns_and_b32 s69, s69, 128\ns_lshl_b32 s74, s80, 8\ns_add_u32 s74, s74, s69\ns_add_u32 s74, s74, s35\n" \
    "v_mov_b32 v253, s74\nv_lshlrev_b32 v252, 7, v2\ns_mov_b64 exec, 1\nbuffer_store_dword v253, v252, s[64:67], s61 offen sc1\ns_mov_b64 exec, -1\n.Lq88_%=:\n.Lq87_%=:\n" \
    "s_memrealtime s[72:73]\ns_waitcnt vmcnt(0) lgkmcnt(0)\ns_sub_u32 s72, s72, s80\ns_cmp_lt_i32 s72, 0\ns_cbranch_scc1 .Lq87_%=\n" \
    "buffer_load_dwordx4 v[84:87], v2, s[64:67], s90 offen offset:1024 sc1\ns_waitcnt vmcnt(0)\ns_add_u32 s68, s68, 1\ns_and_b32 s69, s68, 63\ns_cbranch_scc1 .Lq83_%=\n" \
    "s_cmp_gt_u32 s68, 0x400000\ns_cbranch_scc1 .Lq1_%=\nbuffer_load_dword v253, off, s[64:67], 0 sc1\ns_waitcnt vmcnt(0)\nv_readfirstlane_b32 s69, v253\ns_nop 0\n" \
    "s_cmp_eq_u32 s69, 1\ns_cbranch_scc0 .Lq83_%=\ns_endpgm\n.Lq12_%=:\ns_mov_b32 s68, 0\n.Lq89_%=:\nds_read_b128 v[36:39], v6 offset:0\nds_read_b128 v[44:47], v8 offset:0\n" \
    "ds_read_b128 v[64:67], v6 offset:512\nds_read_b128 v[84:87], v8 offset:512\nds_read_b128 v[68:71], v6 offset:1024\nds_read_b128 v[88:91], v8 offset:1024\n" \
    "ds_read_b128 v[40:43], v6 offset:1536\nds_read_b128 v[28:31], v8 offset:1536\nv_mov_b32 v252, 0\ns_waitcnt lgkmcnt(0)\nv_pk_max_u16 v252, v252, v36\n" \
    "v_pk_max_u16 v252, v252, v37\nv_pk_max_u16 v252, v252, v38\nv_pk_max_u16 v252, v252, v39\nv_pk_max_u16 v252, v252, v64\nv_pk_max_u16 v252, v252, v65\n" \
    "v_pk_max_u16 v252, v252, v66\nv_pk_max_u16 v252, v252, v67\nv_pk_max_u16 v252, v252, v68\nv_pk_max_u16 v252, v252, v69\nv_pk_max_u16 v252, v252, v70\n" \
    "v_pk_max_u16 v252, v252, v71\nv_pk_max_u16 v252, v252, v40\nv_pk_max_u16 v252, v252, v41\nv_pk_max_u16 v252, v252, v42\nv_pk_max_u16 v252, v252, v43\n" \
    "v_pk_max_u16 v252, v252, v44\nv_pk_max_u16 v252, v252, v45\nv_pk_max_u16 v252, v252, v46\nv_pk_max_u16 v252, v252, v47\nv_pk_max_u16 v252, v252, v84\n" \
    "v_pk_max_u16 v252, v252, v85\nv_pk_max_u16 v252, v252, v86\nv_pk_max_u16 v252, v252, v87\nv_pk_max_u16 v252, v252, v88\nv_pk_max_u16 v252, v252, v89\n" \
    "v_pk_max_u16 v252, v252, v90\nv_pk_max_u16 v252, v252, v91\nv_pk_max_u16 v252, v252, v28\nv_pk_max_u16 v252, v252, v29\nv_pk_max_u16 v252, v252, v30\n" \
    "v_pk_max_u16 v252, v252, v31\nv_pk_max_u16 v252, v252, v252 op_sel:[0,1] op_sel_hi:[1,0]\nv_cmp_eq_u32 vcc, -1, v252\ns_nop 0\ns_cbranch_vccnz .Lq90_%=\n" \
    "v_mfma_f32_16x16x32_bf16 v[16:19], v[48:51], v[36:39], 0\nv_mfma_f32_16x16x32_bf16 v[20:23], v[52:55], v[36:39], 0\n" \
    "v_mfma_f32_16x16x32_bf16 v[16:19], v[72:75], v[64:67], v[16:19]\nv_mfma_f32_16x16x32_bf16 v[20:23], v[76:79], v[64:67], v[20:23]\n" \
    "v_mfma_f32_16x16x32_bf16 v[16:19], v[80:83], v[68:71], v[16:19]\nv_mfma_f32_16x16x32_bf16 v[20:23], v[56:59], v[68:71], v[20:23]\n" \
    "v_mfma_f32_16x16x32_bf16 v[16:19], v[60:63], v[40:43], v[16:19]\nv_mfma_f32_16x16x32_bf16 v[20:23], v[32:35], v[40:43], v[20:23]\n" \
    "v_mfma_f32_16x16x32_bf16 v[24:27], v[44:47], v[36:39], 0\nv_mfma_f32_16x16x32_bf16 v[24:27], v[84:87], v[64:67], v[24:27]\n" \
    "v_mfma_f32_16x16x32_bf16 v[24:27], v[88:91], v[68:71], v[24:27]\nv_mfma_f32_16x16x32_bf16 v[24:27], v[28:31], v[40:43], v[24:27]\ns_nop 3\n" \
    "v_mov_b32_dpp v16, v20 quad_perm:[0,1,2,3] row_mask:0xf bank_mask:0xc\nv_mov_b32_dpp v17, v21 quad_perm:[0,1,2,3] row_mask:0xf bank_mask:0xc\n" \
    "v_mov_b32_dpp v18, v22 quad_perm:[0,1,2,3] row_mask:0xf bank_mask:0xc\nv_mov_b32_dpp v19, v23 quad_perm:[0,1,2,3] row_mask:0xf bank_mask:0xc\n" \
    "v_max3_f32 v254, v16, v17, v18\nv_max_f32 v254, v254, v19\ns_nop 1\nv_max_f32_dpp v252, v254, v254 row_ror:8 row_mask:0xf bank_mask:0xf\n" \
    "v_max_f32_dpp v253, v254, v254 row_ror:8 row_mask:0xf bank_mask:0xf\ns_nop 1\nv_permlane32_swap_b32 v252, v253\nv_max_f32 v252, v252, v253\n" \
    "v_max3_f32 v254, v252, v24, v25\nv_max3_f32 v252, v254, v26, v27\nv_max3_f32 v253, v254, v26, v27\ns_nop 1\nv_permlane16_swap_b32 v252, v253\nv_max_f32 v252, v252, v253\n" \
    "v_pk_add_f32 v[16:17], v[16:17], v[252:253] op_sel_hi:[1,0] neg_lo:[0,1] neg_hi:[0,1]\n" \
    "v_pk_add_f32 v[18:19], v[18:19], v[252:253] op_sel_hi:[1,0] neg_lo:[0,1] neg_hi:[0,1]\n" \
    "v_pk_add_f32 v[24:25], v[24:25], v[252:253] op_sel_hi:[1,0] neg_lo:[0,1] neg_hi:[0,1]\n" \
    "v_pk_add_f32 v[26:27], v[26:27], v[252:253] op_sel_hi:[1,0] neg_lo:[0,1] neg_hi:[0,1]\nv_exp_f32 v16, v16\nv_exp_f32 v17, v17\nv_exp_f32 v18, v18\nv_exp_f32 v19, v19\n" \
    "v_exp_f32 v24, v24\nv_pk_add_f32 v[254:255], v[16:17], v[18:19]\nv_exp_f32 v25, v25\nv_add_f32 v254, v254, v255\nv_exp_f32 v26, v26\nv_exp_f32 v27, v27\n" \
    "v_add_f32_dpp v252, v254, v254 row_ror:8 row_mask:0xf bank_mask:0xf\nv_add_f32_dpp v253, v254, v254 row_ror:8 row_mask:0xf bank_mask:0xf\n" \
    "v_pk_add_f32 v[254:255], v[24:25], v[26:27]\ns_nop 0\nv_permlane32_swap_b32 v252, v253\nv_pk_add_f32 v[254:255], v[254:255], v[252:253]\n" \
    "v_pk_add_f32 v[252:253], v[254:255], v[254:255] op_sel:[0,1] op_sel_hi:[1,0]\ns_nop 1\nv_permlane16_swap_b32 v252, v253\nv_add_f32 v252, v252, v253\nv_rcp_f32 v252, v252\n" \
    "s_nop 0\nv_pk_mul_f32 v[16:17], v[16:17], v[252:253] op_sel_hi:[1,0]\nv_pk_mul_f32 v[24:25], v[24:25], v[252:253] op_sel_hi:[1,0]\nv_cvt_pk_bf16_f32 v20, v16, v17\n" \
    "v_cvt_pk_bf16_f32 v22, v24, v25\nv_pk_mul_f32 v[18:19], v[18:19], v[252:253] op_sel_hi:[1,0]\nv_pk_mul_f32 v[26:27], v[26:27], v[252:253] op_sel_hi:[1,0]\n" \
    "v_cvt_pk_bf16_f32 v21, v18, v19\nv_cvt_pk_bf16_f32 v23, v26, v27\nv_permlane32_swap_b32 v20, v22\ns_nop 0\nv_permlane32_swap_b32 v21, v23\n" \
    "buffer_store_dwordx4 v[20:23], v7, s[64:67], s82 offen sc1\nbuffer_store_dwordx4 v[20:23], v7, s[64:67], s74 offen sc1\ns_branch .Lq3_%=\n.Lq90_%=:\ns_add_u32 s68, s68, 1\n" \
    "s_and_b32 s69, s68, 63\ns_cbranch_scc1 .Lq89_%=\ns_cmp_gt_u32 s68, 0x400000\ns_cbranch_scc1 .Lq1_%=\nbuffer_load_dword v253, off, s[64:67], 0 sc1\ns_waitcnt vmcnt(0)\n" \
    "v_readfirstlane_b32 s69, v253\ns_nop 0\ns_cmp_eq_u32 s69, 1\ns_cbranch_scc0 .Lq89_%=\ns_endpgm\n.Lq1_%=:\nv_mov_b32 v253, 1\nbuffer_store_dword v253, off, s[64:67], 0 sc1\n" \
    "s_endpgm\n"
#define ASM_V \
    "s_add_u32 m0, s41, 0\ns_xor_b32 s85, s62, 0\ns_add_u32 s85, s85, s40\nbuffer_load_dwordx4 v11, s[64:67], s85 offen sc1 lds\n" \
    "buffer_load_dwordx4 v12, s[64:67], s85 offen offset:1024 sc1 lds\ns_add_u32 m0, s41, 8192\ns_xor_b32 s85, s62, 256\ns_add_u32 s85, s85, s40\n" \
    "buffer_load_dwordx4 v11, s[64:67], s85 offen sc1 lds\nbuffer_load_dwordx4 v12, s[64:67], s85 offen offset:1024 sc1 lds\ns_add_u32 m0, s41, 16384\ns_xor_b32 s85, s62, 512\n" \
    "s_add_u32 s85, s85, s40\nbuffer_load_dwordx4 v11, s[64:67], s85 offen sc1 lds\nbuffer_load_dwordx4 v12, s[64:67], s85 offen offset:1024 sc1 lds\ns_add_u32 m0, s41, 40960\n" \
    "s_xor_b32 s85, s62, 768\ns_add_u32 s85, s85, s40\nbuffer_load_dwordx4 v11, s[64:67], s85 offen sc1 lds\nbuffer_load_dwordx4 v12, s[64:67], s85 offen offset:1024 sc1 lds\n" \
    "s_memrealtime s[72:73]\ns_waitcnt lgkmcnt(0)\ns_mov_b32 s80, s72\ns_waitcnt vmcnt(0)\ns_barrier\nds_read_b128 v[56:59], v3 offset:0\nds_read_b128 v[60:63], v9 offset:0\n" \
    "ds_read_b128 v[64:67], v9 offset:4096\nds_read_b128 v[68:71], v3 offset:1024\nds_read_b128 v[72:75], v10 offset:0\nds_read_b128 v[76:79], v10 offset:4096\n" \
    "s_waitcnt lgkmcnt(3)\nv_mfma_f32_16x16x32_bf16 v[16:19], a[0:3], v[60:63], 0\ns_add_u32 m0, s41, 24576\ns_xor_b32 s85, s62, 1024\n" \
    "v_mfma_f32_16x16x32_bf16 v[24:27], v[56:59], v[60:63], 0\nds_read_b128 v[60:63], v3 offset:2048\ns_add_u32 s85, s85, s40\n" \
    "v_mfma_f32_16x16x32_bf16 v[20:23], a[0:3], v[64:67], 0\nds_read_b128 v[80:83], v9 offset:1024\nbuffer_load_dwordx4 v11, s[64:67], s85 offen sc1 lds\n" \
    "v_mfma_f32_16x16x32_bf16 v[28:31], v[56:59], v[64:67], 0\nds_read_b128 v[56:59], v9 offset:5120\ns_waitcnt lgkmcnt(3)\n" \
    "v_mfma_f32_16x16x32_bf16 v[16:19], a[4:7], v[72:75], v[16:19]\nv_mfma_f32_16x16x32_bf16 v[24:27], v[68:71], v[72:75], v[24:27]\nds_read_b128 v[64:67], v3 offset:3072\n" \
    "buffer_load_dwordx4 v12, s[64:67], s85 offen offset:1024 sc1 lds\nv_mfma_f32_16x16x32_bf16 v[20:23], a[4:7], v[76:79], v[20:23]\nds_read_b128 v[72:75], v10 offset:1024\n" \
    "s_add_u32 m0, s41, 32768\nv_mfma_f32_16x16x32_bf16 v[28:31], v[68:71], v[76:79], v[28:31]\nds_read_b128 v[68:71], v10 offset:5120\ns_xor_b32 s85, s62, 1280\n" \
    "s_waitcnt lgkmcnt(3)\nv_mfma_f32_16x16x32_bf16 v[16:19], a[8:11], v[80:83], v[16:19]\ns_add_u32 s85, s85, s40\nbuffer_load_dwordx4 v11, s[64:67], s85 offen sc1 lds\n" \
    "v_mfma_f32_16x16x32_bf16 v[24:27], v[60:63], v[80:83], v[24:27]\nds_read_b128 v[76:79], v3 offset:4096\nv_mfma_f32_16x16x32_bf16 v[20:23], a[8:11], v[56:59], v[20:23]\n" \
    "ds_read_b128 v[80:83], v9 offset:8192\nv_mfma_f32_16x16x32_bf16 v[28:31], v[60:63], v[56:59], v[28:31]\nds_read_b128 v[56:59], v9 offset:12288\n" \
    "buffer_load_dwordx4 v12, s[64:67], s85 offen offset:1024 sc1 lds\ns_waitcnt lgkmcnt(3)\nv_mfma_f32_16x16x32_bf16 v[16:19], a[12:15], v[72:75], v[16:19]\n" \
    "s_add_u32 s84, s40, 0x20000\ns_cmp_lt_u32 s49, 62\ns_cselect_b32 s84, s84, s40\nv_mfma_f32_16x16x32_bf16 v[24:27], v[64:67], v[72:75], v[24:27]\n" \
    "ds_read_b128 v[60:63], v3 offset:5120\nv_mov_b64 v[252:253], -1\nv_mov_b64 v[254:255], -1\nv_mfma_f32_16x16x32_bf16 v[20:23], a[12:15], v[68:71], v[20:23]\n" \
    "ds_read_b128 v[72:75], v10 offset:8192\ns_and_b32 s69, s49, 15\nv_mfma_f32_16x16x32_bf16 v[28:31], v[64:67], v[68:71], v[28:31]\nds_read_b128 v[64:67], v10 offset:12288\n" \
    "s_lshl_b32 s69, s69, 19\ns_waitcnt lgkmcnt(3)\nv_mfma_f32_16x16x32_bf16 v[16:19], a[16:19], v[80:83], v[16:19]\ns_add_u32 s82, s38, s69\ns_lshr_b32 s69, s82, 8\n" \
    "v_mfma_f32_16x16x32_bf16 v[24:27], v[76:79], v[80:83], v[24:27]\nds_read_b128 v[68:71], v3 offset:6144\ns_and_b32 s69, s69, 0x2000\n" \
    "v_mfma_f32_16x16x32_bf16 v[20:23], a[16:19], v[56:59], v[20:23]\nds_read_b128 v[80:83], v9 offset:9216\ns_xor_b32 s82, s82, s69\n" \
    "v_mfma_f32_16x16x32_bf16 v[28:31], v[76:79], v[56:59], v[28:31]\nds_read_b128 v[56:59], v9 offset:13312\ns_and_b32 s69, s35, 15\ns_waitcnt lgkmcnt(3)\n" \
    "v_mfma_f32_16x16x32_bf16 v[16:19], a[20:23], v[72:75], v[16:19]\ns_lshl_b32 s69, s69, 19\ns_add_u32 s0, s38, s69\n" \
    "v_mfma_f32_16x16x32_bf16 v[24:27], v[60:63], v[72:75], v[24:27]\nds_read_b128 v[72:75], v3 offset:7168\ns_lshr_b32 s69, s0, 8\n" \
    "v_mfma_f32_16x16x32_bf16 v[20:23], a[20:23], v[64:67], v[20:23]\nds_read_b128 v[76:79], v10 offset:9216\ns_and_b32 s69, s69, 0x2000\n" \
    "v_mfma_f32_16x16x32_bf16 v[28:31], v[60:63], v[64:67], v[28:31]\nds_read_b128 v[60:63], v10 offset:13312\ns_xor_b32 s0, s0, s69\ns_waitcnt lgkmcnt(3)\n" \
    "v_mfma_f32_16x16x32_bf16 v[16:19], a[24:27], v[80:83], v[16:19]\ns_xor_b32 s1, s0, 0x10000\ns_xor_b32 s2, s0, 0x20000\n" \
    "v_mfma_f32_16x16x32_bf16 v[24:27], v[68:71], v[80:83], v[24:27]\nds_read_b128 v[64:67], v3 offset:8192\ns_xor_b32 s3, s0, 0x30000\nv_lshrrev_b32 v15, 1, v2\n" \
    "v_mfma_f32_16x16x32_bf16 v[20:23], a[24:27], v[56:59], v[20:23]\nds_read_b128 v[80:83], v9 offset:16384\nv_add_u32 v15, s60, v15\n" \
    "v_mfma_f32_16x16x32_bf16 v[28:31], v[68:71], v[56:59], v[28:31]\nds_read_b128 v[56:59], v9 offset:20480\ns_waitcnt lgkmcnt(3)\n" \
    "v_mfma_f32_16x16x32_bf16 v[16:19], a[28:31], v[76:79], v[16:19]\nds_write_b64 v15, v[252:253]\nv_mfma_f32_16x16x32_bf16 v[24:27], v[72:75], v[76:79], v[24:27]\n" \
    "ds_read_b128 v[68:71], v3 offset:9216\nv_mfma_f32_16x16x32_bf16 v[20:23], a[28:31], v[60:63], v[20:23]\nds_read_b128 v[76:79], v10 offset:16384\n" \
    "v_mfma_f32_16x16x32_bf16 v[28:31], v[72:75], v[60:63], v[28:31]\nds_read_b128 v[60:63], v10 offset:20480\ns_waitcnt lgkmcnt(4)\n" \
    "v_mfma_f32_16x16x32_bf16 v[16:19], a[32:35], v[80:83], v[16:19]\nv_mfma_f32_16x16x32_bf16 v[24:27], v[64:67], v[80:83], v[24:27]\nds_read_b128 v[72:75], v3 offset:10240\n" \
    "v_mfma_f32_16x16x32_bf16 v[20:23], a[32:35], v[56:59], v[20:23]\nds_read_b128 v[80:83], v9 offset:17408\nbuffer_load_dword v13, off, s[64:67], s42 sc1\n" \
    "v_mfma_f32_16x16x32_bf16 v[28:31], v[64:67], v[56:59], v[28:31]\nds_read_b128 v[56:59], v9 offset:21504\ns_and_b32 s69, s49, 255\ns_waitcnt lgkmcnt(3)\n" \
    "v_mfma_f32_16x16x32_bf16 v[16:19], a[36:39], v[76:79], v[16:19]\ns_lshl_b32 s69, s69, 19\ns_add_u32 s74, s63, s69\n" \
    "v_mfma_f32_16x16x32_bf16 v[24:27], v[68:71], v[76:79], v[24:27]\nds_read_b128 v[64:67], v3 offset:11264\ns_cmp_lt_u32 s49, 64\ns_cselect_b32 s74, s74, 0x7fff0000\n" \
    "v_mfma_f32_16x16x32_bf16 v[20:23], a[36:39], v[60:63], v[20:23]\nds_read_b128 v[76:79], v10 offset:17408\nv_mfma_f32_16x16x32_bf16 v[28:31], v[68:71], v[60:63], v[28:31]\n" \
    "ds_read_b128 v[60:63], v10 offset:21504\ns_waitcnt lgkmcnt(3)\nv_mfma_f32_16x16x32_bf16 v[16:19], a[40:43], v[80:83], v[16:19]\n" \
    "v_mfma_f32_16x16x32_bf16 v[24:27], v[72:75], v[80:83], v[24:27]\nds_read_b128 v[68:71], v3 offset:12288\ns_barrier\n" \
    "v_mfma_f32_16x16x32_bf16 v[20:23], a[40:43], v[56:59], v[20:23]\nds_read_b128 v[80:83], v9 offset:40960\nv_mfma_f32_16x16x32_bf16 v[28:31], v[72:75], v[56:59], v[28:31]\n" \
    "ds_read_b128 v[56:59], v9 offset:45056\ns_add_u32 m0, s41, 0\ns_waitcnt lgkmcnt(3)\nv_mfma_f32_16x16x32_bf16 v[16:19], a[44:47], v[76:79], v[16:19]\n" \
    "s_xor_b32 s85, s62, 1536\ns_add_u32 s85, s85, s40\nv_mfma_f32_16x16x32_bf16 v[24:27], v[64:67], v[76:79], v[24:27]\nds_read_b128 v[72:75], v3 offset:13312\n" \
    "buffer_load_dwordx4 v11, s[64:67], s85 offen sc1 lds\nv_mfma_f32_16x16x32_bf16 v[20:23], a[44:47], v[60:63], v[20:23]\nds_read_b128 v[76:79], v10 offset:40960\n" \
    "buffer_store_dwordx4 v[252:255], v2, s[64:67], s74 offen offset:0 sc1\nv_mfma_f32_16x16x32_bf16 v[28:31], v[64:67], v[60:63], v[28:31]\n" \
    "ds_read_b128 v[60:63], v10 offset:45056\ns_waitcnt lgkmcnt(3)\nv_mfma_f32_16x16x32_bf16 v[16:19], a[48:51], v[80:83], v[16:19]\n" \
    "buffer_load_dwordx4 v12, s[64:67], s85 offen offset:1024 sc1 lds\ns_add_u32 m0, s41, 8192\nv_mfma_f32_16x16x32_bf16 v[24:27], v[68:71], v[80:83], v[24:27]\n" \
    "ds_read_b128 v[64:67], v3 offset:14336\ns_xor_b32 s85, s62, 1792\nv_mfma_f32_16x16x32_bf16 v[20:23], a[48:51], v[56:59], v[20:23]\nds_read_b128 v[80:83], v9 offset:41984\n" \
    "s_add_u32 s85, s85, s40\nv_mfma_f32_16x16x32_bf16 v[28:31], v[68:71], v[56:59], v[28:31]\nds_read_b128 v[56:59], v9 offset:46080\n" \
    "buffer_load_dwordx4 v11, s[64:67], s85 offen sc1 lds\ns_waitcnt lgkmcnt(3)\nv_mfma_f32_16x16x32_bf16 v[16:19], a[52:55], v[76:79], v[16:19]\n" \
    "buffer_store_dwordx4 v[252:255], v2, s[64:67], s74 offen offset:1024 sc1\nv_mfma_f32_16x16x32_bf16 v[24:27], v[72:75], v[76:79], v[24:27]\n" \
    "ds_read_b128 v[68:71], v3 offset:15360\nv_mfma_f32_16x16x32_bf16 v[20:23], a[52:55], v[60:63], v[20:23]\nds_read_b128 v[76:79], v10 offset:41984\n" \
    "buffer_load_dwordx4 v12, s[64:67], s85 offen offset:1024 sc1 lds\nv_mfma_f32_16x16x32_bf16 v[28:31], v[72:75], v[60:63], v[28:31]\nds_read_b128 v[60:63], v10 offset:46080\n" \
    "s_waitcnt lgkmcnt(3)\nv_mfma_f32_16x16x32_bf16 v[16:19], a[56:59], v[80:83], v[16:19]\nbuffer_store_dwordx4 v[252:255], v7, s[64:67], s82 offen sc1\n" \
    "s_xor_b32 s82, s82, 0x10000\nv_mfma_f32_16x16x32_bf16 v[24:27], v[64:67], v[80:83], v[24:27]\nds_read_b128 v[72:75], v3 offset:16384\ns_waitcnt vmcnt(8)\ns_barrier\n" \
    "v_mfma_f32_16x16x32_bf16 v[20:23], a[56:59], v[56:59], v[20:23]\nds_read_b128 v[80:83], v9 offset:24576\nv_mfma_f32_16x16x32_bf16 v[28:31], v[64:67], v[56:59], v[28:31]\n" \
    "ds_read_b128 v[56:59], v9 offset:28672\ns_waitcnt lgkmcnt(3)\nv_mfma_f32_16x16x32_bf16 v[16:19], a[60:63], v[76:79], v[16:19]\n" \
    "buffer_store_dwordx4 v[252:255], v7, s[64:67], s82 offen sc1\ns_xor_b32 s82, s82, 0x30000\nv_mfma_f32_16x16x32_bf16 v[24:27], v[68:71], v[76:79], v[24:27]\n" \
    "ds_read_b128 v[64:67], v3 offset:17408\nv_mfma_f32_16x16x32_bf16 v[20:23], a[60:63], v[60:63], v[20:23]\nds_read_b128 v[76:79], v10 offset:24576\n" \
    "v_mfma_f32_16x16x32_bf16 v[28:31], v[68:71], v[60:63], v[28:31]\nds_read_b128 v[60:63], v10 offset:28672\ns_waitcnt lgkmcnt(3)\n" \
    "v_mfma_f32_16x16x32_bf16 v[16:19], a[64:67], v[80:83], v[16:19]\nbuffer_store_dwordx4 v[252:255], v7, s[64:67], s82 offen sc1\ns_xor_b32 s82, s82, 0x10000\n" \
    "v_mfma_f32_16x16x32_bf16 v[24:27], v[72:75], v[80:83], v[24:27]\nds_read_b128 v[68:71], v3 offset:18432\nv_mfma_f32_16x16x32_bf16 v[20:23], a[64:67], v[56:59], v[20:23]\n" \
    "ds_read_b128 v[80:83], v9 offset:25600\nv_mfma_f32_16x16x32_bf16 v[28:31], v[72:75], v[56:59], v[28:31]\nds_read_b128 v[56:59], v9 offset:29696\ns_waitcnt lgkmcnt(3)\n" \
    "v_mfma_f32_16x16x32_bf16 v[16:19], a[68:71], v[76:79], v[16:19]\nbuffer_store_dwordx4 v[252:255], v7, s[64:67], s82 offen sc1\ns_xor_b32 s82, s82, 0x30000\n" \
    "v_mfma_f32_16x16x32_bf16 v[24:27], v[64:67], v[76:79], v[24:27]\nds_read_b128 v[72:75], v3 offset:19456\ns_add_u32 s49, s49, 2\n" \
    "v_mfma_f32_16x16x32_bf16 v[20:23], a[68:71], v[60:63], v[20:23]\nds_read_b128 v[76:79], v10 offset:25600\ns_and_b32 s69, s35, 15\n" \
    "v_mfma_f32_16x16x32_bf16 v[28:31], v[64:67], v[60:63], v[28:31]\nds_read_b128 v[60:63], v10 offset:29696\ns_lshl_b32 s69, s69, 17\ns_waitcnt lgkmcnt(3)\n" \
    "v_mfma_f32_16x16x32_bf16 v[16:19], a[72:75], v[80:83], v[16:19]\ns_add_u32 s81, s37, s69\ns_lshr_b32 s69, s81, 8\n" \
    "v_mfma_f32_16x16x32_bf16 v[24:27], v[68:71], v[80:83], v[24:27]\nds_read_b128 v[64:67], v3 offset:20480\ns_and_b32 s69, s69, 0x2000\n" \
    "v_mfma_f32_16x16x32_bf16 v[20:23], a[72:75], v[56:59], v[20:23]\nds_read_b128 v[80:83], v9 offset:32768\ns_xor_b32 s81, s81, s69\n" \
    "v_mfma_f32_16x16x32_bf16 v[28:31], v[68:71], v[56:59], v[28:31]\nds_read_b128 v[56:59], v9 offset:36864\ns_mov_b32 s68, 0\ns_waitcnt lgkmcnt(3)\n" \
    "v_mfma_f32_16x16x32_bf16 v[16:19], a[76:79], v[76:79], v[16:19]\nv_mfma_f32_16x16x32_bf16 v[24:27], v[72:75], v[76:79], v[24:27]\nds_read_b128 v[68:71], v3 offset:21504\n" \
    "v_mfma_f32_16x16x32_bf16 v[20:23], a[76:79], v[60:63], v[20:23]\nds_read_b128 v[76:79], v10 offset:32768\nv_mfma_f32_16x16x32_bf16 v[28:31], v[72:75], v[60:63], v[28:31]\n" \
    "ds_read_b128 v[60:63], v10 offset:36864\ns_waitcnt lgkmcnt(3)\nv_mfma_f32_16x16x32_bf16 v[16:19], a[80:83], v[80:83], v[16:19]\n" \
    "v_mfma_f32_16x16x32_bf16 v[24:27], v[64:67], v[80:83], v[24:27]\nds_read_b128 v[72:75], v3 offset:22528\nv_mfma_f32_16x16x32_bf16 v[20:23], a[80:83], v[56:59], v[20:23]\n" \
    "ds_read_b128 v[80:83], v9 offset:33792\nv_mfma_f32_16x16x32_bf16 v[28:31], v[64:67], v[56:59], v[28:31]\nds_read_b128 v[56:59], v9 offset:37888\ns_waitcnt lgkmcnt(3)\n" \
    "v_mfma_f32_16x16x32_bf16 v[16:19], a[84:87], v[76:79], v[16:19]\nv_mfma_f32_16x16x32_bf16 v[24:27], v[68:71], v[76:79], v[24:27]\nds_read_b128 v[64:67], v3 offset:23552\n" \
    "v_mfma_f32_16x16x32_bf16 v[20:23], a[84:87], v[60:63], v[20:23]\nds_read_b128 v[76:79], v10 offset:33792\nv_mfma_f32_16x16x32_bf16 v[28:31], v[68:71], v[60:63], v[28:31]\n" \
    "ds_read_b128 v[60:63], v10 offset:37888\ns_waitcnt lgkmcnt(3)\nv_mfma_f32_16x16x32_bf16 v[16:19], a[88:91], v[80:83], v[16:19]\n" \
    "v_mfma_f32_16x16x32_bf16 v[24:27], v[72:75], v[80:83], v[24:27]\ns_waitcnt vmcnt(4)\ns_barrier\nv_mfma_f32_16x16x32_bf16 v[20:23], a[88:91], v[56:59], v[20:23]\n" \
    "ds_read_b128 v[68:71], v9 offset:0\nv_mfma_f32_16x16x32_bf16 v[28:31], v[72:75], v[56:59], v[28:31]\nds_read_b128 v[56:59], v9 offset:4096\ns_add_u32 m0, s41, 16384\n" \
    "s_waitcnt lgkmcnt(2)\nv_mfma_f32_16x16x32_bf16 v[16:19], a[92:95], v[76:79], v[16:19]\ns_xor_b32 s85, s62, 512\ns_add_u32 s85, s85, s84\n" \
    "v_mfma_f32_16x16x32_bf16 v[24:27], v[64:67], v[76:79], v[24:27]\nbuffer_load_dwordx4 v11, s[64:67], s85 offen sc1 lds\n" \
    "v_mfma_f32_16x16x32_bf16 v[20:23], a[92:95], v[60:63], v[20:23]\nds_read_b128 v[72:75], v10 offset:0\nv_mfma_f32_16x16x32_bf16 v[28:31], v[64:67], v[60:63], v[28:31]\n" \
    "ds_read_b128 v[60:63], v10 offset:4096\ns_waitcnt lgkmcnt(2)\nv_mfma_f32_16x16x32_bf16 v[16:19], a[96:99], v[68:71], v[16:19]\n" \
    "buffer_load_dwordx4 v12, s[64:67], s85 offen offset:1024 sc1 lds\ns_add_u32 m0, s41, 40960\nv_mfma_f32_16x16x32_bf16 v[24:27], a[104:107], v[68:71], v[24:27]\n" \
    "s_xor_b32 s85, s62, 768\ns_add_u32 s85, s85, s84\nv_mfma_f32_16x16x32_bf16 v[20:23], a[96:99], v[56:59], v[20:23]\nds_read_b128 v[64:67], v9 offset:1024\n" \
    "buffer_load_dwordx4 v11, s[64:67], s85 offen sc1 lds\nv_mfma_f32_16x16x32_bf16 v[28:31], a[104:107], v[56:59], v[28:31]\nds_read_b128 v[56:59], v9 offset:5120\n" \
    "s_waitcnt lgkmcnt(2)\nv_mfma_f32_16x16x32_bf16 v[16:19], a[100:103], v[72:75], v[16:19]\nv_mfma_f32_16x16x32_bf16 v[24:27], a[108:111], v[72:75], v[24:27]\n" \
    "buffer_load_dwordx4 v12, s[64:67], s85 offen offset:1024 sc1 lds\nv_mfma_f32_16x16x32_bf16 v[20:23], a[100:103], v[60:63], v[20:23]\n" \
    "ds_read_b128 v[68:71], v10 offset:1024\nv_mfma_f32_16x16x32_bf16 v[28:31], a[108:111], v[60:63], v[28:31]\nds_read_b128 v[60:63], v10 offset:5120\ns_waitcnt lgkmcnt(2)\n" \
    "v_mfma_f32_16x16x32_bf16 v[16:19], a[112:115], v[64:67], v[16:19]\nv_mfma_f32_16x16x32_bf16 v[24:27], a[120:123], v[64:67], v[24:27]\n" \
    "v_mfma_f32_16x16x32_bf16 v[20:23], a[112:115], v[56:59], v[20:23]\nds_read_b128 v[64:67], v9 offset:8192\n" \
    "v_mfma_f32_16x16x32_bf16 v[28:31], a[120:123], v[56:59], v[28:31]\nds_read_b128 v[56:59], v9 offset:12288\ns_waitcnt lgkmcnt(2)\n" \
    "v_mfma_f32_16x16x32_bf16 v[16:19], a[116:119], v[68:71], v[16:19]\nv_mfma_f32_16x16x32_bf16 v[24:27], a[124:127], v[68:71], v[24:27]\n" \
    "v_mfma_f32_16x16x32_bf16 v[20:23], a[116:119], v[60:63], v[20:23]\nds_read_b128 v[68:71], v10 offset:8192\n" \
    "v_mfma_f32_16x16x32_bf16 v[28:31], a[124:127], v[60:63], v[28:31]\nds_read_b128 v[60:63], v10 offset:12288\ns_waitcnt lgkmcnt(2)\n" \
    "v_mfma_f32_16x16x32_bf16 v[16:19], a[128:131], v[64:67], v[16:19]\nv_readfirstlane_b32 s74, v13\nv_mfma_f32_16x16x32_bf16 v[24:27], a[136:139], v[64:67], v[24:27]\n" \
    "s_and_b32 s75, s74, 127\ns_lshr_b32 s69, s74, 8\nv_mfma_f32_16x16x32_bf16 v[20:23], a[128:131], v[56:59], v[20:23]\nds_read_b128 v[64:67], v9 offset:9216\n" \
    "s_sub_u32 s69, s69, s80\nv_mfma_f32_16x16x32_bf16 v[28:31], a[136:139], v[56:59], v[28:31]\nds_read_b128 v[56:59], v9 offset:13312\ns_bfe_i32 s69, s69, 0x180000\n" \
    "s_waitcnt lgkmcnt(2)\nv_mfma_f32_16x16x32_bf16 v[16:19], a[132:135], v[68:71], v[16:19]\nds_read_b128 v[72:75], v10 offset:9216\ns_cmp_eq_u32 s75, 0\n" \
    "s_cselect_b32 s69, 0, s69\nv_mfma_f32_16x16x32_bf16 v[24:27], a[140:143], v[68:71], v[24:27]\nds_read_b128 v[68:71], v10 offset:13312\ns_add_u32 s80, s80, s69\n" \
    "v_mfma_f32_16x16x32_bf16 v[20:23], a[132:135], v[60:63], v[20:23]\ns_lshl_b32 s74, s74, 5\ns_and_b32 s74, s74, 0x1000\n" \
    "v_mfma_f32_16x16x32_bf16 v[28:31], a[140:143], v[60:63], v[28:31]\ns_andn2_b32 s81, s81, 0x1000\ns_or_b32 s81, s81, s74\ns_waitcnt lgkmcnt(2)\n" \
    "v_mfma_f32_16x16x32_bf16 v[16:19], a[144:147], v[64:67], v[16:19]\nv_mfma_f32_16x16x32_bf16 v[24:27], a[152:155], v[64:67], v[24:27]\ns_waitcnt lgkmcnt(0)\ns_barrier\n" \
    "v_mfma_f32_16x16x32_bf16 v[16:19], a[148:151], v[72:75], v[16:19]\ns_add_u32 m0, s41, 0\ns_xor_b32 s85, s62, 0\n" \
    "v_mfma_f32_16x16x32_bf16 v[24:27], a[156:159], v[72:75], v[24:27]\ns_add_u32 s85, s85, s84\nbuffer_load_dwordx4 v11, s[64:67], s85 offen sc1 lds\n" \
    "v_mfma_f32_16x16x32_bf16 v[20:23], a[144:147], v[56:59], v[20:23]\ns_nop 1\nv_cvt_pk_bf16_f32 v32, v16, v17\nv_cvt_pk_bf16_f32 v33, v18, v19\n" \
    "v_mfma_f32_16x16x32_bf16 v[28:31], a[152:155], v[56:59], v[28:31]\nv_cvt_pk_bf16_f32 v34, v24, v25\nv_cvt_pk_bf16_f32 v35, v26, v27\n" \
    "v_mfma_f32_16x16x32_bf16 v[20:23], a[148:151], v[68:71], v[20:23]\nbuffer_load_dwordx4 v12, s[64:67], s85 offen offset:1024 sc1 lds\ns_add_u32 m0, s41, 8192\n" \
    "v_mfma_f32_16x16x32_bf16 v[28:31], a[156:159], v[68:71], v[28:31]\ns_xor_b32 s85, s62, 256\ns_add_u32 s85, s85, s84\nds_write_b128 v14, v[32:35] offset:0\n" \
    "buffer_load_dwordx4 v11, s[64:67], s85 offen sc1 lds\nbuffer_load_dwordx4 v12, s[64:67], s85 offen offset:1024 sc1 lds\nv_cvt_pk_bf16_f32 v36, v20, v21\n" \
    "v_cvt_pk_bf16_f32 v37, v22, v23\ns_mov_b32 s40, s84\nv_cvt_pk_bf16_f32 v38, v28, v29\nv_cvt_pk_bf16_f32 v39, v30, v31\nds_write_b128 v14, v[36:39] offset:4096\n" \
    "s_waitcnt vmcnt(0) lgkmcnt(0)\ns_memrealtime s[72:73]\ns_waitcnt lgkmcnt(0)\nv_mov_b32 v253, 1\nv_mov_b32 v252, s72\ns_or_b32 s69, s35, s36\n" \
    "s_cselect_b32 s69, 0x7fff0000, 0\ns_mov_b64 exec, 1\nbuffer_atomic_umax v252, off, s[64:67], s69 offset:260\nbuffer_atomic_add v253, off, s[64:67], s69 offset:256\n" \
    "s_mov_b64 exec, -1\ns_mov_b32 s16, 0\ns_cmp_eq_u32 s36, 0\ns_cselect_b32 s17, 511, -1\ns_mov_b32 s18, s44\ns_mov_b32 s72, s80\ns_barrier\n.Lv5_%=:\n" \
    "ds_read_b128 v[56:59], v6 offset:0\nds_read_b128 v[60:63], v6 offset:1024\nds_read_b128 v[64:67], v6 offset:2048\nv_mov_b64 v[32:33], 0\nv_mov_b64 v[34:35], 0\n" \
    "v_mov_b64 v[36:37], 0\nv_mov_b64 v[38:39], 0\ns_and_b32 s69, s50, 7\ns_lshl_b32 s69, s69, 21\ns_add_u32 s86, s39, s69\ns_lshr_b32 s69, s86, 8\ns_and_b32 s69, s69, 0x2000\n" \
    "s_xor_b32 s86, s86, s69\ns_waitcnt lgkmcnt(2)\nv_smfmac_f32_16x16x64_bf16 v[32:35], v[56:59], a[160:167], v0\nds_read_b128 v[68:71], v6 offset:3072\nv_mov_b64 v[40:41], 0\n" \
    "v_smfmac_f32_16x16x64_bf16 v[36:39], v[56:59], a[176:183], v0\nds_read_b128 v[72:75], v3 offset:0\ns_waitcnt lgkmcnt(3)\nv_mov_b64 v[42:43], 0\n" \
    "v_smfmac_f32_16x16x64_bf16 v[32:35], v[60:63], a[168:175], v0\nds_read_b128 v[76:79], v9 offset:0\nv_mov_b64 v[44:45], 0\n" \
    "v_smfmac_f32_16x16x64_bf16 v[36:39], v[60:63], a[184:191], v0\nds_read_b128 v[80:83], v9 offset:4096\ns_waitcnt lgkmcnt(4)\nv_mov_b64 v[46:47], 0\n" \
    "v_smfmac_f32_16x16x64_bf16 v[32:35], v[64:67], a[192:199], v0\ns_xor_b32 s87, s86, 0x40000\ns_xor_b32 s88, s86, 0x80000\n" \
    "v_smfmac_f32_16x16x64_bf16 v[36:39], v[64:67], a[208:215], v0\ns_xor_b32 s89, s86, 0xc0000\ns_add_u32 s50, s50, 2\ns_waitcnt lgkmcnt(3)\n" \
    "v_smfmac_f32_16x16x64_bf16 v[32:35], v[68:71], a[200:207], v0\ns_add_u32 m0, s41, 24576\ns_xor_b32 s85, s62, 1024\n" \
    "v_smfmac_f32_16x16x64_bf16 v[36:39], v[68:71], a[216:223], v0\ns_add_u32 s85, s85, s40\nbuffer_load_dwordx4 v11, s[64:67], s85 offen sc1 lds\ns_waitcnt lgkmcnt(0)\n" \
    "v_mfma_f32_16x16x32_bf16 v[16:19], a[0:3], v[76:79], 0\ns_nop 0\nv_cvt_pk_bf16_f32 v48, v32, v33\nv_cvt_pk_bf16_f32 v50, v34, v35\n" \
    "v_mfma_f32_16x16x32_bf16 v[24:27], v[72:75], v[76:79], 0\nv_cvt_pk_bf16_f32 v49, v36, v37\nv_cvt_pk_bf16_f32 v51, v38, v39\n" \
    "v_smfmac_f32_16x16x64_bf16 v[40:43], v[56:59], a[224:231], v0\nbuffer_load_dwordx4 v12, s[64:67], s85 offen offset:1024 sc1 lds\nv_permlane16_swap_b32 v48, v49\n" \
    "v_smfmac_f32_16x16x64_bf16 v[44:47], v[56:59], a[240:247], v0\ns_add_u32 m0, s41, 32768\nv_permlane16_swap_b32 v50, v51\n" \
    "v_smfmac_f32_16x16x64_bf16 v[40:43], v[60:63], a[232:239], v0\nbuffer_store_dwordx4 v[48:51], v8, s[64:67], s86 offen offset:0 sc1\nv_mov_b64 v[32:33], 0\n" \
    "v_smfmac_f32_16x16x64_bf16 v[44:47], v[60:63], a[248:255], v0\ns_xor_b32 s85, s62, 1280\nv_mov_b64 v[34:35], 0\n" \
    "v_smfmac_f32_16x16x64_bf16 v[40:43], v[64:67], v[92:99], v0\ns_add_u32 s85, s85, s40\nv_mov_b64 v[36:37], 0\nv_smfmac_f32_16x16x64_bf16 v[44:47], v[64:67], v[108:115], v0\n" \
    "buffer_load_dwordx4 v11, s[64:67], s85 offen sc1 lds\nv_mov_b64 v[38:39], 0\nv_smfmac_f32_16x16x64_bf16 v[40:43], v[68:71], v[100:107], v0\n" \
    "v_smfmac_f32_16x16x64_bf16 v[44:47], v[68:71], v[116:123], v0\ns_nop 1\nv_mfma_f32_16x16x32_bf16 v[20:23], a[0:3], v[80:83], 0\n" \
    "buffer_load_dwordx4 v12, s[64:67], s85 offen offset:1024 sc1 lds\ns_sub_u32 s20, s72, s80\nv_mfma_f32_16x16x32_bf16 v[28:31], v[72:75], v[80:83], 0\n" \
    "s_add_u32 s20, s20, 10\nv_cvt_pk_bf16_f32 v52, v40, v41\nv_cvt_pk_bf16_f32 v54, v42, v43\nv_smfmac_f32_16x16x64_bf16 v[32:35], v[56:59], v[124:131], v0\n" \
    "s_min_u32 s21, s20, s16\nv_cvt_pk_bf16_f32 v53, v44, v45\nv_smfmac_f32_16x16x64_bf16 v[36:39], v[56:59], v[140:147], v0\nds_read_b128 v[72:75], v3 offset:1024\n" \
    "v_cvt_pk_bf16_f32 v55, v46, v47\nv_smfmac_f32_16x16x64_bf16 v[32:35], v[60:63], v[132:139], v0\nds_read_b128 v[76:79], v10 offset:0\nv_permlane16_swap_b32 v52, v53\n" \
    "v_smfmac_f32_16x16x64_bf16 v[36:39], v[60:63], v[148:155], v0\nds_read_b128 v[80:83], v10 offset:4096\nv_permlane16_swap_b32 v54, v55\n" \
    "v_smfmac_f32_16x16x64_bf16 v[32:35], v[64:67], v[156:163], v0\nbuffer_store_dwordx4 v[52:55], v8, s[64:67], s87 offen offset:0 sc1\nv_mov_b64 v[40:41], 0\n" \
    "v_smfmac_f32_16x16x64_bf16 v[36:39], v[64:67], v[172:179], v0\ns_mov_b32 s16, s20\nv_mov_b64 v[42:43], 0\nv_smfmac_f32_16x16x64_bf16 v[32:35], v[68:71], v[164:171], v0\n" \
    "s_cmp_gt_u32 s21, 156\ns_cselect_b32 s21, 0, s21\nv_mov_b64 v[44:45], 0\nv_smfmac_f32_16x16x64_bf16 v[36:39], v[68:71], v[180:187], v0\ns_lshr_b32 s20, s35, 2\n" \
    "s_waitcnt lgkmcnt(0)\nv_mov_b64 v[46:47], 0\nv_mfma_f32_16x16x32_bf16 v[16:19], a[4:7], v[76:79], v[16:19]\ns_lshl_b32 s22, s20, 8\nv_cvt_pk_bf16_f32 v48, v32, v33\n" \
    "v_cvt_pk_bf16_f32 v50, v34, v35\nv_mfma_f32_16x16x32_bf16 v[24:27], v[72:75], v[76:79], v[24:27]\ns_or_b32 s21, s21, s22\nv_cvt_pk_bf16_f32 v49, v36, v37\n" \
    "v_cvt_pk_bf16_f32 v51, v38, v39\nv_smfmac_f32_16x16x64_bf16 v[40:43], v[56:59], v[188:195], v0\ns_and_b32 s20, s20, 1\nv_permlane16_swap_b32 v48, v49\n" \
    "v_smfmac_f32_16x16x64_bf16 v[44:47], v[56:59], v[204:211], v0\ns_lshl_b32 s20, s20, 2\nv_permlane16_swap_b32 v50, v51\n" \
    "v_smfmac_f32_16x16x64_bf16 v[40:43], v[60:63], v[196:203], v0\nbuffer_store_dwordx4 v[48:51], v8, s[64:67], s88 offen offset:0 sc1\nv_mov_b64 v[32:33], 0\n" \
    "v_smfmac_f32_16x16x64_bf16 v[44:47], v[60:63], v[212:219], v0\ns_cmp_gt_u32 s21, s17\ns_cselect_b32 s20, s20, 0x7fff0000\nv_mov_b64 v[34:35], 0\n" \
    "v_smfmac_f32_16x16x64_bf16 v[40:43], v[64:67], v[220:227], v0\ns_max_u32 s17, s17, s21\nv_mov_b64 v[36:37], 0\n" \
    "v_smfmac_f32_16x16x64_bf16 v[44:47], v[64:67], v[236:243], v0\nds_read_b128 v[76:79], v6 offset:128\nv_mov_b64 v[38:39], 0\n" \
    "v_smfmac_f32_16x16x64_bf16 v[40:43], v[68:71], v[228:235], v0\nv_mov_b32 v15, s21\ns_mov_b64 exec, 1\nbuffer_atomic_umax v15, off, s[64:67], s20 offset:768\n" \
    "s_mov_b64 exec, -1\nv_smfmac_f32_16x16x64_bf16 v[44:47], v[68:71], v[244:251], v0\nds_read_b128 v[56:59], v6 offset:1152\ns_add_u32 s84, s40, 0x20000\n" \
    "v_mfma_f32_16x16x32_bf16 v[20:23], a[4:7], v[80:83], v[20:23]\ns_cmp_lt_u32 s49, 62\ns_cselect_b32 s84, s84, s40\nv_cvt_pk_bf16_f32 v52, v40, v41\n" \
    "v_mfma_f32_16x16x32_bf16 v[28:31], v[72:75], v[80:83], v[28:31]\nds_read_b128 v[60:63], v6 offset:2176\ns_waitcnt lgkmcnt(2)\nv_cvt_pk_bf16_f32 v54, v42, v43\n" \
    "v_smfmac_f32_16x16x64_bf16 v[32:35], v[76:79], a[160:167], v0\nds_read_b128 v[64:67], v6 offset:3200\nv_cvt_pk_bf16_f32 v53, v44, v45\n" \
    "v_smfmac_f32_16x16x64_bf16 v[36:39], v[76:79], a[176:183], v0\nds_read_b128 v[68:71], v3 offset:2048\ns_waitcnt lgkmcnt(3)\nv_cvt_pk_bf16_f32 v55, v46, v47\n" \
    "v_smfmac_f32_16x16x64_bf16 v[32:35], v[56:59], a[168:175], v0\nds_read_b128 v[72:75], v9 offset:1024\nv_permlane16_swap_b32 v52, v53\n" \
    "v_smfmac_f32_16x16x64_bf16 v[36:39], v[56:59], a[184:191], v0\nds_read_b128 v[80:83], v9 offset:5120\ns_waitcnt lgkmcnt(4)\nv_permlane16_swap_b32 v54, v55\n" \
    "v_smfmac_f32_16x16x64_bf16 v[32:35], v[60:63], a[192:199], v0\nbuffer_store_dwordx4 v[52:55], v8, s[64:67], s89 offen offset:0 sc1\nv_mov_b64 v[40:41], 0\n" \
    "v_smfmac_f32_16x16x64_bf16 v[36:39], v[60:63], a[208:215], v0\ns_waitcnt lgkmcnt(3)\nv_mov_b64 v[42:43], 0\nv_smfmac_f32_16x16x64_bf16 v[32:35], v[64:67], a[200:207], v0\n" \
    "v_mov_b64 v[44:45], 0\nv_smfmac_f32_16x16x64_bf16 v[36:39], v[64:67], a[216:223], v0\ns_waitcnt lgkmcnt(0)\nv_mov_b64 v[46:47], 0\n" \
    "v_mfma_f32_16x16x32_bf16 v[16:19], a[8:11], v[72:75], v[16:19]\ns_nop 0\nv_mov_b64 v[252:253], -1\nv_mov_b64 v[254:255], -1\n" \
    "v_mfma_f32_16x16x32_bf16 v[24:27], v[68:71], v[72:75], v[24:27]\ns_and_b32 s69, s49, 15\nv_cvt_pk_bf16_f32 v48, v32, v33\nv_cvt_pk_bf16_f32 v50, v34, v35\n" \
    "v_smfmac_f32_16x16x64_bf16 v[40:43], v[76:79], a[224:231], v0\ns_lshl_b32 s69, s69, 19\nv_cvt_pk_bf16_f32 v49, v36, v37\n" \
    "v_smfmac_f32_16x16x64_bf16 v[44:47], v[76:79], a[240:247], v0\ns_add_u32 s82, s38, s69\nv_cvt_pk_bf16_f32 v51, v38, v39\n" \
    "v_smfmac_f32_16x16x64_bf16 v[40:43], v[56:59], a[232:239], v0\ns_lshr_b32 s69, s82, 8\nv_permlane16_swap_b32 v48, v49\n" \
    "v_smfmac_f32_16x16x64_bf16 v[44:47], v[56:59], a[248:255], v0\ns_and_b32 s69, s69, 0x2000\nv_permlane16_swap_b32 v50, v51\n" \
    "v_smfmac_f32_16x16x64_bf16 v[40:43], v[60:63], v[92:99], v0\nbuffer_store_dwordx4 v[48:51], v8, s[64:67], s86 offen offset:256 sc1\nv_mov_b64 v[32:33], 0\n" \
    "v_smfmac_f32_16x16x64_bf16 v[44:47], v[60:63], v[108:115], v0\ns_xor_b32 s82, s82, s69\nv_mov_b64 v[34:35], 0\n" \
    "v_smfmac_f32_16x16x64_bf16 v[40:43], v[64:67], v[100:107], v0\ns_and_b32 s69, s35, 15\nv_mov_b64 v[36:37], 0\n" \
    "v_smfmac_f32_16x16x64_bf16 v[44:47], v[64:67], v[116:123], v0\ns_lshl_b32 s69, s69, 19\nv_mov_b64 v[38:39], 0\n" \
    "v_mfma_f32_16x16x32_bf16 v[20:23], a[8:11], v[80:83], v[20:23]\ns_add_u32 s0, s38, s69\ns_lshr_b32 s69, s0, 8\nv_cvt_pk_bf16_f32 v52, v40, v41\n" \
    "v_mfma_f32_16x16x32_bf16 v[28:31], v[68:71], v[80:83], v[28:31]\ns_and_b32 s69, s69, 0x2000\nv_cvt_pk_bf16_f32 v54, v42, v43\nv_cvt_pk_bf16_f32 v53, v44, v45\n" \
    "v_smfmac_f32_16x16x64_bf16 v[32:35], v[76:79], v[124:131], v0\ns_xor_b32 s0, s0, s69\nv_cvt_pk_bf16_f32 v55, v46, v47\n" \
    "v_smfmac_f32_16x16x64_bf16 v[36:39], v[76:79], v[140:147], v0\nds_read_b128 v[68:71], v3 offset:3072\nv_permlane16_swap_b32 v52, v53\n" \
    "v_smfmac_f32_16x16x64_bf16 v[32:35], v[56:59], v[132:139], v0\nds_read_b128 v[72:75], v10 offset:1024\nv_permlane16_swap_b32 v54, v55\n" \
    "v_smfmac_f32_16x16x64_bf16 v[36:39], v[56:59], v[148:155], v0\nds_read_b128 v[80:83], v10 offset:5120\n" \
    "buffer_store_dwordx4 v[52:55], v8, s[64:67], s87 offen offset:256 sc1\nv_mov_b64 v[40:41], 0\nv_smfmac_f32_16x16x64_bf16 v[32:35], v[60:63], v[156:163], v0\n" \
    "s_xor_b32 s1, s0, 0x10000\nv_mov_b64 v[42:43], 0\nv_smfmac_f32_16x16x64_bf16 v[36:39], v[60:63], v[172:179], v0\ns_xor_b32 s2, s0, 0x20000\nv_mov_b64 v[44:45], 0\n" \
    "v_smfmac_f32_16x16x64_bf16 v[32:35], v[64:67], v[164:171], v0\ns_xor_b32 s3, s0, 0x30000\nv_mov_b64 v[46:47], 0\n" \
    "v_smfmac_f32_16x16x64_bf16 v[36:39], v[64:67], v[180:187], v0\ns_nop 0\ns_waitcnt lgkmcnt(0)\nv_lshrrev_b32 v15, 1, v2\n" \
    "v_mfma_f32_16x16x32_bf16 v[16:19], a[12:15], v[72:75], v[16:19]\ns_nop 0\nv_cvt_pk_bf16_f32 v48, v32, v33\nv_cvt_pk_bf16_f32 v50, v34, v35\n" \
    "v_mfma_f32_16x16x32_bf16 v[24:27], v[68:71], v[72:75], v[24:27]\nv_cvt_pk_bf16_f32 v49, v36, v37\nv_cvt_pk_bf16_f32 v51, v38, v39\n" \
    "v_smfmac_f32_16x16x64_bf16 v[40:43], v[76:79], v[188:195], v0\nv_permlane16_swap_b32 v48, v49\nv_smfmac_f32_16x16x64_bf16 v[44:47], v[76:79], v[204:211], v0\n" \
    "v_permlane16_swap_b32 v50, v51\nv_smfmac_f32_16x16x64_bf16 v[40:43], v[56:59], v[196:203], v0\nbuffer_store_dwordx4 v[48:51], v8, s[64:67], s88 offen offset:256 sc1\n" \
    "v_mov_b64 v[32:33], 0\nv_smfmac_f32_16x16x64_bf16 v[44:47], v[56:59], v[212:219], v0\nv_mov_b64 v[34:35], 0\nv_smfmac_f32_16x16x64_bf16 v[40:43], v[60:63], v[220:227], v0\n" \
    "v_mov_b64 v[36:37], 0\nv_smfmac_f32_16x16x64_bf16 v[44:47], v[60:63], v[236:243], v0\nds_read_b128 v[56:59], v6 offset:4096\nv_mov_b64 v[38:39], 0\n" \
    "v_smfmac_f32_16x16x64_bf16 v[40:43], v[64:67], v[228:235], v0\nv_add_u32 v15, s60, v15\nv_smfmac_f32_16x16x64_bf16 v[44:47], v[64:67], v[244:251], v0\n" \
    "ds_read_b128 v[60:63], v6 offset:5120\ns_nop 0\nv_mfma_f32_16x16x32_bf16 v[20:23], a[12:15], v[80:83], v[20:23]\nds_write_b64 v15, v[252:253]\ns_nop 0\n" \
    "v_mfma_f32_16x16x32_bf16 v[28:31], v[68:71], v[80:83], v[28:31]\nds_read_b128 v[64:67], v6 offset:6144\ns_waitcnt lgkmcnt(3)\nv_cvt_pk_bf16_f32 v52, v40, v41\n" \
    "v_smfmac_f32_16x16x64_bf16 v[32:35], v[56:59], a[160:167], v0\nds_read_b128 v[68:71], v6 offset:7168\nv_cvt_pk_bf16_f32 v54, v42, v43\n" \
    "v_smfmac_f32_16x16x64_bf16 v[36:39], v[56:59], a[176:183], v0\nds_read_b128 v[72:75], v3 offset:4096\ns_waitcnt lgkmcnt(4)\nv_cvt_pk_bf16_f32 v53, v44, v45\n" \
    "v_smfmac_f32_16x16x64_bf16 v[32:35], v[60:63], a[168:175], v0\nds_read_b128 v[76:79], v9 offset:8192\nv_cvt_pk_bf16_f32 v55, v46, v47\n" \
    "v_smfmac_f32_16x16x64_bf16 v[36:39], v[60:63], a[184:191], v0\nds_read_b128 v[80:83], v9 offset:12288\ns_waitcnt lgkmcnt(4)\nv_permlane16_swap_b32 v52, v53\n" \
    "v_smfmac_f32_16x16x64_bf16 v[32:35], v[64:67], a[192:199], v0\nv_permlane16_swap_b32 v54, v55\nv_smfmac_f32_16x16x64_bf16 v[36:39], v[64:67], a[208:215], v0\n" \
    "buffer_store_dwordx4 v[52:55], v8, s[64:67], s89 offen offset:256 sc1\ns_waitcnt lgkmcnt(3)\nv_mov_b64 v[40:41], 0\n" \
    "v_smfmac_f32_16x16x64_bf16 v[32:35], v[68:71], a[200:207], v0\nv_mov_b64 v[42:43], 0\nv_smfmac_f32_16x16x64_bf16 v[36:39], v[68:71], a[216:223], v0\ns_waitcnt lgkmcnt(0)\n" \
    "v_mov_b64 v[44:45], 0\nv_mfma_f32_16x16x32_bf16 v[16:19], a[16:19], v[76:79], v[16:19]\nv_mov_b64 v[46:47], 0\n" \
    "v_mfma_f32_16x16x32_bf16 v[24:27], v[72:75], v[76:79], v[24:27]\ns_nop 0\nv_cvt_pk_bf16_f32 v48, v32, v33\nv_cvt_pk_bf16_f32 v50, v34, v35\n" \
    "v_smfmac_f32_16x16x64_bf16 v[40:43], v[56:59], a[224:231], v0\nv_cvt_pk_bf16_f32 v49, v36, v37\nv_smfmac_f32_16x16x64_bf16 v[44:47], v[56:59], a[240:247], v0\n" \
    "v_cvt_pk_bf16_f32 v51, v38, v39\nv_smfmac_f32_16x16x64_bf16 v[40:43], v[60:63], a[232:239], v0\nv_permlane16_swap_b32 v48, v49\n" \
    "v_smfmac_f32_16x16x64_bf16 v[44:47], v[60:63], a[248:255], v0\nv_permlane16_swap_b32 v50, v51\nv_smfmac_f32_16x16x64_bf16 v[40:43], v[64:67], v[92:99], v0\n" \
    "buffer_store_dwordx4 v[48:51], v8, s[64:67], s86 offen offset:512 sc1\nv_mov_b64 v[32:33], 0\nv_smfmac_f32_16x16x64_bf16 v[44:47], v[64:67], v[108:115], v0\n" \
    "v_mov_b64 v[34:35], 0\nv_smfmac_f32_16x16x64_bf16 v[40:43], v[68:71], v[100:107], v0\nv_mov_b64 v[36:37], 0\nv_smfmac_f32_16x16x64_bf16 v[44:47], v[68:71], v[116:123], v0\n" \
    "v_mov_b64 v[38:39], 0\nv_mfma_f32_16x16x32_bf16 v[20:23], a[16:19], v[80:83], v[20:23]\ns_nop 1\nv_mfma_f32_16x16x32_bf16 v[28:31], v[72:75], v[80:83], v[28:31]\ns_nop 0\n" \
    "v_cvt_pk_bf16_f32 v52, v40, v41\nv_cvt_pk_bf16_f32 v54, v42, v43\nv_smfmac_f32_16x16x64_bf16 v[32:35], v[56:59], v[124:131], v0\nv_cvt_pk_bf16_f32 v53, v44, v45\n" \
    "v_smfmac_f32_16x16x64_bf16 v[36:39], v[56:59], v[140:147], v0\nds_read_b128 v[72:75], v3 offset:5120\nv_cvt_pk_bf16_f32 v55, v46, v47\n" \
    "v_smfmac_f32_16x16x64_bf16 v[32:35], v[60:63], v[132:139], v0\nds_read_b128 v[76:79], v10 offset:8192\nv_permlane16_swap_b32 v52, v53\n" \
    "v_smfmac_f32_16x16x64_bf16 v[36:39], v[60:63], v[148:155], v0\nds_read_b128 v[80:83], v10 offset:12288\nv_permlane16_swap_b32 v54, v55\n" \
    "v_smfmac_f32_16x16x64_bf16 v[32:35], v[64:67], v[156:163], v0\nbuffer_store_dwordx4 v[52:55], v8, s[64:67], s87 offen offset:512 sc1\nv_mov_b64 v[40:41], 0\n" \
    "v_smfmac_f32_16x16x64_bf16 v[36:39], v[64:67], v[172:179], v0\nv_mov_b64 v[42:43], 0\nv_smfmac_f32_16x16x64_bf16 v[32:35], v[68:71], v[164:171], v0\nv_mov_b64 v[44:45], 0\n" \
    "v_smfmac_f32_16x16x64_bf16 v[36:39], v[68:71], v[180:187], v0\ns_waitcnt lgkmcnt(0)\nv_mov_b64 v[46:47], 0\n" \
    "v_mfma_f32_16x16x32_bf16 v[16:19], a[20:23], v[76:79], v[16:19]\ns_nop 1\nv_mfma_f32_16x16x32_bf16 v[24:27], v[72:75], v[76:79], v[24:27]\nv_cvt_pk_bf16_f32 v48, v32, v33\n" \
    "v_cvt_pk_bf16_f32 v50, v34, v35\nv_smfmac_f32_16x16x64_bf16 v[40:43], v[56:59], v[188:195], v0\nv_cvt_pk_bf16_f32 v49, v36, v37\n" \
    "v_smfmac_f32_16x16x64_bf16 v[44:47], v[56:59], v[204:211], v0\nv_cvt_pk_bf16_f32 v51, v38, v39\nv_smfmac_f32_16x16x64_bf16 v[40:43], v[60:63], v[196:203], v0\n" \
    "v_permlane16_swap_b32 v48, v49\nv_smfmac_f32_16x16x64_bf16 v[44:47], v[60:63], v[212:219], v0\nv_permlane16_swap_b32 v50, v51\n" \
    "v_smfmac_f32_16x16x64_bf16 v[40:43], v[64:67], v[220:227], v0\nbuffer_store_dwordx4 v[48:51], v8, s[64:67], s88 offen offset:512 sc1\nv_mov_b64 v[32:33], 0\n" \
    "v_smfmac_f32_16x16x64_bf16 v[44:47], v[64:67], v[236:243], v0\nds_read_b128 v[76:79], v6 offset:4224\nv_mov_b64 v[34:35], 0\n" \
    "v_smfmac_f32_16x16x64_bf16 v[40:43], v[68:71], v[228:235], v0\nv_mov_b64 v[36:37], 0\nv_smfmac_f32_16x16x64_bf16 v[44:47], v[68:71], v[244:251], v0\n" \
    "ds_read_b128 v[56:59], v6 offset:5248\nv_mov_b64 v[38:39], 0\nv_mfma_f32_16x16x32_bf16 v[20:23], a[20:23], v[80:83], v[20:23]\ns_nop 1\n" \
    "v_mfma_f32_16x16x32_bf16 v[28:31], v[72:75], v[80:83], v[28:31]\nds_read_b128 v[60:63], v6 offset:6272\ns_waitcnt lgkmcnt(2)\nv_cvt_pk_bf16_f32 v52, v40, v41\n" \
    "v_smfmac_f32_16x16x64_bf16 v[32:35], v[76:79], a[160:167], v0\nds_read_b128 v[64:67], v6 offset:7296\nv_cvt_pk_bf16_f32 v54, v42, v43\n" \
    "v_smfmac_f32_16x16x64_bf16 v[36:39], v[76:79], a[176:183], v0\nds_read_b128 v[68:71], v3 offset:6144\ns_waitcnt lgkmcnt(3)\nv_cvt_pk_bf16_f32 v53, v44, v45\n" \
    "v_smfmac_f32_16x16x64_bf16 v[32:35], v[56:59], a[168:175], v0\nds_read_b128 v[72:75], v9 offset:9216\nv_cvt_pk_bf16_f32 v55, v46, v47\n" \
    "v_smfmac_f32_16x16x64_bf16 v[36:39], v[56:59], a[184:191], v0\nds_read_b128 v[80:83], v9 offset:13312\ns_waitcnt lgkmcnt(4)\nv_permlane16_swap_b32 v52, v53\n" \
    "v_smfmac_f32_16x16x64_bf16 v[32:35], v[60:63], a[192:199], v0\nv_permlane16_swap_b32 v54, v55\nv_smfmac_f32_16x16x64_bf16 v[36:39], v[60:63], a[208:215], v0\n" \
    "buffer_store_dwordx4 v[52:55], v8, s[64:67], s89 offen offset:512 sc1\ns_waitcnt lgkmcnt(3)\nv_mov_b64 v[40:41], 0\n" \
    "v_smfmac_f32_16x16x64_bf16 v[32:35], v[64:67], a[200:207], v0\nv_mov_b64 v[42:43], 0\nv_smfmac_f32_16x16x64_bf16 v[36:39], v[64:67], a[216:223], v0\ns_waitcnt lgkmcnt(0)\n" \
    "v_mov_b64 v[44:45], 0\nv_mfma_f32_16x16x32_bf16 v[16:19], a[24:27], v[72:75], v[16:19]\nv_mov_b64 v[46:47], 0\n" \
    "v_mfma_f32_16x16x32_bf16 v[24:27], v[68:71], v[72:75], v[24:27]\ns_nop 0\nv_cvt_pk_bf16_f32 v48, v32, v33\nv_cvt_pk_bf16_f32 v50, v34, v35\n" \
    "v_smfmac_f32_16x16x64_bf16 v[40:43], v[76:79], a[224:231], v0\nv_cvt_pk_bf16_f32 v49, v36, v37\nv_smfmac_f32_16x16x64_bf16 v[44:47], v[76:79], a[240:247], v0\n" \
    "v_cvt_pk_bf16_f32 v51, v38, v39\nv_smfmac_f32_16x16x64_bf16 v[40:43], v[56:59], a[232:239], v0\nv_permlane16_swap_b32 v48, v49\n" \
    "v_smfmac_f32_16x16x64_bf16 v[44:47], v[56:59], a[248:255], v0\nv_permlane16_swap_b32 v50, v51\nv_smfmac_f32_16x16x64_bf16 v[40:43], v[60:63], v[92:99], v0\n" \
    "buffer_store_dwordx4 v[48:51], v8, s[64:67], s86 offen offset:768 sc1\nv_mov_b64 v[32:33], 0\nv_smfmac_f32_16x16x64_bf16 v[44:47], v[60:63], v[108:115], v0\n" \
    "v_mov_b64 v[34:35], 0\nv_smfmac_f32_16x16x64_bf16 v[40:43], v[64:67], v[100:107], v0\nv_mov_b64 v[36:37], 0\nv_smfmac_f32_16x16x64_bf16 v[44:47], v[64:67], v[116:123], v0\n" \
    "v_mov_b64 v[38:39], 0\nv_mfma_f32_16x16x32_bf16 v[20:23], a[24:27], v[80:83], v[20:23]\ns_nop 1\nv_mfma_f32_16x16x32_bf16 v[28:31], v[68:71], v[80:83], v[28:31]\ns_nop 0\n" \
    "v_cvt_pk_bf16_f32 v52, v40, v41\nv_cvt_pk_bf16_f32 v54, v42, v43\nv_smfmac_f32_16x16x64_bf16 v[32:35], v[76:79], v[124:131], v0\nv_cvt_pk_bf16_f32 v53, v44, v45\n" \
    "v_smfmac_f32_16x16x64_bf16 v[36:39], v[76:79], v[140:147], v0\nds_read_b128 v[68:71], v3 offset:7168\nv_cvt_pk_bf16_f32 v55, v46, v47\n" \
    "v_smfmac_f32_16x16x64_bf16 v[32:35], v[56:59], v[132:139], v0\nds_read_b128 v[72:75], v10 offset:9216\nv_permlane16_swap_b32 v52, v53\n" \
    "v_smfmac_f32_16x16x64_bf16 v[36:39], v[56:59], v[148:155], v0\nds_read_b128 v[80:83], v10 offset:13312\nv_permlane16_swap_b32 v54, v55\n" \
    "v_smfmac_f32_16x16x64_bf16 v[32:35], v[60:63], v[156:163], v0\nbuffer_store_dwordx4 v[52:55], v8, s[64:67], s87 offen offset:768 sc1\nv_mov_b64 v[40:41], 0\n" \
    "v_smfmac_f32_16x16x64_bf16 v[36:39], v[60:63], v[172:179], v0\nv_mov_b64 v[42:43], 0\nv_smfmac_f32_16x16x64_bf16 v[32:35], v[64:67], v[164:171], v0\nv_mov_b64 v[44:45], 0\n" \
    "v_smfmac_f32_16x16x64_bf16 v[36:39], v[64:67], v[180:187], v0\ns_waitcnt lgkmcnt(0)\nv_mov_b64 v[46:47], 0\n" \
    "v_mfma_f32_16x16x32_bf16 v[16:19], a[28:31], v[72:75], v[16:19]\ns_nop 1\nv_mfma_f32_16x16x32_bf16 v[24:27], v[68:71], v[72:75], v[24:27]\nv_cvt_pk_bf16_f32 v48, v32, v33\n" \
    "v_cvt_pk_bf16_f32 v50, v34, v35\nv_smfmac_f32_16x16x64_bf16 v[40:43], v[76:79], v[188:195], v0\nv_cvt_pk_bf16_f32 v49, v36, v37\n" \
    "v_smfmac_f32_16x16x64_bf16 v[44:47], v[76:79], v[204:211], v0\nv_cvt_pk_bf16_f32 v51, v38, v39\nv_smfmac_f32_16x16x64_bf16 v[40:43], v[56:59], v[196:203], v0\n" \
    "v_permlane16_swap_b32 v48, v49\nv_smfmac_f32_16x16x64_bf16 v[44:47], v[56:59], v[212:219], v0\nds_read_b128 v[56:59], v3 offset:8192\nv_permlane16_swap_b32 v50, v51\n" \
    "v_smfmac_f32_16x16x64_bf16 v[40:43], v[60:63], v[220:227], v0\nds_read_b128 v[72:75], v9 offset:16384\n" \
    "buffer_store_dwordx4 v[48:51], v8, s[64:67], s88 offen offset:768 sc1\nv_smfmac_f32_16x16x64_bf16 v[44:47], v[60:63], v[236:243], v0\n" \
    "ds_read_b128 v[60:63], v9 offset:20480\nv_smfmac_f32_16x16x64_bf16 v[40:43], v[64:67], v[228:235], v0\nv_smfmac_f32_16x16x64_bf16 v[44:47], v[64:67], v[244:251], v0\n" \
    "ds_read_b128 v[64:67], v3 offset:9216\ns_nop 0\nv_mfma_f32_16x16x32_bf16 v[20:23], a[28:31], v[80:83], v[20:23]\nds_read_b128 v[76:79], v10 offset:16384\ns_nop 0\n" \
    "v_mfma_f32_16x16x32_bf16 v[28:31], v[68:71], v[80:83], v[28:31]\nds_read_b128 v[68:71], v10 offset:20480\ns_waitcnt lgkmcnt(3)\nv_cvt_pk_bf16_f32 v52, v40, v41\n" \
    "v_mfma_f32_16x16x32_bf16 v[16:19], a[32:35], v[72:75], v[16:19]\nv_cvt_pk_bf16_f32 v54, v42, v43\nv_cvt_pk_bf16_f32 v53, v44, v45\n" \
    "v_mfma_f32_16x16x32_bf16 v[24:27], v[56:59], v[72:75], v[24:27]\nds_read_b128 v[72:75], v3 offset:10240\nv_cvt_pk_bf16_f32 v55, v46, v47\nv_permlane16_swap_b32 v52, v53\n" \
    "v_mfma_f32_16x16x32_bf16 v[20:23], a[32:35], v[60:63], v[20:23]\nds_read_b128 v[80:83], v9 offset:17408\nv_permlane16_swap_b32 v54, v55\n" \
    "v_mfma_f32_16x16x32_bf16 v[28:31], v[56:59], v[60:63], v[28:31]\nds_read_b128 v[56:59], v9 offset:21504\n" \
    "buffer_store_dwordx4 v[52:55], v8, s[64:67], s89 offen offset:768 sc1\ns_waitcnt lgkmcnt(3)\nv_mfma_f32_16x16x32_bf16 v[16:19], a[36:39], v[76:79], v[16:19]\n" \
    "buffer_load_dword v13, off, s[64:67], s42 sc1\ns_and_b32 s69, s49, 255\nv_mfma_f32_16x16x32_bf16 v[24:27], v[64:67], v[76:79], v[24:27]\n" \
    "ds_read_b128 v[60:63], v3 offset:11264\ns_lshl_b32 s69, s69, 19\nv_mfma_f32_16x16x32_bf16 v[20:23], a[36:39], v[68:71], v[20:23]\nds_read_b128 v[76:79], v10 offset:17408\n" \
    "s_add_u32 s74, s63, s69\nv_mfma_f32_16x16x32_bf16 v[28:31], v[64:67], v[68:71], v[28:31]\nds_read_b128 v[64:67], v10 offset:21504\ns_cmp_lt_u32 s49, 64\n" \
    "s_cselect_b32 s74, s74, 0x7fff0000\ns_waitcnt lgkmcnt(3)\nv_mfma_f32_16x16x32_bf16 v[16:19], a[40:43], v[80:83], v[16:19]\n" \
    "v_mfma_f32_16x16x32_bf16 v[24:27], v[72:75], v[80:83], v[24:27]\nds_read_b128 v[68:71], v3 offset:12288\ns_waitcnt vmcnt(17)\ns_barrier\n" \
    "v_mfma_f32_16x16x32_bf16 v[20:23], a[40:43], v[56:59], v[20:23]\nds_read_b128 v[80:83], v9 offset:40960\nv_mfma_f32_16x16x32_bf16 v[28:31], v[72:75], v[56:59], v[28:31]\n" \
    "ds_read_b128 v[56:59], v9 offset:45056\ns_add_u32 m0, s41, 0\ns_waitcnt lgkmcnt(3)\nv_mfma_f32_16x16x32_bf16 v[16:19], a[44:47], v[76:79], v[16:19]\n" \
    "s_xor_b32 s85, s62, 1536\ns_add_u32 s85, s85, s40\nv_mfma_f32_16x16x32_bf16 v[24:27], v[60:63], v[76:79], v[24:27]\nds_read_b128 v[72:75], v3 offset:13312\n" \
    "buffer_load_dwordx4 v11, s[64:67], s85 offen sc1 lds\nv_mfma_f32_16x16x32_bf16 v[20:23], a[44:47], v[64:67], v[20:23]\nds_read_b128 v[76:79], v10 offset:40960\n" \
    "buffer_store_dwordx4 v[252:255], v2, s[64:67], s74 offen offset:0 sc1\nv_mfma_f32_16x16x32_bf16 v[28:31], v[60:63], v[64:67], v[28:31]\n" \
    "ds_read_b128 v[60:63], v10 offset:45056\ns_waitcnt lgkmcnt(3)\nv_mfma_f32_16x16x32_bf16 v[16:19], a[48:51], v[80:83], v[16:19]\n" \
    "buffer_load_dwordx4 v12, s[64:67], s85 offen offset:1024 sc1 lds\ns_add_u32 m0, s41, 8192\nv_mfma_f32_16x16x32_bf16 v[24:27], v[68:71], v[80:83], v[24:27]\n" \
    "ds_read_b128 v[64:67], v3 offset:14336\ns_xor_b32 s85, s62, 1792\nv_mfma_f32_16x16x32_bf16 v[20:23], a[48:51], v[56:59], v[20:23]\nds_read_b128 v[80:83], v9 offset:41984\n" \
    "s_add_u32 s85, s85, s40\nv_mfma_f32_16x16x32_bf16 v[28:31], v[68:71], v[56:59], v[28:31]\nds_read_b128 v[56:59], v9 offset:46080\n" \
    "buffer_load_dwordx4 v11, s[64:67], s85 offen sc1 lds\ns_waitcnt lgkmcnt(3)\nv_mfma_f32_16x16x32_bf16 v[16:19], a[52:55], v[76:79], v[16:19]\n" \
    "buffer_store_dwordx4 v[252:255], v2, s[64:67], s74 offen offset:1024 sc1\nv_mfma_f32_16x16x32_bf16 v[24:27], v[72:75], v[76:79], v[24:27]\n" \
    "ds_read_b128 v[68:71], v3 offset:15360\nv_mfma_f32_16x16x32_bf16 v[20:23], a[52:55], v[60:63], v[20:23]\nds_read_b128 v[76:79], v10 offset:41984\n" \
    "buffer_load_dwordx4 v12, s[64:67], s85 offen offset:1024 sc1 lds\nv_mfma_f32_16x16x32_bf16 v[28:31], v[72:75], v[60:63], v[28:31]\nds_read_b128 v[60:63], v10 offset:46080\n" \
    "s_waitcnt lgkmcnt(3)\nv_mfma_f32_16x16x32_bf16 v[16:19], a[56:59], v[80:83], v[16:19]\nbuffer_store_dwordx4 v[252:255], v7, s[64:67], s82 offen sc1\n" \
    "s_xor_b32 s82, s82, 0x10000\nv_mfma_f32_16x16x32_bf16 v[24:27], v[64:67], v[80:83], v[24:27]\nds_read_b128 v[72:75], v3 offset:16384\n" \
    "v_mfma_f32_16x16x32_bf16 v[20:23], a[56:59], v[56:59], v[20:23]\nds_read_b128 v[80:83], v9 offset:24576\nv_mfma_f32_16x16x32_bf16 v[28:31], v[64:67], v[56:59], v[28:31]\n" \
    "ds_read_b128 v[56:59], v9 offset:28672\ns_waitcnt lgkmcnt(3)\nv_mfma_f32_16x16x32_bf16 v[16:19], a[60:63], v[76:79], v[16:19]\n" \
    "buffer_store_dwordx4 v[252:255], v7, s[64:67], s82 offen sc1\ns_xor_b32 s82, s82, 0x30000\nv_mfma_f32_16x16x32_bf16 v[24:27], v[68:71], v[76:79], v[24:27]\n" \
    "ds_read_b128 v[64:67], v3 offset:17408\nv_mfma_f32_16x16x32_bf16 v[20:23], a[60:63], v[60:63], v[20:23]\nds_read_b128 v[76:79], v10 offset:24576\n" \
    "v_mfma_f32_16x16x32_bf16 v[28:31], v[68:71], v[60:63], v[28:31]\nds_read_b128 v[60:63], v10 offset:28672\ns_waitcnt lgkmcnt(3)\n" \
    "v_mfma_f32_16x16x32_bf16 v[16:19], a[64:67], v[80:83], v[16:19]\nbuffer_store_dwordx4 v[252:255], v7, s[64:67], s82 offen sc1\ns_xor_b32 s82, s82, 0x10000\n" \
    "v_mfma_f32_16x16x32_bf16 v[24:27], v[72:75], v[80:83], v[24:27]\nds_read_b128 v[68:71], v3 offset:18432\nv_mfma_f32_16x16x32_bf16 v[20:23], a[64:67], v[56:59], v[20:23]\n" \
    "ds_read_b128 v[80:83], v9 offset:25600\nv_mfma_f32_16x16x32_bf16 v[28:31], v[72:75], v[56:59], v[28:31]\nds_read_b128 v[56:59], v9 offset:29696\ns_waitcnt lgkmcnt(3)\n" \
    "v_mfma_f32_16x16x32_bf16 v[16:19], a[68:71], v[76:79], v[16:19]\nbuffer_store_dwordx4 v[252:255], v7, s[64:67], s82 offen sc1\ns_xor_b32 s82, s82, 0x30000\n" \
    "v_mfma_f32_16x16x32_bf16 v[24:27], v[64:67], v[76:79], v[24:27]\nds_read_b128 v[72:75], v3 offset:19456\ns_add_u32 s49, s49, 2\n" \
    "v_mfma_f32_16x16x32_bf16 v[20:23], a[68:71], v[60:63], v[20:23]\nds_read_b128 v[76:79], v10 offset:25600\ns_and_b32 s69, s35, 15\n" \
    "v_mfma_f32_16x16x32_bf16 v[28:31], v[64:67], v[60:63], v[28:31]\nds_read_b128 v[60:63], v10 offset:29696\ns_lshl_b32 s69, s69, 17\ns_waitcnt lgkmcnt(3)\n" \
    "v_mfma_f32_16x16x32_bf16 v[16:19], a[72:75], v[80:83], v[16:19]\ns_add_u32 s81, s37, s69\ns_lshr_b32 s69, s81, 8\n" \
    "v_mfma_f32_16x16x32_bf16 v[24:27], v[68:71], v[80:83], v[24:27]\nds_read_b128 v[64:67], v3 offset:20480\ns_and_b32 s69, s69, 0x2000\n" \
    "v_mfma_f32_16x16x32_bf16 v[20:23], a[72:75], v[56:59], v[20:23]\nds_read_b128 v[80:83], v9 offset:32768\ns_xor_b32 s81, s81, s69\n" \
    "v_mfma_f32_16x16x32_bf16 v[28:31], v[68:71], v[56:59], v[28:31]\nds_read_b128 v[56:59], v9 offset:36864\ns_mov_b32 s68, 0\ns_waitcnt lgkmcnt(3)\n" \
    "v_mfma_f32_16x16x32_bf16 v[16:19], a[76:79], v[76:79], v[16:19]\ns_waitcnt vmcnt(11)\nv_mov_b32 v15, s50\ns_mov_b64 exec, 1\n" \
    "buffer_store_dword v15, off, s[64:67], s48 sc1\ns_mov_b64 exec, -1\ns_xor_b32 s48, s48, 0x8000\nv_mfma_f32_16x16x32_bf16 v[24:27], v[72:75], v[76:79], v[24:27]\n" \
    "ds_read_b128 v[68:71], v3 offset:21504\nv_mfma_f32_16x16x32_bf16 v[20:23], a[76:79], v[60:63], v[20:23]\nds_read_b128 v[76:79], v10 offset:32768\n" \
    "v_mfma_f32_16x16x32_bf16 v[28:31], v[72:75], v[60:63], v[28:31]\nds_read_b128 v[60:63], v10 offset:36864\ns_waitcnt lgkmcnt(3)\n" \
    "v_mfma_f32_16x16x32_bf16 v[16:19], a[80:83], v[80:83], v[16:19]\nv_mfma_f32_16x16x32_bf16 v[24:27], v[64:67], v[80:83], v[24:27]\nds_read_b128 v[72:75], v3 offset:22528\n" \
    "v_mfma_f32_16x16x32_bf16 v[20:23], a[80:83], v[56:59], v[20:23]\nds_read_b128 v[80:83], v9 offset:33792\nv_mfma_f32_16x16x32_bf16 v[28:31], v[64:67], v[56:59], v[28:31]\n" \
    "ds_read_b128 v[56:59], v9 offset:37888\ns_waitcnt lgkmcnt(3)\nv_mfma_f32_16x16x32_bf16 v[16:19], a[84:87], v[76:79], v[16:19]\n" \
    "v_mfma_f32_16x16x32_bf16 v[24:27], v[68:71], v[76:79], v[24:27]\nds_read_b128 v[64:67], v3 offset:23552\nv_mfma_f32_16x16x32_bf16 v[20:23], a[84:87], v[60:63], v[20:23]\n" \
    "ds_read_b128 v[76:79], v10 offset:33792\nv_mfma_f32_16x16x32_bf16 v[28:31], v[68:71], v[60:63], v[28:31]\nds_read_b128 v[60:63], v10 offset:37888\ns_waitcnt lgkmcnt(3)\n" \
    "v_mfma_f32_16x16x32_bf16 v[16:19], a[88:91], v[80:83], v[16:19]\nv_mfma_f32_16x16x32_bf16 v[24:27], v[72:75], v[80:83], v[24:27]\ns_waitcnt vmcnt(5)\ns_barrier\n" \
    "v_mfma_f32_16x16x32_bf16 v[20:23], a[88:91], v[56:59], v[20:23]\nds_read_b128 v[68:71], v9 offset:0\nv_mfma_f32_16x16x32_bf16 v[28:31], v[72:75], v[56:59], v[28:31]\n" \
    "ds_read_b128 v[56:59], v9 offset:4096\ns_add_u32 m0, s41, 16384\ns_waitcnt lgkmcnt(2)\nv_mfma_f32_16x16x32_bf16 v[16:19], a[92:95], v[76:79], v[16:19]\n" \
    "s_xor_b32 s85, s62, 512\ns_add_u32 s85, s85, s84\nv_mfma_f32_16x16x32_bf16 v[24:27], v[64:67], v[76:79], v[24:27]\nbuffer_load_dwordx4 v11, s[64:67], s85 offen sc1 lds\n" \
    "v_mfma_f32_16x16x32_bf16 v[20:23], a[92:95], v[60:63], v[20:23]\nds_read_b128 v[72:75], v10 offset:0\nv_mfma_f32_16x16x32_bf16 v[28:31], v[64:67], v[60:63], v[28:31]\n" \
    "ds_read_b128 v[60:63], v10 offset:4096\ns_waitcnt lgkmcnt(2)\nv_mfma_f32_16x16x32_bf16 v[16:19], a[96:99], v[68:71], v[16:19]\n" \
    "buffer_load_dwordx4 v12, s[64:67], s85 offen offset:1024 sc1 lds\ns_add_u32 m0, s41, 40960\nv_mfma_f32_16x16x32_bf16 v[24:27], a[104:107], v[68:71], v[24:27]\n" \
    "s_xor_b32 s85, s62, 768\ns_add_u32 s85, s85, s84\nv_mfma_f32_16x16x32_bf16 v[20:23], a[96:99], v[56:59], v[20:23]\nds_read_b128 v[64:67], v9 offset:1024\n" \
    "buffer_load_dwordx4 v11, s[64:67], s85 offen sc1 lds\nv_mfma_f32_16x16x32_bf16 v[28:31], a[104:107], v[56:59], v[28:31]\nds_read_b128 v[56:59], v9 offset:5120\n" \
    "s_waitcnt lgkmcnt(2)\nv_mfma_f32_16x16x32_bf16 v[16:19], a[100:103], v[72:75], v[16:19]\nv_mfma_f32_16x16x32_bf16 v[24:27], a[108:111], v[72:75], v[24:27]\n" \
    "buffer_load_dwordx4 v12, s[64:67], s85 offen offset:1024 sc1 lds\nv_mfma_f32_16x16x32_bf16 v[20:23], a[100:103], v[60:63], v[20:23]\n" \
    "ds_read_b128 v[68:71], v10 offset:1024\nv_mfma_f32_16x16x32_bf16 v[28:31], a[108:111], v[60:63], v[28:31]\nds_read_b128 v[60:63], v10 offset:5120\ns_waitcnt lgkmcnt(2)\n" \
    "v_mfma_f32_16x16x32_bf16 v[16:19], a[112:115], v[64:67], v[16:19]\nv_mfma_f32_16x16x32_bf16 v[24:27], a[120:123], v[64:67], v[24:27]\n" \
    "v_mfma_f32_16x16x32_bf16 v[20:23], a[112:115], v[56:59], v[20:23]\nds_read_b128 v[64:67], v9 offset:8192\n" \
    "v_mfma_f32_16x16x32_bf16 v[28:31], a[120:123], v[56:59], v[28:31]\nds_read_b128 v[56:59], v9 offset:12288\ns_waitcnt lgkmcnt(2)\n" \
    "v_mfma_f32_16x16x32_bf16 v[16:19], a[116:119], v[68:71], v[16:19]\nv_mfma_f32_16x16x32_bf16 v[24:27], a[124:127], v[68:71], v[24:27]\n" \
    "v_mfma_f32_16x16x32_bf16 v[20:23], a[116:119], v[60:63], v[20:23]\nds_read_b128 v[68:71], v10 offset:8192\n" \
    "v_mfma_f32_16x16x32_bf16 v[28:31], a[124:127], v[60:63], v[28:31]\nds_read_b128 v[60:63], v10 offset:12288\ns_waitcnt lgkmcnt(2)\n" \
    "v_mfma_f32_16x16x32_bf16 v[16:19], a[128:131], v[64:67], v[16:19]\nv_readfirstlane_b32 s74, v13\nv_mfma_f32_16x16x32_bf16 v[24:27], a[136:139], v[64:67], v[24:27]\n" \
    "s_and_b32 s75, s74, 127\ns_lshr_b32 s69, s74, 8\nv_mfma_f32_16x16x32_bf16 v[20:23], a[128:131], v[56:59], v[20:23]\nds_read_b128 v[64:67], v9 offset:9216\n" \
    "s_sub_u32 s69, s69, s80\nv_mfma_f32_16x16x32_bf16 v[28:31], a[136:139], v[56:59], v[28:31]\nds_read_b128 v[56:59], v9 offset:13312\ns_bfe_i32 s69, s69, 0x180000\n" \
    "s_waitcnt lgkmcnt(2)\nv_mfma_f32_16x16x32_bf16 v[16:19], a[132:135], v[68:71], v[16:19]\nds_read_b128 v[72:75], v10 offset:9216\ns_cmp_eq_u32 s75, 0\n" \
    "s_cselect_b32 s69, 0, s69\nv_mfma_f32_16x16x32_bf16 v[24:27], a[140:143], v[68:71], v[24:27]\nds_read_b128 v[68:71], v10 offset:13312\ns_add_u32 s80, s80, s69\n" \
    "v_mfma_f32_16x16x32_bf16 v[20:23], a[132:135], v[60:63], v[20:23]\ns_lshl_b32 s74, s74, 5\ns_and_b32 s74, s74, 0x1000\n" \
    "v_mfma_f32_16x16x32_bf16 v[28:31], a[140:143], v[60:63], v[28:31]\ns_andn2_b32 s81, s81, 0x1000\ns_or_b32 s81, s81, s74\ns_waitcnt lgkmcnt(2)\n" \
    "v_mfma_f32_16x16x32_bf16 v[16:19], a[144:147], v[64:67], v[16:19]\nv_mfma_f32_16x16x32_bf16 v[24:27], a[152:155], v[64:67], v[24:27]\ns_waitcnt lgkmcnt(0)\ns_barrier\n" \
    "v_mfma_f32_16x16x32_bf16 v[16:19], a[148:151], v[72:75], v[16:19]\ns_add_u32 m0, s41, 0\ns_xor_b32 s85, s62, 0\n" \
    "v_mfma_f32_16x16x32_bf16 v[24:27], a[156:159], v[72:75], v[24:27]\ns_add_u32 s85, s85, s84\nbuffer_load_dwordx4 v11, s[64:67], s85 offen sc1 lds\n" \
    "v_mfma_f32_16x16x32_bf16 v[20:23], a[144:147], v[56:59], v[20:23]\ns_nop 1\nv_cvt_pk_bf16_f32 v32, v16, v17\nv_cvt_pk_bf16_f32 v33, v18, v19\n" \
    "v_mfma_f32_16x16x32_bf16 v[28:31], a[152:155], v[56:59], v[28:31]\nv_cvt_pk_bf16_f32 v34, v24, v25\nv_cvt_pk_bf16_f32 v35, v26, v27\n" \
    "v_mfma_f32_16x16x32_bf16 v[20:23], a[148:151], v[68:71], v[20:23]\nbuffer_load_dwordx4 v12, s[64:67], s85 offen offset:1024 sc1 lds\ns_add_u32 m0, s41, 8192\n" \
    "v_mfma_f32_16x16x32_bf16 v[28:31], a[156:159], v[68:71], v[28:31]\ns_xor_b32 s85, s62, 256\ns_add_u32 s85, s85, s84\nds_write_b128 v14, v[32:35] offset:0\n" \
    "buffer_load_dwordx4 v11, s[64:67], s85 offen sc1 lds\nbuffer_load_dwordx4 v12, s[64:67], s85 offen offset:1024 sc1 lds\ns_mov_b32 s40, s84\ns_sub_u32 s69, s50, s35\n" \
    "s_max_u32 s76, s35, 1\ns_add_u32 s69, s69, s76\ns_sub_u32 s69, s69, s35\ns_cmp_eq_u32 s69, 2\ns_cbranch_scc1 .Lv12_%=\n.Lv4_%=:\n.Lv13_%=:\ns_cmp_lt_u32 s75, s35\n" \
    "s_cbranch_scc1 .Lv14_%=\ns_cmp_eq_u32 s36, 1\ns_cbranch_scc1 .Lv9_%=\ns_cmp_eq_u32 s36, 2\ns_cbranch_scc1 .Lv10_%=\ns_cmp_eq_u32 s36, 3\ns_cbranch_scc1 .Lv11_%=\n.Lv8_%=:\n" \
    ".Lv15_%=:\ns_memrealtime s[72:73]\ns_waitcnt lgkmcnt(0)\ns_mov_b32 s68, 0\ns_sub_u32 s76, s80, s45\ns_sub_u32 s69, s72, s76\ns_cmp_lt_i32 s69, -8\ns_cbranch_scc1 .Lv18_%=\n" \
    "s_xor_b32 s90, s81, 2048\n.Lv20_%=:\ns_memrealtime s[72:73]\ns_waitcnt lgkmcnt(0)\ns_sub_u32 s72, s72, s80\ns_cmp_lt_i32 s72, 0\ns_cbranch_scc1 .Lv20_%=\n" \
    "buffer_load_dwordx4 v[72:75], v2, s[64:67], s81 offen offset:0 sc1\nv_cvt_pk_bf16_f32 v36, v20, v21\nv_cvt_pk_bf16_f32 v37, v22, v23\nv_cvt_pk_bf16_f32 v38, v28, v29\n" \
    "buffer_load_dwordx4 v[76:79], v2, s[64:67], s81 offen offset:1024 sc1\nv_cvt_pk_bf16_f32 v39, v30, v31\nds_write_b128 v14, v[36:39] offset:4096\n" \
    "ds_read_b128 v[24:27], v3 offset:0\nbuffer_load_dwordx4 v[80:83], v2, s[64:67], s90 offen offset:0 sc1\nds_read_b128 v[28:31], v3 offset:1024\n" \
    "ds_read_b128 v[32:35], v3 offset:8192\nds_read_b128 v[36:39], v3 offset:9216\nbuffer_load_dwordx4 v[84:87], v2, s[64:67], s90 offen offset:1024 sc1\n" \
    "ds_read_b128 v[40:43], v3 offset:16384\nds_read_b128 v[44:47], v3 offset:17408\nds_read_b128 v[48:51], v3 offset:2048\nds_read_b128 v[52:55], v3 offset:3072\n" \
    "ds_read_b128 v[56:59], v3 offset:4096\nds_read_b128 v[60:63], v3 offset:5120\nds_read_b128 v[64:67], v3 offset:6144\nds_read_b128 v[68:71], v3 offset:7168\n.Lv19_%=:\n" \
    "v_mov_b64 v[16:17], 0\nv_mov_b64 v[18:19], 0\nv_mov_b64 v[20:21], 0\nv_mov_b64 v[22:23], 0\ns_waitcnt vmcnt(3) lgkmcnt(10)\ns_nop 0\n" \
    "v_smfmac_f32_16x16x64_bf16 v[20:23], v[72:75], v[24:31], v0\nds_write_b128 v1, v[72:75] offset:24576\nv_max3_u32 v252, v72, v73, v74\n" \
    "v_smfmac_f32_16x16x64_bf16 v[16:19], v[72:75], a[0:7], v0\ns_waitcnt vmcnt(2)\nds_write_b128 v1, v[76:79] offset:28672\ns_waitcnt lgkmcnt(10)\n" \
    "v_max3_u32 v252, v252, v75, v76\nv_max3_u32 v252, v252, v77, v78\nv_cmp_u_f32 vcc, v252, v79\nv_smfmac_f32_16x16x64_bf16 v[20:23], v[76:79], v[32:39], v0\n" \
    "s_cbranch_vccnz .Lv26_%=\n.Lv22_%=:\nv_smfmac_f32_16x16x64_bf16 v[16:19], v[76:79], a[32:39], v0\nds_read_b128 v[24:27], v3 offset:10240\n" \
    "ds_read_b128 v[28:31], v3 offset:11264\ns_waitcnt vmcnt(1)\nv_max3_u32 v252, v80, v81, v82\nv_cmp_u_f32 vcc, v252, v83\ns_nop 0\ns_cbranch_vccnz .Lv27_%=\n.Lv23_%=:\n" \
    "s_waitcnt lgkmcnt(10)\nv_smfmac_f32_16x16x64_bf16 v[20:23], v[80:83], v[40:47], v0\nds_write_b128 v1, v[80:83] offset:32768\nds_read_b128 v[36:39], v3 offset:13312\n" \
    "s_waitcnt lgkmcnt(4)\ns_barrier\nds_read_b128 v[88:91], v1 offset:25600\nds_read_b128 v[40:43], v1 offset:26624\n" \
    "v_smfmac_f32_16x16x64_bf16 v[16:19], v[80:83], a[64:71], v0\nds_read_b128 v[32:35], v3 offset:12288\nds_read_b128 v[44:47], v1 offset:27648\ns_waitcnt vmcnt(0)\n" \
    "v_max3_u32 v252, v84, v85, v86\nv_cmp_u_f32 vcc, v252, v87\ns_nop 0\ns_cbranch_vccnz .Lv28_%=\n.Lv24_%=:\nv_smfmac_f32_16x16x64_bf16 v[20:23], v[84:87], a[104:111], v0\n" \
    "ds_write_b128 v1, v[84:87] offset:36864\nv_smfmac_f32_16x16x64_bf16 v[16:19], v[84:87], a[96:103], v0\nds_read_b128 v[72:75], v1 offset:29696\ns_waitcnt lgkmcnt(5)\n" \
    "v_smfmac_f32_16x16x64_bf16 v[20:23], v[88:91], v[48:55], v0\nds_read_b128 v[48:51], v1 offset:30720\nv_smfmac_f32_16x16x64_bf16 v[16:19], v[88:91], a[8:15], v0\n" \
    "ds_read_b128 v[52:55], v1 offset:31744\ns_waitcnt lgkmcnt(6)\nv_smfmac_f32_16x16x64_bf16 v[20:23], v[40:43], v[56:63], v0\nds_read_b128 v[56:59], v3 offset:14336\n" \
    "s_waitcnt lgkmcnt(4)\ns_barrier\nv_smfmac_f32_16x16x64_bf16 v[16:19], v[40:43], a[16:23], v0\nds_read_b128 v[60:63], v3 offset:15360\n" \
    "v_smfmac_f32_16x16x64_bf16 v[20:23], v[44:47], v[64:71], v0\nds_read_b128 v[40:43], v1 offset:33792\nv_smfmac_f32_16x16x64_bf16 v[16:19], v[44:47], a[24:31], v0\n" \
    "ds_read_b128 v[64:67], v3 offset:18432\ns_waitcnt lgkmcnt(6)\nv_smfmac_f32_16x16x64_bf16 v[20:23], v[72:75], v[24:31], v0\nds_read_b128 v[68:71], v3 offset:19456\n" \
    "v_smfmac_f32_16x16x64_bf16 v[16:19], v[72:75], a[40:47], v0\nds_read_b128 v[44:47], v1 offset:34816\ns_waitcnt lgkmcnt(7)\n" \
    "v_smfmac_f32_16x16x64_bf16 v[20:23], v[48:51], v[32:39], v0\nds_read_b128 v[24:27], v3 offset:20480\nv_smfmac_f32_16x16x64_bf16 v[16:19], v[48:51], a[48:55], v0\n" \
    "ds_read_b128 v[28:31], v3 offset:21504\ns_waitcnt lgkmcnt(6)\nv_smfmac_f32_16x16x64_bf16 v[20:23], v[52:55], v[56:63], v0\nds_read_b128 v[48:51], v1 offset:35840\n" \
    "v_smfmac_f32_16x16x64_bf16 v[16:19], v[52:55], a[56:63], v0\nds_read_b128 v[32:35], v3 offset:22528\ns_waitcnt lgkmcnt(5)\n" \
    "v_smfmac_f32_16x16x64_bf16 v[20:23], v[40:43], v[64:71], v0\nds_read_b128 v[36:39], v3 offset:23552\nv_smfmac_f32_16x16x64_bf16 v[16:19], v[40:43], a[72:79], v0\n" \
    "ds_read_b128 v[40:43], v1 offset:37888\ns_waitcnt lgkmcnt(4)\nv_smfmac_f32_16x16x64_bf16 v[20:23], v[44:47], v[24:31], v0\nds_read_b128 v[24:27], v1 offset:38912\n" \
    "v_smfmac_f32_16x16x64_bf16 v[16:19], v[44:47], a[80:87], v0\nds_read_b128 v[28:31], v1 offset:39936\ns_waitcnt lgkmcnt(3)\n" \
    "v_smfmac_f32_16x16x64_bf16 v[20:23], v[48:51], v[32:39], v0\ns_waitcnt lgkmcnt(2)\nv_smfmac_f32_16x16x64_bf16 v[20:23], v[40:43], a[120:127], v0\ns_waitcnt lgkmcnt(1)\n" \
    "v_smfmac_f32_16x16x64_bf16 v[20:23], v[24:27], a[136:143], v0\ns_waitcnt lgkmcnt(0)\nv_smfmac_f32_16x16x64_bf16 v[20:23], v[28:31], a[152:159], v0\ns_nop 2\n" \
    "v_smfmac_f32_16x16x64_bf16 v[16:19], v[48:51], a[88:95], v0\ns_nop 2\nv_smfmac_f32_16x16x64_bf16 v[16:19], v[40:43], a[112:119], v0\nv_permlane32_swap_b32 v20, v22\n" \
    "v_smfmac_f32_16x16x64_bf16 v[16:19], v[24:27], a[128:135], v0\nv_permlane32_swap_b32 v21, v23\nv_smfmac_f32_16x16x64_bf16 v[16:19], v[28:31], a[144:151], v0\n" \
    "v_add_f32 v20, v20, v22\nv_add_f32 v21, v21, v23\nv_cvt_pk_bf16_f32 v20, v20, v21\nds_write_b16 v4, v20 offset:8\nds_write_b16_d16_hi v4, v20 offset:24\n" \
    "v_mov_b32 v15, v15\nv_mov_b32 v15, v15\nv_mov_b32 v15, v15\nv_permlane32_swap_b32 v16, v18\nv_permlane32_swap_b32 v17, v19\nv_pk_add_f32 v[16:17], v[16:17], v[18:19]\n" \
    "v_cvt_pk_bf16_f32 v16, v16, v17\nds_write_b16 v4, v16 offset:0\nds_write_b16_d16_hi v4, v16 offset:16\ns_branch .Lv7_%=\n.Lv9_%=:\n.Lv31_%=:\ns_memrealtime s[72:73]\n" \
    "s_waitcnt lgkmcnt(0)\ns_mov_b32 s68, 0\ns_sub_u32 s76, s80, s45\ns_sub_u32 s69, s72, s76\ns_cmp_lt_i32 s69, -8\ns_cbranch_scc1 .Lv34_%=\ns_xor_b32 s90, s81, 2048\n" \
    ".Lv36_%=:\ns_memrealtime s[72:73]\ns_waitcnt lgkmcnt(0)\ns_sub_u32 s72, s72, s80\ns_cmp_lt_i32 s72, 0\ns_cbranch_scc1 .Lv36_%=\n" \
    "buffer_load_dwordx4 v[72:75], v2, s[64:67], s81 offen offset:0 sc1\nv_cvt_pk_bf16_f32 v36, v20, v21\nv_cvt_pk_bf16_f32 v37, v22, v23\nv_cvt_pk_bf16_f32 v38, v28, v29\n" \
    "buffer_load_dwordx4 v[76:79], v2, s[64:67], s81 offen offset:1024 sc1\nv_cvt_pk_bf16_f32 v39, v30, v31\nds_write_b128 v14, v[36:39] offset:4096\n" \
    "ds_read_b128 v[24:27], v3 offset:2048\nbuffer_load_dwordx4 v[80:83], v2, s[64:67], s90 offen offset:0 sc1\nds_read_b128 v[28:31], v3 offset:3072\n" \
    "ds_read_b128 v[32:35], v3 offset:10240\nds_read_b128 v[36:39], v3 offset:11264\nbuffer_load_dwordx4 v[84:87], v2, s[64:67], s90 offen offset:1024 sc1\n" \
    "ds_read_b128 v[40:43], v3 offset:18432\nds_read_b128 v[44:47], v3 offset:19456\nds_read_b128 v[48:51], v3 offset:0\nds_read_b128 v[52:55], v3 offset:1024\n" \
    "ds_read_b128 v[56:59], v3 offset:4096\nds_read_b128 v[60:63], v3 offset:5120\nds_read_b128 v[64:67], v3 offset:6144\nds_read_b128 v[68:71], v3 offset:7168\n.Lv35_%=:\n" \
    "v_mov_b64 v[16:17], 0\nv_mov_b64 v[18:19], 0\nv_mov_b64 v[20:21], 0\nv_mov_b64 v[22:23], 0\ns_waitcnt vmcnt(3) lgkmcnt(10)\ns_nop 0\n" \
    "v_smfmac_f32_16x16x64_bf16 v[20:23], v[72:75], v[24:31], v0\nds_write_b128 v1, v[72:75] offset:25600\nv_max3_u32 v252, v72, v73, v74\n" \
    "v_smfmac_f32_16x16x64_bf16 v[16:19], v[72:75], a[8:15], v0\ns_waitcnt vmcnt(2)\nds_write_b128 v1, v[76:79] offset:29696\ns_waitcnt lgkmcnt(10)\n" \
    "v_max3_u32 v252, v252, v75, v76\nv_max3_u32 v252, v252, v77, v78\nv_cmp_u_f32 vcc, v252, v79\nv_smfmac_f32_16x16x64_bf16 v[20:23], v[76:79], v[32:39], v0\n" \
    "s_cbranch_vccnz .Lv42_%=\n.Lv38_%=:\nv_smfmac_f32_16x16x64_bf16 v[16:19], v[76:79], a[40:47], v0\nds_read_b128 v[24:27], v3 offset:8192\n" \
    "ds_read_b128 v[28:31], v3 offset:9216\ns_waitcnt vmcnt(1)\nv_max3_u32 v252, v80, v81, v82\nv_cmp_u_f32 vcc, v252, v83\ns_nop 0\ns_cbranch_vccnz .Lv43_%=\n.Lv39_%=:\n" \
    "s_waitcnt lgkmcnt(10)\nv_smfmac_f32_16x16x64_bf16 v[20:23], v[80:83], v[40:47], v0\nds_write_b128 v1, v[80:83] offset:33792\nds_read_b128 v[36:39], v3 offset:13312\n" \
    "s_waitcnt lgkmcnt(4)\ns_barrier\nds_read_b128 v[88:91], v1 offset:24576\nds_read_b128 v[40:43], v1 offset:26624\n" \
    "v_smfmac_f32_16x16x64_bf16 v[16:19], v[80:83], a[72:79], v0\nds_read_b128 v[32:35], v3 offset:12288\nds_read_b128 v[44:47], v1 offset:27648\ns_waitcnt vmcnt(0)\n" \
    "v_max3_u32 v252, v84, v85, v86\nv_cmp_u_f32 vcc, v252, v87\ns_nop 0\ns_cbranch_vccnz .Lv44_%=\n.Lv40_%=:\nv_smfmac_f32_16x16x64_bf16 v[20:23], v[84:87], a[120:127], v0\n" \
    "ds_write_b128 v1, v[84:87] offset:37888\nv_smfmac_f32_16x16x64_bf16 v[16:19], v[84:87], a[112:119], v0\nds_read_b128 v[72:75], v1 offset:28672\ns_waitcnt lgkmcnt(5)\n" \
    "v_smfmac_f32_16x16x64_bf16 v[20:23], v[88:91], v[48:55], v0\nds_read_b128 v[48:51], v1 offset:30720\nv_smfmac_f32_16x16x64_bf16 v[16:19], v[88:91], a[0:7], v0\n" \
    "ds_read_b128 v[52:55], v1 offset:31744\ns_waitcnt lgkmcnt(6)\nv_smfmac_f32_16x16x64_bf16 v[20:23], v[40:43], v[56:63], v0\nds_read_b128 v[56:59], v3 offset:14336\n" \
    "s_waitcnt lgkmcnt(4)\ns_barrier\nv_smfmac_f32_16x16x64_bf16 v[16:19], v[40:43], a[16:23], v0\nds_read_b128 v[60:63], v3 offset:15360\n" \
    "v_smfmac_f32_16x16x64_bf16 v[20:23], v[44:47], v[64:71], v0\nds_read_b128 v[40:43], v1 offset:32768\nv_smfmac_f32_16x16x64_bf16 v[16:19], v[44:47], a[24:31], v0\n" \
    "ds_read_b128 v[64:67], v3 offset:16384\ns_waitcnt lgkmcnt(6)\nv_smfmac_f32_16x16x64_bf16 v[20:23], v[72:75], v[24:31], v0\nds_read_b128 v[68:71], v3 offset:17408\n" \
    "v_smfmac_f32_16x16x64_bf16 v[16:19], v[72:75], a[32:39], v0\nds_read_b128 v[44:47], v1 offset:34816\ns_waitcnt lgkmcnt(7)\n" \
    "v_smfmac_f32_16x16x64_bf16 v[20:23], v[48:51], v[32:39], v0\nds_read_b128 v[24:27], v3 offset:20480\nv_smfmac_f32_16x16x64_bf16 v[16:19], v[48:51], a[48:55], v0\n" \
    "ds_read_b128 v[28:31], v3 offset:21504\ns_waitcnt lgkmcnt(6)\nv_smfmac_f32_16x16x64_bf16 v[20:23], v[52:55], v[56:63], v0\nds_read_b128 v[48:51], v1 offset:35840\n" \
    "v_smfmac_f32_16x16x64_bf16 v[16:19], v[52:55], a[56:63], v0\nds_read_b128 v[32:35], v3 offset:22528\ns_waitcnt lgkmcnt(5)\n" \
    "v_smfmac_f32_16x16x64_bf16 v[20:23], v[40:43], v[64:71], v0\nds_read_b128 v[36:39], v3 offset:23552\nv_smfmac_f32_16x16x64_bf16 v[16:19], v[40:43], a[64:71], v0\n" \
    "ds_read_b128 v[40:43], v1 offset:36864\ns_waitcnt lgkmcnt(4)\nv_smfmac_f32_16x16x64_bf16 v[20:23], v[44:47], v[24:31], v0\nds_read_b128 v[24:27], v1 offset:38912\n" \
    "v_smfmac_f32_16x16x64_bf16 v[16:19], v[44:47], a[80:87], v0\nds_read_b128 v[28:31], v1 offset:39936\ns_waitcnt lgkmcnt(3)\n" \
    "v_smfmac_f32_16x16x64_bf16 v[20:23], v[48:51], v[32:39], v0\ns_waitcnt lgkmcnt(2)\nv_smfmac_f32_16x16x64_bf16 v[20:23], v[40:43], a[104:111], v0\ns_waitcnt lgkmcnt(1)\n" \
    "v_smfmac_f32_16x16x64_bf16 v[20:23], v[24:27], a[136:143], v0\ns_waitcnt lgkmcnt(0)\nv_smfmac_f32_16x16x64_bf16 v[20:23], v[28:31], a[152:159], v0\ns_nop 2\n" \
    "v_smfmac_f32_16x16x64_bf16 v[16:19], v[48:51], a[88:95], v0\ns_nop 2\nv_smfmac_f32_16x16x64_bf16 v[16:19], v[40:43], a[96:103], v0\nv_permlane32_swap_b32 v20, v22\n" \
    "v_smfmac_f32_16x16x64_bf16 v[16:19], v[24:27], a[128:135], v0\nv_permlane32_swap_b32 v21, v23\nv_smfmac_f32_16x16x64_bf16 v[16:19], v[28:31], a[144:151], v0\n" \
    "v_add_f32 v20, v20, v22\nv_add_f32 v21, v21, v23\nv_cvt_pk_bf16_f32 v20, v20, v21\nds_write_b16 v4, v20 offset:8\nds_write_b16_d16_hi v4, v20 offset:24\n" \
    "v_mov_b32 v15, v15\nv_mov_b32 v15, v15\nv_mov_b32 v15, v15\nv_permlane32_swap_b32 v16, v18\nv_permlane32_swap_b32 v17, v19\nv_pk_add_f32 v[16:17], v[16:17], v[18:19]\n" \
    "v_cvt_pk_bf16_f32 v16, v16, v17\nds_write_b16 v4, v16 offset:0\nds_write_b16_d16_hi v4, v16 offset:16\ns_branch .Lv7_%=\n.Lv10_%=:\n.Lv47_%=:\ns_memrealtime s[72:73]\n" \
    "s_waitcnt lgkmcnt(0)\ns_mov_b32 s68, 0\ns_sub_u32 s76, s80, s45\ns_sub_u32 s69, s72, s76\ns_cmp_lt_i32 s69, -8\ns_cbranch_scc1 .Lv50_%=\ns_xor_b32 s90, s81, 2048\n" \
    ".Lv52_%=:\ns_memrealtime s[72:73]\ns_waitcnt lgkmcnt(0)\ns_sub_u32 s72, s72, s80\ns_cmp_lt_i32 s72, 0\ns_cbranch_scc1 .Lv52_%=\n" \
    "buffer_load_dwordx4 v[72:75], v2, s[64:67], s81 offen offset:0 sc1\nv_cvt_pk_bf16_f32 v36, v20, v21\nv_cvt_pk_bf16_f32 v37, v22, v23\nv_cvt_pk_bf16_f32 v38, v28, v29\n" \
    "buffer_load_dwordx4 v[76:79], v2, s[64:67], s81 offen offset:1024 sc1\nv_cvt_pk_bf16_f32 v39, v30, v31\nds_write_b128 v14, v[36:39] offset:4096\n" \
    "ds_read_b128 v[24:27], v3 offset:4096\nbuffer_load_dwordx4 v[80:83], v2, s[64:67], s90 offen offset:0 sc1\nds_read_b128 v[28:31], v3 offset:5120\n" \
    "ds_read_b128 v[32:35], v3 offset:12288\nds_read_b128 v[36:39], v3 offset:13312\nbuffer_load_dwordx4 v[84:87], v2, s[64:67], s90 offen offset:1024 sc1\n" \
    "ds_read_b128 v[40:43], v3 offset:20480\nds_read_b128 v[44:47], v3 offset:21504\nds_read_b128 v[48:51], v3 offset:0\nds_read_b128 v[52:55], v3 offset:1024\n" \
    "ds_read_b128 v[56:59], v3 offset:2048\nds_read_b128 v[60:63], v3 offset:3072\nds_read_b128 v[64:67], v3 offset:6144\nds_read_b128 v[68:71], v3 offset:7168\n.Lv51_%=:\n" \
    "v_mov_b64 v[16:17], 0\nv_mov_b64 v[18:19], 0\nv_mov_b64 v[20:21], 0\nv_mov_b64 v[22:23], 0\ns_waitcnt vmcnt(3) lgkmcnt(10)\ns_nop 0\n" \
    "v_smfmac_f32_16x16x64_bf16 v[20:23], v[72:75], v[24:31], v0\nds_write_b128 v1, v[72:75] offset:26624\nv_max3_u32 v252, v72, v73, v74\n" \
    "v_smfmac_f32_16x16x64_bf16 v[16:19], v[72:75], a[16:23], v0\ns_waitcnt vmcnt(2)\nds_write_b128 v1, v[76:79] offset:30720\ns_waitcnt lgkmcnt(10)\n" \
    "v_max3_u32 v252, v252, v75, v76\nv_max3_u32 v252, v252, v77, v78\nv_cmp_u_f32 vcc, v252, v79\nv_smfmac_f32_16x16x64_bf16 v[20:23], v[76:79], v[32:39], v0\n" \
    "s_cbranch_vccnz .Lv58_%=\n.Lv54_%=:\nv_smfmac_f32_16x16x64_bf16 v[16:19], v[76:79], a[48:55], v0\nds_read_b128 v[24:27], v3 offset:8192\n" \
    "ds_read_b128 v[28:31], v3 offset:9216\ns_waitcnt vmcnt(1)\nv_max3_u32 v252, v80, v81, v82\nv_cmp_u_f32 vcc, v252, v83\ns_nop 0\ns_cbranch_vccnz .Lv59_%=\n.Lv55_%=:\n" \
    "s_waitcnt lgkmcnt(10)\nv_smfmac_f32_16x16x64_bf16 v[20:23], v[80:83], v[40:47], v0\nds_write_b128 v1, v[80:83] offset:34816\nds_read_b128 v[36:39], v3 offset:11264\n" \
    "s_waitcnt lgkmcnt(4)\ns_barrier\nds_read_b128 v[88:91], v1 offset:24576\nds_read_b128 v[40:43], v1 offset:25600\n" \
    "v_smfmac_f32_16x16x64_bf16 v[16:19], v[80:83], a[80:87], v0\nds_read_b128 v[32:35], v3 offset:10240\nds_read_b128 v[44:47], v1 offset:27648\ns_waitcnt vmcnt(0)\n" \
    "v_max3_u32 v252, v84, v85, v86\nv_cmp_u_f32 vcc, v252, v87\ns_nop 0\ns_cbranch_vccnz .Lv60_%=\n.Lv56_%=:\nv_smfmac_f32_16x16x64_bf16 v[20:23], v[84:87], a[136:143], v0\n" \
    "ds_write_b128 v1, v[84:87] offset:38912\nv_smfmac_f32_16x16x64_bf16 v[16:19], v[84:87], a[128:135], v0\nds_read_b128 v[72:75], v1 offset:28672\ns_waitcnt lgkmcnt(5)\n" \
    "v_smfmac_f32_16x16x64_bf16 v[20:23], v[88:91], v[48:55], v0\nds_read_b128 v[48:51], v1 offset:29696\nv_smfmac_f32_16x16x64_bf16 v[16:19], v[88:91], a[0:7], v0\n" \
    "ds_read_b128 v[52:55], v1 offset:31744\ns_waitcnt lgkmcnt(6)\nv_smfmac_f32_16x16x64_bf16 v[20:23], v[40:43], v[56:63], v0\nds_read_b128 v[56:59], v3 offset:14336\n" \
    "s_waitcnt lgkmcnt(4)\ns_barrier\nv_smfmac_f32_16x16x64_bf16 v[16:19], v[40:43], a[8:15], v0\nds_read_b128 v[60:63], v3 offset:15360\n" \
    "v_smfmac_f32_16x16x64_bf16 v[20:23], v[44:47], v[64:71], v0\nds_read_b128 v[40:43], v1 offset:32768\nv_smfmac_f32_16x16x64_bf16 v[16:19], v[44:47], a[24:31], v0\n" \
    "ds_read_b128 v[64:67], v3 offset:16384\ns_waitcnt lgkmcnt(6)\nv_smfmac_f32_16x16x64_bf16 v[20:23], v[72:75], v[24:31], v0\nds_read_b128 v[68:71], v3 offset:17408\n" \
    "v_smfmac_f32_16x16x64_bf16 v[16:19], v[72:75], a[32:39], v0\nds_read_b128 v[44:47], v1 offset:33792\ns_waitcnt lgkmcnt(7)\n" \
    "v_smfmac_f32_16x16x64_bf16 v[20:23], v[48:51], v[32:39], v0\nds_read_b128 v[24:27], v3 offset:18432\nv_smfmac_f32_16x16x64_bf16 v[16:19], v[48:51], a[40:47], v0\n" \
    "ds_read_b128 v[28:31], v3 offset:19456\ns_waitcnt lgkmcnt(6)\nv_smfmac_f32_16x16x64_bf16 v[20:23], v[52:55], v[56:63], v0\nds_read_b128 v[48:51], v1 offset:35840\n" \
    "v_smfmac_f32_16x16x64_bf16 v[16:19], v[52:55], a[56:63], v0\nds_read_b128 v[32:35], v3 offset:22528\ns_waitcnt lgkmcnt(5)\n" \
    "v_smfmac_f32_16x16x64_bf16 v[20:23], v[40:43], v[64:71], v0\nds_read_b128 v[36:39], v3 offset:23552\nv_smfmac_f32_16x16x64_bf16 v[16:19], v[40:43], a[64:71], v0\n" \
    "ds_read_b128 v[40:43], v1 offset:36864\ns_waitcnt lgkmcnt(4)\nv_smfmac_f32_16x16x64_bf16 v[20:23], v[44:47], v[24:31], v0\nds_read_b128 v[24:27], v1 offset:37888\n" \
    "v_smfmac_f32_16x16x64_bf16 v[16:19], v[44:47], a[72:79], v0\nds_read_b128 v[28:31], v1 offset:39936\ns_waitcnt lgkmcnt(3)\n" \
    "v_smfmac_f32_16x16x64_bf16 v[20:23], v[48:51], v[32:39], v0\ns_waitcnt lgkmcnt(2)\nv_smfmac_f32_16x16x64_bf16 v[20:23], v[40:43], a[104:111], v0\ns_waitcnt lgkmcnt(1)\n" \
    "v_smfmac_f32_16x16x64_bf16 v[20:23], v[24:27], a[120:127], v0\ns_waitcnt lgkmcnt(0)\nv_smfmac_f32_16x16x64_bf16 v[20:23], v[28:31], a[152:159], v0\ns_nop 2\n" \
    "v_smfmac_f32_16x16x64_bf16 v[16:19], v[48:51], a[88:95], v0\ns_nop 2\nv_smfmac_f32_16x16x64_bf16 v[16:19], v[40:43], a[96:103], v0\nv_permlane32_swap_b32 v20, v22\n" \
    "v_smfmac_f32_16x16x64_bf16 v[16:19], v[24:27], a[112:119], v0\nv_permlane32_swap_b32 v21, v23\nv_smfmac_f32_16x16x64_bf16 v[16:19], v[28:31], a[144:151], v0\n" \
    "v_add_f32 v20, v20, v22\nv_add_f32 v21, v21, v23\nv_cvt_pk_bf16_f32 v20, v20, v21\nds_write_b16 v4, v20 offset:8\nds_write_b16_d16_hi v4, v20 offset:24\n" \
    "v_mov_b32 v15, v15\nv_mov_b32 v15, v15\nv_mov_b32 v15, v15\nv_permlane32_swap_b32 v16, v18\nv_permlane32_swap_b32 v17, v19\nv_pk_add_f32 v[16:17], v[16:17], v[18:19]\n" \
    "v_cvt_pk_bf16_f32 v16, v16, v17\nds_write_b16 v4, v16 offset:0\nds_write_b16_d16_hi v4, v16 offset:16\ns_branch .Lv7_%=\n.Lv11_%=:\n.Lv63_%=:\ns_memrealtime s[72:73]\n" \
    "s_waitcnt lgkmcnt(0)\ns_mov_b32 s68, 0\ns_sub_u32 s76, s80, s45\ns_sub_u32 s69, s72, s76\ns_cmp_lt_i32 s69, -8\ns_cbranch_scc1 .Lv66_%=\ns_xor_b32 s90, s81, 2048\n" \
    ".Lv68_%=:\ns_memrealtime s[72:73]\ns_waitcnt lgkmcnt(0)\ns_sub_u32 s72, s72, s80\ns_cmp_lt_i32 s72, 0\ns_cbranch_scc1 .Lv68_%=\n" \
    "buffer_load_dwordx4 v[72:75], v2, s[64:67], s81 offen offset:0 sc1\nv_cvt_pk_bf16_f32 v36, v20, v21\nv_cvt_pk_bf16_f32 v37, v22, v23\nv_cvt_pk_bf16_f32 v38, v28, v29\n" \
    "buffer_load_dwordx4 v[76:79], v2, s[64:67], s81 offen offset:1024 sc1\nv_cvt_pk_bf16_f32 v39, v30, v31\nds_write_b128 v14, v[36:39] offset:4096\n" \
    "ds_read_b128 v[24:27], v3 offset:6144\nbuffer_load_dwordx4 v[80:83], v2, s[64:67], s90 offen offset:0 sc1\nds_read_b128 v[28:31], v3 offset:7168\n" \
    "ds_read_b128 v[32:35], v3 offset:14336\nds_read_b128 v[36:39], v3 offset:15360\nbuffer_load_dwordx4 v[84:87], v2, s[64:67], s90 offen offset:1024 sc1\n" \
    "ds_read_b128 v[40:43], v3 offset:22528\nds_read_b128 v[44:47], v3 offset:23552\nds_read_b128 v[48:51], v3 offset:0\nds_read_b128 v[52:55], v3 offset:1024\n" \
    "ds_read_b128 v[56:59], v3 offset:2048\nds_read_b128 v[60:63], v3 offset:3072\nds_read_b128 v[64:67], v3 offset:4096\nds_read_b128 v[68:71], v3 offset:5120\n.Lv67_%=:\n" \
    "v_mov_b64 v[16:17], 0\nv_mov_b64 v[18:19], 0\nv_mov_b64 v[20:21], 0\nv_mov_b64 v[22:23], 0\ns_waitcnt vmcnt(3) lgkmcnt(10)\ns_nop 0\n" \
    "v_smfmac_f32_16x16x64_bf16 v[20:23], v[72:75], v[24:31], v0\nds_write_b128 v1, v[72:75] offset:27648\nv_max3_u32 v252, v72, v73, v74\n" \
    "v_smfmac_f32_16x16x64_bf16 v[16:19], v[72:75], a[24:31], v0\ns_waitcnt vmcnt(2)\nds_write_b128 v1, v[76:79] offset:31744\ns_waitcnt lgkmcnt(10)\n" \
    "v_max3_u32 v252, v252, v75, v76\nv_max3_u32 v252, v252, v77, v78\nv_cmp_u_f32 vcc, v252, v79\nv_smfmac_f32_16x16x64_bf16 v[20:23], v[76:79], v[32:39], v0\n" \
    "s_cbranch_vccnz .Lv74_%=\n.Lv70_%=:\nv_smfmac_f32_16x16x64_bf16 v[16:19], v[76:79], a[56:63], v0\nds_read_b128 v[24:27], v3 offset:8192\n" \
    "ds_read_b128 v[28:31], v3 offset:9216\ns_waitcnt vmcnt(1)\nv_max3_u32 v252, v80, v81, v82\nv_cmp_u_f32 vcc, v252, v83\ns_nop 0\ns_cbranch_vccnz .Lv75_%=\n.Lv71_%=:\n" \
    "s_waitcnt lgkmcnt(10)\nv_smfmac_f32_16x16x64_bf16 v[20:23], v[80:83], v[40:47], v0\nds_write_b128 v1, v[80:83] offset:35840\nds_read_b128 v[36:39], v3 offset:11264\n" \
    "s_waitcnt lgkmcnt(4)\ns_barrier\nds_read_b128 v[88:91], v1 offset:24576\nds_read_b128 v[40:43], v1 offset:25600\n" \
    "v_smfmac_f32_16x16x64_bf16 v[16:19], v[80:83], a[88:95], v0\nds_read_b128 v[32:35], v3 offset:10240\nds_read_b128 v[44:47], v1 offset:26624\ns_waitcnt vmcnt(0)\n" \
    "v_max3_u32 v252, v84, v85, v86\nv_cmp_u_f32 vcc, v252, v87\ns_nop 0\ns_cbranch_vccnz .Lv76_%=\n.Lv72_%=:\nv_smfmac_f32_16x16x64_bf16 v[20:23], v[84:87], a[152:159], v0\n" \
    "ds_write_b128 v1, v[84:87] offset:39936\nv_smfmac_f32_16x16x64_bf16 v[16:19], v[84:87], a[144:151], v0\nds_read_b128 v[72:75], v1 offset:28672\ns_waitcnt lgkmcnt(5)\n" \
    "v_smfmac_f32_16x16x64_bf16 v[20:23], v[88:91], v[48:55], v0\nds_read_b128 v[48:51], v1 offset:29696\nv_smfmac_f32_16x16x64_bf16 v[16:19], v[88:91], a[0:7], v0\n" \
    "ds_read_b128 v[52:55], v1 offset:30720\ns_waitcnt lgkmcnt(6)\nv_smfmac_f32_16x16x64_bf16 v[20:23], v[40:43], v[56:63], v0\nds_read_b128 v[56:59], v3 offset:12288\n" \
    "s_waitcnt lgkmcnt(4)\ns_barrier\nv_smfmac_f32_16x16x64_bf16 v[16:19], v[40:43], a[8:15], v0\nds_read_b128 v[60:63], v3 offset:13312\n" \
    "v_smfmac_f32_16x16x64_bf16 v[20:23], v[44:47], v[64:71], v0\nds_read_b128 v[40:43], v1 offset:32768\nv_smfmac_f32_16x16x64_bf16 v[16:19], v[44:47], a[16:23], v0\n" \
    "ds_read_b128 v[64:67], v3 offset:16384\ns_waitcnt lgkmcnt(6)\nv_smfmac_f32_16x16x64_bf16 v[20:23], v[72:75], v[24:31], v0\nds_read_b128 v[68:71], v3 offset:17408\n" \
    "v_smfmac_f32_16x16x64_bf16 v[16:19], v[72:75], a[32:39], v0\nds_read_b128 v[44:47], v1 offset:33792\ns_waitcnt lgkmcnt(7)\n" \
    "v_smfmac_f32_16x16x64_bf16 v[20:23], v[48:51], v[32:39], v0\nds_read_b128 v[24:27], v3 offset:18432\nv_smfmac_f32_16x16x64_bf16 v[16:19], v[48:51], a[40:47], v0\n" \
    "ds_read_b128 v[28:31], v3 offset:19456\ns_waitcnt lgkmcnt(6)\nv_smfmac_f32_16x16x64_bf16 v[20:23], v[52:55], v[56:63], v0\nds_read_b128 v[48:51], v1 offset:34816\n" \
    "v_smfmac_f32_16x16x64_bf16 v[16:19], v[52:55], a[48:55], v0\nds_read_b128 v[32:35], v3 offset:20480\ns_waitcnt lgkmcnt(5)\n" \
    "v_smfmac_f32_16x16x64_bf16 v[20:23], v[40:43], v[64:71], v0\nds_read_b128 v[36:39], v3 offset:21504\nv_smfmac_f32_16x16x64_bf16 v[16:19], v[40:43], a[64:71], v0\n" \
    "ds_read_b128 v[40:43], v1 offset:36864\ns_waitcnt lgkmcnt(4)\nv_smfmac_f32_16x16x64_bf16 v[20:23], v[44:47], v[24:31], v0\nds_read_b128 v[24:27], v1 offset:37888\n" \
    "v_smfmac_f32_16x16x64_bf16 v[16:19], v[44:47], a[72:79], v0\nds_read_b128 v[28:31], v1 offset:38912\ns_waitcnt lgkmcnt(3)\n" \
    "v_smfmac_f32_16x16x64_bf16 v[20:23], v[48:51], v[32:39], v0\ns_waitcnt lgkmcnt(2)\nv_smfmac_f32_16x16x64_bf16 v[20:23], v[40:43], a[104:111], v0\ns_waitcnt lgkmcnt(1)\n" \
    "v_smfmac_f32_16x16x64_bf16 v[20:23], v[24:27], a[120:127], v0\ns_waitcnt lgkmcnt(0)\nv_smfmac_f32_16x16x64_bf16 v[20:23], v[28:31], a[136:143], v0\ns_nop 2\n" \
    "v_smfmac_f32_16x16x64_bf16 v[16:19], v[48:51], a[80:87], v0\ns_nop 2\nv_smfmac_f32_16x16x64_bf16 v[16:19], v[40:43], a[96:103], v0\nv_permlane32_swap_b32 v20, v22\n" \
    "v_smfmac_f32_16x16x64_bf16 v[16:19], v[24:27], a[112:119], v0\nv_permlane32_swap_b32 v21, v23\nv_smfmac_f32_16x16x64_bf16 v[16:19], v[28:31], a[128:135], v0\n" \
    "v_add_f32 v20, v20, v22\nv_add_f32 v21, v21, v23\nv_cvt_pk_bf16_f32 v20, v20, v21\nds_write_b16 v4, v20 offset:8\nds_write_b16_d16_hi v4, v20 offset:24\n" \
    "v_mov_b32 v15, v15\nv_mov_b32 v15, v15\nv_mov_b32 v15, v15\nv_permlane32_swap_b32 v16, v18\nv_permlane32_swap_b32 v17, v19\nv_pk_add_f32 v[16:17], v[16:17], v[18:19]\n" \
    "v_cvt_pk_bf16_f32 v16, v16, v17\nds_write_b16 v4, v16 offset:0\nds_write_b16_d16_hi v4, v16 offset:16\nv_mov_b32 v15, v15\nv_mov_b32 v15, v15\n.Lv7_%=:\n.Lv79_%=:\n" \
    "v_mov_b64 v[54:55], 0\nv_mov_b64 v[52:53], 0\nv_mov_b64 v[50:51], 0\nv_mov_b64 v[48:49], 0\nv_mov_b64 v[70:71], 0\nv_mov_b64 v[68:69], 0\nv_mov_b64 v[66:67], 0\n" \
    "v_mov_b64 v[64:65], 0\nds_read_b128 v[32:35], v5 offset:0\nds_read_b128 v[36:39], v5 offset:512\nds_read_b128 v[40:43], v5 offset:1024\n" \
    "ds_read_b128 v[44:47], v5 offset:1536\nv_mov_b64 v[30:31], 0\nv_mov_b64 v[28:29], 0\nv_mov_b64 v[26:27], 0\nv_mov_b64 v[24:25], 0\nv_mov_b64 v[22:23], 0\n" \
    "v_mov_b64 v[20:21], 0\nv_mov_b64 v[18:19], 0\nv_mov_b64 v[16:17], 0\ns_waitcnt lgkmcnt(3)\ns_nop 0\nv_smfmac_f32_16x16x64_bf16 v[16:19], v[32:35], a[160:167], v0\n" \
    "v_smfmac_f32_16x16x64_bf16 v[20:23], v[32:35], a[176:183], v0\ns_waitcnt lgkmcnt(2)\nv_smfmac_f32_16x16x64_bf16 v[16:19], v[36:39], a[168:175], v0\n" \
    "v_smfmac_f32_16x16x64_bf16 v[20:23], v[36:39], a[184:191], v0\ns_waitcnt lgkmcnt(1)\nv_smfmac_f32_16x16x64_bf16 v[16:19], v[40:43], a[192:199], v0\n" \
    "v_smfmac_f32_16x16x64_bf16 v[20:23], v[40:43], a[208:215], v0\ns_waitcnt lgkmcnt(0)\nv_smfmac_f32_16x16x64_bf16 v[16:19], v[44:47], a[200:207], v0\n" \
    "v_smfmac_f32_16x16x64_bf16 v[20:23], v[44:47], a[216:223], v0\ns_nop 2\nv_smfmac_f32_16x16x64_bf16 v[24:27], v[32:35], a[224:231], v0\ns_nop 2\n" \
    "v_cmp_u_f32 s[4:5], v16, v17\nv_smfmac_f32_16x16x64_bf16 v[28:31], v[32:35], a[240:247], v0\nv_cmp_u_f32 vcc, v18, v19\n" \
    "v_smfmac_f32_16x16x64_bf16 v[24:27], v[36:39], a[232:239], v0\nv_cvt_pk_bf16_f32 v72, v16, v17\nv_smfmac_f32_16x16x64_bf16 v[28:31], v[36:39], a[248:255], v0\n" \
    "v_cvt_pk_bf16_f32 v74, v18, v19\nv_smfmac_f32_16x16x64_bf16 v[24:27], v[40:43], v[92:99], v0\ns_or_b64 vcc, vcc, s[4:5]\ns_cbranch_scc1 .Lv80_%=\n.Lv81_%=:\n" \
    "v_cvt_pk_bf16_f32 v73, v20, v21\nv_smfmac_f32_16x16x64_bf16 v[28:31], v[40:43], v[108:115], v0\nv_cvt_pk_bf16_f32 v75, v22, v23\n" \
    "v_smfmac_f32_16x16x64_bf16 v[24:27], v[44:47], v[100:107], v0\nv_permlane16_swap_b32 v72, v73\nv_smfmac_f32_16x16x64_bf16 v[28:31], v[44:47], v[116:123], v0\n" \
    "v_permlane16_swap_b32 v74, v75\nv_smfmac_f32_16x16x64_bf16 v[64:67], v[32:35], v[124:131], v0\nbuffer_store_dwordx4 v[72:75], v7, s[64:67], s0 offen offset:0 sc1\ns_nop 1\n" \
    "v_smfmac_f32_16x16x64_bf16 v[68:71], v[32:35], v[140:147], v0\nv_cvt_pk_bf16_f32 v76, v24, v25\nv_smfmac_f32_16x16x64_bf16 v[64:67], v[36:39], v[132:139], v0\n" \
    "v_cvt_pk_bf16_f32 v78, v26, v27\nv_smfmac_f32_16x16x64_bf16 v[68:71], v[36:39], v[148:155], v0\nv_cvt_pk_bf16_f32 v77, v28, v29\n" \
    "v_smfmac_f32_16x16x64_bf16 v[64:67], v[40:43], v[156:163], v0\nv_cvt_pk_bf16_f32 v79, v30, v31\nv_smfmac_f32_16x16x64_bf16 v[68:71], v[40:43], v[172:179], v0\n" \
    "v_permlane16_swap_b32 v76, v77\nv_smfmac_f32_16x16x64_bf16 v[64:67], v[44:47], v[164:171], v0\nv_permlane16_swap_b32 v78, v79\n" \
    "v_smfmac_f32_16x16x64_bf16 v[68:71], v[44:47], v[180:187], v0\nbuffer_store_dwordx4 v[76:79], v7, s[64:67], s1 offen offset:0 sc1\ns_nop 1\n" \
    "v_smfmac_f32_16x16x64_bf16 v[48:51], v[32:35], v[188:195], v0\ns_nop 1\nv_cvt_pk_bf16_f32 v72, v64, v65\nv_smfmac_f32_16x16x64_bf16 v[52:55], v[32:35], v[204:211], v0\n" \
    "v_cvt_pk_bf16_f32 v74, v66, v67\nv_smfmac_f32_16x16x64_bf16 v[48:51], v[36:39], v[196:203], v0\nv_cvt_pk_bf16_f32 v73, v68, v69\n" \
    "v_smfmac_f32_16x16x64_bf16 v[52:55], v[36:39], v[212:219], v0\nv_cvt_pk_bf16_f32 v75, v70, v71\nv_smfmac_f32_16x16x64_bf16 v[48:51], v[40:43], v[220:227], v0\n" \
    "v_permlane16_swap_b32 v72, v73\nv_smfmac_f32_16x16x64_bf16 v[52:55], v[40:43], v[236:243], v0\nv_permlane16_swap_b32 v74, v75\n" \
    "v_smfmac_f32_16x16x64_bf16 v[48:51], v[44:47], v[228:235], v0\nbuffer_store_dwordx4 v[72:75], v7, s[64:67], s2 offen offset:0 sc1\n" \
    "v_smfmac_f32_16x16x64_bf16 v[52:55], v[44:47], v[244:251], v0\ns_nop 5\nv_cvt_pk_bf16_f32 v76, v48, v49\nv_cvt_pk_bf16_f32 v78, v50, v51\nv_cvt_pk_bf16_f32 v77, v52, v53\n" \
    "v_cvt_pk_bf16_f32 v79, v54, v55\ns_nop 0\nv_permlane16_swap_b32 v76, v77\nv_permlane16_swap_b32 v78, v79\n" \
    "buffer_store_dwordx4 v[76:79], v7, s[64:67], s3 offen offset:0 sc1\ns_barrier\ns_memrealtime s[72:73]\ns_waitcnt lgkmcnt(0)\ns_add_u32 s35, s35, 2\ns_cmp_lt_u32 s35, 62\n" \
    "s_cbranch_scc1 .Lv5_%=\ns_cmp_lt_u32 s35, 64\ns_cbranch_scc0 .Lv6_%=\nv_mov_b64 v[252:253], -1\ns_and_b32 s69, s35, 15\ns_lshl_b32 s69, s69, 19\ns_add_u32 s0, s38, s69\n" \
    "s_lshr_b32 s69, s0, 8\ns_and_b32 s69, s69, 0x2000\ns_xor_b32 s0, s0, s69\ns_xor_b32 s1, s0, 0x10000\ns_xor_b32 s2, s0, 0x20000\ns_xor_b32 s3, s0, 0x30000\n" \
    "buffer_load_dword v13, off, s[64:67], s42 sc1\ns_and_b32 s69, s35, 15\ns_lshl_b32 s69, s69, 17\ns_add_u32 s81, s37, s69\ns_lshr_b32 s69, s81, 8\n" \
    "s_and_b32 s69, s69, 0x2000\ns_xor_b32 s81, s81, s69\ns_mov_b32 s68, 0\ns_waitcnt vmcnt(0)\nv_readfirstlane_b32 s74, v13\ns_nop 0\ns_and_b32 s75, s74, 127\n" \
    "s_lshr_b32 s69, s74, 8\ns_sub_u32 s69, s69, s80\ns_bfe_i32 s69, s69, 0x180000\ns_cmp_eq_u32 s75, 0\ns_cselect_b32 s69, 0, s69\ns_add_u32 s80, s80, s69\n" \
    "s_lshl_b32 s74, s74, 5\ns_and_b32 s74, s74, 0x1000\ns_andn2_b32 s81, s81, 0x1000\ns_or_b32 s81, s81, s74\nv_lshrrev_b32 v15, 1, v2\nv_add_u32 v15, s60, v15\n" \
    "ds_write_b64 v15, v[252:253]\ns_branch .Lv4_%=\n.Lv6_%=:\ns_endpgm\n.Lv12_%=:\ns_nop 7\nv_cvt_pk_bf16_f32 v36, v20, v21\nv_cvt_pk_bf16_f32 v37, v22, v23\n" \
    "v_cvt_pk_bf16_f32 v38, v28, v29\nv_cvt_pk_bf16_f32 v39, v30, v31\nds_write_b128 v14, v[36:39] offset:4096\ns_waitcnt vmcnt(0) lgkmcnt(0)\ns_barrier\ns_branch .Lv5_%=\n" \
    ".Lv14_%=:\ns_sleep 6\nbuffer_load_dword v13, off, s[64:67], s42 sc1\ns_waitcnt vmcnt(0)\nv_readfirstlane_b32 s74, v13\ns_nop 0\ns_and_b32 s75, s74, 127\n" \
    "s_lshr_b32 s69, s74, 8\ns_sub_u32 s69, s69, s80\ns_bfe_i32 s69, s69, 0x180000\ns_cmp_eq_u32 s75, 0\ns_cselect_b32 s69, 0, s69\ns_add_u32 s80, s80, s69\n" \
    "s_lshl_b32 s74, s74, 5\ns_and_b32 s74, s74, 0x1000\ns_andn2_b32 s81, s81, 0x1000\ns_or_b32 s81, s81, s74\ns_add_u32 s68, s68, 1\ns_and_b32 s69, s68, 63\n" \
    "s_cbranch_scc1 .Lv13_%=\ns_cmp_gt_u32 s68, 0x400000\ns_cbranch_scc1 .Lv1_%=\nbuffer_load_dword v253, off, s[64:67], 0 sc1\ns_waitcnt vmcnt(0)\n" \
    "v_readfirstlane_b32 s69, v253\ns_nop 0\ns_cmp_eq_u32 s69, 1\ns_cbranch_scc0 .Lv13_%=\ns_endpgm\n.Lv18_%=:\ns_memrealtime s[72:73]\ns_waitcnt lgkmcnt(0)\n" \
    "s_sub_u32 s72, s72, s76\ns_cmp_lt_i32 s72, 0\ns_cbranch_scc1 .Lv18_%=\nbuffer_load_dword v13, off, s[64:67], s42 sc1\ns_waitcnt vmcnt(0)\nv_readfirstlane_b32 s74, v13\n" \
    "s_nop 0\ns_and_b32 s75, s74, 127\ns_lshr_b32 s69, s74, 8\ns_sub_u32 s69, s69, s80\ns_bfe_i32 s69, s69, 0x180000\ns_cmp_eq_u32 s75, 0\ns_cselect_b32 s69, 0, s69\n" \
    "s_add_u32 s80, s80, s69\ns_lshl_b32 s74, s74, 5\ns_and_b32 s74, s74, 0x1000\ns_andn2_b32 s81, s81, 0x1000\ns_or_b32 s81, s81, s74\ns_branch .Lv15_%=\n.Lv26_%=:\n" \
    "v_mov_b32 v252, 0\nv_max3_u32 v252, v252, v72, v73\nv_max3_u32 v252, v252, v74, v75\nv_max3_u32 v252, v252, v76, v77\nv_max3_u32 v252, v252, v78, v79\n" \
    "v_cmp_eq_u32 vcc, -1, v252\ns_nop 0\ns_cbranch_vccz .Lv22_%=\ns_add_u32 s80, s80, s43\ns_xor_b32 s81, s81, 0x1000\ns_xor_b32 s90, s90, 0x1000\ns_add_u32 s68, s68, 1\n" \
    "s_and_b32 s69, s68, 63\ns_cbranch_scc1 .Lv16_%=\ns_cmp_gt_u32 s68, 0x400000\ns_cbranch_scc1 .Lv1_%=\nbuffer_load_dword v253, off, s[64:67], 0 sc1\ns_waitcnt vmcnt(0)\n" \
    "v_readfirstlane_b32 s69, v253\ns_nop 0\ns_cmp_eq_u32 s69, 1\ns_cbranch_scc0 .Lv16_%=\ns_endpgm\n.Lv16_%=:\ns_memrealtime s[72:73]\ns_waitcnt lgkmcnt(0)\n" \
    "s_sub_u32 s72, s72, s80\ns_cmp_lt_i32 s72, 0\ns_cbranch_scc1 .Lv16_%=\nbuffer_load_dwordx4 v[72:75], v2, s[64:67], s81 offen offset:0 sc1\ns_nop 7\n" \
    "buffer_load_dwordx4 v[76:79], v2, s[64:67], s81 offen offset:1024 sc1\ns_nop 7\nbuffer_load_dwordx4 v[80:83], v2, s[64:67], s90 offen offset:0 sc1\ns_nop 7\n" \
    "buffer_load_dwordx4 v[84:87], v2, s[64:67], s90 offen offset:1024 sc1\ns_nop 7\ns_branch .Lv19_%=\n.Lv27_%=:\nv_mov_b32 v252, 0\nv_max3_u32 v252, v252, v80, v81\n" \
    "v_max3_u32 v252, v252, v82, v83\nv_cmp_eq_u32 vcc, -1, v252\ns_nop 0\ns_cbranch_vccz .Lv23_%=\ns_add_u32 s80, s80, s43\ns_xor_b32 s81, s81, 0x1000\n" \
    "s_xor_b32 s90, s90, 0x1000\n.Lv29_%=:\ns_memrealtime s[72:73]\ns_waitcnt vmcnt(0) lgkmcnt(0)\ns_sub_u32 s72, s72, s80\ns_cmp_lt_i32 s72, 0\ns_cbranch_scc1 .Lv29_%=\n" \
    "buffer_load_dwordx4 v[80:83], v2, s[64:67], s90 offen offset:0 sc1\nbuffer_load_dwordx4 v[84:87], v2, s[64:67], s90 offen offset:1024 sc1\ns_waitcnt vmcnt(0)\n" \
    "s_add_u32 s68, s68, 1\ns_and_b32 s69, s68, 63\ns_cbranch_scc1 .Lv27_%=\ns_cmp_gt_u32 s68, 0x400000\ns_cbranch_scc1 .Lv1_%=\nbuffer_load_dword v253, off, s[64:67], 0 sc1\n" \
    "s_waitcnt vmcnt(0)\nv_readfirstlane_b32 s69, v253\ns_nop 0\ns_cmp_eq_u32 s69, 1\ns_cbranch_scc0 .Lv27_%=\ns_endpgm\n.Lv28_%=:\nv_mov_b32 v252, 0\n" \
    "v_max3_u32 v252, v252, v84, v85\nv_max3_u32 v252, v252, v86, v87\nv_cmp_eq_u32 vcc, -1, v252\ns_nop 0\ns_cbranch_vccz .Lv24_%=\ns_add_u32 s80, s80, s43\n" \
    "s_xor_b32 s81, s81, 0x1000\ns_xor_b32 s90, s90, 0x1000\n.Lv30_%=:\ns_memrealtime s[72:73]\ns_waitcnt vmcnt(0) lgkmcnt(0)\ns_sub_u32 s72, s72, s80\ns_cmp_lt_i32 s72, 0\n" \
    "s_cbranch_scc1 .Lv30_%=\nbuffer_load_dwordx4 v[84:87], v2, s[64:67], s90 offen offset:1024 sc1\ns_waitcnt vmcnt(0)\ns_add_u32 s68, s68, 1\ns_and_b32 s69, s68, 63\n" \
    "s_cbranch_scc1 .Lv28_%=\ns_cmp_gt_u32 s68, 0x400000\ns_cbranch_scc1 .Lv1_%=\nbuffer_load_dword v253, off, s[64:67], 0 sc1\ns_waitcnt vmcnt(0)\n" \
    "v_readfirstlane_b32 s69, v253\ns_nop 0\ns_cmp_eq_u32 s69, 1\ns_cbranch_scc0 .Lv28_%=\ns_endpgm\n.Lv34_%=:\ns_memrealtime s[72:73]\ns_waitcnt lgkmcnt(0)\n" \
    "s_sub_u32 s72, s72, s76\ns_cmp_lt_i32 s72, 0\ns_cbranch_scc1 .Lv34_%=\nbuffer_load_dword v13, off, s[64:67], s42 sc1\ns_waitcnt vmcnt(0)\nv_readfirstlane_b32 s74, v13\n" \
    "s_nop 0\ns_and_b32 s75, s74, 127\ns_lshr_b32 s69, s74, 8\ns_sub_u32 s69, s69, s80\ns_bfe_i32 s69, s69, 0x180000\ns_cmp_eq_u32 s75, 0\ns_cselect_b32 s69, 0, s69\n" \
    "s_add_u32 s80, s80, s69\ns_lshl_b32 s74, s74, 5\ns_and_b32 s74, s74, 0x1000\ns_andn2_b32 s81, s81, 0x1000\ns_or_b32 s81, s81, s74\ns_branch .Lv31_%=\n.Lv42_%=:\n" \
    "v_mov_b32 v252, 0\nv_max3_u32 v252, v252, v72, v73\nv_max3_u32 v252, v252, v74, v75\nv_max3_u32 v252, v252, v76, v77\nv_max3_u32 v252, v252, v78, v79\n" \
    "v_cmp_eq_u32 vcc, -1, v252\ns_nop 0\ns_cbranch_vccz .Lv38_%=\ns_add_u32 s80, s80, s43\ns_xor_b32 s81, s81, 0x1000\ns_xor_b32 s90, s90, 0x1000\ns_add_u32 s68, s68, 1\n" \
    "s_and_b32 s69, s68, 63\ns_cbranch_scc1 .Lv32_%=\ns_cmp_gt_u32 s68, 0x400000\ns_cbranch_scc1 .Lv1_%=\nbuffer_load_dword v253, off, s[64:67], 0 sc1\ns_waitcnt vmcnt(0)\n" \
    "v_readfirstlane_b32 s69, v253\ns_nop 0\ns_cmp_eq_u32 s69, 1\ns_cbranch_scc0 .Lv32_%=\ns_endpgm\n.Lv32_%=:\ns_memrealtime s[72:73]\ns_waitcnt lgkmcnt(0)\n" \
    "s_sub_u32 s72, s72, s80\ns_cmp_lt_i32 s72, 0\ns_cbranch_scc1 .Lv32_%=\nbuffer_load_dwordx4 v[72:75], v2, s[64:67], s81 offen offset:0 sc1\ns_nop 7\n" \
    "buffer_load_dwordx4 v[76:79], v2, s[64:67], s81 offen offset:1024 sc1\ns_nop 7\nbuffer_load_dwordx4 v[80:83], v2, s[64:67], s90 offen offset:0 sc1\ns_nop 7\n" \
    "buffer_load_dwordx4 v[84:87], v2, s[64:67], s90 offen offset:1024 sc1\ns_nop 7\ns_branch .Lv35_%=\n.Lv43_%=:\nv_mov_b32 v252, 0\nv_max3_u32 v252, v252, v80, v81\n" \
    "v_max3_u32 v252, v252, v82, v83\nv_cmp_eq_u32 vcc, -1, v252\ns_nop 0\ns_cbranch_vccz .Lv39_%=\ns_add_u32 s80, s80, s43\ns_xor_b32 s81, s81, 0x1000\n" \
    "s_xor_b32 s90, s90, 0x1000\n.Lv45_%=:\ns_memrealtime s[72:73]\ns_waitcnt vmcnt(0) lgkmcnt(0)\ns_sub_u32 s72, s72, s80\ns_cmp_lt_i32 s72, 0\ns_cbranch_scc1 .Lv45_%=\n" \
    "buffer_load_dwordx4 v[80:83], v2, s[64:67], s90 offen offset:0 sc1\nbuffer_load_dwordx4 v[84:87], v2, s[64:67], s90 offen offset:1024 sc1\ns_waitcnt vmcnt(0)\n" \
    "s_add_u32 s68, s68, 1\ns_and_b32 s69, s68, 63\ns_cbranch_scc1 .Lv43_%=\ns_cmp_gt_u32 s68, 0x400000\ns_cbranch_scc1 .Lv1_%=\nbuffer_load_dword v253, off, s[64:67], 0 sc1\n" \
    "s_waitcnt vmcnt(0)\nv_readfirstlane_b32 s69, v253\ns_nop 0\ns_cmp_eq_u32 s69, 1\ns_cbranch_scc0 .Lv43_%=\ns_endpgm\n.Lv44_%=:\nv_mov_b32 v252, 0\n" \
    "v_max3_u32 v252, v252, v84, v85\nv_max3_u32 v252, v252, v86, v87\nv_cmp_eq_u32 vcc, -1, v252\ns_nop 0\ns_cbranch_vccz .Lv40_%=\ns_add_u32 s80, s80, s43\n" \
    "s_xor_b32 s81, s81, 0x1000\ns_xor_b32 s90, s90, 0x1000\n.Lv46_%=:\ns_memrealtime s[72:73]\ns_waitcnt vmcnt(0) lgkmcnt(0)\ns_sub_u32 s72, s72, s80\ns_cmp_lt_i32 s72, 0\n" \
    "s_cbranch_scc1 .Lv46_%=\nbuffer_load_dwordx4 v[84:87], v2, s[64:67], s90 offen offset:1024 sc1\ns_waitcnt vmcnt(0)\ns_add_u32 s68, s68, 1\ns_and_b32 s69, s68, 63\n" \
    "s_cbranch_scc1 .Lv44_%=\ns_cmp_gt_u32 s68, 0x400000\ns_cbranch_scc1 .Lv1_%=\nbuffer_load_dword v253, off, s[64:67], 0 sc1\ns_waitcnt vmcnt(0)\n" \
    "v_readfirstlane_b32 s69, v253\ns_nop 0\ns_cmp_eq_u32 s69, 1\ns_cbranch_scc0 .Lv44_%=\ns_endpgm\n.Lv50_%=:\ns_memrealtime s[72:73]\ns_waitcnt lgkmcnt(0)\n" \
    "s_sub_u32 s72, s72, s76\ns_cmp_lt_i32 s72, 0\ns_cbranch_scc1 .Lv50_%=\nbuffer_load_dword v13, off, s[64:67], s42 sc1\ns_waitcnt vmcnt(0)\nv_readfirstlane_b32 s74, v13\n" \
    "s_nop 0\ns_and_b32 s75, s74, 127\ns_lshr_b32 s69, s74, 8\ns_sub_u32 s69, s69, s80\ns_bfe_i32 s69, s69, 0x180000\ns_cmp_eq_u32 s75, 0\ns_cselect_b32 s69, 0, s69\n" \
    "s_add_u32 s80, s80, s69\ns_lshl_b32 s74, s74, 5\ns_and_b32 s74, s74, 0x1000\ns_andn2_b32 s81, s81, 0x1000\ns_or_b32 s81, s81, s74\ns_branch .Lv47_%=\n.Lv58_%=:\n" \
    "v_mov_b32 v252, 0\nv_max3_u32 v252, v252, v72, v73\nv_max3_u32 v252, v252, v74, v75\nv_max3_u32 v252, v252, v76, v77\nv_max3_u32 v252, v252, v78, v79\n" \
    "v_cmp_eq_u32 vcc, -1, v252\ns_nop 0\ns_cbranch_vccz .Lv54_%=\ns_add_u32 s80, s80, s43\ns_xor_b32 s81, s81, 0x1000\ns_xor_b32 s90, s90, 0x1000\ns_add_u32 s68, s68, 1\n" \
    "s_and_b32 s69, s68, 63\ns_cbranch_scc1 .Lv48_%=\ns_cmp_gt_u32 s68, 0x400000\ns_cbranch_scc1 .Lv1_%=\nbuffer_load_dword v253, off, s[64:67], 0 sc1\ns_waitcnt vmcnt(0)\n" \
    "v_readfirstlane_b32 s69, v253\ns_nop 0\ns_cmp_eq_u32 s69, 1\ns_cbranch_scc0 .Lv48_%=\ns_endpgm\n.Lv48_%=:\ns_memrealtime s[72:73]\ns_waitcnt lgkmcnt(0)\n" \
    "s_sub_u32 s72, s72, s80\ns_cmp_lt_i32 s72, 0\ns_cbranch_scc1 .Lv48_%=\nbuffer_load_dwordx4 v[72:75], v2, s[64:67], s81 offen offset:0 sc1\ns_nop 7\n" \
    "buffer_load_dwordx4 v[76:79], v2, s[64:67], s81 offen offset:1024 sc1\ns_nop 7\nbuffer_load_dwordx4 v[80:83], v2, s[64:67], s90 offen offset:0 sc1\ns_nop 7\n" \
    "buffer_load_dwordx4 v[84:87], v2, s[64:67], s90 offen offset:1024 sc1\ns_nop 7\ns_branch .Lv51_%=\n.Lv59_%=:\nv_mov_b32 v252, 0\nv_max3_u32 v252, v252, v80, v81\n" \
    "v_max3_u32 v252, v252, v82, v83\nv_cmp_eq_u32 vcc, -1, v252\ns_nop 0\ns_cbranch_vccz .Lv55_%=\ns_add_u32 s80, s80, s43\ns_xor_b32 s81, s81, 0x1000\n" \
    "s_xor_b32 s90, s90, 0x1000\n.Lv61_%=:\ns_memrealtime s[72:73]\ns_waitcnt vmcnt(0) lgkmcnt(0)\ns_sub_u32 s72, s72, s80\ns_cmp_lt_i32 s72, 0\ns_cbranch_scc1 .Lv61_%=\n" \
    "buffer_load_dwordx4 v[80:83], v2, s[64:67], s90 offen offset:0 sc1\nbuffer_load_dwordx4 v[84:87], v2, s[64:67], s90 offen offset:1024 sc1\ns_waitcnt vmcnt(0)\n" \
    "s_add_u32 s68, s68, 1\ns_and_b32 s69, s68, 63\ns_cbranch_scc1 .Lv59_%=\ns_cmp_gt_u32 s68, 0x400000\ns_cbranch_scc1 .Lv1_%=\nbuffer_load_dword v253, off, s[64:67], 0 sc1\n" \
    "s_waitcnt vmcnt(0)\nv_readfirstlane_b32 s69, v253\ns_nop 0\ns_cmp_eq_u32 s69, 1\ns_cbranch_scc0 .Lv59_%=\ns_endpgm\n.Lv60_%=:\nv_mov_b32 v252, 0\n" \
    "v_max3_u32 v252, v252, v84, v85\nv_max3_u32 v252, v252, v86, v87\nv_cmp_eq_u32 vcc, -1, v252\ns_nop 0\ns_cbranch_vccz .Lv56_%=\ns_add_u32 s80, s80, s43\n" \
    "s_xor_b32 s81, s81, 0x1000\ns_xor_b32 s90, s90, 0x1000\n.Lv62_%=:\ns_memrealtime s[72:73]\ns_waitcnt vmcnt(0) lgkmcnt(0)\ns_sub_u32 s72, s72, s80\ns_cmp_lt_i32 s72, 0\n" \
    "s_cbranch_scc1 .Lv62_%=\nbuffer_load_dwordx4 v[84:87], v2, s[64:67], s90 offen offset:1024 sc1\ns_waitcnt vmcnt(0)\ns_add_u32 s68, s68, 1\ns_and_b32 s69, s68, 63\n" \
    "s_cbranch_scc1 .Lv60_%=\ns_cmp_gt_u32 s68, 0x400000\ns_cbranch_scc1 .Lv1_%=\nbuffer_load_dword v253, off, s[64:67], 0 sc1\ns_waitcnt vmcnt(0)\n" \
    "v_readfirstlane_b32 s69, v253\ns_nop 0\ns_cmp_eq_u32 s69, 1\ns_cbranch_scc0 .Lv60_%=\ns_endpgm\n.Lv66_%=:\ns_memrealtime s[72:73]\ns_waitcnt lgkmcnt(0)\n" \
    "s_sub_u32 s72, s72, s76\ns_cmp_lt_i32 s72, 0\ns_cbranch_scc1 .Lv66_%=\nbuffer_load_dword v13, off, s[64:67], s42 sc1\ns_waitcnt vmcnt(0)\nv_readfirstlane_b32 s74, v13\n" \
    "s_nop 0\ns_and_b32 s75, s74, 127\ns_lshr_b32 s69, s74, 8\ns_sub_u32 s69, s69, s80\ns_bfe_i32 s69, s69, 0x180000\ns_cmp_eq_u32 s75, 0\ns_cselect_b32 s69, 0, s69\n" \
    "s_add_u32 s80, s80, s69\ns_lshl_b32 s74, s74, 5\ns_and_b32 s74, s74, 0x1000\ns_andn2_b32 s81, s81, 0x1000\ns_or_b32 s81, s81, s74\ns_branch .Lv63_%=\n.Lv74_%=:\n" \
    "v_mov_b32 v252, 0\nv_max3_u32 v252, v252, v72, v73\nv_max3_u32 v252, v252, v74, v75\nv_max3_u32 v252, v252, v76, v77\nv_max3_u32 v252, v252, v78, v79\n" \
    "v_cmp_eq_u32 vcc, -1, v252\ns_nop 0\ns_cbranch_vccz .Lv70_%=\ns_add_u32 s80, s80, s43\ns_xor_b32 s81, s81, 0x1000\ns_xor_b32 s90, s90, 0x1000\ns_add_u32 s68, s68, 1\n" \
    "s_and_b32 s69, s68, 63\ns_cbranch_scc1 .Lv64_%=\ns_cmp_gt_u32 s68, 0x400000\ns_cbranch_scc1 .Lv1_%=\nbuffer_load_dword v253, off, s[64:67], 0 sc1\ns_waitcnt vmcnt(0)\n" \
    "v_readfirstlane_b32 s69, v253\ns_nop 0\ns_cmp_eq_u32 s69, 1\ns_cbranch_scc0 .Lv64_%=\ns_endpgm\n.Lv64_%=:\ns_memrealtime s[72:73]\ns_waitcnt lgkmcnt(0)\n" \
    "s_sub_u32 s72, s72, s80\ns_cmp_lt_i32 s72, 0\ns_cbranch_scc1 .Lv64_%=\nbuffer_load_dwordx4 v[72:75], v2, s[64:67], s81 offen offset:0 sc1\ns_nop 7\n" \
    "buffer_load_dwordx4 v[76:79], v2, s[64:67], s81 offen offset:1024 sc1\ns_nop 7\nbuffer_load_dwordx4 v[80:83], v2, s[64:67], s90 offen offset:0 sc1\ns_nop 7\n" \
    "buffer_load_dwordx4 v[84:87], v2, s[64:67], s90 offen offset:1024 sc1\ns_nop 7\ns_branch .Lv67_%=\n.Lv75_%=:\nv_mov_b32 v252, 0\nv_max3_u32 v252, v252, v80, v81\n" \
    "v_max3_u32 v252, v252, v82, v83\nv_cmp_eq_u32 vcc, -1, v252\ns_nop 0\ns_cbranch_vccz .Lv71_%=\ns_add_u32 s80, s80, s43\ns_xor_b32 s81, s81, 0x1000\n" \
    "s_xor_b32 s90, s90, 0x1000\n.Lv77_%=:\ns_memrealtime s[72:73]\ns_waitcnt vmcnt(0) lgkmcnt(0)\ns_sub_u32 s72, s72, s80\ns_cmp_lt_i32 s72, 0\ns_cbranch_scc1 .Lv77_%=\n" \
    "buffer_load_dwordx4 v[80:83], v2, s[64:67], s90 offen offset:0 sc1\nbuffer_load_dwordx4 v[84:87], v2, s[64:67], s90 offen offset:1024 sc1\ns_waitcnt vmcnt(0)\n" \
    "s_add_u32 s68, s68, 1\ns_and_b32 s69, s68, 63\ns_cbranch_scc1 .Lv75_%=\ns_cmp_gt_u32 s68, 0x400000\ns_cbranch_scc1 .Lv1_%=\nbuffer_load_dword v253, off, s[64:67], 0 sc1\n" \
    "s_waitcnt vmcnt(0)\nv_readfirstlane_b32 s69, v253\ns_nop 0\ns_cmp_eq_u32 s69, 1\ns_cbranch_scc0 .Lv75_%=\ns_endpgm\n.Lv76_%=:\nv_mov_b32 v252, 0\n" \
    "v_max3_u32 v252, v252, v84, v85\nv_max3_u32 v252, v252, v86, v87\nv_cmp_eq_u32 vcc, -1, v252\ns_nop 0\ns_cbranch_vccz .Lv72_%=\ns_add_u32 s80, s80, s43\n" \
    "s_xor_b32 s81, s81, 0x1000\ns_xor_b32 s90, s90, 0x1000\n.Lv78_%=:\ns_memrealtime s[72:73]\ns_waitcnt vmcnt(0) lgkmcnt(0)\ns_sub_u32 s72, s72, s80\ns_cmp_lt_i32 s72, 0\n" \
    "s_cbranch_scc1 .Lv78_%=\nbuffer_load_dwordx4 v[84:87], v2, s[64:67], s90 offen offset:1024 sc1\ns_waitcnt vmcnt(0)\ns_add_u32 s68, s68, 1\ns_and_b32 s69, s68, 63\n" \
    "s_cbranch_scc1 .Lv76_%=\ns_cmp_gt_u32 s68, 0x400000\ns_cbranch_scc1 .Lv1_%=\nbuffer_load_dword v253, off, s[64:67], 0 sc1\ns_waitcnt vmcnt(0)\n" \
    "v_readfirstlane_b32 s69, v253\ns_nop 0\ns_cmp_eq_u32 s69, 1\ns_cbranch_scc0 .Lv76_%=\ns_endpgm\n.Lv80_%=:\nv_mov_b32 v252, 0\nv_pk_max_u16 v252, v252, v32\n" \
    "v_pk_max_u16 v252, v252, v33\nv_pk_max_u16 v252, v252, v34\nv_pk_max_u16 v252, v252, v35\nv_pk_max_u16 v252, v252, v36\nv_pk_max_u16 v252, v252, v37\n" \
    "v_pk_max_u16 v252, v252, v38\nv_pk_max_u16 v252, v252, v39\nv_pk_max_u16 v252, v252, v40\nv_pk_max_u16 v252, v252, v41\nv_pk_max_u16 v252, v252, v42\n" \
    "v_pk_max_u16 v252, v252, v43\nv_pk_max_u16 v252, v252, v44\nv_pk_max_u16 v252, v252, v45\nv_pk_max_u16 v252, v252, v46\nv_pk_max_u16 v252, v252, v47\n" \
    "v_pk_max_u16 v252, v252, v252 op_sel:[0,1] op_sel_hi:[1,0]\nv_cmp_eq_u32 vcc, -1, v252\ns_nop 0\ns_cbranch_vccz .Lv81_%=\ns_add_u32 s68, s68, 1\ns_and_b32 s69, s68, 63\n" \
    "s_cbranch_scc1 .Lv79_%=\ns_cmp_gt_u32 s68, 0x400000\ns_cbranch_scc1 .Lv1_%=\nbuffer_load_dword v253, off, s[64:67], 0 sc1\ns_waitcnt vmcnt(0)\n" \
    "v_readfirstlane_b32 s69, v253\ns_nop 0\ns_cmp_eq_u32 s69, 1\ns_cbranch_scc0 .Lv79_%=\ns_endpgm\n.Lv1_%=:\nv_mov_b32 v253, 1\nbuffer_store_dword v253, off, s[64:67], 0 sc1\n" \
    "s_endpgm\n"
#define ASM_R \
    ".Lr2_%=:\nglobal_load_dwordx4 v[224:227], v7, s[54:55]\ns_mov_b32 s68, 0\ns_cmp_eq_u32 s32, 7\ns_cselect_b32 s79, 64, 0x7fff0000\ns_and_b32 s69, s32, 7\n" \
    "s_lshl_b32 s69, s69, 21\ns_add_u32 s82, s42, s69\ns_lshr_b32 s69, s82, 8\ns_and_b32 s69, s69, 0x2000\ns_xor_b32 s82, s82, s69\ns_and_b32 s69, s32, 15\n" \
    "s_lshl_b32 s69, s69, 19\ns_add_u32 s83, s43, s69\ns_lshr_b32 s69, s83, 8\ns_and_b32 s69, s69, 0x2000\ns_xor_b32 s83, s83, s69\ns_and_b32 s69, s32, 15\n" \
    "s_lshl_b32 s69, s69, 18\ns_add_u32 s84, s44, s69\ns_lshr_b32 s69, s84, 8\ns_and_b32 s69, s69, 0x2000\ns_xor_b32 s84, s84, s69\ns_and_b32 s69, s32, 15\n" \
    "s_lshl_b32 s69, s69, 18\ns_add_u32 s94, s45, s69\ns_lshr_b32 s69, s94, 8\ns_and_b32 s69, s69, 0x2000\ns_xor_b32 s94, s94, s69\ns_add_u32 s69, s32, 1\n" \
    "s_and_b32 s69, s69, 15\ns_lshl_b32 s69, s69, 17\ns_add_u32 s92, s46, s69\ns_and_b32 s69, s32, 63\ns_lshl_b32 s69, s69, 19\ns_add_u32 s93, s47, s69\ns_mov_b32 s68, 0\n" \
    "s_cmp_eq_u32 s32, 0\ns_cbranch_scc1 .Lr6_%=\ns_sub_u32 s77, s32, 1\ns_max_u32 s77, s77, 1\n.Lr4_%=:\nds_read_b32 v253, v10\ns_waitcnt lgkmcnt(0)\n" \
    "v_readfirstlane_b32 s74, v253\ns_nop 0\ns_and_b32 s75, s74, 127\ns_cmp_lt_u32 s75, s77\ns_cbranch_scc1 .Lr5_%=\ns_cmp_ge_u32 s75, s32\ns_cbranch_scc1 .Lr6_%=\n" \
    "s_memrealtime s[72:73]\ns_waitcnt lgkmcnt(0)\ns_lshr_b32 s69, s74, 8\ns_sub_u32 s69, s69, s72\ns_bfe_i32 s69, s69, 0x180000\ns_add_u32 s78, s72, s69\n" \
    "s_sub_u32 s76, s78, 60\n.Lr26_%=:\ns_memrealtime s[72:73]\ns_waitcnt lgkmcnt(0)\ns_sub_u32 s72, s72, s76\ns_cmp_lt_i32 s72, 0\ns_cbranch_scc1 .Lr26_%=\n" \
    "buffer_load_dword v254, v8, s[64:67], 0 offen sc1\ns_waitcnt vmcnt(0)\nv_cmp_ge_u32 vcc, s32, v254\ns_nop 0\ns_cmp_eq_u64 vcc, 0\ns_cselect_b32 s76, 4, s58\n" \
    "s_add_u32 s76, s76, s78\n.Lr27_%=:\ns_memrealtime s[72:73]\ns_waitcnt lgkmcnt(0)\ns_sub_u32 s72, s72, s76\ns_cmp_lt_i32 s72, 0\ns_cbranch_scc1 .Lr27_%=\n" \
    "s_cbranch_vccz .Lr15_%=\n.Lr6_%=:\nbuffer_load_dword v254, v8, s[64:67], 0 offen sc1\ns_waitcnt vmcnt(0)\nv_cmp_ge_u32 vcc, s32, v254\ns_nop 0\ns_cbranch_vccnz .Lr7_%=\n" \
    ".Lr15_%=:\nbuffer_load_dwordx4 a[0:3], v11, s[64:67], s82 offen offset:0 sc1\nbuffer_load_dwordx4 a[16:19], v11, s[64:67], s82 offen offset:512 sc1\n" \
    "buffer_load_dwordx4 a[4:7], v11, s[64:67], s82 offen offset:2048 sc1\nbuffer_load_dwordx4 a[20:23], v11, s[64:67], s82 offen offset:2560 sc1\ns_add_u32 s82, s82, 4096\n" \
    "buffer_load_dwordx4 a[8:11], v11, s[64:67], s82 offen offset:0 sc1\nbuffer_load_dwordx4 a[24:27], v11, s[64:67], s82 offen offset:512 sc1\n" \
    "buffer_load_dwordx4 a[12:15], v11, s[64:67], s82 offen offset:2048 sc1\nbuffer_load_dwordx4 a[28:31], v11, s[64:67], s82 offen offset:2560 sc1\ns_add_u32 s82, s82, 12288\n" \
    "buffer_load_dwordx4 a[32:35], v11, s[64:67], s82 offen offset:0 sc1\nbuffer_load_dwordx4 a[48:51], v11, s[64:67], s82 offen offset:512 sc1\n" \
    "buffer_load_dwordx4 a[36:39], v11, s[64:67], s82 offen offset:2048 sc1\nbuffer_load_dwordx4 a[52:55], v11, s[64:67], s82 offen offset:2560 sc1\ns_add_u32 s82, s82, 4096\n" \
    "buffer_load_dwordx4 a[40:43], v11, s[64:67], s82 offen offset:0 sc1\nbuffer_load_dwordx4 a[56:59], v11, s[64:67], s82 offen offset:512 sc1\n" \
    "buffer_load_dwordx4 a[44:47], v11, s[64:67], s82 offen offset:2048 sc1\nbuffer_load_dwordx4 a[60:63], v11, s[64:67], s82 offen offset:2560 sc1\ns_add_u32 s82, s82, 12288\n" \
    "buffer_load_dwordx4 a[64:67], v11, s[64:67], s82 offen offset:0 sc1\nbuffer_load_dwordx4 a[80:83], v11, s[64:67], s82 offen offset:512 sc1\n" \
    "buffer_load_dwordx4 a[68:71], v11, s[64:67], s82 offen offset:2048 sc1\nbuffer_load_dwordx4 a[84:87], v11, s[64:67], s82 offen offset:2560 sc1\ns_add_u32 s82, s82, 4096\n" \
    "buffer_load_dwordx4 a[72:75], v11, s[64:67], s82 offen offset:0 sc1\nbuffer_load_dwordx4 a[88:91], v11, s[64:67], s82 offen offset:512 sc1\n" \
    "buffer_load_dwordx4 a[76:79], v11, s[64:67], s82 offen offset:2048 sc1\nbuffer_load_dwordx4 a[92:95], v11, s[64:67], s82 offen offset:2560 sc1\ns_add_u32 s82, s82, 12288\n" \
    "buffer_load_dwordx4 a[96:99], v11, s[64:67], s82 offen offset:0 sc1\nbuffer_load_dwordx4 a[112:115], v11, s[64:67], s82 offen offset:512 sc1\n" \
    "buffer_load_dwordx4 a[100:103], v11, s[64:67], s82 offen offset:2048 sc1\nbuffer_load_dwordx4 a[116:119], v11, s[64:67], s82 offen offset:2560 sc1\n" \
    "s_add_u32 s82, s82, 4096\nbuffer_load_dwordx4 a[104:107], v11, s[64:67], s82 offen offset:0 sc1\nbuffer_load_dwordx4 a[120:123], v11, s[64:67], s82 offen offset:512 sc1\n" \
    "buffer_load_dwordx4 a[108:111], v11, s[64:67], s82 offen offset:2048 sc1\nbuffer_load_dwordx4 a[124:127], v11, s[64:67], s82 offen offset:2560 sc1\n" \
    "buffer_load_dword v255, off, s[64:67], s79 offset:64 sc1\ns_waitcnt vmcnt(0)\n.Lr25_%=:\nv_readfirstlane_b32 s74, v255\ns_lshr_b32 s69, s79, 16\ns_add_u32 s74, s74, s69\n" \
    "s_cmp_lt_u32 s74, 64\ns_cbranch_scc1 .Lr24_%=\ns_cmp_eq_u32 s32, 0\ns_cbranch_scc1 .Lr20_%=\n.Lr13_%=:\nds_read_b32 v253, v10\ns_waitcnt lgkmcnt(0)\n" \
    "v_readfirstlane_b32 s74, v253\ns_nop 0\ns_and_b32 s75, s74, 127\ns_cmp_lt_u32 s75, s32\ns_cbranch_scc1 .Lr14_%=\ns_and_b32 s57, s74, 128\ns_memrealtime s[72:73]\n" \
    "s_waitcnt lgkmcnt(0)\ns_lshr_b32 s69, s74, 8\ns_sub_u32 s69, s69, s72\ns_bfe_i32 s69, s69, 0x180000\ns_add_u32 s56, s72, s69\n.Lr3_%=:\ns_sub_u32 s76, s56, s59\n.Lr29_%=:\n" \
    "s_memrealtime s[72:73]\ns_waitcnt lgkmcnt(0)\ns_sub_u32 s72, s72, s76\ns_cmp_lt_i32 s72, 0\ns_cbranch_scc1 .Lr29_%=\ns_cmp_gt_i32 s72, 28\ns_cbranch_scc1 .Lr22_%=\n" \
    "v_bfe_u32 v24, v1, 2, 7\nbuffer_load_dword v24, v24, s[64:67], s60 offen sc1\ns_sub_u32 s76, s56, s34\ns_add_u32 s76, s76, 24\ns_lshl_b32 s76, s76, 8\n" \
    "s_lshl_b32 s77, s38, 8\ns_waitcnt vmcnt(0)\nv_and_b32 v25, 127, v24\nv_cmp_eq_u32 s[74:75], s32, v25\nv_subrev_u32 v25, s76, v24\nv_cmp_lt_i32 vcc, 0, v25\ns_nop 0\n" \
    "s_and_b64 vcc, vcc, s[74:75]\ns_cbranch_scc0 .Lr22_%=\ns_cmp_eq_u32 s32, 0\ns_cbranch_scc1 .Lr22_%=\n.Lr23_%=:\ns_add_u32 s56, s56, s38\ns_xor_b32 s57, s57, 128\n" \
    "v_subrev_u32 v25, s77, v25\nv_cmp_lt_i32 vcc, 0, v25\ns_nop 0\ns_and_b64 vcc, vcc, s[74:75]\ns_cbranch_scc1 .Lr23_%=\ns_add_u32 s76, s56, s35\ns_cmp_eq_u32 s48, 0\n" \
    "s_cbranch_scc1 .Lr30_%=\ns_lshl_b32 s74, s76, 8\ns_add_u32 s74, s74, s57\ns_add_u32 s74, s74, s32\ns_add_u32 s74, s74, 1\nv_mov_b32 v253, s74\ns_xor_b32 s69, s48, 512\n" \
    "s_mov_b64 exec, 1\nbuffer_store_dword v253, off, s[64:67], s69 sc1\ns_mov_b64 exec, -1\n.Lr30_%=:\n.Lr22_%=:\ns_lshl_b32 s69, s57, 7\ns_xor_b32 s84, s84, s69\n" \
    "s_lshl_b32 s69, s57, 5\ns_xor_b32 s92, s92, s69\ns_add_u32 s75, s83, 4096\ns_mov_b32 s68, 0\n.Lr8_%=:\ns_add_u32 s85, s84, 0x8000\ns_add_u32 s86, s84, 0x10000\n" \
    "s_add_u32 s87, s84, 0x18000\ns_sub_u32 s76, s56, s36\n.Lr31_%=:\ns_memrealtime s[72:73]\ns_waitcnt lgkmcnt(0)\ns_sub_u32 s72, s72, s76\ns_cmp_lt_i32 s72, 0\n" \
    "s_cbranch_scc1 .Lr31_%=\nbuffer_load_dwordx4 v[32:35], v1, s[64:67], s83 offen offset:0 sc1\nbuffer_load_dwordx4 v[36:39], v1, s[64:67], s83 offen offset:1024 sc1\n" \
    "buffer_load_dwordx4 v[40:43], v1, s[64:67], s83 offen offset:2048 sc1\nbuffer_load_dwordx4 v[44:47], v1, s[64:67], s83 offen offset:3072 sc1\n" \
    "buffer_load_dwordx4 v[48:51], v1, s[64:67], s75 offen offset:0 sc1\nbuffer_load_dwordx4 v[52:55], v1, s[64:67], s75 offen offset:1024 sc1\n" \
    "buffer_load_dwordx4 v[56:59], v1, s[64:67], s75 offen offset:2048 sc1\nbuffer_load_dwordx4 v[60:63], v1, s[64:67], s75 offen offset:3072 sc1\ns_cmp_lg_u32 s32, 63\n" \
    "s_cbranch_scc0 .Lr10_%=\n.Lr34_%=:\ns_memrealtime s[72:73]\ns_waitcnt lgkmcnt(0)\ns_sub_u32 s72, s72, s56\ns_cmp_lt_i32 s72, 0\ns_cbranch_scc1 .Lr34_%=\n" \
    "buffer_load_dwordx4 v[68:71], v1, s[64:67], s84 offen offset:0 sc1\nbuffer_load_dwordx4 v[72:75], v1, s[64:67], s84 offen offset:1024 sc1\n" \
    "buffer_load_dwordx4 v[76:79], v1, s[64:67], s84 offen offset:2048 sc1\nbuffer_load_dwordx4 v[80:83], v1, s[64:67], s84 offen offset:3072 sc1\n" \
    "buffer_load_dwordx4 v[88:91], v1, s[64:67], s85 offen offset:0 sc1\nbuffer_load_dwordx4 v[92:95], v1, s[64:67], s85 offen offset:1024 sc1\n" \
    "buffer_load_dwordx4 v[96:99], v1, s[64:67], s85 offen offset:2048 sc1\nbuffer_load_dwordx4 v[100:103], v1, s[64:67], s85 offen offset:3072 sc1\n" \
    "buffer_load_dwordx4 v[108:111], v1, s[64:67], s86 offen offset:0 sc1\nbuffer_load_dwordx4 v[112:115], v1, s[64:67], s86 offen offset:1024 sc1\n" \
    "buffer_load_dwordx4 v[116:119], v1, s[64:67], s86 offen offset:2048 sc1\nbuffer_load_dwordx4 v[120:123], v1, s[64:67], s86 offen offset:3072 sc1\n" \
    "buffer_load_dwordx4 v[128:131], v1, s[64:67], s87 offen offset:0 sc1\nbuffer_load_dwordx4 v[132:135], v1, s[64:67], s87 offen offset:1024 sc1\n" \
    "buffer_load_dwordx4 v[136:139], v1, s[64:67], s87 offen offset:2048 sc1\nbuffer_load_dwordx4 v[140:143], v1, s[64:67], s87 offen offset:3072 sc1\n" \
    "buffer_load_dwordx4 v[64:67], v2, s[64:67], s84 offen offset:0 sc1\nbuffer_load_dwordx4 v[84:87], v2, s[64:67], s85 offen offset:0 sc1\n" \
    "buffer_load_dwordx4 v[104:107], v2, s[64:67], s86 offen offset:0 sc1\nbuffer_load_dwordx4 v[124:127], v2, s[64:67], s87 offen offset:0 sc1\nv_mov_b64 v[24:25], 0\n" \
    "v_mov_b64 v[26:27], 0\ns_xor_b32 s77, s92, 0x1000\n.Lr33_%=:\ns_cmp_eq_u32 s32, 63\ns_waitcnt vmcnt(26)\nv_permlane16_swap_b32 v32, v33\nv_permlane16_swap_b32 v34, v35\n" \
    "v_permlane16_swap_b32 v36, v37\nv_permlane16_swap_b32 v38, v39\ns_waitcnt vmcnt(24)\nv_permlane16_swap_b32 v40, v41\nv_permlane16_swap_b32 v42, v43\n" \
    "v_permlane16_swap_b32 v44, v45\nv_permlane16_swap_b32 v46, v47\ns_waitcnt vmcnt(22)\nv_permlane16_swap_b32 v48, v49\nv_permlane16_swap_b32 v50, v51\n" \
    "v_permlane16_swap_b32 v52, v53\nv_permlane16_swap_b32 v54, v55\ns_waitcnt vmcnt(20)\nv_permlane16_swap_b32 v56, v57\nv_permlane16_swap_b32 v58, v59\n" \
    "v_permlane16_swap_b32 v60, v61\nv_permlane16_swap_b32 v62, v63\ns_cbranch_scc1 .Lr35_%=\ns_waitcnt vmcnt(19)\nv_smfmac_f32_16x16x64_bf16 v[24:27], v[68:71], a[0:7], v0\n" \
    "s_waitcnt vmcnt(18)\nv_smfmac_f32_16x16x64_bf16 v[24:27], v[72:75], a[8:15], v0\ns_waitcnt vmcnt(17)\nv_smfmac_f32_16x16x64_bf16 v[24:27], v[76:79], a[16:23], v0\n" \
    "s_waitcnt vmcnt(16)\nv_smfmac_f32_16x16x64_bf16 v[24:27], v[80:83], a[24:31], v0\ns_waitcnt vmcnt(15)\nv_smfmac_f32_16x16x64_bf16 v[24:27], v[88:91], a[32:39], v0\n" \
    "s_waitcnt vmcnt(14)\nv_smfmac_f32_16x16x64_bf16 v[24:27], v[92:95], a[40:47], v0\ns_waitcnt vmcnt(13)\nv_smfmac_f32_16x16x64_bf16 v[24:27], v[96:99], a[48:55], v0\n" \
    "s_waitcnt vmcnt(12)\nv_smfmac_f32_16x16x64_bf16 v[24:27], v[100:103], a[56:63], v0\ns_waitcnt vmcnt(11)\nv_smfmac_f32_16x16x64_bf16 v[24:27], v[108:111], a[64:71], v0\n" \
    "s_waitcnt vmcnt(10)\nv_smfmac_f32_16x16x64_bf16 v[24:27], v[112:115], a[72:79], v0\ns_waitcnt vmcnt(9)\nv_smfmac_f32_16x16x64_bf16 v[24:27], v[116:119], a[80:87], v0\n" \
    "s_waitcnt vmcnt(8)\nv_smfmac_f32_16x16x64_bf16 v[24:27], v[120:123], a[88:95], v0\ns_waitcnt vmcnt(7)\nv_smfmac_f32_16x16x64_bf16 v[24:27], v[128:131], a[96:103], v0\n" \
    "s_waitcnt vmcnt(6)\nv_smfmac_f32_16x16x64_bf16 v[24:27], v[132:135], a[104:111], v0\ns_waitcnt vmcnt(5)\nv_smfmac_f32_16x16x64_bf16 v[24:27], v[136:139], a[112:119], v0\n" \
    "s_waitcnt vmcnt(4)\nv_smfmac_f32_16x16x64_bf16 v[24:27], v[140:143], a[120:127], v0\ns_waitcnt vmcnt(3)\nv_smfmac_f32_16x16x64_bf16 v[24:27], v[64:67], v[32:39], v0\n" \
    "s_waitcnt vmcnt(2)\nv_smfmac_f32_16x16x64_bf16 v[24:27], v[84:87], v[40:47], v0\ns_waitcnt vmcnt(1)\nv_smfmac_f32_16x16x64_bf16 v[24:27], v[104:107], v[48:55], v0\n" \
    "s_waitcnt vmcnt(0)\nv_smfmac_f32_16x16x64_bf16 v[24:27], v[124:127], v[56:63], v0\ns_nop 7\nv_permlane32_swap_b32 v24, v26\nv_permlane32_swap_b32 v25, v27\n" \
    "v_pk_add_f32 v[24:25], v[24:25], v[26:27]\nv_cvt_pk_bf16_f32 v26, v24, v25\nds_write_b32 v3, v26\nds_read_b64_tr_b16 v[28:29], v4\n" \
    "ds_read_b64_tr_b16 v[30:31], v4 offset:64\nv_cmp_u_f32 vcc, v24, v25\ns_nop 0\ns_cbranch_vccnz .Lr17_%=\n.Lr16_%=:\ns_waitcnt lgkmcnt(0)\n" \
    "buffer_store_dwordx4 v[28:31], v6, s[64:67], s92 offen sc1\nbuffer_store_dwordx4 v[28:31], v6, s[64:67], s77 offen sc1\n.Lr10_%=:\ns_memrealtime s[72:73]\n" \
    "s_waitcnt lgkmcnt(0)\ns_add_u32 s76, s56, s35\n.Lr37_%=:\ns_sub_u32 s69, s76, s72\ns_cmp_ge_i32 s69, s37\ns_cbranch_scc1 .Lr38_%=\ns_add_u32 s76, s76, s38\n" \
    "s_xor_b32 s57, s57, 128\ns_branch .Lr37_%=\n.Lr38_%=:\ns_add_u32 s76, s76, s34\ns_add_u32 s76, s76, s41\ns_cmp_ge_u32 s32, 57\ns_cselect_b32 s41, -8, 0\n" \
    "s_lshl_b32 s74, s76, 8\ns_add_u32 s74, s74, s57\ns_add_u32 s74, s74, s32\ns_add_u32 s74, s74, 1\nv_mov_b32 v253, s74\nds_write_b32 v10, v253\ns_sub_u32 s78, s76, s40\n" \
    "s_add_u32 s76, s76, s35\ns_cmp_ge_u32 s32, 60\ns_cselect_b32 s79, 60, s39\ns_cmp_eq_u32 s32, 63\ns_cselect_b32 s79, s61, s79\ns_add_u32 s79, s79, s56\n" \
    "s_cmp_ge_u32 s32, 62\ns_cselect_b32 s76, s79, s76\ns_cmp_eq_u32 s48, 0\ns_cbranch_scc1 .Lr39_%=\ns_lshl_b32 s74, s76, 8\ns_add_u32 s74, s74, s57\ns_add_u32 s74, s74, s32\n" \
    "s_add_u32 s74, s74, 2\nv_mov_b32 v253, s74\ns_xor_b32 s69, s48, 0\ns_mov_b64 exec, 1\nbuffer_store_dword v253, off, s[64:67], s69 sc1\ns_mov_b64 exec, -1\n.Lr39_%=:\n" \
    "s_xor_b32 s69, s49, 0x20000\ns_add_u32 s0, s94, s69\ns_add_u32 s1, s0, 0x4000\ns_add_u32 s2, s0, 0x8000\ns_add_u32 s3, s0, 0xc000\ns_xor_b32 s69, s49, 0x30000\n" \
    "s_add_u32 s4, s94, s69\ns_add_u32 s5, s4, 0x4000\ns_add_u32 s6, s4, 0x8000\ns_add_u32 s7, s4, 0xc000\ns_xor_b32 s69, s49, 0x0\ns_add_u32 s8, s94, s69\n" \
    "s_add_u32 s9, s8, 0x4000\ns_add_u32 s10, s8, 0x8000\ns_add_u32 s11, s8, 0xc000\ns_xor_b32 s69, s49, 0x10000\ns_add_u32 s12, s94, s69\ns_add_u32 s13, s12, 0x4000\n" \
    "s_add_u32 s14, s12, 0x8000\ns_add_u32 s15, s12, 0xc000\ns_mov_b32 s68, 0\ns_mov_b32 s80, 0\n.Lr40_%=:\ns_memrealtime s[72:73]\ns_waitcnt lgkmcnt(0)\n" \
    "s_sub_u32 s72, s72, s79\ns_cmp_lt_i32 s72, 0\ns_cbranch_scc1 .Lr40_%=\n.Lr11_%=:\nv_mov_b64 v[24:25], 0\nv_mov_b64 v[26:27], 0\nv_mov_b64 v[28:29], 0\n" \
    "v_mov_b64 v[30:31], 0\nv_mov_b64 v[228:229], 0\nv_mov_b64 v[230:231], 0\nv_mov_b64 v[232:233], 0\nv_mov_b64 v[234:235], 0\nv_mov_b32 v252, 0\ns_add_u32 s76, s94, 5120\n" \
    "buffer_load_dword v250, off, s[64:67], s76 sc1\ns_lshr_b32 s69, s32, 2\ns_cmp_eq_u32 s69, 1\ns_cselect_b32 s35, s63, s35\n" \
    "buffer_load_dwordx4 v[64:67], v2, s[64:67], s0 offen offset:0 sc1\nbuffer_load_dwordx4 v[84:87], v2, s[64:67], s1 offen offset:0 sc1\n" \
    "buffer_load_dwordx4 v[104:107], v2, s[64:67], s2 offen offset:0 sc1\nbuffer_load_dwordx4 v[124:127], v2, s[64:67], s3 offen offset:0 sc1\n" \
    "buffer_load_dwordx4 v[144:147], v2, s[64:67], s4 offen offset:0 sc1\nbuffer_load_dwordx4 v[148:151], v2, s[64:67], s5 offen offset:0 sc1\n" \
    "buffer_load_dwordx4 v[152:155], v2, s[64:67], s6 offen offset:0 sc1\nbuffer_load_dwordx4 v[156:159], v2, s[64:67], s7 offen offset:0 sc1\n" \
    "buffer_load_dwordx4 v[160:163], v2, s[64:67], s8 offen offset:0 sc1\nbuffer_load_dwordx4 v[164:167], v2, s[64:67], s9 offen offset:0 sc1\n" \
    "buffer_load_dwordx4 v[168:171], v2, s[64:67], s10 offen offset:0 sc1\nbuffer_load_dwordx4 v[172:175], v2, s[64:67], s11 offen offset:0 sc1\n" \
    "buffer_load_dwordx4 v[176:179], v2, s[64:67], s12 offen offset:0 sc1\nbuffer_load_dwordx4 v[180:183], v2, s[64:67], s13 offen offset:0 sc1\n" \
    "buffer_load_dwordx4 v[184:187], v2, s[64:67], s14 offen offset:0 sc1\nbuffer_load_dwordx4 v[188:191], v2, s[64:67], s15 offen offset:0 sc1\n" \
    "buffer_load_dwordx4 v[68:71], v1, s[64:67], s0 offen offset:0 sc1\nbuffer_load_dwordx4 v[72:75], v1, s[64:67], s0 offen offset:1024 sc1\n" \
    "buffer_load_dwordx4 v[76:79], v1, s[64:67], s0 offen offset:2048 sc1\nbuffer_load_dwordx4 v[80:83], v1, s[64:67], s0 offen offset:3072 sc1\n" \
    "buffer_load_dwordx4 v[88:91], v1, s[64:67], s1 offen offset:0 sc1\nbuffer_load_dwordx4 v[92:95], v1, s[64:67], s1 offen offset:1024 sc1\n" \
    "buffer_load_dwordx4 v[96:99], v1, s[64:67], s1 offen offset:2048 sc1\nbuffer_load_dwordx4 v[100:103], v1, s[64:67], s1 offen offset:3072 sc1\n" \
    "buffer_load_dwordx4 v[108:111], v1, s[64:67], s2 offen offset:0 sc1\nbuffer_load_dwordx4 v[112:115], v1, s[64:67], s2 offen offset:1024 sc1\n" \
    "buffer_load_dwordx4 v[116:119], v1, s[64:67], s2 offen offset:2048 sc1\nbuffer_load_dwordx4 v[120:123], v1, s[64:67], s2 offen offset:3072 sc1\n" \
    "buffer_load_dwordx4 v[128:131], v1, s[64:67], s3 offen offset:0 sc1\nbuffer_load_dwordx4 v[132:135], v1, s[64:67], s3 offen offset:1024 sc1\n" \
    "buffer_load_dwordx4 v[136:139], v1, s[64:67], s3 offen offset:2048 sc1\nbuffer_load_dwordx4 v[140:143], v1, s[64:67], s3 offen offset:3072 sc1\ns_sub_u32 s69, s32, s68\n" \
    "s_cmp_lg_u32 s69, 63\ns_cbranch_scc1 .Lr35_%=\ns_branch .Lr33_%=\n.Lr35_%=:\ns_waitcnt vmcnt(30)\nv_smfmac_f32_16x16x64_bf16 v[24:27], v[64:67], v[32:39], v0\n" \
    "buffer_load_dwordx4 a[128:131], v1, s[64:67], s4 offen offset:0 sc1\nv_readfirstlane_b32 s34, v250\nv_smfmac_f32_16x16x64_bf16 v[24:27], v[84:87], v[40:47], v0\n" \
    "buffer_load_dwordx4 a[132:135], v1, s[64:67], s4 offen offset:1024 sc1\ns_waitcnt vmcnt(30)\nv_smfmac_f32_16x16x64_bf16 v[24:27], v[104:107], v[48:55], v0\n" \
    "buffer_load_dwordx4 a[136:139], v1, s[64:67], s4 offen offset:2048 sc1\nv_max3_u32 v250, v250, v64, v84\nv_smfmac_f32_16x16x64_bf16 v[24:27], v[124:127], v[56:63], v0\n" \
    "buffer_load_dwordx4 a[140:143], v1, s[64:67], s4 offen offset:3072 sc1\ns_waitcnt vmcnt(30)\nv_max3_u32 v250, v250, v104, v124\n" \
    "v_smfmac_f32_16x16x64_bf16 v[28:31], v[144:147], v[32:39], v0\nbuffer_load_dwordx4 a[144:147], v1, s[64:67], s5 offen offset:0 sc1\n" \
    "v_smfmac_f32_16x16x64_bf16 v[28:31], v[148:151], v[40:47], v0\nbuffer_load_dwordx4 a[148:151], v1, s[64:67], s5 offen offset:1024 sc1\ns_waitcnt vmcnt(30)\n" \
    "v_smfmac_f32_16x16x64_bf16 v[28:31], v[152:155], v[48:55], v0\nbuffer_load_dwordx4 a[152:155], v1, s[64:67], s5 offen offset:2048 sc1\nv_max3_u32 v250, v250, v144, v148\n" \
    "v_smfmac_f32_16x16x64_bf16 v[28:31], v[156:159], v[56:63], v0\nbuffer_load_dwordx4 a[156:159], v1, s[64:67], s5 offen offset:3072 sc1\ns_waitcnt vmcnt(30)\n" \
    "v_max3_u32 v250, v250, v152, v156\nv_smfmac_f32_16x16x64_bf16 v[228:231], v[160:163], v[32:39], v0\nbuffer_load_dwordx4 a[160:163], v1, s[64:67], s6 offen offset:0 sc1\n" \
    "v_smfmac_f32_16x16x64_bf16 v[228:231], v[164:167], v[40:47], v0\nbuffer_load_dwordx4 a[164:167], v1, s[64:67], s6 offen offset:1024 sc1\ns_waitcnt vmcnt(30)\n" \
    "v_smfmac_f32_16x16x64_bf16 v[228:231], v[168:171], v[48:55], v0\nbuffer_load_dwordx4 a[168:171], v1, s[64:67], s6 offen offset:2048 sc1\nv_max3_u32 v250, v250, v160, v164\n" \
    "v_smfmac_f32_16x16x64_bf16 v[228:231], v[172:175], v[56:63], v0\nbuffer_load_dwordx4 a[172:175], v1, s[64:67], s6 offen offset:3072 sc1\ns_waitcnt vmcnt(30)\n" \
    "v_max3_u32 v250, v250, v168, v172\nv_smfmac_f32_16x16x64_bf16 v[232:235], v[176:179], v[32:39], v0\nbuffer_load_dwordx4 a[176:179], v1, s[64:67], s7 offen offset:0 sc1\n" \
    "v_smfmac_f32_16x16x64_bf16 v[232:235], v[180:183], v[40:47], v0\nbuffer_load_dwordx4 a[180:183], v1, s[64:67], s7 offen offset:1024 sc1\ns_waitcnt vmcnt(30)\n" \
    "v_smfmac_f32_16x16x64_bf16 v[232:235], v[184:187], v[48:55], v0\nbuffer_load_dwordx4 a[184:187], v1, s[64:67], s7 offen offset:2048 sc1\nv_max3_u32 v250, v250, v176, v180\n" \
    "v_smfmac_f32_16x16x64_bf16 v[232:235], v[188:191], v[56:63], v0\nbuffer_load_dwordx4 a[188:191], v1, s[64:67], s7 offen offset:3072 sc1\ns_waitcnt vmcnt(30)\n" \
    "v_max3_u32 v250, v250, v184, v188\nv_smfmac_f32_16x16x64_bf16 v[24:27], v[68:71], a[0:7], v0\nbuffer_load_dwordx4 a[192:195], v1, s[64:67], s8 offen offset:0 sc1\n" \
    "v_cmp_eq_u32 vcc, -1, v250\nv_smfmac_f32_16x16x64_bf16 v[24:27], v[72:75], a[8:15], v0\nbuffer_load_dwordx4 a[196:199], v1, s[64:67], s8 offen offset:1024 sc1\n" \
    "s_waitcnt vmcnt(30)\nv_smfmac_f32_16x16x64_bf16 v[24:27], v[76:79], a[16:23], v0\nbuffer_load_dwordx4 a[200:203], v1, s[64:67], s9 offen offset:0 sc1\n" \
    "s_cbranch_vccnz .Lr41_%=\nv_smfmac_f32_16x16x64_bf16 v[24:27], v[80:83], a[24:31], v0\nbuffer_load_dwordx4 a[204:207], v1, s[64:67], s9 offen offset:1024 sc1\n" \
    "s_waitcnt vmcnt(30)\nv_smfmac_f32_16x16x64_bf16 v[24:27], v[88:91], a[32:39], v0\nbuffer_load_dwordx4 a[208:211], v1, s[64:67], s10 offen offset:0 sc1\n" \
    "v_smfmac_f32_16x16x64_bf16 v[24:27], v[92:95], a[40:47], v0\nbuffer_load_dwordx4 a[212:215], v1, s[64:67], s10 offen offset:1024 sc1\ns_waitcnt vmcnt(30)\n" \
    "v_smfmac_f32_16x16x64_bf16 v[24:27], v[96:99], a[48:55], v0\nbuffer_load_dwordx4 a[216:219], v1, s[64:67], s11 offen offset:0 sc1\n" \
    "v_smfmac_f32_16x16x64_bf16 v[24:27], v[100:103], a[56:63], v0\nbuffer_load_dwordx4 a[220:223], v1, s[64:67], s11 offen offset:1024 sc1\ns_waitcnt vmcnt(30)\n" \
    "v_smfmac_f32_16x16x64_bf16 v[24:27], v[108:111], a[64:71], v0\nbuffer_load_dwordx4 a[224:227], v1, s[64:67], s12 offen offset:0 sc1\n" \
    "v_smfmac_f32_16x16x64_bf16 v[24:27], v[112:115], a[72:79], v0\nbuffer_load_dwordx4 a[228:231], v1, s[64:67], s12 offen offset:1024 sc1\ns_waitcnt vmcnt(30)\n" \
    "v_smfmac_f32_16x16x64_bf16 v[24:27], v[116:119], a[80:87], v0\nbuffer_load_dwordx4 a[232:235], v1, s[64:67], s13 offen offset:0 sc1\n" \
    "v_smfmac_f32_16x16x64_bf16 v[24:27], v[120:123], a[88:95], v0\nbuffer_load_dwordx4 a[236:239], v1, s[64:67], s13 offen offset:1024 sc1\ns_waitcnt vmcnt(30)\n" \
    "v_smfmac_f32_16x16x64_bf16 v[24:27], v[128:131], a[96:103], v0\nbuffer_load_dwordx4 a[240:243], v1, s[64:67], s14 offen offset:0 sc1\n" \
    "v_smfmac_f32_16x16x64_bf16 v[24:27], v[132:135], a[104:111], v0\nbuffer_load_dwordx4 a[244:247], v1, s[64:67], s14 offen offset:1024 sc1\ns_waitcnt vmcnt(30)\n" \
    "v_smfmac_f32_16x16x64_bf16 v[24:27], v[136:139], a[112:119], v0\nbuffer_load_dwordx4 a[248:251], v1, s[64:67], s15 offen offset:0 sc1\n" \
    "v_smfmac_f32_16x16x64_bf16 v[24:27], v[140:143], a[120:127], v0\nbuffer_load_dwordx4 a[252:255], v1, s[64:67], s15 offen offset:1024 sc1\ns_nop 1\ns_waitcnt vmcnt(30)\n" \
    "v_smfmac_f32_16x16x64_bf16 v[28:31], a[128:131], a[0:7], v0\ns_nop 2\nv_permlane32_swap_b32 v24, v26\nv_smfmac_f32_16x16x64_bf16 v[28:31], a[132:135], a[8:15], v0\n" \
    "s_waitcnt vmcnt(28)\nv_permlane32_swap_b32 v25, v27\nv_smfmac_f32_16x16x64_bf16 v[28:31], a[136:139], a[16:23], v0\nv_add_f32 v24, v24, v26\n" \
    "v_smfmac_f32_16x16x64_bf16 v[28:31], a[140:143], a[24:31], v0\ns_waitcnt vmcnt(26)\nv_add_f32 v25, v25, v27\nv_smfmac_f32_16x16x64_bf16 v[28:31], a[144:147], a[32:39], v0\n" \
    "v_fma_f32 v252, v24, v25, v252\nv_smfmac_f32_16x16x64_bf16 v[28:31], a[148:151], a[40:47], v0\ns_waitcnt vmcnt(24)\nv_cvt_pk_bf16_f32 v24, v24, v25\n" \
    "v_smfmac_f32_16x16x64_bf16 v[28:31], a[152:155], a[48:55], v0\nds_write_b32 v3, v24 offset:512\nv_smfmac_f32_16x16x64_bf16 v[28:31], a[156:159], a[56:63], v0\n" \
    "s_waitcnt vmcnt(22)\nv_smfmac_f32_16x16x64_bf16 v[28:31], a[160:163], a[64:71], v0\nv_smfmac_f32_16x16x64_bf16 v[28:31], a[164:167], a[72:79], v0\ns_waitcnt vmcnt(20)\n" \
    "v_smfmac_f32_16x16x64_bf16 v[28:31], a[168:171], a[80:87], v0\nv_smfmac_f32_16x16x64_bf16 v[28:31], a[172:175], a[88:95], v0\ns_waitcnt vmcnt(18)\n" \
    "v_smfmac_f32_16x16x64_bf16 v[28:31], a[176:179], a[96:103], v0\nv_smfmac_f32_16x16x64_bf16 v[28:31], a[180:183], a[104:111], v0\ns_waitcnt vmcnt(16)\n" \
    "v_smfmac_f32_16x16x64_bf16 v[28:31], a[184:187], a[112:119], v0\nv_smfmac_f32_16x16x64_bf16 v[28:31], a[188:191], a[120:127], v0\ns_nop 2\ns_waitcnt vmcnt(14)\n" \
    "v_smfmac_f32_16x16x64_bf16 v[228:231], a[192:195], a[0:7], v0\ns_nop 2\nv_permlane32_swap_b32 v28, v30\nv_smfmac_f32_16x16x64_bf16 v[228:231], a[196:199], a[8:15], v0\n" \
    "s_waitcnt vmcnt(12)\nv_permlane32_swap_b32 v29, v31\nv_smfmac_f32_16x16x64_bf16 v[228:231], a[200:203], a[32:39], v0\nv_add_f32 v28, v28, v30\n" \
    "v_smfmac_f32_16x16x64_bf16 v[228:231], a[204:207], a[40:47], v0\ns_waitcnt vmcnt(10)\nv_add_f32 v29, v29, v31\n" \
    "v_smfmac_f32_16x16x64_bf16 v[228:231], a[208:211], a[64:71], v0\nv_fma_f32 v252, v28, v29, v252\nv_smfmac_f32_16x16x64_bf16 v[228:231], a[212:215], a[72:79], v0\n" \
    "s_waitcnt vmcnt(8)\nv_cvt_pk_bf16_f32 v28, v28, v29\nv_smfmac_f32_16x16x64_bf16 v[228:231], a[216:219], a[96:103], v0\nds_write_b32 v3, v28 offset:768\n" \
    "v_smfmac_f32_16x16x64_bf16 v[228:231], a[220:223], a[104:111], v0\ns_nop 2\ns_waitcnt vmcnt(6)\nv_smfmac_f32_16x16x64_bf16 v[232:235], a[224:227], a[0:7], v0\ns_nop 2\n" \
    "v_permlane32_swap_b32 v228, v230\nv_smfmac_f32_16x16x64_bf16 v[232:235], a[228:231], a[8:15], v0\ns_waitcnt vmcnt(4)\nv_permlane32_swap_b32 v229, v231\n" \
    "v_smfmac_f32_16x16x64_bf16 v[232:235], a[232:235], a[32:39], v0\nv_add_f32 v228, v228, v230\nv_smfmac_f32_16x16x64_bf16 v[232:235], a[236:239], a[40:47], v0\n" \
    "s_waitcnt vmcnt(2)\nv_add_f32 v229, v229, v231\nv_smfmac_f32_16x16x64_bf16 v[232:235], a[240:243], a[64:71], v0\nv_fma_f32 v252, v228, v229, v252\n" \
    "v_smfmac_f32_16x16x64_bf16 v[232:235], a[244:247], a[72:79], v0\ns_waitcnt vmcnt(0)\nv_cvt_pk_bf16_f32 v228, v228, v229\n" \
    "v_smfmac_f32_16x16x64_bf16 v[232:235], a[248:251], a[96:103], v0\nds_write_b32 v3, v228 offset:0\nv_smfmac_f32_16x16x64_bf16 v[232:235], a[252:255], a[104:111], v0\n" \
    "s_nop 7\nv_permlane32_swap_b32 v232, v234\nv_permlane32_swap_b32 v233, v235\nv_add_f32 v232, v232, v234\nv_add_f32 v233, v233, v235\nv_fma_f32 v252, v232, v233, v252\n" \
    "v_cvt_pk_bf16_f32 v232, v232, v233\nv_cmp_u_f32 vcc, v252, v252\nds_write_b32 v3, v232 offset:256\nds_read_b64_tr_b16 v[24:25], v5\n" \
    "ds_read_b64_tr_b16 v[26:27], v5 offset:64\ns_cbranch_vccnz .Lr12_%=\n.Lr18_%=:\ns_cmp_lt_u32 s32, 4\ns_cbranch_scc1 .Lr43_%=\n.Lr44_%=:\ns_memrealtime s[72:73]\n" \
    "v_lshlrev_b32 v72, 16, v224\nv_and_b32 v73, 0xffff0000, v224\nv_lshlrev_b32 v74, 16, v225\nv_and_b32 v75, 0xffff0000, v225\nv_lshlrev_b32 v76, 16, v226\n" \
    "v_and_b32 v77, 0xffff0000, v226\nv_lshlrev_b32 v78, 16, v227\nv_and_b32 v79, 0xffff0000, v227\ns_xor_b32 s77, s93, 0x2000\ns_waitcnt lgkmcnt(0)\ns_sub_u32 s78, s72, s78\n" \
    "s_cmp_lt_u32 s32, 60\ns_cselect_b32 s78, 0, s78\ns_cmp_ge_u32 s32, 62\ns_cselect_b32 s78, -1, s78\nv_lshlrev_b32 v64, 16, v24\nv_and_b32 v65, 0xffff0000, v24\n" \
    "v_lshlrev_b32 v66, 16, v25\nv_and_b32 v67, 0xffff0000, v25\nv_lshlrev_b32 v68, 16, v26\nv_and_b32 v69, 0xffff0000, v26\nv_lshlrev_b32 v70, 16, v27\n" \
    "v_and_b32 v71, 0xffff0000, v27\nv_mul_f32 v64, v64, v16\nv_mul_f32 v65, v65, v17\nv_mul_f32 v66, v66, v18\nv_mul_f32 v67, v67, v19\nv_mul_f32 v68, v68, v20\n" \
    "v_mul_f32 v69, v69, v21\nv_mul_f32 v70, v70, v22\nv_mul_f32 v71, v71, v23\nv_cvt_pk_bf16_f32 v64, 0, v64\nv_cvt_pk_bf16_f32 v65, 0, v65\nv_cvt_pk_bf16_f32 v66, 0, v66\n" \
    "v_cvt_pk_bf16_f32 v67, 0, v67\nv_cvt_pk_bf16_f32 v68, 0, v68\nv_cvt_pk_bf16_f32 v69, 0, v69\nv_cvt_pk_bf16_f32 v70, 0, v70\nv_cvt_pk_bf16_f32 v71, 0, v71\n" \
    "v_add_f32 v64, v64, v72\nv_add_f32 v65, v65, v73\nv_add_f32 v66, v66, v74\nv_add_f32 v67, v67, v75\nv_add_f32 v68, v68, v76\nv_add_f32 v69, v69, v77\n" \
    "v_add_f32 v70, v70, v78\nv_add_f32 v71, v71, v79\nv_cvt_pk_bf16_f32 v24, v64, v65\nv_cvt_pk_bf16_f32 v25, v66, v67\nv_cvt_pk_bf16_f32 v26, v68, v69\n" \
    "v_cvt_pk_bf16_f32 v27, v70, v71\nv_min_u32 v28, -2, v24\nv_min_u32 v29, -2, v25\nv_min_u32 v30, -2, v26\nv_min_u32 v31, -2, v27\ns_cmp_ge_i32 s78, 0\n" \
    "s_cbranch_scc1 .Lr45_%=\nbuffer_store_dwordx4 v[28:31], v1, s[64:67], s93 offen sc1\nbuffer_store_dwordx4 v[28:31], v1, s[64:67], s77 offen sc1\n.Lr45_%=:\n" \
    "v_lshlrev_b32 v64, 16, v24\nv_and_b32 v65, 0xffff0000, v24\nv_lshlrev_b32 v66, 16, v25\nv_and_b32 v67, 0xffff0000, v25\nv_lshlrev_b32 v68, 16, v26\n" \
    "v_and_b32 v69, 0xffff0000, v26\nv_lshlrev_b32 v70, 16, v27\nv_and_b32 v71, 0xffff0000, v27\nv_add_f32 v72, v64, v65\nv_add_f32 v73, v66, v67\nv_add_f32 v74, v68, v69\n" \
    "v_add_f32 v75, v70, v71\nv_add_f32 v72, v72, v73\nv_add_f32 v74, v74, v75\nv_add_f32 v76, v72, v74\nv_add_f32 v77, v72, v74\ns_nop 1\nv_permlane32_swap_b32 v76, v77\n" \
    "v_add_f32 v76, v76, v77\nv_mul_f32 v76, 0x3d800000, v76\nv_sub_f32 v64, v64, v76\nv_sub_f32 v65, v65, v76\nv_sub_f32 v66, v66, v76\nv_sub_f32 v67, v67, v76\n" \
    "v_sub_f32 v68, v68, v76\nv_sub_f32 v69, v69, v76\nv_sub_f32 v70, v70, v76\nv_sub_f32 v71, v71, v76\nv_mul_f32 v72, v64, v64\nv_mul_f32 v73, v65, v65\n" \
    "v_fmac_f32 v72, v66, v66\nv_fmac_f32 v73, v67, v67\nv_fmac_f32 v72, v68, v68\nv_fmac_f32 v73, v69, v69\nv_fmac_f32 v72, v70, v70\nv_fmac_f32 v73, v71, v71\n" \
    "v_add_f32 v74, v72, v73\nv_add_f32 v75, v72, v73\ns_nop 1\nv_permlane32_swap_b32 v74, v75\nv_add_f32 v77, v74, v75\nv_min_u32 v76, -2, v76\nv_min_u32 v77, -2, v77\n" \
    "s_cmp_lt_i32 s78, 0\ns_cbranch_scc1 .Lr46_%=\ns_add_u32 s76, s32, 2\n.Lr48_%=:\nds_read_b32 v253, v10\ns_waitcnt lgkmcnt(0)\nv_readfirstlane_b32 s74, v253\ns_nop 0\n" \
    "s_and_b32 s75, s74, 127\ns_cmp_lt_u32 s75, s76\ns_cbranch_scc1 .Lr49_%=\nbuffer_store_dwordx4 v[28:31], v1, s[64:67], s93 offen sc1\n" \
    "buffer_store_dwordx4 v[28:31], v1, s[64:67], s77 offen sc1\n.Lr46_%=:\nbuffer_store_dwordx2 v[76:77], v9, s[64:67], s93 offen sc1\ns_cmp_ge_u32 s32, 60\n" \
    "s_cbranch_scc1 .Lr50_%=\ns_add_u32 s76, s32, 2\n.Lr52_%=:\nds_read_b32 v253, v10\ns_waitcnt lgkmcnt(0)\nv_readfirstlane_b32 s74, v253\ns_nop 0\ns_and_b32 s75, s74, 127\n" \
    "s_cmp_lt_u32 s75, s76\ns_cbranch_scc1 .Lr53_%=\ns_cmp_ge_u32 s32, 56\ns_cbranch_scc1 .Lr50_%=\nv_mov_b64 v[80:81], -1\nv_mov_b64 v[82:83], -1\ns_add_u32 s69, s32, 9\n" \
    "s_and_b32 s69, s69, 15\ns_lshl_b32 s69, s69, 17\ns_add_u32 s92, s46, s69\ns_xor_b32 s74, s92, 0x1000\nbuffer_store_dwordx4 v[80:83], v6, s[64:67], s92 offen sc1\n" \
    "buffer_store_dwordx4 v[80:83], v6, s[64:67], s74 offen sc1\n.Lr50_%=:\nv_add_u32 v7, 0x40000, v7\ns_add_u32 s32, s32, 4\ns_cmp_lt_u32 s32, 64\ns_cbranch_scc1 .Lr2_%=\n" \
    "s_endpgm\n.Lr24_%=:\ns_sleep 16\nbuffer_load_dword v255, off, s[64:67], s79 offset:64 sc1\ns_waitcnt vmcnt(0)\ns_add_u32 s68, s68, 1\ns_and_b32 s69, s68, 63\n" \
    "s_cbranch_scc1 .Lr25_%=\ns_cmp_gt_u32 s68, 0x400000\ns_cbranch_scc1 .Lr1_%=\nbuffer_load_dword v253, off, s[64:67], 0 sc1\ns_waitcnt vmcnt(0)\n" \
    "v_readfirstlane_b32 s69, v253\ns_nop 0\ns_cmp_eq_u32 s69, 1\ns_cbranch_scc0 .Lr25_%=\ns_endpgm\n.Lr5_%=:\ns_sleep 2\ns_add_u32 s68, s68, 1\ns_and_b32 s69, s68, 63\n" \
    "s_cbranch_scc1 .Lr4_%=\ns_cmp_gt_u32 s68, 0x400000\ns_cbranch_scc1 .Lr1_%=\nbuffer_load_dword v253, off, s[64:67], 0 sc1\ns_waitcnt vmcnt(0)\n" \
    "v_readfirstlane_b32 s69, v253\ns_nop 0\ns_cmp_eq_u32 s69, 1\ns_cbranch_scc0 .Lr4_%=\ns_endpgm\n.Lr7_%=:\ns_sleep 6\ns_add_u32 s68, s68, 1\ns_and_b32 s69, s68, 63\n" \
    "s_cbranch_scc1 .Lr6_%=\ns_cmp_gt_u32 s68, 0x400000\ns_cbranch_scc1 .Lr1_%=\nbuffer_load_dword v253, off, s[64:67], 0 sc1\ns_waitcnt vmcnt(0)\n" \
    "v_readfirstlane_b32 s69, v253\ns_nop 0\ns_cmp_eq_u32 s69, 1\ns_cbranch_scc0 .Lr6_%=\ns_endpgm\n.Lr14_%=:\ns_sleep 2\ns_add_u32 s68, s68, 1\ns_and_b32 s69, s68, 63\n" \
    "s_cbranch_scc1 .Lr13_%=\ns_cmp_gt_u32 s68, 0x400000\ns_cbranch_scc1 .Lr1_%=\nbuffer_load_dword v253, off, s[64:67], 0 sc1\ns_waitcnt vmcnt(0)\n" \
    "v_readfirstlane_b32 s69, v253\ns_nop 0\ns_cmp_eq_u32 s69, 1\ns_cbranch_scc0 .Lr13_%=\ns_endpgm\n.Lr20_%=:\nbuffer_load_dword v253, off, s[64:67], s60 offset:128 sc1\n" \
    "s_waitcnt vmcnt(0)\nv_readfirstlane_b32 s74, v253\ns_nop 0\ns_cmp_eq_u32 s74, 0\ns_cbranch_scc1 .Lr21_%=\ns_andn2_b32 s56, s74, 3\ns_add_u32 s76, s56, s35\n" \
    "s_cmp_eq_u32 s48, 0\ns_cbranch_scc1 .Lr28_%=\ns_lshl_b32 s74, s76, 8\ns_add_u32 s74, s74, s57\ns_add_u32 s74, s74, s32\ns_add_u32 s74, s74, 1\nv_mov_b32 v253, s74\n" \
    "s_xor_b32 s69, s48, 512\ns_mov_b64 exec, 1\nbuffer_store_dword v253, off, s[64:67], s69 sc1\ns_mov_b64 exec, -1\n.Lr28_%=:\ns_branch .Lr3_%=\n.Lr21_%=:\ns_sleep 4\n" \
    "s_add_u32 s68, s68, 1\ns_and_b32 s69, s68, 63\ns_cbranch_scc1 .Lr20_%=\ns_cmp_gt_u32 s68, 0x400000\ns_cbranch_scc1 .Lr1_%=\nbuffer_load_dword v253, off, s[64:67], 0 sc1\n" \
    "s_waitcnt vmcnt(0)\nv_readfirstlane_b32 s69, v253\ns_nop 0\ns_cmp_eq_u32 s69, 1\ns_cbranch_scc0 .Lr20_%=\ns_endpgm\n.Lr17_%=:\ns_cmp_lt_u32 s68, 3\ns_cbranch_scc1 .Lr9_%=\n" \
    "v_max3_u32 v252, v32, v33, v34\nv_max3_u32 v253, v35, v36, v37\nv_max3_u32 v252, v252, v38, v39\nv_max3_u32 v253, v253, v40, v41\nv_max3_u32 v252, v252, v42, v43\n" \
    "v_max3_u32 v253, v253, v44, v45\nv_max3_u32 v252, v252, v46, v47\nv_max3_u32 v253, v253, v48, v49\nv_max3_u32 v252, v252, v50, v51\nv_max3_u32 v253, v253, v52, v53\n" \
    "v_max3_u32 v252, v252, v54, v55\nv_max3_u32 v253, v253, v56, v57\nv_max3_u32 v252, v252, v58, v59\nv_max3_u32 v253, v253, v60, v61\nv_max3_u32 v252, v252, v62, v63\n" \
    "v_max3_u32 v252, v252, v64, v65\nv_max3_u32 v253, v253, v66, v67\nv_max3_u32 v252, v252, v68, v69\nv_max3_u32 v253, v253, v70, v71\nv_max3_u32 v252, v252, v72, v73\n" \
    "v_max3_u32 v253, v253, v74, v75\nv_max3_u32 v252, v252, v76, v77\nv_max3_u32 v253, v253, v78, v79\nv_max3_u32 v252, v252, v80, v81\nv_max3_u32 v253, v253, v82, v83\n" \
    "v_max3_u32 v252, v252, v84, v85\nv_max3_u32 v253, v253, v86, v87\nv_max3_u32 v252, v252, v88, v89\nv_max3_u32 v253, v253, v90, v91\nv_max3_u32 v252, v252, v92, v93\n" \
    "v_max3_u32 v253, v253, v94, v95\nv_max3_u32 v252, v252, v96, v97\nv_max3_u32 v253, v253, v98, v99\nv_max3_u32 v252, v252, v100, v101\nv_max3_u32 v253, v253, v102, v103\n" \
    "v_max3_u32 v252, v252, v104, v105\nv_max3_u32 v253, v253, v106, v107\nv_max3_u32 v252, v252, v108, v109\nv_max3_u32 v253, v253, v110, v111\n" \
    "v_max3_u32 v252, v252, v112, v113\nv_max3_u32 v253, v253, v114, v115\nv_max3_u32 v252, v252, v116, v117\nv_max3_u32 v253, v253, v118, v119\n" \
    "v_max3_u32 v252, v252, v120, v121\nv_max3_u32 v253, v253, v122, v123\nv_max3_u32 v252, v252, v124, v125\nv_max3_u32 v253, v253, v126, v127\n" \
    "v_max3_u32 v252, v252, v128, v129\nv_max3_u32 v253, v253, v130, v131\nv_max3_u32 v252, v252, v132, v133\nv_max3_u32 v253, v253, v134, v135\n" \
    "v_max3_u32 v252, v252, v136, v137\nv_max3_u32 v253, v253, v138, v139\nv_max3_u32 v252, v252, v140, v141\nv_max3_u32 v253, v253, v142, v143\nv_max_u32 v252, v252, v253\n" \
    "v_cmp_eq_u32 vcc, -1, v252\ns_nop 0\ns_cbranch_vccz .Lr16_%=\n.Lr9_%=:\ns_add_u32 s56, s56, s38\ns_xor_b32 s57, s57, 128\ns_xor_b32 s84, s84, 0x4000\n" \
    "s_xor_b32 s92, s92, 0x1000\ns_add_u32 s76, s56, s35\ns_cmp_eq_u32 s48, 0\ns_cbranch_scc1 .Lr36_%=\ns_lshl_b32 s74, s76, 8\ns_add_u32 s74, s74, s57\n" \
    "s_add_u32 s74, s74, s32\ns_add_u32 s74, s74, 1\nv_mov_b32 v253, s74\ns_xor_b32 s69, s48, 512\ns_mov_b64 exec, 1\nbuffer_store_dword v253, off, s[64:67], s69 sc1\n" \
    "s_mov_b64 exec, -1\n.Lr36_%=:\ns_add_u32 s68, s68, 1\ns_and_b32 s69, s68, 63\ns_cbranch_scc1 .Lr8_%=\ns_cmp_gt_u32 s68, 0x400000\ns_cbranch_scc1 .Lr1_%=\n" \
    "buffer_load_dword v253, off, s[64:67], 0 sc1\ns_waitcnt vmcnt(0)\nv_readfirstlane_b32 s69, v253\ns_nop 0\ns_cmp_eq_u32 s69, 1\ns_cbranch_scc0 .Lr8_%=\ns_endpgm\n.Lr41_%=:\n" \
    "s_waitcnt vmcnt(0)\ns_add_u32 s68, s68, 1\ns_and_b32 s69, s68, 63\ns_cbranch_scc1 .Lr11_%=\ns_cmp_gt_u32 s68, 0x400000\ns_cbranch_scc1 .Lr1_%=\n" \
    "buffer_load_dword v253, off, s[64:67], 0 sc1\ns_waitcnt vmcnt(0)\nv_readfirstlane_b32 s69, v253\ns_nop 0\ns_cmp_eq_u32 s69, 1\ns_cbranch_scc0 .Lr11_%=\ns_endpgm\n" \
    ".Lr12_%=:\ns_cmp_lg_u32 s80, 0\ns_cbranch_scc1 .Lr18_%=\ns_sleep 4\nbuffer_load_dwordx4 v[32:35], v1, s[64:67], s83 offen offset:0 sc1\n" \
    "buffer_load_dwordx4 v[36:39], v1, s[64:67], s83 offen offset:1024 sc1\nbuffer_load_dwordx4 v[40:43], v1, s[64:67], s83 offen offset:2048 sc1\n" \
    "buffer_load_dwordx4 v[44:47], v1, s[64:67], s83 offen offset:3072 sc1\nbuffer_load_dwordx4 v[48:51], v1, s[64:67], s75 offen offset:0 sc1\n" \
    "buffer_load_dwordx4 v[52:55], v1, s[64:67], s75 offen offset:1024 sc1\nbuffer_load_dwordx4 v[56:59], v1, s[64:67], s75 offen offset:2048 sc1\n" \
    "buffer_load_dwordx4 v[60:63], v1, s[64:67], s75 offen offset:3072 sc1\ns_waitcnt vmcnt(0)\nv_permlane16_swap_b32 v32, v33\nv_permlane16_swap_b32 v34, v35\n" \
    "v_permlane16_swap_b32 v36, v37\nv_permlane16_swap_b32 v38, v39\nv_permlane16_swap_b32 v40, v41\nv_permlane16_swap_b32 v42, v43\nv_permlane16_swap_b32 v44, v45\n" \
    "v_permlane16_swap_b32 v46, v47\nv_permlane16_swap_b32 v48, v49\nv_permlane16_swap_b32 v50, v51\nv_permlane16_swap_b32 v52, v53\nv_permlane16_swap_b32 v54, v55\n" \
    "v_permlane16_swap_b32 v56, v57\nv_permlane16_swap_b32 v58, v59\nv_permlane16_swap_b32 v60, v61\nv_permlane16_swap_b32 v62, v63\nv_max3_u32 v252, v32, v33, v34\n" \
    "v_max3_u32 v253, v35, v36, v37\nv_max3_u32 v252, v252, v38, v39\nv_max3_u32 v253, v253, v40, v41\nv_max3_u32 v252, v252, v42, v43\nv_max3_u32 v253, v253, v44, v45\n" \
    "v_max3_u32 v252, v252, v46, v47\nv_max3_u32 v253, v253, v48, v49\nv_max3_u32 v252, v252, v50, v51\nv_max3_u32 v253, v253, v52, v53\nv_max3_u32 v252, v252, v54, v55\n" \
    "v_max3_u32 v253, v253, v56, v57\nv_max3_u32 v252, v252, v58, v59\nv_max3_u32 v253, v253, v60, v61\nv_max3_u32 v252, v252, v62, v63\ns_mov_b32 s76, s94\n.Lr19_%=:\n" \
    "s_add_u32 s84, s76, 0x0\ns_add_u32 s85, s76, 0x4000\ns_add_u32 s86, s76, 0x8000\ns_add_u32 s87, s76, 0xc000\n" \
    "buffer_load_dwordx4 v[64:67], v2, s[64:67], s84 offen offset:0 sc1\nbuffer_load_dwordx4 v[68:71], v1, s[64:67], s84 offen offset:0 sc1\n" \
    "buffer_load_dwordx4 v[72:75], v1, s[64:67], s84 offen offset:1024 sc1\nbuffer_load_dwordx4 v[76:79], v1, s[64:67], s84 offen offset:2048 sc1\n" \
    "buffer_load_dwordx4 v[80:83], v1, s[64:67], s84 offen offset:3072 sc1\nbuffer_load_dwordx4 v[84:87], v2, s[64:67], s85 offen offset:0 sc1\n" \
    "buffer_load_dwordx4 v[88:91], v1, s[64:67], s85 offen offset:0 sc1\nbuffer_load_dwordx4 v[92:95], v1, s[64:67], s85 offen offset:1024 sc1\n" \
    "buffer_load_dwordx4 v[96:99], v1, s[64:67], s85 offen offset:2048 sc1\nbuffer_load_dwordx4 v[100:103], v1, s[64:67], s85 offen offset:3072 sc1\n" \
    "buffer_load_dwordx4 v[104:107], v2, s[64:67], s86 offen offset:0 sc1\nbuffer_load_dwordx4 v[108:111], v1, s[64:67], s86 offen offset:0 sc1\n" \
    "buffer_load_dwordx4 v[112:115], v1, s[64:67], s86 offen offset:1024 sc1\nbuffer_load_dwordx4 v[116:119], v1, s[64:67], s86 offen offset:2048 sc1\n" \
    "buffer_load_dwordx4 v[120:123], v1, s[64:67], s86 offen offset:3072 sc1\nbuffer_load_dwordx4 v[124:127], v2, s[64:67], s87 offen offset:0 sc1\n" \
    "buffer_load_dwordx4 v[128:131], v1, s[64:67], s87 offen offset:0 sc1\nbuffer_load_dwordx4 v[132:135], v1, s[64:67], s87 offen offset:1024 sc1\n" \
    "buffer_load_dwordx4 v[136:139], v1, s[64:67], s87 offen offset:2048 sc1\nbuffer_load_dwordx4 v[140:143], v1, s[64:67], s87 offen offset:3072 sc1\ns_waitcnt vmcnt(0)\n" \
    "v_max3_u32 v252, v252, v64, v65\nv_max3_u32 v253, v253, v66, v67\nv_max3_u32 v252, v252, v68, v69\nv_max3_u32 v253, v253, v70, v71\nv_max3_u32 v252, v252, v72, v73\n" \
    "v_max3_u32 v253, v253, v74, v75\nv_max3_u32 v252, v252, v76, v77\nv_max3_u32 v253, v253, v78, v79\nv_max3_u32 v252, v252, v80, v81\nv_max3_u32 v253, v253, v82, v83\n" \
    "v_max3_u32 v252, v252, v84, v85\nv_max3_u32 v253, v253, v86, v87\nv_max3_u32 v252, v252, v88, v89\nv_max3_u32 v253, v253, v90, v91\nv_max3_u32 v252, v252, v92, v93\n" \
    "v_max3_u32 v253, v253, v94, v95\nv_max3_u32 v252, v252, v96, v97\nv_max3_u32 v253, v253, v98, v99\nv_max3_u32 v252, v252, v100, v101\nv_max3_u32 v253, v253, v102, v103\n" \
    "v_max3_u32 v252, v252, v104, v105\nv_max3_u32 v253, v253, v106, v107\nv_max3_u32 v252, v252, v108, v109\nv_max3_u32 v253, v253, v110, v111\n" \
    "v_max3_u32 v252, v252, v112, v113\nv_max3_u32 v253, v253, v114, v115\nv_max3_u32 v252, v252, v116, v117\nv_max3_u32 v253, v253, v118, v119\n" \
    "v_max3_u32 v252, v252, v120, v121\nv_max3_u32 v253, v253, v122, v123\nv_max3_u32 v252, v252, v124, v125\nv_max3_u32 v253, v253, v126, v127\n" \
    "v_max3_u32 v252, v252, v128, v129\nv_max3_u32 v253, v253, v130, v131\nv_max3_u32 v252, v252, v132, v133\nv_max3_u32 v253, v253, v134, v135\n" \
    "v_max3_u32 v252, v252, v136, v137\nv_max3_u32 v253, v253, v138, v139\nv_max3_u32 v252, v252, v140, v141\nv_max3_u32 v253, v253, v142, v143\ns_add_u32 s76, s76, 0x10000\n" \
    "s_sub_u32 s69, s76, s94\ns_cmp_lt_u32 s69, 0x40000\ns_cbranch_scc1 .Lr19_%=\nv_max_u32 v252, v252, v253\nv_cmp_eq_u32 vcc, -1, v252\ns_nop 0\ns_cmp_eq_u64 vcc, 0\n" \
    "s_cselect_b32 s80, 1, 0\ns_add_u32 s68, s68, 1\ns_and_b32 s69, s68, 63\ns_cbranch_scc1 .Lr11_%=\ns_cmp_gt_u32 s68, 0x400000\ns_cbranch_scc1 .Lr1_%=\n" \
    "buffer_load_dword v253, off, s[64:67], 0 sc1\ns_waitcnt vmcnt(0)\nv_readfirstlane_b32 s69, v253\ns_nop 0\ns_cmp_eq_u32 s69, 1\ns_cbranch_scc0 .Lr11_%=\ns_endpgm\n" \
    ".Lr43_%=:\nbuffer_load_dwordx4 v[16:19], v12, s[64:67], 0 offen offset:0 sc1\nbuffer_load_dwordx4 v[20:23], v12, s[64:67], 0 offen offset:16 sc1\ns_waitcnt vmcnt(0)\n" \
    "v_max3_u32 v252, v16, v17, v18\nv_max3_u32 v253, v19, v20, v21\nv_max3_u32 v252, v252, v22, v23\nv_max_u32 v252, v252, v253\nv_cmp_eq_u32 vcc, -1, v252\ns_nop 0\n" \
    "s_cbranch_vccz .Lr44_%=\ns_sleep 8\ns_add_u32 s68, s68, 1\ns_and_b32 s69, s68, 63\ns_cbranch_scc1 .Lr43_%=\ns_cmp_gt_u32 s68, 0x400000\ns_cbranch_scc1 .Lr1_%=\n" \
    "buffer_load_dword v253, off, s[64:67], 0 sc1\ns_waitcnt vmcnt(0)\nv_readfirstlane_b32 s69, v253\ns_nop 0\ns_cmp_eq_u32 s69, 1\ns_cbranch_scc0 .Lr43_%=\ns_endpgm\n" \
    ".Lr49_%=:\ns_sleep 2\ns_add_u32 s68, s68, 1\ns_and_b32 s69, s68, 63\ns_cbranch_scc1 .Lr48_%=\ns_cmp_gt_u32 s68, 0x400000\ns_cbranch_scc1 .Lr1_%=\n" \
    "buffer_load_dword v253, off, s[64:67], 0 sc1\ns_waitcnt vmcnt(0)\nv_readfirstlane_b32 s69, v253\ns_nop 0\ns_cmp_eq_u32 s69, 1\ns_cbranch_scc0 .Lr48_%=\ns_endpgm\n" \
    ".Lr53_%=:\ns_sleep 2\ns_add_u32 s68, s68, 1\ns_and_b32 s69, s68, 63\ns_cbranch_scc1 .Lr52_%=\ns_cmp_gt_u32 s68, 0x400000\ns_cbranch_scc1 .Lr1_%=\n" \
    "buffer_load_dword v253, off, s[64:67], 0 sc1\ns_waitcnt vmcnt(0)\nv_readfirstlane_b32 s69, v253\ns_nop 0\ns_cmp_eq_u32 s69, 1\ns_cbranch_scc0 .Lr52_%=\ns_endpgm\n.Lr1_%=:\n" \
    "v_mov_b32 v253, 1\nbuffer_store_dword v253, off, s[64:67], 0 sc1\ns_endpgm\n"
#define ASM_F1 \
    "v_mov_b32 v128, 0x3f803f80\nv_mov_b32 v129, 0x3f803f80\nv_mov_b32 v130, 0x3f803f80\nv_mov_b32 v131, 0x3f803f80\nds_read_b128 v[64:67], v12 offset:0\n" \
    "ds_read_b128 v[68:71], v12 offset:16\nds_read_b128 v[72:75], v13 offset:0\nds_read_b128 v[80:83], v12 offset:64\nds_read_b128 v[84:87], v12 offset:80\n" \
    "ds_read_b128 v[88:91], v13 offset:32\nds_read_b128 v[96:99], v12 offset:128\nds_read_b128 v[100:103], v12 offset:144\nds_read_b128 v[104:107], v13 offset:64\n" \
    "v_accvgpr_read_b32 v112, a0\nv_accvgpr_read_b32 v113, a1\nv_accvgpr_read_b32 v114, a2\nv_accvgpr_read_b32 v115, a3\nv_lshlrev_b32 v116, 16, v112\n" \
    "v_and_b32 v117, 0xffff0000, v112\nv_lshlrev_b32 v118, 16, v113\nv_and_b32 v119, 0xffff0000, v113\nv_lshlrev_b32 v120, 16, v114\nv_and_b32 v121, 0xffff0000, v114\n" \
    "v_lshlrev_b32 v122, 16, v115\nv_and_b32 v123, 0xffff0000, v115\ns_waitcnt lgkmcnt(6)\nv_mul_f32 v116, v116, v64\nv_mul_f32 v117, v117, v65\nv_mul_f32 v118, v118, v66\n" \
    "v_mul_f32 v119, v119, v67\nv_mul_f32 v120, v120, v68\nv_mul_f32 v121, v121, v69\nv_mul_f32 v122, v122, v70\nv_mul_f32 v123, v123, v71\n" \
    "v_mfma_f32_32x32x16_bf16 v[32:47], a[0:3], v[72:75], v[32:47]\nv_cvt_pk_bf16_f32 v132, v116, v117\nv_cvt_pk_bf16_f32 v133, v118, v119\nv_cvt_pk_bf16_f32 v134, v120, v121\n" \
    "v_cvt_pk_bf16_f32 v135, v122, v123\nv_accvgpr_write_b32 a0, v132\nv_accvgpr_write_b32 a1, v133\nv_accvgpr_write_b32 a2, v134\nv_accvgpr_write_b32 a3, v135\n" \
    "v_mfma_f32_32x32x16_bf16 v[16:31], v[132:135], v[128:131], 0\nds_read_b128 v[64:67], v12 offset:192\nds_read_b128 v[68:71], v12 offset:208\n" \
    "ds_read_b128 v[72:75], v13 offset:96\nv_accvgpr_read_b32 v112, a4\nv_accvgpr_read_b32 v113, a5\nv_accvgpr_read_b32 v114, a6\nv_accvgpr_read_b32 v115, a7\n" \
    "v_lshlrev_b32 v116, 16, v112\nv_and_b32 v117, 0xffff0000, v112\nv_lshlrev_b32 v118, 16, v113\nv_and_b32 v119, 0xffff0000, v113\nv_lshlrev_b32 v120, 16, v114\n" \
    "v_and_b32 v121, 0xffff0000, v114\nv_lshlrev_b32 v122, 16, v115\nv_and_b32 v123, 0xffff0000, v115\ns_waitcnt lgkmcnt(6)\nv_mul_f32 v116, v116, v80\n" \
    "v_mul_f32 v117, v117, v81\nv_mul_f32 v118, v118, v82\nv_mul_f32 v119, v119, v83\nv_mul_f32 v120, v120, v84\nv_mul_f32 v121, v121, v85\nv_mul_f32 v122, v122, v86\n" \
    "v_mul_f32 v123, v123, v87\nv_mfma_f32_32x32x16_bf16 v[32:47], a[4:7], v[88:91], v[32:47]\nv_cvt_pk_bf16_f32 v136, v116, v117\nv_cvt_pk_bf16_f32 v137, v118, v119\n" \
    "v_cvt_pk_bf16_f32 v138, v120, v121\nv_cvt_pk_bf16_f32 v139, v122, v123\nv_accvgpr_write_b32 a4, v136\nv_accvgpr_write_b32 a5, v137\nv_accvgpr_write_b32 a6, v138\n" \
    "v_accvgpr_write_b32 a7, v139\nv_mfma_f32_32x32x16_bf16 v[16:31], v[136:139], v[128:131], v[16:31]\nds_read_b128 v[80:83], v12 offset:256\n" \
    "ds_read_b128 v[84:87], v12 offset:272\nds_read_b128 v[88:91], v13 offset:128\nv_accvgpr_read_b32 v112, a8\nv_accvgpr_read_b32 v113, a9\nv_accvgpr_read_b32 v114, a10\n" \
    "v_accvgpr_read_b32 v115, a11\nv_lshlrev_b32 v116, 16, v112\nv_and_b32 v117, 0xffff0000, v112\nv_lshlrev_b32 v118, 16, v113\nv_and_b32 v119, 0xffff0000, v113\n" \
    "v_lshlrev_b32 v120, 16, v114\nv_and_b32 v121, 0xffff0000, v114\nv_lshlrev_b32 v122, 16, v115\nv_and_b32 v123, 0xffff0000, v115\ns_waitcnt lgkmcnt(6)\n" \
    "v_mul_f32 v116, v116, v96\nv_mul_f32 v117, v117, v97\nv_mul_f32 v118, v118, v98\nv_mul_f32 v119, v119, v99\nv_mul_f32 v120, v120, v100\nv_mul_f32 v121, v121, v101\n" \
    "v_mul_f32 v122, v122, v102\nv_mul_f32 v123, v123, v103\nv_mfma_f32_32x32x16_bf16 v[32:47], a[8:11], v[104:107], v[32:47]\nv_cvt_pk_bf16_f32 v132, v116, v117\n" \
    "v_cvt_pk_bf16_f32 v133, v118, v119\nv_cvt_pk_bf16_f32 v134, v120, v121\nv_cvt_pk_bf16_f32 v135, v122, v123\nv_accvgpr_write_b32 a8, v132\nv_accvgpr_write_b32 a9, v133\n" \
    "v_accvgpr_write_b32 a10, v134\nv_accvgpr_write_b32 a11, v135\nv_mfma_f32_32x32x16_bf16 v[16:31], v[132:135], v[128:131], v[16:31]\nds_read_b128 v[96:99], v12 offset:320\n" \
    "ds_read_b128 v[100:103], v12 offset:336\nds_read_b128 v[104:107], v13 offset:160\nv_accvgpr_read_b32 v112, a12\nv_accvgpr_read_b32 v113, a13\nv_accvgpr_read_b32 v114, a14\n" \
    "v_accvgpr_read_b32 v115, a15\nv_lshlrev_b32 v116, 16, v112\nv_and_b32 v117, 0xffff0000, v112\nv_lshlrev_b32 v118, 16, v113\nv_and_b32 v119, 0xffff0000, v113\n" \
    "v_lshlrev_b32 v120, 16, v114\nv_and_b32 v121, 0xffff0000, v114\nv_lshlrev_b32 v122, 16, v115\nv_and_b32 v123, 0xffff0000, v115\ns_waitcnt lgkmcnt(6)\n" \
    "v_mul_f32 v116, v116, v64\nv_mul_f32 v117, v117, v65\nv_mul_f32 v118, v118, v66\nv_mul_f32 v119, v119, v67\nv_mul_f32 v120, v120, v68\nv_mul_f32 v121, v121, v69\n" \
    "v_mul_f32 v122, v122, v70\nv_mul_f32 v123, v123, v71\nv_mfma_f32_32x32x16_bf16 v[32:47], a[12:15], v[72:75], v[32:47]\nv_cvt_pk_bf16_f32 v136, v116, v117\n" \
    "v_cvt_pk_bf16_f32 v137, v118, v119\nv_cvt_pk_bf16_f32 v138, v120, v121\nv_cvt_pk_bf16_f32 v139, v122, v123\nv_accvgpr_write_b32 a12, v136\nv_accvgpr_write_b32 a13, v137\n" \
    "v_accvgpr_write_b32 a14, v138\nv_accvgpr_write_b32 a15, v139\nv_mfma_f32_32x32x16_bf16 v[16:31], v[136:139], v[128:131], v[16:31]\nds_read_b128 v[64:67], v12 offset:384\n" \
    "ds_read_b128 v[68:71], v12 offset:400\nds_read_b128 v[72:75], v13 offset:192\nv_accvgpr_read_b32 v112, a16\nv_accvgpr_read_b32 v113, a17\nv_accvgpr_read_b32 v114, a18\n" \
    "v_accvgpr_read_b32 v115, a19\nv_lshlrev_b32 v116, 16, v112\nv_and_b32 v117, 0xffff0000, v112\nv_lshlrev_b32 v118, 16, v113\nv_and_b32 v119, 0xffff0000, v113\n" \
    "v_lshlrev_b32 v120, 16, v114\nv_and_b32 v121, 0xffff0000, v114\nv_lshlrev_b32 v122, 16, v115\nv_and_b32 v123, 0xffff0000, v115\ns_waitcnt lgkmcnt(6)\n" \
    "v_mul_f32 v116, v116, v80\nv_mul_f32 v117, v117, v81\nv_mul_f32 v118, v118, v82\nv_mul_f32 v119, v119, v83\nv_mul_f32 v120, v120, v84\nv_mul_f32 v121, v121, v85\n" \
    "v_mul_f32 v122, v122, v86\nv_mul_f32 v123, v123, v87\nv_mfma_f32_32x32x16_bf16 v[32:47], a[16:19], v[88:91], v[32:47]\nv_cvt_pk_bf16_f32 v132, v116, v117\n" \
    "v_cvt_pk_bf16_f32 v133, v118, v119\nv_cvt_pk_bf16_f32 v134, v120, v121\nv_cvt_pk_bf16_f32 v135, v122, v123\nv_accvgpr_write_b32 a16, v132\nv_accvgpr_write_b32 a17, v133\n" \
    "v_accvgpr_write_b32 a18, v134\nv_accvgpr_write_b32 a19, v135\nv_mfma_f32_32x32x16_bf16 v[16:31], v[132:135], v[128:131], v[16:31]\nds_read_b128 v[80:83], v12 offset:448\n" \
    "ds_read_b128 v[84:87], v12 offset:464\nds_read_b128 v[88:91], v13 offset:224\nv_accvgpr_read_b32 v112, a20\nv_accvgpr_read_b32 v113, a21\nv_accvgpr_read_b32 v114, a22\n" \
    "v_accvgpr_read_b32 v115, a23\nv_lshlrev_b32 v116, 16, v112\nv_and_b32 v117, 0xffff0000, v112\nv_lshlrev_b32 v118, 16, v113\nv_and_b32 v119, 0xffff0000, v113\n" \
    "v_lshlrev_b32 v120, 16, v114\nv_and_b32 v121, 0xffff0000, v114\nv_lshlrev_b32 v122, 16, v115\nv_and_b32 v123, 0xffff0000, v115\ns_waitcnt lgkmcnt(6)\n" \
    "v_mul_f32 v116, v116, v96\nv_mul_f32 v117, v117, v97\nv_mul_f32 v118, v118, v98\nv_mul_f32 v119, v119, v99\nv_mul_f32 v120, v120, v100\nv_mul_f32 v121, v121, v101\n" \
    "v_mul_f32 v122, v122, v102\nv_mul_f32 v123, v123, v103\nv_mfma_f32_32x32x16_bf16 v[32:47], a[20:23], v[104:107], v[32:47]\nv_cvt_pk_bf16_f32 v136, v116, v117\n" \
    "v_cvt_pk_bf16_f32 v137, v118, v119\nv_cvt_pk_bf16_f32 v138, v120, v121\nv_cvt_pk_bf16_f32 v139, v122, v123\nv_accvgpr_write_b32 a20, v136\nv_accvgpr_write_b32 a21, v137\n" \
    "v_accvgpr_write_b32 a22, v138\nv_accvgpr_write_b32 a23, v139\nv_mfma_f32_32x32x16_bf16 v[16:31], v[136:139], v[128:131], v[16:31]\nds_read_b128 v[96:99], v12 offset:512\n" \
    "ds_read_b128 v[100:103], v12 offset:528\nds_read_b128 v[104:107], v13 offset:256\nv_accvgpr_read_b32 v112, a24\nv_accvgpr_read_b32 v113, a25\nv_accvgpr_read_b32 v114, a26\n" \
    "v_accvgpr_read_b32 v115, a27\nv_lshlrev_b32 v116, 16, v112\nv_and_b32 v117, 0xffff0000, v112\nv_lshlrev_b32 v118, 16, v113\nv_and_b32 v119, 0xffff0000, v113\n" \
    "v_lshlrev_b32 v120, 16, v114\nv_and_b32 v121, 0xffff0000, v114\nv_lshlrev_b32 v122, 16, v115\nv_and_b32 v123, 0xffff0000, v115\ns_waitcnt lgkmcnt(6)\n" \
    "v_mul_f32 v116, v116, v64\nv_mul_f32 v117, v117, v65\nv_mul_f32 v118, v118, v66\nv_mul_f32 v119, v119, v67\nv_mul_f32 v120, v120, v68\nv_mul_f32 v121, v121, v69\n" \
    "v_mul_f32 v122, v122, v70\nv_mul_f32 v123, v123, v71\nv_mfma_f32_32x32x16_bf16 v[32:47], a[24:27], v[72:75], v[32:47]\nv_cvt_pk_bf16_f32 v132, v116, v117\n" \
    "v_cvt_pk_bf16_f32 v133, v118, v119\nv_cvt_pk_bf16_f32 v134, v120, v121\nv_cvt_pk_bf16_f32 v135, v122, v123\nv_accvgpr_write_b32 a24, v132\nv_accvgpr_write_b32 a25, v133\n" \
    "v_accvgpr_write_b32 a26, v134\nv_accvgpr_write_b32 a27, v135\nv_mfma_f32_32x32x16_bf16 v[16:31], v[132:135], v[128:131], v[16:31]\nds_read_b128 v[64:67], v12 offset:576\n" \
    "ds_read_b128 v[68:71], v12 offset:592\nds_read_b128 v[72:75], v13 offset:288\nv_accvgpr_read_b32 v112, a28\nv_accvgpr_read_b32 v113, a29\nv_accvgpr_read_b32 v114, a30\n" \
    "v_accvgpr_read_b32 v115, a31\nv_lshlrev_b32 v116, 16, v112\nv_and_b32 v117, 0xffff0000, v112\nv_lshlrev_b32 v118, 16, v113\nv_and_b32 v119, 0xffff0000, v113\n" \
    "v_lshlrev_b32 v120, 16, v114\nv_and_b32 v121, 0xffff0000, v114\nv_lshlrev_b32 v122, 16, v115\nv_and_b32 v123, 0xffff0000, v115\ns_waitcnt lgkmcnt(6)\n" \
    "v_mul_f32 v116, v116, v80\nv_mul_f32 v117, v117, v81\nv_mul_f32 v118, v118, v82\nv_mul_f32 v119, v119, v83\nv_mul_f32 v120, v120, v84\nv_mul_f32 v121, v121, v85\n" \
    "v_mul_f32 v122, v122, v86\nv_mul_f32 v123, v123, v87\nv_mfma_f32_32x32x16_bf16 v[32:47], a[28:31], v[88:91], v[32:47]\nv_cvt_pk_bf16_f32 v136, v116, v117\n" \
    "v_cvt_pk_bf16_f32 v137, v118, v119\nv_cvt_pk_bf16_f32 v138, v120, v121\nv_cvt_pk_bf16_f32 v139, v122, v123\nv_accvgpr_write_b32 a28, v136\nv_accvgpr_write_b32 a29, v137\n" \
    "v_accvgpr_write_b32 a30, v138\nv_accvgpr_write_b32 a31, v139\nv_mfma_f32_32x32x16_bf16 v[16:31], v[136:139], v[128:131], v[16:31]\nds_read_b128 v[80:83], v12 offset:640\n" \
    "ds_read_b128 v[84:87], v12 offset:656\nds_read_b128 v[88:91], v13 offset:320\nv_accvgpr_read_b32 v112, a32\nv_accvgpr_read_b32 v113, a33\nv_accvgpr_read_b32 v114, a34\n" \
    "v_accvgpr_read_b32 v115, a35\nv_lshlrev_b32 v116, 16, v112\nv_and_b32 v117, 0xffff0000, v112\nv_lshlrev_b32 v118, 16, v113\nv_and_b32 v119, 0xffff0000, v113\n" \
    "v_lshlrev_b32 v120, 16, v114\nv_and_b32 v121, 0xffff0000, v114\nv_lshlrev_b32 v122, 16, v115\nv_and_b32 v123, 0xffff0000, v115\ns_waitcnt lgkmcnt(6)\n" \
    "v_mul_f32 v116, v116, v96\nv_mul_f32 v117, v117, v97\nv_mul_f32 v118, v118, v98\nv_mul_f32 v119, v119, v99\nv_mul_f32 v120, v120, v100\nv_mul_f32 v121, v121, v101\n" \
    "v_mul_f32 v122, v122, v102\nv_mul_f32 v123, v123, v103\nv_mfma_f32_32x32x16_bf16 v[32:47], a[32:35], v[104:107], v[32:47]\nv_cvt_pk_bf16_f32 v132, v116, v117\n" \
    "v_cvt_pk_bf16_f32 v133, v118, v119\nv_cvt_pk_bf16_f32 v134, v120, v121\nv_cvt_pk_bf16_f32 v135, v122, v123\nv_accvgpr_write_b32 a32, v132\nv_accvgpr_write_b32 a33, v133\n" \
    "v_accvgpr_write_b32 a34, v134\nv_accvgpr_write_b32 a35, v135\nv_mfma_f32_32x32x16_bf16 v[16:31], v[132:135], v[128:131], v[16:31]\nds_read_b128 v[96:99], v12 offset:704\n" \
    "ds_read_b128 v[100:103], v12 offset:720\nds_read_b128 v[104:107], v13 offset:352\nv_accvgpr_read_b32 v112, a36\nv_accvgpr_read_b32 v113, a37\nv_accvgpr_read_b32 v114, a38\n" \
    "v_accvgpr_read_b32 v115, a39\nv_lshlrev_b32 v116, 16, v112\nv_and_b32 v117, 0xffff0000, v112\nv_lshlrev_b32 v118, 16, v113\nv_and_b32 v119, 0xffff0000, v113\n" \
    "v_lshlrev_b32 v120, 16, v114\nv_and_b32 v121, 0xffff0000, v114\nv_lshlrev_b32 v122, 16, v115\nv_and_b32 v123, 0xffff0000, v115\ns_waitcnt lgkmcnt(6)\n" \
    "v_mul_f32 v116, v116, v64\nv_mul_f32 v117, v117, v65\nv_mul_f32 v118, v118, v66\nv_mul_f32 v119, v119, v67\nv_mul_f32 v120, v120, v68\nv_mul_f32 v121, v121, v69\n" \
    "v_mul_f32 v122, v122, v70\nv_mul_f32 v123, v123, v71\nv_mfma_f32_32x32x16_bf16 v[32:47], a[36:39], v[72:75], v[32:47]\nv_cvt_pk_bf16_f32 v136, v116, v117\n" \
    "v_cvt_pk_bf16_f32 v137, v118, v119\nv_cvt_pk_bf16_f32 v138, v120, v121\nv_cvt_pk_bf16_f32 v139, v122, v123\nv_accvgpr_write_b32 a36, v136\nv_accvgpr_write_b32 a37, v137\n" \
    "v_accvgpr_write_b32 a38, v138\nv_accvgpr_write_b32 a39, v139\nv_mfma_f32_32x32x16_bf16 v[16:31], v[136:139], v[128:131], v[16:31]\nds_read_b128 v[64:67], v12 offset:768\n" \
    "ds_read_b128 v[68:71], v12 offset:784\nds_read_b128 v[72:75], v13 offset:384\nv_accvgpr_read_b32 v112, a40\nv_accvgpr_read_b32 v113, a41\nv_accvgpr_read_b32 v114, a42\n" \
    "v_accvgpr_read_b32 v115, a43\nv_lshlrev_b32 v116, 16, v112\nv_and_b32 v117, 0xffff0000, v112\nv_lshlrev_b32 v118, 16, v113\nv_and_b32 v119, 0xffff0000, v113\n" \
    "v_lshlrev_b32 v120, 16, v114\nv_and_b32 v121, 0xffff0000, v114\nv_lshlrev_b32 v122, 16, v115\nv_and_b32 v123, 0xffff0000, v115\ns_waitcnt lgkmcnt(6)\n" \
    "v_mul_f32 v116, v116, v80\nv_mul_f32 v117, v117, v81\nv_mul_f32 v118, v118, v82\nv_mul_f32 v119, v119, v83\nv_mul_f32 v120, v120, v84\nv_mul_f32 v121, v121, v85\n" \
    "v_mul_f32 v122, v122, v86\nv_mul_f32 v123, v123, v87\nv_mfma_f32_32x32x16_bf16 v[32:47], a[40:43], v[88:91], v[32:47]\nv_cvt_pk_bf16_f32 v132, v116, v117\n" \
    "v_cvt_pk_bf16_f32 v133, v118, v119\nv_cvt_pk_bf16_f32 v134, v120, v121\nv_cvt_pk_bf16_f32 v135, v122, v123\nv_accvgpr_write_b32 a40, v132\nv_accvgpr_write_b32 a41, v133\n" \
    "v_accvgpr_write_b32 a42, v134\nv_accvgpr_write_b32 a43, v135\nv_mfma_f32_32x32x16_bf16 v[16:31], v[132:135], v[128:131], v[16:31]\nds_read_b128 v[80:83], v12 offset:832\n" \
    "ds_read_b128 v[84:87], v12 offset:848\nds_read_b128 v[88:91], v13 offset:416\nv_accvgpr_read_b32 v112, a44\nv_accvgpr_read_b32 v113, a45\nv_accvgpr_read_b32 v114, a46\n" \
    "v_accvgpr_read_b32 v115, a47\nv_lshlrev_b32 v116, 16, v112\nv_and_b32 v117, 0xffff0000, v112\nv_lshlrev_b32 v118, 16, v113\nv_and_b32 v119, 0xffff0000, v113\n" \
    "v_lshlrev_b32 v120, 16, v114\nv_and_b32 v121, 0xffff0000, v114\nv_lshlrev_b32 v122, 16, v115\nv_and_b32 v123, 0xffff0000, v115\ns_waitcnt lgkmcnt(6)\n" \
    "v_mul_f32 v116, v116, v96\nv_mul_f32 v117, v117, v97\nv_mul_f32 v118, v118, v98\nv_mul_f32 v119, v119, v99\nv_mul_f32 v120, v120, v100\nv_mul_f32 v121, v121, v101\n" \
    "v_mul_f32 v122, v122, v102\nv_mul_f32 v123, v123, v103\nv_mfma_f32_32x32x16_bf16 v[32:47], a[44:47], v[104:107], v[32:47]\nv_cvt_pk_bf16_f32 v136, v116, v117\n" \
    "v_cvt_pk_bf16_f32 v137, v118, v119\nv_cvt_pk_bf16_f32 v138, v120, v121\nv_cvt_pk_bf16_f32 v139, v122, v123\nv_accvgpr_write_b32 a44, v136\nv_accvgpr_write_b32 a45, v137\n" \
    "v_accvgpr_write_b32 a46, v138\nv_accvgpr_write_b32 a47, v139\nv_mfma_f32_32x32x16_bf16 v[16:31], v[136:139], v[128:131], v[16:31]\nds_read_b128 v[96:99], v12 offset:896\n" \
    "ds_read_b128 v[100:103], v12 offset:912\nds_read_b128 v[104:107], v13 offset:448\nv_accvgpr_read_b32 v112, a48\nv_accvgpr_read_b32 v113, a49\nv_accvgpr_read_b32 v114, a50\n" \
    "v_accvgpr_read_b32 v115, a51\nv_lshlrev_b32 v116, 16, v112\nv_and_b32 v117, 0xffff0000, v112\nv_lshlrev_b32 v118, 16, v113\nv_and_b32 v119, 0xffff0000, v113\n" \
    "v_lshlrev_b32 v120, 16, v114\nv_and_b32 v121, 0xffff0000, v114\nv_lshlrev_b32 v122, 16, v115\nv_and_b32 v123, 0xffff0000, v115\ns_waitcnt lgkmcnt(6)\n" \
    "v_mul_f32 v116, v116, v64\nv_mul_f32 v117, v117, v65\nv_mul_f32 v118, v118, v66\nv_mul_f32 v119, v119, v67\nv_mul_f32 v120, v120, v68\nv_mul_f32 v121, v121, v69\n" \
    "v_mul_f32 v122, v122, v70\nv_mul_f32 v123, v123, v71\nv_mfma_f32_32x32x16_bf16 v[32:47], a[48:51], v[72:75], v[32:47]\nv_cvt_pk_bf16_f32 v132, v116, v117\n" \
    "v_cvt_pk_bf16_f32 v133, v118, v119\nv_cvt_pk_bf16_f32 v134, v120, v121\nv_cvt_pk_bf16_f32 v135, v122, v123\nv_accvgpr_write_b32 a48, v132\nv_accvgpr_write_b32 a49, v133\n" \
    "v_accvgpr_write_b32 a50, v134\nv_accvgpr_write_b32 a51, v135\nv_mfma_f32_32x32x16_bf16 v[16:31], v[132:135], v[128:131], v[16:31]\nds_read_b128 v[64:67], v12 offset:960\n" \
    "ds_read_b128 v[68:71], v12 offset:976\nds_read_b128 v[72:75], v13 offset:480\nv_accvgpr_read_b32 v112, a52\nv_accvgpr_read_b32 v113, a53\nv_accvgpr_read_b32 v114, a54\n" \
    "v_accvgpr_read_b32 v115, a55\nv_lshlrev_b32 v116, 16, v112\nv_and_b32 v117, 0xffff0000, v112\nv_lshlrev_b32 v118, 16, v113\nv_and_b32 v119, 0xffff0000, v113\n" \
    "v_lshlrev_b32 v120, 16, v114\nv_and_b32 v121, 0xffff0000, v114\nv_lshlrev_b32 v122, 16, v115\nv_and_b32 v123, 0xffff0000, v115\ns_waitcnt lgkmcnt(6)\n" \
    "v_mul_f32 v116, v116, v80\nv_mul_f32 v117, v117, v81\nv_mul_f32 v118, v118, v82\nv_mul_f32 v119, v119, v83\nv_mul_f32 v120, v120, v84\nv_mul_f32 v121, v121, v85\n" \
    "v_mul_f32 v122, v122, v86\nv_mul_f32 v123, v123, v87\nv_mfma_f32_32x32x16_bf16 v[32:47], a[52:55], v[88:91], v[32:47]\nv_cvt_pk_bf16_f32 v136, v116, v117\n" \
    "v_cvt_pk_bf16_f32 v137, v118, v119\nv_cvt_pk_bf16_f32 v138, v120, v121\nv_cvt_pk_bf16_f32 v139, v122, v123\nv_accvgpr_write_b32 a52, v136\nv_accvgpr_write_b32 a53, v137\n" \
    "v_accvgpr_write_b32 a54, v138\nv_accvgpr_write_b32 a55, v139\nv_mfma_f32_32x32x16_bf16 v[16:31], v[136:139], v[128:131], v[16:31]\nds_read_b128 v[80:83], v12 offset:1024\n" \
    "ds_read_b128 v[84:87], v12 offset:1040\nds_read_b128 v[88:91], v13 offset:512\nv_accvgpr_read_b32 v112, a56\nv_accvgpr_read_b32 v113, a57\nv_accvgpr_read_b32 v114, a58\n" \
    "v_accvgpr_read_b32 v115, a59\nv_lshlrev_b32 v116, 16, v112\nv_and_b32 v117, 0xffff0000, v112\nv_lshlrev_b32 v118, 16, v113\nv_and_b32 v119, 0xffff0000, v113\n" \
    "v_lshlrev_b32 v120, 16, v114\nv_and_b32 v121, 0xffff0000, v114\nv_lshlrev_b32 v122, 16, v115\nv_and_b32 v123, 0xffff0000, v115\ns_waitcnt lgkmcnt(6)\n" \
    "v_mul_f32 v116, v116, v96\nv_mul_f32 v117, v117, v97\nv_mul_f32 v118, v118, v98\nv_mul_f32 v119, v119, v99\nv_mul_f32 v120, v120, v100\nv_mul_f32 v121, v121, v101\n" \
    "v_mul_f32 v122, v122, v102\nv_mul_f32 v123, v123, v103\nv_mfma_f32_32x32x16_bf16 v[32:47], a[56:59], v[104:107], v[32:47]\nv_cvt_pk_bf16_f32 v132, v116, v117\n" \
    "v_cvt_pk_bf16_f32 v133, v118, v119\nv_cvt_pk_bf16_f32 v134, v120, v121\nv_cvt_pk_bf16_f32 v135, v122, v123\nv_accvgpr_write_b32 a56, v132\nv_accvgpr_write_b32 a57, v133\n" \
    "v_accvgpr_write_b32 a58, v134\nv_accvgpr_write_b32 a59, v135\nv_mfma_f32_32x32x16_bf16 v[16:31], v[132:135], v[128:131], v[16:31]\nds_read_b128 v[96:99], v12 offset:1088\n" \
    "ds_read_b128 v[100:103], v12 offset:1104\nds_read_b128 v[104:107], v13 offset:544\nv_accvgpr_read_b32 v112, a60\nv_accvgpr_read_b32 v113, a61\n" \
    "v_accvgpr_read_b32 v114, a62\nv_accvgpr_read_b32 v115, a63\nv_lshlrev_b32 v116, 16, v112\nv_and_b32 v117, 0xffff0000, v112\nv_lshlrev_b32 v118, 16, v113\n" \
    "v_and_b32 v119, 0xffff0000, v113\nv_lshlrev_b32 v120, 16, v114\nv_and_b32 v121, 0xffff0000, v114\nv_lshlrev_b32 v122, 16, v115\nv_and_b32 v123, 0xffff0000, v115\n" \
    "s_waitcnt lgkmcnt(6)\nv_mul_f32 v116, v116, v64\nv_mul_f32 v117, v117, v65\nv_mul_f32 v118, v118, v66\nv_mul_f32 v119, v119, v67\nv_mul_f32 v120, v120, v68\n" \
    "v_mul_f32 v121, v121, v69\nv_mul_f32 v122, v122, v70\nv_mul_f32 v123, v123, v71\nv_mfma_f32_32x32x16_bf16 v[32:47], a[60:63], v[72:75], v[32:47]\n" \
    "v_cvt_pk_bf16_f32 v136, v116, v117\nv_cvt_pk_bf16_f32 v137, v118, v119\nv_cvt_pk_bf16_f32 v138, v120, v121\nv_cvt_pk_bf16_f32 v139, v122, v123\n" \
    "v_accvgpr_write_b32 a60, v136\nv_accvgpr_write_b32 a61, v137\nv_accvgpr_write_b32 a62, v138\nv_accvgpr_write_b32 a63, v139\n" \
    "v_mfma_f32_32x32x16_bf16 v[16:31], v[136:139], v[128:131], v[16:31]\nds_read_b128 v[64:67], v12 offset:1152\nds_read_b128 v[68:71], v12 offset:1168\n" \
    "ds_read_b128 v[72:75], v13 offset:576\nv_accvgpr_read_b32 v112, a64\nv_accvgpr_read_b32 v113, a65\nv_accvgpr_read_b32 v114, a66\nv_accvgpr_read_b32 v115, a67\n" \
    "v_lshlrev_b32 v116, 16, v112\nv_and_b32 v117, 0xffff0000, v112\nv_lshlrev_b32 v118, 16, v113\nv_and_b32 v119, 0xffff0000, v113\nv_lshlrev_b32 v120, 16, v114\n" \
    "v_and_b32 v121, 0xffff0000, v114\nv_lshlrev_b32 v122, 16, v115\nv_and_b32 v123, 0xffff0000, v115\ns_waitcnt lgkmcnt(6)\nv_mul_f32 v116, v116, v80\n" \
    "v_mul_f32 v117, v117, v81\nv_mul_f32 v118, v118, v82\nv_mul_f32 v119, v119, v83\nv_mul_f32 v120, v120, v84\nv_mul_f32 v121, v121, v85\nv_mul_f32 v122, v122, v86\n" \
    "v_mul_f32 v123, v123, v87\nv_mfma_f32_32x32x16_bf16 v[32:47], a[64:67], v[88:91], v[32:47]\nv_cvt_pk_bf16_f32 v132, v116, v117\nv_cvt_pk_bf16_f32 v133, v118, v119\n" \
    "v_cvt_pk_bf16_f32 v134, v120, v121\nv_cvt_pk_bf16_f32 v135, v122, v123\nv_accvgpr_write_b32 a64, v132\nv_accvgpr_write_b32 a65, v133\nv_accvgpr_write_b32 a66, v134\n" \
    "v_accvgpr_write_b32 a67, v135\nv_mfma_f32_32x32x16_bf16 v[16:31], v[132:135], v[128:131], v[16:31]\nds_read_b128 v[80:83], v12 offset:1216\n" \
    "ds_read_b128 v[84:87], v12 offset:1232\nds_read_b128 v[88:91], v13 offset:608\nv_accvgpr_read_b32 v112, a68\nv_accvgpr_read_b32 v113, a69\nv_accvgpr_read_b32 v114, a70\n" \
    "v_accvgpr_read_b32 v115, a71\nv_lshlrev_b32 v116, 16, v112\nv_and_b32 v117, 0xffff0000, v112\nv_lshlrev_b32 v118, 16, v113\nv_and_b32 v119, 0xffff0000, v113\n" \
    "v_lshlrev_b32 v120, 16, v114\nv_and_b32 v121, 0xffff0000, v114\nv_lshlrev_b32 v122, 16, v115\nv_and_b32 v123, 0xffff0000, v115\ns_waitcnt lgkmcnt(6)\n" \
    "v_mul_f32 v116, v116, v96\nv_mul_f32 v117, v117, v97\nv_mul_f32 v118, v118, v98\nv_mul_f32 v119, v119, v99\nv_mul_f32 v120, v120, v100\nv_mul_f32 v121, v121, v101\n" \
    "v_mul_f32 v122, v122, v102\nv_mul_f32 v123, v123, v103\nv_mfma_f32_32x32x16_bf16 v[32:47], a[68:71], v[104:107], v[32:47]\nv_cvt_pk_bf16_f32 v136, v116, v117\n" \
    "v_cvt_pk_bf16_f32 v137, v118, v119\nv_cvt_pk_bf16_f32 v138, v120, v121\nv_cvt_pk_bf16_f32 v139, v122, v123\nv_accvgpr_write_b32 a68, v136\nv_accvgpr_write_b32 a69, v137\n" \
    "v_accvgpr_write_b32 a70, v138\nv_accvgpr_write_b32 a71, v139\nv_mfma_f32_32x32x16_bf16 v[16:31], v[136:139], v[128:131], v[16:31]\nds_read_b128 v[96:99], v12 offset:1280\n" \
    "ds_read_b128 v[100:103], v12 offset:1296\nds_read_b128 v[104:107], v13 offset:640\nv_accvgpr_read_b32 v112, a72\nv_accvgpr_read_b32 v113, a73\n" \
    "v_accvgpr_read_b32 v114, a74\nv_accvgpr_read_b32 v115, a75\nv_lshlrev_b32 v116, 16, v112\nv_and_b32 v117, 0xffff0000, v112\nv_lshlrev_b32 v118, 16, v113\n" \
    "v_and_b32 v119, 0xffff0000, v113\nv_lshlrev_b32 v120, 16, v114\nv_and_b32 v121, 0xffff0000, v114\nv_lshlrev_b32 v122, 16, v115\nv_and_b32 v123, 0xffff0000, v115\n" \
    "s_waitcnt lgkmcnt(6)\nv_mul_f32 v116, v116, v64\nv_mul_f32 v117, v117, v65\nv_mul_f32 v118, v118, v66\nv_mul_f32 v119, v119, v67\nv_mul_f32 v120, v120, v68\n" \
    "v_mul_f32 v121, v121, v69\nv_mul_f32 v122, v122, v70\nv_mul_f32 v123, v123, v71\nv_mfma_f32_32x32x16_bf16 v[32:47], a[72:75], v[72:75], v[32:47]\n" \
    "v_cvt_pk_bf16_f32 v132, v116, v117\nv_cvt_pk_bf16_f32 v133, v118, v119\nv_cvt_pk_bf16_f32 v134, v120, v121\nv_cvt_pk_bf16_f32 v135, v122, v123\n" \
    "v_accvgpr_write_b32 a72, v132\nv_accvgpr_write_b32 a73, v133\nv_accvgpr_write_b32 a74, v134\nv_accvgpr_write_b32 a75, v135\n" \
    "v_mfma_f32_32x32x16_bf16 v[16:31], v[132:135], v[128:131], v[16:31]\nds_read_b128 v[64:67], v12 offset:1344\nds_read_b128 v[68:71], v12 offset:1360\n" \
    "ds_read_b128 v[72:75], v13 offset:672\nv_accvgpr_read_b32 v112, a76\nv_accvgpr_read_b32 v113, a77\nv_accvgpr_read_b32 v114, a78\nv_accvgpr_read_b32 v115, a79\n" \
    "v_lshlrev_b32 v116, 16, v112\nv_and_b32 v117, 0xffff0000, v112\nv_lshlrev_b32 v118, 16, v113\nv_and_b32 v119, 0xffff0000, v113\nv_lshlrev_b32 v120, 16, v114\n" \
    "v_and_b32 v121, 0xffff0000, v114\nv_lshlrev_b32 v122, 16, v115\nv_and_b32 v123, 0xffff0000, v115\ns_waitcnt lgkmcnt(6)\nv_mul_f32 v116, v116, v80\n" \
    "v_mul_f32 v117, v117, v81\nv_mul_f32 v118, v118, v82\nv_mul_f32 v119, v119, v83\nv_mul_f32 v120, v120, v84\nv_mul_f32 v121, v121, v85\nv_mul_f32 v122, v122, v86\n" \
    "v_mul_f32 v123, v123, v87\nv_mfma_f32_32x32x16_bf16 v[32:47], a[76:79], v[88:91], v[32:47]\nv_cvt_pk_bf16_f32 v136, v116, v117\nv_cvt_pk_bf16_f32 v137, v118, v119\n" \
    "v_cvt_pk_bf16_f32 v138, v120, v121\nv_cvt_pk_bf16_f32 v139, v122, v123\nv_accvgpr_write_b32 a76, v136\nv_accvgpr_write_b32 a77, v137\nv_accvgpr_write_b32 a78, v138\n" \
    "v_accvgpr_write_b32 a79, v139\nv_mfma_f32_32x32x16_bf16 v[16:31], v[136:139], v[128:131], v[16:31]\nds_read_b128 v[80:83], v12 offset:1408\n" \
    "ds_read_b128 v[84:87], v12 offset:1424\nds_read_b128 v[88:91], v13 offset:704\nv_accvgpr_read_b32 v112, a80\nv_accvgpr_read_b32 v113, a81\nv_accvgpr_read_b32 v114, a82\n" \
    "v_accvgpr_read_b32 v115, a83\nv_lshlrev_b32 v116, 16, v112\nv_and_b32 v117, 0xffff0000, v112\nv_lshlrev_b32 v118, 16, v113\nv_and_b32 v119, 0xffff0000, v113\n" \
    "v_lshlrev_b32 v120, 16, v114\nv_and_b32 v121, 0xffff0000, v114\nv_lshlrev_b32 v122, 16, v115\nv_and_b32 v123, 0xffff0000, v115\ns_waitcnt lgkmcnt(6)\n" \
    "v_mul_f32 v116, v116, v96\nv_mul_f32 v117, v117, v97\nv_mul_f32 v118, v118, v98\nv_mul_f32 v119, v119, v99\nv_mul_f32 v120, v120, v100\nv_mul_f32 v121, v121, v101\n" \
    "v_mul_f32 v122, v122, v102\nv_mul_f32 v123, v123, v103\nv_mfma_f32_32x32x16_bf16 v[32:47], a[80:83], v[104:107], v[32:47]\nv_cvt_pk_bf16_f32 v132, v116, v117\n" \
    "v_cvt_pk_bf16_f32 v133, v118, v119\nv_cvt_pk_bf16_f32 v134, v120, v121\nv_cvt_pk_bf16_f32 v135, v122, v123\nv_accvgpr_write_b32 a80, v132\nv_accvgpr_write_b32 a81, v133\n" \
    "v_accvgpr_write_b32 a82, v134\nv_accvgpr_write_b32 a83, v135\nv_mfma_f32_32x32x16_bf16 v[16:31], v[132:135], v[128:131], v[16:31]\nds_read_b128 v[96:99], v12 offset:1472\n" \
    "ds_read_b128 v[100:103], v12 offset:1488\nds_read_b128 v[104:107], v13 offset:736\nv_accvgpr_read_b32 v112, a84\nv_accvgpr_read_b32 v113, a85\n" \
    "v_accvgpr_read_b32 v114, a86\nv_accvgpr_read_b32 v115, a87\nv_lshlrev_b32 v116, 16, v112\nv_and_b32 v117, 0xffff0000, v112\nv_lshlrev_b32 v118, 16, v113\n" \
    "v_and_b32 v119, 0xffff0000, v113\nv_lshlrev_b32 v120, 16, v114\nv_and_b32 v121, 0xffff0000, v114\nv_lshlrev_b32 v122, 16, v115\nv_and_b32 v123, 0xffff0000, v115\n" \
    "s_waitcnt lgkmcnt(6)\nv_mul_f32 v116, v116, v64\nv_mul_f32 v117, v117, v65\nv_mul_f32 v118, v118, v66\nv_mul_f32 v119, v119, v67\nv_mul_f32 v120, v120, v68\n" \
    "v_mul_f32 v121, v121, v69\nv_mul_f32 v122, v122, v70\nv_mul_f32 v123, v123, v71\nv_mfma_f32_32x32x16_bf16 v[32:47], a[84:87], v[72:75], v[32:47]\n" \
    "v_cvt_pk_bf16_f32 v136, v116, v117\nv_cvt_pk_bf16_f32 v137, v118, v119\nv_cvt_pk_bf16_f32 v138, v120, v121\nv_cvt_pk_bf16_f32 v139, v122, v123\n" \
    "v_accvgpr_write_b32 a84, v136\nv_accvgpr_write_b32 a85, v137\nv_accvgpr_write_b32 a86, v138\nv_accvgpr_write_b32 a87, v139\n" \
    "v_mfma_f32_32x32x16_bf16 v[16:31], v[136:139], v[128:131], v[16:31]\nds_read_b128 v[64:67], v12 offset:1536\nds_read_b128 v[68:71], v12 offset:1552\n" \
    "ds_read_b128 v[72:75], v13 offset:768\nv_accvgpr_read_b32 v112, a88\nv_accvgpr_read_b32 v113, a89\nv_accvgpr_read_b32 v114, a90\nv_accvgpr_read_b32 v115, a91\n" \
    "v_lshlrev_b32 v116, 16, v112\nv_and_b32 v117, 0xffff0000, v112\nv_lshlrev_b32 v118, 16, v113\nv_and_b32 v119, 0xffff0000, v113\nv_lshlrev_b32 v120, 16, v114\n" \
    "v_and_b32 v121, 0xffff0000, v114\nv_lshlrev_b32 v122, 16, v115\nv_and_b32 v123, 0xffff0000, v115\ns_waitcnt lgkmcnt(6)\nv_mul_f32 v116, v116, v80\n" \
    "v_mul_f32 v117, v117, v81\nv_mul_f32 v118, v118, v82\nv_mul_f32 v119, v119, v83\nv_mul_f32 v120, v120, v84\nv_mul_f32 v121, v121, v85\nv_mul_f32 v122, v122, v86\n" \
    "v_mul_f32 v123, v123, v87\nv_mfma_f32_32x32x16_bf16 v[32:47], a[88:91], v[88:91], v[32:47]\nv_cvt_pk_bf16_f32 v132, v116, v117\nv_cvt_pk_bf16_f32 v133, v118, v119\n" \
    "v_cvt_pk_bf16_f32 v134, v120, v121\nv_cvt_pk_bf16_f32 v135, v122, v123\nv_accvgpr_write_b32 a88, v132\nv_accvgpr_write_b32 a89, v133\nv_accvgpr_write_b32 a90, v134\n" \
    "v_accvgpr_write_b32 a91, v135\nv_mfma_f32_32x32x16_bf16 v[16:31], v[132:135], v[128:131], v[16:31]\nds_read_b128 v[80:83], v12 offset:1600\n" \
    "ds_read_b128 v[84:87], v12 offset:1616\nds_read_b128 v[88:91], v13 offset:800\nv_accvgpr_read_b32 v112, a92\nv_accvgpr_read_b32 v113, a93\nv_accvgpr_read_b32 v114, a94\n" \
    "v_accvgpr_read_b32 v115, a95\nv_lshlrev_b32 v116, 16, v112\nv_and_b32 v117, 0xffff0000, v112\nv_lshlrev_b32 v118, 16, v113\nv_and_b32 v119, 0xffff0000, v113\n" \
    "v_lshlrev_b32 v120, 16, v114\nv_and_b32 v121, 0xffff0000, v114\nv_lshlrev_b32 v122, 16, v115\nv_and_b32 v123, 0xffff0000, v115\ns_waitcnt lgkmcnt(6)\n" \
    "v_mul_f32 v116, v116, v96\nv_mul_f32 v117, v117, v97\nv_mul_f32 v118, v118, v98\nv_mul_f32 v119, v119, v99\nv_mul_f32 v120, v120, v100\nv_mul_f32 v121, v121, v101\n" \
    "v_mul_f32 v122, v122, v102\nv_mul_f32 v123, v123, v103\nv_mfma_f32_32x32x16_bf16 v[32:47], a[92:95], v[104:107], v[32:47]\nv_cvt_pk_bf16_f32 v136, v116, v117\n" \
    "v_cvt_pk_bf16_f32 v137, v118, v119\nv_cvt_pk_bf16_f32 v138, v120, v121\nv_cvt_pk_bf16_f32 v139, v122, v123\nv_accvgpr_write_b32 a92, v136\nv_accvgpr_write_b32 a93, v137\n" \
    "v_accvgpr_write_b32 a94, v138\nv_accvgpr_write_b32 a95, v139\nv_mfma_f32_32x32x16_bf16 v[16:31], v[136:139], v[128:131], v[16:31]\nds_read_b128 v[96:99], v12 offset:1664\n" \
    "ds_read_b128 v[100:103], v12 offset:1680\nds_read_b128 v[104:107], v13 offset:832\nv_accvgpr_read_b32 v112, a96\nv_accvgpr_read_b32 v113, a97\n" \
    "v_accvgpr_read_b32 v114, a98\nv_accvgpr_read_b32 v115, a99\nv_lshlrev_b32 v116, 16, v112\nv_and_b32 v117, 0xffff0000, v112\nv_lshlrev_b32 v118, 16, v113\n" \
    "v_and_b32 v119, 0xffff0000, v113\nv_lshlrev_b32 v120, 16, v114\nv_and_b32 v121, 0xffff0000, v114\nv_lshlrev_b32 v122, 16, v115\nv_and_b32 v123, 0xffff0000, v115\n" \
    "s_waitcnt lgkmcnt(6)\nv_mul_f32 v116, v116, v64\nv_mul_f32 v117, v117, v65\nv_mul_f32 v118, v118, v66\nv_mul_f32 v119, v119, v67\nv_mul_f32 v120, v120, v68\n" \
    "v_mul_f32 v121, v121, v69\nv_mul_f32 v122, v122, v70\nv_mul_f32 v123, v123, v71\nv_mfma_f32_32x32x16_bf16 v[32:47], a[96:99], v[72:75], v[32:47]\n" \
    "v_cvt_pk_bf16_f32 v132, v116, v117\nv_cvt_pk_bf16_f32 v133, v118, v119\nv_cvt_pk_bf16_f32 v134, v120, v121\nv_cvt_pk_bf16_f32 v135, v122, v123\n" \
    "v_accvgpr_write_b32 a96, v132\nv_accvgpr_write_b32 a97, v133\nv_accvgpr_write_b32 a98, v134\nv_accvgpr_write_b32 a99, v135\n" \
    "v_mfma_f32_32x32x16_bf16 v[16:31], v[132:135], v[128:131], v[16:31]\nds_read_b128 v[64:67], v12 offset:1728\nds_read_b128 v[68:71], v12 offset:1744\n" \
    "ds_read_b128 v[72:75], v13 offset:864\nv_accvgpr_read_b32 v112, a100\nv_accvgpr_read_b32 v113, a101\nv_accvgpr_read_b32 v114, a102\nv_accvgpr_read_b32 v115, a103\n" \
    "v_lshlrev_b32 v116, 16, v112\nv_and_b32 v117, 0xffff0000, v112\nv_lshlrev_b32 v118, 16, v113\nv_and_b32 v119, 0xffff0000, v113\nv_lshlrev_b32 v120, 16, v114\n" \
    "v_and_b32 v121, 0xffff0000, v114\nv_lshlrev_b32 v122, 16, v115\nv_and_b32 v123, 0xffff0000, v115\ns_waitcnt lgkmcnt(6)\nv_mul_f32 v116, v116, v80\n" \
    "v_mul_f32 v117, v117, v81\nv_mul_f32 v118, v118, v82\nv_mul_f32 v119, v119, v83\nv_mul_f32 v120, v120, v84\nv_mul_f32 v121, v121, v85\nv_mul_f32 v122, v122, v86\n" \
    "v_mul_f32 v123, v123, v87\nv_mfma_f32_32x32x16_bf16 v[32:47], a[100:103], v[88:91], v[32:47]\nv_cvt_pk_bf16_f32 v136, v116, v117\nv_cvt_pk_bf16_f32 v137, v118, v119\n" \
    "v_cvt_pk_bf16_f32 v138, v120, v121\nv_cvt_pk_bf16_f32 v139, v122, v123\nv_accvgpr_write_b32 a100, v136\nv_accvgpr_write_b32 a101, v137\nv_accvgpr_write_b32 a102, v138\n" \
    "v_accvgpr_write_b32 a103, v139\nv_mfma_f32_32x32x16_bf16 v[16:31], v[136:139], v[128:131], v[16:31]\nds_read_b128 v[80:83], v12 offset:1792\n" \
    "ds_read_b128 v[84:87], v12 offset:1808\nds_read_b128 v[88:91], v13 offset:896\nv_accvgpr_read_b32 v112, a104\nv_accvgpr_read_b32 v113, a105\nv_accvgpr_read_b32 v114, a106\n" \
    "v_accvgpr_read_b32 v115, a107\nv_lshlrev_b32 v116, 16, v112\nv_and_b32 v117, 0xffff0000, v112\nv_lshlrev_b32 v118, 16, v113\nv_and_b32 v119, 0xffff0000, v113\n" \
    "v_lshlrev_b32 v120, 16, v114\nv_and_b32 v121, 0xffff0000, v114\nv_lshlrev_b32 v122, 16, v115\nv_and_b32 v123, 0xffff0000, v115\ns_waitcnt lgkmcnt(6)\n" \
    "v_mul_f32 v116, v116, v96\nv_mul_f32 v117, v117, v97\nv_mul_f32 v118, v118, v98\nv_mul_f32 v119, v119, v99\nv_mul_f32 v120, v120, v100\nv_mul_f32 v121, v121, v101\n" \
    "v_mul_f32 v122, v122, v102\nv_mul_f32 v123, v123, v103\nv_mfma_f32_32x32x16_bf16 v[32:47], a[104:107], v[104:107], v[32:47]\nv_cvt_pk_bf16_f32 v132, v116, v117\n" \
    "v_cvt_pk_bf16_f32 v133, v118, v119\nv_cvt_pk_bf16_f32 v134, v120, v121\nv_cvt_pk_bf16_f32 v135, v122, v123\nv_accvgpr_write_b32 a104, v132\nv_accvgpr_write_b32 a105, v133\n" \
    "v_accvgpr_write_b32 a106, v134\nv_accvgpr_write_b32 a107, v135\nv_mfma_f32_32x32x16_bf16 v[16:31], v[132:135], v[128:131], v[16:31]\n" \
    "ds_read_b128 v[96:99], v12 offset:1856\nds_read_b128 v[100:103], v12 offset:1872\nds_read_b128 v[104:107], v13 offset:928\nv_accvgpr_read_b32 v112, a108\n" \
    "v_accvgpr_read_b32 v113, a109\nv_accvgpr_read_b32 v114, a110\nv_accvgpr_read_b32 v115, a111\nv_lshlrev_b32 v116, 16, v112\nv_and_b32 v117, 0xffff0000, v112\n" \
    "v_lshlrev_b32 v118, 16, v113\nv_and_b32 v119, 0xffff0000, v113\nv_lshlrev_b32 v120, 16, v114\nv_and_b32 v121, 0xffff0000, v114\nv_lshlrev_b32 v122, 16, v115\n" \
    "v_and_b32 v123, 0xffff0000, v115\ns_waitcnt lgkmcnt(6)\nv_mul_f32 v116, v116, v64\nv_mul_f32 v117, v117, v65\nv_mul_f32 v118, v118, v66\nv_mul_f32 v119, v119, v67\n" \
    "v_mul_f32 v120, v120, v68\nv_mul_f32 v121, v121, v69\nv_mul_f32 v122, v122, v70\nv_mul_f32 v123, v123, v71\n" \
    "v_mfma_f32_32x32x16_bf16 v[32:47], a[108:111], v[72:75], v[32:47]\nv_cvt_pk_bf16_f32 v136, v116, v117\nv_cvt_pk_bf16_f32 v137, v118, v119\n" \
    "v_cvt_pk_bf16_f32 v138, v120, v121\nv_cvt_pk_bf16_f32 v139, v122, v123\nv_accvgpr_write_b32 a108, v136\nv_accvgpr_write_b32 a109, v137\nv_accvgpr_write_b32 a110, v138\n" \
    "v_accvgpr_write_b32 a111, v139\nv_mfma_f32_32x32x16_bf16 v[16:31], v[136:139], v[128:131], v[16:31]\nds_read_b128 v[64:67], v12 offset:1920\n" \
    "ds_read_b128 v[68:71], v12 offset:1936\nds_read_b128 v[72:75], v13 offset:960\nv_accvgpr_read_b32 v112, a112\nv_accvgpr_read_b32 v113, a113\nv_accvgpr_read_b32 v114, a114\n" \
    "v_accvgpr_read_b32 v115, a115\nv_lshlrev_b32 v116, 16, v112\nv_and_b32 v117, 0xffff0000, v112\nv_lshlrev_b32 v118, 16, v113\nv_and_b32 v119, 0xffff0000, v113\n" \
    "v_lshlrev_b32 v120, 16, v114\nv_and_b32 v121, 0xffff0000, v114\nv_lshlrev_b32 v122, 16, v115\nv_and_b32 v123, 0xffff0000, v115\ns_waitcnt lgkmcnt(6)\n" \
    "v_mul_f32 v116, v116, v80\nv_mul_f32 v117, v117, v81\nv_mul_f32 v118, v118, v82\nv_mul_f32 v119, v119, v83\nv_mul_f32 v120, v120, v84\nv_mul_f32 v121, v121, v85\n" \
    "v_mul_f32 v122, v122, v86\nv_mul_f32 v123, v123, v87\nv_mfma_f32_32x32x16_bf16 v[32:47], a[112:115], v[88:91], v[32:47]\nv_cvt_pk_bf16_f32 v132, v116, v117\n" \
    "v_cvt_pk_bf16_f32 v133, v118, v119\nv_cvt_pk_bf16_f32 v134, v120, v121\nv_cvt_pk_bf16_f32 v135, v122, v123\nv_accvgpr_write_b32 a112, v132\nv_accvgpr_write_b32 a113, v133\n" \
    "v_accvgpr_write_b32 a114, v134\nv_accvgpr_write_b32 a115, v135\nv_mfma_f32_32x32x16_bf16 v[16:31], v[132:135], v[128:131], v[16:31]\n" \
    "ds_read_b128 v[80:83], v12 offset:1984\nds_read_b128 v[84:87], v12 offset:2000\nds_read_b128 v[88:91], v13 offset:992\nv_accvgpr_read_b32 v112, a116\n" \
    "v_accvgpr_read_b32 v113, a117\nv_accvgpr_read_b32 v114, a118\nv_accvgpr_read_b32 v115, a119\nv_lshlrev_b32 v116, 16, v112\nv_and_b32 v117, 0xffff0000, v112\n" \
    "v_lshlrev_b32 v118, 16, v113\nv_and_b32 v119, 0xffff0000, v113\nv_lshlrev_b32 v120, 16, v114\nv_and_b32 v121, 0xffff0000, v114\nv_lshlrev_b32 v122, 16, v115\n" \
    "v_and_b32 v123, 0xffff0000, v115\ns_waitcnt lgkmcnt(6)\nv_mul_f32 v116, v116, v96\nv_mul_f32 v117, v117, v97\nv_mul_f32 v118, v118, v98\nv_mul_f32 v119, v119, v99\n" \
    "v_mul_f32 v120, v120, v100\nv_mul_f32 v121, v121, v101\nv_mul_f32 v122, v122, v102\nv_mul_f32 v123, v123, v103\n" \
    "v_mfma_f32_32x32x16_bf16 v[32:47], a[116:119], v[104:107], v[32:47]\nv_cvt_pk_bf16_f32 v136, v116, v117\nv_cvt_pk_bf16_f32 v137, v118, v119\n" \
    "v_cvt_pk_bf16_f32 v138, v120, v121\nv_cvt_pk_bf16_f32 v139, v122, v123\nv_accvgpr_write_b32 a116, v136\nv_accvgpr_write_b32 a117, v137\nv_accvgpr_write_b32 a118, v138\n" \
    "v_accvgpr_write_b32 a119, v139\nv_mfma_f32_32x32x16_bf16 v[16:31], v[136:139], v[128:131], v[16:31]\nds_read_b128 v[96:99], v12 offset:2048\n" \
    "ds_read_b128 v[100:103], v12 offset:2064\nds_read_b128 v[104:107], v13 offset:1024\nv_accvgpr_read_b32 v112, a120\nv_accvgpr_read_b32 v113, a121\n" \
    "v_accvgpr_read_b32 v114, a122\nv_accvgpr_read_b32 v115, a123\nv_lshlrev_b32 v116, 16, v112\nv_and_b32 v117, 0xffff0000, v112\nv_lshlrev_b32 v118, 16, v113\n" \
    "v_and_b32 v119, 0xffff0000, v113\nv_lshlrev_b32 v120, 16, v114\nv_and_b32 v121, 0xffff0000, v114\nv_lshlrev_b32 v122, 16, v115\nv_and_b32 v123, 0xffff0000, v115\n" \
    "s_waitcnt lgkmcnt(6)\nv_mul_f32 v116, v116, v64\nv_mul_f32 v117, v117, v65\nv_mul_f32 v118, v118, v66\nv_mul_f32 v119, v119, v67\nv_mul_f32 v120, v120, v68\n" \
    "v_mul_f32 v121, v121, v69\nv_mul_f32 v122, v122, v70\nv_mul_f32 v123, v123, v71\nv_mfma_f32_32x32x16_bf16 v[32:47], a[120:123], v[72:75], v[32:47]\n" \
    "v_cvt_pk_bf16_f32 v132, v116, v117\nv_cvt_pk_bf16_f32 v133, v118, v119\nv_cvt_pk_bf16_f32 v134, v120, v121\nv_cvt_pk_bf16_f32 v135, v122, v123\n" \
    "v_accvgpr_write_b32 a120, v132\nv_accvgpr_write_b32 a121, v133\nv_accvgpr_write_b32 a122, v134\nv_accvgpr_write_b32 a123, v135\n" \
    "v_mfma_f32_32x32x16_bf16 v[16:31], v[132:135], v[128:131], v[16:31]\nds_read_b128 v[64:67], v12 offset:2112\nds_read_b128 v[68:71], v12 offset:2128\n" \
    "ds_read_b128 v[72:75], v13 offset:1056\nv_accvgpr_read_b32 v112, a124\nv_accvgpr_read_b32 v113, a125\nv_accvgpr_read_b32 v114, a126\nv_accvgpr_read_b32 v115, a127\n" \
    "v_lshlrev_b32 v116, 16, v112\nv_and_b32 v117, 0xffff0000, v112\nv_lshlrev_b32 v118, 16, v113\nv_and_b32 v119, 0xffff0000, v113\nv_lshlrev_b32 v120, 16, v114\n" \
    "v_and_b32 v121, 0xffff0000, v114\nv_lshlrev_b32 v122, 16, v115\nv_and_b32 v123, 0xffff0000, v115\ns_waitcnt lgkmcnt(6)\nv_mul_f32 v116, v116, v80\n" \
    "v_mul_f32 v117, v117, v81\nv_mul_f32 v118, v118, v82\nv_mul_f32 v119, v119, v83\nv_mul_f32 v120, v120, v84\nv_mul_f32 v121, v121, v85\nv_mul_f32 v122, v122, v86\n" \
    "v_mul_f32 v123, v123, v87\nv_mfma_f32_32x32x16_bf16 v[32:47], a[124:127], v[88:91], v[32:47]\nv_cvt_pk_bf16_f32 v136, v116, v117\nv_cvt_pk_bf16_f32 v137, v118, v119\n" \
    "v_cvt_pk_bf16_f32 v138, v120, v121\nv_cvt_pk_bf16_f32 v139, v122, v123\nv_accvgpr_write_b32 a124, v136\nv_accvgpr_write_b32 a125, v137\nv_accvgpr_write_b32 a126, v138\n" \
    "v_accvgpr_write_b32 a127, v139\nv_mfma_f32_32x32x16_bf16 v[16:31], v[136:139], v[128:131], v[16:31]\nds_read_b128 v[80:83], v12 offset:2176\n" \
    "ds_read_b128 v[84:87], v12 offset:2192\nds_read_b128 v[88:91], v13 offset:1088\nv_accvgpr_read_b32 v112, a128\nv_accvgpr_read_b32 v113, a129\n" \
    "v_accvgpr_read_b32 v114, a130\nv_accvgpr_read_b32 v115, a131\nv_lshlrev_b32 v116, 16, v112\nv_and_b32 v117, 0xffff0000, v112\nv_lshlrev_b32 v118, 16, v113\n" \
    "v_and_b32 v119, 0xffff0000, v113\nv_lshlrev_b32 v120, 16, v114\nv_and_b32 v121, 0xffff0000, v114\nv_lshlrev_b32 v122, 16, v115\nv_and_b32 v123, 0xffff0000, v115\n" \
    "s_waitcnt lgkmcnt(6)\nv_mul_f32 v116, v116, v96\nv_mul_f32 v117, v117, v97\nv_mul_f32 v118, v118, v98\nv_mul_f32 v119, v119, v99\nv_mul_f32 v120, v120, v100\n" \
    "v_mul_f32 v121, v121, v101\nv_mul_f32 v122, v122, v102\nv_mul_f32 v123, v123, v103\nv_mfma_f32_32x32x16_bf16 v[32:47], a[128:131], v[104:107], v[32:47]\n" \
    "v_cvt_pk_bf16_f32 v132, v116, v117\nv_cvt_pk_bf16_f32 v133, v118, v119\nv_cvt_pk_bf16_f32 v134, v120, v121\nv_cvt_pk_bf16_f32 v135, v122, v123\n" \
    "v_accvgpr_write_b32 a128, v132\nv_accvgpr_write_b32 a129, v133\nv_accvgpr_write_b32 a130, v134\nv_accvgpr_write_b32 a131, v135\n" \
    "v_mfma_f32_32x32x16_bf16 v[16:31], v[132:135], v[128:131], v[16:31]\nds_read_b128 v[96:99], v12 offset:2240\nds_read_b128 v[100:103], v12 offset:2256\n" \
    "ds_read_b128 v[104:107], v13 offset:1120\nv_accvgpr_read_b32 v112, a132\nv_accvgpr_read_b32 v113, a133\nv_accvgpr_read_b32 v114, a134\nv_accvgpr_read_b32 v115, a135\n" \
    "v_lshlrev_b32 v116, 16, v112\nv_and_b32 v117, 0xffff0000, v112\nv_lshlrev_b32 v118, 16, v113\nv_and_b32 v119, 0xffff0000, v113\nv_lshlrev_b32 v120, 16, v114\n" \
    "v_and_b32 v121, 0xffff0000, v114\nv_lshlrev_b32 v122, 16, v115\nv_and_b32 v123, 0xffff0000, v115\ns_waitcnt lgkmcnt(6)\nv_mul_f32 v116, v116, v64\n" \
    "v_mul_f32 v117, v117, v65\nv_mul_f32 v118, v118, v66\nv_mul_f32 v119, v119, v67\nv_mul_f32 v120, v120, v68\nv_mul_f32 v121, v121, v69\nv_mul_f32 v122, v122, v70\n" \
    "v_mul_f32 v123, v123, v71\nv_mfma_f32_32x32x16_bf16 v[32:47], a[132:135], v[72:75], v[32:47]\nv_cvt_pk_bf16_f32 v136, v116, v117\nv_cvt_pk_bf16_f32 v137, v118, v119\n" \
    "v_cvt_pk_bf16_f32 v138, v120, v121\nv_cvt_pk_bf16_f32 v139, v122, v123\nv_accvgpr_write_b32 a132, v136\nv_accvgpr_write_b32 a133, v137\nv_accvgpr_write_b32 a134, v138\n" \
    "v_accvgpr_write_b32 a135, v139\nv_mfma_f32_32x32x16_bf16 v[16:31], v[136:139], v[128:131], v[16:31]\nds_read_b128 v[64:67], v12 offset:2304\n" \
    "ds_read_b128 v[68:71], v12 offset:2320\nds_read_b128 v[72:75], v13 offset:1152\nv_accvgpr_read_b32 v112, a136\nv_accvgpr_read_b32 v113, a137\n" \
    "v_accvgpr_read_b32 v114, a138\nv_accvgpr_read_b32 v115, a139\nv_lshlrev_b32 v116, 16, v112\nv_and_b32 v117, 0xffff0000, v112\nv_lshlrev_b32 v118, 16, v113\n" \
    "v_and_b32 v119, 0xffff0000, v113\nv_lshlrev_b32 v120, 16, v114\nv_and_b32 v121, 0xffff0000, v114\nv_lshlrev_b32 v122, 16, v115\nv_and_b32 v123, 0xffff0000, v115\n" \
    "s_waitcnt lgkmcnt(6)\nv_mul_f32 v116, v116, v80\nv_mul_f32 v117, v117, v81\nv_mul_f32 v118, v118, v82\nv_mul_f32 v119, v119, v83\nv_mul_f32 v120, v120, v84\n" \
    "v_mul_f32 v121, v121, v85\nv_mul_f32 v122, v122, v86\nv_mul_f32 v123, v123, v87\nv_mfma_f32_32x32x16_bf16 v[32:47], a[136:139], v[88:91], v[32:47]\n" \
    "v_cvt_pk_bf16_f32 v132, v116, v117\nv_cvt_pk_bf16_f32 v133, v118, v119\nv_cvt_pk_bf16_f32 v134, v120, v121\nv_cvt_pk_bf16_f32 v135, v122, v123\n" \
    "v_accvgpr_write_b32 a136, v132\nv_accvgpr_write_b32 a137, v133\nv_accvgpr_write_b32 a138, v134\nv_accvgpr_write_b32 a139, v135\n" \
    "v_mfma_f32_32x32x16_bf16 v[16:31], v[132:135], v[128:131], v[16:31]\nds_read_b128 v[80:83], v12 offset:2368\nds_read_b128 v[84:87], v12 offset:2384\n" \
    "ds_read_b128 v[88:91], v13 offset:1184\nv_accvgpr_read_b32 v112, a140\nv_accvgpr_read_b32 v113, a141\nv_accvgpr_read_b32 v114, a142\nv_accvgpr_read_b32 v115, a143\n" \
    "v_lshlrev_b32 v116, 16, v112\nv_and_b32 v117, 0xffff0000, v112\nv_lshlrev_b32 v118, 16, v113\nv_and_b32 v119, 0xffff0000, v113\nv_lshlrev_b32 v120, 16, v114\n" \
    "v_and_b32 v121, 0xffff0000, v114\nv_lshlrev_b32 v122, 16, v115\nv_and_b32 v123, 0xffff0000, v115\ns_waitcnt lgkmcnt(6)\nv_mul_f32 v116, v116, v96\n" \
    "v_mul_f32 v117, v117, v97\nv_mul_f32 v118, v118, v98\nv_mul_f32 v119, v119, v99\nv_mul_f32 v120, v120, v100\nv_mul_f32 v121, v121, v101\nv_mul_f32 v122, v122, v102\n" \
    "v_mul_f32 v123, v123, v103\nv_mfma_f32_32x32x16_bf16 v[32:47], a[140:143], v[104:107], v[32:47]\nv_cvt_pk_bf16_f32 v136, v116, v117\nv_cvt_pk_bf16_f32 v137, v118, v119\n" \
    "v_cvt_pk_bf16_f32 v138, v120, v121\nv_cvt_pk_bf16_f32 v139, v122, v123\nv_accvgpr_write_b32 a140, v136\nv_accvgpr_write_b32 a141, v137\nv_accvgpr_write_b32 a142, v138\n" \
    "v_accvgpr_write_b32 a143, v139\nv_mfma_f32_32x32x16_bf16 v[16:31], v[136:139], v[128:131], v[16:31]\nds_read_b128 v[96:99], v12 offset:2432\n" \
    "ds_read_b128 v[100:103], v12 offset:2448\nds_read_b128 v[104:107], v13 offset:1216\nv_accvgpr_read_b32 v112, a144\nv_accvgpr_read_b32 v113, a145\n" \
    "v_accvgpr_read_b32 v114, a146\nv_accvgpr_read_b32 v115, a147\nv_lshlrev_b32 v116, 16, v112\nv_and_b32 v117, 0xffff0000, v112\nv_lshlrev_b32 v118, 16, v113\n" \
    "v_and_b32 v119, 0xffff0000, v113\nv_lshlrev_b32 v120, 16, v114\nv_and_b32 v121, 0xffff0000, v114\nv_lshlrev_b32 v122, 16, v115\nv_and_b32 v123, 0xffff0000, v115\n" \
    "s_waitcnt lgkmcnt(6)\nv_mul_f32 v116, v116, v64\nv_mul_f32 v117, v117, v65\nv_mul_f32 v118, v118, v66\nv_mul_f32 v119, v119, v67\nv_mul_f32 v120, v120, v68\n" \
    "v_mul_f32 v121, v121, v69\nv_mul_f32 v122, v122, v70\nv_mul_f32 v123, v123, v71\nv_mfma_f32_32x32x16_bf16 v[32:47], a[144:147], v[72:75], v[32:47]\n" \
    "v_cvt_pk_bf16_f32 v132, v116, v117\nv_cvt_pk_bf16_f32 v133, v118, v119\nv_cvt_pk_bf16_f32 v134, v120, v121\nv_cvt_pk_bf16_f32 v135, v122, v123\n" \
    "v_accvgpr_write_b32 a144, v132\nv_accvgpr_write_b32 a145, v133\nv_accvgpr_write_b32 a146, v134\nv_accvgpr_write_b32 a147, v135\n" \
    "v_mfma_f32_32x32x16_bf16 v[16:31], v[132:135], v[128:131], v[16:31]\nds_read_b128 v[64:67], v12 offset:2496\nds_read_b128 v[68:71], v12 offset:2512\n" \
    "ds_read_b128 v[72:75], v13 offset:1248\nv_accvgpr_read_b32 v112, a148\nv_accvgpr_read_b32 v113, a149\nv_accvgpr_read_b32 v114, a150\nv_accvgpr_read_b32 v115, a151\n" \
    "v_lshlrev_b32 v116, 16, v112\nv_and_b32 v117, 0xffff0000, v112\nv_lshlrev_b32 v118, 16, v113\nv_and_b32 v119, 0xffff0000, v113\nv_lshlrev_b32 v120, 16, v114\n" \
    "v_and_b32 v121, 0xffff0000, v114\nv_lshlrev_b32 v122, 16, v115\nv_and_b32 v123, 0xffff0000, v115\ns_waitcnt lgkmcnt(6)\nv_mul_f32 v116, v116, v80\n" \
    "v_mul_f32 v117, v117, v81\nv_mul_f32 v118, v118, v82\nv_mul_f32 v119, v119, v83\nv_mul_f32 v120, v120, v84\nv_mul_f32 v121, v121, v85\nv_mul_f32 v122, v122, v86\n" \
    "v_mul_f32 v123, v123, v87\nv_mfma_f32_32x32x16_bf16 v[32:47], a[148:151], v[88:91], v[32:47]\nv_cvt_pk_bf16_f32 v136, v116, v117\nv_cvt_pk_bf16_f32 v137, v118, v119\n" \
    "v_cvt_pk_bf16_f32 v138, v120, v121\nv_cvt_pk_bf16_f32 v139, v122, v123\nv_accvgpr_write_b32 a148, v136\nv_accvgpr_write_b32 a149, v137\nv_accvgpr_write_b32 a150, v138\n" \
    "v_accvgpr_write_b32 a151, v139\nv_mfma_f32_32x32x16_bf16 v[16:31], v[136:139], v[128:131], v[16:31]\nds_read_b128 v[80:83], v12 offset:2560\n" \
    "ds_read_b128 v[84:87], v12 offset:2576\nds_read_b128 v[88:91], v13 offset:1280\nv_accvgpr_read_b32 v112, a152\nv_accvgpr_read_b32 v113, a153\n" \
    "v_accvgpr_read_b32 v114, a154\nv_accvgpr_read_b32 v115, a155\nv_lshlrev_b32 v116, 16, v112\nv_and_b32 v117, 0xffff0000, v112\nv_lshlrev_b32 v118, 16, v113\n" \
    "v_and_b32 v119, 0xffff0000, v113\nv_lshlrev_b32 v120, 16, v114\nv_and_b32 v121, 0xffff0000, v114\nv_lshlrev_b32 v122, 16, v115\nv_and_b32 v123, 0xffff0000, v115\n" \
    "s_waitcnt lgkmcnt(6)\nv_mul_f32 v116, v116, v96\nv_mul_f32 v117, v117, v97\nv_mul_f32 v118, v118, v98\nv_mul_f32 v119, v119, v99\nv_mul_f32 v120, v120, v100\n" \
    "v_mul_f32 v121, v121, v101\nv_mul_f32 v122, v122, v102\nv_mul_f32 v123, v123, v103\nv_mfma_f32_32x32x16_bf16 v[32:47], a[152:155], v[104:107], v[32:47]\n" \
    "v_cvt_pk_bf16_f32 v132, v116, v117\nv_cvt_pk_bf16_f32 v133, v118, v119\nv_cvt_pk_bf16_f32 v134, v120, v121\nv_cvt_pk_bf16_f32 v135, v122, v123\n" \
    "v_accvgpr_write_b32 a152, v132\nv_accvgpr_write_b32 a153, v133\nv_accvgpr_write_b32 a154, v134\nv_accvgpr_write_b32 a155, v135\n" \
    "v_mfma_f32_32x32x16_bf16 v[16:31], v[132:135], v[128:131], v[16:31]\nds_read_b128 v[96:99], v12 offset:2624\nds_read_b128 v[100:103], v12 offset:2640\n" \
    "ds_read_b128 v[104:107], v13 offset:1312\nv_accvgpr_read_b32 v112, a156\nv_accvgpr_read_b32 v113, a157\nv_accvgpr_read_b32 v114, a158\nv_accvgpr_read_b32 v115, a159\n" \
    "v_lshlrev_b32 v116, 16, v112\nv_and_b32 v117, 0xffff0000, v112\nv_lshlrev_b32 v118, 16, v113\nv_and_b32 v119, 0xffff0000, v113\nv_lshlrev_b32 v120, 16, v114\n" \
    "v_and_b32 v121, 0xffff0000, v114\nv_lshlrev_b32 v122, 16, v115\nv_and_b32 v123, 0xffff0000, v115\ns_waitcnt lgkmcnt(6)\nv_mul_f32 v116, v116, v64\n" \
    "v_mul_f32 v117, v117, v65\nv_mul_f32 v118, v118, v66\nv_mul_f32 v119, v119, v67\nv_mul_f32 v120, v120, v68\nv_mul_f32 v121, v121, v69\nv_mul_f32 v122, v122, v70\n" \
    "v_mul_f32 v123, v123, v71\nv_mfma_f32_32x32x16_bf16 v[32:47], a[156:159], v[72:75], v[32:47]\nv_cvt_pk_bf16_f32 v136, v116, v117\nv_cvt_pk_bf16_f32 v137, v118, v119\n" \
    "v_cvt_pk_bf16_f32 v138, v120, v121\nv_cvt_pk_bf16_f32 v139, v122, v123\nv_accvgpr_write_b32 a156, v136\nv_accvgpr_write_b32 a157, v137\nv_accvgpr_write_b32 a158, v138\n" \
    "v_accvgpr_write_b32 a159, v139\nv_mfma_f32_32x32x16_bf16 v[16:31], v[136:139], v[128:131], v[16:31]\nds_read_b128 v[64:67], v12 offset:2688\n" \
    "ds_read_b128 v[68:71], v12 offset:2704\nds_read_b128 v[72:75], v13 offset:1344\nv_accvgpr_read_b32 v112, a160\nv_accvgpr_read_b32 v113, a161\n" \
    "v_accvgpr_read_b32 v114, a162\nv_accvgpr_read_b32 v115, a163\nv_lshlrev_b32 v116, 16, v112\nv_and_b32 v117, 0xffff0000, v112\nv_lshlrev_b32 v118, 16, v113\n" \
    "v_and_b32 v119, 0xffff0000, v113\nv_lshlrev_b32 v120, 16, v114\nv_and_b32 v121, 0xffff0000, v114\nv_lshlrev_b32 v122, 16, v115\nv_and_b32 v123, 0xffff0000, v115\n" \
    "s_waitcnt lgkmcnt(6)\nv_mul_f32 v116, v116, v80\nv_mul_f32 v117, v117, v81\nv_mul_f32 v118, v118, v82\nv_mul_f32 v119, v119, v83\nv_mul_f32 v120, v120, v84\n" \
    "v_mul_f32 v121, v121, v85\nv_mul_f32 v122, v122, v86\nv_mul_f32 v123, v123, v87\nv_mfma_f32_32x32x16_bf16 v[32:47], a[160:163], v[88:91], v[32:47]\n" \
    "v_cvt_pk_bf16_f32 v132, v116, v117\nv_cvt_pk_bf16_f32 v133, v118, v119\nv_cvt_pk_bf16_f32 v134, v120, v121\nv_cvt_pk_bf16_f32 v135, v122, v123\n" \
    "v_accvgpr_write_b32 a160, v132\nv_accvgpr_write_b32 a161, v133\nv_accvgpr_write_b32 a162, v134\nv_accvgpr_write_b32 a163, v135\n" \
    "v_mfma_f32_32x32x16_bf16 v[16:31], v[132:135], v[128:131], v[16:31]\nds_read_b128 v[80:83], v12 offset:2752\nds_read_b128 v[84:87], v12 offset:2768\n" \
    "ds_read_b128 v[88:91], v13 offset:1376\nv_accvgpr_read_b32 v112, a164\nv_accvgpr_read_b32 v113, a165\nv_accvgpr_read_b32 v114, a166\nv_accvgpr_read_b32 v115, a167\n" \
    "v_lshlrev_b32 v116, 16, v112\nv_and_b32 v117, 0xffff0000, v112\nv_lshlrev_b32 v118, 16, v113\nv_and_b32 v119, 0xffff0000, v113\nv_lshlrev_b32 v120, 16, v114\n" \
    "v_and_b32 v121, 0xffff0000, v114\nv_lshlrev_b32 v122, 16, v115\nv_and_b32 v123, 0xffff0000, v115\ns_waitcnt lgkmcnt(6)\nv_mul_f32 v116, v116, v96\n" \
    "v_mul_f32 v117, v117, v97\nv_mul_f32 v118, v118, v98\nv_mul_f32 v119, v119, v99\nv_mul_f32 v120, v120, v100\nv_mul_f32 v121, v121, v101\nv_mul_f32 v122, v122, v102\n" \
    "v_mul_f32 v123, v123, v103\nv_mfma_f32_32x32x16_bf16 v[32:47], a[164:167], v[104:107], v[32:47]\nv_cvt_pk_bf16_f32 v136, v116, v117\nv_cvt_pk_bf16_f32 v137, v118, v119\n" \
    "v_cvt_pk_bf16_f32 v138, v120, v121\nv_cvt_pk_bf16_f32 v139, v122, v123\nv_accvgpr_write_b32 a164, v136\nv_accvgpr_write_b32 a165, v137\nv_accvgpr_write_b32 a166, v138\n" \
    "v_accvgpr_write_b32 a167, v139\nv_mfma_f32_32x32x16_bf16 v[16:31], v[136:139], v[128:131], v[16:31]\nds_read_b128 v[96:99], v12 offset:2816\n" \
    "ds_read_b128 v[100:103], v12 offset:2832\nds_read_b128 v[104:107], v13 offset:1408\nv_accvgpr_read_b32 v112, a168\nv_accvgpr_read_b32 v113, a169\n" \
    "v_accvgpr_read_b32 v114, a170\nv_accvgpr_read_b32 v115, a171\nv_lshlrev_b32 v116, 16, v112\nv_and_b32 v117, 0xffff0000, v112\nv_lshlrev_b32 v118, 16, v113\n" \
    "v_and_b32 v119, 0xffff0000, v113\nv_lshlrev_b32 v120, 16, v114\nv_and_b32 v121, 0xffff0000, v114\nv_lshlrev_b32 v122, 16, v115\nv_and_b32 v123, 0xffff0000, v115\n" \
    "s_waitcnt lgkmcnt(6)\nv_mul_f32 v116, v116, v64\nv_mul_f32 v117, v117, v65\nv_mul_f32 v118, v118, v66\nv_mul_f32 v119, v119, v67\nv_mul_f32 v120, v120, v68\n" \
    "v_mul_f32 v121, v121, v69\nv_mul_f32 v122, v122, v70\nv_mul_f32 v123, v123, v71\nv_mfma_f32_32x32x16_bf16 v[32:47], a[168:171], v[72:75], v[32:47]\n" \
    "v_cvt_pk_bf16_f32 v132, v116, v117\nv_cvt_pk_bf16_f32 v133, v118, v119\nv_cvt_pk_bf16_f32 v134, v120, v121\nv_cvt_pk_bf16_f32 v135, v122, v123\n" \
    "v_accvgpr_write_b32 a168, v132\nv_accvgpr_write_b32 a169, v133\nv_accvgpr_write_b32 a170, v134\nv_accvgpr_write_b32 a171, v135\n" \
    "v_mfma_f32_32x32x16_bf16 v[16:31], v[132:135], v[128:131], v[16:31]\nds_read_b128 v[64:67], v12 offset:2880\nds_read_b128 v[68:71], v12 offset:2896\n" \
    "ds_read_b128 v[72:75], v13 offset:1440\nv_accvgpr_read_b32 v112, a172\nv_accvgpr_read_b32 v113, a173\nv_accvgpr_read_b32 v114, a174\nv_accvgpr_read_b32 v115, a175\n" \
    "v_lshlrev_b32 v116, 16, v112\nv_and_b32 v117, 0xffff0000, v112\nv_lshlrev_b32 v118, 16, v113\nv_and_b32 v119, 0xffff0000, v113\nv_lshlrev_b32 v120, 16, v114\n" \
    "v_and_b32 v121, 0xffff0000, v114\nv_lshlrev_b32 v122, 16, v115\nv_and_b32 v123, 0xffff0000, v115\ns_waitcnt lgkmcnt(6)\nv_mul_f32 v116, v116, v80\n" \
    "v_mul_f32 v117, v117, v81\nv_mul_f32 v118, v118, v82\nv_mul_f32 v119, v119, v83\nv_mul_f32 v120, v120, v84\nv_mul_f32 v121, v121, v85\nv_mul_f32 v122, v122, v86\n" \
    "v_mul_f32 v123, v123, v87\nv_mfma_f32_32x32x16_bf16 v[32:47], a[172:175], v[88:91], v[32:47]\nv_cvt_pk_bf16_f32 v136, v116, v117\nv_cvt_pk_bf16_f32 v137, v118, v119\n" \
    "v_cvt_pk_bf16_f32 v138, v120, v121\nv_cvt_pk_bf16_f32 v139, v122, v123\nv_accvgpr_write_b32 a172, v136\nv_accvgpr_write_b32 a173, v137\nv_accvgpr_write_b32 a174, v138\n" \
    "v_accvgpr_write_b32 a175, v139\nv_mfma_f32_32x32x16_bf16 v[16:31], v[136:139], v[128:131], v[16:31]\nds_read_b128 v[80:83], v12 offset:2944\n" \
    "ds_read_b128 v[84:87], v12 offset:2960\nds_read_b128 v[88:91], v13 offset:1472\nv_accvgpr_read_b32 v112, a176\nv_accvgpr_read_b32 v113, a177\n" \
    "v_accvgpr_read_b32 v114, a178\nv_accvgpr_read_b32 v115, a179\nv_lshlrev_b32 v116, 16, v112\nv_and_b32 v117, 0xffff0000, v112\nv_lshlrev_b32 v118, 16, v113\n" \
    "v_and_b32 v119, 0xffff0000, v113\nv_lshlrev_b32 v120, 16, v114\nv_and_b32 v121, 0xffff0000, v114\nv_lshlrev_b32 v122, 16, v115\nv_and_b32 v123, 0xffff0000, v115\n" \
    "s_waitcnt lgkmcnt(6)\nv_mul_f32 v116, v116, v96\nv_mul_f32 v117, v117, v97\nv_mul_f32 v118, v118, v98\nv_mul_f32 v119, v119, v99\nv_mul_f32 v120, v120, v100\n" \
    "v_mul_f32 v121, v121, v101\nv_mul_f32 v122, v122, v102\nv_mul_f32 v123, v123, v103\nv_mfma_f32_32x32x16_bf16 v[32:47], a[176:179], v[104:107], v[32:47]\n" \
    "v_cvt_pk_bf16_f32 v132, v116, v117\nv_cvt_pk_bf16_f32 v133, v118, v119\nv_cvt_pk_bf16_f32 v134, v120, v121\nv_cvt_pk_bf16_f32 v135, v122, v123\n" \
    "v_accvgpr_write_b32 a176, v132\nv_accvgpr_write_b32 a177, v133\nv_accvgpr_write_b32 a178, v134\nv_accvgpr_write_b32 a179, v135\n" \
    "v_mfma_f32_32x32x16_bf16 v[16:31], v[132:135], v[128:131], v[16:31]\nds_read_b128 v[96:99], v12 offset:3008\nds_read_b128 v[100:103], v12 offset:3024\n" \
    "ds_read_b128 v[104:107], v13 offset:1504\nv_accvgpr_read_b32 v112, a180\nv_accvgpr_read_b32 v113, a181\nv_accvgpr_read_b32 v114, a182\nv_accvgpr_read_b32 v115, a183\n" \
    "v_lshlrev_b32 v116, 16, v112\nv_and_b32 v117, 0xffff0000, v112\nv_lshlrev_b32 v118, 16, v113\nv_and_b32 v119, 0xffff0000, v113\nv_lshlrev_b32 v120, 16, v114\n" \
    "v_and_b32 v121, 0xffff0000, v114\nv_lshlrev_b32 v122, 16, v115\nv_and_b32 v123, 0xffff0000, v115\ns_waitcnt lgkmcnt(6)\nv_mul_f32 v116, v116, v64\n" \
    "v_mul_f32 v117, v117, v65\nv_mul_f32 v118, v118, v66\nv_mul_f32 v119, v119, v67\nv_mul_f32 v120, v120, v68\nv_mul_f32 v121, v121, v69\nv_mul_f32 v122, v122, v70\n" \
    "v_mul_f32 v123, v123, v71\nv_mfma_f32_32x32x16_bf16 v[32:47], a[180:183], v[72:75], v[32:47]\nv_cvt_pk_bf16_f32 v136, v116, v117\nv_cvt_pk_bf16_f32 v137, v118, v119\n" \
    "v_cvt_pk_bf16_f32 v138, v120, v121\nv_cvt_pk_bf16_f32 v139, v122, v123\nv_accvgpr_write_b32 a180, v136\nv_accvgpr_write_b32 a181, v137\nv_accvgpr_write_b32 a182, v138\n" \
    "v_accvgpr_write_b32 a183, v139\nv_mfma_f32_32x32x16_bf16 v[16:31], v[136:139], v[128:131], v[16:31]\nds_read_b128 v[64:67], v12 offset:3072\n" \
    "ds_read_b128 v[68:71], v12 offset:3088\nds_read_b128 v[72:75], v13 offset:1536\nv_accvgpr_read_b32 v112, a184\nv_accvgpr_read_b32 v113, a185\n" \
    "v_accvgpr_read_b32 v114, a186\nv_accvgpr_read_b32 v115, a187\nv_lshlrev_b32 v116, 16, v112\nv_and_b32 v117, 0xffff0000, v112\nv_lshlrev_b32 v118, 16, v113\n" \
    "v_and_b32 v119, 0xffff0000, v113\nv_lshlrev_b32 v120, 16, v114\nv_and_b32 v121, 0xffff0000, v114\nv_lshlrev_b32 v122, 16, v115\nv_and_b32 v123, 0xffff0000, v115\n" \
    "s_waitcnt lgkmcnt(6)\nv_mul_f32 v116, v116, v80\nv_mul_f32 v117, v117, v81\nv_mul_f32 v118, v118, v82\nv_mul_f32 v119, v119, v83\nv_mul_f32 v120, v120, v84\n" \
    "v_mul_f32 v121, v121, v85\nv_mul_f32 v122, v122, v86\nv_mul_f32 v123, v123, v87\nv_mfma_f32_32x32x16_bf16 v[32:47], a[184:187], v[88:91], v[32:47]\n" \
    "v_cvt_pk_bf16_f32 v132, v116, v117\nv_cvt_pk_bf16_f32 v133, v118, v119\nv_cvt_pk_bf16_f32 v134, v120, v121\nv_cvt_pk_bf16_f32 v135, v122, v123\n" \
    "v_accvgpr_write_b32 a184, v132\nv_accvgpr_write_b32 a185, v133\nv_accvgpr_write_b32 a186, v134\nv_accvgpr_write_b32 a187, v135\n" \
    "v_mfma_f32_32x32x16_bf16 v[16:31], v[132:135], v[128:131], v[16:31]\nds_read_b128 v[80:83], v12 offset:3136\nds_read_b128 v[84:87], v12 offset:3152\n" \
    "ds_read_b128 v[88:91], v13 offset:1568\nv_accvgpr_read_b32 v112, a188\nv_accvgpr_read_b32 v113, a189\nv_accvgpr_read_b32 v114, a190\nv_accvgpr_read_b32 v115, a191\n" \
    "v_lshlrev_b32 v116, 16, v112\nv_and_b32 v117, 0xffff0000, v112\nv_lshlrev_b32 v118, 16, v113\nv_and_b32 v119, 0xffff0000, v113\nv_lshlrev_b32 v120, 16, v114\n" \
    "v_and_b32 v121, 0xffff0000, v114\nv_lshlrev_b32 v122, 16, v115\nv_and_b32 v123, 0xffff0000, v115\ns_waitcnt lgkmcnt(6)\nv_mul_f32 v116, v116, v96\n" \
    "v_mul_f32 v117, v117, v97\nv_mul_f32 v118, v118, v98\nv_mul_f32 v119, v119, v99\nv_mul_f32 v120, v120, v100\nv_mul_f32 v121, v121, v101\nv_mul_f32 v122, v122, v102\n" \
    "v_mul_f32 v123, v123, v103\nv_mfma_f32_32x32x16_bf16 v[32:47], a[188:191], v[104:107], v[32:47]\nv_cvt_pk_bf16_f32 v136, v116, v117\nv_cvt_pk_bf16_f32 v137, v118, v119\n" \
    "v_cvt_pk_bf16_f32 v138, v120, v121\nv_cvt_pk_bf16_f32 v139, v122, v123\nv_accvgpr_write_b32 a188, v136\nv_accvgpr_write_b32 a189, v137\nv_accvgpr_write_b32 a190, v138\n" \
    "v_accvgpr_write_b32 a191, v139\nv_mfma_f32_32x32x16_bf16 v[16:31], v[136:139], v[128:131], v[16:31]\nds_read_b128 v[96:99], v12 offset:3200\n" \
    "ds_read_b128 v[100:103], v12 offset:3216\nds_read_b128 v[104:107], v13 offset:1600\nv_accvgpr_read_b32 v112, a192\nv_accvgpr_read_b32 v113, a193\n" \
    "v_accvgpr_read_b32 v114, a194\nv_accvgpr_read_b32 v115, a195\nv_lshlrev_b32 v116, 16, v112\nv_and_b32 v117, 0xffff0000, v112\nv_lshlrev_b32 v118, 16, v113\n" \
    "v_and_b32 v119, 0xffff0000, v113\nv_lshlrev_b32 v120, 16, v114\nv_and_b32 v121, 0xffff0000, v114\nv_lshlrev_b32 v122, 16, v115\nv_and_b32 v123, 0xffff0000, v115\n" \
    "s_waitcnt lgkmcnt(6)\nv_mul_f32 v116, v116, v64\nv_mul_f32 v117, v117, v65\nv_mul_f32 v118, v118, v66\nv_mul_f32 v119, v119, v67\nv_mul_f32 v120, v120, v68\n" \
    "v_mul_f32 v121, v121, v69\nv_mul_f32 v122, v122, v70\nv_mul_f32 v123, v123, v71\nv_mfma_f32_32x32x16_bf16 v[32:47], a[192:195], v[72:75], v[32:47]\n" \
    "v_cvt_pk_bf16_f32 v132, v116, v117\nv_cvt_pk_bf16_f32 v133, v118, v119\nv_cvt_pk_bf16_f32 v134, v120, v121\nv_cvt_pk_bf16_f32 v135, v122, v123\n" \
    "v_accvgpr_write_b32 a192, v132\nv_accvgpr_write_b32 a193, v133\nv_accvgpr_write_b32 a194, v134\nv_accvgpr_write_b32 a195, v135\n" \
    "v_mfma_f32_32x32x16_bf16 v[16:31], v[132:135], v[128:131], v[16:31]\nds_read_b128 v[64:67], v12 offset:3264\nds_read_b128 v[68:71], v12 offset:3280\n" \
    "ds_read_b128 v[72:75], v13 offset:1632\nv_accvgpr_read_b32 v112, a196\nv_accvgpr_read_b32 v113, a197\nv_accvgpr_read_b32 v114, a198\nv_accvgpr_read_b32 v115, a199\n" \
    "v_lshlrev_b32 v116, 16, v112\nv_and_b32 v117, 0xffff0000, v112\nv_lshlrev_b32 v118, 16, v113\nv_and_b32 v119, 0xffff0000, v113\nv_lshlrev_b32 v120, 16, v114\n" \
    "v_and_b32 v121, 0xffff0000, v114\nv_lshlrev_b32 v122, 16, v115\nv_and_b32 v123, 0xffff0000, v115\ns_waitcnt lgkmcnt(6)\nv_mul_f32 v116, v116, v80\n" \
    "v_mul_f32 v117, v117, v81\nv_mul_f32 v118, v118, v82\nv_mul_f32 v119, v119, v83\nv_mul_f32 v120, v120, v84\nv_mul_f32 v121, v121, v85\nv_mul_f32 v122, v122, v86\n" \
    "v_mul_f32 v123, v123, v87\nv_mfma_f32_32x32x16_bf16 v[32:47], a[196:199], v[88:91], v[32:47]\nv_cvt_pk_bf16_f32 v136, v116, v117\nv_cvt_pk_bf16_f32 v137, v118, v119\n" \
    "v_cvt_pk_bf16_f32 v138, v120, v121\nv_cvt_pk_bf16_f32 v139, v122, v123\nv_accvgpr_write_b32 a196, v136\nv_accvgpr_write_b32 a197, v137\nv_accvgpr_write_b32 a198, v138\n" \
    "v_accvgpr_write_b32 a199, v139\nv_mfma_f32_32x32x16_bf16 v[16:31], v[136:139], v[128:131], v[16:31]\nds_read_b128 v[80:83], v12 offset:3328\n" \
    "ds_read_b128 v[84:87], v12 offset:3344\nds_read_b128 v[88:91], v13 offset:1664\nv_accvgpr_read_b32 v112, a200\nv_accvgpr_read_b32 v113, a201\n" \
    "v_accvgpr_read_b32 v114, a202\nv_accvgpr_read_b32 v115, a203\nv_lshlrev_b32 v116, 16, v112\nv_and_b32 v117, 0xffff0000, v112\nv_lshlrev_b32 v118, 16, v113\n" \
    "v_and_b32 v119, 0xffff0000, v113\nv_lshlrev_b32 v120, 16, v114\nv_and_b32 v121, 0xffff0000, v114\nv_lshlrev_b32 v122, 16, v115\nv_and_b32 v123, 0xffff0000, v115\n" \
    "s_waitcnt lgkmcnt(6)\nv_mul_f32 v116, v116, v96\nv_mul_f32 v117, v117, v97\nv_mul_f32 v118, v118, v98\nv_mul_f32 v119, v119, v99\nv_mul_f32 v120, v120, v100\n" \
    "v_mul_f32 v121, v121, v101\nv_mul_f32 v122, v122, v102\nv_mul_f32 v123, v123, v103\nv_mfma_f32_32x32x16_bf16 v[32:47], a[200:203], v[104:107], v[32:47]\n" \
    "v_cvt_pk_bf16_f32 v132, v116, v117\nv_cvt_pk_bf16_f32 v133, v118, v119\nv_cvt_pk_bf16_f32 v134, v120, v121\nv_cvt_pk_bf16_f32 v135, v122, v123\n" \
    "v_accvgpr_write_b32 a200, v132\nv_accvgpr_write_b32 a201, v133\nv_accvgpr_write_b32 a202, v134\nv_accvgpr_write_b32 a203, v135\n" \
    "v_mfma_f32_32x32x16_bf16 v[16:31], v[132:135], v[128:131], v[16:31]\nds_read_b128 v[96:99], v12 offset:3392\nds_read_b128 v[100:103], v12 offset:3408\n" \
    "ds_read_b128 v[104:107], v13 offset:1696\nv_accvgpr_read_b32 v112, a204\nv_accvgpr_read_b32 v113, a205\nv_accvgpr_read_b32 v114, a206\nv_accvgpr_read_b32 v115, a207\n" \
    "v_lshlrev_b32 v116, 16, v112\nv_and_b32 v117, 0xffff0000, v112\nv_lshlrev_b32 v118, 16, v113\nv_and_b32 v119, 0xffff0000, v113\nv_lshlrev_b32 v120, 16, v114\n" \
    "v_and_b32 v121, 0xffff0000, v114\nv_lshlrev_b32 v122, 16, v115\nv_and_b32 v123, 0xffff0000, v115\ns_waitcnt lgkmcnt(6)\nv_mul_f32 v116, v116, v64\n" \
    "v_mul_f32 v117, v117, v65\nv_mul_f32 v118, v118, v66\nv_mul_f32 v119, v119, v67\nv_mul_f32 v120, v120, v68\nv_mul_f32 v121, v121, v69\nv_mul_f32 v122, v122, v70\n" \
    "v_mul_f32 v123, v123, v71\nv_mfma_f32_32x32x16_bf16 v[32:47], a[204:207], v[72:75], v[32:47]\nv_cvt_pk_bf16_f32 v136, v116, v117\nv_cvt_pk_bf16_f32 v137, v118, v119\n" \
    "v_cvt_pk_bf16_f32 v138, v120, v121\nv_cvt_pk_bf16_f32 v139, v122, v123\nv_accvgpr_write_b32 a204, v136\nv_accvgpr_write_b32 a205, v137\nv_accvgpr_write_b32 a206, v138\n" \
    "v_accvgpr_write_b32 a207, v139\nv_mfma_f32_32x32x16_bf16 v[16:31], v[136:139], v[128:131], v[16:31]\nds_read_b128 v[64:67], v12 offset:3456\n" \
    "ds_read_b128 v[68:71], v12 offset:3472\nds_read_b128 v[72:75], v13 offset:1728\nv_accvgpr_read_b32 v112, a208\nv_accvgpr_read_b32 v113, a209\n" \
    "v_accvgpr_read_b32 v114, a210\nv_accvgpr_read_b32 v115, a211\nv_lshlrev_b32 v116, 16, v112\nv_and_b32 v117, 0xffff0000, v112\nv_lshlrev_b32 v118, 16, v113\n" \
    "v_and_b32 v119, 0xffff0000, v113\nv_lshlrev_b32 v120, 16, v114\nv_and_b32 v121, 0xffff0000, v114\nv_lshlrev_b32 v122, 16, v115\nv_and_b32 v123, 0xffff0000, v115\n" \
    "s_waitcnt lgkmcnt(6)\nv_mul_f32 v116, v116, v80\nv_mul_f32 v117, v117, v81\nv_mul_f32 v118, v118, v82\nv_mul_f32 v119, v119, v83\nv_mul_f32 v120, v120, v84\n" \
    "v_mul_f32 v121, v121, v85\nv_mul_f32 v122, v122, v86\nv_mul_f32 v123, v123, v87\nv_mfma_f32_32x32x16_bf16 v[32:47], a[208:211], v[88:91], v[32:47]\n" \
    "v_cvt_pk_bf16_f32 v132, v116, v117\nv_cvt_pk_bf16_f32 v133, v118, v119\nv_cvt_pk_bf16_f32 v134, v120, v121\nv_cvt_pk_bf16_f32 v135, v122, v123\n" \
    "v_accvgpr_write_b32 a208, v132\nv_accvgpr_write_b32 a209, v133\nv_accvgpr_write_b32 a210, v134\nv_accvgpr_write_b32 a211, v135\n" \
    "v_mfma_f32_32x32x16_bf16 v[16:31], v[132:135], v[128:131], v[16:31]\nds_read_b128 v[80:83], v12 offset:3520\nds_read_b128 v[84:87], v12 offset:3536\n" \
    "ds_read_b128 v[88:91], v13 offset:1760\nv_accvgpr_read_b32 v112, a212\nv_accvgpr_read_b32 v113, a213\nv_accvgpr_read_b32 v114, a214\nv_accvgpr_read_b32 v115, a215\n" \
    "v_lshlrev_b32 v116, 16, v112\nv_and_b32 v117, 0xffff0000, v112\nv_lshlrev_b32 v118, 16, v113\nv_and_b32 v119, 0xffff0000, v113\nv_lshlrev_b32 v120, 16, v114\n" \
    "v_and_b32 v121, 0xffff0000, v114\nv_lshlrev_b32 v122, 16, v115\nv_and_b32 v123, 0xffff0000, v115\ns_waitcnt lgkmcnt(6)\nv_mul_f32 v116, v116, v96\n" \
    "v_mul_f32 v117, v117, v97\nv_mul_f32 v118, v118, v98\nv_mul_f32 v119, v119, v99\nv_mul_f32 v120, v120, v100\nv_mul_f32 v121, v121, v101\nv_mul_f32 v122, v122, v102\n" \
    "v_mul_f32 v123, v123, v103\nv_mfma_f32_32x32x16_bf16 v[32:47], a[212:215], v[104:107], v[32:47]\nv_cvt_pk_bf16_f32 v136, v116, v117\nv_cvt_pk_bf16_f32 v137, v118, v119\n" \
    "v_cvt_pk_bf16_f32 v138, v120, v121\nv_cvt_pk_bf16_f32 v139, v122, v123\nv_accvgpr_write_b32 a212, v136\nv_accvgpr_write_b32 a213, v137\nv_accvgpr_write_b32 a214, v138\n" \
    "v_accvgpr_write_b32 a215, v139\nv_mfma_f32_32x32x16_bf16 v[16:31], v[136:139], v[128:131], v[16:31]\nds_read_b128 v[96:99], v12 offset:3584\n" \
    "ds_read_b128 v[100:103], v12 offset:3600\nds_read_b128 v[104:107], v13 offset:1792\nv_accvgpr_read_b32 v112, a216\nv_accvgpr_read_b32 v113, a217\n" \
    "v_accvgpr_read_b32 v114, a218\nv_accvgpr_read_b32 v115, a219\nv_lshlrev_b32 v116, 16, v112\nv_and_b32 v117, 0xffff0000, v112\nv_lshlrev_b32 v118, 16, v113\n" \
    "v_and_b32 v119, 0xffff0000, v113\nv_lshlrev_b32 v120, 16, v114\nv_and_b32 v121, 0xffff0000, v114\nv_lshlrev_b32 v122, 16, v115\nv_and_b32 v123, 0xffff0000, v115\n" \
    "s_waitcnt lgkmcnt(6)\nv_mul_f32 v116, v116, v64\nv_mul_f32 v117, v117, v65\nv_mul_f32 v118, v118, v66\nv_mul_f32 v119, v119, v67\nv_mul_f32 v120, v120, v68\n" \
    "v_mul_f32 v121, v121, v69\nv_mul_f32 v122, v122, v70\nv_mul_f32 v123, v123, v71\nv_mfma_f32_32x32x16_bf16 v[32:47], a[216:219], v[72:75], v[32:47]\n" \
    "v_cvt_pk_bf16_f32 v132, v116, v117\nv_cvt_pk_bf16_f32 v133, v118, v119\nv_cvt_pk_bf16_f32 v134, v120, v121\nv_cvt_pk_bf16_f32 v135, v122, v123\n" \
    "v_accvgpr_write_b32 a216, v132\nv_accvgpr_write_b32 a217, v133\nv_accvgpr_write_b32 a218, v134\nv_accvgpr_write_b32 a219, v135\n" \
    "v_mfma_f32_32x32x16_bf16 v[16:31], v[132:135], v[128:131], v[16:31]\nds_read_b128 v[64:67], v12 offset:3648\nds_read_b128 v[68:71], v12 offset:3664\n" \
    "ds_read_b128 v[72:75], v13 offset:1824\nv_accvgpr_read_b32 v112, a220\nv_accvgpr_read_b32 v113, a221\nv_accvgpr_read_b32 v114, a222\nv_accvgpr_read_b32 v115, a223\n" \
    "v_lshlrev_b32 v116, 16, v112\nv_and_b32 v117, 0xffff0000, v112\nv_lshlrev_b32 v118, 16, v113\nv_and_b32 v119, 0xffff0000, v113\nv_lshlrev_b32 v120, 16, v114\n" \
    "v_and_b32 v121, 0xffff0000, v114\nv_lshlrev_b32 v122, 16, v115\nv_and_b32 v123, 0xffff0000, v115\ns_waitcnt lgkmcnt(6)\nv_mul_f32 v116, v116, v80\n" \
    "v_mul_f32 v117, v117, v81\nv_mul_f32 v118, v118, v82\nv_mul_f32 v119, v119, v83\nv_mul_f32 v120, v120, v84\nv_mul_f32 v121, v121, v85\nv_mul_f32 v122, v122, v86\n" \
    "v_mul_f32 v123, v123, v87\nv_mfma_f32_32x32x16_bf16 v[32:47], a[220:223], v[88:91], v[32:47]\nv_cvt_pk_bf16_f32 v136, v116, v117\nv_cvt_pk_bf16_f32 v137, v118, v119\n" \
    "v_cvt_pk_bf16_f32 v138, v120, v121\nv_cvt_pk_bf16_f32 v139, v122, v123\nv_accvgpr_write_b32 a220, v136\nv_accvgpr_write_b32 a221, v137\nv_accvgpr_write_b32 a222, v138\n" \
    "v_accvgpr_write_b32 a223, v139\nv_mfma_f32_32x32x16_bf16 v[16:31], v[136:139], v[128:131], v[16:31]\nds_read_b128 v[80:83], v12 offset:3712\n" \
    "ds_read_b128 v[84:87], v12 offset:3728\nds_read_b128 v[88:91], v13 offset:1856\nv_accvgpr_read_b32 v112, a224\nv_accvgpr_read_b32 v113, a225\n" \
    "v_accvgpr_read_b32 v114, a226\nv_accvgpr_read_b32 v115, a227\nv_lshlrev_b32 v116, 16, v112\nv_and_b32 v117, 0xffff0000, v112\nv_lshlrev_b32 v118, 16, v113\n" \
    "v_and_b32 v119, 0xffff0000, v113\nv_lshlrev_b32 v120, 16, v114\nv_and_b32 v121, 0xffff0000, v114\nv_lshlrev_b32 v122, 16, v115\nv_and_b32 v123, 0xffff0000, v115\n" \
    "s_waitcnt lgkmcnt(6)\nv_mul_f32 v116, v116, v96\nv_mul_f32 v117, v117, v97\nv_mul_f32 v118, v118, v98\nv_mul_f32 v119, v119, v99\nv_mul_f32 v120, v120, v100\n" \
    "v_mul_f32 v121, v121, v101\nv_mul_f32 v122, v122, v102\nv_mul_f32 v123, v123, v103\nv_mfma_f32_32x32x16_bf16 v[32:47], a[224:227], v[104:107], v[32:47]\n" \
    "v_cvt_pk_bf16_f32 v132, v116, v117\nv_cvt_pk_bf16_f32 v133, v118, v119\nv_cvt_pk_bf16_f32 v134, v120, v121\nv_cvt_pk_bf16_f32 v135, v122, v123\n" \
    "v_accvgpr_write_b32 a224, v132\nv_accvgpr_write_b32 a225, v133\nv_accvgpr_write_b32 a226, v134\nv_accvgpr_write_b32 a227, v135\n" \
    "v_mfma_f32_32x32x16_bf16 v[16:31], v[132:135], v[128:131], v[16:31]\nds_read_b128 v[96:99], v12 offset:3776\nds_read_b128 v[100:103], v12 offset:3792\n" \
    "ds_read_b128 v[104:107], v13 offset:1888\nv_accvgpr_read_b32 v112, a228\nv_accvgpr_read_b32 v113, a229\nv_accvgpr_read_b32 v114, a230\nv_accvgpr_read_b32 v115, a231\n" \
    "v_lshlrev_b32 v116, 16, v112\nv_and_b32 v117, 0xffff0000, v112\nv_lshlrev_b32 v118, 16, v113\nv_and_b32 v119, 0xffff0000, v113\nv_lshlrev_b32 v120, 16, v114\n" \
    "v_and_b32 v121, 0xffff0000, v114\nv_lshlrev_b32 v122, 16, v115\nv_and_b32 v123, 0xffff0000, v115\ns_waitcnt lgkmcnt(6)\nv_mul_f32 v116, v116, v64\n" \
    "v_mul_f32 v117, v117, v65\nv_mul_f32 v118, v118, v66\nv_mul_f32 v119, v119, v67\nv_mul_f32 v120, v120, v68\nv_mul_f32 v121, v121, v69\nv_mul_f32 v122, v122, v70\n" \
    "v_mul_f32 v123, v123, v71\nv_mfma_f32_32x32x16_bf16 v[32:47], a[228:231], v[72:75], v[32:47]\nv_cvt_pk_bf16_f32 v136, v116, v117\nv_cvt_pk_bf16_f32 v137, v118, v119\n" \
    "v_cvt_pk_bf16_f32 v138, v120, v121\nv_cvt_pk_bf16_f32 v139, v122, v123\nv_accvgpr_write_b32 a228, v136\nv_accvgpr_write_b32 a229, v137\nv_accvgpr_write_b32 a230, v138\n" \
    "v_accvgpr_write_b32 a231, v139\nv_mfma_f32_32x32x16_bf16 v[16:31], v[136:139], v[128:131], v[16:31]\nds_read_b128 v[64:67], v12 offset:3840\n" \
    "ds_read_b128 v[68:71], v12 offset:3856\nds_read_b128 v[72:75], v13 offset:1920\nv_accvgpr_read_b32 v112, a232\nv_accvgpr_read_b32 v113, a233\n" \
    "v_accvgpr_read_b32 v114, a234\nv_accvgpr_read_b32 v115, a235\nv_lshlrev_b32 v116, 16, v112\nv_and_b32 v117, 0xffff0000, v112\nv_lshlrev_b32 v118, 16, v113\n" \
    "v_and_b32 v119, 0xffff0000, v113\nv_lshlrev_b32 v120, 16, v114\nv_and_b32 v121, 0xffff0000, v114\nv_lshlrev_b32 v122, 16, v115\nv_and_b32 v123, 0xffff0000, v115\n" \
    "s_waitcnt lgkmcnt(6)\nv_mul_f32 v116, v116, v80\nv_mul_f32 v117, v117, v81\nv_mul_f32 v118, v118, v82\nv_mul_f32 v119, v119, v83\nv_mul_f32 v120, v120, v84\n" \
    "v_mul_f32 v121, v121, v85\nv_mul_f32 v122, v122, v86\nv_mul_f32 v123, v123, v87\nv_mfma_f32_32x32x16_bf16 v[32:47], a[232:235], v[88:91], v[32:47]\n" \
    "v_cvt_pk_bf16_f32 v132, v116, v117\nv_cvt_pk_bf16_f32 v133, v118, v119\nv_cvt_pk_bf16_f32 v134, v120, v121\nv_cvt_pk_bf16_f32 v135, v122, v123\n" \
    "v_accvgpr_write_b32 a232, v132\nv_accvgpr_write_b32 a233, v133\nv_accvgpr_write_b32 a234, v134\nv_accvgpr_write_b32 a235, v135\n" \
    "v_mfma_f32_32x32x16_bf16 v[16:31], v[132:135], v[128:131], v[16:31]\nds_read_b128 v[80:83], v12 offset:3904\nds_read_b128 v[84:87], v12 offset:3920\n" \
    "ds_read_b128 v[88:91], v13 offset:1952\nv_accvgpr_read_b32 v112, a236\nv_accvgpr_read_b32 v113, a237\nv_accvgpr_read_b32 v114, a238\nv_accvgpr_read_b32 v115, a239\n" \
    "v_lshlrev_b32 v116, 16, v112\nv_and_b32 v117, 0xffff0000, v112\nv_lshlrev_b32 v118, 16, v113\nv_and_b32 v119, 0xffff0000, v113\nv_lshlrev_b32 v120, 16, v114\n" \
    "v_and_b32 v121, 0xffff0000, v114\nv_lshlrev_b32 v122, 16, v115\nv_and_b32 v123, 0xffff0000, v115\ns_waitcnt lgkmcnt(6)\nv_mul_f32 v116, v116, v96\n" \
    "v_mul_f32 v117, v117, v97\nv_mul_f32 v118, v118, v98\nv_mul_f32 v119, v119, v99\nv_mul_f32 v120, v120, v100\nv_mul_f32 v121, v121, v101\nv_mul_f32 v122, v122, v102\n" \
    "v_mul_f32 v123, v123, v103\nv_mfma_f32_32x32x16_bf16 v[32:47], a[236:239], v[104:107], v[32:47]\nv_cvt_pk_bf16_f32 v136, v116, v117\nv_cvt_pk_bf16_f32 v137, v118, v119\n" \
    "v_cvt_pk_bf16_f32 v138, v120, v121\nv_cvt_pk_bf16_f32 v139, v122, v123\nv_accvgpr_write_b32 a236, v136\nv_accvgpr_write_b32 a237, v137\nv_accvgpr_write_b32 a238, v138\n" \
    "v_accvgpr_write_b32 a239, v139\nv_mfma_f32_32x32x16_bf16 v[16:31], v[136:139], v[128:131], v[16:31]\nds_read_b128 v[96:99], v12 offset:3968\n" \
    "ds_read_b128 v[100:103], v12 offset:3984\nds_read_b128 v[104:107], v13 offset:1984\nv_accvgpr_read_b32 v112, a240\nv_accvgpr_read_b32 v113, a241\n" \
    "v_accvgpr_read_b32 v114, a242\nv_accvgpr_read_b32 v115, a243\nv_lshlrev_b32 v116, 16, v112\nv_and_b32 v117, 0xffff0000, v112\nv_lshlrev_b32 v118, 16, v113\n" \
    "v_and_b32 v119, 0xffff0000, v113\nv_lshlrev_b32 v120, 16, v114\nv_and_b32 v121, 0xffff0000, v114\nv_lshlrev_b32 v122, 16, v115\nv_and_b32 v123, 0xffff0000, v115\n" \
    "s_waitcnt lgkmcnt(6)\nv_mul_f32 v116, v116, v64\nv_mul_f32 v117, v117, v65\nv_mul_f32 v118, v118, v66\nv_mul_f32 v119, v119, v67\nv_mul_f32 v120, v120, v68\n" \
    "v_mul_f32 v121, v121, v69\nv_mul_f32 v122, v122, v70\nv_mul_f32 v123, v123, v71\nv_mfma_f32_32x32x16_bf16 v[32:47], a[240:243], v[72:75], v[32:47]\n" \
    "v_cvt_pk_bf16_f32 v132, v116, v117\nv_cvt_pk_bf16_f32 v133, v118, v119\nv_cvt_pk_bf16_f32 v134, v120, v121\nv_cvt_pk_bf16_f32 v135, v122, v123\n" \
    "v_accvgpr_write_b32 a240, v132\nv_accvgpr_write_b32 a241, v133\nv_accvgpr_write_b32 a242, v134\nv_accvgpr_write_b32 a243, v135\n" \
    "v_mfma_f32_32x32x16_bf16 v[16:31], v[132:135], v[128:131], v[16:31]\nds_read_b128 v[64:67], v12 offset:4032\nds_read_b128 v[68:71], v12 offset:4048\n" \
    "ds_read_b128 v[72:75], v13 offset:2016\nv_accvgpr_read_b32 v112, a244\nv_accvgpr_read_b32 v113, a245\nv_accvgpr_read_b32 v114, a246\nv_accvgpr_read_b32 v115, a247\n" \
    "v_lshlrev_b32 v116, 16, v112\nv_and_b32 v117, 0xffff0000, v112\nv_lshlrev_b32 v118, 16, v113\nv_and_b32 v119, 0xffff0000, v113\nv_lshlrev_b32 v120, 16, v114\n" \
    "v_and_b32 v121, 0xffff0000, v114\nv_lshlrev_b32 v122, 16, v115\nv_and_b32 v123, 0xffff0000, v115\ns_waitcnt lgkmcnt(6)\nv_mul_f32 v116, v116, v80\n" \
    "v_mul_f32 v117, v117, v81\nv_mul_f32 v118, v118, v82\nv_mul_f32 v119, v119, v83\nv_mul_f32 v120, v120, v84\nv_mul_f32 v121, v121, v85\nv_mul_f32 v122, v122, v86\n" \
    "v_mul_f32 v123, v123, v87\nv_mfma_f32_32x32x16_bf16 v[32:47], a[244:247], v[88:91], v[32:47]\nv_cvt_pk_bf16_f32 v136, v116, v117\nv_cvt_pk_bf16_f32 v137, v118, v119\n" \
    "v_cvt_pk_bf16_f32 v138, v120, v121\nv_cvt_pk_bf16_f32 v139, v122, v123\nv_accvgpr_write_b32 a244, v136\nv_accvgpr_write_b32 a245, v137\nv_accvgpr_write_b32 a246, v138\n" \
    "v_accvgpr_write_b32 a247, v139\nv_mfma_f32_32x32x16_bf16 v[16:31], v[136:139], v[128:131], v[16:31]\nv_accvgpr_read_b32 v112, a248\nv_accvgpr_read_b32 v113, a249\n" \
    "v_accvgpr_read_b32 v114, a250\nv_accvgpr_read_b32 v115, a251\nv_lshlrev_b32 v116, 16, v112\nv_and_b32 v117, 0xffff0000, v112\nv_lshlrev_b32 v118, 16, v113\n" \
    "v_and_b32 v119, 0xffff0000, v113\nv_lshlrev_b32 v120, 16, v114\nv_and_b32 v121, 0xffff0000, v114\nv_lshlrev_b32 v122, 16, v115\nv_and_b32 v123, 0xffff0000, v115\n" \
    "s_waitcnt lgkmcnt(3)\nv_mul_f32 v116, v116, v96\nv_mul_f32 v117, v117, v97\nv_mul_f32 v118, v118, v98\nv_mul_f32 v119, v119, v99\nv_mul_f32 v120, v120, v100\n" \
    "v_mul_f32 v121, v121, v101\nv_mul_f32 v122, v122, v102\nv_mul_f32 v123, v123, v103\nv_mfma_f32_32x32x16_bf16 v[32:47], a[248:251], v[104:107], v[32:47]\n" \
    "v_cvt_pk_bf16_f32 v132, v116, v117\nv_cvt_pk_bf16_f32 v133, v118, v119\nv_cvt_pk_bf16_f32 v134, v120, v121\nv_cvt_pk_bf16_f32 v135, v122, v123\n" \
    "v_accvgpr_write_b32 a248, v132\nv_accvgpr_write_b32 a249, v133\nv_accvgpr_write_b32 a250, v134\nv_accvgpr_write_b32 a251, v135\n" \
    "v_mfma_f32_32x32x16_bf16 v[16:31], v[132:135], v[128:131], v[16:31]\nv_accvgpr_read_b32 v112, a252\nv_accvgpr_read_b32 v113, a253\nv_accvgpr_read_b32 v114, a254\n" \
    "v_accvgpr_read_b32 v115, a255\nv_lshlrev_b32 v116, 16, v112\nv_and_b32 v117, 0xffff0000, v112\nv_lshlrev_b32 v118, 16, v113\nv_and_b32 v119, 0xffff0000, v113\n" \
    "v_lshlrev_b32 v120, 16, v114\nv_and_b32 v121, 0xffff0000, v114\nv_lshlrev_b32 v122, 16, v115\nv_and_b32 v123, 0xffff0000, v115\ns_waitcnt lgkmcnt(0)\n" \
    "v_mul_f32 v116, v116, v64\nv_mul_f32 v117, v117, v65\nv_mul_f32 v118, v118, v66\nv_mul_f32 v119, v119, v67\nv_mul_f32 v120, v120, v68\nv_mul_f32 v121, v121, v69\n" \
    "v_mul_f32 v122, v122, v70\nv_mul_f32 v123, v123, v71\nv_mfma_f32_32x32x16_bf16 v[32:47], a[252:255], v[72:75], v[32:47]\nv_cvt_pk_bf16_f32 v136, v116, v117\n" \
    "v_cvt_pk_bf16_f32 v137, v118, v119\nv_cvt_pk_bf16_f32 v138, v120, v121\nv_cvt_pk_bf16_f32 v139, v122, v123\nv_accvgpr_write_b32 a252, v136\nv_accvgpr_write_b32 a253, v137\n" \
    "v_accvgpr_write_b32 a254, v138\nv_accvgpr_write_b32 a255, v139\nv_mfma_f32_32x32x16_bf16 v[16:31], v[136:139], v[128:131], v[16:31]\ns_mov_b32 s90, 0x7fff0000\n" \
    "s_mov_b32 s91, 0x7fff0000\ns_mov_b32 s92, 0x7fff0000\ns_mov_b32 s93, 0x7fff0000\ns_mov_b32 s80, 0\ns_mov_b32 s81, 0\ns_mov_b32 s68, 0\n.Lf2_%=:\ns_xor_b32 s85, s38, 512\n" \
    "s_and_b32 s69, s32, 63\ns_lshl_b32 s69, s69, 19\ns_add_u32 s82, s36, s69\ns_lshr_b32 s69, s82, 8\ns_and_b32 s69, s69, 0x2000\ns_xor_b32 s82, s82, s69\n" \
    "s_add_u32 s69, s32, 1\ns_and_b32 s69, s69, 63\ns_lshl_b32 s69, s69, 19\ns_add_u32 s83, s36, s69\ns_lshr_b32 s69, s83, 8\ns_and_b32 s69, s69, 0x2000\n" \
    "s_xor_b32 s83, s83, s69\ns_add_u32 s84, s37, s80\ns_cmp_eq_u32 s81, 1\ns_cbranch_scc1 .Lf6_%=\ns_add_u32 s77, s32, 2\ns_cmp_eq_u32 s81, 2\ns_cbranch_scc1 .Lf5_%=\n" \
    ".Lf3_%=:\nbuffer_load_dword v150, off, s[64:67], s38 sc1\ns_waitcnt vmcnt(0)\nv_and_b32 v112, 127, v150\nv_cmp_gt_u32 vcc, s77, v112\ns_nop 0\ns_cbranch_vccnz .Lf4_%=\n" \
    ".Lf5_%=:\nv_and_b32 v112, 127, v150\n.Lf11_%=:\ns_memrealtime s[72:73]\ns_waitcnt lgkmcnt(0)\ns_cmp_ge_u32 s77, 62\ns_cselect_b32 s69, s61, s34\ns_cmp_ge_u32 s77, 64\n" \
    "s_cselect_b32 s69, s60, s69\ns_sub_u32 s72, s72, s69\ns_lshl_b32 s72, s72, 8\nv_sub_u32 v113, s72, v150\nv_cmp_gt_i32 s[74:75], 0, v113\nv_cmp_eq_u32 vcc, s77, v112\n" \
    "s_nop 0\ns_and_b64 vcc, vcc, s[74:75]\ns_cbranch_scc1 .Lf11_%=\n.Lf12_%=:\nbuffer_load_dword v151, v2, s[64:67], s82 offen sc1\ns_add_u32 m0, s84, 0\n" \
    "s_add_u32 s69, s82, s48\nbuffer_load_dwordx4 v0, s[64:67], s69 offen offset:0 sc1 lds\nbuffer_load_dwordx4 v0, s[64:67], s69 offen offset:1024 sc1 lds\n" \
    "s_add_u32 m0, s84, 8192\ns_add_u32 s69, s82, s49\nbuffer_load_dwordx4 v0, s[64:67], s69 offen offset:0 sc1 lds\n" \
    "buffer_load_dwordx4 v0, s[64:67], s69 offen offset:1024 sc1 lds\ns_add_u32 m0, s84, 16384\ns_add_u32 s69, s82, s50\n" \
    "buffer_load_dwordx4 v0, s[64:67], s69 offen offset:0 sc1 lds\nbuffer_load_dwordx4 v0, s[64:67], s69 offen offset:1024 sc1 lds\n.Lf6_%=:\ns_waitcnt vmcnt(6)\n" \
    "v_cmp_eq_u32 vcc, -1, v151\ns_nop 0\ns_cbranch_vccnz .Lf13_%=\ns_cmp_lg_u32 s33, 0\ns_cbranch_scc1 .Lf15_%=\ns_memrealtime s[72:73]\ns_waitcnt lgkmcnt(0)\n" \
    "s_lshl_b32 s72, s72, 8\ns_add_u32 s72, s72, s32\ns_add_u32 s72, s72, 1\nv_mov_b32 v112, s72\ns_mov_b64 exec, 1\nbuffer_store_dword v112, off, s[64:67], s39 sc1\n" \
    "s_mov_b64 exec, -1\n.Lf15_%=:\ns_add_u32 s77, s32, 3\ns_add_u32 m0, s84, 24576\ns_add_u32 s69, s82, s51\nbuffer_load_dwordx4 v0, s[64:67], s69 offen offset:0 sc1 lds\n" \
    "buffer_load_dwordx4 v0, s[64:67], s69 offen offset:1024 sc1 lds\ns_waitcnt vmcnt(6)\ns_barrier\nds_read_b128 v[64:67], v1 offset:0\nds_read_b128 v[68:71], v1 offset:1024\n" \
    "ds_read_b128 v[72:75], v1 offset:2048\nds_read_b128 v[76:79], v1 offset:3072\ns_waitcnt lgkmcnt(3)\nv_mfma_f32_32x32x16_bf16 v[48:63], a[0:3], v[64:67], 0\n" \
    "ds_read_b128 v[80:83], v1 offset:4096\ns_add_u32 s69, s82, s41\nbuffer_load_dwordx4 v[96:99], v0, s[64:67], s69 offen offset:0 sc1\ns_waitcnt lgkmcnt(3)\n" \
    "v_mfma_f32_32x32x16_bf16 v[48:63], a[4:7], v[68:71], v[48:63]\nds_read_b128 v[84:87], v1 offset:5120\n" \
    "buffer_load_dwordx4 v[100:103], v0, s[64:67], s69 offen offset:1024 sc1\ns_waitcnt lgkmcnt(3)\nv_mfma_f32_32x32x16_bf16 v[48:63], a[8:11], v[72:75], v[48:63]\n" \
    "ds_read_b128 v[88:91], v1 offset:6144\nbuffer_load_dwordx4 v[104:107], v0, s[64:67], s69 offen offset:2048 sc1\ns_waitcnt lgkmcnt(3)\n" \
    "v_mfma_f32_32x32x16_bf16 v[48:63], a[12:15], v[76:79], v[48:63]\nds_read_b128 v[92:95], v1 offset:7168\n" \
    "buffer_load_dwordx4 v[108:111], v0, s[64:67], s69 offen offset:3072 sc1\ns_waitcnt vmcnt(8) lgkmcnt(3)\ns_barrier\n" \
    "v_mfma_f32_32x32x16_bf16 v[48:63], a[16:19], v[80:83], v[48:63]\nds_read_b128 v[64:67], v1 offset:8192\ns_add_u32 m0, s84, 32768\ns_add_u32 s69, s82, s52\n" \
    "buffer_load_dwordx4 v0, s[64:67], s69 offen offset:0 sc1 lds\ns_waitcnt lgkmcnt(3)\nv_mfma_f32_32x32x16_bf16 v[48:63], a[20:23], v[84:87], v[48:63]\n" \
    "ds_read_b128 v[68:71], v1 offset:9216\ns_waitcnt lgkmcnt(3)\nv_mfma_f32_32x32x16_bf16 v[48:63], a[24:27], v[88:91], v[48:63]\nds_read_b128 v[72:75], v1 offset:10240\n" \
    "buffer_load_dwordx4 v0, s[64:67], s69 offen offset:1024 sc1 lds\ns_waitcnt lgkmcnt(3)\nv_mfma_f32_32x32x16_bf16 v[48:63], a[28:31], v[92:95], v[48:63]\n" \
    "ds_read_b128 v[76:79], v1 offset:11264\ns_add_u32 m0, s84, 40960\ns_add_u32 s69, s82, s53\nbuffer_load_dwordx4 v0, s[64:67], s69 offen offset:0 sc1 lds\n" \
    "s_waitcnt lgkmcnt(3)\nv_mfma_f32_32x32x16_bf16 v[48:63], a[32:35], v[64:67], v[48:63]\nds_read_b128 v[80:83], v1 offset:12288\ns_waitcnt lgkmcnt(3)\n" \
    "v_mfma_f32_32x32x16_bf16 v[48:63], a[36:39], v[68:71], v[48:63]\nds_read_b128 v[84:87], v1 offset:13312\nbuffer_load_dwordx4 v0, s[64:67], s69 offen offset:1024 sc1 lds\n" \
    "s_waitcnt lgkmcnt(3)\nv_mfma_f32_32x32x16_bf16 v[48:63], a[40:43], v[72:75], v[48:63]\nds_read_b128 v[88:91], v1 offset:14336\ns_add_u32 m0, s84, 49152\n" \
    "s_add_u32 s69, s82, s54\nbuffer_load_dwordx4 v0, s[64:67], s69 offen offset:0 sc1 lds\ns_waitcnt lgkmcnt(3)\n" \
    "v_mfma_f32_32x32x16_bf16 v[48:63], a[44:47], v[76:79], v[48:63]\nds_read_b128 v[92:95], v1 offset:15360\ns_waitcnt vmcnt(9) lgkmcnt(3)\ns_barrier\n" \
    "v_mfma_f32_32x32x16_bf16 v[48:63], a[48:51], v[80:83], v[48:63]\nds_read_b128 v[64:67], v1 offset:16384\nbuffer_load_dwordx4 v0, s[64:67], s69 offen offset:1024 sc1 lds\n" \
    "s_waitcnt lgkmcnt(3)\nv_mfma_f32_32x32x16_bf16 v[48:63], a[52:55], v[84:87], v[48:63]\nds_read_b128 v[68:71], v1 offset:17408\ns_add_u32 m0, s84, 57344\n" \
    "s_add_u32 s69, s82, s55\nbuffer_load_dwordx4 v0, s[64:67], s69 offen offset:0 sc1 lds\ns_waitcnt lgkmcnt(3)\n" \
    "v_mfma_f32_32x32x16_bf16 v[48:63], a[56:59], v[88:91], v[48:63]\nds_read_b128 v[72:75], v1 offset:18432\ns_waitcnt lgkmcnt(3)\n" \
    "v_mfma_f32_32x32x16_bf16 v[48:63], a[60:63], v[92:95], v[48:63]\nds_read_b128 v[76:79], v1 offset:19456\nbuffer_load_dwordx4 v0, s[64:67], s69 offen offset:1024 sc1 lds\n" \
    "s_waitcnt lgkmcnt(3)\nv_mfma_f32_32x32x16_bf16 v[48:63], a[64:67], v[64:67], v[48:63]\nds_read_b128 v[80:83], v1 offset:20480\ns_and_b32 s69, s32, 63\n" \
    "s_lshl_b32 s69, s69, 19\ns_add_u32 s82, s40, s69\ns_lshr_b32 s69, s82, 8\ns_and_b32 s69, s69, 0x2000\ns_waitcnt lgkmcnt(3)\n" \
    "v_mfma_f32_32x32x16_bf16 v[48:63], a[68:71], v[68:71], v[48:63]\nds_read_b128 v[84:87], v1 offset:21504\ns_xor_b32 s82, s82, s69\ns_waitcnt vmcnt(8)\ns_waitcnt lgkmcnt(3)\n" \
    "v_readfirstlane_b32 s76, v150\nv_max3_u32 v116, v96, v97, v98\nv_max3_u32 v116, v116, v99, v100\nv_mfma_f32_32x32x16_bf16 v[48:63], a[72:75], v[72:75], v[48:63]\n" \
    "ds_read_b128 v[88:91], v1 offset:22528\ns_and_b32 s76, s76, 127\ns_waitcnt lgkmcnt(3)\nv_max3_u32 v116, v116, v101, v102\nv_max3_u32 v116, v116, v103, v104\n" \
    "v_max3_u32 v116, v116, v105, v106\nv_mfma_f32_32x32x16_bf16 v[48:63], a[76:79], v[76:79], v[48:63]\nds_read_b128 v[92:95], v1 offset:23552\ns_waitcnt lgkmcnt(3)\n" \
    "v_max3_u32 v116, v116, v107, v108\nv_max3_u32 v116, v116, v109, v110\nv_max_u32 v116, v116, v111\nv_mfma_f32_32x32x16_bf16 v[48:63], a[80:83], v[80:83], v[48:63]\n" \
    "ds_read_b128 v[64:67], v1 offset:24576\ns_waitcnt lgkmcnt(3)\nv_cmp_eq_u32 s[70:71], -1, v116\nv_sub_f32 v114, v96, v100\nv_add_f32 v96, v96, v100\n" \
    "v_mfma_f32_32x32x16_bf16 v[48:63], a[84:87], v[84:87], v[48:63]\nds_read_b128 v[68:71], v1 offset:25600\ns_waitcnt lgkmcnt(3)\nv_add_f32 v97, v97, v101\n" \
    "v_mul_f32 v114, v114, v114\nv_mul_f32 v96, 0.5, v96\nv_mfma_f32_32x32x16_bf16 v[48:63], a[88:91], v[88:91], v[48:63]\nds_read_b128 v[72:75], v1 offset:26624\n" \
    "s_waitcnt lgkmcnt(3)\nv_fmac_f32 v97, 0x41000000, v114\nv_sub_f32 v114, v104, v108\nv_add_f32 v104, v104, v108\n" \
    "v_mfma_f32_32x32x16_bf16 v[48:63], a[92:95], v[92:95], v[48:63]\nds_read_b128 v[76:79], v1 offset:27648\ns_waitcnt lgkmcnt(3)\nv_add_f32 v105, v105, v109\n" \
    "v_mul_f32 v114, v114, v114\nv_mul_f32 v104, 0.5, v104\nv_mfma_f32_32x32x16_bf16 v[48:63], a[96:99], v[64:67], v[48:63]\nds_read_b128 v[80:83], v1 offset:28672\n" \
    "s_waitcnt lgkmcnt(3)\nv_fmac_f32 v105, 0x41000000, v114\nv_sub_f32 v114, v96, v104\nv_add_f32 v96, v96, v104\n" \
    "v_mfma_f32_32x32x16_bf16 v[48:63], a[100:103], v[68:71], v[48:63]\nds_read_b128 v[84:87], v1 offset:29696\ns_waitcnt lgkmcnt(3)\nv_add_f32 v97, v97, v105\n" \
    "v_mul_f32 v114, v114, v114\nv_mul_f32 v96, 0.5, v96\nv_mfma_f32_32x32x16_bf16 v[48:63], a[104:107], v[72:75], v[48:63]\nds_read_b128 v[88:91], v1 offset:30720\n" \
    "s_waitcnt lgkmcnt(3)\nv_fmac_f32 v97, 0x41800000, v114\nv_sub_f32 v114, v98, v102\nv_add_f32 v98, v98, v102\n" \
    "v_mfma_f32_32x32x16_bf16 v[48:63], a[108:111], v[76:79], v[48:63]\nds_read_b128 v[92:95], v1 offset:31744\ns_waitcnt vmcnt(4) lgkmcnt(3)\ns_barrier\n" \
    "v_add_f32 v99, v99, v103\nv_mul_f32 v114, v114, v114\nv_mul_f32 v98, 0.5, v98\nv_mfma_f32_32x32x16_bf16 v[48:63], a[112:115], v[80:83], v[48:63]\n" \
    "ds_read_b128 v[64:67], v1 offset:32768\ns_waitcnt lgkmcnt(3)\nv_fmac_f32 v99, 0x41000000, v114\nv_sub_f32 v114, v106, v110\nv_add_f32 v106, v106, v110\n" \
    "v_mfma_f32_32x32x16_bf16 v[48:63], a[116:119], v[84:87], v[48:63]\nds_read_b128 v[68:71], v1 offset:33792\ns_waitcnt lgkmcnt(3)\nv_add_f32 v107, v107, v111\n" \
    "v_mul_f32 v114, v114, v114\nv_mul_f32 v106, 0.5, v106\nv_mfma_f32_32x32x16_bf16 v[48:63], a[120:123], v[88:91], v[48:63]\nds_read_b128 v[72:75], v1 offset:34816\n" \
    "s_waitcnt lgkmcnt(3)\nv_fmac_f32 v107, 0x41000000, v114\nv_sub_f32 v114, v98, v106\nv_add_f32 v98, v98, v106\n" \
    "v_mfma_f32_32x32x16_bf16 v[48:63], a[124:127], v[92:95], v[48:63]\nds_read_b128 v[76:79], v1 offset:35840\ns_waitcnt lgkmcnt(3)\nv_add_f32 v99, v99, v107\n" \
    "v_mul_f32 v114, v114, v114\nv_mul_f32 v98, 0.5, v98\nv_mfma_f32_32x32x16_bf16 v[48:63], a[128:131], v[64:67], v[48:63]\nds_read_b128 v[80:83], v1 offset:36864\n" \
    "s_waitcnt lgkmcnt(3)\nv_fmac_f32 v99, 0x41800000, v114\nv_mov_b64 v[100:101], v[96:97]\nv_mov_b64 v[102:103], v[98:99]\n" \
    "v_mfma_f32_32x32x16_bf16 v[48:63], a[132:135], v[68:71], v[48:63]\nds_read_b128 v[84:87], v1 offset:37888\ns_waitcnt lgkmcnt(3)\nv_permlane16_swap_b32 v96, v100\n" \
    "v_permlane16_swap_b32 v97, v101\nv_mfma_f32_32x32x16_bf16 v[48:63], a[136:139], v[72:75], v[48:63]\nds_read_b128 v[88:91], v1 offset:38912\ns_waitcnt lgkmcnt(3)\n" \
    "v_permlane16_swap_b32 v98, v102\nv_permlane16_swap_b32 v99, v103\nv_mfma_f32_32x32x16_bf16 v[48:63], a[140:143], v[76:79], v[48:63]\n" \
    "ds_read_b128 v[92:95], v1 offset:39936\ns_waitcnt lgkmcnt(3)\nv_sub_f32 v114, v96, v100\nv_add_f32 v96, v96, v100\nv_add_f32 v97, v97, v101\n" \
    "v_mfma_f32_32x32x16_bf16 v[48:63], a[144:147], v[80:83], v[48:63]\nds_read_b128 v[64:67], v1 offset:40960\ns_waitcnt lgkmcnt(3)\nv_mul_f32 v114, v114, v114\n" \
    "v_mul_f32 v96, 0.5, v96\nv_fmac_f32 v97, 0x42000000, v114\nv_mfma_f32_32x32x16_bf16 v[48:63], a[148:151], v[84:87], v[48:63]\nds_read_b128 v[68:71], v1 offset:41984\n" \
    "s_waitcnt lgkmcnt(3)\nv_sub_f32 v114, v98, v102\nv_add_f32 v98, v98, v102\nv_add_f32 v99, v99, v103\nv_mfma_f32_32x32x16_bf16 v[48:63], a[152:155], v[88:91], v[48:63]\n" \
    "ds_read_b128 v[72:75], v1 offset:43008\ns_waitcnt lgkmcnt(3)\nv_mul_f32 v114, v114, v114\nv_mul_f32 v98, 0.5, v98\nv_fmac_f32 v99, 0x42000000, v114\n" \
    "v_mfma_f32_32x32x16_bf16 v[48:63], a[156:159], v[92:95], v[48:63]\nds_read_b128 v[76:79], v1 offset:44032\ns_waitcnt lgkmcnt(3)\nv_mov_b64 v[100:101], v[96:97]\n" \
    "v_mov_b64 v[102:103], v[98:99]\nv_mfma_f32_32x32x16_bf16 v[48:63], a[160:163], v[64:67], v[48:63]\nds_read_b128 v[80:83], v1 offset:45056\ns_waitcnt lgkmcnt(3)\n" \
    "v_permlane32_swap_b32 v96, v100\nv_permlane32_swap_b32 v97, v101\nv_mfma_f32_32x32x16_bf16 v[48:63], a[164:167], v[68:71], v[48:63]\n" \
    "ds_read_b128 v[84:87], v1 offset:46080\ns_waitcnt lgkmcnt(3)\nv_permlane32_swap_b32 v98, v102\nv_permlane32_swap_b32 v99, v103\n" \
    "v_mfma_f32_32x32x16_bf16 v[48:63], a[168:171], v[72:75], v[48:63]\nds_read_b128 v[88:91], v1 offset:47104\ns_waitcnt lgkmcnt(3)\nv_sub_f32 v114, v96, v100\n" \
    "v_add_f32 v96, v96, v100\nv_add_f32 v97, v97, v101\nv_mfma_f32_32x32x16_bf16 v[48:63], a[172:175], v[76:79], v[48:63]\nds_read_b128 v[92:95], v1 offset:48128\n" \
    "s_waitcnt vmcnt(0) lgkmcnt(3)\ns_barrier\nv_mul_f32 v114, v114, v114\nv_mul_f32 v96, 0.5, v96\nv_fmac_f32 v97, 0x42800000, v114\n" \
    "v_mfma_f32_32x32x16_bf16 v[48:63], a[176:179], v[80:83], v[48:63]\nds_read_b128 v[64:67], v1 offset:49152\ns_waitcnt lgkmcnt(3)\nv_sub_f32 v114, v98, v102\n" \
    "v_add_f32 v98, v98, v102\nv_add_f32 v99, v99, v103\nv_mfma_f32_32x32x16_bf16 v[48:63], a[180:183], v[84:87], v[48:63]\nds_read_b128 v[68:71], v1 offset:50176\n" \
    "s_waitcnt lgkmcnt(3)\nv_mul_f32 v114, v114, v114\nv_mul_f32 v98, 0.5, v98\nv_fmac_f32 v99, 0x42800000, v114\n" \
    "v_mfma_f32_32x32x16_bf16 v[48:63], a[184:187], v[88:91], v[48:63]\nds_read_b128 v[72:75], v1 offset:51200\ns_cmp_gt_u32 s76, s77\ns_cbranch_scc1 .Lf16_%=\n" \
    "buffer_load_dword v150, off, s[64:67], s85 sc1\n.Lf16_%=:\ns_cmp_lg_u64 s[70:71], 0\ns_cselect_b32 s78, -1, 0\ns_waitcnt lgkmcnt(3)\nv_min_u32 v96, -2, v96\n" \
    "v_min_u32 v98, -2, v98\nv_or_b32 v96, s78, v96\nv_mfma_f32_32x32x16_bf16 v[48:63], a[188:191], v[92:95], v[48:63]\nds_read_b128 v[76:79], v1 offset:52224\n" \
    "s_waitcnt lgkmcnt(3)\nv_or_b32 v98, s78, v98\nv_mfma_f32_32x32x16_bf16 v[48:63], a[192:195], v[64:67], v[48:63]\nds_read_b128 v[80:83], v1 offset:53248\n" \
    "ds_write_b128 v3, v[96:99]\ns_waitcnt lgkmcnt(4)\nv_mfma_f32_32x32x16_bf16 v[48:63], a[196:199], v[68:71], v[48:63]\nds_read_b128 v[84:87], v1 offset:54272\n" \
    "s_waitcnt lgkmcnt(4)\nv_mfma_f32_32x32x16_bf16 v[48:63], a[200:203], v[72:75], v[48:63]\nds_read_b128 v[88:91], v1 offset:55296\ns_waitcnt lgkmcnt(2)\ns_barrier\n" \
    "ds_read_b64 v[96:97], v4 offset:0\nds_read_b64 v[98:99], v4 offset:256\nds_read_b64 v[100:101], v4 offset:512\n" \
    "v_mfma_f32_32x32x16_bf16 v[48:63], a[204:207], v[76:79], v[48:63]\nds_read_b128 v[92:95], v1 offset:56320\nds_read_b64 v[102:103], v4 offset:768\n" \
    "v_mfma_f32_32x32x16_bf16 v[48:63], a[208:211], v[80:83], v[48:63]\nds_read_b128 v[64:67], v1 offset:57344\ns_waitcnt lgkmcnt(7)\n" \
    "v_mfma_f32_32x32x16_bf16 v[48:63], a[212:215], v[84:87], v[48:63]\nds_read_b128 v[68:71], v1 offset:58368\ns_waitcnt lgkmcnt(2)\nv_max3_u32 v116, v96, v98, v100\n" \
    "v_max_u32 v116, v116, v102\nv_cmp_eq_u32 s[70:71], -1, v116\nv_mfma_f32_32x32x16_bf16 v[48:63], a[216:219], v[88:91], v[48:63]\nds_read_b128 v[72:75], v1 offset:59392\n" \
    "v_sub_f32 v114, v96, v98\nv_add_f32 v96, v96, v98\nv_add_f32 v97, v97, v99\nv_mfma_f32_32x32x16_bf16 v[48:63], a[220:223], v[92:95], v[48:63]\n" \
    "ds_read_b128 v[76:79], v1 offset:60416\ns_waitcnt lgkmcnt(3)\nv_mul_f32 v114, v114, v114\nv_mul_f32 v96, 0.5, v96\nv_fmac_f32 v97, 0x43000000, v114\n" \
    "v_mfma_f32_32x32x16_bf16 v[48:63], a[224:227], v[64:67], v[48:63]\nds_read_b128 v[80:83], v1 offset:61440\ns_waitcnt lgkmcnt(3)\nv_sub_f32 v114, v100, v102\n" \
    "v_add_f32 v100, v100, v102\nv_add_f32 v101, v101, v103\nv_mfma_f32_32x32x16_bf16 v[48:63], a[228:231], v[68:71], v[48:63]\nds_read_b128 v[84:87], v1 offset:62464\n" \
    "s_waitcnt lgkmcnt(3)\nv_mul_f32 v114, v114, v114\nv_mul_f32 v100, 0.5, v100\nv_fmac_f32 v101, 0x43000000, v114\n" \
    "v_mfma_f32_32x32x16_bf16 v[48:63], a[232:235], v[72:75], v[48:63]\nds_read_b128 v[88:91], v1 offset:63488\ns_waitcnt vmcnt(0)\ns_waitcnt lgkmcnt(3)\n" \
    "v_and_b32 v112, 127, v150\nv_cmp_gt_u32 vcc, s77, v112\nv_sub_f32 v114, v96, v100\nv_mfma_f32_32x32x16_bf16 v[48:63], a[236:239], v[76:79], v[48:63]\n" \
    "ds_read_b128 v[92:95], v1 offset:64512\ns_cmp_eq_u64 vcc, 0\ns_cselect_b32 s81, 1, 0\ns_waitcnt lgkmcnt(3)\nv_add_f32 v96, v96, v100\nv_add_f32 v97, v97, v101\n" \
    "v_mul_f32 v114, v114, v114\nv_mfma_f32_32x32x16_bf16 v[48:63], a[240:243], v[80:83], v[48:63]\ns_waitcnt lgkmcnt(2)\nv_mul_f32 v96, 0.5, v96\n" \
    "v_fmac_f32 v97, 0x43800000, v114\nv_mul_f32 v98, 0x3a800000, v97\nv_mfma_f32_32x32x16_bf16 v[48:63], a[244:247], v[84:87], v[48:63]\ns_waitcnt lgkmcnt(0)\n" \
    "s_memrealtime s[72:73]\nv_cvt_pk_bf16_f32 v96, 0, v96\nv_cvt_pk_bf16_f32 v98, 0, v98\nv_add_f32 v98, 0x3727c5ac, v98\n" \
    "v_mfma_f32_32x32x16_bf16 v[48:63], a[248:251], v[88:91], v[48:63]\nv_cvt_pk_bf16_f32 v98, 0, v98\nv_sqrt_f32 v98, v98\n" \
    "v_mfma_f32_32x32x16_bf16 v[48:63], a[252:255], v[92:95], v[48:63]\ns_waitcnt lgkmcnt(0)\ns_cmp_ge_u32 s77, 62\ns_cselect_b32 s69, s61, s34\ns_cmp_ge_u32 s77, 64\n" \
    "s_cselect_b32 s69, s60, s69\ns_sub_u32 s72, s72, s69\ns_lshl_b32 s72, s72, 8\nv_sub_u32 v113, s72, v150\nv_cmp_gt_i32 s[74:75], 0, v113\nv_cmp_eq_u32 vcc, s77, v112\n" \
    "s_nop 0\ns_and_b64 vcc, vcc, s[74:75]\ns_cselect_b32 s69, 1, 0\ns_lshl_b32 s81, s81, s69\nv_cvt_pk_bf16_f32 v98, 0, v98\nv_rcp_f32 v98, v98\ns_cmp_lg_u64 s[70:71], 0\n" \
    "s_cbranch_scc1 .Lf9_%=\nv_cmp_u_f32 vcc, v48, v48\ns_nop 0\ns_cbranch_vccnz .Lf7_%=\n.Lf8_%=:\ns_cmp_eq_u32 s81, 1\ns_cbranch_scc1 .Lf17_%=\n" \
    "v_pk_fma_f32 v[48:49], v[16:17], v[96:97], v[48:49] op_sel_hi:[1,0,1] neg_lo:[0,1,0] neg_hi:[0,1,0]\n" \
    "v_pk_fma_f32 v[50:51], v[18:19], v[96:97], v[50:51] op_sel_hi:[1,0,1] neg_lo:[0,1,0] neg_hi:[0,1,0]\n" \
    "v_pk_fma_f32 v[52:53], v[20:21], v[96:97], v[52:53] op_sel_hi:[1,0,1] neg_lo:[0,1,0] neg_hi:[0,1,0]\n" \
    "v_pk_fma_f32 v[54:55], v[22:23], v[96:97], v[54:55] op_sel_hi:[1,0,1] neg_lo:[0,1,0] neg_hi:[0,1,0]\nv_pk_fma_f32 v[48:49], v[48:49], v[98:99], v[32:33] op_sel_hi:[1,0,1]\n" \
    "v_pk_fma_f32 v[50:51], v[50:51], v[98:99], v[34:35] op_sel_hi:[1,0,1]\nv_pk_fma_f32 v[52:53], v[52:53], v[98:99], v[36:37] op_sel_hi:[1,0,1]\n" \
    "v_pk_fma_f32 v[54:55], v[54:55], v[98:99], v[38:39] op_sel_hi:[1,0,1]\nv_pk_mul_f32 v[72:73], v[48:49], v[48:49]\nv_pk_mul_f32 v[74:75], v[50:51], v[50:51]\n" \
    "v_pk_mul_f32 v[76:77], v[52:53], v[52:53]\nv_pk_mul_f32 v[78:79], v[54:55], v[54:55]\nv_pk_fma_f32 v[72:73], v[72:73], v[6:7], v[8:9]\n" \
    "v_pk_fma_f32 v[74:75], v[74:75], v[6:7], v[8:9]\nv_pk_fma_f32 v[76:77], v[76:77], v[6:7], v[8:9]\nv_pk_fma_f32 v[78:79], v[78:79], v[6:7], v[8:9]\n" \
    "v_pk_mul_f32 v[72:73], v[72:73], v[48:49]\nv_pk_mul_f32 v[74:75], v[74:75], v[50:51]\nv_pk_mul_f32 v[76:77], v[76:77], v[52:53]\nv_pk_mul_f32 v[78:79], v[78:79], v[54:55]\n" \
    "v_exp_f32 v72, v72\nv_exp_f32 v73, v73\nv_exp_f32 v74, v74\nv_exp_f32 v75, v75\nv_exp_f32 v76, v76\nv_exp_f32 v77, v77\nv_exp_f32 v78, v78\nv_exp_f32 v79, v79\n" \
    "v_pk_add_f32 v[72:73], v[72:73], v[10:11]\nv_pk_add_f32 v[74:75], v[74:75], v[10:11]\nv_pk_add_f32 v[76:77], v[76:77], v[10:11]\nv_pk_add_f32 v[78:79], v[78:79], v[10:11]\n" \
    "v_rcp_f32 v72, v72\nv_rcp_f32 v73, v73\nv_rcp_f32 v74, v74\nv_rcp_f32 v75, v75\nv_rcp_f32 v76, v76\nv_rcp_f32 v77, v77\nv_rcp_f32 v78, v78\nv_rcp_f32 v79, v79\n" \
    "v_pk_mul_f32 v[72:73], v[72:73], v[48:49]\nv_pk_mul_f32 v[74:75], v[74:75], v[50:51]\nv_pk_mul_f32 v[76:77], v[76:77], v[52:53]\nv_pk_mul_f32 v[78:79], v[78:79], v[54:55]\n" \
    "v_cvt_pk_bf16_f32 v64, v72, v73\nv_cvt_pk_bf16_f32 v65, v74, v75\nv_cvt_pk_bf16_f32 v66, v76, v77\nv_cvt_pk_bf16_f32 v67, v78, v79\nv_min_u32 v64, -2, v64\n" \
    "v_min_u32 v65, -2, v65\nv_min_u32 v66, -2, v66\nv_min_u32 v67, -2, v67\nbuffer_store_dwordx4 v[64:67], v0, s[64:67], s82 offen offset:0 sc1\n" \
    "v_pk_fma_f32 v[56:57], v[24:25], v[96:97], v[56:57] op_sel_hi:[1,0,1] neg_lo:[0,1,0] neg_hi:[0,1,0]\n" \
    "v_pk_fma_f32 v[58:59], v[26:27], v[96:97], v[58:59] op_sel_hi:[1,0,1] neg_lo:[0,1,0] neg_hi:[0,1,0]\n" \
    "v_pk_fma_f32 v[60:61], v[28:29], v[96:97], v[60:61] op_sel_hi:[1,0,1] neg_lo:[0,1,0] neg_hi:[0,1,0]\n" \
    "v_pk_fma_f32 v[62:63], v[30:31], v[96:97], v[62:63] op_sel_hi:[1,0,1] neg_lo:[0,1,0] neg_hi:[0,1,0]\nv_pk_fma_f32 v[56:57], v[56:57], v[98:99], v[40:41] op_sel_hi:[1,0,1]\n" \
    "v_pk_fma_f32 v[58:59], v[58:59], v[98:99], v[42:43] op_sel_hi:[1,0,1]\nv_pk_fma_f32 v[60:61], v[60:61], v[98:99], v[44:45] op_sel_hi:[1,0,1]\n" \
    "v_pk_fma_f32 v[62:63], v[62:63], v[98:99], v[46:47] op_sel_hi:[1,0,1]\nv_pk_mul_f32 v[72:73], v[56:57], v[56:57]\nv_pk_mul_f32 v[74:75], v[58:59], v[58:59]\n" \
    "v_pk_mul_f32 v[76:77], v[60:61], v[60:61]\nv_pk_mul_f32 v[78:79], v[62:63], v[62:63]\nv_pk_fma_f32 v[72:73], v[72:73], v[6:7], v[8:9]\n" \
    "v_pk_fma_f32 v[74:75], v[74:75], v[6:7], v[8:9]\nv_pk_fma_f32 v[76:77], v[76:77], v[6:7], v[8:9]\nv_pk_fma_f32 v[78:79], v[78:79], v[6:7], v[8:9]\n" \
    "v_pk_mul_f32 v[72:73], v[72:73], v[56:57]\nv_pk_mul_f32 v[74:75], v[74:75], v[58:59]\nv_pk_mul_f32 v[76:77], v[76:77], v[60:61]\nv_pk_mul_f32 v[78:79], v[78:79], v[62:63]\n" \
    "v_exp_f32 v72, v72\nv_exp_f32 v73, v73\nv_exp_f32 v74, v74\nv_exp_f32 v75, v75\nv_exp_f32 v76, v76\nv_exp_f32 v77, v77\nv_exp_f32 v78, v78\nv_exp_f32 v79, v79\n" \
    "v_pk_add_f32 v[72:73], v[72:73], v[10:11]\nv_pk_add_f32 v[74:75], v[74:75], v[10:11]\nv_pk_add_f32 v[76:77], v[76:77], v[10:11]\nv_pk_add_f32 v[78:79], v[78:79], v[10:11]\n" \
    "v_rcp_f32 v72, v72\nv_rcp_f32 v73, v73\nv_rcp_f32 v74, v74\nv_rcp_f32 v75, v75\nv_rcp_f32 v76, v76\nv_rcp_f32 v77, v77\nv_rcp_f32 v78, v78\nv_rcp_f32 v79, v79\n" \
    "v_pk_mul_f32 v[72:73], v[72:73], v[56:57]\nv_pk_mul_f32 v[74:75], v[74:75], v[58:59]\nv_pk_mul_f32 v[76:77], v[76:77], v[60:61]\nv_pk_mul_f32 v[78:79], v[78:79], v[62:63]\n" \
    "v_cvt_pk_bf16_f32 v68, v72, v73\nv_cvt_pk_bf16_f32 v69, v74, v75\nv_cvt_pk_bf16_f32 v70, v76, v77\nv_cvt_pk_bf16_f32 v71, v78, v79\nv_min_u32 v68, -2, v68\n" \
    "v_min_u32 v69, -2, v69\nv_min_u32 v70, -2, v70\nv_min_u32 v71, -2, v71\nbuffer_store_dwordx4 v[68:71], v0, s[64:67], s82 offen offset:1024 sc1\n.Lf18_%=:\n" \
    "s_mov_b32 s38, s85\ns_xor_b32 s39, s39, 128\ns_xor_b32 s80, s80, 0x10000\nv_xor_b32 v1, 0x10000, v1\ns_mov_b32 s68, 0\ns_add_u32 s32, s32, 1\ns_cmp_lt_u32 s32, 64\n" \
    "s_cbranch_scc1 .Lf2_%=\ns_endpgm\n.Lf4_%=:\ns_sleep 8\ns_add_u32 s68, s68, 1\ns_and_b32 s69, s68, 63\ns_cbranch_scc1 .Lf3_%=\ns_cmp_gt_u32 s68, 0x400000\n" \
    "s_cbranch_scc1 .Lf1_%=\nbuffer_load_dword v253, off, s[64:67], 0 sc1\ns_waitcnt vmcnt(0)\nv_readfirstlane_b32 s69, v253\ns_nop 0\ns_cmp_eq_u32 s69, 1\n" \
    "s_cbranch_scc0 .Lf3_%=\ns_endpgm\n.Lf13_%=:\ns_cmp_ge_u32 s32, 61\ns_cbranch_scc1 .Lf14_%=\ns_sleep 10\n.Lf14_%=:\ns_sleep 3\n" \
    "buffer_load_dword v151, v2, s[64:67], s82 offen sc1\ns_waitcnt vmcnt(0)\nv_cmp_eq_u32 vcc, -1, v151\ns_nop 0\ns_cbranch_vccz .Lf12_%=\ns_add_u32 s68, s68, 1\n" \
    "s_and_b32 s69, s68, 63\ns_cbranch_scc1 .Lf13_%=\ns_cmp_gt_u32 s68, 0x400000\ns_cbranch_scc1 .Lf1_%=\nbuffer_load_dword v253, off, s[64:67], 0 sc1\ns_waitcnt vmcnt(0)\n" \
    "v_readfirstlane_b32 s69, v253\ns_nop 0\ns_cmp_eq_u32 s69, 1\ns_cbranch_scc0 .Lf13_%=\ns_endpgm\n.Lf17_%=:\ns_xor_b32 s84, s84, 0x10000\n" \
    "buffer_load_dword v151, v2, s[64:67], s83 offen sc1\nv_pk_fma_f32 v[48:49], v[16:17], v[96:97], v[48:49] op_sel_hi:[1,0,1] neg_lo:[0,1,0] neg_hi:[0,1,0]\n" \
    "v_pk_fma_f32 v[50:51], v[18:19], v[96:97], v[50:51] op_sel_hi:[1,0,1] neg_lo:[0,1,0] neg_hi:[0,1,0]\n" \
    "v_pk_fma_f32 v[52:53], v[20:21], v[96:97], v[52:53] op_sel_hi:[1,0,1] neg_lo:[0,1,0] neg_hi:[0,1,0]\n" \
    "v_pk_fma_f32 v[54:55], v[22:23], v[96:97], v[54:55] op_sel_hi:[1,0,1] neg_lo:[0,1,0] neg_hi:[0,1,0]\nv_pk_fma_f32 v[48:49], v[48:49], v[98:99], v[32:33] op_sel_hi:[1,0,1]\n" \
    "v_pk_fma_f32 v[50:51], v[50:51], v[98:99], v[34:35] op_sel_hi:[1,0,1]\nv_pk_fma_f32 v[52:53], v[52:53], v[98:99], v[36:37] op_sel_hi:[1,0,1]\n" \
    "v_pk_fma_f32 v[54:55], v[54:55], v[98:99], v[38:39] op_sel_hi:[1,0,1]\nv_pk_mul_f32 v[72:73], v[48:49], v[48:49]\nv_pk_mul_f32 v[74:75], v[50:51], v[50:51]\n" \
    "v_pk_mul_f32 v[76:77], v[52:53], v[52:53]\nv_pk_mul_f32 v[78:79], v[54:55], v[54:55]\ns_add_u32 m0, s84, 0\ns_add_u32 s69, s83, s48\n" \
    "buffer_load_dwordx4 v0, s[64:67], s69 offen offset:0 sc1 lds\nv_pk_fma_f32 v[72:73], v[72:73], v[6:7], v[8:9]\nv_pk_fma_f32 v[74:75], v[74:75], v[6:7], v[8:9]\n" \
    "v_pk_fma_f32 v[76:77], v[76:77], v[6:7], v[8:9]\nv_pk_fma_f32 v[78:79], v[78:79], v[6:7], v[8:9]\nv_pk_mul_f32 v[72:73], v[72:73], v[48:49]\n" \
    "v_pk_mul_f32 v[74:75], v[74:75], v[50:51]\nv_pk_mul_f32 v[76:77], v[76:77], v[52:53]\nv_pk_mul_f32 v[78:79], v[78:79], v[54:55]\nv_exp_f32 v72, v72\nv_exp_f32 v73, v73\n" \
    "v_exp_f32 v74, v74\nv_exp_f32 v75, v75\nbuffer_load_dwordx4 v0, s[64:67], s69 offen offset:1024 sc1 lds\nv_exp_f32 v76, v76\nv_exp_f32 v77, v77\nv_exp_f32 v78, v78\n" \
    "v_exp_f32 v79, v79\nv_pk_add_f32 v[72:73], v[72:73], v[10:11]\nv_pk_add_f32 v[74:75], v[74:75], v[10:11]\nv_pk_add_f32 v[76:77], v[76:77], v[10:11]\n" \
    "v_pk_add_f32 v[78:79], v[78:79], v[10:11]\nv_rcp_f32 v72, v72\nv_rcp_f32 v73, v73\nv_rcp_f32 v74, v74\nv_rcp_f32 v75, v75\ns_add_u32 m0, s84, 8192\n" \
    "s_add_u32 s69, s83, s49\nbuffer_load_dwordx4 v0, s[64:67], s69 offen offset:0 sc1 lds\nv_rcp_f32 v76, v76\nv_rcp_f32 v77, v77\nv_rcp_f32 v78, v78\nv_rcp_f32 v79, v79\n" \
    "v_pk_mul_f32 v[72:73], v[72:73], v[48:49]\nv_pk_mul_f32 v[74:75], v[74:75], v[50:51]\nv_pk_mul_f32 v[76:77], v[76:77], v[52:53]\nv_pk_mul_f32 v[78:79], v[78:79], v[54:55]\n" \
    "v_cvt_pk_bf16_f32 v64, v72, v73\nv_cvt_pk_bf16_f32 v65, v74, v75\nv_cvt_pk_bf16_f32 v66, v76, v77\nv_cvt_pk_bf16_f32 v67, v78, v79\n" \
    "buffer_load_dwordx4 v0, s[64:67], s69 offen offset:1024 sc1 lds\nv_min_u32 v64, -2, v64\nv_min_u32 v65, -2, v65\nv_min_u32 v66, -2, v66\nv_min_u32 v67, -2, v67\n" \
    "buffer_store_dwordx4 v[64:67], v0, s[64:67], s82 offen offset:0 sc1\nv_pk_fma_f32 v[56:57], v[24:25], v[96:97], v[56:57] op_sel_hi:[1,0,1] neg_lo:[0,1,0] neg_hi:[0,1,0]\n" \
    "v_pk_fma_f32 v[58:59], v[26:27], v[96:97], v[58:59] op_sel_hi:[1,0,1] neg_lo:[0,1,0] neg_hi:[0,1,0]\n" \
    "v_pk_fma_f32 v[60:61], v[28:29], v[96:97], v[60:61] op_sel_hi:[1,0,1] neg_lo:[0,1,0] neg_hi:[0,1,0]\n" \
    "v_pk_fma_f32 v[62:63], v[30:31], v[96:97], v[62:63] op_sel_hi:[1,0,1] neg_lo:[0,1,0] neg_hi:[0,1,0]\nv_pk_fma_f32 v[56:57], v[56:57], v[98:99], v[40:41] op_sel_hi:[1,0,1]\n" \
    "v_pk_fma_f32 v[58:59], v[58:59], v[98:99], v[42:43] op_sel_hi:[1,0,1]\nv_pk_fma_f32 v[60:61], v[60:61], v[98:99], v[44:45] op_sel_hi:[1,0,1]\ns_add_u32 m0, s84, 16384\n" \
    "s_add_u32 s69, s83, s50\nbuffer_load_dwordx4 v0, s[64:67], s69 offen offset:0 sc1 lds\nv_pk_fma_f32 v[62:63], v[62:63], v[98:99], v[46:47] op_sel_hi:[1,0,1]\n" \
    "v_pk_mul_f32 v[72:73], v[56:57], v[56:57]\nv_pk_mul_f32 v[74:75], v[58:59], v[58:59]\nv_pk_mul_f32 v[76:77], v[60:61], v[60:61]\nv_pk_mul_f32 v[78:79], v[62:63], v[62:63]\n" \
    "v_pk_fma_f32 v[72:73], v[72:73], v[6:7], v[8:9]\nv_pk_fma_f32 v[74:75], v[74:75], v[6:7], v[8:9]\nv_pk_fma_f32 v[76:77], v[76:77], v[6:7], v[8:9]\n" \
    "v_pk_fma_f32 v[78:79], v[78:79], v[6:7], v[8:9]\nv_pk_mul_f32 v[72:73], v[72:73], v[56:57]\nv_pk_mul_f32 v[74:75], v[74:75], v[58:59]\n" \
    "v_pk_mul_f32 v[76:77], v[76:77], v[60:61]\nbuffer_load_dwordx4 v0, s[64:67], s69 offen offset:1024 sc1 lds\nv_pk_mul_f32 v[78:79], v[78:79], v[62:63]\nv_exp_f32 v72, v72\n" \
    "v_exp_f32 v73, v73\nv_exp_f32 v74, v74\nv_exp_f32 v75, v75\nv_exp_f32 v76, v76\nv_exp_f32 v77, v77\nv_exp_f32 v78, v78\nv_exp_f32 v79, v79\n" \
    "v_pk_add_f32 v[72:73], v[72:73], v[10:11]\nv_pk_add_f32 v[74:75], v[74:75], v[10:11]\nv_pk_add_f32 v[76:77], v[76:77], v[10:11]\nv_pk_add_f32 v[78:79], v[78:79], v[10:11]\n" \
    "v_rcp_f32 v72, v72\nv_rcp_f32 v73, v73\nv_rcp_f32 v74, v74\nv_rcp_f32 v75, v75\nv_rcp_f32 v76, v76\nv_rcp_f32 v77, v77\nv_rcp_f32 v78, v78\nv_rcp_f32 v79, v79\n" \
    "v_pk_mul_f32 v[72:73], v[72:73], v[56:57]\nv_pk_mul_f32 v[74:75], v[74:75], v[58:59]\nv_pk_mul_f32 v[76:77], v[76:77], v[60:61]\nv_pk_mul_f32 v[78:79], v[78:79], v[62:63]\n" \
    "v_cvt_pk_bf16_f32 v68, v72, v73\nv_cvt_pk_bf16_f32 v69, v74, v75\nv_cvt_pk_bf16_f32 v70, v76, v77\nv_cvt_pk_bf16_f32 v71, v78, v79\nv_min_u32 v68, -2, v68\n" \
    "v_min_u32 v69, -2, v69\nv_min_u32 v70, -2, v70\nv_min_u32 v71, -2, v71\nbuffer_store_dwordx4 v[68:71], v0, s[64:67], s82 offen offset:1024 sc1\ns_branch .Lf18_%=\n" \
    ".Lf7_%=:\nv_mov_b32 v120, 0\nv_mov_b32 v121, v1\ns_mov_b32 s76, 16\n.Lf10_%=:\nds_read_b128 v[124:127], v121 offset:0\nds_read_b128 v[128:131], v121 offset:1024\n" \
    "ds_read_b128 v[132:135], v121 offset:2048\nds_read_b128 v[136:139], v121 offset:3072\nv_add_u32 v121, 0x1000, v121\ns_waitcnt lgkmcnt(0)\n" \
    "v_max3_u32 v120, v120, v124, v125\nv_max3_u32 v120, v120, v126, v127\nv_max3_u32 v120, v120, v128, v129\nv_max3_u32 v120, v120, v130, v131\n" \
    "v_max3_u32 v120, v120, v132, v133\nv_max3_u32 v120, v120, v134, v135\nv_max3_u32 v120, v120, v136, v137\nv_max3_u32 v120, v120, v138, v139\ns_sub_u32 s76, s76, 1\n" \
    "s_cmp_lg_u32 s76, 0\ns_cbranch_scc1 .Lf10_%=\nv_cmp_eq_u32 vcc, -1, v120\ns_nop 0\ns_cbranch_vccz .Lf8_%=\n.Lf9_%=:\ns_mov_b32 s81, 0\ns_barrier\ns_sleep 4\n" \
    "s_add_u32 s68, s68, 1\ns_and_b32 s69, s68, 63\ns_cbranch_scc1 .Lf2_%=\ns_cmp_gt_u32 s68, 0x400000\ns_cbranch_scc1 .Lf1_%=\nbuffer_load_dword v253, off, s[64:67], 0 sc1\n" \
    "s_waitcnt vmcnt(0)\nv_readfirstlane_b32 s69, v253\ns_nop 0\ns_cmp_eq_u32 s69, 1\ns_cbranch_scc0 .Lf2_%=\ns_endpgm\n.Lf1_%=:\nv_mov_b32 v253, 1\n" \
    "buffer_store_dword v253, off, s[64:67], 0 sc1\ns_endpgm\n"
#define ASM_F2 \
    "s_mov_b32 s90, 0x7fff0000\ns_mov_b32 s91, 0x7fff0000\ns_mov_b32 s92, 0x7fff0000\ns_mov_b32 s93, 0x7fff0000\ns_mov_b32 s80, 0\ns_mov_b32 s81, 0\ns_mov_b32 s68, 0\n.Lg2_%=:\n" \
    "s_xor_b32 s85, s38, 128\ns_and_b32 s69, s32, 63\ns_lshl_b32 s69, s69, 19\ns_add_u32 s82, s36, s69\ns_lshr_b32 s69, s82, 8\ns_and_b32 s69, s69, 0x2000\n" \
    "s_xor_b32 s82, s82, s69\ns_add_u32 s69, s32, 1\ns_and_b32 s69, s69, 63\ns_lshl_b32 s69, s69, 19\ns_add_u32 s83, s36, s69\ns_lshr_b32 s69, s83, 8\n" \
    "s_and_b32 s69, s69, 0x2000\ns_xor_b32 s83, s83, s69\ns_add_u32 s84, s37, s80\ns_cmp_eq_u32 s81, 1\ns_cbranch_scc1 .Lg6_%=\ns_add_u32 s77, s32, 1\ns_cmp_eq_u32 s81, 2\n" \
    "s_cbranch_scc1 .Lg5_%=\n.Lg3_%=:\nbuffer_load_dword v150, v3, s[64:67], s38 offen sc1\ns_waitcnt vmcnt(0)\nv_and_b32 v112, 127, v150\nv_cmp_gt_u32 vcc, s77, v112\ns_nop 0\n" \
    "s_cbranch_vccnz .Lg4_%=\n.Lg5_%=:\nv_and_b32 v112, 127, v150\n.Lg11_%=:\ns_memrealtime s[72:73]\ns_waitcnt lgkmcnt(0)\ns_cmp_ge_u32 s77, 62\ns_cselect_b32 s69, s61, s34\n" \
    "s_cmp_ge_u32 s77, 64\ns_cselect_b32 s69, s60, s69\ns_sub_u32 s72, s72, s69\ns_lshl_b32 s72, s72, 8\nv_sub_u32 v113, s72, v150\nv_cmp_gt_i32 s[74:75], 0, v113\n" \
    "v_cmp_eq_u32 vcc, s77, v112\ns_nop 0\ns_and_b64 vcc, vcc, s[74:75]\ns_cbranch_scc1 .Lg11_%=\n.Lg12_%=:\nbuffer_load_dword v151, v2, s[64:67], s82 offen sc1\n" \
    "s_add_u32 m0, s84, 0\ns_add_u32 s69, s82, s48\nbuffer_load_dwordx4 v0, s[64:67], s69 offen offset:0 sc1 lds\n" \
    "buffer_load_dwordx4 v0, s[64:67], s69 offen offset:1024 sc1 lds\ns_add_u32 m0, s84, 8192\ns_add_u32 s69, s82, s49\n" \
    "buffer_load_dwordx4 v0, s[64:67], s69 offen offset:0 sc1 lds\nbuffer_load_dwordx4 v0, s[64:67], s69 offen offset:1024 sc1 lds\ns_add_u32 m0, s84, 16384\n" \
    "s_add_u32 s69, s82, s50\nbuffer_load_dwordx4 v0, s[64:67], s69 offen offset:0 sc1 lds\nbuffer_load_dwordx4 v0, s[64:67], s69 offen offset:1024 sc1 lds\n.Lg6_%=:\n" \
    "s_waitcnt vmcnt(6)\nv_cmp_eq_u32 vcc, -1, v151\ns_nop 0\ns_cbranch_vccnz .Lg13_%=\ns_add_u32 s77, s32, 2\ns_add_u32 m0, s84, 24576\ns_add_u32 s69, s82, s51\n" \
    "buffer_load_dwordx4 v0, s[64:67], s69 offen offset:0 sc1 lds\nbuffer_load_dwordx4 v0, s[64:67], s69 offen offset:1024 sc1 lds\ns_waitcnt vmcnt(6)\ns_barrier\n" \
    "ds_read_b128 v[64:67], v1 offset:0\nds_read_b128 v[68:71], v1 offset:1024\nds_read_b128 v[72:75], v1 offset:2048\nds_read_b128 v[76:79], v1 offset:3072\n" \
    "s_waitcnt lgkmcnt(3)\nv_mfma_f32_32x32x16_bf16 v[48:63], a[0:3], v[64:67], 0\nds_read_b128 v[80:83], v1 offset:4096\ns_add_u32 m0, s84, 32768\ns_add_u32 s69, s82, s52\n" \
    "buffer_load_dwordx4 v0, s[64:67], s69 offen offset:0 sc1 lds\ns_waitcnt lgkmcnt(3)\nv_mfma_f32_32x32x16_bf16 v[48:63], a[4:7], v[68:71], v[48:63]\n" \
    "ds_read_b128 v[84:87], v1 offset:5120\ns_waitcnt lgkmcnt(3)\nv_mfma_f32_32x32x16_bf16 v[48:63], a[8:11], v[72:75], v[48:63]\nds_read_b128 v[88:91], v1 offset:6144\n" \
    "buffer_load_dwordx4 v0, s[64:67], s69 offen offset:1024 sc1 lds\ns_waitcnt lgkmcnt(3)\nv_mfma_f32_32x32x16_bf16 v[48:63], a[12:15], v[76:79], v[48:63]\n" \
    "ds_read_b128 v[92:95], v1 offset:7168\ns_waitcnt vmcnt(6) lgkmcnt(3)\ns_barrier\nv_mfma_f32_32x32x16_bf16 v[48:63], a[16:19], v[80:83], v[48:63]\n" \
    "ds_read_b128 v[64:67], v1 offset:8192\ns_add_u32 m0, s84, 40960\ns_add_u32 s69, s82, s53\nbuffer_load_dwordx4 v0, s[64:67], s69 offen offset:0 sc1 lds\n" \
    "s_waitcnt lgkmcnt(3)\nv_mfma_f32_32x32x16_bf16 v[48:63], a[20:23], v[84:87], v[48:63]\nds_read_b128 v[68:71], v1 offset:9216\ns_waitcnt lgkmcnt(3)\n" \
    "v_mfma_f32_32x32x16_bf16 v[48:63], a[24:27], v[88:91], v[48:63]\nds_read_b128 v[72:75], v1 offset:10240\nbuffer_load_dwordx4 v0, s[64:67], s69 offen offset:1024 sc1 lds\n" \
    "s_waitcnt lgkmcnt(3)\nv_mfma_f32_32x32x16_bf16 v[48:63], a[28:31], v[92:95], v[48:63]\nds_read_b128 v[76:79], v1 offset:11264\ns_waitcnt lgkmcnt(3)\n" \
    "v_mfma_f32_32x32x16_bf16 v[48:63], a[32:35], v[64:67], v[48:63]\nds_read_b128 v[80:83], v1 offset:12288\ns_add_u32 m0, s84, 49152\ns_add_u32 s69, s82, s54\n" \
    "buffer_load_dwordx4 v0, s[64:67], s69 offen offset:0 sc1 lds\ns_waitcnt lgkmcnt(3)\nv_mfma_f32_32x32x16_bf16 v[48:63], a[36:39], v[68:71], v[48:63]\n" \
    "ds_read_b128 v[84:87], v1 offset:13312\ns_waitcnt lgkmcnt(3)\nv_mfma_f32_32x32x16_bf16 v[48:63], a[40:43], v[72:75], v[48:63]\nds_read_b128 v[88:91], v1 offset:14336\n" \
    "buffer_load_dwordx4 v0, s[64:67], s69 offen offset:1024 sc1 lds\ns_waitcnt lgkmcnt(3)\nv_mfma_f32_32x32x16_bf16 v[48:63], a[44:47], v[76:79], v[48:63]\n" \
    "ds_read_b128 v[92:95], v1 offset:15360\ns_waitcnt vmcnt(6) lgkmcnt(3)\ns_barrier\nv_mfma_f32_32x32x16_bf16 v[48:63], a[48:51], v[80:83], v[48:63]\n" \
    "ds_read_b128 v[64:67], v1 offset:16384\ns_and_b32 s69, s32, 3\ns_lshl_b32 s69, s69, 19\ns_add_u32 s86, s56, s69\ns_lshr_b32 s69, s86, 8\ns_and_b32 s69, s69, 0x2000\n" \
    "s_waitcnt lgkmcnt(3)\nv_mfma_f32_32x32x16_bf16 v[48:63], a[52:55], v[84:87], v[48:63]\nds_read_b128 v[68:71], v1 offset:17408\ns_xor_b32 s86, s86, s69\n" \
    "s_and_b32 s69, s32, 3\ns_lshl_b32 s69, s69, 19\ns_add_u32 s87, s57, s69\ns_lshr_b32 s69, s87, 8\ns_waitcnt lgkmcnt(3)\n" \
    "v_mfma_f32_32x32x16_bf16 v[48:63], a[56:59], v[88:91], v[48:63]\nds_read_b128 v[72:75], v1 offset:18432\ns_and_b32 s69, s69, 0x2000\ns_xor_b32 s87, s87, s69\n" \
    "s_and_b32 s69, s32, 3\ns_lshl_b32 s69, s69, 19\ns_add_u32 s88, s58, s69\ns_waitcnt lgkmcnt(3)\nv_mfma_f32_32x32x16_bf16 v[48:63], a[60:63], v[92:95], v[48:63]\n" \
    "ds_read_b128 v[76:79], v1 offset:19456\ns_lshr_b32 s69, s88, 8\ns_and_b32 s69, s69, 0x2000\ns_xor_b32 s88, s88, s69\ns_and_b32 s69, s32, 3\ns_lshl_b32 s69, s69, 19\n" \
    "s_waitcnt lgkmcnt(3)\nv_mfma_f32_32x32x16_bf16 v[48:63], a[64:67], v[64:67], v[48:63]\nds_read_b128 v[80:83], v1 offset:20480\ns_add_u32 s89, s59, s69\n" \
    "s_lshr_b32 s69, s89, 8\ns_and_b32 s69, s69, 0x2000\ns_xor_b32 s89, s89, s69\ns_bfe_u32 s78, s32, 0x10002\ns_waitcnt lgkmcnt(3)\n" \
    "v_mfma_f32_32x32x16_bf16 v[48:63], a[68:71], v[68:71], v[48:63]\nds_read_b128 v[84:87], v1 offset:21504\ns_waitcnt lgkmcnt(3)\n" \
    "v_mfma_f32_32x32x16_bf16 v[48:63], a[72:75], v[72:75], v[48:63]\nds_read_b128 v[88:91], v1 offset:22528\nbuffer_load_dwordx2 v[108:109], v4, s[64:67], s93 offen sc1\n" \
    "s_waitcnt lgkmcnt(3)\nv_mfma_f32_32x32x16_bf16 v[48:63], a[76:79], v[76:79], v[48:63]\nds_read_b128 v[92:95], v1 offset:23552\n" \
    "buffer_load_dwordx4 v[96:99], v0, s[64:67], s90 offen sc1\ns_waitcnt lgkmcnt(3)\nv_mfma_f32_32x32x16_bf16 v[48:63], a[80:83], v[80:83], v[48:63]\n" \
    "ds_read_b128 v[64:67], v1 offset:24576\nbuffer_load_dwordx4 v[100:103], v0, s[64:67], s91 offen sc1\ns_waitcnt lgkmcnt(3)\n" \
    "v_mfma_f32_32x32x16_bf16 v[48:63], a[84:87], v[84:87], v[48:63]\nds_read_b128 v[68:71], v1 offset:25600\nbuffer_load_dwordx4 v[104:107], v0, s[64:67], s92 offen sc1\n" \
    "s_waitcnt lgkmcnt(3)\nv_mfma_f32_32x32x16_bf16 v[48:63], a[88:91], v[88:91], v[48:63]\nds_read_b128 v[72:75], v1 offset:26624\ns_waitcnt lgkmcnt(3)\n" \
    "v_mfma_f32_32x32x16_bf16 v[48:63], a[92:95], v[92:95], v[48:63]\nds_read_b128 v[76:79], v1 offset:27648\ns_waitcnt lgkmcnt(3)\n" \
    "v_mfma_f32_32x32x16_bf16 v[48:63], a[96:99], v[64:67], v[48:63]\nds_read_b128 v[80:83], v1 offset:28672\ns_waitcnt lgkmcnt(3)\n" \
    "v_mfma_f32_32x32x16_bf16 v[48:63], a[100:103], v[68:71], v[48:63]\nds_read_b128 v[84:87], v1 offset:29696\ns_waitcnt lgkmcnt(3)\n" \
    "v_mfma_f32_32x32x16_bf16 v[48:63], a[104:107], v[72:75], v[48:63]\nds_read_b128 v[88:91], v1 offset:30720\ns_waitcnt lgkmcnt(3)\n" \
    "v_mfma_f32_32x32x16_bf16 v[48:63], a[108:111], v[76:79], v[48:63]\nds_read_b128 v[92:95], v1 offset:31744\ns_waitcnt vmcnt(6) lgkmcnt(3)\ns_barrier\n" \
    "v_mfma_f32_32x32x16_bf16 v[48:63], a[112:115], v[80:83], v[48:63]\nds_read_b128 v[64:67], v1 offset:32768\ns_waitcnt lgkmcnt(3)\n" \
    "v_mfma_f32_32x32x16_bf16 v[48:63], a[116:119], v[84:87], v[48:63]\nds_read_b128 v[68:71], v1 offset:33792\ns_waitcnt lgkmcnt(3)\n" \
    "v_mfma_f32_32x32x16_bf16 v[48:63], a[120:123], v[88:91], v[48:63]\nds_read_b128 v[72:75], v1 offset:34816\ns_waitcnt lgkmcnt(3)\n" \
    "v_mfma_f32_32x32x16_bf16 v[48:63], a[124:127], v[92:95], v[48:63]\nds_read_b128 v[76:79], v1 offset:35840\ns_waitcnt lgkmcnt(3)\n" \
    "v_mfma_f32_32x32x16_bf16 v[48:63], a[128:131], v[64:67], v[48:63]\nds_read_b128 v[80:83], v1 offset:36864\nbuffer_load_dword v150, v3, s[64:67], s85 offen sc1\n" \
    "s_waitcnt lgkmcnt(3)\nv_mfma_f32_32x32x16_bf16 v[48:63], a[132:135], v[68:71], v[48:63]\nds_read_b128 v[84:87], v1 offset:37888\ns_waitcnt lgkmcnt(3)\n" \
    "v_mfma_f32_32x32x16_bf16 v[48:63], a[136:139], v[72:75], v[48:63]\nds_read_b128 v[88:91], v1 offset:38912\ns_waitcnt lgkmcnt(3)\n" \
    "v_mfma_f32_32x32x16_bf16 v[48:63], a[140:143], v[76:79], v[48:63]\nds_read_b128 v[92:95], v1 offset:39936\ns_waitcnt lgkmcnt(3)\n" \
    "v_mfma_f32_32x32x16_bf16 v[48:63], a[144:147], v[80:83], v[48:63]\nds_read_b128 v[64:67], v1 offset:40960\ns_waitcnt lgkmcnt(3)\n" \
    "v_mfma_f32_32x32x16_bf16 v[48:63], a[148:151], v[84:87], v[48:63]\nds_read_b128 v[68:71], v1 offset:41984\ns_waitcnt vmcnt(1)\ns_waitcnt lgkmcnt(3)\n" \
    "v_max_u32 v116, v108, v109\nv_cmp_eq_u32 s[70:71], -1, v116\nv_mfma_f32_32x32x16_bf16 v[48:63], a[152:155], v[88:91], v[48:63]\nds_read_b128 v[72:75], v1 offset:43008\n" \
    "s_waitcnt lgkmcnt(3)\nv_cndmask_b32 v116, 0, 1, s[70:71]\nv_xor_b32 v96, s79, v96\nv_xor_b32 v97, s79, v97\nv_xor_b32 v98, s79, v98\n" \
    "v_mfma_f32_32x32x16_bf16 v[48:63], a[156:159], v[92:95], v[48:63]\nds_read_b128 v[76:79], v1 offset:44032\ns_waitcnt lgkmcnt(3)\nv_xor_b32 v99, s79, v99\n" \
    "v_xor_b32 v100, s79, v100\nv_xor_b32 v101, s79, v101\nv_xor_b32 v102, s79, v102\nv_mfma_f32_32x32x16_bf16 v[48:63], a[160:163], v[64:67], v[48:63]\n" \
    "ds_read_b128 v[80:83], v1 offset:45056\ns_waitcnt lgkmcnt(3)\nv_xor_b32 v103, s79, v103\nv_xor_b32 v104, s79, v104\nv_xor_b32 v105, s79, v105\nv_xor_b32 v106, s79, v106\n" \
    "v_mfma_f32_32x32x16_bf16 v[48:63], a[164:167], v[68:71], v[48:63]\nds_read_b128 v[84:87], v1 offset:46080\ns_waitcnt lgkmcnt(3)\nv_xor_b32 v107, s79, v107\n" \
    "v_or3_b32 v116, v116, v96, v97\nv_or3_b32 v116, v116, v98, v99\nv_or3_b32 v116, v116, v100, v101\nv_mfma_f32_32x32x16_bf16 v[48:63], a[168:171], v[72:75], v[48:63]\n" \
    "ds_read_b128 v[88:91], v1 offset:47104\ns_waitcnt lgkmcnt(3)\nv_or3_b32 v116, v116, v102, v103\nv_or3_b32 v116, v116, v104, v105\nv_or3_b32 v116, v116, v106, v107\n" \
    "v_and_b32 v116, 1, v116\nv_mfma_f32_32x32x16_bf16 v[48:63], a[172:175], v[76:79], v[48:63]\nds_read_b128 v[92:95], v1 offset:48128\ns_waitcnt lgkmcnt(3)\ns_barrier\n" \
    "v_cmp_eq_u32 s[70:71], 1, v116\nv_add_f32 v96, v96, v16\nv_add_f32 v97, v97, v17\nv_add_f32 v98, v98, v18\n" \
    "v_mfma_f32_32x32x16_bf16 v[48:63], a[176:179], v[80:83], v[48:63]\nds_read_b128 v[64:67], v1 offset:49152\ns_waitcnt lgkmcnt(3)\nv_add_f32 v99, v99, v19\n" \
    "v_add_f32 v100, v100, v104\nv_add_f32 v101, v101, v105\nv_add_f32 v102, v102, v106\nv_mfma_f32_32x32x16_bf16 v[48:63], a[180:183], v[84:87], v[48:63]\n" \
    "ds_read_b128 v[68:71], v1 offset:50176\ns_waitcnt lgkmcnt(3)\nv_add_f32 v103, v103, v107\nv_add_f32 v96, v96, v100\nv_add_f32 v97, v97, v101\nv_add_f32 v98, v98, v102\n" \
    "v_mfma_f32_32x32x16_bf16 v[48:63], a[184:187], v[88:91], v[48:63]\nds_read_b128 v[72:75], v1 offset:51200\ns_waitcnt lgkmcnt(3)\nv_add_f32 v99, v99, v103\n" \
    "v_cvt_pk_bf16_f32 v96, 0, v96\nv_cvt_pk_bf16_f32 v97, 0, v97\nv_cvt_pk_bf16_f32 v98, 0, v98\nv_mfma_f32_32x32x16_bf16 v[48:63], a[188:191], v[92:95], v[48:63]\n" \
    "ds_read_b128 v[76:79], v1 offset:52224\ns_waitcnt lgkmcnt(3)\nv_cvt_pk_bf16_f32 v99, 0, v99\nv_add_f32 v96, v96, v8\nv_add_f32 v97, v97, v9\nv_add_f32 v98, v98, v10\n" \
    "v_mfma_f32_32x32x16_bf16 v[48:63], a[192:195], v[64:67], v[48:63]\nds_read_b128 v[80:83], v1 offset:53248\ns_waitcnt lgkmcnt(3)\nv_add_f32 v99, v99, v11\n" \
    "v_cvt_pk_bf16_f32 v96, 0, v96\nv_cvt_pk_bf16_f32 v97, 0, v97\nv_cvt_pk_bf16_f32 v98, 0, v98\nv_mfma_f32_32x32x16_bf16 v[48:63], a[196:199], v[68:71], v[48:63]\n" \
    "ds_read_b128 v[84:87], v1 offset:54272\ns_waitcnt lgkmcnt(3)\nv_cvt_pk_bf16_f32 v99, 0, v99\nv_mul_f32 v96, v96, v12\nv_mul_f32 v97, v97, v13\nv_mul_f32 v98, v98, v14\n" \
    "v_mfma_f32_32x32x16_bf16 v[48:63], a[200:203], v[72:75], v[48:63]\nds_read_b128 v[88:91], v1 offset:55296\ns_waitcnt vmcnt(0)\ns_waitcnt lgkmcnt(3)\n" \
    "v_and_b32 v112, 127, v150\nv_cmp_gt_u32 vcc, s77, v112\nv_mul_f32 v99, v99, v15\nv_cvt_pk_bf16_f32 v96, 0, v96\n" \
    "v_mfma_f32_32x32x16_bf16 v[48:63], a[204:207], v[76:79], v[48:63]\nds_read_b128 v[92:95], v1 offset:56320\ns_cmp_eq_u64 vcc, 0\ns_cselect_b32 s81, 1, 0\n" \
    "s_waitcnt lgkmcnt(3)\nv_cvt_pk_bf16_f32 v97, 0, v97\nv_cvt_pk_bf16_f32 v98, 0, v98\nv_cvt_pk_bf16_f32 v99, 0, v99\nv_lshlrev_b32 v100, 16, v108\n" \
    "v_mfma_f32_32x32x16_bf16 v[48:63], a[208:211], v[80:83], v[48:63]\ns_waitcnt lgkmcnt(2)\nv_and_b32 v101, 0xffff0000, v108\nv_lshlrev_b32 v102, 16, v109\n" \
    "v_and_b32 v103, 0xffff0000, v109\nv_add_f32 v96, v96, v100\nv_mfma_f32_32x32x16_bf16 v[48:63], a[212:215], v[84:87], v[48:63]\ns_waitcnt lgkmcnt(0)\n" \
    "s_memrealtime s[72:73]\nv_add_f32 v97, v97, v101\nv_add_f32 v98, v98, v102\nv_add_f32 v99, v99, v103\nv_cvt_pk_bf16_f32 v116, v96, v97\n" \
    "v_mfma_f32_32x32x16_bf16 v[48:63], a[216:219], v[88:91], v[48:63]\nv_cvt_pk_bf16_f32 v117, v98, v99\nv_mfma_f32_32x32x16_bf16 v[48:63], a[220:223], v[92:95], v[48:63]\n" \
    "s_waitcnt lgkmcnt(0)\ns_cmp_ge_u32 s77, 62\ns_cselect_b32 s69, s61, s34\ns_cmp_ge_u32 s77, 64\ns_cselect_b32 s69, s60, s69\ns_sub_u32 s72, s72, s69\n" \
    "s_lshl_b32 s72, s72, 8\nv_sub_u32 v113, s72, v150\nv_cmp_gt_i32 s[74:75], 0, v113\nv_cmp_eq_u32 vcc, s77, v112\ns_nop 0\ns_and_b64 vcc, vcc, s[74:75]\n" \
    "s_cselect_b32 s69, 1, 0\ns_lshl_b32 s81, s81, s69\ns_nop 7\ns_cmp_eq_u32 s90, 0x7fff0000\ns_cbranch_scc1 .Lg16_%=\ns_cmp_lg_u64 s[70:71], 0\ns_cbranch_scc1 .Lg17_%=\n" \
    ".Lg18_%=:\nglobal_store_dwordx2 v5, v[116:117], s[46:47]\nv_add_u32 v5, 0x10000, v5\ns_mov_b32 s90, 0x7fff0000\ns_mov_b32 s91, 0x7fff0000\ns_mov_b32 s92, 0x7fff0000\n" \
    "s_mov_b32 s93, 0x7fff0000\n.Lg16_%=:\ns_nop 1\nv_cmp_u_f32 vcc, v48, v48\ns_nop 0\ns_cbranch_vccnz .Lg7_%=\n.Lg8_%=:\nv_and_or_b32 v52, v52, -2, s78\n" \
    "v_and_or_b32 v53, v53, -2, s78\nv_and_or_b32 v54, v54, -2, s78\nv_and_or_b32 v55, v55, -2, s78\nbuffer_store_dwordx4 v[52:55], v0, s[64:67], s87 offen sc1\n" \
    "v_and_or_b32 v56, v56, -2, s78\nv_and_or_b32 v57, v57, -2, s78\nv_and_or_b32 v58, v58, -2, s78\nv_and_or_b32 v59, v59, -2, s78\n" \
    "buffer_store_dwordx4 v[56:59], v0, s[64:67], s88 offen sc1\nv_and_or_b32 v60, v60, -2, s78\nv_and_or_b32 v61, v61, -2, s78\nv_and_or_b32 v62, v62, -2, s78\n" \
    "v_and_or_b32 v63, v63, -2, s78\nbuffer_store_dwordx4 v[60:63], v0, s[64:67], s89 offen sc1\ns_cmp_lg_u32 s81, 1\ns_cbranch_scc1 .Lg21_%=\ns_xor_b32 s84, s84, 0x10000\n" \
    "buffer_load_dword v151, v2, s[64:67], s83 offen sc1\ns_add_u32 m0, s84, 0\ns_add_u32 s69, s83, s48\nbuffer_load_dwordx4 v0, s[64:67], s69 offen offset:0 sc1 lds\n" \
    "buffer_load_dwordx4 v0, s[64:67], s69 offen offset:1024 sc1 lds\ns_add_u32 m0, s84, 8192\ns_add_u32 s69, s83, s49\n" \
    "buffer_load_dwordx4 v0, s[64:67], s69 offen offset:0 sc1 lds\nbuffer_load_dwordx4 v0, s[64:67], s69 offen offset:1024 sc1 lds\ns_add_u32 m0, s84, 16384\n" \
    "s_add_u32 s69, s83, s50\nbuffer_load_dwordx4 v0, s[64:67], s69 offen offset:0 sc1 lds\nbuffer_load_dwordx4 v0, s[64:67], s69 offen offset:1024 sc1 lds\n.Lg21_%=:\n" \
    "v_mov_b64 v[16:17], v[48:49]\nv_mov_b64 v[18:19], v[50:51]\ns_add_u32 s90, s86, 0x1000\ns_add_u32 s91, s86, 0x4000\ns_add_u32 s92, s86, 0x5000\ns_mov_b32 s93, s82\n" \
    "s_mov_b32 s79, s78\ns_sub_u32 s69, s32, 1\ns_cmp_lt_u32 s69, 62\ns_cbranch_scc1 .Lg15_%=\ns_memrealtime s[72:73]\ns_waitcnt lgkmcnt(0)\ns_add_u32 s76, s72, s35\n.Lg22_%=:\n" \
    "s_memrealtime s[72:73]\ns_waitcnt lgkmcnt(0)\ns_sub_u32 s72, s72, s76\ns_cmp_lt_i32 s72, 0\ns_cbranch_scc1 .Lg22_%=\n.Lg19_%=:\n" \
    "buffer_load_dwordx2 v[108:109], v4, s[64:67], s93 offen sc1\nbuffer_load_dwordx4 v[96:99], v0, s[64:67], s90 offen sc1\n" \
    "buffer_load_dwordx4 v[100:103], v0, s[64:67], s91 offen sc1\nbuffer_load_dwordx4 v[104:107], v0, s[64:67], s92 offen sc1\ns_waitcnt vmcnt(0)\nv_max_u32 v116, v108, v109\n" \
    "v_cmp_eq_u32 s[70:71], -1, v116\ns_nop 1\nv_cndmask_b32 v116, 0, 1, s[70:71]\nv_xor_b32 v96, s79, v96\nv_xor_b32 v97, s79, v97\nv_xor_b32 v98, s79, v98\n" \
    "v_xor_b32 v99, s79, v99\nv_xor_b32 v100, s79, v100\nv_xor_b32 v101, s79, v101\nv_xor_b32 v102, s79, v102\nv_xor_b32 v103, s79, v103\nv_xor_b32 v104, s79, v104\n" \
    "v_xor_b32 v105, s79, v105\nv_xor_b32 v106, s79, v106\nv_xor_b32 v107, s79, v107\nv_or3_b32 v116, v116, v96, v97\nv_or3_b32 v116, v116, v98, v99\n" \
    "v_or3_b32 v116, v116, v100, v101\nv_or3_b32 v116, v116, v102, v103\nv_or3_b32 v116, v116, v104, v105\nv_or3_b32 v116, v116, v106, v107\nv_and_b32 v116, 1, v116\n" \
    "v_cmp_eq_u32 s[70:71], 1, v116\nv_pk_add_f32 v[96:97], v[96:97], v[16:17]\nv_pk_add_f32 v[98:99], v[98:99], v[18:19]\nv_pk_add_f32 v[100:101], v[100:101], v[104:105]\n" \
    "v_pk_add_f32 v[102:103], v[102:103], v[106:107]\nv_pk_add_f32 v[96:97], v[96:97], v[100:101]\nv_pk_add_f32 v[98:99], v[98:99], v[102:103]\nv_cvt_pk_bf16_f32 v96, 0, v96\n" \
    "v_cvt_pk_bf16_f32 v97, 0, v97\nv_cvt_pk_bf16_f32 v98, 0, v98\nv_cvt_pk_bf16_f32 v99, 0, v99\nv_pk_add_f32 v[96:97], v[96:97], v[8:9]\n" \
    "v_pk_add_f32 v[98:99], v[98:99], v[10:11]\nv_cvt_pk_bf16_f32 v96, 0, v96\nv_cvt_pk_bf16_f32 v97, 0, v97\nv_cvt_pk_bf16_f32 v98, 0, v98\nv_cvt_pk_bf16_f32 v99, 0, v99\n" \
    "v_pk_mul_f32 v[96:97], v[96:97], v[12:13]\nv_pk_mul_f32 v[98:99], v[98:99], v[14:15]\nv_cvt_pk_bf16_f32 v96, 0, v96\nv_cvt_pk_bf16_f32 v97, 0, v97\n" \
    "v_cvt_pk_bf16_f32 v98, 0, v98\nv_cvt_pk_bf16_f32 v99, 0, v99\nv_lshlrev_b32 v100, 16, v108\nv_and_b32 v101, 0xffff0000, v108\nv_lshlrev_b32 v102, 16, v109\n" \
    "v_and_b32 v103, 0xffff0000, v109\nv_pk_add_f32 v[96:97], v[96:97], v[100:101]\nv_pk_add_f32 v[98:99], v[98:99], v[102:103]\nv_cvt_pk_bf16_f32 v116, v96, v97\n" \
    "v_cvt_pk_bf16_f32 v117, v98, v99\ns_cmp_lg_u64 s[70:71], 0\ns_cbranch_scc1 .Lg20_%=\nglobal_store_dwordx2 v5, v[116:117], s[46:47]\nv_add_u32 v5, 0x10000, v5\n" \
    "s_mov_b32 s90, 0x7fff0000\ns_mov_b32 s91, 0x7fff0000\ns_mov_b32 s92, 0x7fff0000\ns_mov_b32 s93, 0x7fff0000\n.Lg15_%=:\ns_mov_b32 s38, s85\ns_xor_b32 s39, s39, 128\n" \
    "s_xor_b32 s80, s80, 0x10000\nv_xor_b32 v1, 0x10000, v1\ns_mov_b32 s68, 0\ns_add_u32 s32, s32, 1\ns_cmp_lt_u32 s32, 64\ns_cbranch_scc1 .Lg2_%=\ns_endpgm\n.Lg4_%=:\n" \
    "s_sleep 8\ns_add_u32 s68, s68, 1\ns_and_b32 s69, s68, 63\ns_cbranch_scc1 .Lg3_%=\ns_cmp_gt_u32 s68, 0x400000\ns_cbranch_scc1 .Lg1_%=\n" \
    "buffer_load_dword v253, off, s[64:67], 0 sc1\ns_waitcnt vmcnt(0)\nv_readfirstlane_b32 s69, v253\ns_nop 0\ns_cmp_eq_u32 s69, 1\ns_cbranch_scc0 .Lg3_%=\ns_endpgm\n.Lg13_%=:\n" \
    "s_cmp_ge_u32 s32, 61\ns_cbranch_scc1 .Lg14_%=\ns_sleep 10\n.Lg14_%=:\ns_sleep 3\nbuffer_load_dword v151, v2, s[64:67], s82 offen sc1\ns_waitcnt vmcnt(0)\n" \
    "v_cmp_eq_u32 vcc, -1, v151\ns_nop 0\ns_cbranch_vccz .Lg12_%=\ns_add_u32 s68, s68, 1\ns_and_b32 s69, s68, 63\ns_cbranch_scc1 .Lg13_%=\ns_cmp_gt_u32 s68, 0x400000\n" \
    "s_cbranch_scc1 .Lg1_%=\nbuffer_load_dword v253, off, s[64:67], 0 sc1\ns_waitcnt vmcnt(0)\nv_readfirstlane_b32 s69, v253\ns_nop 0\ns_cmp_eq_u32 s69, 1\n" \
    "s_cbranch_scc0 .Lg13_%=\ns_endpgm\n.Lg17_%=:\ns_sleep 4\nbuffer_load_dwordx2 v[108:109], v4, s[64:67], s93 offen sc1\n" \
    "buffer_load_dwordx4 v[96:99], v0, s[64:67], s90 offen sc1\nbuffer_load_dwordx4 v[100:103], v0, s[64:67], s91 offen sc1\n" \
    "buffer_load_dwordx4 v[104:107], v0, s[64:67], s92 offen sc1\ns_waitcnt vmcnt(0)\nv_max_u32 v116, v108, v109\nv_cmp_eq_u32 s[70:71], -1, v116\ns_nop 1\n" \
    "v_cndmask_b32 v116, 0, 1, s[70:71]\nv_xor_b32 v96, s79, v96\nv_xor_b32 v97, s79, v97\nv_xor_b32 v98, s79, v98\nv_xor_b32 v99, s79, v99\nv_xor_b32 v100, s79, v100\n" \
    "v_xor_b32 v101, s79, v101\nv_xor_b32 v102, s79, v102\nv_xor_b32 v103, s79, v103\nv_xor_b32 v104, s79, v104\nv_xor_b32 v105, s79, v105\nv_xor_b32 v106, s79, v106\n" \
    "v_xor_b32 v107, s79, v107\nv_or3_b32 v116, v116, v96, v97\nv_or3_b32 v116, v116, v98, v99\nv_or3_b32 v116, v116, v100, v101\nv_or3_b32 v116, v116, v102, v103\n" \
    "v_or3_b32 v116, v116, v104, v105\nv_or3_b32 v116, v116, v106, v107\nv_and_b32 v116, 1, v116\nv_cmp_eq_u32 s[70:71], 1, v116\nv_pk_add_f32 v[96:97], v[96:97], v[16:17]\n" \
    "v_pk_add_f32 v[98:99], v[98:99], v[18:19]\nv_pk_add_f32 v[100:101], v[100:101], v[104:105]\nv_pk_add_f32 v[102:103], v[102:103], v[106:107]\n" \
    "v_pk_add_f32 v[96:97], v[96:97], v[100:101]\nv_pk_add_f32 v[98:99], v[98:99], v[102:103]\nv_cvt_pk_bf16_f32 v96, 0, v96\nv_cvt_pk_bf16_f32 v97, 0, v97\n" \
    "v_cvt_pk_bf16_f32 v98, 0, v98\nv_cvt_pk_bf16_f32 v99, 0, v99\nv_pk_add_f32 v[96:97], v[96:97], v[8:9]\nv_pk_add_f32 v[98:99], v[98:99], v[10:11]\n" \
    "v_cvt_pk_bf16_f32 v96, 0, v96\nv_cvt_pk_bf16_f32 v97, 0, v97\nv_cvt_pk_bf16_f32 v98, 0, v98\nv_cvt_pk_bf16_f32 v99, 0, v99\nv_pk_mul_f32 v[96:97], v[96:97], v[12:13]\n" \
    "v_pk_mul_f32 v[98:99], v[98:99], v[14:15]\nv_cvt_pk_bf16_f32 v96, 0, v96\nv_cvt_pk_bf16_f32 v97, 0, v97\nv_cvt_pk_bf16_f32 v98, 0, v98\nv_cvt_pk_bf16_f32 v99, 0, v99\n" \
    "v_lshlrev_b32 v100, 16, v108\nv_and_b32 v101, 0xffff0000, v108\nv_lshlrev_b32 v102, 16, v109\nv_and_b32 v103, 0xffff0000, v109\n" \
    "v_pk_add_f32 v[96:97], v[96:97], v[100:101]\nv_pk_add_f32 v[98:99], v[98:99], v[102:103]\nv_cvt_pk_bf16_f32 v116, v96, v97\nv_cvt_pk_bf16_f32 v117, v98, v99\n" \
    "s_cmp_lg_u64 s[70:71], 0\ns_cbranch_scc0 .Lg18_%=\ns_add_u32 s68, s68, 1\ns_and_b32 s69, s68, 63\ns_cbranch_scc1 .Lg17_%=\ns_cmp_gt_u32 s68, 0x400000\n" \
    "s_cbranch_scc1 .Lg1_%=\nbuffer_load_dword v253, off, s[64:67], 0 sc1\ns_waitcnt vmcnt(0)\nv_readfirstlane_b32 s69, v253\ns_nop 0\ns_cmp_eq_u32 s69, 1\n" \
    "s_cbranch_scc0 .Lg17_%=\ns_endpgm\n.Lg20_%=:\ns_sleep 4\ns_add_u32 s68, s68, 1\ns_and_b32 s69, s68, 63\ns_cbranch_scc1 .Lg19_%=\ns_cmp_gt_u32 s68, 0x400000\n" \
    "s_cbranch_scc1 .Lg1_%=\nbuffer_load_dword v253, off, s[64:67], 0 sc1\ns_waitcnt vmcnt(0)\nv_readfirstlane_b32 s69, v253\ns_nop 0\ns_cmp_eq_u32 s69, 1\n" \
    "s_cbranch_scc0 .Lg19_%=\ns_endpgm\n.Lg7_%=:\nv_mov_b32 v120, 0\nv_mov_b32 v121, v1\ns_mov_b32 s76, 14\n.Lg10_%=:\nds_read_b128 v[124:127], v121 offset:0\n" \
    "ds_read_b128 v[128:131], v121 offset:1024\nds_read_b128 v[132:135], v121 offset:2048\nds_read_b128 v[136:139], v121 offset:3072\nv_add_u32 v121, 0x1000, v121\n" \
    "s_waitcnt lgkmcnt(0)\nv_max3_u32 v120, v120, v124, v125\nv_max3_u32 v120, v120, v126, v127\nv_max3_u32 v120, v120, v128, v129\nv_max3_u32 v120, v120, v130, v131\n" \
    "v_max3_u32 v120, v120, v132, v133\nv_max3_u32 v120, v120, v134, v135\nv_max3_u32 v120, v120, v136, v137\nv_max3_u32 v120, v120, v138, v139\ns_sub_u32 s76, s76, 1\n" \
    "s_cmp_lg_u32 s76, 0\ns_cbranch_scc1 .Lg10_%=\nv_cmp_eq_u32 vcc, -1, v120\ns_nop 0\ns_cbranch_vccz .Lg8_%=\n.Lg9_%=:\ns_mov_b32 s81, 0\ns_barrier\ns_sleep 4\n" \
    "s_add_u32 s68, s68, 1\ns_and_b32 s69, s68, 63\ns_cbranch_scc1 .Lg2_%=\ns_cmp_gt_u32 s68, 0x400000\ns_cbranch_scc1 .Lg1_%=\nbuffer_load_dword v253, off, s[64:67], 0 sc1\n" \
    "s_waitcnt vmcnt(0)\nv_readfirstlane_b32 s69, v253\ns_nop 0\ns_cmp_eq_u32 s69, 1\ns_cbranch_scc0 .Lg2_%=\ns_endpgm\n.Lg1_%=:\nv_mov_b32 v253, 1\n" \
    "buffer_store_dword v253, off, s[64:67], 0 sc1\ns_endpgm\n"
typedef unsigned v32u __attribute__((ext_vector_type(32)));
typedef unsigned v16u __attribute__((ext_vector_type(16)));
#define QSH 10
#define HX 0
template <bool isV>
__device__ __forceinline__ void s1_main(const Args& a, unsigned char* lds, const int xg, const int ii) {
    const int wave = a.pad0, head = xg * 4 + (ii >> 2), set = (ii >> 1) & 1;
    char* const ws = a.ws;
    unsigned* const ctl = (unsigned*)(ws + O_CTL);
    const unsigned pubb = isV ? O_UM + (wave & 1) * 262144 + head * 256 : O_EP + ((wave & 1) * 4 + (head >> 3)) * 32768;
    const unsigned psb = isV ? O_UX + (wave & 1) * MiB + (head >> 3) * 16384 + (head & 7) * 1024 : O_PS + (wave * 4 + (head >> 3)) * 16384;
    v16bf w[52], pend[12];
    v4u rX = mkrs(isV ? a.Wv : wave < 2 ? a.Wq : a.Wk, DM * HD * 2), rO = mkrs(a.Wo, DM * HD * 2);
    unsigned bo[4];
#pragma unroll
    for (int i = 0; i < 4; ++i) {
        const int lane = lane_id(), row = lane >> 3, ch = (lane & 7) ^ ((2 * (i & 1) + (row >> 2)) << 1);
        bo[i] = i < 2 ? (((isV ? 64 * (wave >> 1) : 0) + row) * HD + head * DH + 64 * (wave & 1) + 8 * ch) * 2 : ((head * DH + row) * DM + 256 * wave + 8 * ch) * 2;
    }
    asm volatile("s_nop 4" : "+s"(rX), "+s"(rO));
    const int kx = set << (isV ? 10 : QSH), hx = HX;
    unsigned go;
    {
        const int lane = lane_id(), l15 = lane & 15, gb = lane >> 4;
        auto bufp = [&](int u, int wv) { const int b = (u + 4) % 5; return lds + (b < 3 ? L_W + wv * 24576 + b * 8192 : L_H + wv * 16384 + (b - 3) * 8192); };
        auto issue = [&](auto uu) {
            constexpr int u = decltype(uu)::v, c = u;
            constexpr bool wo = isV && c >= 8;
            const unsigned d = (unsigned)(size_t)(LAS unsigned char*)bufp(u, wave);
#pragma unroll
            for (int r8 = 0; r8 < 8; ++r8)
                bdma<(NTW & (u < 4 ? 1 : u < 8 ? 2 : 64))>(wo ? rO : rX, wo ? bo[2 + (r8 & 1)] : bo[r8 & 1], wo ? ((64 * (c & 1) + 8 * r8) * DM + 64 * (((c - 8) >> 1) ^ hx)) * 2 : (((isV ? 128 : 64) * c + 8 * r8) ^ (kx >> 1)) * HD * 2, d + r8 * 1024);
        };
        const unsigned q = l15 >> 2, p = lane & 3;
        const unsigned row0 = 16 * (gb >> 1) + 4 * (gb & 1) + (q & 1) + 8 * (q >> 1);
        const unsigned sw = ((gb & 1) | ((q >> 1) << 1)) << 1;
        auto flush = [&](int j) {
            const v8u v = __builtin_bit_cast(v8u, pend[j]);
            unsigned char* d = lds + L_W + wave * 24576 + j * 2048 + lane * 16;
            *(LAS v4u*)d = (v4u){v[0], v[1], v[2], v[3]};
            *(LAS v4u*)(d + 1024) = (v4u){v[4], v[5], v[6], v[7]};
        };
        sfor<0, 16>([&](auto cc) {
            constexpr int u = decltype(cc)::v, c = u;
            asm volatile("s_waitcnt vmcnt(%0)\n\ts_barrier" :: "n"(u == 0 ? 32 : u < 13 ? 24 : 8 * (15 - u) + (u == 15)) : "memory");
            if constexpr (u == 14) asm volatile("s_nop 4\n\tglobal_load_dword %0, %1, %2 sc1" : "=&v"(go) : "v"(O_GO + blockIdx.x * 128), "s"(ws));
            if constexpr (u > 0 && u + 4 < 16) issue(IC<u + 4>{});
            if (u >= 12 && u < 15) { flush(4 * u - 48); flush(4 * u - 47); flush(4 * u - 46); flush(4 * u - 45); }
            v16bf t[4];
#pragma unroll
            for (int n = 0; n < 4; ++n) {
                constexpr bool pk = isV && c >= 8;
                const unsigned char* buf = bufp(u, pk ? wave : (n & 2) + (wave >> 1));
                const unsigned o = pk ? (32 * (n & 1) + 16 * (gb & 1) + 4 * (gb >> 1) + (q & 1)) * 128 + (((4 * (n >> 1) + 2 * (q >> 1) + (p >> 1)) ^ (gb & 2)) << 4) + (p & 1) * 8
                                      : row0 * 128 + (((4 * (wave & 1) + 2 * (n & 1) + (p >> 1)) ^ sw) << 4) + (p & 1) * 8;
                const unsigned o2 = pk ? (o ^ 64) + 8 * 128 : o + 32 * 128;
                const v2u r0 = tr_rd(buf, o), r1 = tr_rd(buf, o + 2 * 128), r2 = tr_rd(buf, o2), r3 = tr_rd(buf, o2 + 2 * 128);
                const v8u v = {r0.x, r0.y, r1.x, r1.y, r2.x, r2.y, r3.x, r3.y};
                t[n] = __builtin_bit_cast(v16bf, v);
            }
            asm volatile("s_waitcnt lgkmcnt(0)" ::: "memory");
            sfor<0, 4>([&](auto nn) {
                constexpr int n = decltype(nn)::v, j = lsl(isV, c, n), r = ridx(isV, c, n);
                if constexpr (j >= 0) { pend[j] = t[n]; asm volatile("" : "+v"(pend[j])); }
                else {
                    w[r] = t[n];
                    if constexpr (r < 32) asm volatile("" : "+a"(w[r])); else asm volatile("" : "+v"(w[r]));
                }
            });
        });
    }
    RSTAMP(74, 0);
    asm volatile("s_waitcnt vmcnt(0)\n\tv_readfirstlane_b32 %0, %1" : "=s"(go) : "v"(go));
    const unsigned pol = (go ? go : wait_go(ws)) & 1;
    lds_barrier();
    RSTAMP(74, 1);
    const unsigned lb0 = (unsigned)(size_t)(LAS unsigned char*)lds;
    const unsigned lane = lane_id(), l15 = lane & 15, g = lane >> 4, q = l15 >> 2, p = lane & 3, l16 = lane * 16, to = g * 128 + (l15 & 7) * 16, xb = ((g & 1) << 1) | (g >> 1);
    const unsigned r = 8 * wave + (lane >> 3), ao = r * 2048 + 16 * ((lane & 7) ^ ((r >> 1) & 7)) + 1024, ra = lb0 + L_H + (l15 >> 3) * 2048 + (l15 & 7) * 128;
    const v16u c0 = {(lane & 8) ? 0xEEEEu : 0x4444u, lb0 + L_H + l16, l16, lb0 + L_W + wave * 24576 + l16, lb0 + L_T + wave * 512 + q * 128 + (g & 1) * 64 + (g >> 1) * 32 + p * 2,
                     lb0 + (isV ? L_T + to : L_QX + (wave >> 1) * 4096 + (wave & 1) * 128 + g * 128 + to), isV ? lb0 + L_QX + g * 128 + to : lb0 + L_T + to,
                     isV ? xb * 16384 + l15 * 16 : g < 2 ? (l15 >> 3) * 2048 + (head & 7) * 256 + to : lane < 40 ? 4096 + (head & 7) * 128 + l15 * 16 : lane == 40 && head + wave == 0 ? 5120 : 0x7FFF0000u, isV ? xb * 65536 + l15 * 16 : lb0 + L_T + 2048 + (to ^ (l15 >> 3) * 64),
                     ra + 16 * (g ^ (l15 >> 1)), ra + 16 * ((4 + g) ^ (l15 >> 1)), ao, ao - 896,
                     0u, lb0 + (isV ? L_QX : L_KX) + wave * 1024 + l16, 0u};
    const unsigned sd = ((pol ^ (wave >> 1)) & 1) * 8192;
    PARS;
    const v16u s0 = {0x3e0293eeu, 0x3db504f3u, 0x3fb8aa3bu, (unsigned)set, (unsigned)wave, O_E1 + (((xg >> 1) & 1) * 4 + wave) * 16384 + ((pol ^ (xg >> 2)) & 1) * 8192 + 2 * kx, pubb + sd + (isV ? hx * 65536 : 0), psb + (isV ? sd + hx * 262144 : 0),
                     O_H1 - 1024 + set * 65536, lb0 + L_H + wave * 2048, hw(pol, xg) + H_C + 512 * set, par(P_R), par(P_A), par(P_X), lb0 + L_ST + (wave == STAMP_WAVE ? 0 : MiB), lb0 + L_ST};
    u64 cm[4];
#pragma unroll
    for (int i = 0; i < 4; ++i) cm[i] = __ballot(16 * (l15 >> 3) + 4 * g + i <= 8 * wave + (l15 & 7));
    const u64 dbg = DBG(a, blockIdx.x);
    const v16u s1 = {isV ? O_FL + set * 16384 + sd + (wave & 1) * 4096 + head * 128 : head + wave ? 0x7FFF0000u : 0u, isV ? set : par(P_M), isV ? set : (unsigned)cm[0], (unsigned)(cm[0] >> 32), (unsigned)cm[1], (unsigned)(cm[1] >> 32),
                     (unsigned)cm[2], (unsigned)(cm[2] >> 32), (unsigned)cm[3], (unsigned)(cm[3] >> 32), (unsigned)dbg, (unsigned)(dbg >> 32), lb0 + L_T + wave * 512, ii < 4 ? hw(pol, xg) + H_D + 4 * wave : 0u, (unsigned)kx,
                     head < (isV ? 20 : 28) ? O_D + (isV ? D_X2 : 0) + head * 8192 + wave * 2048 : 0x70000000u};
    const v4u rws = mkrs(ws, WS_END);
    v32u ww[13];
#pragma unroll
    for (int i = 0; i < 13; ++i) {
        const v8u x0 = __builtin_bit_cast(v8u, w[4 * i]), x1 = __builtin_bit_cast(v8u, w[4 * i + 1]), x2 = __builtin_bit_cast(v8u, w[4 * i + 2]), x3 = __builtin_bit_cast(v8u, w[4 * i + 3]);
        ww[i] = __builtin_shufflevector(__builtin_shufflevector(x0, x1, 0, 1, 2, 3, 4, 5, 6, 7, 8, 9, 10, 11, 12, 13, 14, 15), __builtin_shufflevector(x2, x3, 0, 1, 2, 3, 4, 5, 6, 7, 8, 9, 10, 11, 12, 13, 14, 15),
                                        0, 1, 2, 3, 4, 5, 6, 7, 8, 9, 10, 11, 12, 13, 14, 15, 16, 17, 18, 19, 20, 21, 22, 23, 24, 25, 26, 27, 28, 29, 30, 31);
    }
#define S1_CALL(TXT) asm volatile("s_nop 4\n" TXT :: "{v[0:15]}"(c0), "{s[32:47]}"(s0), "{s[48:63]}"(s1), "{s[64:67]}"(rws), \
              "{a[0:31]}"(ww[0]), "{a[32:63]}"(ww[1]), "{a[64:95]}"(ww[2]), "{a[96:127]}"(ww[3]), "{a[128:159]}"(ww[4]), "{a[160:191]}"(ww[5]), "{a[192:223]}"(ww[6]), "{a[224:255]}"(ww[7]), \
              "{v[92:123]}"(ww[8]), "{v[124:155]}"(ww[9]), "{v[156:187]}"(ww[10]), "{v[188:219]}"(ww[11]), "{v[220:251]}"(ww[12]) : "memory", "s95", "v253")
    if constexpr (isV) S1_CALL(ASM_V); else S1_CALL(ASM_Q);
}

__device__ __forceinline__ float gelu_tanh(float v) {
    const float e = v * (-2.302208198f - 0.1029432397f * (v * v));
    return v * __builtin_amdgcn_rcpf(1.f + __builtin_amdgcn_exp2f(e));
}
__device__ __forceinline__ v4u unsent4(v4u v) { return (v4u){unsent(v.x), unsent(v.y), unsent(v.z), unsent(v.w)}; }
__device__ __forceinline__ unsigned rt_now() { return (unsigned)__builtin_amdgcn_s_memrealtime(); }
__device__ __forceinline__ void rt_delay(unsigned ticks  , unsigned t0 = rt_now()) {
    while ((unsigned)__builtin_amdgcn_s_memrealtime() - t0 < ticks) __builtin_amdgcn_s_sleep(2);
}
__device__ __forceinline__ float wave_sum(float x) {
    x += __uint_as_float(__builtin_amdgcn_update_dpp(0, __float_as_uint(x), 0xB1, 0xF, 0xF, false));
    x += __uint_as_float(__builtin_amdgcn_update_dpp(0, __float_as_uint(x), 0x4E, 0xF, 0xF, false));
    x += __uint_as_float(__builtin_amdgcn_update_dpp(0, __float_as_uint(x), 0x124, 0xF, 0xF, false));
    x += __uint_as_float(__builtin_amdgcn_update_dpp(0, __float_as_uint(x), 0x128, 0xF, 0xF, false));
    return red32_add(red16_add(x));
}
__device__ __forceinline__ void ln_row(const v4u (&xr)[2], const f32x4 (&c1)[4], const f32x4 (&sh)[4], v4u (&o)[2]) {
    f32x2 v[8];
    f32x2 s2 = {0.f, 0.f};
#pragma unroll
    for (int j = 0; j < 8; ++j) { v[j] = (f32x2){lo16f(xr[j >> 2][j & 3]), hi16f(xr[j >> 2][j & 3])}; s2 += v[j]; }
    const float mean = wave_sum(s2[0] + s2[1]) * (1.f / DM);
    f32x2 q2 = {0.f, 0.f};
#pragma unroll
    for (int j = 0; j < 8; ++j) { const f32x2 d = v[j] - mean; q2 += d * d; }
    const float vs = wave_sum(q2[0] + q2[1]);
    const float mb = rbf(mean), vb = rbf(vs * (1.f / DM));
    const float rsd = 1.f / rbf(sqrtf(rbf(vb + 1e-5f)));
    auto r2 = [](f32x2 z) { const unsigned u = pack2(z[0], z[1]); return (f32x2){lo16f(u), hi16f(u)}; };
#pragma unroll
    for (int j = 0; j < 8; ++j) {
        const f32x2 cc = {c1[j >> 1][2 * (j & 1)], c1[j >> 1][2 * (j & 1) + 1]}, ss = {sh[j >> 1][2 * (j & 1)], sh[j >> 1][2 * (j & 1) + 1]};
        const f32x2 y = r2(r2(r2(v[j] - mb) * rsd) * cc) + ss;
        o[j >> 2][j & 3] = pack2(y[0], y[1]);
    }
}
template <int N> __device__ __forceinline__ void ld_mod(rsrc_t rs, unsigned* ctl, const int (&col)[N], f32x4* m) {
    for (unsigned spins = 0;;) {
        unsigned mx = 0;
#pragma unroll
        for (int i = 0; i < N; ++i) { const v4u v = bld16(rs, O_MOD + col[i] * 4); mx = max(mx, umax4(v)); m[i] = __builtin_bit_cast(f32x4, v); }
        if (!__any(mx == SENT)) break;
        __builtin_amdgcn_s_sleep(6);
        if (spin_fail(spins, ctl + C_ABORT)) asm volatile("s_endpgm");
    }
}
__device__ __forceinline__ void set_m0(unsigned v) { asm volatile("s_mov_b32 m0, %0" :: "s"(v) : "m0"); }
__device__ __forceinline__ void mod8(rsrc_t rs, int col, f32x4 (&m)[2]) {
    f32x4 p[4][2];
#pragma unroll
    for (int k = 0; k < 8; ++k) p[k >> 1][k & 1] = __builtin_bit_cast(f32x4, bld16(rs, O_MODP + ((k >> 1) * 2048 + col + 4 * (k & 1)) * 4));
#pragma unroll
    for (int h = 0; h < 2; ++h) {
        const f32x4 t = (p[0][h] + p[1][h]) + (p[2][h] + p[3][h]);
#pragma unroll
        for (int i = 0; i < 4; ++i) { float v = rbf(t[i]); if (col >= DM) v = rbf(1.f + v); m[h][i] = v; }
    }
}
template <int N, class F> __device__ __forceinline__ void mod_cols(const Args& a, rsrc_t rs, unsigned char* lds, int cg, int k0, F&& mid) {
    const int tid = otid();
    v4u wv[N];
    const v2u cr = *(const v2u*)(a.c + 4 * tid);
#pragma unroll
    for (int i = 0; i < N; ++i) {
        const v4u* p = (const v4u*)(a.w_mod + (size_t)(k0 + (tid >> 3) + 32 * i) * 6144 + cg * 64 + 8 * (tid & 7));
        wv[i] = NTW & 4 ? __builtin_nontemporal_load(p) : *p;
    }
    SB(); mid(); SB();
    f32x4 sv = {lo16f(cr.x), hi16f(cr.x), lo16f(cr.y), hi16f(cr.y)};
#pragma unroll
    for (int i = 0; i < 4; ++i) sv[i] = rbf(sv[i] / (1.f + __expf(-sv[i])));
    *(LAS f32x4*)(lds + 16384 + tid * 16) = sv;
    lds_barrier();
    f32x2 acc[4] = {{0.f, 0.f}, {0.f, 0.f}, {0.f, 0.f}, {0.f, 0.f}};
#pragma unroll
    for (int i = 0; i < N; ++i) {
        const float sv = *(const LAS float*)(lds + 16384 + (k0 + (tid >> 3) + 32 * i) * 4);
#pragma unroll
        for (int j = 0; j < 4; ++j) acc[j] += sv * (f32x2){lo16f(wv[i][j]), hi16f(wv[i][j])};
    }
    unsigned char* red = lds + 20480;
    *(LAS f32x4*)(red + (tid >> 3) * 272 + (tid & 7) * 32) = (f32x4){acc[0][0], acc[0][1], acc[1][0], acc[1][1]};
    *(LAS f32x4*)(red + (tid >> 3) * 272 + (tid & 7) * 32 + 16) = (f32x4){acc[2][0], acc[2][1], acc[3][0], acc[3][1]};
    lds_barrier();
    if (tid < 64) {
        f32x4 t = {0.f, 0.f, 0.f, 0.f};
#pragma unroll
        for (int i = 0; i < 8; ++i) t += *(const LAS f32x4*)(red + ((tid >> 4) * 8 + i) * 272 + (tid & 15) * 16);
#pragma unroll
        for (int i = 0; i < 4; ++i) { float v = red32_add(red16_add(t[i])); if (N == 32) { v = rbf(v); if ((cg >> 4) == 4) v = rbf(1.f + v); } t[i] = v; }
        if (tid < 16) bst16(rs, (N == 32 ? O_MOD : O_MODP + k0 * 32) + (cg * 64 + 4 * tid) * 4, unsent4(__builtin_bit_cast(v4u, t)));
    }
}
#define TD_A 300
#define TD_L 1000
#define TD_M 850
#define TD_G 120
constexpr int NARM0 = 2216, NARM1 = 4712;
__device__ __forceinline__ unsigned arm0(unsigned v) {
    if (v < 104) return (v < 64 ? O_FL : O_HW - 65536) + v * 1024;
    if ((v -= 104) < 768) return (v < 384 ? O_EP : O_PS - MiB / 2) + v / 6 * 8192 + v % 6 * 1024;
    if ((v -= 768) < 1024) return O_E1 + 131072 + v * 1024;
    if ((v -= 1024) < 320) return O_D + D_X2 + v / 160 * D_SLOT + v % 160 * 1024;
    if ((v -= 320) < 448) return O_D + v / 224 * D_SLOT + v % 224 * 1024;
    return O_SL + (v - 448) * 1024;
}
__device__ __forceinline__ void f_start(const Args& a, rsrc_t rs, unsigned char* lds, int idx  , unsigned t0) {
    const int tid = otid(), lane = tid & 63, wave = a.pad0;
    unsigned* const ctl = (unsigned*)(a.ws + O_CTL);
    const int r0 = 4 * idx + wave;
    const bool boss = idx == 63;
    unsigned gv = wave >> 1;
    auto serve = [&](bool all) { if (gv && (all || !ldg4_sc1(a.ws, O_GO + tid * 128))) __hip_atomic_store((gu32*)(a.ws + O_GO + tid * 128), gv, RLX_AGENT); };
    if (boss) bst16(rs, tid * 16, (v4u){0u, 0u, 0u, 0u});
    v4u xr[6][2];
    auto ldx = [&](int k, int row) { for (int c = 0; c < 2; ++c) xr[k][c] = *(const v4u*)(a.x + (size_t)row * DM + 512 * c + 8 * lane); };
    SB();
    mod_cols<8>(a, rs, lds, idx & 31, (idx >> 5) * 256, [&]() {
        if (boss) { asm volatile("s_waitcnt vmcnt(8)\n\ts_barrier" ::: "memory"); rt_delay(TD_G, t0); serve(true); }
        ldx(0, r0); if (idx == 55) ldx(1, r0 + 16);
    });
    RSTAMP(73, 1);
    if (boss) serve(false); else if (wave == 0) { wait_vm0(); wait_go(a.ws); }
    drained(ctl + C_M + 32 * (idx & 7));
    RSTAMP(73, 2);
    if (idx >= 64) {
        for (unsigned v = r0 - 256; v < NARM0; v += 256) bst16(rs, arm0(v) + lane * 16, v < 80 ? (v4u){0u, 0u, 0u, 0u} : sentv());
        if (idx < 80) {
            const unsigned u = wave * 256 + ((idx >> 1) & 3) * 64 + lane;
            const v4u m = unsent4(*(const v4u*)(a.mem0 + (u & 7) * DM + (u >> 4) * 16 + ((u >> 3) & 1) * 8));
            bst16(rs, O_E1 + (idx - 64) * 8192 + tid * 16, m); bst16(rs, O_E1 + (idx - 64) * 8192 + 4096 + tid * 16, m);
        }
    }
    if (idx == 59) {
        unsigned best[2] = {~0u, ~0u};
        for (int r = 0; r < NPR; ++r) {
            const u64 t0 = __builtin_readcyclecounter();
            asm volatile("s_mov_b64 exec, 1\n\ts_nop 4\n\tglobal_store_dword %0, %1, %2 sc1\n\ts_waitcnt vmcnt(0)\n\ts_mov_b64 exec, -1" :: "v"(O_POL + 256 + ((r ^ wave) & 1) * 8192 + wave * 1280), "v"(0u), "s"(a.ws) : "memory");
            const unsigned d = (unsigned)(__builtin_readcyclecounter() - t0);
            if ((r ^ wave) & 1) best[1] = min(best[1], d); else best[0] = min(best[0], d);
        }
        *(LAS v2u*)(lds + 40960 + wave * 8) = (v2u){best[0], best[1]};
        lds_barrier();
        const v4u p = lds16(lds + 40960), q = lds16(lds + 40976);
        const bool second = min(min(p.y, p.w), min(q.y, q.w)) < min(min(p.x, p.z), min(q.x, q.z));
        if (tid == 0) __hip_atomic_store((gu32*)(a.ws + O_POL), (second ^ (__builtin_amdgcn_s_getreg((3 << 11) | 20) >> 2)) & 1, RLX_AGENT);
    }
    RSTAMP(72, 0);
    for (unsigned spins = 0; idx != 59 && __any(lane < 8 && __hip_atomic_load((gu32*)(ctl + C_M + 32 * (lane & 7)), RLX_AGENT) < 16u);) {
        if (boss) serve(false);
        __builtin_amdgcn_s_sleep(8);
        if (spin_fail(spins, ctl + C_ABORT)) asm volatile("s_endpgm");
    }
    RSTAMP(73, 0);
    f32x4 c1[4], sh[4];
    auto prep = [&]() {
        f32x4 m[2];
        mod8(rs, 8 * tid, m);
        *(LAS f32x4*)(lds + 32 * tid) = m[0]; *(LAS f32x4*)(lds + 32 * tid + 16) = m[1];
        lds_barrier();
#pragma unroll
        for (int i = 0; i < 4; ++i) { const int col = 512 * (i >> 1) + 8 * lane + 4 * (i & 1); sh[i] = *(const LAS f32x4*)(lds + col * 4); c1[i] = *(const LAS f32x4*)(lds + (DM + col) * 4); }
    };
    prep();
    auto row = [&](int k, int r) {
        v4u o[2];
        ln_row(xr[k], c1, sh, o);
#pragma unroll
        for (int c = 0; c < 2; ++c) {
            const bool od = lane & 1;
            const unsigned y0 = __builtin_amdgcn_update_dpp(0, od ? o[c].x : o[c].z, 0xB1, 0xF, 0xF, false), y1 = __builtin_amdgcn_update_dpp(0, od ? o[c].y : o[c].w, 0xB1, 0xF, 0xF, false);
            bst16(rs, O_H1 + (unsigned)(r * DM + 512 * c + 8 * lane) * 2, od ? (v4u){y0, o[c].z, y1, o[c].w} : (v4u){o[c].x, y0, o[c].y, y1});
        }
    };
    if (idx != 59) row(0, r0);
    if (idx == 55) row(1, r0 + 16);
    drained(ctl + C_A + 32 * (idx & 7));
    RSTAMP(75, 2);
    if (boss) { gv = 2 + wait_set<4>(a.ws, ctl); serve(true); }
    rt_delay(idx >= 64 ? TD_M : TD_L, t0);
    if (idx >= 64) {
#pragma unroll
        for (int k = 0; k < 6; ++k) ldx(k, 6 * r0 - 1024 + k);
        for (unsigned v = NARM0 + r0 - 256; v < NARM1; v += 256) bst16(rs, arm0(v) + lane * 16, sentv());
#pragma unroll 1
        for (int k = 0; k < 6; ++k) {
            row(0, 6 * r0 - 1024 + k);
#pragma unroll
            for (int i = 0; i < 10; ++i) xr[i >> 1][i & 1] = xr[(i >> 1) + 1][i & 1];
        }
        drained(ctl + C_H);
        return;
    }
    const unsigned pol = wait_set(a.ws, ctl);
    if (idx < 32) {
#pragma unroll 1
        for (int h = 0; h < 2; ++h) { mod_cols<32>(a, rs, lds, h ? (idx < 16 ? idx + 32 : idx + 64) : idx + 48, 0, []() {}); lds_barrier(); }
    }
    if (idx == 51 && wave == 0) {
        PARS;
        if (!wave_wait<8>(ctl + C_Q, 1, 1, 128, ctl + C_ABORT)) return;
        const unsigned now = rt_now(), d = __hip_atomic_load((gu32*)(ctl + C_Q + 1), RLX_AGENT) + par(P_T0) - now;
        if (lane < 8) __hip_atomic_store((gu32*)(a.ws + O_HW + H_D + 128 + lane * 2048), (now + (d < par(P_T0) && d > 80 ? d : 80)) | 1, RLX_AGENT);
    }
    if (boss) {
        for (unsigned spins = 0; (__builtin_amdgcn_readfirstlane(ldg4_sc1(a.ws, hw(pol, 7) + H_C + 512)) & 127) < 3;) {
            serve(false);
            __builtin_amdgcn_s_sleep(64);
            if (spin_fail(spins, ctl + C_ABORT)) break;
        }
    }
}
#define TD_W 1300
__device__ __forceinline__ unsigned r_ew(unsigned pol, unsigned lane) { return ((lane >> 4) & 1) * 65536 + (((O_E1 >> 21) ^ pol ^ (lane >> 5)) & 1) * 8192 + (lane & 15) * 16; }
__device__ __forceinline__ unsigned r_e1(int cb) { return O_E1 + ((cb >> 2) & 3) * 16384 + (cb >> 4) * 1024 + (cb & 3) * 256; }
__device__ __forceinline__ unsigned r_db(int cb) { return D_X2 + (cb >> 3) * 16384 + (cb & 7) * 1024; }
__device__ __forceinline__ unsigned r_st(int cb, unsigned lane) { return D_ST + (cb >> 5) * 16384 + (lane >> 5) * 8192 + (cb & 31) * 256 + (lane & 31) * 8; }
__device__ __forceinline__ void r_main(const Args& a, rsrc_t rs, unsigned char* lds, int xg, int i8, unsigned* ctl, unsigned t0) {
    const unsigned lane = lane_id(), wave = a.pad0, l15 = lane & 15, g = lane >> 4, q = l15 >> 2, p = lane & 3, cb = 8 * xg + i8;
    const unsigned pol = wait_set(a.ws, ctl), lb = (unsigned)(size_t)(LAS unsigned char*)lds;
    *(volatile LAS unsigned*)(lds + 4096) = 0u;
    lds_barrier();
    if (threadIdx.x == 0) __hip_atomic_fetch_add((gu32*)(ctl + C_Q), 1u, RLX_AGENT);
    RSTAMP(74, 0);
    const v16u c0 = {(lane & 8) ? 0xEEEEu : 0x4444u, lane * 16, 4096 + lane * 16, lb + wave * 1024 + l15 * 16 + (g & 1) * 8 + (g >> 1) * 4,
                     lb + wave * 1024 + (8 * (p >> 1) + q) * 16 + (p & 1) * 8, lb + wave * 1024 + ((2 * (g & 1) + (p >> 1)) ^ (cb & 1)) * 256 + (8 * (g >> 1) + q) * 16 + (p & 1) * 8,
                     r_ew(pol, lane), (wave * 32 + (lane & 31)) * 2048 + cb * 32 + (lane >> 5) * 16, nearg(pol, O_FL + wave * 16384, xg >> 2) + ((cb >> 4) & 1) * 4096 + (lane & 31) * 128, r_st(cb, lane) - r_db(cb), lb + 4096, (g >> 1) * 1024 + (g & 1) * 256 + l15 * 16,
                     O_MOD + (2 * DM + 16 * cb + 8 * (lane >> 5)) * 4, (lane >> 3) * 16384 + 4096 + (lane & 7) * 128, 0u, 0u};
    const unsigned sd = ((pol ^ (xg >> 2)) & 1) * 8192;
    PARS;
    const v16u s0 = {wave, wave, par(P_A), par(P_B), par(P_EU), par(P_DMIN), par(P_R), par(P_TP), par(P_TU), wave == 0 ? par(P_A0) : wave == 1 ? par(P_A1) : 0,
                     O_UX + (cb & 31) * 65536 + sd, O_UM + (cb & 31) * 16384 + sd, O_EP + ((xg >> 1) & 1) * 131072 + sd, O_PS + sd, r_e1(cb), O_D + r_db(cb)};
    const u64 dbg = DBG(a, blockIdx.x);
    const v8u s1 = {i8 ? 0u : hw(pol, xg) + H_C + 512 * (wave & 1), (cb & 1) * 65536, lb + 8192 + wave * 32, lb + 8192, (unsigned)dbg, (unsigned)(dbg >> 32), (unsigned)(u64)a.x, (unsigned)((u64)a.x >> 32)};
    const v4u rws = mkrs(a.ws, WS_END);
    const v8u s2 = {0u, 0u, par(P_TF), par(P_X2), hw(pol, xg) + H_D, par(P_TT), t0, par(P_B2)};
    asm volatile("s_nop 4\n" ASM_R :: "{v[0:15]}"(c0), "{s[32:47]}"(s0), "{s[48:55]}"(s1), "{s[56:63]}"(s2), "{s[64:67]}"(rws) : "memory", "s95", "v253", "a255");
}

typedef __attribute__((ext_vector_type(16))) float f32x16;
template <bool is2> __device__ __forceinline__ void f_main(const Args& a, rsrc_t rs, unsigned char* lds, int xg, int kq, int cg, unsigned* ctl) {
    constexpr int L_C = 131072, L_S = L_C + 8192, NG = is2 ? 7 : 8, LD = is2 ? DM : FF;
    const int tid = otid(), lane = tid & 63, wave = a.pad0, l15 = lane & 15, g = lane >> 4;
    const unsigned lb = (unsigned)(size_t)(LAS unsigned char*)lds, q = l15 >> 2, p = lane & 3, l16 = lane * 16;
    const unsigned pol = __builtin_amdgcn_readfirstlane(ldg4_sc1(a.ws, O_POL));
    auto gr = [&](int G) { const int x = G + 2 * ((blockIdx.x >> 3) & 3); return x >= NG ? x - NG : x; };
    lds_barrier();
    v4u w[64];
    f32x16 u2;
    {
        v4u rW = mkrs(is2 ? a.w2 : a.w1, DM * FF * 2);
        unsigned bo[2];
#pragma unroll
        for (int i = 0; i < 2; ++i) bo[i] = (g * LD + 16 * ((l15 >> 1) ^ (g | i << 2)) + 8 * (lane & 1)) * 2;
        asm volatile("s_nop 4" : "+s"(rW));
        auto issue = [&](int G) {
            const unsigned r0 = 896 * kq + 128 * gr(G) + 32 * wave;
#pragma unroll
            for (int j = 0; j < 8; ++j) bdma<(NTW & 8)>(rW, bo[(j >> 1) & 1], ((r0 + 4 * j) * LD + 128 * cg) * 2, lb + (G & 3) * 32768 + wave * 8192 + j * 1024);
        };
        issue(0); issue(1); issue(2); issue(3);
        const unsigned ox = is2 ? (4 * (g >> 1) + q) * 256 + 32 * ((2 * (kq ^ (2 * (g & 1) + (p >> 1))) + (wave >> 1)) ^ q) + 16 * (wave & 1) + 8 * (p & 1)
                                : (8 * (g >> 1) + q) * 256 + 32 * ((2 * wave + (g & 1)) ^ (q | (g >> 1) << 2)) + 8 * p;
        sfor<0, NG>([&](auto gg) {
            constexpr int G = decltype(gg)::v;
            asm volatile("s_waitcnt vmcnt(%0)\n\ts_barrier" :: "n"(G < NG - 3 ? 24 : 8 * (NG - 1 - G)) : "memory");
#pragma unroll
            for (int k = 0; k < 8; ++k) {
                const unsigned o = (G & 3) * 32768 + k * 4096;
                const v2u lo = tr_rd(lds, o + ox), hi = tr_rd(lds, o + (is2 ? (ox ^ 128) + 2048 : ox + 1024));
                w[8 * G + k] = (v4u){lo.x, lo.y, hi.x, hi.y};
            }
#pragma unroll
            for (int k = 8 * G; k < 8 * G + 8; ++k) asm volatile("" : "+a"(w[k]));
            lds_barrier();
            if (G + 4 < NG) issue(G + 4);
        });
    }
    const unsigned sd = ((pol ^ (xg >> 2)) & 1) * 8192;
    v16u c0 = {l16, lb + l16, is2 ? min(lane, 6) * 16384 + wave * 2048 + 2044 : D_ST - D_X2 + (l15 >> 3) * 16384 + ((l15 & 6) * 4 + 2 * wave + (lane & 1)) * 256, lb + L_S + wave * 256 + l15 * 16, lb + L_S + (lane & 31) * 8, 0u, 0xbdd2d3e8u, 0xbdd2d3e8u, 0xc0135761u, 0xc0135761u, 0x3f800000u, 0x3f800000u, 0u, 0u, 0u, 0u};
    if (is2) {
        const int col = 128 * cg + 32 * kq + 8 * wave + 4 * (lane >> 5);
        const v2u br = *(const v2u*)(a.b2 + col);
        f32x4 gv;
        ld_mod<1>(rs, ctl, {5 * DM + col}, &gv);
        const int f1 = 7 * (kq & 1) + min(lane, 6);
        c0[3] = hw(pol, (xg & 4) + (f1 >> 2)) + H_P + 256 * (f1 & 3); c0[4] = r_db(col >> 4) - (kq & 1) * 7 * 16384 + (wave & 1) * 512 + (lane & 31) * 16 + (lane >> 5) * 8; c0[5] = ((lane & 31) * DM + col) * 2;
        c0[8] = br.x << 16; c0[9] = br.x & 0xFFFF0000u; c0[10] = br.y << 16; c0[11] = br.y & 0xFFFF0000u;
#pragma unroll
        for (int i = 0; i < 4; ++i) c0[12 + i] = __float_as_uint(gv[i]);
    } else {
#pragma unroll
        for (int h = 0; h < 4; ++h) {
            const v2u br = *(const v2u*)(a.b1 + 128 * cg + 32 * wave + 8 * h + 4 * (g >> 1));
            u2[4 * h] = lo16f(br.x); u2[4 * h + 1] = hi16f(br.x); u2[4 * h + 2] = lo16f(br.y); u2[4 * h + 3] = hi16f(br.y);
        }
        f32x4 md[2];
        const int k0 = 128 * gr(tid >> 5) + 4 * (tid & 31);
        ld_mod<2>(rs, ctl, {4 * DM + k0, 3 * DM + k0}, md);
        *(LAS f32x4*)(lds + L_C + 16 * tid) = md[0]; *(LAS v2u*)(lds + L_C + 4096 + 8 * tid) = (v2u){pack2(md[1][0], md[1][1]), pack2(md[1][2], md[1][3])};
        lds_barrier();
        c0[12] = lb + L_C + 32 * (g >> 1); c0[13] = lb + L_C + 4096 + 16 * (g >> 1);
    }
    if (!wave_wait<16>(ctl + C_H, 1, 1, 64, ctl + C_ABORT)) asm volatile("s_endpgm");
    RSTAMP(74, 0);
    const u64 dbg = DBG(a, blockIdx.x);
    PARS;
    const v16u s0 = {0u, (unsigned)wave, is2 ? par(P_T1) : -par(P_TZ), par(P_TS), O_D + (is2 ? (kq & 1) * 7 * 16384 : D_X2) + sd, lb + wave * 2048, is2 ? 0u : hw(pol, xg) + H_C, hw(pol, xg) + H_P + 256 * ((blockIdx.x >> 3) & 3),
                     O_D + (cg >= 14 ? cg - 14 : cg) * 16384 + wave * 2048 + sd, D_ST - D_X2 + (wave >> 1) * 16384 + (wave & 1) * 4096, lb + L_S + 2048 + (wave == STAMP_WAVE ? 0 : MiB), lb + L_S + 2048,
                     (unsigned)dbg, (unsigned)(dbg >> 32), (unsigned)(u64)a.out, (unsigned)((u64)a.out >> 32)};
    v16u s1;
#pragma unroll
    for (int i = 0; i < 16; ++i) {
        const int o = kq ^ (i & 3);
        s1[i] = i < 8 ? gr(i) * 16384 + wave * 2048 : O_SL + ((cg * 2 + (o & 1)) * 2 + ((i >> 1) & 1)) * 16384 + ((pol ^ (o >> 1)) & 1) * 8192 + (i & 1) * 4096 + wave * 1024;
    }
    s1[12] = is2 ? par(P_T2) : par(P_TY); s1[13] = is2 ? par(P_T2) + 10 : -par(P_TW);
    const v4u rws = mkrs(a.ws, WS_END);
    v32u ww[8];
#pragma unroll
    for (int i = 0; i < NG; ++i) {
        const v16u x0 = __builtin_shufflevector(__builtin_shufflevector(w[8 * i], w[8 * i + 1], 0, 1, 2, 3, 4, 5, 6, 7), __builtin_shufflevector(w[8 * i + 2], w[8 * i + 3], 0, 1, 2, 3, 4, 5, 6, 7), 0, 1, 2, 3, 4, 5, 6, 7, 8, 9, 10, 11, 12, 13, 14, 15);
        const v16u x1 = __builtin_shufflevector(__builtin_shufflevector(w[8 * i + 4], w[8 * i + 5], 0, 1, 2, 3, 4, 5, 6, 7), __builtin_shufflevector(w[8 * i + 6], w[8 * i + 7], 0, 1, 2, 3, 4, 5, 6, 7), 0, 1, 2, 3, 4, 5, 6, 7, 8, 9, 10, 11, 12, 13, 14, 15);
        ww[i] = __builtin_shufflevector(x0, x1, 0, 1, 2, 3, 4, 5, 6, 7, 8, 9, 10, 11, 12, 13, 14, 15, 16, 17, 18, 19, 20, 21, 22, 23, 24, 25, 26, 27, 28, 29, 30, 31);
    }
#define F_IN "{v[0:15]}"(c0), "{s[32:47]}"(s0), "{s[48:63]}"(s1), "{s[64:67]}"(rws), "{a[0:31]}"(ww[0]), "{a[32:63]}"(ww[1]), "{a[64:95]}"(ww[2]), "{a[96:127]}"(ww[3]), "{a[128:159]}"(ww[4]), "{a[160:191]}"(ww[5]), "{a[192:223]}"(ww[6])
    if constexpr (is2) asm volatile("s_nop 4\n" ASM_F2 :: F_IN : "memory", "s95");
    else asm volatile("s_nop 7\ns_nop 7\n" ASM_F1 :: F_IN, "{a[224:255]}"(ww[7]), "{v[32:47]}"(u2) : "memory", "s95");
}

#define NTS ""
__global__ void __launch_bounds__(256, 1) k_main(Args) {
    extern __shared__ __attribute__((aligned(16))) unsigned char lds[];
    const int xg = blockIdx.x & 7, ii = blockIdx.x >> 3;
    int wv = __builtin_amdgcn_readfirstlane(threadIdx.x >> 6); asm volatile("" : "+s"(wv));
    asm volatile("s_load_dwordx8 s[8:15], %0, 0\n s_load_dword s16, %0, 0x68\n s_getpc_b64 s[4:5]\n s_bfe_u32 s3, %1, 0x20003\n s_lshl_b32 s3, s3, 15\n v_lshl_add_u32 v1, %2, 7, s3\n v_min_u32 v1, %4, v1\n"
                 "s_mul_i32 s17, %3, 24576\n s_mov_b32 m0, s17\n s_cmp_gt_u32 %1, 127\n global_load_lds_dword v1, s[4:5]\n s_cbranch_scc1 3f\n"
                 "v_and_b32 v1, 63, %2\n v_lshrrev_b32 v2, 3, v1\n v_lshrrev_b32 v3, 4, v1\n v_and_b32 v3, 2, v3\n v_xor_b32 v1, v1, v3\n v_and_b32 v1, 7, v1\n v_lshlrev_b32 v1, 4, v1\n v_lshl_add_u32 v1, v2, 13, v1\n"
                 "s_and_b32 s3, %1, 7\n s_lshl_b32 s3, s3, 2\n s_lshr_b32 s18, %1, 5\n s_add_u32 s3, s3, s18\n s_lshl_b32 s3, s3, 1\n s_and_b32 s18, %3, 1\n s_add_u32 s3, s3, s18\n v_lshl_add_u32 v1, s3, 7, v1\n v_xor_b32 v2, 64, v1\n"
                 "s_bfe_u32 s19, %1, 0x10004\n s_bitcmp1_b32 %1, 3\n s_cselect_b32 s18, 22, %6\n s_lshl_b32 s19, s19, s18\n s_lshl_b32 s20, %3, 14\n s_add_u32 s20, s20, %5\n s_add_u32 m0, s20, 0x2000\n s_bfe_u32 s21, %1, 0x10003\n s_lshl_b32 s21, s21, 19\n s_lshr_b32 s18, %3, 1\n s_mul_i32 s22, s18, s21\n"
                 "s_mov_b32 s6, 0x800000\n s_mov_b32 s7, 0x20000\n s_mov_b32 s23, 0\n s_waitcnt lgkmcnt(0)\n s_cmp_lg_u32 s18, 0\n s_cselect_b64 s[4:5], s[10:11], s[8:9]\n s_cmp_lg_u32 s21, 0\n s_cselect_b64 s[4:5], s[12:13], s[4:5]\n s_and_b32 s5, s5, 0xffff\n"
                 "1: s_xor_b32 s3, s22, s19\n buffer_load_dwordx4 v1, s[4:7], s3 offen " NTS "lds\n s_add_u32 m0, m0, 0x400\n s_add_u32 s3, s3, 0x10000\n buffer_load_dwordx4 v2, s[4:7], s3 offen " NTS "lds\n s_add_u32 m0, m0, 0x400\n s_add_u32 s22, s22, 0x20000\n"
                 "s_add_u32 s23, s23, 1\n s_and_b32 s3, s23, 3\n s_cbranch_scc1 1b\n s_add_u32 s22, s22, s21\n s_cmp_eq_u32 s23, 4\n s_cselect_b32 m0, s17, m0\n s_cmp_eq_u32 s23, 16\n s_cselect_b32 m0, s20, m0\n s_cmp_lt_u32 s23, 20\n s_cbranch_scc1 1b\n"
                 "3: s_waitcnt lgkmcnt(0)"
                 :: "s"(__builtin_amdgcn_kernarg_segment_ptr()), "s"(blockIdx.x), "v"(threadIdx.x), "s"(wv), "n"(CODE_KB * 1024 - 4), "n"(L_H), "n"(QSH + 12)
                 : "memory", "scc", "m0", "v1", "v2", "v3", "s3", "s4", "s5", "s6", "s7", "s8", "s9", "s10", "s11", "s12", "s13", "s14", "s15", "s16", "s17", "s18", "s19", "s20", "s21", "s22", "s23");
    const unsigned t0 = rt_now();
#define MODE_ blockIdx.x
    auto args = [&]() { PARS; Args r = *(const Args*)pp_; r.pad0 = wv; r.mode = MODE_; return r; };
    const Args a = args();
    if (threadIdx.x == 0) __hip_atomic_store((gu32*)(a.ws + O_GO + blockIdx.x * 128), 0u, RLX_AGENT);
    RSTAMP(75, 0);
    if (ii < 16) {
        if (ii & 1) s1_main<true>(args(), lds, xg, ii); else s1_main<false>(args(), lds, xg, ii);
        return;
    }
    const rsrc_t rs = __builtin_amdgcn_make_buffer_rsrc((void*)a.ws, (short)0, (int)WS_END, 0x00020000);
    unsigned* const ctl = (unsigned*)(a.ws + O_CTL);
    const int i4 = ii & 3, cg = 4 * (xg & 3) + i4;
    f_start(args(), rs, lds, (blockIdx.x - 64) & 127, t0);
    if (ii < 24) {
        rt_delay(TD_W - 200, t0);
        if (wv < 2) {
            v4u r = wv ? mkrs(a.x, S_TOK * DM * 2) : mkrs(a.ws, WS_END);
            asm volatile("s_mov_b32 m0, 0x8000\n\ts_nop 4\n\tbuffer_load_dword %0, %1, 0 offen sc1 lds" :: "v"(wv ? lane_id() * 65536 : lane_id() * 2 * MiB + (ii - 16) * 262144 + 7168), "s"(r) : "m0");
        }
        rt_delay(TD_W, t0);
        r_main(args(), rs, lds, xg, ii - 16, ctl, t0);
        return;
    }
    RSTAMP(75, 1);
    if (ii >= 28 && cg >= 14) return;
    if (ii < 28) f_main<true>(args(), rs, lds, xg, xg >> 1, 4 * (xg & 1) + i4, ctl);
    else f_main<false>(args(), rs, lds, xg, 0, 14 * (xg >> 2) + cg, ctl);
}

extern "C" void kernel_launch(void* const* d_in, const int* in_sizes, int n_in,
                              void* d_out, int out_size, void* d_ws, size_t ws_size,
                              hipStream_t stream) {
    Args a;
    a.x = (const u16*)d_in[0]; a.mem0 = (const u16*)d_in[1]; a.Wq = (const u16*)d_in[2]; a.Wk = (const u16*)d_in[3];
    a.Wv = (const u16*)d_in[4]; a.Wo = (const u16*)d_in[5];
    a.w1 = (const u16*)d_in[8]; a.b1 = (const u16*)d_in[9]; a.w2 = (const u16*)d_in[10]; a.b2 = (const u16*)d_in[11];
    a.out = (u16*)d_out; a.ws = (char*)d_ws + (size_t)WSB * MiB; a.mode = 0; a.pad0 = 0;
    a.c = (const u16*)d_in[12]; a.w_mod = (const u16*)d_in[13];
    hipFuncSetAttribute((const void*)k_main, hipFuncAttributeMaxDynamicSharedMemorySize, LDS_BYTES);
    k_main<<<256, 256, LDS_BYTES, stream>>>(a);
}
```

```cpp
#include <hip/hip_runtime.h>
#include <hip/hip_bf16.h>
#include <stdint.h>

typedef unsigned short u16;
typedef unsigned long long u64;
typedef __attribute__((ext_vector_type(8))) short bf16x8;
typedef __attribute__((ext_vector_type(4))) short bf16x4;
typedef __attribute__((ext_vector_type(4))) __bf16 bf16x4_t;
typedef __attribute__((ext_vector_type(8))) __bf16 v8bf;
typedef __attribute__((ext_vector_type(16))) __bf16 v16bf;
typedef __attribute__((ext_vector_type(4))) float f32x4;
typedef __attribute__((ext_vector_type(2))) float f32x2;
typedef unsigned v8u __attribute__((ext_vector_type(8)));
typedef unsigned v4u __attribute__((ext_vector_type(4)));
typedef unsigned v2u __attribute__((ext_vector_type(2)));
#define GAS __attribute__((address_space(1)))
#define LAS __attribute__((address_space(3)))
typedef GAS unsigned gu32;
#define RLX_AGENT __ATOMIC_RELAXED, __HIP_MEMORY_SCOPE_AGENT

constexpr int S_TOK = 2048, DM = 1024, NH = 32, DH = 128, MEMR = 8, SEG = 32, NSEG = 64;
constexpr int HD = NH * DH;
constexpr int QKN = 2 * HD;
constexpr int FF = 3584;

#define NTW 12
constexpr int RG = 16;
#define RGX 8
constexpr unsigned MiB = 1u << 20;
constexpr unsigned O_CTL = 0;
constexpr unsigned O_MOD = 65536;
constexpr unsigned O_POL = O_MOD + 32768;
constexpr unsigned O_MODP = 131072;
constexpr unsigned O_GO = 196608;
constexpr unsigned O_FL = 262144;
constexpr unsigned O_EP = 2 * MiB;
constexpr unsigned O_PS = O_EP + RG * 262144;
constexpr unsigned O_UM = O_PS + RG * 262144;
constexpr unsigned O_E1 = O_UM + RG * 524288;
constexpr unsigned O_H1 = O_E1 + RG * 131072;
constexpr unsigned O_UX = O_H1 + 4 * MiB;
constexpr unsigned O_D = O_UX + RGX * 2 * MiB, D_SLOT = 524288, D_X2 = 14 * 16384, D_ST = 22 * 16384;
constexpr unsigned O_SL = O_D + 32 * MiB;
constexpr unsigned WS_END = O_SL + 2 * MiB;
#define WSB 0
static_assert(WSB * MiB + WS_END <= 256 * MiB && O_UX % (2 * MiB) == 0 && O_E1 % (2 * MiB) == 0, "ws");
__device__ __forceinline__ unsigned nearg(unsigned pol, unsigned off, unsigned h) { return off + (((off >> 21) ^ pol ^ h) & 1) * 8192; }
constexpr int C_ABORT = 0;
constexpr int C_H = 32;
constexpr int C_Q = 64;
constexpr int C_T = 192;
constexpr int C_M = 256;
constexpr int C_A = 512;
constexpr unsigned O_HW = 49152;
constexpr int H_C = 0;
constexpr int H_D = 256;
constexpr int H_P = 1024;
__device__ __forceinline__ unsigned hw(unsigned pol, int x) { return nearg(pol, O_HW, x >> 2) + (x & 3) * 2048; }

__device__ __forceinline__ float bf2f(u16 v) { return __uint_as_float(((unsigned)v) << 16); }
__device__ __forceinline__ u16 f2bf(float f) {
    __hip_bfloat16 h = __float2bfloat16(f);
    return __builtin_bit_cast(u16, h);
}
__device__ __forceinline__ float rbf(float f) { return bf2f(f2bf(f)); }

#define CODE_KB 96
#define NPR 8
enum { P_A, P_B, P_EU, P_DMIN, P_R, P_TP, P_TU, P_T0, P_X, P_TZ, P_TF, P_X2, P_T1, P_TS, P_TT, P_TY, P_T2, P_A0, P_A1, P_M, P_B2, P_TW, NPAR };
#define CH_PAR 152, 72, 8, 4, 60, 100, 36, 520, 36, -30u, 40, 72, 190, 28, 12, 128, 144, 20, 24, 6, 72, 84
struct Args {
    const u16* Wq; const u16* Wk; const u16* Wv; const u16* Wo;
    const u16* c; const u16* w_mod; const u16* mem0;
    const u16* x; const u16* w1; const u16* b1; const u16* w2; const u16* b2;
    u16* out; char* ws;
    unsigned mode, pad0;
};
#define PARS auto pp_ = (__attribute__((address_space(4))) const Args*)__builtin_amdgcn_kernarg_segment_ptr(); asm volatile("" : "+s"(pp_))
constexpr unsigned PARV[NPAR] = {CH_PAR};
#define par(i) PARV[i]
#define RSTAMP(sg, i) do { } while (0)
#define STAMP_WAVE 0
#define DBG(a, i) 0ull
constexpr unsigned SPIN_CAP = 1u << 22;
constexpr unsigned SENT = 0xFFFFFFFFu;

__device__ __forceinline__ void lds_barrier() { asm volatile("s_waitcnt lgkmcnt(0)\n\ts_barrier" ::: "memory"); }
__device__ __forceinline__ void wait_vm0() { asm volatile("s_waitcnt vmcnt(0)" ::: "memory"); }
typedef __bf16 bf16x2_t __attribute__((ext_vector_type(2)));
__device__ __forceinline__ unsigned pack2(float a, float b) { const f32x2 v = {a, b}; return __builtin_bit_cast(unsigned, __builtin_convertvector(v, bf16x2_t)); }
__device__ __forceinline__ float lo16f(unsigned u) { return __uint_as_float(u << 16); }
__device__ __forceinline__ float hi16f(unsigned u) { return __uint_as_float(u & 0xFFFF0000u); }
__device__ __forceinline__ unsigned umax4(v4u x) { return max(max(x.x, x.y), max(x.z, x.w)); }
__device__ __forceinline__ unsigned unsent(unsigned x) { return min(x, 0xFFFFFFFEu); }

__device__ __forceinline__ unsigned ldg4_sc1(const void* sb, unsigned vo) {
    unsigned r;
    asm volatile("global_load_dword %0, %1, %2 sc1\n\ts_waitcnt vmcnt(0)" : "=&v"(r) : "v"(vo), "s"(sb) : "memory");
    return r;
}
__device__ __forceinline__ bool spin_fail(unsigned& spins, unsigned* abortw) {
    if (++spins > SPIN_CAP) { __hip_atomic_store((gu32*)abortw, 1u, RLX_AGENT); return true; }
    if ((spins & 63u) == 0 && __builtin_amdgcn_readfirstlane(__hip_atomic_load((gu32*)abortw, RLX_AGENT)) == 1u) return true;
    return false;
}
__device__ __forceinline__ float red16_add(float x) { auto r = __builtin_amdgcn_permlane16_swap(__float_as_uint(x), __float_as_uint(x), false, false); return __uint_as_float(r[0]) + __uint_as_float(r[1]); }
__device__ __forceinline__ float red32_add(float x) { auto r = __builtin_amdgcn_permlane32_swap(__float_as_uint(x), __float_as_uint(x), false, false); return __uint_as_float(r[0]) + __uint_as_float(r[1]); }
__device__ __forceinline__ v2u tr_rd(const unsigned char* lds_base, unsigned off) {
    return __builtin_bit_cast(v2u, __builtin_amdgcn_ds_read_tr16_b64_v4bf16((LAS bf16x4_t*)(lds_base + off)));
}
__device__ __forceinline__ v4u lds16(const unsigned char* p) { return *(const LAS v4u*)p; }
__device__ __forceinline__ void dma16(const void* src, unsigned char* dst, int aux) {
    if (aux) __builtin_amdgcn_global_load_lds((const unsigned*)src, (unsigned*)dst, 16, 0, 2);
    else __builtin_amdgcn_global_load_lds((const unsigned*)src, (unsigned*)dst, 16, 0, 0);
}

__device__ __forceinline__ v4u mkrs(const void* p, unsigned bytes) { return (v4u){(unsigned)(u64)p, (unsigned)((u64)p >> 32) & 0xFFFFu, bytes, 0x00020000u}; }
template <int AUX> __device__ __forceinline__ void bdma(v4u r, unsigned vo, unsigned so, unsigned ldsaddr) {
    if (AUX == 16) asm volatile("s_mov_b32 m0, %0\n\ts_nop 0\n\tbuffer_load_dwordx4 %1, %2, %3 offen sc1 lds" :: "s"(ldsaddr), "v"(vo), "s"(r), "s"(so) : "m0");
    else if (AUX) asm volatile("s_mov_b32 m0, %0\n\ts_nop 0\n\tbuffer_load_dwordx4 %1, %2, %3 offen nt lds" :: "s"(ldsaddr), "v"(vo), "s"(r), "s"(so) : "m0");
    else asm volatile("s_mov_b32 m0, %0\n\ts_nop 0\n\tbuffer_load_dwordx4 %1, %2, %3 offen lds" :: "s"(ldsaddr), "v"(vo), "s"(r), "s"(so) : "m0");
}

template <int I> struct IC { static constexpr int v = I; };
template <int B, int E, class F> __device__ __forceinline__ void sfor(F&& f) { if constexpr (B < E) { f(IC<B>{}); sfor<B + 1, E>(f); } }
#define SB() __builtin_amdgcn_sched_barrier(0)

__device__ __forceinline__ int lane_id() { int l; asm volatile("v_mbcnt_lo_u32_b32 %0, -1, 0\n\tv_mbcnt_hi_u32_b32 %0, -1, %0" : "=v"(l)); return l; }
constexpr int LDS_BYTES = 163840;
typedef __amdgpu_buffer_rsrc_t rsrc_t;
__device__ __forceinline__ v4u bld16(rsrc_t r, unsigned off) { return __builtin_bit_cast(v4u, __builtin_amdgcn_raw_buffer_load_b128(r, off, 0, 16)); }
__device__ __forceinline__ v2u bld8(rsrc_t r, unsigned off) { return __builtin_bit_cast(v2u, __builtin_amdgcn_raw_buffer_load_b64(r, off, 0, 16)); }
__device__ __forceinline__ void bst16(rsrc_t r, unsigned off, v4u v) { __builtin_amdgcn_raw_buffer_store_b128(__builtin_bit_cast(__attribute__((ext_vector_type(4))) unsigned, v), r, off, 0, 16); }
__device__ __forceinline__ void bst8(rsrc_t r, unsigned off, v2u v) { __builtin_amdgcn_raw_buffer_store_b64(__builtin_bit_cast(__attribute__((ext_vector_type(2))) unsigned, v), r, off, 0, 16); }

constexpr int L_FLAG = LDS_BYTES - 64;
#define otid() (a.pad0 * 64 + lane_id())
__device__ __forceinline__ v4u sld16(rsrc_t r, unsigned vo, unsigned so) { return __builtin_bit_cast(v4u, __builtin_amdgcn_raw_buffer_load_b128(r, vo, so, 16)); }
__device__ __forceinline__ v4u sentv() { unsigned x = SENT; asm volatile("" : "+v"(x)); return (v4u){x, x, x, x}; }
#define BW_SLEEP 16
template <int SLP = BW_SLEEP> __device__ __forceinline__ bool wave_wait(const unsigned* p, int stride, int n, unsigned target, unsigned* abortw, unsigned char* lds = nullptr) {
    const int lane = lane_id();
    for (unsigned spins = 0;;) {
        const unsigned v = lane < n ? __hip_atomic_load((gu32*)(p + lane * stride), RLX_AGENT) : target;
        if (!__any(v < target)) { if (lds && lane == 0) *(volatile LAS unsigned*)(lds + L_FLAG + 32) = v; return true; }
        __builtin_amdgcn_s_sleep(SLP);
        if (++spins > (SPIN_CAP << 1)) { __hip_atomic_store((gu32*)abortw, 1u, RLX_AGENT); return false; }
        if ((spins & 63u) == 0 && __builtin_amdgcn_readfirstlane(__hip_atomic_load((gu32*)abortw, RLX_AGENT)) == 1u) return false;
    }
}
__device__ __forceinline__ unsigned wait_go(const char* ws) {
    for (unsigned spins = 0;;) {
        const unsigned v = __builtin_amdgcn_readfirstlane(ldg4_sc1(ws, O_GO + blockIdx.x * 128));
        if (v) return v;
        __builtin_amdgcn_s_sleep(8);
        if (++spins > SPIN_CAP) asm volatile("s_endpgm");
    }
}
template <int SLP = 16> __device__ __forceinline__ unsigned wait_set(const char* ws, unsigned* ctl) {
    if (!wave_wait<SLP>(ctl + C_A, 32, 8, 16, ctl + C_ABORT)) asm volatile("s_endpgm");
    return __builtin_amdgcn_readfirstlane(ldg4_sc1(ws, O_POL));
}
__device__ __forceinline__ void drained(unsigned* cnt) {
    asm volatile("s_waitcnt vmcnt(0)\n\ts_barrier" ::: "memory");
    if (threadIdx.x == 0) __hip_atomic_fetch_add((gu32*)cnt, 1u, RLX_AGENT);
}

constexpr int L_W = 0;
constexpr int L_H = 98304;
constexpr int L_KX = L_H + 40960;
constexpr int L_QX = L_KX + 8192;
constexpr int L_T = L_QX + 8192;
constexpr int L_ST = L_T + 4096;
constexpr int lsl(bool isV, int c, int n) { return isV ? (c < 6 && (n & 1) ? 2 * c + (n >> 1) : -1) : (c < 12 && n == 1 ? c : -1); }
constexpr int ridx(bool isV, int c, int n) { int r = 0; for (int i = 0; i < 4 * c + n; ++i) r += lsl(isV, i >> 2, i & 3) < 0; return r; }
#define ASM_Q \
    "s_add_u32 m0, s41, 0\ns_xor_b32 s85, s62, 0\ns_add_u32 s85, s85, s40\nbuffer_load_dwordx4 v11, s[64:67], s85 offen sc1 lds\n" \
    "buffer_load_dwordx4 v12, s[64:67], s85 offen offset:1024 sc1 lds\ns_add_u32 m0, s41, 8192\ns_xor_b32 s85, s62, 256\ns_add_u32 s85, s85, s40\n" \
    "buffer_load_dwordx4 v11, s[64:67], s85 offen sc1 lds\nbuffer_load_dwordx4 v12, s[64:67], s85 offen offset:1024 sc1 lds\ns_add_u32 m0, s41, 16384\ns_xor_b32 s85, s62, 512\n" \
    "s_add_u32 s85, s85, s40\nbuffer_load_dwordx4 v11, s[64:67], s85 offen sc1 lds\nbuffer_load_dwordx4 v12, s[64:67], s85 offen offset:1024 sc1 lds\ns_memrealtime s[72:73]\n" \
    "s_waitcnt lgkmcnt(0)\ns_mov_b32 s80, s72\ns_mov_b32 s83, 0x7fff0000\ns_mov_b32 s19, s35\ns_branch .Lq8_%=\n.Lq9_%=:\ns_mov_b32 s35, s19\ns_mov_b32 s19, 64\n" \
    "s_waitcnt vmcnt(0)\ns_memrealtime s[72:73]\ns_waitcnt lgkmcnt(0)\nv_mov_b32 v253, 1\nv_mov_b32 v252, s72\ns_or_b32 s69, s35, s36\ns_cselect_b32 s69, 0x7fff0000, 0\n" \
    "s_mov_b64 exec, 1\nbuffer_atomic_umax v252, off, s[64:67], s69 offset:260\nbuffer_atomic_add v253, off, s[64:67], s69 offset:256\ns_mov_b64 exec, -1\ns_mov_b32 s16, 0\n" \
    "s_cmp_eq_u32 s36, 0\ns_cselect_b32 s17, 511, -1\ns_mov_b32 s18, s44\ns_mov_b32 s72, s80\ns_barrier\n.Lq2_%=:\nds_read_b128 v[64:67], v3 offset:0\n" \
    "ds_read_b128 v[68:71], v9 offset:0\nds_read_b128 v[72:75], v9 offset:4096\nv_mov_b32_dpp v16, v20 quad_perm:[0,1,2,3] row_mask:0xf bank_mask:0xc\n" \
    "v_mov_b32_dpp v17, v21 quad_perm:[0,1,2,3] row_mask:0xf bank_mask:0xc\nv_mov_b32_dpp v18, v22 quad_perm:[0,1,2,3] row_mask:0xf bank_mask:0xc\n" \
    "v_mov_b32_dpp v19, v23 quad_perm:[0,1,2,3] row_mask:0xf bank_mask:0xc\nv_mov_b32 v254, 0xff800000\nv_cvt_pk_bf16_f32 v16, 0, v16\nv_cvt_pk_bf16_f32 v17, 0, v17\n" \
    "v_cvt_pk_bf16_f32 v18, 0, v18\nv_cvt_pk_bf16_f32 v19, 0, v19\nv_mul_f32 v16, s33, v16\nv_mul_f32 v17, s33, v17\nv_mul_f32 v18, s33, v18\ns_waitcnt lgkmcnt(0)\n" \
    "v_mfma_f32_16x16x32_bf16 v[48:51], a[8:11], v[68:71], 0\ns_add_u32 m0, s41, 24576\nv_mul_f32 v19, s33, v19\nv_cvt_pk_bf16_f32 v16, 0, v16\n" \
    "v_mfma_f32_16x16x32_bf16 v[52:55], a[8:11], v[72:75], 0\nds_read_b128 v[76:79], v3 offset:1024\nv_cvt_pk_bf16_f32 v17, 0, v17\nv_cvt_pk_bf16_f32 v18, 0, v18\n" \
    "v_mfma_f32_16x16x32_bf16 v[56:59], a[16:19], v[68:71], 0\nds_read_b128 v[80:83], v10 offset:0\nv_cvt_pk_bf16_f32 v19, 0, v19\nv_cndmask_b32 v16, v254, v16, s[50:51]\n" \
    "v_mfma_f32_16x16x32_bf16 v[60:63], a[16:19], v[72:75], 0\nds_read_b128 v[84:87], v10 offset:4096\nv_cndmask_b32 v17, v254, v17, s[52:53]\n" \
    "v_cndmask_b32 v18, v254, v18, s[54:55]\nv_mfma_f32_16x16x32_bf16 v[32:35], a[0:3], v[68:71], 0\ns_xor_b32 s85, s62, 768\nv_cndmask_b32 v19, v254, v19, s[56:57]\n" \
    "v_max3_f32 v254, v16, v17, v18\nv_mfma_f32_16x16x32_bf16 v[36:39], a[0:3], v[72:75], 0\ns_add_u32 s85, s85, s40\nv_mfma_f32_16x16x32_bf16 v[40:43], v[64:67], v[68:71], 0\n" \
    "v_max_f32 v254, v254, v19\nv_mfma_f32_16x16x32_bf16 v[44:47], v[64:67], v[72:75], 0\ns_nop 0\ns_waitcnt lgkmcnt(0)\n" \
    "v_max_f32_dpp v252, v254, v254 row_ror:8 row_mask:0xf bank_mask:0xf\nv_mfma_f32_16x16x32_bf16 v[48:51], a[12:15], v[80:83], v[48:51]\n" \
    "buffer_load_dwordx4 v11, s[64:67], s85 offen sc1 lds\nv_mfma_f32_16x16x32_bf16 v[52:55], a[12:15], v[84:87], v[52:55]\nds_read_b128 v[88:91], v3 offset:2048\n" \
    "v_max_f32_dpp v253, v254, v254 row_ror:8 row_mask:0xf bank_mask:0xf\nv_mfma_f32_16x16x32_bf16 v[56:59], a[20:23], v[80:83], v[56:59]\n" \
    "ds_read_b128 v[64:67], v9 offset:1024\nv_permlane32_swap_b32 v252, v253\nv_mfma_f32_16x16x32_bf16 v[60:63], a[20:23], v[84:87], v[60:63]\n" \
    "ds_read_b128 v[68:71], v9 offset:5120\nv_max_f32 v252, v252, v253\nv_cvt_pk_bf16_f32 v24, 0, v24\nv_mfma_f32_16x16x32_bf16 v[32:35], a[4:7], v[80:83], v[32:35]\n" \
    "buffer_load_dwordx4 v12, s[64:67], s85 offen offset:1024 sc1 lds\nv_mfma_f32_16x16x32_bf16 v[36:39], a[4:7], v[84:87], v[36:39]\ns_add_u32 m0, s41, 32768\n" \
    "v_cvt_pk_bf16_f32 v25, 0, v25\nv_cvt_pk_bf16_f32 v26, 0, v26\nv_mfma_f32_16x16x32_bf16 v[40:43], v[76:79], v[80:83], v[40:43]\ns_xor_b32 s85, s62, 1024\n" \
    "v_cvt_pk_bf16_f32 v27, 0, v27\nv_mul_f32 v24, s33, v24\nv_mfma_f32_16x16x32_bf16 v[44:47], v[76:79], v[84:87], v[44:47]\ns_add_u32 s85, s85, s40\ns_waitcnt lgkmcnt(0)\n" \
    "v_mfma_f32_16x16x32_bf16 v[48:51], a[32:35], v[64:67], v[48:51]\nv_mul_f32 v25, s33, v25\nv_mul_f32 v26, s33, v26\n" \
    "v_mfma_f32_16x16x32_bf16 v[52:55], a[32:35], v[68:71], v[52:55]\nds_read_b128 v[72:75], v3 offset:3072\nv_mul_f32 v27, s33, v27\nv_cvt_pk_bf16_f32 v24, 0, v24\n" \
    "v_mfma_f32_16x16x32_bf16 v[56:59], a[40:43], v[64:67], v[56:59]\nds_read_b128 v[76:79], v10 offset:1024\nv_cvt_pk_bf16_f32 v25, 0, v25\nv_cvt_pk_bf16_f32 v26, 0, v26\n" \
    "v_mfma_f32_16x16x32_bf16 v[60:63], a[40:43], v[68:71], v[60:63]\nds_read_b128 v[80:83], v10 offset:5120\nv_cvt_pk_bf16_f32 v27, 0, v27\nv_max3_f32 v254, v252, v24, v25\n" \
    "v_mfma_f32_16x16x32_bf16 v[32:35], a[24:27], v[64:67], v[32:35]\nbuffer_load_dwordx4 v11, s[64:67], s85 offen sc1 lds\n" \
    "v_mfma_f32_16x16x32_bf16 v[36:39], a[24:27], v[68:71], v[36:39]\nv_max3_f32 v252, v254, v26, v27\nv_max3_f32 v253, v254, v26, v27\n" \
    "v_mfma_f32_16x16x32_bf16 v[40:43], v[88:91], v[64:67], v[40:43]\ns_nop 0\nv_permlane16_swap_b32 v252, v253\n" \
    "v_mfma_f32_16x16x32_bf16 v[44:47], v[88:91], v[68:71], v[44:47]\nbuffer_load_dwordx4 v12, s[64:67], s85 offen offset:1024 sc1 lds\ns_waitcnt lgkmcnt(0)\n" \
    "v_mfma_f32_16x16x32_bf16 v[48:51], a[36:39], v[76:79], v[48:51]\ns_add_u32 s84, s40, 0x20000\nv_max_f32 v252, v252, v253\nv_sub_f32 v16, v16, v252\n" \
    "v_mfma_f32_16x16x32_bf16 v[52:55], a[36:39], v[80:83], v[52:55]\nds_read_b128 v[64:67], v3 offset:4096\nv_sub_f32 v17, v17, v252\nv_sub_f32 v18, v18, v252\n" \
    "v_mfma_f32_16x16x32_bf16 v[56:59], a[44:47], v[76:79], v[56:59]\nds_read_b128 v[68:71], v9 offset:8192\nv_sub_f32 v19, v19, v252\nv_sub_f32 v24, v24, v252\n" \
    "v_mfma_f32_16x16x32_bf16 v[60:63], a[44:47], v[80:83], v[60:63]\nds_read_b128 v[84:87], v9 offset:12288\nv_sub_f32 v25, v25, v252\nv_sub_f32 v26, v26, v252\n" \
    "v_mfma_f32_16x16x32_bf16 v[32:35], a[28:31], v[76:79], v[32:35]\ns_cmp_lt_u32 s35, 62\ns_cselect_b32 s84, s84, s40\nv_sub_f32 v27, v27, v252\n" \
    "v_mfma_f32_16x16x32_bf16 v[36:39], a[28:31], v[80:83], v[36:39]\ns_sub_u32 s20, s72, s80\nv_mul_f32 v16, s34, v16\nv_mul_f32 v17, s34, v17\n" \
    "v_mfma_f32_16x16x32_bf16 v[40:43], v[72:75], v[76:79], v[40:43]\ns_add_u32 s20, s20, 0\nv_mul_f32 v18, s34, v18\nv_mul_f32 v19, s34, v19\n" \
    "v_mfma_f32_16x16x32_bf16 v[44:47], v[72:75], v[80:83], v[44:47]\ns_min_u32 s21, s20, s16\ns_waitcnt lgkmcnt(0)\nv_mul_f32 v24, s34, v24\n" \
    "v_mfma_f32_16x16x32_bf16 v[48:51], a[56:59], v[68:71], v[48:51]\ns_mov_b32 s16, s20\nv_mul_f32 v25, s34, v25\nv_mul_f32 v26, s34, v26\n" \
    "v_mfma_f32_16x16x32_bf16 v[52:55], a[56:59], v[84:87], v[52:55]\nds_read_b128 v[88:91], v3 offset:5120\nv_mul_f32 v27, s34, v27\nv_exp_f32 v16, v16\n" \
    "v_mfma_f32_16x16x32_bf16 v[56:59], a[64:67], v[68:71], v[56:59]\nds_read_b128 v[72:75], v10 offset:8192\nv_exp_f32 v17, v17\n" \
    "v_mfma_f32_16x16x32_bf16 v[60:63], a[64:67], v[84:87], v[60:63]\nds_read_b128 v[76:79], v10 offset:12288\nv_exp_f32 v18, v18\n" \
    "v_mfma_f32_16x16x32_bf16 v[32:35], a[48:51], v[68:71], v[32:35]\ns_cmp_gt_u32 s21, 156\ns_cselect_b32 s21, 0, s21\nv_exp_f32 v19, v19\n" \
    "v_mfma_f32_16x16x32_bf16 v[36:39], a[48:51], v[84:87], v[36:39]\ns_lshr_b32 s20, s35, 2\nv_exp_f32 v24, v24\n" \
    "v_mfma_f32_16x16x32_bf16 v[40:43], v[64:67], v[68:71], v[40:43]\ns_lshl_b32 s22, s20, 8\nv_add_f32 v254, v16, v18\nv_add_f32 v255, v17, v19\n" \
    "v_mfma_f32_16x16x32_bf16 v[44:47], v[64:67], v[84:87], v[44:47]\ns_or_b32 s21, s21, s22\ns_waitcnt lgkmcnt(0)\nv_exp_f32 v25, v25\n" \
    "v_mfma_f32_16x16x32_bf16 v[48:51], a[60:63], v[72:75], v[48:51]\ns_and_b32 s20, s20, 1\nv_add_f32 v254, v254, v255\nv_exp_f32 v26, v26\n" \
    "v_mfma_f32_16x16x32_bf16 v[52:55], a[60:63], v[76:79], v[52:55]\nds_read_b128 v[64:67], v3 offset:6144\nv_exp_f32 v27, v27\n" \
    "v_mfma_f32_16x16x32_bf16 v[56:59], a[68:71], v[72:75], v[56:59]\nds_read_b128 v[68:71], v9 offset:9216\n" \
    "v_add_f32_dpp v252, v254, v254 row_ror:8 row_mask:0xf bank_mask:0xf\nv_add_f32_dpp v253, v254, v254 row_ror:8 row_mask:0xf bank_mask:0xf\n" \
    "v_mfma_f32_16x16x32_bf16 v[60:63], a[68:71], v[76:79], v[60:63]\nds_read_b128 v[80:83], v9 offset:13312\nv_add_f32 v254, v24, v26\nv_add_f32 v255, v25, v27\n" \
    "v_mfma_f32_16x16x32_bf16 v[32:35], a[52:55], v[72:75], v[32:35]\ns_lshl_b32 s20, s20, 2\nv_permlane32_swap_b32 v252, v253\n" \
    "v_mfma_f32_16x16x32_bf16 v[36:39], a[52:55], v[76:79], v[36:39]\ns_cmp_gt_u32 s21, s17\ns_cselect_b32 s20, s20, 0x7fff0000\nv_add_f32 v254, v254, v252\n" \
    "v_mfma_f32_16x16x32_bf16 v[40:43], v[88:91], v[72:75], v[40:43]\ns_max_u32 s17, s17, s21\nv_add_f32 v255, v255, v253\nv_add_f32 v252, v254, v255\n" \
    "v_mfma_f32_16x16x32_bf16 v[44:47], v[88:91], v[76:79], v[44:47]\nv_mov_b32 v15, s21\ns_mov_b64 exec, 1\nbuffer_atomic_umax v15, off, s[64:67], s20 offset:768\n" \
    "s_mov_b64 exec, -1\ns_waitcnt lgkmcnt(0)\nv_mfma_f32_16x16x32_bf16 v[48:51], a[80:83], v[68:71], v[48:51]\nv_add_f32 v253, v255, v254\n" \
    "v_mfma_f32_16x16x32_bf16 v[52:55], a[80:83], v[80:83], v[52:55]\nds_read_b128 v[72:75], v3 offset:7168\nv_permlane16_swap_b32 v252, v253\n" \
    "v_mfma_f32_16x16x32_bf16 v[56:59], a[88:91], v[68:71], v[56:59]\nds_read_b128 v[76:79], v10 offset:9216\nv_add_f32 v252, v252, v253\nv_rcp_f32 v252, v252\n" \
    "v_mfma_f32_16x16x32_bf16 v[60:63], a[88:91], v[80:83], v[60:63]\nds_read_b128 v[84:87], v10 offset:13312\nv_mul_f32 v16, v16, v252\nv_mul_f32 v17, v17, v252\n" \
    "v_mfma_f32_16x16x32_bf16 v[32:35], a[72:75], v[68:71], v[32:35]\nv_mul_f32 v24, v24, v252\nv_mul_f32 v25, v25, v252\n" \
    "v_mfma_f32_16x16x32_bf16 v[36:39], a[72:75], v[80:83], v[36:39]\nv_cvt_pk_bf16_f32 v20, v16, v17\nv_cvt_pk_bf16_f32 v22, v24, v25\n" \
    "v_mfma_f32_16x16x32_bf16 v[40:43], v[64:67], v[68:71], v[40:43]\nv_mul_f32 v18, v18, v252\nv_mul_f32 v19, v19, v252\n" \
    "v_mfma_f32_16x16x32_bf16 v[44:47], v[64:67], v[80:83], v[44:47]\ns_waitcnt lgkmcnt(0)\nv_mul_f32 v26, v26, v252\nv_mul_f32 v27, v27, v252\n" \
    "v_mfma_f32_16x16x32_bf16 v[48:51], a[84:87], v[76:79], v[48:51]\nv_cvt_pk_bf16_f32 v21, v18, v19\nv_cvt_pk_bf16_f32 v23, v26, v27\n" \
    "v_mfma_f32_16x16x32_bf16 v[52:55], a[84:87], v[84:87], v[52:55]\nds_read_b128 v[80:83], v3 offset:8192\nv_permlane32_swap_b32 v20, v22\n" \
    "v_mfma_f32_16x16x32_bf16 v[56:59], a[92:95], v[76:79], v[56:59]\nds_read_b128 v[88:91], v9 offset:16384\nv_permlane32_swap_b32 v21, v23\n" \
    "v_mfma_f32_16x16x32_bf16 v[60:63], a[92:95], v[84:87], v[60:63]\nds_read_b128 v[64:67], v9 offset:20480\nv_writelane_b32 v20, s18, 40\n" \
    "v_mfma_f32_16x16x32_bf16 v[32:35], a[76:79], v[76:79], v[32:35]\ns_xor_b32 s74, s83, 0x2000\nbuffer_store_dwordx4 v[20:23], v7, s[64:67], s83 offen sc1\n" \
    "v_mfma_f32_16x16x32_bf16 v[36:39], a[76:79], v[84:87], v[36:39]\nv_mfma_f32_16x16x32_bf16 v[40:43], v[72:75], v[76:79], v[40:43]\n" \
    "v_mfma_f32_16x16x32_bf16 v[44:47], v[72:75], v[84:87], v[44:47]\ns_waitcnt lgkmcnt(0)\nv_mfma_f32_16x16x32_bf16 v[48:51], a[104:107], v[88:91], v[48:51]\n" \
    "buffer_store_dwordx4 v[20:23], v7, s[64:67], s74 offen sc1\nv_mfma_f32_16x16x32_bf16 v[52:55], a[104:107], v[64:67], v[52:55]\nds_read_b128 v[84:87], v3 offset:9216\n" \
    "v_mov_b64 v[28:29], -1\nv_mov_b64 v[30:31], -1\nv_mfma_f32_16x16x32_bf16 v[56:59], a[112:115], v[88:91], v[56:59]\nds_read_b128 v[68:71], v10 offset:16384\n" \
    "s_add_u32 s69, s35, 2\nv_mfma_f32_16x16x32_bf16 v[60:63], a[112:115], v[64:67], v[60:63]\nds_read_b128 v[72:75], v10 offset:20480\ns_and_b32 s69, s69, 15\n" \
    "v_mfma_f32_16x16x32_bf16 v[32:35], a[96:99], v[88:91], v[32:35]\ns_lshl_b32 s69, s69, 18\ns_add_u32 s82, s38, s69\n" \
    "v_mfma_f32_16x16x32_bf16 v[36:39], a[96:99], v[64:67], v[36:39]\ns_lshr_b32 s69, s82, 8\ns_and_b32 s69, s69, 0x2000\n" \
    "v_mfma_f32_16x16x32_bf16 v[40:43], v[80:83], v[88:91], v[40:43]\ns_xor_b32 s82, s82, s69\ns_add_u32 s69, s35, 2\n" \
    "v_mfma_f32_16x16x32_bf16 v[44:47], v[80:83], v[64:67], v[44:47]\ns_and_b32 s69, s69, 15\ns_lshl_b32 s69, s69, 18\ns_waitcnt lgkmcnt(0)\n" \
    "v_mfma_f32_16x16x32_bf16 v[48:51], a[108:111], v[68:71], v[48:51]\ns_add_u32 s83, s39, s69\ns_xor_b32 s74, s82, 0x4000\n" \
    "v_mfma_f32_16x16x32_bf16 v[52:55], a[108:111], v[72:75], v[52:55]\nds_read_b128 v[64:67], v3 offset:10240\ns_xor_b32 s75, s83, 0x2000\n" \
    "v_mfma_f32_16x16x32_bf16 v[56:59], a[116:119], v[68:71], v[56:59]\nds_read_b128 v[88:91], v9 offset:17408\n" \
    "v_mfma_f32_16x16x32_bf16 v[60:63], a[116:119], v[72:75], v[60:63]\nds_read_b128 v[76:79], v9 offset:21504\n" \
    "v_mfma_f32_16x16x32_bf16 v[32:35], a[100:103], v[68:71], v[32:35]\nv_mfma_f32_16x16x32_bf16 v[36:39], a[100:103], v[72:75], v[36:39]\n" \
    "v_mfma_f32_16x16x32_bf16 v[40:43], v[84:87], v[68:71], v[40:43]\nv_mfma_f32_16x16x32_bf16 v[44:47], v[84:87], v[72:75], v[44:47]\n" \
    "buffer_store_dwordx4 v[28:31], v7, s[64:67], s82 offen sc1\ns_waitcnt lgkmcnt(0)\nv_mfma_f32_16x16x32_bf16 v[48:51], a[128:131], v[88:91], v[48:51]\n" \
    "v_mfma_f32_16x16x32_bf16 v[52:55], a[128:131], v[76:79], v[52:55]\nds_read_b128 v[68:71], v3 offset:11264\n" \
    "v_mfma_f32_16x16x32_bf16 v[56:59], a[136:139], v[88:91], v[56:59]\nds_read_b128 v[72:75], v10 offset:17408\n" \
    "v_mfma_f32_16x16x32_bf16 v[60:63], a[136:139], v[76:79], v[60:63]\nds_read_b128 v[80:83], v10 offset:21504\n" \
    "v_mfma_f32_16x16x32_bf16 v[32:35], a[120:123], v[88:91], v[32:35]\nv_mfma_f32_16x16x32_bf16 v[36:39], a[120:123], v[76:79], v[36:39]\n" \
    "buffer_store_dwordx4 v[28:31], v7, s[64:67], s74 offen sc1\nv_mfma_f32_16x16x32_bf16 v[40:43], v[64:67], v[88:91], v[40:43]\n" \
    "v_mfma_f32_16x16x32_bf16 v[44:47], v[64:67], v[76:79], v[44:47]\ns_waitcnt lgkmcnt(0)\nv_mfma_f32_16x16x32_bf16 v[48:51], a[132:135], v[72:75], v[48:51]\n" \
    "v_mfma_f32_16x16x32_bf16 v[52:55], a[132:135], v[80:83], v[52:55]\nds_read_b128 v[64:67], v3 offset:12288\ns_waitcnt vmcnt(5)\ns_barrier\n" \
    "v_mfma_f32_16x16x32_bf16 v[56:59], a[140:143], v[72:75], v[56:59]\nds_read_b128 v[76:79], v9 offset:24576\n" \
    "v_mfma_f32_16x16x32_bf16 v[60:63], a[140:143], v[80:83], v[60:63]\nds_read_b128 v[84:87], v9 offset:28672\ns_add_u32 m0, s41, 0\n" \
    "v_mfma_f32_16x16x32_bf16 v[32:35], a[124:127], v[72:75], v[32:35]\ns_xor_b32 s85, s62, 1280\ns_add_u32 s85, s85, s40\n" \
    "v_mfma_f32_16x16x32_bf16 v[36:39], a[124:127], v[80:83], v[36:39]\nbuffer_load_dwordx4 v11, s[64:67], s85 offen sc1 lds\n" \
    "v_mfma_f32_16x16x32_bf16 v[40:43], v[68:71], v[72:75], v[40:43]\nv_mfma_f32_16x16x32_bf16 v[44:47], v[68:71], v[80:83], v[44:47]\ns_waitcnt lgkmcnt(0)\n" \
    "v_mfma_f32_16x16x32_bf16 v[48:51], a[152:155], v[76:79], v[48:51]\nbuffer_load_dwordx4 v12, s[64:67], s85 offen offset:1024 sc1 lds\ns_add_u32 m0, s41, 8192\n" \
    "v_mfma_f32_16x16x32_bf16 v[52:55], a[152:155], v[84:87], v[52:55]\nds_read_b128 v[80:83], v3 offset:13312\ns_xor_b32 s85, s62, 1536\n" \
    "v_mfma_f32_16x16x32_bf16 v[56:59], a[160:163], v[76:79], v[56:59]\nds_read_b128 v[88:91], v10 offset:24576\ns_add_u32 s85, s85, s40\n" \
    "v_mfma_f32_16x16x32_bf16 v[60:63], a[160:163], v[84:87], v[60:63]\nds_read_b128 v[68:71], v10 offset:28672\n" \
    "v_mfma_f32_16x16x32_bf16 v[32:35], a[144:147], v[76:79], v[32:35]\nbuffer_load_dwordx4 v11, s[64:67], s85 offen sc1 lds\n" \
    "v_mfma_f32_16x16x32_bf16 v[36:39], a[144:147], v[84:87], v[36:39]\nv_mfma_f32_16x16x32_bf16 v[40:43], v[64:67], v[76:79], v[40:43]\n" \
    "v_mfma_f32_16x16x32_bf16 v[44:47], v[64:67], v[84:87], v[44:47]\nbuffer_load_dwordx4 v12, s[64:67], s85 offen offset:1024 sc1 lds\ns_waitcnt lgkmcnt(0)\n" \
    "v_mfma_f32_16x16x32_bf16 v[48:51], a[156:159], v[88:91], v[48:51]\nv_mfma_f32_16x16x32_bf16 v[52:55], a[156:159], v[68:71], v[52:55]\n" \
    "ds_read_b128 v[64:67], v3 offset:14336\nv_mfma_f32_16x16x32_bf16 v[56:59], a[164:167], v[88:91], v[56:59]\nds_read_b128 v[84:87], v9 offset:25600\n" \
    "v_mfma_f32_16x16x32_bf16 v[60:63], a[164:167], v[68:71], v[60:63]\nds_read_b128 v[72:75], v9 offset:29696\n" \
    "v_mfma_f32_16x16x32_bf16 v[32:35], a[148:151], v[88:91], v[32:35]\nbuffer_store_dwordx4 v[28:31], v7, s[64:67], s83 offen sc1\n" \
    "v_mfma_f32_16x16x32_bf16 v[36:39], a[148:151], v[68:71], v[36:39]\nv_mfma_f32_16x16x32_bf16 v[40:43], v[80:83], v[88:91], v[40:43]\n" \
    "v_mfma_f32_16x16x32_bf16 v[44:47], v[80:83], v[68:71], v[44:47]\ns_waitcnt lgkmcnt(0)\nv_mfma_f32_16x16x32_bf16 v[48:51], a[176:179], v[84:87], v[48:51]\n" \
    "v_mfma_f32_16x16x32_bf16 v[52:55], a[176:179], v[72:75], v[52:55]\nds_read_b128 v[68:71], v3 offset:15360\n" \
    "v_mfma_f32_16x16x32_bf16 v[56:59], a[184:187], v[84:87], v[56:59]\nds_read_b128 v[88:91], v10 offset:25600\n" \
    "v_mfma_f32_16x16x32_bf16 v[60:63], a[184:187], v[72:75], v[60:63]\nds_read_b128 v[76:79], v10 offset:29696\n" \
    "v_mfma_f32_16x16x32_bf16 v[32:35], a[168:171], v[84:87], v[32:35]\nbuffer_store_dwordx4 v[28:31], v7, s[64:67], s75 offen sc1\n" \
    "v_mfma_f32_16x16x32_bf16 v[36:39], a[168:171], v[72:75], v[36:39]\nv_lshrrev_b32 v16, 1, v2\nv_add_u32 v16, s60, v16\n" \
    "v_mfma_f32_16x16x32_bf16 v[40:43], v[64:67], v[84:87], v[40:43]\nds_write_b64 v16, v[28:29]\nv_mfma_f32_16x16x32_bf16 v[44:47], v[64:67], v[72:75], v[44:47]\n" \
    "ds_write_b64 v16, v[28:29] offset:2048\ns_add_u32 s69, s35, 2\ns_waitcnt lgkmcnt(2)\nv_mfma_f32_16x16x32_bf16 v[48:51], a[180:183], v[88:91], v[48:51]\n" \
    "s_and_b32 s69, s69, 255\ns_lshl_b32 s69, s69, 19\nv_mfma_f32_16x16x32_bf16 v[52:55], a[180:183], v[76:79], v[52:55]\nds_read_b128 v[64:67], v3 offset:16384\n" \
    "s_add_u32 s74, s63, s69\nv_mfma_f32_16x16x32_bf16 v[56:59], a[188:191], v[88:91], v[56:59]\nds_read_b128 v[72:75], v9 offset:32768\ns_cmp_lt_u32 s35, 62\n" \
    "s_cselect_b32 s74, s74, 0x7fff0000\nv_mfma_f32_16x16x32_bf16 v[60:63], a[188:191], v[76:79], v[60:63]\nds_read_b128 v[80:83], v9 offset:36864\n" \
    "v_mfma_f32_16x16x32_bf16 v[32:35], a[172:175], v[88:91], v[32:35]\nv_mfma_f32_16x16x32_bf16 v[36:39], a[172:175], v[76:79], v[36:39]\n" \
    "v_mfma_f32_16x16x32_bf16 v[40:43], v[68:71], v[88:91], v[40:43]\nv_mfma_f32_16x16x32_bf16 v[44:47], v[68:71], v[76:79], v[44:47]\ns_waitcnt lgkmcnt(0)\n" \
    "v_mfma_f32_16x16x32_bf16 v[48:51], a[200:203], v[72:75], v[48:51]\nbuffer_store_dwordx4 v[28:31], v2, s[64:67], s74 offen offset:0 sc1\n" \
    "v_mfma_f32_16x16x32_bf16 v[52:55], a[200:203], v[80:83], v[52:55]\nds_read_b128 v[68:71], v3 offset:17408\n" \
    "v_mfma_f32_16x16x32_bf16 v[56:59], a[208:211], v[72:75], v[56:59]\nds_read_b128 v[76:79], v10 offset:32768\n" \
    "v_mfma_f32_16x16x32_bf16 v[60:63], a[208:211], v[80:83], v[60:63]\nds_read_b128 v[84:87], v10 offset:36864\n" \
    "v_mfma_f32_16x16x32_bf16 v[32:35], a[192:195], v[72:75], v[32:35]\nv_mfma_f32_16x16x32_bf16 v[36:39], a[192:195], v[80:83], v[36:39]\n" \
    "v_mfma_f32_16x16x32_bf16 v[40:43], v[64:67], v[72:75], v[40:43]\nbuffer_store_dwordx4 v[28:31], v2, s[64:67], s74 offen offset:1024 sc1\ns_and_b32 s69, s35, 15\n" \
    "v_mfma_f32_16x16x32_bf16 v[44:47], v[64:67], v[80:83], v[44:47]\ns_lshl_b32 s69, s69, 18\ns_add_u32 s82, s38, s69\ns_waitcnt lgkmcnt(0)\n" \
    "v_mfma_f32_16x16x32_bf16 v[48:51], a[204:207], v[76:79], v[48:51]\ns_lshr_b32 s69, s82, 8\ns_and_b32 s69, s69, 0x2000\n" \
    "v_mfma_f32_16x16x32_bf16 v[52:55], a[204:207], v[84:87], v[52:55]\nds_read_b128 v[64:67], v3 offset:18432\ns_xor_b32 s82, s82, s69\n" \
    "v_mfma_f32_16x16x32_bf16 v[56:59], a[212:215], v[76:79], v[56:59]\nds_read_b128 v[72:75], v9 offset:33792\ns_and_b32 s69, s35, 15\n" \
    "v_mfma_f32_16x16x32_bf16 v[60:63], a[212:215], v[84:87], v[60:63]\nds_read_b128 v[80:83], v9 offset:37888\ns_lshl_b32 s69, s69, 18\n" \
    "v_mfma_f32_16x16x32_bf16 v[32:35], a[196:199], v[76:79], v[32:35]\ns_add_u32 s83, s39, s69\nv_mfma_f32_16x16x32_bf16 v[36:39], a[196:199], v[84:87], v[36:39]\n" \
    "v_mfma_f32_16x16x32_bf16 v[40:43], v[68:71], v[76:79], v[40:43]\nv_mfma_f32_16x16x32_bf16 v[44:47], v[68:71], v[84:87], v[44:47]\ns_waitcnt lgkmcnt(0)\n" \
    "v_mfma_f32_16x16x32_bf16 v[48:51], a[224:227], v[72:75], v[48:51]\nv_mfma_f32_16x16x32_bf16 v[52:55], a[224:227], v[80:83], v[52:55]\n" \
    "ds_read_b128 v[68:71], v3 offset:19456\nv_mfma_f32_16x16x32_bf16 v[56:59], a[232:235], v[72:75], v[56:59]\nds_read_b128 v[76:79], v10 offset:33792\n" \
    "v_mfma_f32_16x16x32_bf16 v[60:63], a[232:235], v[80:83], v[60:63]\nds_read_b128 v[84:87], v10 offset:37888\n" \
    "v_mfma_f32_16x16x32_bf16 v[32:35], a[216:219], v[72:75], v[32:35]\nv_mfma_f32_16x16x32_bf16 v[36:39], a[216:219], v[80:83], v[36:39]\n" \
    "v_mfma_f32_16x16x32_bf16 v[40:43], v[64:67], v[72:75], v[40:43]\nbuffer_load_dwordx2 v[18:19], off, s[64:67], s48 offset:768 sc1\n" \
    "v_mfma_f32_16x16x32_bf16 v[44:47], v[64:67], v[80:83], v[44:47]\ns_waitcnt lgkmcnt(0)\nv_mfma_f32_16x16x32_bf16 v[48:51], a[228:231], v[76:79], v[48:51]\n" \
    "v_mfma_f32_16x16x32_bf16 v[52:55], a[228:231], v[84:87], v[52:55]\nds_read_b128 v[64:67], v3 offset:20480\ns_waitcnt vmcnt(5)\ns_barrier\n" \
    "v_mfma_f32_16x16x32_bf16 v[56:59], a[236:239], v[76:79], v[56:59]\nds_read_b128 v[72:75], v9 offset:0\nv_mfma_f32_16x16x32_bf16 v[60:63], a[236:239], v[84:87], v[60:63]\n" \
    "ds_read_b128 v[80:83], v9 offset:4096\ns_add_u32 m0, s41, 16384\nv_mfma_f32_16x16x32_bf16 v[32:35], a[220:223], v[76:79], v[32:35]\ns_xor_b32 s85, s62, 1792\n" \
    "s_add_u32 s85, s85, s40\nv_mfma_f32_16x16x32_bf16 v[36:39], a[220:223], v[84:87], v[36:39]\nbuffer_load_dwordx4 v11, s[64:67], s85 offen sc1 lds\n" \
    "v_mfma_f32_16x16x32_bf16 v[40:43], v[68:71], v[76:79], v[40:43]\nv_mfma_f32_16x16x32_bf16 v[44:47], v[68:71], v[84:87], v[44:47]\ns_waitcnt lgkmcnt(0)\n" \
    "v_mfma_f32_16x16x32_bf16 v[48:51], a[248:251], v[72:75], v[48:51]\nbuffer_load_dwordx4 v12, s[64:67], s85 offen offset:1024 sc1 lds\n" \
    "v_mfma_f32_16x16x32_bf16 v[52:55], a[248:251], v[80:83], v[52:55]\nds_read_b128 v[68:71], v3 offset:21504\nv_mfma_f32_16x16x32_bf16 v[56:59], v[92:95], v[72:75], v[56:59]\n" \
    "ds_read_b128 v[76:79], v10 offset:0\nv_mfma_f32_16x16x32_bf16 v[60:63], v[92:95], v[80:83], v[60:63]\nds_read_b128 v[84:87], v10 offset:4096\n" \
    "v_mfma_f32_16x16x32_bf16 v[32:35], a[240:243], v[72:75], v[32:35]\nv_mfma_f32_16x16x32_bf16 v[36:39], a[240:243], v[80:83], v[36:39]\n" \
    "v_mfma_f32_16x16x32_bf16 v[40:43], v[64:67], v[72:75], v[40:43]\nv_mfma_f32_16x16x32_bf16 v[44:47], v[64:67], v[80:83], v[44:47]\ns_waitcnt lgkmcnt(0)\n" \
    "v_mfma_f32_16x16x32_bf16 v[48:51], a[252:255], v[76:79], v[48:51]\nv_mfma_f32_16x16x32_bf16 v[52:55], a[252:255], v[84:87], v[52:55]\n" \
    "ds_read_b128 v[64:67], v3 offset:22528\nv_mfma_f32_16x16x32_bf16 v[56:59], v[96:99], v[76:79], v[56:59]\nds_read_b128 v[72:75], v9 offset:1024\n" \
    "v_mfma_f32_16x16x32_bf16 v[60:63], v[96:99], v[84:87], v[60:63]\nds_read_b128 v[80:83], v9 offset:5120\nv_mfma_f32_16x16x32_bf16 v[32:35], a[244:247], v[76:79], v[32:35]\n" \
    "buffer_load_dword v13, off, s[64:67], s42 sc1\ns_and_b32 s69, s35, 15\nv_mfma_f32_16x16x32_bf16 v[36:39], a[244:247], v[84:87], v[36:39]\ns_lshl_b32 s69, s69, 17\n" \
    "s_add_u32 s81, s37, s69\nv_mfma_f32_16x16x32_bf16 v[40:43], v[68:71], v[76:79], v[40:43]\ns_lshr_b32 s69, s81, 8\ns_and_b32 s69, s69, 0x2000\n" \
    "v_mfma_f32_16x16x32_bf16 v[44:47], v[68:71], v[84:87], v[44:47]\ns_xor_b32 s81, s81, s69\ns_mov_b32 s68, 0\ns_waitcnt lgkmcnt(0)\n" \
    "v_mfma_f32_16x16x32_bf16 v[48:51], v[108:111], v[72:75], v[48:51]\nv_mfma_f32_16x16x32_bf16 v[52:55], v[108:111], v[80:83], v[52:55]\n" \
    "ds_read_b128 v[68:71], v3 offset:23552\nv_mfma_f32_16x16x32_bf16 v[56:59], v[116:119], v[72:75], v[56:59]\nds_read_b128 v[76:79], v10 offset:1024\n" \
    "v_mfma_f32_16x16x32_bf16 v[60:63], v[116:119], v[80:83], v[60:63]\nds_read_b128 v[84:87], v10 offset:5120\n" \
    "v_mfma_f32_16x16x32_bf16 v[32:35], v[100:103], v[72:75], v[32:35]\nv_mfma_f32_16x16x32_bf16 v[36:39], v[100:103], v[80:83], v[36:39]\n" \
    "v_mfma_f32_16x16x32_bf16 v[40:43], v[64:67], v[72:75], v[40:43]\nv_mfma_f32_16x16x32_bf16 v[44:47], v[64:67], v[80:83], v[44:47]\ns_waitcnt lgkmcnt(0)\n" \
    "v_mfma_f32_16x16x32_bf16 v[48:51], v[112:115], v[76:79], v[48:51]\nv_mfma_f32_16x16x32_bf16 v[52:55], v[112:115], v[84:87], v[52:55]\n" \
    "v_mfma_f32_16x16x32_bf16 v[56:59], v[120:123], v[76:79], v[56:59]\nds_read_b128 v[64:67], v9 offset:8192\n" \
    "v_mfma_f32_16x16x32_bf16 v[60:63], v[120:123], v[84:87], v[60:63]\nds_read_b128 v[72:75], v9 offset:12288\n" \
    "v_mfma_f32_16x16x32_bf16 v[32:35], v[104:107], v[76:79], v[32:35]\nv_mfma_f32_16x16x32_bf16 v[36:39], v[104:107], v[84:87], v[36:39]\n" \
    "v_mfma_f32_16x16x32_bf16 v[40:43], v[68:71], v[76:79], v[40:43]\ns_waitcnt vmcnt(3)\nv_and_b32 v18, 255, v18\nv_and_b32 v19, 255, v19\n" \
    "v_mfma_f32_16x16x32_bf16 v[44:47], v[68:71], v[84:87], v[44:47]\ns_waitcnt lgkmcnt(0)\nv_max_u32 v18, v18, v19\n" \
    "v_mfma_f32_16x16x32_bf16 v[48:51], v[140:143], v[64:67], v[48:51]\nv_readfirstlane_b32 s20, v18\nv_mfma_f32_16x16x32_bf16 v[52:55], v[140:143], v[72:75], v[52:55]\n" \
    "s_add_u32 s21, s20, s49\ns_andn2_b32 s21, s21, 3\nv_mfma_f32_16x16x32_bf16 v[56:59], v[148:151], v[64:67], v[56:59]\nds_read_b128 v[68:71], v10 offset:8192\n" \
    "s_add_u32 s22, s18, 4\nv_mfma_f32_16x16x32_bf16 v[60:63], v[148:151], v[72:75], v[60:63]\nds_read_b128 v[76:79], v10 offset:12288\ns_min_u32 s21, s21, s22\n" \
    "v_mfma_f32_16x16x32_bf16 v[32:35], v[124:127], v[64:67], v[32:35]\ns_max_u32 s21, s21, 128\ns_cmp_eq_u32 s20, 0\ns_cselect_b32 s18, s18, s21\n" \
    "v_mfma_f32_16x16x32_bf16 v[36:39], v[124:127], v[72:75], v[36:39]\ns_cmp_lt_u32 s35, 4\ns_cselect_b32 s20, s44, 0\n" \
    "v_mfma_f32_16x16x32_bf16 v[40:43], v[132:135], v[64:67], v[40:43]\ns_max_u32 s18, s18, s20\nv_mfma_f32_16x16x32_bf16 v[44:47], v[132:135], v[72:75], v[44:47]\n" \
    "s_waitcnt lgkmcnt(0)\nv_mfma_f32_16x16x32_bf16 v[48:51], v[144:147], v[68:71], v[48:51]\nv_mfma_f32_16x16x32_bf16 v[52:55], v[144:147], v[76:79], v[52:55]\n" \
    "v_mfma_f32_16x16x32_bf16 v[56:59], v[152:155], v[68:71], v[56:59]\nds_read_b128 v[64:67], v9 offset:9216\n" \
    "v_mfma_f32_16x16x32_bf16 v[60:63], v[152:155], v[76:79], v[60:63]\nds_read_b128 v[72:75], v9 offset:13312\n" \
    "v_mfma_f32_16x16x32_bf16 v[32:35], v[128:131], v[68:71], v[32:35]\nv_mfma_f32_16x16x32_bf16 v[36:39], v[128:131], v[76:79], v[36:39]\n" \
    "v_mfma_f32_16x16x32_bf16 v[40:43], v[136:139], v[68:71], v[40:43]\nv_mfma_f32_16x16x32_bf16 v[44:47], v[136:139], v[76:79], v[44:47]\ns_waitcnt lgkmcnt(0)\n" \
    "v_mfma_f32_16x16x32_bf16 v[48:51], v[172:175], v[64:67], v[48:51]\ns_waitcnt vmcnt(0)\nv_readfirstlane_b32 s74, v13\n" \
    "v_mfma_f32_16x16x32_bf16 v[52:55], v[172:175], v[72:75], v[52:55]\ns_and_b32 s75, s74, 127\ns_lshr_b32 s69, s74, 8\n" \
    "v_mfma_f32_16x16x32_bf16 v[56:59], v[180:183], v[64:67], v[56:59]\nds_read_b128 v[68:71], v10 offset:9216\ns_sub_u32 s69, s69, s80\n" \
    "v_mfma_f32_16x16x32_bf16 v[60:63], v[180:183], v[72:75], v[60:63]\nds_read_b128 v[76:79], v10 offset:13312\ns_bfe_i32 s69, s69, 0x180000\n" \
    "v_mfma_f32_16x16x32_bf16 v[32:35], v[156:159], v[64:67], v[32:35]\ns_cmp_eq_u32 s75, 0\ns_cselect_b32 s69, 0, s69\n" \
    "v_mfma_f32_16x16x32_bf16 v[36:39], v[156:159], v[72:75], v[36:39]\ns_add_u32 s80, s80, s69\ns_lshl_b32 s74, s74, 5\n" \
    "v_mfma_f32_16x16x32_bf16 v[40:43], v[164:167], v[64:67], v[40:43]\ns_and_b32 s74, s74, 0x1000\ns_andn2_b32 s81, s81, 0x1000\n" \
    "v_mfma_f32_16x16x32_bf16 v[44:47], v[164:167], v[72:75], v[44:47]\ns_or_b32 s81, s81, s74\ns_lshl_b32 s74, s74, 2\ns_waitcnt lgkmcnt(0)\n" \
    "v_mfma_f32_16x16x32_bf16 v[48:51], v[176:179], v[68:71], v[48:51]\ns_andn2_b32 s82, s82, 0x4000\ns_or_b32 s82, s82, s74\n" \
    "v_mfma_f32_16x16x32_bf16 v[52:55], v[176:179], v[76:79], v[52:55]\ns_barrier\nv_mfma_f32_16x16x32_bf16 v[56:59], v[184:187], v[68:71], v[56:59]\n" \
    "ds_read_b128 v[64:67], v9 offset:16384\nv_mfma_f32_16x16x32_bf16 v[60:63], v[184:187], v[76:79], v[60:63]\nds_read_b128 v[72:75], v9 offset:20480\ns_add_u32 m0, s41, 0\n" \
    "v_mfma_f32_16x16x32_bf16 v[32:35], v[160:163], v[68:71], v[32:35]\ns_xor_b32 s85, s62, 0\ns_add_u32 s85, s85, s84\n" \
    "v_mfma_f32_16x16x32_bf16 v[36:39], v[160:163], v[76:79], v[36:39]\nbuffer_load_dwordx4 v11, s[64:67], s85 offen sc1 lds\n" \
    "v_mfma_f32_16x16x32_bf16 v[40:43], v[168:171], v[68:71], v[40:43]\nv_mfma_f32_16x16x32_bf16 v[44:47], v[168:171], v[76:79], v[44:47]\ns_waitcnt lgkmcnt(0)\n" \
    "v_mfma_f32_16x16x32_bf16 v[48:51], v[204:207], v[64:67], v[48:51]\nbuffer_load_dwordx4 v12, s[64:67], s85 offen offset:1024 sc1 lds\n" \
    "v_mfma_f32_16x16x32_bf16 v[52:55], v[204:207], v[72:75], v[52:55]\nv_mfma_f32_16x16x32_bf16 v[56:59], v[212:215], v[64:67], v[56:59]\n" \
    "ds_read_b128 v[68:71], v10 offset:16384\nv_mfma_f32_16x16x32_bf16 v[60:63], v[212:215], v[72:75], v[60:63]\nds_read_b128 v[76:79], v10 offset:20480\n" \
    "v_mfma_f32_16x16x32_bf16 v[32:35], v[188:191], v[64:67], v[32:35]\nv_mfma_f32_16x16x32_bf16 v[36:39], v[188:191], v[72:75], v[36:39]\n" \
    "v_mfma_f32_16x16x32_bf16 v[40:43], v[196:199], v[64:67], v[40:43]\nv_mfma_f32_16x16x32_bf16 v[44:47], v[196:199], v[72:75], v[44:47]\ns_waitcnt lgkmcnt(0)\n" \
    "v_mfma_f32_16x16x32_bf16 v[48:51], v[208:211], v[68:71], v[48:51]\nv_mfma_f32_16x16x32_bf16 v[52:55], v[208:211], v[76:79], v[52:55]\n" \
    "v_mfma_f32_16x16x32_bf16 v[56:59], v[216:219], v[68:71], v[56:59]\nds_read_b128 v[64:67], v9 offset:17408\n" \
    "v_mfma_f32_16x16x32_bf16 v[60:63], v[216:219], v[76:79], v[60:63]\nds_read_b128 v[72:75], v9 offset:21504\n" \
    "v_mfma_f32_16x16x32_bf16 v[32:35], v[192:195], v[68:71], v[32:35]\nv_mfma_f32_16x16x32_bf16 v[36:39], v[192:195], v[76:79], v[36:39]\n" \
    "v_mfma_f32_16x16x32_bf16 v[40:43], v[200:203], v[68:71], v[40:43]\nds_read_b128 v[68:71], v10 offset:17408\n" \
    "v_mfma_f32_16x16x32_bf16 v[44:47], v[200:203], v[76:79], v[44:47]\nds_read_b128 v[76:79], v10 offset:21504\ns_waitcnt lgkmcnt(2)\n" \
    "v_mfma_f32_16x16x32_bf16 v[48:51], v[236:239], v[64:67], v[48:51]\nv_mfma_f32_16x16x32_bf16 v[52:55], v[236:239], v[72:75], v[52:55]\n" \
    "v_mfma_f32_16x16x32_bf16 v[56:59], v[244:247], v[64:67], v[56:59]\nv_mfma_f32_16x16x32_bf16 v[60:63], v[244:247], v[72:75], v[60:63]\ns_waitcnt lgkmcnt(0)\ns_barrier\n" \
    "v_mfma_f32_16x16x32_bf16 v[48:51], v[240:243], v[68:71], v[48:51]\ns_add_u32 m0, s41, 8192\ns_xor_b32 s85, s62, 256\n" \
    "v_mfma_f32_16x16x32_bf16 v[52:55], v[240:243], v[76:79], v[52:55]\ns_add_u32 s85, s85, s84\nbuffer_load_dwordx4 v11, s[64:67], s85 offen sc1 lds\n" \
    "v_mfma_f32_16x16x32_bf16 v[56:59], v[248:251], v[68:71], v[56:59]\nv_mfma_f32_16x16x32_bf16 v[60:63], v[248:251], v[76:79], v[60:63]\ns_nop 0\n" \
    "v_cvt_pk_bf16_f32 v16, v48, v49\nv_cvt_pk_bf16_f32 v17, v50, v51\nv_mfma_f32_16x16x32_bf16 v[32:35], v[220:223], v[64:67], v[32:35]\n" \
    "buffer_load_dwordx4 v12, s[64:67], s85 offen offset:1024 sc1 lds\ns_add_u32 m0, s41, 16384\nv_mfma_f32_16x16x32_bf16 v[36:39], v[220:223], v[72:75], v[36:39]\n" \
    "s_xor_b32 s85, s62, 512\nv_cvt_pk_bf16_f32 v18, v56, v57\nv_cvt_pk_bf16_f32 v19, v58, v59\nv_mfma_f32_16x16x32_bf16 v[40:43], v[228:231], v[64:67], v[40:43]\n" \
    "ds_write_b128 v14, v[16:19] offset:0\nv_cvt_pk_bf16_f32 v20, v52, v53\nv_cvt_pk_bf16_f32 v21, v54, v55\nv_mfma_f32_16x16x32_bf16 v[44:47], v[228:231], v[72:75], v[44:47]\n" \
    "s_add_u32 s85, s85, s84\nv_mfma_f32_16x16x32_bf16 v[32:35], v[224:227], v[68:71], v[32:35]\nv_cvt_pk_bf16_f32 v22, v60, v61\nv_cvt_pk_bf16_f32 v23, v62, v63\n" \
    "v_mfma_f32_16x16x32_bf16 v[36:39], v[224:227], v[76:79], v[36:39]\nds_write_b128 v14, v[20:23] offset:4096\n" \
    "v_mfma_f32_16x16x32_bf16 v[40:43], v[232:235], v[68:71], v[40:43]\nbuffer_load_dwordx4 v11, s[64:67], s85 offen sc1 lds\n" \
    "v_mfma_f32_16x16x32_bf16 v[44:47], v[232:235], v[76:79], v[44:47]\nbuffer_load_dwordx4 v12, s[64:67], s85 offen offset:1024 sc1 lds\n.Lq10_%=:\ns_cmp_lt_u32 s75, s35\n" \
    "s_cbranch_scc1 .Lq11_%=\ns_cmp_eq_u32 s36, 1\ns_cbranch_scc1 .Lq5_%=\ns_cmp_eq_u32 s36, 2\ns_cbranch_scc1 .Lq6_%=\ns_cmp_eq_u32 s36, 3\ns_cbranch_scc1 .Lq7_%=\n.Lq4_%=:\n" \
    ".Lq13_%=:\ns_memrealtime s[72:73]\ns_waitcnt lgkmcnt(0)\ns_mov_b32 s68, 0\ns_sub_u32 s76, s80, s45\ns_sub_u32 s69, s72, s76\ns_cmp_lt_i32 s69, -8\ns_cbranch_scc1 .Lq16_%=\n" \
    "s_xor_b32 s24, s81, 0\ns_xor_b32 s25, s81, 1024\ns_xor_b32 s26, s81, 2048\ns_xor_b32 s27, s81, 3072\n.Lq18_%=:\ns_memrealtime s[72:73]\ns_waitcnt lgkmcnt(0)\n" \
    "s_sub_u32 s72, s72, s80\ns_cmp_lt_i32 s72, 0\ns_cbranch_scc1 .Lq18_%=\nbuffer_load_dwordx4 v[72:75], v2, s[64:67], s24 offen sc1\nv_cvt_pk_bf16_f32 v24, v32, v33\n" \
    "v_cvt_pk_bf16_f32 v25, v34, v35\nv_cvt_pk_bf16_f32 v26, v40, v41\nbuffer_load_dwordx4 v[76:79], v2, s[64:67], s25 offen sc1\nv_cvt_pk_bf16_f32 v27, v42, v43\n" \
    "ds_write_b128 v14, v[24:27] offset:8192\nv_cvt_pk_bf16_f32 v28, v36, v37\nbuffer_load_dwordx4 v[80:83], v2, s[64:67], s26 offen sc1\nv_cvt_pk_bf16_f32 v29, v38, v39\n" \
    "v_cvt_pk_bf16_f32 v30, v44, v45\nv_cvt_pk_bf16_f32 v31, v46, v47\nbuffer_load_dwordx4 v[84:87], v2, s[64:67], s27 offen sc1\nds_write_b128 v14, v[28:31] offset:12288\n" \
    "ds_read_b128 v[32:35], v3 offset:0\nds_read_b128 v[36:39], v3 offset:1024\nds_read_b128 v[40:43], v3 offset:8192\nds_read_b128 v[44:47], v3 offset:9216\n" \
    "ds_read_b128 v[48:51], v3 offset:16384\nds_read_b128 v[52:55], v3 offset:17408\nds_read_b128 v[56:59], v3 offset:2048\nds_read_b128 v[60:63], v3 offset:3072\n" \
    "ds_read_b128 v[64:67], v3 offset:4096\nds_read_b128 v[68:71], v3 offset:5120\n.Lq17_%=:\nv_mov_b64 v[16:17], 0\nv_mov_b64 v[18:19], 0\nv_mov_b64 v[20:21], 0\n" \
    "v_mov_b64 v[22:23], 0\nv_mov_b64 v[24:25], 0\nv_mov_b64 v[26:27], 0\nv_mov_b64 v[28:29], 0\nv_mov_b64 v[30:31], 0\ns_waitcnt vmcnt(3) lgkmcnt(8)\n" \
    "v_smfmac_f32_16x16x64_bf16 v[20:23], v[72:75], v[32:39], v0\nds_write_b128 v1, v[72:75] offset:24576\nv_max3_u32 v252, v72, v73, v74\n" \
    "v_smfmac_f32_16x16x64_bf16 v[16:19], v[72:75], a[0:7], v0\nv_smfmac_f32_16x16x64_bf16 v[24:27], v[72:75], a[8:15], v0\n" \
    "v_smfmac_f32_16x16x64_bf16 v[28:31], v[72:75], a[16:23], v0\ns_waitcnt vmcnt(2)\nds_write_b128 v1, v[76:79] offset:28672\ns_waitcnt lgkmcnt(8)\n" \
    "v_max3_u32 v252, v252, v75, v76\nv_smfmac_f32_16x16x64_bf16 v[20:23], v[76:79], v[40:47], v0\nv_max3_u32 v252, v252, v77, v78\n" \
    "v_smfmac_f32_16x16x64_bf16 v[16:19], v[76:79], a[96:103], v0\nv_cmp_u_f32 vcc, v252, v79\nv_smfmac_f32_16x16x64_bf16 v[24:27], v[76:79], a[104:111], v0\n" \
    "s_cbranch_vccnz .Lq24_%=\n.Lq20_%=:\nv_smfmac_f32_16x16x64_bf16 v[28:31], v[76:79], a[112:119], v0\nds_read_b128 v[36:39], v3 offset:7168\ns_waitcnt vmcnt(1)\n" \
    "v_max3_u32 v252, v80, v81, v82\nv_cmp_u_f32 vcc, v252, v83\ns_nop 0\ns_cbranch_vccnz .Lq25_%=\n.Lq21_%=:\ns_waitcnt lgkmcnt(7)\n" \
    "v_smfmac_f32_16x16x64_bf16 v[20:23], v[80:83], v[48:55], v0\nds_write_b128 v1, v[80:83] offset:32768\nv_smfmac_f32_16x16x64_bf16 v[16:19], v[80:83], a[192:199], v0\n" \
    "ds_read_b128 v[32:35], v3 offset:6144\ns_waitcnt lgkmcnt(3)\ns_barrier\nds_read_b128 v[88:91], v1 offset:25600\n" \
    "v_smfmac_f32_16x16x64_bf16 v[24:27], v[80:83], a[200:207], v0\nds_read_b128 v[40:43], v3 offset:10240\nv_smfmac_f32_16x16x64_bf16 v[28:31], v[80:83], a[208:215], v0\n" \
    "ds_read_b128 v[48:51], v1 offset:26624\ns_waitcnt vmcnt(0)\nv_max3_u32 v252, v84, v85, v86\nv_cmp_u_f32 vcc, v252, v87\ns_nop 0\ns_cbranch_vccnz .Lq26_%=\n.Lq22_%=:\n" \
    "v_smfmac_f32_16x16x64_bf16 v[20:23], v[84:87], v[132:139], v0\nds_write_b128 v1, v[84:87] offset:36864\nv_smfmac_f32_16x16x64_bf16 v[16:19], v[84:87], v[124:131], v0\n" \
    "ds_read_b128 v[52:55], v1 offset:27648\nv_smfmac_f32_16x16x64_bf16 v[24:27], v[84:87], v[140:147], v0\nds_read_b128 v[72:75], v1 offset:29696\n" \
    "v_smfmac_f32_16x16x64_bf16 v[28:31], v[84:87], v[148:155], v0\nds_read_b128 v[44:47], v3 offset:11264\ns_waitcnt lgkmcnt(6)\n" \
    "v_smfmac_f32_16x16x64_bf16 v[20:23], v[88:91], v[56:63], v0\nds_read_b128 v[56:59], v1 offset:30720\nv_smfmac_f32_16x16x64_bf16 v[16:19], v[88:91], a[24:31], v0\n" \
    "ds_read_b128 v[76:79], v3 offset:12288\nv_smfmac_f32_16x16x64_bf16 v[24:27], v[88:91], a[32:39], v0\nds_read_b128 v[80:83], v3 offset:13312\n" \
    "v_smfmac_f32_16x16x64_bf16 v[28:31], v[88:91], a[40:47], v0\nds_read_b128 v[60:63], v1 offset:31744\ns_waitcnt lgkmcnt(7)\ns_barrier\n" \
    "v_smfmac_f32_16x16x64_bf16 v[20:23], v[48:51], v[64:71], v0\nds_read_b128 v[64:67], v3 offset:14336\nv_smfmac_f32_16x16x64_bf16 v[16:19], v[48:51], a[48:55], v0\n" \
    "ds_read_b128 v[68:71], v3 offset:15360\ns_waitcnt lgkmcnt(8)\nv_smfmac_f32_16x16x64_bf16 v[20:23], v[52:55], v[32:39], v0\nds_read_b128 v[32:35], v1 offset:33792\n" \
    "v_smfmac_f32_16x16x64_bf16 v[16:19], v[52:55], a[72:79], v0\nds_read_b128 v[84:87], v3 offset:18432\ns_waitcnt lgkmcnt(8)\n" \
    "v_smfmac_f32_16x16x64_bf16 v[20:23], v[72:75], v[40:47], v0\nds_read_b128 v[88:91], v3 offset:19456\nv_smfmac_f32_16x16x64_bf16 v[16:19], v[72:75], a[120:127], v0\n" \
    "ds_read_b128 v[36:39], v1 offset:34816\ns_waitcnt lgkmcnt(7)\nv_smfmac_f32_16x16x64_bf16 v[20:23], v[56:59], v[76:83], v0\nds_read_b128 v[40:43], v3 offset:20480\n" \
    "v_smfmac_f32_16x16x64_bf16 v[16:19], v[56:59], a[144:151], v0\nds_read_b128 v[44:47], v3 offset:21504\ns_waitcnt lgkmcnt(6)\n" \
    "v_smfmac_f32_16x16x64_bf16 v[20:23], v[60:63], v[64:71], v0\nds_read_b128 v[64:67], v1 offset:35840\nv_smfmac_f32_16x16x64_bf16 v[16:19], v[60:63], a[168:175], v0\n" \
    "ds_read_b128 v[76:79], v3 offset:22528\ns_waitcnt lgkmcnt(5)\nv_smfmac_f32_16x16x64_bf16 v[20:23], v[32:35], v[84:91], v0\nds_read_b128 v[80:83], v3 offset:23552\n" \
    "v_smfmac_f32_16x16x64_bf16 v[16:19], v[32:35], a[216:223], v0\nds_read_b128 v[68:71], v1 offset:37888\ns_waitcnt lgkmcnt(4)\n" \
    "v_smfmac_f32_16x16x64_bf16 v[20:23], v[36:39], v[40:47], v0\nds_read_b128 v[40:43], v1 offset:38912\nv_smfmac_f32_16x16x64_bf16 v[16:19], v[36:39], a[240:247], v0\n" \
    "ds_read_b128 v[44:47], v1 offset:39936\ns_waitcnt lgkmcnt(3)\nv_smfmac_f32_16x16x64_bf16 v[20:23], v[64:67], v[76:83], v0\n" \
    "v_smfmac_f32_16x16x64_bf16 v[16:19], v[64:67], v[100:107], v0\ns_waitcnt lgkmcnt(2)\nv_smfmac_f32_16x16x64_bf16 v[20:23], v[68:71], v[164:171], v0\ns_waitcnt lgkmcnt(1)\n" \
    "v_smfmac_f32_16x16x64_bf16 v[20:23], v[40:43], v[196:203], v0\ns_waitcnt lgkmcnt(0)\nv_smfmac_f32_16x16x64_bf16 v[20:23], v[44:47], v[228:235], v0\ns_nop 2\n" \
    "v_smfmac_f32_16x16x64_bf16 v[16:19], v[68:71], v[156:163], v0\ns_nop 2\nv_smfmac_f32_16x16x64_bf16 v[16:19], v[40:43], v[188:195], v0\nv_permlane32_swap_b32 v20, v22\n" \
    "v_smfmac_f32_16x16x64_bf16 v[16:19], v[44:47], v[220:227], v0\ns_nop 1\nv_permlane32_swap_b32 v21, v23\nv_smfmac_f32_16x16x64_bf16 v[24:27], v[48:51], a[56:63], v0\n" \
    "s_nop 1\nv_add_f32 v20, v20, v22\nv_smfmac_f32_16x16x64_bf16 v[28:31], v[48:51], a[64:71], v0\nv_add_f32 v21, v21, v23\n" \
    "v_smfmac_f32_16x16x64_bf16 v[24:27], v[52:55], a[80:87], v0\nv_mul_f32 v20, s32, v20\nv_smfmac_f32_16x16x64_bf16 v[28:31], v[52:55], a[88:95], v0\nv_mul_f32 v21, s32, v21\n" \
    "v_smfmac_f32_16x16x64_bf16 v[24:27], v[72:75], a[128:135], v0\nv_cvt_pk_bf16_f32 v20, v20, v21\nv_smfmac_f32_16x16x64_bf16 v[28:31], v[72:75], a[136:143], v0\n" \
    "ds_write_b16 v4, v20 offset:8\nv_permlane32_swap_b32 v16, v18\nv_smfmac_f32_16x16x64_bf16 v[24:27], v[56:59], a[152:159], v0\nds_write_b16_d16_hi v4, v20 offset:24\n" \
    "v_permlane32_swap_b32 v17, v19\nv_smfmac_f32_16x16x64_bf16 v[24:27], v[60:63], a[176:183], v0\nds_read_b128 v[48:51], v1 offset:40960\nv_add_f32 v16, v16, v18\n" \
    "v_smfmac_f32_16x16x64_bf16 v[24:27], v[32:35], a[224:231], v0\nds_read_b128 v[52:55], v1 offset:45056\nv_add_f32 v17, v17, v19\n" \
    "v_smfmac_f32_16x16x64_bf16 v[24:27], v[36:39], a[248:255], v0\nds_read_b128 v[72:75], v1 offset:41984\nv_mul_f32 v16, s32, v16\n" \
    "v_smfmac_f32_16x16x64_bf16 v[24:27], v[64:67], v[108:115], v0\nds_read_b128 v[76:79], v1 offset:46080\nv_mul_f32 v17, s32, v17\n" \
    "v_smfmac_f32_16x16x64_bf16 v[24:27], v[68:71], v[172:179], v0\nds_read_b128 v[80:83], v1 offset:43008\nv_cvt_pk_bf16_f32 v16, v16, v17\n" \
    "v_smfmac_f32_16x16x64_bf16 v[24:27], v[40:43], v[204:211], v0\nds_write_b16 v4, v16 offset:0\nv_smfmac_f32_16x16x64_bf16 v[24:27], v[44:47], v[236:243], v0\n" \
    "ds_write_b16_d16_hi v4, v16 offset:16\ns_nop 1\nv_smfmac_f32_16x16x64_bf16 v[28:31], v[56:59], a[160:167], v0\nds_read_b128 v[56:59], v1 offset:47104\ns_nop 1\n" \
    "v_smfmac_f32_16x16x64_bf16 v[28:31], v[60:63], a[184:191], v0\nds_read_b128 v[60:63], v1 offset:44032\nv_permlane32_swap_b32 v24, v26\n" \
    "v_smfmac_f32_16x16x64_bf16 v[28:31], v[32:35], a[232:239], v0\nds_read_b128 v[32:35], v1 offset:48128\nv_permlane32_swap_b32 v25, v27\n" \
    "v_smfmac_f32_16x16x64_bf16 v[28:31], v[36:39], v[92:99], v0\nds_read_b128 v[36:39], v6 offset:0\nv_add_f32 v24, v24, v26\n" \
    "v_smfmac_f32_16x16x64_bf16 v[28:31], v[64:67], v[116:123], v0\nds_read_b128 v[64:67], v6 offset:512\nv_add_f32 v25, v25, v27\n" \
    "v_smfmac_f32_16x16x64_bf16 v[28:31], v[68:71], v[180:187], v0\nds_read_b128 v[68:71], v6 offset:1024\nv_cvt_pk_bf16_f32 v24, v24, v25\n" \
    "v_smfmac_f32_16x16x64_bf16 v[28:31], v[40:43], v[212:219], v0\nds_read_b128 v[40:43], v6 offset:1536\nv_smfmac_f32_16x16x64_bf16 v[28:31], v[44:47], v[244:251], v0\n" \
    "ds_write_b16 v4, v24 offset:2048\nds_write_b16_d16_hi v4, v24 offset:2064\ns_waitcnt lgkmcnt(5)\nv_mfma_f32_16x16x32_bf16 v[16:19], v[48:51], v[36:39], 0\ns_nop 2\n" \
    "v_mfma_f32_16x16x32_bf16 v[20:23], v[52:55], v[36:39], 0\nv_permlane32_swap_b32 v28, v30\nv_permlane32_swap_b32 v29, v31\nv_add_f32 v28, v28, v30\nv_add_f32 v29, v29, v31\n" \
    "v_cvt_pk_bf16_f32 v28, v28, v29\nds_write_b16 v4, v28 offset:2056\nds_write_b16_d16_hi v4, v28 offset:2072\ns_waitcnt lgkmcnt(6)\n" \
    "v_mfma_f32_16x16x32_bf16 v[16:19], v[72:75], v[64:67], v[16:19]\ns_cmp_eq_u32 s35, 63\ns_cbranch_scc1 .Lq8_%=\n" \
    "v_mfma_f32_16x16x32_bf16 v[20:23], v[76:79], v[64:67], v[20:23]\ns_waitcnt lgkmcnt(5)\nv_mfma_f32_16x16x32_bf16 v[16:19], v[80:83], v[68:71], v[16:19]\n" \
    "ds_read_b128 v[44:47], v8 offset:0\nds_read_b128 v[84:87], v8 offset:512\nv_mfma_f32_16x16x32_bf16 v[20:23], v[56:59], v[68:71], v[20:23]\n" \
    "ds_read_b128 v[88:91], v8 offset:1024\nds_read_b128 v[28:31], v8 offset:1536\ns_waitcnt lgkmcnt(8)\nv_mfma_f32_16x16x32_bf16 v[16:19], v[60:63], v[40:43], v[16:19]\n" \
    "v_mfma_f32_16x16x32_bf16 v[20:23], v[32:35], v[40:43], v[20:23]\nv_mov_b32 v15, v15\nv_mov_b32 v15, v15\ns_waitcnt lgkmcnt(3)\n" \
    "v_mfma_f32_16x16x32_bf16 v[24:27], v[44:47], v[36:39], 0\ns_nop 2\ns_waitcnt lgkmcnt(2)\nv_mov_b32_dpp v16, v20 quad_perm:[0,1,2,3] row_mask:0xf bank_mask:0xc\n" \
    "v_mov_b32_dpp v17, v21 quad_perm:[0,1,2,3] row_mask:0xf bank_mask:0xc\nv_mfma_f32_16x16x32_bf16 v[24:27], v[84:87], v[64:67], v[24:27]\ns_waitcnt lgkmcnt(1)\n" \
    "v_mov_b32_dpp v18, v22 quad_perm:[0,1,2,3] row_mask:0xf bank_mask:0xc\nv_mov_b32_dpp v19, v23 quad_perm:[0,1,2,3] row_mask:0xf bank_mask:0xc\n" \
    "v_mfma_f32_16x16x32_bf16 v[24:27], v[88:91], v[68:71], v[24:27]\ns_waitcnt lgkmcnt(0)\nv_max3_f32 v254, v16, v17, v18\nv_max_f32 v254, v254, v19\n" \
    "v_mfma_f32_16x16x32_bf16 v[24:27], v[28:31], v[40:43], v[24:27]\ns_nop 0\nv_max_f32_dpp v252, v254, v254 row_ror:8 row_mask:0xf bank_mask:0xf\n" \
    "v_max_f32_dpp v253, v254, v254 row_ror:8 row_mask:0xf bank_mask:0xf\ns_nop 1\nv_permlane32_swap_b32 v252, v253\nv_max_f32 v252, v252, v253\ns_xor_b32 s74, s82, 0x4000\n" \
    "v_max3_f32 v254, v252, v24, v25\nv_max3_f32 v252, v254, v26, v27\nv_max3_f32 v253, v254, v26, v27\ns_nop 1\nv_permlane16_swap_b32 v252, v253\nv_max_f32 v252, v252, v253\n" \
    "v_pk_add_f32 v[16:17], v[16:17], v[252:253] op_sel_hi:[1,0] neg_lo:[0,1] neg_hi:[0,1]\n" \
    "v_pk_add_f32 v[18:19], v[18:19], v[252:253] op_sel_hi:[1,0] neg_lo:[0,1] neg_hi:[0,1]\n" \
    "v_pk_add_f32 v[24:25], v[24:25], v[252:253] op_sel_hi:[1,0] neg_lo:[0,1] neg_hi:[0,1]\n" \
    "v_pk_add_f32 v[26:27], v[26:27], v[252:253] op_sel_hi:[1,0] neg_lo:[0,1] neg_hi:[0,1]\nv_exp_f32 v16, v16\nv_exp_f32 v17, v17\nv_exp_f32 v18, v18\nv_exp_f32 v19, v19\n" \
    "v_exp_f32 v24, v24\nv_pk_add_f32 v[254:255], v[16:17], v[18:19]\nv_exp_f32 v25, v25\nv_add_f32 v254, v254, v255\nv_exp_f32 v26, v26\nv_exp_f32 v27, v27\n" \
    "v_add_f32_dpp v252, v254, v254 row_ror:8 row_mask:0xf bank_mask:0xf\nv_add_f32_dpp v253, v254, v254 row_ror:8 row_mask:0xf bank_mask:0xf\n" \
    "v_pk_add_f32 v[254:255], v[24:25], v[26:27]\ns_nop 0\nv_permlane32_swap_b32 v252, v253\nv_pk_add_f32 v[254:255], v[254:255], v[252:253]\n" \
    "v_pk_add_f32 v[252:253], v[254:255], v[254:255] op_sel:[0,1] op_sel_hi:[1,0]\ns_nop 1\nv_permlane16_swap_b32 v252, v253\nv_add_f32 v252, v252, v253\n" \
    "v_cmp_u_f32 vcc, v252, v252\nv_rcp_f32 v252, v252\ns_cbranch_vccnz .Lq12_%=\nv_pk_mul_f32 v[16:17], v[16:17], v[252:253] op_sel_hi:[1,0]\n" \
    "v_pk_mul_f32 v[24:25], v[24:25], v[252:253] op_sel_hi:[1,0]\nv_cvt_pk_bf16_f32 v20, v16, v17\nv_cvt_pk_bf16_f32 v22, v24, v25\n" \
    "v_pk_mul_f32 v[18:19], v[18:19], v[252:253] op_sel_hi:[1,0]\nv_pk_mul_f32 v[26:27], v[26:27], v[252:253] op_sel_hi:[1,0]\nv_cvt_pk_bf16_f32 v21, v18, v19\n" \
    "v_cvt_pk_bf16_f32 v23, v26, v27\nv_permlane32_swap_b32 v20, v22\ns_nop 0\nv_permlane32_swap_b32 v21, v23\nbuffer_store_dwordx4 v[20:23], v7, s[64:67], s82 offen sc1\n" \
    "buffer_store_dwordx4 v[20:23], v7, s[64:67], s74 offen sc1\ns_branch .Lq3_%=\n.Lq5_%=:\n.Lq32_%=:\ns_memrealtime s[72:73]\ns_waitcnt lgkmcnt(0)\ns_mov_b32 s68, 0\n" \
    "s_sub_u32 s76, s80, s45\ns_sub_u32 s69, s72, s76\ns_cmp_lt_i32 s69, -8\ns_cbranch_scc1 .Lq35_%=\ns_xor_b32 s24, s81, 0\ns_xor_b32 s25, s81, 1024\ns_xor_b32 s26, s81, 2048\n" \
    "s_xor_b32 s27, s81, 3072\n.Lq37_%=:\ns_memrealtime s[72:73]\ns_waitcnt lgkmcnt(0)\ns_sub_u32 s72, s72, s80\ns_cmp_lt_i32 s72, 0\ns_cbranch_scc1 .Lq37_%=\n" \
    "buffer_load_dwordx4 v[72:75], v2, s[64:67], s24 offen sc1\nv_cvt_pk_bf16_f32 v24, v32, v33\nv_cvt_pk_bf16_f32 v25, v34, v35\nv_cvt_pk_bf16_f32 v26, v40, v41\n" \
    "buffer_load_dwordx4 v[76:79], v2, s[64:67], s25 offen sc1\nv_cvt_pk_bf16_f32 v27, v42, v43\nds_write_b128 v14, v[24:27] offset:8192\nv_cvt_pk_bf16_f32 v28, v36, v37\n" \
    "buffer_load_dwordx4 v[80:83], v2, s[64:67], s26 offen sc1\nv_cvt_pk_bf16_f32 v29, v38, v39\nv_cvt_pk_bf16_f32 v30, v44, v45\nv_cvt_pk_bf16_f32 v31, v46, v47\n" \
    "buffer_load_dwordx4 v[84:87], v2, s[64:67], s27 offen sc1\nds_write_b128 v14, v[28:31] offset:12288\nds_read_b128 v[32:35], v3 offset:2048\n" \
    "ds_read_b128 v[36:39], v3 offset:3072\nds_read_b128 v[40:43], v3 offset:10240\nds_read_b128 v[44:47], v3 offset:11264\nds_read_b128 v[48:51], v3 offset:18432\n" \
    "ds_read_b128 v[52:55], v3 offset:19456\nds_read_b128 v[56:59], v3 offset:0\nds_read_b128 v[60:63], v3 offset:1024\nds_read_b128 v[64:67], v3 offset:4096\n" \
    "ds_read_b128 v[68:71], v3 offset:5120\n.Lq36_%=:\nv_mov_b64 v[16:17], 0\nv_mov_b64 v[18:19], 0\nv_mov_b64 v[20:21], 0\nv_mov_b64 v[22:23], 0\nv_mov_b64 v[24:25], 0\n" \
    "v_mov_b64 v[26:27], 0\nv_mov_b64 v[28:29], 0\nv_mov_b64 v[30:31], 0\ns_waitcnt vmcnt(3) lgkmcnt(8)\nv_smfmac_f32_16x16x64_bf16 v[20:23], v[72:75], v[32:39], v0\n" \
    "ds_write_b128 v1, v[72:75] offset:25600\nv_max3_u32 v252, v72, v73, v74\nv_smfmac_f32_16x16x64_bf16 v[16:19], v[72:75], a[24:31], v0\n" \
    "v_smfmac_f32_16x16x64_bf16 v[24:27], v[72:75], a[32:39], v0\nv_smfmac_f32_16x16x64_bf16 v[28:31], v[72:75], a[40:47], v0\ns_waitcnt vmcnt(2)\n" \
    "ds_write_b128 v1, v[76:79] offset:29696\ns_waitcnt lgkmcnt(8)\nv_max3_u32 v252, v252, v75, v76\nv_smfmac_f32_16x16x64_bf16 v[20:23], v[76:79], v[40:47], v0\n" \
    "v_max3_u32 v252, v252, v77, v78\nv_smfmac_f32_16x16x64_bf16 v[16:19], v[76:79], a[120:127], v0\nv_cmp_u_f32 vcc, v252, v79\n" \
    "v_smfmac_f32_16x16x64_bf16 v[24:27], v[76:79], a[128:135], v0\ns_cbranch_vccnz .Lq43_%=\n.Lq39_%=:\nv_smfmac_f32_16x16x64_bf16 v[28:31], v[76:79], a[136:143], v0\n" \
    "ds_read_b128 v[36:39], v3 offset:7168\ns_waitcnt vmcnt(1)\nv_max3_u32 v252, v80, v81, v82\nv_cmp_u_f32 vcc, v252, v83\ns_nop 0\ns_cbranch_vccnz .Lq44_%=\n.Lq40_%=:\n" \
    "s_waitcnt lgkmcnt(7)\nv_smfmac_f32_16x16x64_bf16 v[20:23], v[80:83], v[48:55], v0\nds_write_b128 v1, v[80:83] offset:33792\n" \
    "v_smfmac_f32_16x16x64_bf16 v[16:19], v[80:83], a[216:223], v0\nds_read_b128 v[32:35], v3 offset:6144\ns_waitcnt lgkmcnt(3)\ns_barrier\n" \
    "ds_read_b128 v[88:91], v1 offset:24576\nv_smfmac_f32_16x16x64_bf16 v[24:27], v[80:83], a[224:231], v0\nds_read_b128 v[40:43], v3 offset:8192\n" \
    "v_smfmac_f32_16x16x64_bf16 v[28:31], v[80:83], a[232:239], v0\nds_read_b128 v[48:51], v1 offset:26624\ns_waitcnt vmcnt(0)\nv_max3_u32 v252, v84, v85, v86\n" \
    "v_cmp_u_f32 vcc, v252, v87\ns_nop 0\ns_cbranch_vccnz .Lq45_%=\n.Lq41_%=:\nv_smfmac_f32_16x16x64_bf16 v[20:23], v[84:87], v[164:171], v0\n" \
    "ds_write_b128 v1, v[84:87] offset:37888\nv_smfmac_f32_16x16x64_bf16 v[16:19], v[84:87], v[156:163], v0\nds_read_b128 v[52:55], v1 offset:27648\n" \
    "v_smfmac_f32_16x16x64_bf16 v[24:27], v[84:87], v[172:179], v0\nds_read_b128 v[72:75], v1 offset:28672\nv_smfmac_f32_16x16x64_bf16 v[28:31], v[84:87], v[180:187], v0\n" \
    "ds_read_b128 v[44:47], v3 offset:9216\ns_waitcnt lgkmcnt(6)\nv_smfmac_f32_16x16x64_bf16 v[20:23], v[88:91], v[56:63], v0\nds_read_b128 v[56:59], v1 offset:30720\n" \
    "v_smfmac_f32_16x16x64_bf16 v[16:19], v[88:91], a[0:7], v0\nds_read_b128 v[76:79], v3 offset:12288\nv_smfmac_f32_16x16x64_bf16 v[24:27], v[88:91], a[8:15], v0\n" \
    "ds_read_b128 v[80:83], v3 offset:13312\nv_smfmac_f32_16x16x64_bf16 v[28:31], v[88:91], a[16:23], v0\nds_read_b128 v[60:63], v1 offset:31744\ns_waitcnt lgkmcnt(7)\n" \
    "s_barrier\nv_smfmac_f32_16x16x64_bf16 v[20:23], v[48:51], v[64:71], v0\nds_read_b128 v[64:67], v3 offset:14336\n" \
    "v_smfmac_f32_16x16x64_bf16 v[16:19], v[48:51], a[48:55], v0\nds_read_b128 v[68:71], v3 offset:15360\ns_waitcnt lgkmcnt(8)\n" \
    "v_smfmac_f32_16x16x64_bf16 v[20:23], v[52:55], v[32:39], v0\nds_read_b128 v[32:35], v1 offset:32768\nv_smfmac_f32_16x16x64_bf16 v[16:19], v[52:55], a[72:79], v0\n" \
    "ds_read_b128 v[84:87], v3 offset:16384\ns_waitcnt lgkmcnt(8)\nv_smfmac_f32_16x16x64_bf16 v[20:23], v[72:75], v[40:47], v0\nds_read_b128 v[88:91], v3 offset:17408\n" \
    "v_smfmac_f32_16x16x64_bf16 v[16:19], v[72:75], a[96:103], v0\nds_read_b128 v[36:39], v1 offset:34816\ns_waitcnt lgkmcnt(7)\n" \
    "v_smfmac_f32_16x16x64_bf16 v[20:23], v[56:59], v[76:83], v0\nds_read_b128 v[40:43], v3 offset:20480\nv_smfmac_f32_16x16x64_bf16 v[16:19], v[56:59], a[144:151], v0\n" \
    "ds_read_b128 v[44:47], v3 offset:21504\ns_waitcnt lgkmcnt(6)\nv_smfmac_f32_16x16x64_bf16 v[20:23], v[60:63], v[64:71], v0\nds_read_b128 v[64:67], v1 offset:35840\n" \
    "v_smfmac_f32_16x16x64_bf16 v[16:19], v[60:63], a[168:175], v0\nds_read_b128 v[76:79], v3 offset:22528\ns_waitcnt lgkmcnt(5)\n" \
    "v_smfmac_f32_16x16x64_bf16 v[20:23], v[32:35], v[84:91], v0\nds_read_b128 v[80:83], v3 offset:23552\nv_smfmac_f32_16x16x64_bf16 v[16:19], v[32:35], a[192:199], v0\n" \
    "ds_read_b128 v[68:71], v1 offset:36864\ns_waitcnt lgkmcnt(4)\nv_smfmac_f32_16x16x64_bf16 v[20:23], v[36:39], v[40:47], v0\nds_read_b128 v[40:43], v1 offset:38912\n" \
    "v_smfmac_f32_16x16x64_bf16 v[16:19], v[36:39], a[240:247], v0\nds_read_b128 v[44:47], v1 offset:39936\ns_waitcnt lgkmcnt(3)\n" \
    "v_smfmac_f32_16x16x64_bf16 v[20:23], v[64:67], v[76:83], v0\nv_smfmac_f32_16x16x64_bf16 v[16:19], v[64:67], v[100:107], v0\ns_waitcnt lgkmcnt(2)\n" \
    "v_smfmac_f32_16x16x64_bf16 v[20:23], v[68:71], v[132:139], v0\ns_waitcnt lgkmcnt(1)\nv_smfmac_f32_16x16x64_bf16 v[20:23], v[40:43], v[196:203], v0\ns_waitcnt lgkmcnt(0)\n" \
    "v_smfmac_f32_16x16x64_bf16 v[20:23], v[44:47], v[228:235], v0\ns_nop 2\nv_smfmac_f32_16x16x64_bf16 v[16:19], v[68:71], v[124:131], v0\ns_nop 2\n" \
    "v_smfmac_f32_16x16x64_bf16 v[16:19], v[40:43], v[188:195], v0\nv_permlane32_swap_b32 v20, v22\nv_smfmac_f32_16x16x64_bf16 v[16:19], v[44:47], v[220:227], v0\ns_nop 1\n" \
    "v_permlane32_swap_b32 v21, v23\nv_smfmac_f32_16x16x64_bf16 v[24:27], v[48:51], a[56:63], v0\ns_nop 1\nv_add_f32 v20, v20, v22\n" \
    "v_smfmac_f32_16x16x64_bf16 v[28:31], v[48:51], a[64:71], v0\nv_add_f32 v21, v21, v23\nv_smfmac_f32_16x16x64_bf16 v[24:27], v[52:55], a[80:87], v0\nv_mul_f32 v20, s32, v20\n" \
    "v_smfmac_f32_16x16x64_bf16 v[28:31], v[52:55], a[88:95], v0\nv_mul_f32 v21, s32, v21\nv_smfmac_f32_16x16x64_bf16 v[24:27], v[72:75], a[104:111], v0\n" \
    "v_cvt_pk_bf16_f32 v20, v20, v21\nv_smfmac_f32_16x16x64_bf16 v[28:31], v[72:75], a[112:119], v0\nds_write_b16 v4, v20 offset:8\nv_permlane32_swap_b32 v16, v18\n" \
    "v_smfmac_f32_16x16x64_bf16 v[24:27], v[56:59], a[152:159], v0\nds_write_b16_d16_hi v4, v20 offset:24\nv_permlane32_swap_b32 v17, v19\n" \
    "v_smfmac_f32_16x16x64_bf16 v[24:27], v[60:63], a[176:183], v0\nds_read_b128 v[48:51], v1 offset:40960\nv_add_f32 v16, v16, v18\n" \
    "v_smfmac_f32_16x16x64_bf16 v[24:27], v[32:35], a[200:207], v0\nds_read_b128 v[52:55], v1 offset:45056\nv_add_f32 v17, v17, v19\n" \
    "v_smfmac_f32_16x16x64_bf16 v[24:27], v[36:39], a[248:255], v0\nds_read_b128 v[72:75], v1 offset:41984\nv_mul_f32 v16, s32, v16\n" \
    "v_smfmac_f32_16x16x64_bf16 v[24:27], v[64:67], v[108:115], v0\nds_read_b128 v[76:79], v1 offset:46080\nv_mul_f32 v17, s32, v17\n" \
    "v_smfmac_f32_16x16x64_bf16 v[24:27], v[68:71], v[140:147], v0\nds_read_b128 v[80:83], v1 offset:43008\nv_cvt_pk_bf16_f32 v16, v16, v17\n" \
    "v_smfmac_f32_16x16x64_bf16 v[24:27], v[40:43], v[204:211], v0\nds_write_b16 v4, v16 offset:0\nv_smfmac_f32_16x16x64_bf16 v[24:27], v[44:47], v[236:243], v0\n" \
    "ds_write_b16_d16_hi v4, v16 offset:16\ns_nop 1\nv_smfmac_f32_16x16x64_bf16 v[28:31], v[56:59], a[160:167], v0\nds_read_b128 v[56:59], v1 offset:47104\ns_nop 1\n" \
    "v_smfmac_f32_16x16x64_bf16 v[28:31], v[60:63], a[184:191], v0\nds_read_b128 v[60:63], v1 offset:44032\nv_permlane32_swap_b32 v24, v26\n" \
    "v_smfmac_f32_16x16x64_bf16 v[28:31], v[32:35], a[208:215], v0\nds_read_b128 v[32:35], v1 offset:48128\nv_permlane32_swap_b32 v25, v27\n" \
    "v_smfmac_f32_16x16x64_bf16 v[28:31], v[36:39], v[92:99], v0\nds_read_b128 v[36:39], v6 offset:0\nv_add_f32 v24, v24, v26\n" \
    "v_smfmac_f32_16x16x64_bf16 v[28:31], v[64:67], v[116:123], v0\nds_read_b128 v[64:67], v6 offset:512\nv_add_f32 v25, v25, v27\n" \
    "v_smfmac_f32_16x16x64_bf16 v[28:31], v[68:71], v[148:155], v0\nds_read_b128 v[68:71], v6 offset:1024\nv_cvt_pk_bf16_f32 v24, v24, v25\n" \
    "v_smfmac_f32_16x16x64_bf16 v[28:31], v[40:43], v[212:219], v0\nds_read_b128 v[40:43], v6 offset:1536\nv_smfmac_f32_16x16x64_bf16 v[28:31], v[44:47], v[244:251], v0\n" \
    "ds_write_b16 v4, v24 offset:2048\nds_write_b16_d16_hi v4, v24 offset:2064\ns_waitcnt lgkmcnt(5)\nv_mfma_f32_16x16x32_bf16 v[16:19], v[48:51], v[36:39], 0\ns_nop 2\n" \
    "v_mfma_f32_16x16x32_bf16 v[20:23], v[52:55], v[36:39], 0\nv_permlane32_swap_b32 v28, v30\nv_permlane32_swap_b32 v29, v31\nv_add_f32 v28, v28, v30\nv_add_f32 v29, v29, v31\n" \
    "v_cvt_pk_bf16_f32 v28, v28, v29\nds_write_b16 v4, v28 offset:2056\nds_write_b16_d16_hi v4, v28 offset:2072\ns_waitcnt lgkmcnt(6)\n" \
    "v_mfma_f32_16x16x32_bf16 v[16:19], v[72:75], v[64:67], v[16:19]\ns_cmp_eq_u32 s35, 63\ns_cbranch_scc1 .Lq8_%=\n" \
    "v_mfma_f32_16x16x32_bf16 v[20:23], v[76:79], v[64:67], v[20:23]\ns_waitcnt lgkmcnt(5)\nv_mfma_f32_16x16x32_bf16 v[16:19], v[80:83], v[68:71], v[16:19]\n" \
    "ds_read_b128 v[44:47], v8 offset:0\nds_read_b128 v[84:87], v8 offset:512\nv_mfma_f32_16x16x32_bf16 v[20:23], v[56:59], v[68:71], v[20:23]\n" \
    "ds_read_b128 v[88:91], v8 offset:1024\nds_read_b128 v[28:31], v8 offset:1536\ns_waitcnt lgkmcnt(8)\nv_mfma_f32_16x16x32_bf16 v[16:19], v[60:63], v[40:43], v[16:19]\n" \
    "v_mfma_f32_16x16x32_bf16 v[20:23], v[32:35], v[40:43], v[20:23]\nv_mov_b32 v15, v15\nv_mov_b32 v15, v15\ns_waitcnt lgkmcnt(3)\n" \
    "v_mfma_f32_16x16x32_bf16 v[24:27], v[44:47], v[36:39], 0\ns_nop 2\ns_waitcnt lgkmcnt(2)\nv_mov_b32_dpp v16, v20 quad_perm:[0,1,2,3] row_mask:0xf bank_mask:0xc\n" \
    "v_mov_b32_dpp v17, v21 quad_perm:[0,1,2,3] row_mask:0xf bank_mask:0xc\nv_mfma_f32_16x16x32_bf16 v[24:27], v[84:87], v[64:67], v[24:27]\ns_waitcnt lgkmcnt(1)\n" \
    "v_mov_b32_dpp v18, v22 quad_perm:[0,1,2,3] row_mask:0xf bank_mask:0xc\nv_mov_b32_dpp v19, v23 quad_perm:[0,1,2,3] row_mask:0xf bank_mask:0xc\n" \
    "v_mfma_f32_16x16x32_bf16 v[24:27], v[88:91], v[68:71], v[24:27]\ns_waitcnt lgkmcnt(0)\nv_max3_f32 v254, v16, v17, v18\nv_max_f32 v254, v254, v19\n" \
    "v_mfma_f32_16x16x32_bf16 v[24:27], v[28:31], v[40:43], v[24:27]\ns_nop 0\nv_max_f32_dpp v252, v254, v254 row_ror:8 row_mask:0xf bank_mask:0xf\n" \
    "v_max_f32_dpp v253, v254, v254 row_ror:8 row_mask:0xf bank_mask:0xf\ns_nop 1\nv_permlane32_swap_b32 v252, v253\nv_max_f32 v252, v252, v253\ns_xor_b32 s74, s82, 0x4000\n" \
    "v_max3_f32 v254, v252, v24, v25\nv_max3_f32 v252, v254, v26, v27\nv_max3_f32 v253, v254, v26, v27\ns_nop 1\nv_permlane16_swap_b32 v252, v253\nv_max_f32 v252, v252, v253\n" \
    "v_pk_add_f32 v[16:17], v[16:17], v[252:253] op_sel_hi:[1,0] neg_lo:[0,1] neg_hi:[0,1]\n" \
    "v_pk_add_f32 v[18:19], v[18:19], v[252:253] op_sel_hi:[1,0] neg_lo:[0,1] neg_hi:[0,1]\n" \
    "v_pk_add_f32 v[24:25], v[24:25], v[252:253] op_sel_hi:[1,0] neg_lo:[0,1] neg_hi:[0,1]\n" \
    "v_pk_add_f32 v[26:27], v[26:27], v[252:253] op_sel_hi:[1,0] neg_lo:[0,1] neg_hi:[0,1]\nv_exp_f32 v16, v16\nv_exp_f32 v17, v17\nv_exp_f32 v18, v18\nv_exp_f32 v19, v19\n" \
    "v_exp_f32 v24, v24\nv_pk_add_f32 v[254:255], v[16:17], v[18:19]\nv_exp_f32 v25, v25\nv_add_f32 v254, v254, v255\nv_exp_f32 v26, v26\nv_exp_f32 v27, v27\n" \
    "v_add_f32_dpp v252, v254, v254 row_ror:8 row_mask:0xf bank_mask:0xf\nv_add_f32_dpp v253, v254, v254 row_ror:8 row_mask:0xf bank_mask:0xf\n" \
    "v_pk_add_f32 v[254:255], v[24:25], v[26:27]\ns_nop 0\nv_permlane32_swap_b32 v252, v253\nv_pk_add_f32 v[254:255], v[254:255], v[252:253]\n" \
    "v_pk_add_f32 v[252:253], v[254:255], v[254:255] op_sel:[0,1] op_sel_hi:[1,0]\ns_nop 1\nv_permlane16_swap_b32 v252, v253\nv_add_f32 v252, v252, v253\n" \
    "v_cmp_u_f32 vcc, v252, v252\nv_rcp_f32 v252, v252\ns_cbranch_vccnz .Lq12_%=\nv_pk_mul_f32 v[16:17], v[16:17], v[252:253] op_sel_hi:[1,0]\n" \
    "v_pk_mul_f32 v[24:25], v[24:25], v[252:253] op_sel_hi:[1,0]\nv_cvt_pk_bf16_f32 v20, v16, v17\nv_cvt_pk_bf16_f32 v22, v24, v25\n" \
    "v_pk_mul_f32 v[18:19], v[18:19], v[252:253] op_sel_hi:[1,0]\nv_pk_mul_f32 v[26:27], v[26:27], v[252:253] op_sel_hi:[1,0]\nv_cvt_pk_bf16_f32 v21, v18, v19\n" \
    "v_cvt_pk_bf16_f32 v23, v26, v27\nv_permlane32_swap_b32 v20, v22\ns_nop 0\nv_permlane32_swap_b32 v21, v23\nbuffer_store_dwordx4 v[20:23], v7, s[64:67], s82 offen sc1\n" \
    "buffer_store_dwordx4 v[20:23], v7, s[64:67], s74 offen sc1\ns_branch .Lq3_%=\n.Lq6_%=:\n.Lq51_%=:\ns_memrealtime s[72:73]\ns_waitcnt lgkmcnt(0)\ns_mov_b32 s68, 0\n" \
    "s_sub_u32 s76, s80, s45\ns_sub_u32 s69, s72, s76\ns_cmp_lt_i32 s69, -8\ns_cbranch_scc1 .Lq54_%=\ns_xor_b32 s24, s81, 0\ns_xor_b32 s25, s81, 1024\ns_xor_b32 s26, s81, 2048\n" \
    "s_xor_b32 s27, s81, 3072\n.Lq56_%=:\ns_memrealtime s[72:73]\ns_waitcnt lgkmcnt(0)\ns_sub_u32 s72, s72, s80\ns_cmp_lt_i32 s72, 0\ns_cbranch_scc1 .Lq56_%=\n" \
    "buffer_load_dwordx4 v[72:75], v2, s[64:67], s24 offen sc1\nv_cvt_pk_bf16_f32 v24, v32, v33\nv_cvt_pk_bf16_f32 v25, v34, v35\nv_cvt_pk_bf16_f32 v26, v40, v41\n" \
    "buffer_load_dwordx4 v[76:79], v2, s[64:67], s25 offen sc1\nv_cvt_pk_bf16_f32 v27, v42, v43\nds_write_b128 v14, v[24:27] offset:8192\nv_cvt_pk_bf16_f32 v28, v36, v37\n" \
    "buffer_load_dwordx4 v[80:83], v2, s[64:67], s26 offen sc1\nv_cvt_pk_bf16_f32 v29, v38, v39\nv_cvt_pk_bf16_f32 v30, v44, v45\nv_cvt_pk_bf16_f32 v31, v46, v47\n" \
    "buffer_load_dwordx4 v[84:87], v2, s[64:67], s27 offen sc1\nds_write_b128 v14, v[28:31] offset:12288\nds_read_b128 v[32:35], v3 offset:4096\n" \
    "ds_read_b128 v[36:39], v3 offset:5120\nds_read_b128 v[40:43], v3 offset:12288\nds_read_b128 v[44:47], v3 offset:13312\nds_read_b128 v[48:51], v3 offset:20480\n" \
    "ds_read_b128 v[52:55], v3 offset:21504\nds_read_b128 v[56:59], v3 offset:0\nds_read_b128 v[60:63], v3 offset:1024\nds_read_b128 v[64:67], v3 offset:2048\n" \
    "ds_read_b128 v[68:71], v3 offset:3072\n.Lq55_%=:\nv_mov_b64 v[16:17], 0\nv_mov_b64 v[18:19], 0\nv_mov_b64 v[20:21], 0\nv_mov_b64 v[22:23], 0\nv_mov_b64 v[24:25], 0\n" \
    "v_mov_b64 v[26:27], 0\nv_mov_b64 v[28:29], 0\nv_mov_b64 v[30:31], 0\ns_waitcnt vmcnt(3) lgkmcnt(8)\nv_smfmac_f32_16x16x64_bf16 v[20:23], v[72:75], v[32:39], v0\n" \
    "ds_write_b128 v1, v[72:75] offset:26624\nv_max3_u32 v252, v72, v73, v74\nv_smfmac_f32_16x16x64_bf16 v[16:19], v[72:75], a[48:55], v0\n" \
    "v_smfmac_f32_16x16x64_bf16 v[24:27], v[72:75], a[56:63], v0\nv_smfmac_f32_16x16x64_bf16 v[28:31], v[72:75], a[64:71], v0\ns_waitcnt vmcnt(2)\n" \
    "ds_write_b128 v1, v[76:79] offset:30720\ns_waitcnt lgkmcnt(8)\nv_max3_u32 v252, v252, v75, v76\nv_smfmac_f32_16x16x64_bf16 v[20:23], v[76:79], v[40:47], v0\n" \
    "v_max3_u32 v252, v252, v77, v78\nv_smfmac_f32_16x16x64_bf16 v[16:19], v[76:79], a[144:151], v0\nv_cmp_u_f32 vcc, v252, v79\n" \
    "v_smfmac_f32_16x16x64_bf16 v[24:27], v[76:79], a[152:159], v0\ns_cbranch_vccnz .Lq62_%=\n.Lq58_%=:\nv_smfmac_f32_16x16x64_bf16 v[28:31], v[76:79], a[160:167], v0\n" \
    "ds_read_b128 v[36:39], v3 offset:7168\ns_waitcnt vmcnt(1)\nv_max3_u32 v252, v80, v81, v82\nv_cmp_u_f32 vcc, v252, v83\ns_nop 0\ns_cbranch_vccnz .Lq63_%=\n.Lq59_%=:\n" \
    "s_waitcnt lgkmcnt(7)\nv_smfmac_f32_16x16x64_bf16 v[20:23], v[80:83], v[48:55], v0\nds_write_b128 v1, v[80:83] offset:34816\n" \
    "v_smfmac_f32_16x16x64_bf16 v[16:19], v[80:83], a[240:247], v0\nds_read_b128 v[32:35], v3 offset:6144\ns_waitcnt lgkmcnt(3)\ns_barrier\n" \
    "ds_read_b128 v[88:91], v1 offset:24576\nv_smfmac_f32_16x16x64_bf16 v[24:27], v[80:83], a[248:255], v0\nds_read_b128 v[40:43], v3 offset:8192\n" \
    "v_smfmac_f32_16x16x64_bf16 v[28:31], v[80:83], v[92:99], v0\nds_read_b128 v[48:51], v1 offset:25600\ns_waitcnt vmcnt(0)\nv_max3_u32 v252, v84, v85, v86\n" \
    "v_cmp_u_f32 vcc, v252, v87\ns_nop 0\ns_cbranch_vccnz .Lq64_%=\n.Lq60_%=:\nv_smfmac_f32_16x16x64_bf16 v[20:23], v[84:87], v[196:203], v0\n" \
    "ds_write_b128 v1, v[84:87] offset:38912\nv_smfmac_f32_16x16x64_bf16 v[16:19], v[84:87], v[188:195], v0\nds_read_b128 v[52:55], v1 offset:27648\n" \
    "v_smfmac_f32_16x16x64_bf16 v[24:27], v[84:87], v[204:211], v0\nds_read_b128 v[72:75], v1 offset:28672\nv_smfmac_f32_16x16x64_bf16 v[28:31], v[84:87], v[212:219], v0\n" \
    "ds_read_b128 v[44:47], v3 offset:9216\ns_waitcnt lgkmcnt(6)\nv_smfmac_f32_16x16x64_bf16 v[20:23], v[88:91], v[56:63], v0\nds_read_b128 v[56:59], v1 offset:29696\n" \
    "v_smfmac_f32_16x16x64_bf16 v[16:19], v[88:91], a[0:7], v0\nds_read_b128 v[76:79], v3 offset:10240\nv_smfmac_f32_16x16x64_bf16 v[24:27], v[88:91], a[8:15], v0\n" \
    "ds_read_b128 v[80:83], v3 offset:11264\nv_smfmac_f32_16x16x64_bf16 v[28:31], v[88:91], a[16:23], v0\nds_read_b128 v[60:63], v1 offset:31744\ns_waitcnt lgkmcnt(7)\n" \
    "s_barrier\nv_smfmac_f32_16x16x64_bf16 v[20:23], v[48:51], v[64:71], v0\nds_read_b128 v[64:67], v3 offset:14336\n" \
    "v_smfmac_f32_16x16x64_bf16 v[16:19], v[48:51], a[24:31], v0\nds_read_b128 v[68:71], v3 offset:15360\ns_waitcnt lgkmcnt(8)\n" \
    "v_smfmac_f32_16x16x64_bf16 v[20:23], v[52:55], v[32:39], v0\nds_read_b128 v[32:35], v1 offset:32768\nv_smfmac_f32_16x16x64_bf16 v[16:19], v[52:55], a[72:79], v0\n" \
    "ds_read_b128 v[84:87], v3 offset:16384\ns_waitcnt lgkmcnt(8)\nv_smfmac_f32_16x16x64_bf16 v[20:23], v[72:75], v[40:47], v0\nds_read_b128 v[88:91], v3 offset:17408\n" \
    "v_smfmac_f32_16x16x64_bf16 v[16:19], v[72:75], a[96:103], v0\nds_read_b128 v[36:39], v1 offset:33792\ns_waitcnt lgkmcnt(7)\n" \
    "v_smfmac_f32_16x16x64_bf16 v[20:23], v[56:59], v[76:83], v0\nds_read_b128 v[40:43], v3 offset:18432\nv_smfmac_f32_16x16x64_bf16 v[16:19], v[56:59], a[120:127], v0\n" \
    "ds_read_b128 v[44:47], v3 offset:19456\ns_waitcnt lgkmcnt(6)\nv_smfmac_f32_16x16x64_bf16 v[20:23], v[60:63], v[64:71], v0\nds_read_b128 v[64:67], v1 offset:35840\n" \
    "v_smfmac_f32_16x16x64_bf16 v[16:19], v[60:63], a[168:175], v0\nds_read_b128 v[76:79], v3 offset:22528\ns_waitcnt lgkmcnt(5)\n" \
    "v_smfmac_f32_16x16x64_bf16 v[20:23], v[32:35], v[84:91], v0\nds_read_b128 v[80:83], v3 offset:23552\nv_smfmac_f32_16x16x64_bf16 v[16:19], v[32:35], a[192:199], v0\n" \
    "ds_read_b128 v[68:71], v1 offset:36864\ns_waitcnt lgkmcnt(4)\nv_smfmac_f32_16x16x64_bf16 v[20:23], v[36:39], v[40:47], v0\nds_read_b128 v[40:43], v1 offset:37888\n" \
    "v_smfmac_f32_16x16x64_bf16 v[16:19], v[36:39], a[216:223], v0\nds_read_b128 v[44:47], v1 offset:39936\ns_waitcnt lgkmcnt(3)\n" \
    "v_smfmac_f32_16x16x64_bf16 v[20:23], v[64:67], v[76:83], v0\nv_smfmac_f32_16x16x64_bf16 v[16:19], v[64:67], v[100:107], v0\ns_waitcnt lgkmcnt(2)\n" \
    "v_smfmac_f32_16x16x64_bf16 v[20:23], v[68:71], v[132:139], v0\ns_waitcnt lgkmcnt(1)\nv_smfmac_f32_16x16x64_bf16 v[20:23], v[40:43], v[164:171], v0\ns_waitcnt lgkmcnt(0)\n" \
    "v_smfmac_f32_16x16x64_bf16 v[20:23], v[44:47], v[228:235], v0\ns_nop 2\nv_smfmac_f32_16x16x64_bf16 v[16:19], v[68:71], v[124:131], v0\ns_nop 2\n" \
    "v_smfmac_f32_16x16x64_bf16 v[16:19], v[40:43], v[156:163], v0\nv_permlane32_swap_b32 v20, v22\nv_smfmac_f32_16x16x64_bf16 v[16:19], v[44:47], v[220:227], v0\ns_nop 1\n" \
    "v_permlane32_swap_b32 v21, v23\nv_smfmac_f32_16x16x64_bf16 v[24:27], v[48:51], a[32:39], v0\ns_nop 1\nv_add_f32 v20, v20, v22\n" \
    "v_smfmac_f32_16x16x64_bf16 v[28:31], v[48:51], a[40:47], v0\nv_add_f32 v21, v21, v23\nv_smfmac_f32_16x16x64_bf16 v[24:27], v[52:55], a[80:87], v0\nv_mul_f32 v20, s32, v20\n" \
    "v_smfmac_f32_16x16x64_bf16 v[28:31], v[52:55], a[88:95], v0\nv_mul_f32 v21, s32, v21\nv_smfmac_f32_16x16x64_bf16 v[24:27], v[72:75], a[104:111], v0\n" \
    "v_cvt_pk_bf16_f32 v20, v20, v21\nv_smfmac_f32_16x16x64_bf16 v[28:31], v[72:75], a[112:119], v0\nds_write_b16 v4, v20 offset:8\nv_permlane32_swap_b32 v16, v18\n" \
    "v_smfmac_f32_16x16x64_bf16 v[24:27], v[56:59], a[128:135], v0\nds_write_b16_d16_hi v4, v20 offset:24\nv_permlane32_swap_b32 v17, v19\n" \
    "v_smfmac_f32_16x16x64_bf16 v[24:27], v[60:63], a[176:183], v0\nds_read_b128 v[48:51], v1 offset:40960\nv_add_f32 v16, v16, v18\n" \
    "v_smfmac_f32_16x16x64_bf16 v[24:27], v[32:35], a[200:207], v0\nds_read_b128 v[52:55], v1 offset:45056\nv_add_f32 v17, v17, v19\n" \
    "v_smfmac_f32_16x16x64_bf16 v[24:27], v[36:39], a[224:231], v0\nds_read_b128 v[72:75], v1 offset:41984\nv_mul_f32 v16, s32, v16\n" \
    "v_smfmac_f32_16x16x64_bf16 v[24:27], v[64:67], v[108:115], v0\nds_read_b128 v[76:79], v1 offset:46080\nv_mul_f32 v17, s32, v17\n" \
    "v_smfmac_f32_16x16x64_bf16 v[24:27], v[68:71], v[140:147], v0\nds_read_b128 v[80:83], v1 offset:43008\nv_cvt_pk_bf16_f32 v16, v16, v17\n" \
    "v_smfmac_f32_16x16x64_bf16 v[24:27], v[40:43], v[172:179], v0\nds_write_b16 v4, v16 offset:0\nv_smfmac_f32_16x16x64_bf16 v[24:27], v[44:47], v[236:243], v0\n" \
    "ds_write_b16_d16_hi v4, v16 offset:16\ns_nop 1\nv_smfmac_f32_16x16x64_bf16 v[28:31], v[56:59], a[136:143], v0\nds_read_b128 v[56:59], v1 offset:47104\ns_nop 1\n" \
    "v_smfmac_f32_16x16x64_bf16 v[28:31], v[60:63], a[184:191], v0\nds_read_b128 v[60:63], v1 offset:44032\nv_permlane32_swap_b32 v24, v26\n" \
    "v_smfmac_f32_16x16x64_bf16 v[28:31], v[32:35], a[208:215], v0\nds_read_b128 v[32:35], v1 offset:48128\nv_permlane32_swap_b32 v25, v27\n" \
    "v_smfmac_f32_16x16x64_bf16 v[28:31], v[36:39], a[232:239], v0\nds_read_b128 v[36:39], v6 offset:0\nv_add_f32 v24, v24, v26\n" \
    "v_smfmac_f32_16x16x64_bf16 v[28:31], v[64:67], v[116:123], v0\nds_read_b128 v[64:67], v6 offset:512\nv_add_f32 v25, v25, v27\n" \
    "v_smfmac_f32_16x16x64_bf16 v[28:31], v[68:71], v[148:155], v0\nds_read_b128 v[68:71], v6 offset:1024\nv_cvt_pk_bf16_f32 v24, v24, v25\n" \
    "v_smfmac_f32_16x16x64_bf16 v[28:31], v[40:43], v[180:187], v0\nds_read_b128 v[40:43], v6 offset:1536\nv_smfmac_f32_16x16x64_bf16 v[28:31], v[44:47], v[244:251], v0\n" \
    "ds_write_b16 v4, v24 offset:2048\nds_write_b16_d16_hi v4, v24 offset:2064\ns_waitcnt lgkmcnt(5)\nv_mfma_f32_16x16x32_bf16 v[16:19], v[48:51], v[36:39], 0\ns_nop 2\n" \
    "v_mfma_f32_16x16x32_bf16 v[20:23], v[52:55], v[36:39], 0\nv_permlane32_swap_b32 v28, v30\nv_permlane32_swap_b32 v29, v31\nv_add_f32 v28, v28, v30\nv_add_f32 v29, v29, v31\n" \
    "v_cvt_pk_bf16_f32 v28, v28, v29\nds_write_b16 v4, v28 offset:2056\nds_write_b16_d16_hi v4, v28 offset:2072\ns_waitcnt lgkmcnt(6)\n" \
    "v_mfma_f32_16x16x32_bf16 v[16:19], v[72:75], v[64:67], v[16:19]\ns_cmp_eq_u32 s35, 63\ns_cbranch_scc1 .Lq8_%=\n" \
    "v_mfma_f32_16x16x32_bf16 v[20:23], v[76:79], v[64:67], v[20:23]\ns_waitcnt lgkmcnt(5)\nv_mfma_f32_16x16x32_bf16 v[16:19], v[80:83], v[68:71], v[16:19]\n" \
    "ds_read_b128 v[44:47], v8 offset:0\nds_read_b128 v[84:87], v8 offset:512\nv_mfma_f32_16x16x32_bf16 v[20:23], v[56:59], v[68:71], v[20:23]\n" \
    "ds_read_b128 v[88:91], v8 offset:1024\nds_read_b128 v[28:31], v8 offset:1536\ns_waitcnt lgkmcnt(8)\nv_mfma_f32_16x16x32_bf16 v[16:19], v[60:63], v[40:43], v[16:19]\n" \
    "v_mfma_f32_16x16x32_bf16 v[20:23], v[32:35], v[40:43], v[20:23]\nv_mov_b32 v15, v15\nv_mov_b32 v15, v15\ns_waitcnt lgkmcnt(3)\n" \
    "v_mfma_f32_16x16x32_bf16 v[24:27], v[44:47], v[36:39], 0\ns_nop 2\ns_waitcnt lgkmcnt(2)\nv_mov_b32_dpp v16, v20 quad_perm:[0,1,2,3] row_mask:0xf bank_mask:0xc\n" \
    "v_mov_b32_dpp v17, v21 quad_perm:[0,1,2,3] row_mask:0xf bank_mask:0xc\nv_mfma_f32_16x16x32_bf16 v[24:27], v[84:87], v[64:67], v[24:27]\ns_waitcnt lgkmcnt(1)\n" \
    "v_mov_b32_dpp v18, v22 quad_perm:[0,1,2,3] row_mask:0xf bank_mask:0xc\nv_mov_b32_dpp v19, v23 quad_perm:[0,1,2,3] row_mask:0xf bank_mask:0xc\n" \
    "v_mfma_f32_16x16x32_bf16 v[24:27], v[88:91], v[68:71], v[24:27]\ns_waitcnt lgkmcnt(0)\nv_max3_f32 v254, v16, v17, v18\nv_max_f32 v254, v254, v19\n" \
    "v_mfma_f32_16x16x32_bf16 v[24:27], v[28:31], v[40:43], v[24:27]\ns_nop 0\nv_max_f32_dpp v252, v254, v254 row_ror:8 row_mask:0xf bank_mask:0xf\n" \
    "v_max_f32_dpp v253, v254, v254 row_ror:8 row_mask:0xf bank_mask:0xf\ns_nop 1\nv_permlane32_swap_b32 v252, v253\nv_max_f32 v252, v252, v253\ns_xor_b32 s74, s82, 0x4000\n" \
    "v_max3_f32 v254, v252, v24, v25\nv_max3_f32 v252, v254, v26, v27\nv_max3_f32 v253, v254, v26, v27\ns_nop 1\nv_permlane16_swap_b32 v252, v253\nv_max_f32 v252, v252, v253\n" \
    "v_pk_add_f32 v[16:17], v[16:17], v[252:253] op_sel_hi:[1,0] neg_lo:[0,1] neg_hi:[0,1]\n" \
    "v_pk_add_f32 v[18:19], v[18:19], v[252:253] op_sel_hi:[1,0] neg_lo:[0,1] neg_hi:[0,1]\n" \
    "v_pk_add_f32 v[24:25], v[24:25], v[252:253] op_sel_hi:[1,0] neg_lo:[0,1] neg_hi:[0,1]\n" \
    "v_pk_add_f32 v[26:27], v[26:27], v[252:253] op_sel_hi:[1,0] neg_lo:[0,1] neg_hi:[0,1]\nv_exp_f32 v16, v16\nv_exp_f32 v17, v17\nv_exp_f32 v18, v18\nv_exp_f32 v19, v19\n" \
    "v_exp_f32 v24, v24\nv_pk_add_f32 v[254:255], v[16:17], v[18:19]\nv_exp_f32 v25, v25\nv_add_f32 v254, v254, v255\nv_exp_f32 v26, v26\nv_exp_f32 v27, v27\n" \
    "v_add_f32_dpp v252, v254, v254 row_ror:8 row_mask:0xf bank_mask:0xf\nv_add_f32_dpp v253, v254, v254 row_ror:8 row_mask:0xf bank_mask:0xf\n" \
    "v_pk_add_f32 v[254:255], v[24:25], v[26:27]\ns_nop 0\nv_permlane32_swap_b32 v252, v253\nv_pk_add_f32 v[254:255], v[254:255], v[252:253]\n" \
    "v_pk_add_f32 v[252:253], v[254:255], v[254:255] op_sel:[0,1] op_sel_hi:[1,0]\ns_nop 1\nv_permlane16_swap_b32 v252, v253\nv_add_f32 v252, v252, v253\n" \
    "v_cmp_u_f32 vcc, v252, v252\nv_rcp_f32 v252, v252\ns_cbranch_vccnz .Lq12_%=\nv_pk_mul_f32 v[16:17], v[16:17], v[252:253] op_sel_hi:[1,0]\n" \
    "v_pk_mul_f32 v[24:25], v[24:25], v[252:253] op_sel_hi:[1,0]\nv_cvt_pk_bf16_f32 v20, v16, v17\nv_cvt_pk_bf16_f32 v22, v24, v25\n" \
    "v_pk_mul_f32 v[18:19], v[18:19], v[252:253] op_sel_hi:[1,0]\nv_pk_mul_f32 v[26:27], v[26:27], v[252:253] op_sel_hi:[1,0]\nv_cvt_pk_bf16_f32 v21, v18, v19\n" \
    "v_cvt_pk_bf16_f32 v23, v26, v27\nv_permlane32_swap_b32 v20, v22\ns_nop 0\nv_permlane32_swap_b32 v21, v23\nbuffer_store_dwordx4 v[20:23], v7, s[64:67], s82 offen sc1\n" \
    "buffer_store_dwordx4 v[20:23], v7, s[64:67], s74 offen sc1\ns_branch .Lq3_%=\n.Lq7_%=:\n.Lq70_%=:\ns_memrealtime s[72:73]\ns_waitcnt lgkmcnt(0)\ns_mov_b32 s68, 0\n" \
    "s_sub_u32 s76, s80, s45\ns_sub_u32 s69, s72, s76\ns_cmp_lt_i32 s69, -8\ns_cbranch_scc1 .Lq73_%=\ns_xor_b32 s24, s81, 0\ns_xor_b32 s25, s81, 1024\ns_xor_b32 s26, s81, 2048\n" \
    "s_xor_b32 s27, s81, 3072\n.Lq75_%=:\ns_memrealtime s[72:73]\ns_waitcnt lgkmcnt(0)\ns_sub_u32 s72, s72, s80\ns_cmp_lt_i32 s72, 0\ns_cbranch_scc1 .Lq75_%=\n" \
    "buffer_load_dwordx4 v[72:75], v2, s[64:67], s24 offen sc1\nv_cvt_pk_bf16_f32 v24, v32, v33\nv_cvt_pk_bf16_f32 v25, v34, v35\nv_cvt_pk_bf16_f32 v26, v40, v41\n" \
    "buffer_load_dwordx4 v[76:79], v2, s[64:67], s25 offen sc1\nv_cvt_pk_bf16_f32 v27, v42, v43\nds_write_b128 v14, v[24:27] offset:8192\nv_cvt_pk_bf16_f32 v28, v36, v37\n" \
    "buffer_load_dwordx4 v[80:83], v2, s[64:67], s26 offen sc1\nv_cvt_pk_bf16_f32 v29, v38, v39\nv_cvt_pk_bf16_f32 v30, v44, v45\nv_cvt_pk_bf16_f32 v31, v46, v47\n" \
    "buffer_load_dwordx4 v[84:87], v2, s[64:67], s27 offen sc1\nds_write_b128 v14, v[28:31] offset:12288\nds_read_b128 v[32:35], v3 offset:6144\n" \
    "ds_read_b128 v[36:39], v3 offset:7168\nds_read_b128 v[40:43], v3 offset:14336\nds_read_b128 v[44:47], v3 offset:15360\nds_read_b128 v[48:51], v3 offset:22528\n" \
    "ds_read_b128 v[52:55], v3 offset:23552\nds_read_b128 v[56:59], v3 offset:0\nds_read_b128 v[60:63], v3 offset:1024\nds_read_b128 v[64:67], v3 offset:2048\n" \
    "ds_read_b128 v[68:71], v3 offset:3072\n.Lq74_%=:\nv_mov_b64 v[16:17], 0\nv_mov_b64 v[18:19], 0\nv_mov_b64 v[20:21], 0\nv_mov_b64 v[22:23], 0\nv_mov_b64 v[24:25], 0\n" \
    "v_mov_b64 v[26:27], 0\nv_mov_b64 v[28:29], 0\nv_mov_b64 v[30:31], 0\ns_waitcnt vmcnt(3) lgkmcnt(8)\nv_smfmac_f32_16x16x64_bf16 v[20:23], v[72:75], v[32:39], v0\n" \
    "ds_write_b128 v1, v[72:75] offset:27648\nv_max3_u32 v252, v72, v73, v74\nv_smfmac_f32_16x16x64_bf16 v[16:19], v[72:75], a[72:79], v0\n" \
    "v_smfmac_f32_16x16x64_bf16 v[24:27], v[72:75], a[80:87], v0\nv_smfmac_f32_16x16x64_bf16 v[28:31], v[72:75], a[88:95], v0\ns_waitcnt vmcnt(2)\n" \
    "ds_write_b128 v1, v[76:79] offset:31744\ns_waitcnt lgkmcnt(8)\nv_max3_u32 v252, v252, v75, v76\nv_smfmac_f32_16x16x64_bf16 v[20:23], v[76:79], v[40:47], v0\n" \
    "v_max3_u32 v252, v252, v77, v78\nv_smfmac_f32_16x16x64_bf16 v[16:19], v[76:79], a[168:175], v0\nv_cmp_u_f32 vcc, v252, v79\n" \
    "v_smfmac_f32_16x16x64_bf16 v[24:27], v[76:79], a[176:183], v0\ns_cbranch_vccnz .Lq81_%=\n.Lq77_%=:\nv_smfmac_f32_16x16x64_bf16 v[28:31], v[76:79], a[184:191], v0\n" \
    "ds_read_b128 v[36:39], v3 offset:5120\ns_waitcnt vmcnt(1)\nv_max3_u32 v252, v80, v81, v82\nv_cmp_u_f32 vcc, v252, v83\ns_nop 0\ns_cbranch_vccnz .Lq82_%=\n.Lq78_%=:\n" \
    "s_waitcnt lgkmcnt(7)\nv_smfmac_f32_16x16x64_bf16 v[20:23], v[80:83], v[48:55], v0\nds_write_b128 v1, v[80:83] offset:35840\n" \
    "v_smfmac_f32_16x16x64_bf16 v[16:19], v[80:83], v[100:107], v0\nds_read_b128 v[32:35], v3 offset:4096\ns_waitcnt lgkmcnt(3)\ns_barrier\n" \
    "ds_read_b128 v[88:91], v1 offset:24576\nv_smfmac_f32_16x16x64_bf16 v[24:27], v[80:83], v[108:115], v0\nds_read_b128 v[40:43], v3 offset:8192\n" \
    "v_smfmac_f32_16x16x64_bf16 v[28:31], v[80:83], v[116:123], v0\nds_read_b128 v[48:51], v1 offset:25600\ns_waitcnt vmcnt(0)\nv_max3_u32 v252, v84, v85, v86\n" \
    "v_cmp_u_f32 vcc, v252, v87\ns_nop 0\ns_cbranch_vccnz .Lq83_%=\n.Lq79_%=:\nv_smfmac_f32_16x16x64_bf16 v[20:23], v[84:87], v[228:235], v0\n" \
    "ds_write_b128 v1, v[84:87] offset:39936\nv_smfmac_f32_16x16x64_bf16 v[16:19], v[84:87], v[220:227], v0\nds_read_b128 v[52:55], v1 offset:26624\n" \
    "v_smfmac_f32_16x16x64_bf16 v[24:27], v[84:87], v[236:243], v0\nds_read_b128 v[72:75], v1 offset:28672\nv_smfmac_f32_16x16x64_bf16 v[28:31], v[84:87], v[244:251], v0\n" \
    "ds_read_b128 v[44:47], v3 offset:9216\ns_waitcnt lgkmcnt(6)\nv_smfmac_f32_16x16x64_bf16 v[20:23], v[88:91], v[56:63], v0\nds_read_b128 v[56:59], v1 offset:29696\n" \
    "v_smfmac_f32_16x16x64_bf16 v[16:19], v[88:91], a[0:7], v0\nds_read_b128 v[76:79], v3 offset:10240\nv_smfmac_f32_16x16x64_bf16 v[24:27], v[88:91], a[8:15], v0\n" \
    "ds_read_b128 v[80:83], v3 offset:11264\nv_smfmac_f32_16x16x64_bf16 v[28:31], v[88:91], a[16:23], v0\nds_read_b128 v[60:63], v1 offset:30720\ns_waitcnt lgkmcnt(7)\n" \
    "s_barrier\nv_smfmac_f32_16x16x64_bf16 v[20:23], v[48:51], v[64:71], v0\nds_read_b128 v[64:67], v3 offset:12288\n" \
    "v_smfmac_f32_16x16x64_bf16 v[16:19], v[48:51], a[24:31], v0\nds_read_b128 v[68:71], v3 offset:13312\ns_waitcnt lgkmcnt(8)\n" \
    "v_smfmac_f32_16x16x64_bf16 v[20:23], v[52:55], v[32:39], v0\nds_read_b128 v[32:35], v1 offset:32768\nv_smfmac_f32_16x16x64_bf16 v[16:19], v[52:55], a[48:55], v0\n" \
    "ds_read_b128 v[84:87], v3 offset:16384\ns_waitcnt lgkmcnt(8)\nv_smfmac_f32_16x16x64_bf16 v[20:23], v[72:75], v[40:47], v0\nds_read_b128 v[88:91], v3 offset:17408\n" \
    "v_smfmac_f32_16x16x64_bf16 v[16:19], v[72:75], a[96:103], v0\nds_read_b128 v[36:39], v1 offset:33792\ns_waitcnt lgkmcnt(7)\n" \
    "v_smfmac_f32_16x16x64_bf16 v[20:23], v[56:59], v[76:83], v0\nds_read_b128 v[40:43], v3 offset:18432\nv_smfmac_f32_16x16x64_bf16 v[16:19], v[56:59], a[120:127], v0\n" \
    "ds_read_b128 v[44:47], v3 offset:19456\ns_waitcnt lgkmcnt(6)\nv_smfmac_f32_16x16x64_bf16 v[20:23], v[60:63], v[64:71], v0\nds_read_b128 v[64:67], v1 offset:34816\n" \
    "v_smfmac_f32_16x16x64_bf16 v[16:19], v[60:63], a[144:151], v0\nds_read_b128 v[76:79], v3 offset:20480\ns_waitcnt lgkmcnt(5)\n" \
    "v_smfmac_f32_16x16x64_bf16 v[20:23], v[32:35], v[84:91], v0\nds_read_b128 v[80:83], v3 offset:21504\nv_smfmac_f32_16x16x64_bf16 v[16:19], v[32:35], a[192:199], v0\n" \
    "ds_read_b128 v[68:71], v1 offset:36864\ns_waitcnt lgkmcnt(4)\nv_smfmac_f32_16x16x64_bf16 v[20:23], v[36:39], v[40:47], v0\nds_read_b128 v[40:43], v1 offset:37888\n" \
    "v_smfmac_f32_16x16x64_bf16 v[16:19], v[36:39], a[216:223], v0\nds_read_b128 v[44:47], v1 offset:38912\ns_waitcnt lgkmcnt(3)\n" \
    "v_smfmac_f32_16x16x64_bf16 v[20:23], v[64:67], v[76:83], v0\nv_smfmac_f32_16x16x64_bf16 v[16:19], v[64:67], a[240:247], v0\ns_waitcnt lgkmcnt(2)\n" \
    "v_smfmac_f32_16x16x64_bf16 v[20:23], v[68:71], v[132:139], v0\ns_waitcnt lgkmcnt(1)\nv_smfmac_f32_16x16x64_bf16 v[20:23], v[40:43], v[164:171], v0\ns_waitcnt lgkmcnt(0)\n" \
    "v_smfmac_f32_16x16x64_bf16 v[20:23], v[44:47], v[196:203], v0\ns_nop 2\nv_smfmac_f32_16x16x64_bf16 v[16:19], v[68:71], v[124:131], v0\ns_nop 2\n" \
    "v_smfmac_f32_16x16x64_bf16 v[16:19], v[40:43], v[156:163], v0\nv_permlane32_swap_b32 v20, v22\nv_smfmac_f32_16x16x64_bf16 v[16:19], v[44:47], v[188:195], v0\ns_nop 1\n" \
    "v_permlane32_swap_b32 v21, v23\nv_smfmac_f32_16x16x64_bf16 v[24:27], v[48:51], a[32:39], v0\ns_nop 1\nv_add_f32 v20, v20, v22\n" \
    "v_smfmac_f32_16x16x64_bf16 v[28:31], v[48:51], a[40:47], v0\nv_add_f32 v21, v21, v23\nv_smfmac_f32_16x16x64_bf16 v[24:27], v[52:55], a[56:63], v0\nv_mul_f32 v20, s32, v20\n" \
    "v_smfmac_f32_16x16x64_bf16 v[28:31], v[52:55], a[64:71], v0\nv_mul_f32 v21, s32, v21\nv_smfmac_f32_16x16x64_bf16 v[24:27], v[72:75], a[104:111], v0\n" \
    "v_cvt_pk_bf16_f32 v20, v20, v21\nv_smfmac_f32_16x16x64_bf16 v[28:31], v[72:75], a[112:119], v0\nds_write_b16 v4, v20 offset:8\nv_permlane32_swap_b32 v16, v18\n" \
    "v_smfmac_f32_16x16x64_bf16 v[24:27], v[56:59], a[128:135], v0\nds_write_b16_d16_hi v4, v20 offset:24\nv_permlane32_swap_b32 v17, v19\n" \
    "v_smfmac_f32_16x16x64_bf16 v[24:27], v[60:63], a[152:159], v0\nds_read_b128 v[48:51], v1 offset:40960\nv_add_f32 v16, v16, v18\n" \
    "v_smfmac_f32_16x16x64_bf16 v[24:27], v[32:35], a[200:207], v0\nds_read_b128 v[52:55], v1 offset:45056\nv_add_f32 v17, v17, v19\n" \
    "v_smfmac_f32_16x16x64_bf16 v[24:27], v[36:39], a[224:231], v0\nds_read_b128 v[72:75], v1 offset:41984\nv_mul_f32 v16, s32, v16\n" \
    "v_smfmac_f32_16x16x64_bf16 v[24:27], v[64:67], a[248:255], v0\nds_read_b128 v[76:79], v1 offset:46080\nv_mul_f32 v17, s32, v17\n" \
    "v_smfmac_f32_16x16x64_bf16 v[24:27], v[68:71], v[140:147], v0\nds_read_b128 v[80:83], v1 offset:43008\nv_cvt_pk_bf16_f32 v16, v16, v17\n" \
    "v_smfmac_f32_16x16x64_bf16 v[24:27], v[40:43], v[172:179], v0\nds_write_b16 v4, v16 offset:0\nv_smfmac_f32_16x16x64_bf16 v[24:27], v[44:47], v[204:211], v0\n" \
    "ds_write_b16_d16_hi v4, v16 offset:16\ns_nop 1\nv_smfmac_f32_16x16x64_bf16 v[28:31], v[56:59], a[136:143], v0\nds_read_b128 v[56:59], v1 offset:47104\ns_nop 1\n" \
    "v_smfmac_f32_16x16x64_bf16 v[28:31], v[60:63], a[160:167], v0\nds_read_b128 v[60:63], v1 offset:44032\nv_permlane32_swap_b32 v24, v26\n" \
    "v_smfmac_f32_16x16x64_bf16 v[28:31], v[32:35], a[208:215], v0\nds_read_b128 v[32:35], v1 offset:48128\nv_permlane32_swap_b32 v25, v27\n" \
    "v_smfmac_f32_16x16x64_bf16 v[28:31], v[36:39], a[232:239], v0\nds_read_b128 v[36:39], v6 offset:0\nv_add_f32 v24, v24, v26\n" \
    "v_smfmac_f32_16x16x64_bf16 v[28:31], v[64:67], v[92:99], v0\nds_read_b128 v[64:67], v6 offset:512\nv_add_f32 v25, v25, v27\n" \
    "v_smfmac_f32_16x16x64_bf16 v[28:31], v[68:71], v[148:155], v0\nds_read_b128 v[68:71], v6 offset:1024\nv_cvt_pk_bf16_f32 v24, v24, v25\n" \
    "v_smfmac_f32_16x16x64_bf16 v[28:31], v[40:43], v[180:187], v0\nds_read_b128 v[40:43], v6 offset:1536\nv_smfmac_f32_16x16x64_bf16 v[28:31], v[44:47], v[212:219], v0\n" \
    "ds_write_b16 v4, v24 offset:2048\nds_write_b16_d16_hi v4, v24 offset:2064\ns_waitcnt lgkmcnt(5)\nv_mfma_f32_16x16x32_bf16 v[16:19], v[48:51], v[36:39], 0\ns_nop 2\n" \
    "v_mfma_f32_16x16x32_bf16 v[20:23], v[52:55], v[36:39], 0\nv_permlane32_swap_b32 v28, v30\nv_permlane32_swap_b32 v29, v31\nv_add_f32 v28, v28, v30\nv_add_f32 v29, v29, v31\n" \
    "v_cvt_pk_bf16_f32 v28, v28, v29\nds_write_b16 v4, v28 offset:2056\nds_write_b16_d16_hi v4, v28 offset:2072\ns_waitcnt lgkmcnt(6)\n" \
    "v_mfma_f32_16x16x32_bf16 v[16:19], v[72:75], v[64:67], v[16:19]\ns_cmp_eq_u32 s35, 63\ns_cbranch_scc1 .Lq8_%=\n" \
    "v_mfma_f32_16x16x32_bf16 v[20:23], v[76:79], v[64:67], v[20:23]\ns_waitcnt lgkmcnt(5)\nv_mfma_f32_16x16x32_bf16 v[16:19], v[80:83], v[68:71], v[16:19]\n" \
    "ds_read_b128 v[44:47], v8 offset:0\nds_read_b128 v[84:87], v8 offset:512\nv_mfma_f32_16x16x32_bf16 v[20:23], v[56:59], v[68:71], v[20:23]\n" \
    "ds_read_b128 v[88:91], v8 offset:1024\nds_read_b128 v[28:31], v8 offset:1536\ns_waitcnt lgkmcnt(8)\nv_mfma_f32_16x16x32_bf16 v[16:19], v[60:63], v[40:43], v[16:19]\n" \
    "v_mfma_f32_16x16x32_bf16 v[20:23], v[32:35], v[40:43], v[20:23]\nv_mov_b32 v15, v15\nv_mov_b32 v15, v15\ns_waitcnt lgkmcnt(3)\n" \
    "v_mfma_f32_16x16x32_bf16 v[24:27], v[44:47], v[36:39], 0\ns_nop 2\ns_waitcnt lgkmcnt(2)\nv_mov_b32_dpp v16, v20 quad_perm:[0,1,2,3] row_mask:0xf bank_mask:0xc\n" \
    "v_mov_b32_dpp v17, v21 quad_perm:[0,1,2,3] row_mask:0xf bank_mask:0xc\nv_mfma_f32_16x16x32_bf16 v[24:27], v[84:87], v[64:67], v[24:27]\ns_waitcnt lgkmcnt(1)\n" \
    "v_mov_b32_dpp v18, v22 quad_perm:[0,1,2,3] row_mask:0xf bank_mask:0xc\nv_mov_b32_dpp v19, v23 quad_perm:[0,1,2,3] row_mask:0xf bank_mask:0xc\n" \
    "v_mfma_f32_16x16x32_bf16 v[24:27], v[88:91], v[68:71], v[24:27]\ns_waitcnt lgkmcnt(0)\nv_max3_f32 v254, v16, v17, v18\nv_max_f32 v254, v254, v19\n" \
    "v_mfma_f32_16x16x32_bf16 v[24:27], v[28:31], v[40:43], v[24:27]\ns_nop 0\nv_max_f32_dpp v252, v254, v254 row_ror:8 row_mask:0xf bank_mask:0xf\n" \
    "v_max_f32_dpp v253, v254, v254 row_ror:8 row_mask:0xf bank_mask:0xf\ns_nop 1\nv_permlane32_swap_b32 v252, v253\nv_max_f32 v252, v252, v253\ns_xor_b32 s74, s82, 0x4000\n" \
    "v_max3_f32 v254, v252, v24, v25\nv_max3_f32 v252, v254, v26, v27\nv_max3_f32 v253, v254, v26, v27\ns_nop 1\nv_permlane16_swap_b32 v252, v253\nv_max_f32 v252, v252, v253\n" \
    "v_pk_add_f32 v[16:17], v[16:17], v[252:253] op_sel_hi:[1,0] neg_lo:[0,1] neg_hi:[0,1]\n" \
    "v_pk_add_f32 v[18:19], v[18:19], v[252:253] op_sel_hi:[1,0] neg_lo:[0,1] neg_hi:[0,1]\n" \
    "v_pk_add_f32 v[24:25], v[24:25], v[252:253] op_sel_hi:[1,0] neg_lo:[0,1] neg_hi:[0,1]\n" \
    "v_pk_add_f32 v[26:27], v[26:27], v[252:253] op_sel_hi:[1,0] neg_lo:[0,1] neg_hi:[0,1]\nv_exp_f32 v16, v16\nv_exp_f32 v17, v17\nv_exp_f32 v18, v18\nv_exp_f32 v19, v19\n" \
    "v_exp_f32 v24, v24\nv_pk_add_f32 v[254:255], v[16:17], v[18:19]\nv_exp_f32 v25, v25\nv_add_f32 v254, v254, v255\nv_exp_f32 v26, v26\nv_exp_f32 v27, v27\n" \
    "v_add_f32_dpp v252, v254, v254 row_ror:8 row_mask:0xf bank_mask:0xf\nv_add_f32_dpp v253, v254, v254 row_ror:8 row_mask:0xf bank_mask:0xf\n" \
    "v_pk_add_f32 v[254:255], v[24:25], v[26:27]\ns_nop 0\nv_permlane32_swap_b32 v252, v253\nv_pk_add_f32 v[254:255], v[254:255], v[252:253]\n" \
    "v_pk_add_f32 v[252:253], v[254:255], v[254:255] op_sel:[0,1] op_sel_hi:[1,0]\ns_nop 1\nv_permlane16_swap_b32 v252, v253\nv_add_f32 v252, v252, v253\n" \
    "v_cmp_u_f32 vcc, v252, v252\nv_rcp_f32 v252, v252\ns_cbranch_vccnz .Lq12_%=\nv_pk_mul_f32 v[16:17], v[16:17], v[252:253] op_sel_hi:[1,0]\n" \
    "v_pk_mul_f32 v[24:25], v[24:25], v[252:253] op_sel_hi:[1,0]\nv_cvt_pk_bf16_f32 v20, v16, v17\nv_cvt_pk_bf16_f32 v22, v24, v25\n" \
    "v_pk_mul_f32 v[18:19], v[18:19], v[252:253] op_sel_hi:[1,0]\nv_pk_mul_f32 v[26:27], v[26:27], v[252:253] op_sel_hi:[1,0]\nv_cvt_pk_bf16_f32 v21, v18, v19\n" \
    "v_cvt_pk_bf16_f32 v23, v26, v27\nv_permlane32_swap_b32 v20, v22\ns_nop 0\nv_permlane32_swap_b32 v21, v23\nbuffer_store_dwordx4 v[20:23], v7, s[64:67], s82 offen sc1\n" \
    "buffer_store_dwordx4 v[20:23], v7, s[64:67], s74 offen sc1\n.Lq3_%=:\nds_read_b128 v[36:39], v5 offset:0\nds_read_b128 v[64:67], v5 offset:1024\n" \
    "ds_read_b128 v[68:71], v5 offset:2048\nds_read_b128 v[40:43], v5 offset:3072\ns_mov_b32 s40, s84\ns_add_u32 s35, s35, 2\ns_barrier\ns_memrealtime s[72:73]\n" \
    "s_waitcnt lgkmcnt(0)\n.Lq91_%=:\nv_mfma_f32_16x16x32_bf16 v[16:19], v[48:51], v[36:39], 0\nv_mfma_f32_16x16x32_bf16 v[20:23], v[52:55], v[36:39], 0\n" \
    "v_mfma_f32_16x16x32_bf16 v[16:19], v[72:75], v[64:67], v[16:19]\nv_mfma_f32_16x16x32_bf16 v[20:23], v[76:79], v[64:67], v[20:23]\n" \
    "v_mfma_f32_16x16x32_bf16 v[16:19], v[80:83], v[68:71], v[16:19]\nv_mfma_f32_16x16x32_bf16 v[20:23], v[56:59], v[68:71], v[20:23]\n" \
    "v_mfma_f32_16x16x32_bf16 v[16:19], v[60:63], v[40:43], v[16:19]\nv_mfma_f32_16x16x32_bf16 v[20:23], v[32:35], v[40:43], v[20:23]\n" \
    "v_mfma_f32_16x16x32_bf16 v[24:27], v[44:47], v[36:39], 0\nv_mfma_f32_16x16x32_bf16 v[24:27], v[84:87], v[64:67], v[24:27]\n" \
    "v_mfma_f32_16x16x32_bf16 v[24:27], v[88:91], v[68:71], v[24:27]\nv_mfma_f32_16x16x32_bf16 v[24:27], v[28:31], v[40:43], v[24:27]\ns_cmp_lt_u32 s35, 62\n" \
    "s_cbranch_scc1 .Lq2_%=\ns_nop 1\nv_mov_b32_dpp v16, v20 quad_perm:[0,1,2,3] row_mask:0xf bank_mask:0xc\n" \
    "v_mov_b32_dpp v17, v21 quad_perm:[0,1,2,3] row_mask:0xf bank_mask:0xc\nv_mov_b32_dpp v18, v22 quad_perm:[0,1,2,3] row_mask:0xf bank_mask:0xc\n" \
    "v_mov_b32_dpp v19, v23 quad_perm:[0,1,2,3] row_mask:0xf bank_mask:0xc\nv_mov_b32 v254, 0xff800000\nv_cvt_pk_bf16_f32 v16, 0, v16\nv_cvt_pk_bf16_f32 v17, 0, v17\n" \
    "v_cvt_pk_bf16_f32 v18, 0, v18\nv_cvt_pk_bf16_f32 v19, 0, v19\nv_mul_f32 v16, s33, v16\nv_mul_f32 v17, s33, v17\nv_mul_f32 v18, s33, v18\nv_mul_f32 v19, s33, v19\n" \
    "v_cvt_pk_bf16_f32 v16, 0, v16\nv_cvt_pk_bf16_f32 v17, 0, v17\nv_cvt_pk_bf16_f32 v18, 0, v18\nv_cvt_pk_bf16_f32 v19, 0, v19\nv_cndmask_b32 v16, v254, v16, s[50:51]\n" \
    "v_cndmask_b32 v17, v254, v17, s[52:53]\nv_cndmask_b32 v18, v254, v18, s[54:55]\nv_cndmask_b32 v19, v254, v19, s[56:57]\nv_max3_f32 v254, v16, v17, v18\n" \
    "v_max_f32 v254, v254, v19\ns_nop 1\nv_max_f32_dpp v252, v254, v254 row_ror:8 row_mask:0xf bank_mask:0xf\n" \
    "v_max_f32_dpp v253, v254, v254 row_ror:8 row_mask:0xf bank_mask:0xf\ns_nop 1\nv_permlane32_swap_b32 v252, v253\nv_max_f32 v252, v252, v253\nv_cvt_pk_bf16_f32 v24, 0, v24\n" \
    "v_cvt_pk_bf16_f32 v25, 0, v25\nv_cvt_pk_bf16_f32 v26, 0, v26\nv_cvt_pk_bf16_f32 v27, 0, v27\nv_mul_f32 v24, s33, v24\nv_mul_f32 v25, s33, v25\nv_mul_f32 v26, s33, v26\n" \
    "v_mul_f32 v27, s33, v27\nv_cvt_pk_bf16_f32 v24, 0, v24\nv_cvt_pk_bf16_f32 v25, 0, v25\nv_cvt_pk_bf16_f32 v26, 0, v26\nv_cvt_pk_bf16_f32 v27, 0, v27\n" \
    "v_max3_f32 v254, v252, v24, v25\nv_max3_f32 v252, v254, v26, v27\nv_max3_f32 v253, v254, v26, v27\ns_nop 1\nv_permlane16_swap_b32 v252, v253\nv_max_f32 v252, v252, v253\n" \
    "v_pk_add_f32 v[16:17], v[16:17], v[252:253] op_sel_hi:[1,0] neg_lo:[0,1] neg_hi:[0,1]\n" \
    "v_pk_add_f32 v[18:19], v[18:19], v[252:253] op_sel_hi:[1,0] neg_lo:[0,1] neg_hi:[0,1]\n" \
    "v_pk_add_f32 v[24:25], v[24:25], v[252:253] op_sel_hi:[1,0] neg_lo:[0,1] neg_hi:[0,1]\n" \
    "v_pk_add_f32 v[26:27], v[26:27], v[252:253] op_sel_hi:[1,0] neg_lo:[0,1] neg_hi:[0,1]\nv_mul_f32 v16, s34, v16\nv_mul_f32 v17, s34, v17\nv_mul_f32 v18, s34, v18\n" \
    "v_mul_f32 v19, s34, v19\nv_mul_f32 v24, s34, v24\nv_mul_f32 v25, s34, v25\nv_mul_f32 v26, s34, v26\nv_mul_f32 v27, s34, v27\nv_exp_f32 v16, v16\nv_exp_f32 v17, v17\n" \
    "v_exp_f32 v18, v18\nv_exp_f32 v19, v19\nv_exp_f32 v24, v24\nv_pk_add_f32 v[254:255], v[16:17], v[18:19]\nv_exp_f32 v25, v25\nv_add_f32 v254, v254, v255\n" \
    "v_exp_f32 v26, v26\nv_exp_f32 v27, v27\nv_add_f32_dpp v252, v254, v254 row_ror:8 row_mask:0xf bank_mask:0xf\n" \
    "v_add_f32_dpp v253, v254, v254 row_ror:8 row_mask:0xf bank_mask:0xf\nv_pk_add_f32 v[254:255], v[24:25], v[26:27]\ns_nop 0\nv_permlane32_swap_b32 v252, v253\n" \
    "v_pk_add_f32 v[254:255], v[254:255], v[252:253]\nv_pk_add_f32 v[252:253], v[254:255], v[254:255] op_sel:[0,1] op_sel_hi:[1,0]\ns_nop 1\nv_permlane16_swap_b32 v252, v253\n" \
    "v_add_f32 v252, v252, v253\nv_rcp_f32 v252, v252\ns_nop 0\nv_pk_mul_f32 v[16:17], v[16:17], v[252:253] op_sel_hi:[1,0]\n" \
    "v_pk_mul_f32 v[24:25], v[24:25], v[252:253] op_sel_hi:[1,0]\nv_cvt_pk_bf16_f32 v20, v16, v17\nv_cvt_pk_bf16_f32 v22, v24, v25\n" \
    "v_pk_mul_f32 v[18:19], v[18:19], v[252:253] op_sel_hi:[1,0]\nv_pk_mul_f32 v[26:27], v[26:27], v[252:253] op_sel_hi:[1,0]\nv_cvt_pk_bf16_f32 v21, v18, v19\n" \
    "v_cvt_pk_bf16_f32 v23, v26, v27\nv_permlane32_swap_b32 v20, v22\ns_nop 0\nv_permlane32_swap_b32 v21, v23\nv_writelane_b32 v20, s18, 40\ns_xor_b32 s74, s83, 0x2000\n" \
    "buffer_store_dwordx4 v[20:23], v7, s[64:67], s83 offen sc1\nbuffer_store_dwordx4 v[20:23], v7, s[64:67], s74 offen sc1\ns_mov_b32 s83, 0x7fff0000\ns_cmp_lt_u32 s35, 64\n" \
    "s_cbranch_scc1 .Lq2_%=\ns_cmp_lt_u32 s19, 64\ns_cbranch_scc1 .Lq9_%=\ns_endpgm\n.Lq8_%=:\ns_mov_b32 s35, 65\nds_read_b128 v[36:39], v5 offset:0\n" \
    "ds_read_b128 v[64:67], v5 offset:1024\nds_read_b128 v[68:71], v5 offset:2048\nds_read_b128 v[40:43], v5 offset:3072\ns_waitcnt lgkmcnt(0)\ns_barrier\n" \
    "ds_read_b128 v[44:47], v8 offset:0\nds_read_b128 v[84:87], v8 offset:512\nds_read_b128 v[88:91], v8 offset:1024\nds_read_b128 v[28:31], v8 offset:1536\n" \
    "s_waitcnt lgkmcnt(0)\ns_branch .Lq91_%=\n.Lq11_%=:\ns_sleep 6\nbuffer_load_dword v13, off, s[64:67], s42 sc1\ns_waitcnt vmcnt(0)\nv_readfirstlane_b32 s74, v13\ns_nop 0\n" \
    "s_and_b32 s75, s74, 127\ns_lshr_b32 s69, s74, 8\ns_sub_u32 s69, s69, s80\ns_bfe_i32 s69, s69, 0x180000\ns_cmp_eq_u32 s75, 0\ns_cselect_b32 s69, 0, s69\n" \
    "s_add_u32 s80, s80, s69\ns_lshl_b32 s74, s74, 5\ns_and_b32 s74, s74, 0x1000\ns_andn2_b32 s81, s81, 0x1000\ns_or_b32 s81, s81, s74\ns_lshl_b32 s74, s74, 2\n" \
    "s_andn2_b32 s82, s82, 0x4000\ns_or_b32 s82, s82, s74\ns_add_u32 s68, s68, 1\ns_and_b32 s69, s68, 63\ns_cbranch_scc1 .Lq10_%=\ns_cmp_gt_u32 s68, 0x400000\n" \
    "s_cbranch_scc1 .Lq1_%=\nbuffer_load_dword v253, off, s[64:67], 0 sc1\ns_waitcnt vmcnt(0)\nv_readfirstlane_b32 s69, v253\ns_nop 0\ns_cmp_eq_u32 s69, 1\n" \
    "s_cbranch_scc0 .Lq10_%=\ns_endpgm\n.Lq16_%=:\ns_memrealtime s[72:73]\ns_waitcnt lgkmcnt(0)\ns_sub_u32 s72, s72, s76\ns_cmp_lt_i32 s72, 0\ns_cbranch_scc1 .Lq16_%=\n" \
    "buffer_load_dword v13, off, s[64:67], s42 sc1\ns_waitcnt vmcnt(0)\nv_readfirstlane_b32 s74, v13\ns_nop 0\ns_and_b32 s75, s74, 127\ns_lshr_b32 s69, s74, 8\n" \
    "s_sub_u32 s69, s69, s80\ns_bfe_i32 s69, s69, 0x180000\ns_cmp_eq_u32 s75, 0\ns_cselect_b32 s69, 0, s69\ns_add_u32 s80, s80, s69\ns_lshl_b32 s74, s74, 5\n" \
    "s_and_b32 s74, s74, 0x1000\ns_andn2_b32 s81, s81, 0x1000\ns_or_b32 s81, s81, s74\ns_lshl_b32 s74, s74, 2\ns_andn2_b32 s82, s82, 0x4000\ns_or_b32 s82, s82, s74\n" \
    "s_branch .Lq13_%=\n.Lq24_%=:\nv_mov_b32 v252, 0\nv_max3_u32 v252, v252, v72, v73\nv_max3_u32 v252, v252, v74, v75\nv_max3_u32 v252, v252, v76, v77\n" \
    "v_max3_u32 v252, v252, v78, v79\nv_cmp_eq_u32 vcc, -1, v252\ns_nop 0\ns_cbranch_vccz .Lq20_%=\ns_add_u32 s80, s80, s43\ns_xor_b32 s81, s81, 0x1000\ns_xor_b32 s24, s81, 0\n" \
    "s_xor_b32 s25, s81, 1024\ns_xor_b32 s26, s81, 2048\ns_xor_b32 s27, s81, 3072\ns_xor_b32 s82, s82, 0x4000\ns_cmp_eq_u32 s61, 0\ns_cbranch_scc1 .Lq27_%=\n" \
    "s_lshr_b32 s69, s81, 5\ns_and_b32 s69, s69, 128\ns_lshl_b32 s74, s80, 8\ns_add_u32 s74, s74, s69\ns_add_u32 s74, s74, s35\nv_mov_b32 v253, s74\nv_lshlrev_b32 v252, 7, v2\n" \
    "s_mov_b64 exec, 1\nbuffer_store_dword v253, v252, s[64:67], s61 offen sc1\ns_mov_b64 exec, -1\n.Lq27_%=:\ns_add_u32 s68, s68, 1\ns_and_b32 s69, s68, 63\n" \
    "s_cbranch_scc1 .Lq14_%=\ns_cmp_gt_u32 s68, 0x400000\ns_cbranch_scc1 .Lq1_%=\nbuffer_load_dword v253, off, s[64:67], 0 sc1\ns_waitcnt vmcnt(0)\n" \
    "v_readfirstlane_b32 s69, v253\ns_nop 0\ns_cmp_eq_u32 s69, 1\ns_cbranch_scc0 .Lq14_%=\ns_endpgm\n.Lq14_%=:\ns_memrealtime s[72:73]\ns_waitcnt lgkmcnt(0)\n" \
    "s_sub_u32 s72, s72, s80\ns_cmp_lt_i32 s72, 0\ns_cbranch_scc1 .Lq14_%=\nbuffer_load_dwordx4 v[72:75], v2, s[64:67], s24 offen sc1\ns_nop 7\n" \
    "buffer_load_dwordx4 v[76:79], v2, s[64:67], s25 offen sc1\ns_nop 7\nbuffer_load_dwordx4 v[80:83], v2, s[64:67], s26 offen sc1\ns_nop 7\n" \
    "buffer_load_dwordx4 v[84:87], v2, s[64:67], s27 offen sc1\ns_nop 7\ns_branch .Lq17_%=\n.Lq25_%=:\nv_mov_b32 v252, 0\nv_max3_u32 v252, v252, v80, v81\n" \
    "v_max3_u32 v252, v252, v82, v83\nv_cmp_eq_u32 vcc, -1, v252\ns_nop 0\ns_cbranch_vccz .Lq21_%=\ns_add_u32 s80, s80, s43\ns_xor_b32 s81, s81, 0x1000\ns_xor_b32 s24, s81, 0\n" \
    "s_xor_b32 s25, s81, 1024\ns_xor_b32 s26, s81, 2048\ns_xor_b32 s27, s81, 3072\ns_xor_b32 s82, s82, 0x4000\ns_cmp_eq_u32 s61, 0\ns_cbranch_scc1 .Lq29_%=\n" \
    "s_lshr_b32 s69, s81, 5\ns_and_b32 s69, s69, 128\ns_lshl_b32 s74, s80, 8\ns_add_u32 s74, s74, s69\ns_add_u32 s74, s74, s35\nv_mov_b32 v253, s74\nv_lshlrev_b32 v252, 7, v2\n" \
    "s_mov_b64 exec, 1\nbuffer_store_dword v253, v252, s[64:67], s61 offen sc1\ns_mov_b64 exec, -1\n.Lq29_%=:\n.Lq28_%=:\ns_memrealtime s[72:73]\ns_waitcnt vmcnt(0) lgkmcnt(0)\n" \
    "s_sub_u32 s72, s72, s80\ns_cmp_lt_i32 s72, 0\ns_cbranch_scc1 .Lq28_%=\nbuffer_load_dwordx4 v[80:83], v2, s[64:67], s26 offen sc1\n" \
    "buffer_load_dwordx4 v[84:87], v2, s[64:67], s27 offen sc1\ns_waitcnt vmcnt(0)\ns_add_u32 s68, s68, 1\ns_and_b32 s69, s68, 63\ns_cbranch_scc1 .Lq25_%=\n" \
    "s_cmp_gt_u32 s68, 0x400000\ns_cbranch_scc1 .Lq1_%=\nbuffer_load_dword v253, off, s[64:67], 0 sc1\ns_waitcnt vmcnt(0)\nv_readfirstlane_b32 s69, v253\ns_nop 0\n" \
    "s_cmp_eq_u32 s69, 1\ns_cbranch_scc0 .Lq25_%=\ns_endpgm\n.Lq26_%=:\nv_mov_b32 v252, 0\nv_max3_u32 v252, v252, v84, v85\nv_max3_u32 v252, v252, v86, v87\n" \
    "v_cmp_eq_u32 vcc, -1, v252\ns_nop 0\ns_cbranch_vccz .Lq22_%=\ns_add_u32 s80, s80, s43\ns_xor_b32 s81, s81, 0x1000\ns_xor_b32 s24, s81, 0\ns_xor_b32 s25, s81, 1024\n" \
    "s_xor_b32 s26, s81, 2048\ns_xor_b32 s27, s81, 3072\ns_xor_b32 s82, s82, 0x4000\ns_cmp_eq_u32 s61, 0\ns_cbranch_scc1 .Lq31_%=\ns_lshr_b32 s69, s81, 5\n" \
    "s_and_b32 s69, s69, 128\ns_lshl_b32 s74, s80, 8\ns_add_u32 s74, s74, s69\ns_add_u32 s74, s74, s35\nv_mov_b32 v253, s74\nv_lshlrev_b32 v252, 7, v2\ns_mov_b64 exec, 1\n" \
    "buffer_store_dword v253, v252, s[64:67], s61 offen sc1\ns_mov_b64 exec, -1\n.Lq31_%=:\n.Lq30_%=:\ns_memrealtime s[72:73]\ns_waitcnt vmcnt(0) lgkmcnt(0)\n" \
    "s_sub_u32 s72, s72, s80\ns_cmp_lt_i32 s72, 0\ns_cbranch_scc1 .Lq30_%=\nbuffer_load_dwordx4 v[84:87], v2, s[64:67], s27 offen sc1\ns_waitcnt vmcnt(0)\n" \
    "s_add_u32 s68, s68, 1\ns_and_b32 s69, s68, 63\ns_cbranch_scc1 .Lq26_%=\ns_cmp_gt_u32 s68, 0x400000\ns_cbranch_scc1 .Lq1_%=\nbuffer_load_dword v253, off, s[64:67], 0 sc1\n" \
    "s_waitcnt vmcnt(0)\nv_readfirstlane_b32 s69, v253\ns_nop 0\ns_cmp_eq_u32 s69, 1\ns_cbranch_scc0 .Lq26_%=\ns_endpgm\n.Lq35_%=:\ns_memrealtime s[72:73]\n" \
    "s_waitcnt lgkmcnt(0)\ns_sub_u32 s72, s72, s76\ns_cmp_lt_i32 s72, 0\ns_cbranch_scc1 .Lq35_%=\nbuffer_load_dword v13, off, s[64:67], s42 sc1\ns_waitcnt vmcnt(0)\n" \
    "v_readfirstlane_b32 s74, v13\ns_nop 0\ns_and_b32 s75, s74, 127\ns_lshr_b32 s69, s74, 8\ns_sub_u32 s69, s69, s80\ns_bfe_i32 s69, s69, 0x180000\ns_cmp_eq_u32 s75, 0\n" \
    "s_cselect_b32 s69, 0, s69\ns_add_u32 s80, s80, s69\ns_lshl_b32 s74, s74, 5\ns_and_b32 s74, s74, 0x1000\ns_andn2_b32 s81, s81, 0x1000\ns_or_b32 s81, s81, s74\n" \
    "s_lshl_b32 s74, s74, 2\ns_andn2_b32 s82, s82, 0x4000\ns_or_b32 s82, s82, s74\ns_branch .Lq32_%=\n.Lq43_%=:\nv_mov_b32 v252, 0\nv_max3_u32 v252, v252, v72, v73\n" \
    "v_max3_u32 v252, v252, v74, v75\nv_max3_u32 v252, v252, v76, v77\nv_max3_u32 v252, v252, v78, v79\nv_cmp_eq_u32 vcc, -1, v252\ns_nop 0\ns_cbranch_vccz .Lq39_%=\n" \
    "s_add_u32 s80, s80, s43\ns_xor_b32 s81, s81, 0x1000\ns_xor_b32 s24, s81, 0\ns_xor_b32 s25, s81, 1024\ns_xor_b32 s26, s81, 2048\ns_xor_b32 s27, s81, 3072\n" \
    "s_xor_b32 s82, s82, 0x4000\ns_cmp_eq_u32 s61, 0\ns_cbranch_scc1 .Lq46_%=\ns_lshr_b32 s69, s81, 5\ns_and_b32 s69, s69, 128\ns_lshl_b32 s74, s80, 8\ns_add_u32 s74, s74, s69\n" \
    "s_add_u32 s74, s74, s35\nv_mov_b32 v253, s74\nv_lshlrev_b32 v252, 7, v2\ns_mov_b64 exec, 1\nbuffer_store_dword v253, v252, s[64:67], s61 offen sc1\ns_mov_b64 exec, -1\n" \
    ".Lq46_%=:\ns_add_u32 s68, s68, 1\ns_and_b32 s69, s68, 63\ns_cbranch_scc1 .Lq33_%=\ns_cmp_gt_u32 s68, 0x400000\ns_cbranch_scc1 .Lq1_%=\n" \
    "buffer_load_dword v253, off, s[64:67], 0 sc1\ns_waitcnt vmcnt(0)\nv_readfirstlane_b32 s69, v253\ns_nop 0\ns_cmp_eq_u32 s69, 1\ns_cbranch_scc0 .Lq33_%=\ns_endpgm\n" \
    ".Lq33_%=:\ns_memrealtime s[72:73]\ns_waitcnt lgkmcnt(0)\ns_sub_u32 s72, s72, s80\ns_cmp_lt_i32 s72, 0\ns_cbranch_scc1 .Lq33_%=\n" \
    "buffer_load_dwordx4 v[72:75], v2, s[64:67], s24 offen sc1\ns_nop 7\nbuffer_load_dwordx4 v[76:79], v2, s[64:67], s25 offen sc1\ns_nop 7\n" \
    "buffer_load_dwordx4 v[80:83], v2, s[64:67], s26 offen sc1\ns_nop 7\nbuffer_load_dwordx4 v[84:87], v2, s[64:67], s27 offen sc1\ns_nop 7\ns_branch .Lq36_%=\n.Lq44_%=:\n" \
    "v_mov_b32 v252, 0\nv_max3_u32 v252, v252, v80, v81\nv_max3_u32 v252, v252, v82, v83\nv_cmp_eq_u32 vcc, -1, v252\ns_nop 0\ns_cbranch_vccz .Lq40_%=\ns_add_u32 s80, s80, s43\n" \
    "s_xor_b32 s81, s81, 0x1000\ns_xor_b32 s24, s81, 0\ns_xor_b32 s25, s81, 1024\ns_xor_b32 s26, s81, 2048\ns_xor_b32 s27, s81, 3072\ns_xor_b32 s82, s82, 0x4000\n" \
    "s_cmp_eq_u32 s61, 0\ns_cbranch_scc1 .Lq48_%=\ns_lshr_b32 s69, s81, 5\ns_and_b32 s69, s69, 128\ns_lshl_b32 s74, s80, 8\ns_add_u32 s74, s74, s69\ns_add_u32 s74, s74, s35\n" \
    "v_mov_b32 v253, s74\nv_lshlrev_b32 v252, 7, v2\ns_mov_b64 exec, 1\nbuffer_store_dword v253, v252, s[64:67], s61 offen sc1\ns_mov_b64 exec, -1\n.Lq48_%=:\n.Lq47_%=:\n" \
    "s_memrealtime s[72:73]\ns_waitcnt vmcnt(0) lgkmcnt(0)\ns_sub_u32 s72, s72, s80\ns_cmp_lt_i32 s72, 0\ns_cbranch_scc1 .Lq47_%=\n" \
    "buffer_load_dwordx4 v[80:83], v2, s[64:67], s26 offen sc1\nbuffer_load_dwordx4 v[84:87], v2, s[64:67], s27 offen sc1\ns_waitcnt vmcnt(0)\ns_add_u32 s68, s68, 1\n" \
    "s_and_b32 s69, s68, 63\ns_cbranch_scc1 .Lq44_%=\ns_cmp_gt_u32 s68, 0x400000\ns_cbranch_scc1 .Lq1_%=\nbuffer_load_dword v253, off, s[64:67], 0 sc1\ns_waitcnt vmcnt(0)\n" \
    "v_readfirstlane_b32 s69, v253\ns_nop 0\ns_cmp_eq_u32 s69, 1\ns_cbranch_scc0 .Lq44_%=\ns_endpgm\n.Lq45_%=:\nv_mov_b32 v252, 0\nv_max3_u32 v252, v252, v84, v85\n" \
    "v_max3_u32 v252, v252, v86, v87\nv_cmp_eq_u32 vcc, -1, v252\ns_nop 0\ns_cbranch_vccz .Lq41_%=\ns_add_u32 s80, s80, s43\ns_xor_b32 s81, s81, 0x1000\ns_xor_b32 s24, s81, 0\n" \
    "s_xor_b32 s25, s81, 1024\ns_xor_b32 s26, s81, 2048\ns_xor_b32 s27, s81, 3072\ns_xor_b32 s82, s82, 0x4000\ns_cmp_eq_u32 s61, 0\ns_cbranch_scc1 .Lq50_%=\n" \
    "s_lshr_b32 s69, s81, 5\ns_and_b32 s69, s69, 128\ns_lshl_b32 s74, s80, 8\ns_add_u32 s74, s74, s69\ns_add_u32 s74, s74, s35\nv_mov_b32 v253, s74\nv_lshlrev_b32 v252, 7, v2\n" \
    "s_mov_b64 exec, 1\nbuffer_store_dword v253, v252, s[64:67], s61 offen sc1\ns_mov_b64 exec, -1\n.Lq50_%=:\n.Lq49_%=:\ns_memrealtime s[72:73]\ns_waitcnt vmcnt(0) lgkmcnt(0)\n" \
    "s_sub_u32 s72, s72, s80\ns_cmp_lt_i32 s72, 0\ns_cbranch_scc1 .Lq49_%=\nbuffer_load_dwordx4 v[84:87], v2, s[64:67], s27 offen sc1\ns_waitcnt vmcnt(0)\n" \
    "s_add_u32 s68, s68, 1\ns_and_b32 s69, s68, 63\ns_cbranch_scc1 .Lq45_%=\ns_cmp_gt_u32 s68, 0x400000\ns_cbranch_scc1 .Lq1_%=\nbuffer_load_dword v253, off, s[64:67], 0 sc1\n" \
    "s_waitcnt vmcnt(0)\nv_readfirstlane_b32 s69, v253\ns_nop 0\ns_cmp_eq_u32 s69, 1\ns_cbranch_scc0 .Lq45_%=\ns_endpgm\n.Lq54_%=:\ns_memrealtime s[72:73]\n" \
    "s_waitcnt lgkmcnt(0)\ns_sub_u32 s72, s72, s76\ns_cmp_lt_i32 s72, 0\ns_cbranch_scc1 .Lq54_%=\nbuffer_load_dword v13, off, s[64:67], s42 sc1\ns_waitcnt vmcnt(0)\n" \
    "v_readfirstlane_b32 s74, v13\ns_nop 0\ns_and_b32 s75, s74, 127\ns_lshr_b32 s69, s74, 8\ns_sub_u32 s69, s69, s80\ns_bfe_i32 s69, s69, 0x180000\ns_cmp_eq_u32 s75, 0\n" \
    "s_cselect_b32 s69, 0, s69\ns_add_u32 s80, s80, s69\ns_lshl_b32 s74, s74, 5\ns_and_b32 s74, s74, 0x1000\ns_andn2_b32 s81, s81, 0x1000\ns_or_b32 s81, s81, s74\n" \
    "s_lshl_b32 s74, s74, 2\ns_andn2_b32 s82, s82, 0x4000\ns_or_b32 s82, s82, s74\ns_branch .Lq51_%=\n.Lq62_%=:\nv_mov_b32 v252, 0\nv_max3_u32 v252, v252, v72, v73\n" \
    "v_max3_u32 v252, v252, v74, v75\nv_max3_u32 v252, v252, v76, v77\nv_max3_u32 v252, v252, v78, v79\nv_cmp_eq_u32 vcc, -1, v252\ns_nop 0\ns_cbranch_vccz .Lq58_%=\n" \
    "s_add_u32 s80, s80, s43\ns_xor_b32 s81, s81, 0x1000\ns_xor_b32 s24, s81, 0\ns_xor_b32 s25, s81, 1024\ns_xor_b32 s26, s81, 2048\ns_xor_b32 s27, s81, 3072\n" \
    "s_xor_b32 s82, s82, 0x4000\ns_cmp_eq_u32 s61, 0\ns_cbranch_scc1 .Lq65_%=\ns_lshr_b32 s69, s81, 5\ns_and_b32 s69, s69, 128\ns_lshl_b32 s74, s80, 8\ns_add_u32 s74, s74, s69\n" \
    "s_add_u32 s74, s74, s35\nv_mov_b32 v253, s74\nv_lshlrev_b32 v252, 7, v2\ns_mov_b64 exec, 1\nbuffer_store_dword v253, v252, s[64:67], s61 offen sc1\ns_mov_b64 exec, -1\n" \
    ".Lq65_%=:\ns_add_u32 s68, s68, 1\ns_and_b32 s69, s68, 63\ns_cbranch_scc1 .Lq52_%=\ns_cmp_gt_u32 s68, 0x400000\ns_cbranch_scc1 .Lq1_%=\n" \
    "buffer_load_dword v253, off, s[64:67], 0 sc1\ns_waitcnt vmcnt(0)\nv_readfirstlane_b32 s69, v253\ns_nop 0\ns_cmp_eq_u32 s69, 1\ns_cbranch_scc0 .Lq52_%=\ns_endpgm\n" \
    ".Lq52_%=:\ns_memrealtime s[72:73]\ns_waitcnt lgkmcnt(0)\ns_sub_u32 s72, s72, s80\ns_cmp_lt_i32 s72, 0\ns_cbranch_scc1 .Lq52_%=\n" \
    "buffer_load_dwordx4 v[72:75], v2, s[64:67], s24 offen sc1\ns_nop 7\nbuffer_load_dwordx4 v[76:79], v2, s[64:67], s25 offen sc1\ns_nop 7\n" \
    "buffer_load_dwordx4 v[80:83], v2, s[64:67], s26 offen sc1\ns_nop 7\nbuffer_load_dwordx4 v[84:87], v2, s[64:67], s27 offen sc1\ns_nop 7\ns_branch .Lq55_%=\n.Lq63_%=:\n" \
    "v_mov_b32 v252, 0\nv_max3_u32 v252, v252, v80, v81\nv_max3_u32 v252, v252, v82, v83\nv_cmp_eq_u32 vcc, -1, v252\ns_nop 0\ns_cbranch_vccz .Lq59_%=\ns_add_u32 s80, s80, s43\n" \
    "s_xor_b32 s81, s81, 0x1000\ns_xor_b32 s24, s81, 0\ns_xor_b32 s25, s81, 1024\ns_xor_b32 s26, s81, 2048\ns_xor_b32 s27, s81, 3072\ns_xor_b32 s82, s82, 0x4000\n" \
    "s_cmp_eq_u32 s61, 0\ns_cbranch_scc1 .Lq67_%=\ns_lshr_b32 s69, s81, 5\ns_and_b32 s69, s69, 128\ns_lshl_b32 s74, s80, 8\ns_add_u32 s74, s74, s69\ns_add_u32 s74, s74, s35\n" \
    "v_mov_b32 v253, s74\nv_lshlrev_b32 v252, 7, v2\ns_mov_b64 exec, 1\nbuffer_store_dword v253, v252, s[64:67], s61 offen sc1\ns_mov_b64 exec, -1\n.Lq67_%=:\n.Lq66_%=:\n" \
    "s_memrealtime s[72:73]\ns_waitcnt vmcnt(0) lgkmcnt(0)\ns_sub_u32 s72, s72, s80\ns_cmp_lt_i32 s72, 0\ns_cbranch_scc1 .Lq66_%=\n" \
    "buffer_load_dwordx4 v[80:83], v2, s[64:67], s26 offen sc1\nbuffer_load_dwordx4 v[84:87], v2, s[64:67], s27 offen sc1\ns_waitcnt vmcnt(0)\ns_add_u32 s68, s68, 1\n" \
    "s_and_b32 s69, s68, 63\ns_cbranch_scc1 .Lq63_%=\ns_cmp_gt_u32 s68, 0x400000\ns_cbranch_scc1 .Lq1_%=\nbuffer_load_dword v253, off, s[64:67], 0 sc1\ns_waitcnt vmcnt(0)\n" \
    "v_readfirstlane_b32 s69, v253\ns_nop 0\ns_cmp_eq_u32 s69, 1\ns_cbranch_scc0 .Lq63_%=\ns_endpgm\n.Lq64_%=:\nv_mov_b32 v252, 0\nv_max3_u32 v252, v252, v84, v85\n" \
    "v_max3_u32 v252, v252, v86, v87\nv_cmp_eq_u32 vcc, -1, v252\ns_nop 0\ns_cbranch_vccz .Lq60_%=\ns_add_u32 s80, s80, s43\ns_xor_b32 s81, s81, 0x1000\ns_xor_b32 s24, s81, 0\n" \
    "s_xor_b32 s25, s81, 1024\ns_xor_b32 s26, s81, 2048\ns_xor_b32 s27, s81, 3072\ns_xor_b32 s82, s82, 0x4000\ns_cmp_eq_u32 s61, 0\ns_cbranch_scc1 .Lq69_%=\n" \
    "s_lshr_b32 s69, s81, 5\ns_and_b32 s69, s69, 128\ns_lshl_b32 s74, s80, 8\ns_add_u32 s74, s74, s69\ns_add_u32 s74, s74, s35\nv_mov_b32 v253, s74\nv_lshlrev_b32 v252, 7, v2\n" \
    "s_mov_b64 exec, 1\nbuffer_store_dword v253, v252, s[64:67], s61 offen sc1\ns_mov_b64 exec, -1\n.Lq69_%=:\n.Lq68_%=:\ns_memrealtime s[72:73]\ns_waitcnt vmcnt(0) lgkmcnt(0)\n" \
    "s_sub_u32 s72, s72, s80\ns_cmp_lt_i32 s72, 0\ns_cbranch_scc1 .Lq68_%=\nbuffer_load_dwordx4 v[84:87], v2, s[64:67], s27 offen sc1\ns_waitcnt vmcnt(0)\n" \
    "s_add_u32 s68, s68, 1\ns_and_b32 s69, s68, 63\ns_cbranch_scc1 .Lq64_%=\ns_cmp_gt_u32 s68, 0x400000\ns_cbranch_scc1 .Lq1_%=\nbuffer_load_dword v253, off, s[64:67], 0 sc1\n" \
    "s_waitcnt vmcnt(0)\nv_readfirstlane_b32 s69, v253\ns_nop 0\ns_cmp_eq_u32 s69, 1\ns_cbranch_scc0 .Lq64_%=\ns_endpgm\n.Lq73_%=:\ns_memrealtime s[72:73]\n" \
    "s_waitcnt lgkmcnt(0)\ns_sub_u32 s72, s72, s76\ns_cmp_lt_i32 s72, 0\ns_cbranch_scc1 .Lq73_%=\nbuffer_load_dword v13, off, s[64:67], s42 sc1\ns_waitcnt vmcnt(0)\n" \
    "v_readfirstlane_b32 s74, v13\ns_nop 0\ns_and_b32 s75, s74, 127\ns_lshr_b32 s69, s74, 8\ns_sub_u32 s69, s69, s80\ns_bfe_i32 s69, s69, 0x180000\ns_cmp_eq_u32 s75, 0\n" \
    "s_cselect_b32 s69, 0, s69\ns_add_u32 s80, s80, s69\ns_lshl_b32 s74, s74, 5\ns_and_b32 s74, s74, 0x1000\ns_andn2_b32 s81, s81, 0x1000\ns_or_b32 s81, s81, s74\n" \
    "s_lshl_b32 s74, s74, 2\ns_andn2_b32 s82, s82, 0x4000\ns_or_b32 s82, s82, s74\ns_branch .Lq70_%=\n.Lq81_%=:\nv_mov_b32 v252, 0\nv_max3_u32 v252, v252, v72, v73\n" \
    "v_max3_u32 v252, v252, v74, v75\nv_max3_u32 v252, v252, v76, v77\nv_max3_u32 v252, v252, v78, v79\nv_cmp_eq_u32 vcc, -1, v252\ns_nop 0\ns_cbranch_vccz .Lq77_%=\n" \
    "s_add_u32 s80, s80, s43\ns_xor_b32 s81, s81, 0x1000\ns_xor_b32 s24, s81, 0\ns_xor_b32 s25, s81, 1024\ns_xor_b32 s26, s81, 2048\ns_xor_b32 s27, s81, 3072\n" \
    "s_xor_b32 s82, s82, 0x4000\ns_cmp_eq_u32 s61, 0\ns_cbranch_scc1 .Lq84_%=\ns_lshr_b32 s69, s81, 5\ns_and_b32 s69, s69, 128\ns_lshl_b32 s74, s80, 8\ns_add_u32 s74, s74, s69\n" \
    "s_add_u32 s74, s74, s35\nv_mov_b32 v253, s74\nv_lshlrev_b32 v252, 7, v2\ns_mov_b64 exec, 1\nbuffer_store_dword v253, v252, s[64:67], s61 offen sc1\ns_mov_b64 exec, -1\n" \
    ".Lq84_%=:\ns_add_u32 s68, s68, 1\ns_and_b32 s69, s68, 63\ns_cbranch_scc1 .Lq71_%=\ns_cmp_gt_u32 s68, 0x400000\ns_cbranch_scc1 .Lq1_%=\n" \
    "buffer_load_dword v253, off, s[64:67], 0 sc1\ns_waitcnt vmcnt(0)\nv_readfirstlane_b32 s69, v253\ns_nop 0\ns_cmp_eq_u32 s69, 1\ns_cbranch_scc0 .Lq71_%=\ns_endpgm\n" \
    ".Lq71_%=:\ns_memrealtime s[72:73]\ns_waitcnt lgkmcnt(0)\ns_sub_u32 s72, s72, s80\ns_cmp_lt_i32 s72, 0\ns_cbranch_scc1 .Lq71_%=\n" \
    "buffer_load_dwordx4 v[72:75], v2, s[64:67], s24 offen sc1\ns_nop 7\nbuffer_load_dwordx4 v[76:79], v2, s[64:67], s25 offen sc1\ns_nop 7\n" \
    "buffer_load_dwordx4 v[80:83], v2, s[64:67], s26 offen sc1\ns_nop 7\nbuffer_load_dwordx4 v[84:87], v2, s[64:67], s27 offen sc1\ns_nop 7\ns_branch .Lq74_%=\n.Lq82_%=:\n" \
    "v_mov_b32 v252, 0\nv_max3_u32 v252, v252, v80, v81\nv_max3_u32 v252, v252, v82, v83\nv_cmp_eq_u32 vcc, -1, v252\ns_nop 0\ns_cbranch_vccz .Lq78_%=\ns_add_u32 s80, s80, s43\n" \
    "s_xor_b32 s81, s81, 0x1000\ns_xor_b32 s24, s81, 0\ns_xor_b32 s25, s81, 1024\ns_xor_b32 s26, s81, 2048\ns_xor_b32 s27, s81, 3072\ns_xor_b32 s82, s82, 0x4000\n" \
    "s_cmp_eq_u32 s61, 0\ns_cbranch_scc1 .Lq86_%=\ns_lshr_b32 s69, s81, 5\ns_and_b32 s69, s69, 128\ns_lshl_b32 s74, s80, 8\ns_add_u32 s74, s74, s69\ns_add_u32 s74, s74, s35\n" \
    "v_mov_b32 v253, s74\nv_lshlrev_b32 v252, 7, v2\ns_mov_b64 exec, 1\nbuffer_store_dword v253, v252, s[64:67], s61 offen sc1\ns_mov_b64 exec, -1\n.Lq86_%=:\n.Lq85_%=:\n" \
    "s_memrealtime s[72:73]\ns_waitcnt vmcnt(0) lgkmcnt(0)\ns_sub_u32 s72, s72, s80\ns_cmp_lt_i32 s72, 0\ns_cbranch_scc1 .Lq85_%=\n" \
    "buffer_load_dwordx4 v[80:83], v2, s[64:67], s26 offen sc1\nbuffer_load_dwordx4 v[84:87], v2, s[64:67], s27 offen sc1\ns_waitcnt vmcnt(0)\ns_add_u32 s68, s68, 1\n" \
    "s_and_b32 s69, s68, 63\ns_cbranch_scc1 .Lq82_%=\ns_cmp_gt_u32 s68, 0x400000\ns_cbranch_scc1 .Lq1_%=\nbuffer_load_dword v253, off, s[64:67], 0 sc1\ns_waitcnt vmcnt(0)\n" \
    "v_readfirstlane_b32 s69, v253\ns_nop 0\ns_cmp_eq_u32 s69, 1\ns_cbranch_scc0 .Lq82_%=\ns_endpgm\n.Lq83_%=:\nv_mov_b32 v252, 0\nv_max3_u32 v252, v252, v84, v85\n" \
    "v_max3_u32 v252, v252, v86, v87\nv_cmp_eq_u32 vcc, -1, v252\ns_nop 0\ns_cbranch_vccz .Lq79_%=\ns_add_u32 s80, s80, s43\ns_xor_b32 s81, s81, 0x1000\ns_xor_b32 s24, s81, 0\n" \
    "s_xor_b32 s25, s81, 1024\ns_xor_b32 s26, s81, 2048\ns_xor_b32 s27, s81, 3072\ns_xor_b32 s82, s82, 0x4000\ns_cmp_eq_u32 s61, 0\ns_cbranch_scc1 .Lq88_%=\n" \
    "s_lshr_b32 s69, s81, 5\ns_and_b32 s69, s69, 128\ns_lshl_b32 s74, s80, 8\ns_add_u32 s74, s74, s69\ns_add_u32 s74, s74, s35\nv_mov_b32 v253, s74\nv_lshlrev_b32 v252, 7, v2\n" \
    "s_mov_b64 exec, 1\nbuffer_store_dword v253, v252, s[64:67], s61 offen sc1\ns_mov_b64 exec, -1\n.Lq88_%=:\n.Lq87_%=:\ns_memrealtime s[72:73]\ns_waitcnt vmcnt(0) lgkmcnt(0)\n" \
    "s_sub_u32 s72, s72, s80\ns_cmp_lt_i32 s72, 0\ns_cbranch_scc1 .Lq87_%=\nbuffer_load_dwordx4 v[84:87], v2, s[64:67], s27 offen sc1\ns_waitcnt vmcnt(0)\n" \
    "s_add_u32 s68, s68, 1\ns_and_b32 s69, s68, 63\ns_cbranch_scc1 .Lq83_%=\ns_cmp_gt_u32 s68, 0x400000\ns_cbranch_scc1 .Lq1_%=\nbuffer_load_dword v253, off, s[64:67], 0 sc1\n" \
    "s_waitcnt vmcnt(0)\nv_readfirstlane_b32 s69, v253\ns_nop 0\ns_cmp_eq_u32 s69, 1\ns_cbranch_scc0 .Lq83_%=\ns_endpgm\n.Lq12_%=:\ns_mov_b32 s68, 0\n.Lq89_%=:\n" \
    "ds_read_b128 v[36:39], v6 offset:0\nds_read_b128 v[44:47], v8 offset:0\nds_read_b128 v[64:67], v6 offset:512\nds_read_b128 v[84:87], v8 offset:512\n" \
    "ds_read_b128 v[68:71], v6 offset:1024\nds_read_b128 v[88:91], v8 offset:1024\nds_read_b128 v[40:43], v6 offset:1536\nds_read_b128 v[28:31], v8 offset:1536\n" \
    "v_mov_b32 v252, 0\ns_waitcnt lgkmcnt(0)\nv_pk_max_u16 v252, v252, v36\nv_pk_max_u16 v252, v252, v37\nv_pk_max_u16 v252, v252, v38\nv_pk_max_u16 v252, v252, v39\n" \
    "v_pk_max_u16 v252, v252, v64\nv_pk_max_u16 v252, v252, v65\nv_pk_max_u16 v252, v252, v66\nv_pk_max_u16 v252, v252, v67\nv_pk_max_u16 v252, v252, v68\n" \
    "v_pk_max_u16 v252, v252, v69\nv_pk_max_u16 v252, v252, v70\nv_pk_max_u16 v252, v252, v71\nv_pk_max_u16 v252, v252, v40\nv_pk_max_u16 v252, v252, v41\n" \
    "v_pk_max_u16 v252, v252, v42\nv_pk_max_u16 v252, v252, v43\nv_pk_max_u16 v252, v252, v44\nv_pk_max_u16 v252, v252, v45\nv_pk_max_u16 v252, v252, v46\n" \
    "v_pk_max_u16 v252, v252, v47\nv_pk_max_u16 v252, v252, v84\nv_pk_max_u16 v252, v252, v85\nv_pk_max_u16 v252, v252, v86\nv_pk_max_u16 v252, v252, v87\n" \
    "v_pk_max_u16 v252, v252, v88\nv_pk_max_u16 v252, v252, v89\nv_pk_max_u16 v252, v252, v90\nv_pk_max_u16 v252, v252, v91\nv_pk_max_u16 v252, v252, v28\n" \
    "v_pk_max_u16 v252, v252, v29\nv_pk_max_u16 v252, v252, v30\nv_pk_max_u16 v252, v252, v31\nv_pk_max_u16 v252, v252, v252 op_sel:[0,1] op_sel_hi:[1,0]\n" \
    "v_cmp_eq_u32 vcc, -1, v252\ns_nop 0\ns_cbranch_vccnz .Lq90_%=\nv_mfma_f32_16x16x32_bf16 v[16:19], v[48:51], v[36:39], 0\n" \
    "v_mfma_f32_16x16x32_bf16 v[20:23], v[52:55], v[36:39], 0\nv_mfma_f32_16x16x32_bf16 v[16:19], v[72:75], v[64:67], v[16:19]\n" \
    "v_mfma_f32_16x16x32_bf16 v[20:23], v[76:79], v[64:67], v[20:23]\nv_mfma_f32_16x16x32_bf16 v[16:19], v[80:83], v[68:71], v[16:19]\n" \
    "v_mfma_f32_16x16x32_bf16 v[20:23], v[56:59], v[68:71], v[20:23]\nv_mfma_f32_16x16x32_bf16 v[16:19], v[60:63], v[40:43], v[16:19]\n" \
    "v_mfma_f32_16x16x32_bf16 v[20:23], v[32:35], v[40:43], v[20:23]\nv_mfma_f32_16x16x32_bf16 v[24:27], v[44:47], v[36:39], 0\n" \
    "v_mfma_f32_16x16x32_bf16 v[24:27], v[84:87], v[64:67], v[24:27]\nv_mfma_f32_16x16x32_bf16 v[24:27], v[88:91], v[68:71], v[24:27]\n" \
    "v_mfma_f32_16x16x32_bf16 v[24:27], v[28:31], v[40:43], v[24:27]\ns_nop 3\nv_mov_b32_dpp v16, v20 quad_perm:[0,1,2,3] row_mask:0xf bank_mask:0xc\n" \
    "v_mov_b32_dpp v17, v21 quad_perm:[0,1,2,3] row_mask:0xf bank_mask:0xc\nv_mov_b32_dpp v18, v22 quad_perm:[0,1,2,3] row_mask:0xf bank_mask:0xc\n" \
    "v_mov_b32_dpp v19, v23 quad_perm:[0,1,2,3] row_mask:0xf bank_mask:0xc\nv_max3_f32 v254, v16, v17, v18\nv_max_f32 v254, v254, v19\ns_nop 1\n" \
    "v_max_f32_dpp v252, v254, v254 row_ror:8 row_mask:0xf bank_mask:0xf\nv_max_f32_dpp v253, v254, v254 row_ror:8 row_mask:0xf bank_mask:0xf\ns_nop 1\n" \
    "v_permlane32_swap_b32 v252, v253\nv_max_f32 v252, v252, v253\nv_max3_f32 v254, v252, v24, v25\nv_max3_f32 v252, v254, v26, v27\nv_max3_f32 v253, v254, v26, v27\ns_nop 1\n" \
    "v_permlane16_swap_b32 v252, v253\nv_max_f32 v252, v252, v253\nv_pk_add_f32 v[16:17], v[16:17], v[252:253] op_sel_hi:[1,0] neg_lo:[0,1] neg_hi:[0,1]\n" \
    "v_pk_add_f32 v[18:19], v[18:19], v[252:253] op_sel_hi:[1,0] neg_lo:[0,1] neg_hi:[0,1]\n" \
    "v_pk_add_f32 v[24:25], v[24:25], v[252:253] op_sel_hi:[1,0] neg_lo:[0,1] neg_hi:[0,1]\n" \
    "v_pk_add_f32 v[26:27], v[26:27], v[252:253] op_sel_hi:[1,0] neg_lo:[0,1] neg_hi:[0,1]\nv_exp_f32 v16, v16\nv_exp_f32 v17, v17\nv_exp_f32 v18, v18\nv_exp_f32 v19, v19\n" \
    "v_exp_f32 v24, v24\nv_pk_add_f32 v[254:255], v[16:17], v[18:19]\nv_exp_f32 v25, v25\nv_add_f32 v254, v254, v255\nv_exp_f32 v26, v26\nv_exp_f32 v27, v27\n" \
    "v_add_f32_dpp v252, v254, v254 row_ror:8 row_mask:0xf bank_mask:0xf\nv_add_f32_dpp v253, v254, v254 row_ror:8 row_mask:0xf bank_mask:0xf\n" \
    "v_pk_add_f32 v[254:255], v[24:25], v[26:27]\ns_nop 0\nv_permlane32_swap_b32 v252, v253\nv_pk_add_f32 v[254:255], v[254:255], v[252:253]\n" \
    "v_pk_add_f32 v[252:253], v[254:255], v[254:255] op_sel:[0,1] op_sel_hi:[1,0]\ns_nop 1\nv_permlane16_swap_b32 v252, v253\nv_add_f32 v252, v252, v253\nv_rcp_f32 v252, v252\n" \
    "s_nop 0\nv_pk_mul_f32 v[16:17], v[16:17], v[252:253] op_sel_hi:[1,0]\nv_pk_mul_f32 v[24:25], v[24:25], v[252:253] op_sel_hi:[1,0]\nv_cvt_pk_bf16_f32 v20, v16, v17\n" \
    "v_cvt_pk_bf16_f32 v22, v24, v25\nv_pk_mul_f32 v[18:19], v[18:19], v[252:253] op_sel_hi:[1,0]\nv_pk_mul_f32 v[26:27], v[26:27], v[252:253] op_sel_hi:[1,0]\n" \
    "v_cvt_pk_bf16_f32 v21, v18, v19\nv_cvt_pk_bf16_f32 v23, v26, v27\nv_permlane32_swap_b32 v20, v22\ns_nop 0\nv_permlane32_swap_b32 v21, v23\n" \
    "buffer_store_dwordx4 v[20:23], v7, s[64:67], s82 offen sc1\nbuffer_store_dwordx4 v[20:23], v7, s[64:67], s74 offen sc1\ns_branch .Lq3_%=\n.Lq90_%=:\ns_add_u32 s68, s68, 1\n" \
    "s_and_b32 s69, s68, 63\ns_cbranch_scc1 .Lq89_%=\ns_cmp_gt_u32 s68, 0x400000\ns_cbranch_scc1 .Lq1_%=\nbuffer_load_dword v253, off, s[64:67], 0 sc1\ns_waitcnt vmcnt(0)\n" \
    "v_readfirstlane_b32 s69, v253\ns_nop 0\ns_cmp_eq_u32 s69, 1\ns_cbranch_scc0 .Lq89_%=\ns_endpgm\n.Lq1_%=:\nv_mov_b32 v253, 1\nbuffer_store_dword v253, off, s[64:67], 0 sc1\n" \
    "s_endpgm\n"
#define ASM_V \
    "s_add_u32 m0, s41, 0\ns_xor_b32 s85, s62, 0\ns_add_u32 s85, s85, s40\nbuffer_load_dwordx4 v11, s[64:67], s85 offen sc1 lds\n" \
    "buffer_load_dwordx4 v12, s[64:67], s85 offen offset:1024 sc1 lds\ns_add_u32 m0, s41, 8192\ns_xor_b32 s85, s62, 256\ns_add_u32 s85, s85, s40\n" \
    "buffer_load_dwordx4 v11, s[64:67], s85 offen sc1 lds\nbuffer_load_dwordx4 v12, s[64:67], s85 offen offset:1024 sc1 lds\ns_add_u32 m0, s41, 16384\ns_xor_b32 s85, s62, 512\n" \
    "s_add_u32 s85, s85, s40\nbuffer_load_dwordx4 v11, s[64:67], s85 offen sc1 lds\nbuffer_load_dwordx4 v12, s[64:67], s85 offen offset:1024 sc1 lds\ns_add_u32 m0, s41, 40960\n" \
    "s_xor_b32 s85, s62, 768\ns_add_u32 s85, s85, s40\nbuffer_load_dwordx4 v11, s[64:67], s85 offen sc1 lds\nbuffer_load_dwordx4 v12, s[64:67], s85 offen offset:1024 sc1 lds\n" \
    "s_memrealtime s[72:73]\ns_waitcnt lgkmcnt(0)\ns_mov_b32 s80, s72\ns_waitcnt vmcnt(0)\ns_barrier\nds_read_b128 v[52:55], v3 offset:0\nds_read_b128 v[56:59], v9 offset:0\n" \
    "ds_read_b128 v[60:63], v9 offset:4096\nds_read_b128 v[64:67], v3 offset:1024\nds_read_b128 v[68:71], v10 offset:0\nds_read_b128 v[72:75], v10 offset:4096\n" \
    "s_waitcnt lgkmcnt(3)\nv_mfma_f32_16x16x32_bf16 v[16:19], a[0:3], v[56:59], 0\ns_add_u32 m0, s41, 24576\ns_xor_b32 s85, s62, 1024\n" \
    "v_mfma_f32_16x16x32_bf16 v[24:27], v[52:55], v[56:59], 0\nds_read_b128 v[56:59], v3 offset:2048\ns_add_u32 s85, s85, s40\n" \
    "v_mfma_f32_16x16x32_bf16 v[20:23], a[0:3], v[60:63], 0\nds_read_b128 v[76:79], v9 offset:1024\nv_mfma_f32_16x16x32_bf16 v[28:31], v[52:55], v[60:63], 0\n" \
    "ds_read_b128 v[52:55], v9 offset:5120\ns_waitcnt lgkmcnt(3)\nv_mfma_f32_16x16x32_bf16 v[16:19], a[4:7], v[68:71], v[16:19]\n" \
    "buffer_load_dwordx4 v11, s[64:67], s85 offen sc1 lds\nv_mfma_f32_16x16x32_bf16 v[24:27], v[64:67], v[68:71], v[24:27]\nds_read_b128 v[60:63], v3 offset:3072\n" \
    "v_mfma_f32_16x16x32_bf16 v[20:23], a[4:7], v[72:75], v[20:23]\nds_read_b128 v[68:71], v10 offset:1024\nv_mfma_f32_16x16x32_bf16 v[28:31], v[64:67], v[72:75], v[28:31]\n" \
    "ds_read_b128 v[64:67], v10 offset:5120\ns_waitcnt lgkmcnt(3)\nv_mfma_f32_16x16x32_bf16 v[16:19], a[8:11], v[76:79], v[16:19]\n" \
    "buffer_load_dwordx4 v12, s[64:67], s85 offen offset:1024 sc1 lds\ns_add_u32 m0, s41, 32768\nv_mfma_f32_16x16x32_bf16 v[24:27], v[56:59], v[76:79], v[24:27]\n" \
    "ds_read_b128 v[72:75], v3 offset:4096\ns_xor_b32 s85, s62, 1280\nv_mfma_f32_16x16x32_bf16 v[20:23], a[8:11], v[52:55], v[20:23]\nds_read_b128 v[76:79], v9 offset:8192\n" \
    "s_add_u32 s85, s85, s40\nv_mfma_f32_16x16x32_bf16 v[28:31], v[56:59], v[52:55], v[28:31]\nds_read_b128 v[52:55], v9 offset:12288\ns_waitcnt lgkmcnt(3)\n" \
    "v_mfma_f32_16x16x32_bf16 v[16:19], a[12:15], v[68:71], v[16:19]\nbuffer_load_dwordx4 v11, s[64:67], s85 offen sc1 lds\n" \
    "v_mfma_f32_16x16x32_bf16 v[24:27], v[60:63], v[68:71], v[24:27]\nds_read_b128 v[56:59], v3 offset:5120\nv_mfma_f32_16x16x32_bf16 v[20:23], a[12:15], v[64:67], v[20:23]\n" \
    "ds_read_b128 v[68:71], v10 offset:8192\nv_mfma_f32_16x16x32_bf16 v[28:31], v[60:63], v[64:67], v[28:31]\nds_read_b128 v[60:63], v10 offset:12288\ns_waitcnt lgkmcnt(3)\n" \
    "v_mfma_f32_16x16x32_bf16 v[16:19], a[16:19], v[76:79], v[16:19]\nbuffer_load_dwordx4 v12, s[64:67], s85 offen offset:1024 sc1 lds\ns_add_u32 s84, s40, 0x20000\n" \
    "v_mfma_f32_16x16x32_bf16 v[24:27], v[72:75], v[76:79], v[24:27]\nds_read_b128 v[64:67], v3 offset:6144\ns_cmp_lt_u32 s49, 62\ns_cselect_b32 s84, s84, s40\n" \
    "v_mfma_f32_16x16x32_bf16 v[20:23], a[16:19], v[52:55], v[20:23]\nds_read_b128 v[76:79], v9 offset:9216\nv_mov_b64 v[252:253], -1\nv_mov_b64 v[254:255], -1\n" \
    "v_mfma_f32_16x16x32_bf16 v[28:31], v[72:75], v[52:55], v[28:31]\nds_read_b128 v[52:55], v9 offset:13312\ns_and_b32 s69, s49, 15\ns_waitcnt lgkmcnt(3)\n" \
    "v_mfma_f32_16x16x32_bf16 v[16:19], a[20:23], v[68:71], v[16:19]\ns_lshl_b32 s69, s69, 19\ns_add_u32 s82, s38, s69\n" \
    "v_mfma_f32_16x16x32_bf16 v[24:27], v[56:59], v[68:71], v[24:27]\nds_read_b128 v[68:71], v3 offset:7168\ns_lshr_b32 s69, s82, 8\n" \
    "v_mfma_f32_16x16x32_bf16 v[20:23], a[20:23], v[60:63], v[20:23]\nds_read_b128 v[72:75], v10 offset:9216\ns_and_b32 s69, s69, 0x2000\n" \
    "v_mfma_f32_16x16x32_bf16 v[28:31], v[56:59], v[60:63], v[28:31]\nds_read_b128 v[56:59], v10 offset:13312\ns_xor_b32 s82, s82, s69\ns_waitcnt lgkmcnt(3)\n" \
    "v_mfma_f32_16x16x32_bf16 v[16:19], a[24:27], v[76:79], v[16:19]\ns_and_b32 s69, s35, 15\ns_lshl_b32 s69, s69, 19\n" \
    "v_mfma_f32_16x16x32_bf16 v[24:27], v[64:67], v[76:79], v[24:27]\nds_read_b128 v[60:63], v3 offset:8192\ns_add_u32 s0, s38, s69\n" \
    "v_mfma_f32_16x16x32_bf16 v[20:23], a[24:27], v[52:55], v[20:23]\nds_read_b128 v[76:79], v9 offset:16384\ns_lshr_b32 s69, s0, 8\n" \
    "v_mfma_f32_16x16x32_bf16 v[28:31], v[64:67], v[52:55], v[28:31]\nds_read_b128 v[52:55], v9 offset:20480\ns_and_b32 s69, s69, 0x2000\ns_waitcnt lgkmcnt(3)\n" \
    "v_mfma_f32_16x16x32_bf16 v[16:19], a[28:31], v[72:75], v[16:19]\ns_xor_b32 s0, s0, s69\ns_xor_b32 s1, s0, 0x10000\n" \
    "v_mfma_f32_16x16x32_bf16 v[24:27], v[68:71], v[72:75], v[24:27]\nds_read_b128 v[64:67], v3 offset:9216\ns_xor_b32 s2, s0, 0x20000\n" \
    "v_mfma_f32_16x16x32_bf16 v[20:23], a[28:31], v[56:59], v[20:23]\nds_read_b128 v[72:75], v10 offset:16384\ns_xor_b32 s3, s0, 0x30000\nv_lshrrev_b32 v15, 1, v2\n" \
    "v_mfma_f32_16x16x32_bf16 v[28:31], v[68:71], v[56:59], v[28:31]\nds_read_b128 v[56:59], v10 offset:20480\ns_waitcnt lgkmcnt(3)\nv_add_u32 v15, s60, v15\n" \
    "v_mfma_f32_16x16x32_bf16 v[16:19], a[32:35], v[76:79], v[16:19]\nds_write_b64 v15, v[252:253]\ns_and_b32 s69, s49, 255\n" \
    "v_mfma_f32_16x16x32_bf16 v[24:27], v[60:63], v[76:79], v[24:27]\nds_read_b128 v[68:71], v3 offset:10240\ns_lshl_b32 s69, s69, 19\n" \
    "v_mfma_f32_16x16x32_bf16 v[20:23], a[32:35], v[52:55], v[20:23]\nds_read_b128 v[76:79], v9 offset:17408\ns_add_u32 s74, s63, s69\n" \
    "v_mfma_f32_16x16x32_bf16 v[28:31], v[60:63], v[52:55], v[28:31]\nds_read_b128 v[52:55], v9 offset:21504\ns_cmp_lt_u32 s49, 64\ns_cselect_b32 s74, s74, 0x7fff0000\n" \
    "s_waitcnt lgkmcnt(4)\nv_mfma_f32_16x16x32_bf16 v[16:19], a[36:39], v[72:75], v[16:19]\nv_mfma_f32_16x16x32_bf16 v[24:27], v[64:67], v[72:75], v[24:27]\n" \
    "ds_read_b128 v[60:63], v3 offset:11264\nv_mfma_f32_16x16x32_bf16 v[20:23], a[36:39], v[56:59], v[20:23]\nds_read_b128 v[72:75], v10 offset:17408\n" \
    "v_mfma_f32_16x16x32_bf16 v[28:31], v[64:67], v[56:59], v[28:31]\nds_read_b128 v[56:59], v10 offset:21504\ns_waitcnt lgkmcnt(3)\n" \
    "v_mfma_f32_16x16x32_bf16 v[16:19], a[40:43], v[76:79], v[16:19]\nv_mfma_f32_16x16x32_bf16 v[24:27], v[68:71], v[76:79], v[24:27]\nds_read_b128 v[64:67], v3 offset:12288\n" \
    "s_barrier\nv_mfma_f32_16x16x32_bf16 v[20:23], a[40:43], v[52:55], v[20:23]\nds_read_b128 v[76:79], v9 offset:40960\n" \
    "v_mfma_f32_16x16x32_bf16 v[28:31], v[68:71], v[52:55], v[28:31]\nds_read_b128 v[52:55], v9 offset:45056\ns_add_u32 m0, s41, 0\ns_waitcnt lgkmcnt(3)\n" \
    "v_mfma_f32_16x16x32_bf16 v[16:19], a[44:47], v[72:75], v[16:19]\ns_xor_b32 s85, s62, 1536\ns_add_u32 s85, s85, s40\n" \
    "v_mfma_f32_16x16x32_bf16 v[24:27], v[60:63], v[72:75], v[24:27]\nds_read_b128 v[68:71], v3 offset:13312\nv_mfma_f32_16x16x32_bf16 v[20:23], a[44:47], v[56:59], v[20:23]\n" \
    "ds_read_b128 v[72:75], v10 offset:40960\nv_mfma_f32_16x16x32_bf16 v[28:31], v[60:63], v[56:59], v[28:31]\nds_read_b128 v[56:59], v10 offset:45056\ns_waitcnt lgkmcnt(3)\n" \
    "v_mfma_f32_16x16x32_bf16 v[16:19], a[48:51], v[76:79], v[16:19]\nbuffer_load_dwordx4 v11, s[64:67], s85 offen sc1 lds\n" \
    "v_mfma_f32_16x16x32_bf16 v[24:27], v[64:67], v[76:79], v[24:27]\nds_read_b128 v[60:63], v3 offset:14336\nv_mfma_f32_16x16x32_bf16 v[20:23], a[48:51], v[52:55], v[20:23]\n" \
    "ds_read_b128 v[76:79], v9 offset:41984\nv_mfma_f32_16x16x32_bf16 v[28:31], v[64:67], v[52:55], v[28:31]\nds_read_b128 v[52:55], v9 offset:46080\ns_waitcnt lgkmcnt(3)\n" \
    "v_mfma_f32_16x16x32_bf16 v[16:19], a[52:55], v[72:75], v[16:19]\nbuffer_load_dwordx4 v12, s[64:67], s85 offen offset:1024 sc1 lds\ns_add_u32 m0, s41, 8192\n" \
    "v_mfma_f32_16x16x32_bf16 v[24:27], v[68:71], v[72:75], v[24:27]\nds_read_b128 v[64:67], v3 offset:15360\ns_xor_b32 s85, s62, 1792\n" \
    "v_mfma_f32_16x16x32_bf16 v[20:23], a[52:55], v[56:59], v[20:23]\nds_read_b128 v[72:75], v10 offset:41984\ns_add_u32 s85, s85, s40\n" \
    "v_mfma_f32_16x16x32_bf16 v[28:31], v[68:71], v[56:59], v[28:31]\nds_read_b128 v[56:59], v10 offset:46080\ns_waitcnt lgkmcnt(3)\n" \
    "v_mfma_f32_16x16x32_bf16 v[16:19], a[56:59], v[76:79], v[16:19]\nbuffer_load_dwordx4 v11, s[64:67], s85 offen sc1 lds\n" \
    "v_mfma_f32_16x16x32_bf16 v[24:27], v[60:63], v[76:79], v[24:27]\nds_read_b128 v[68:71], v3 offset:16384\ns_waitcnt vmcnt(3)\ns_barrier\n" \
    "v_mfma_f32_16x16x32_bf16 v[20:23], a[56:59], v[52:55], v[20:23]\nds_read_b128 v[76:79], v9 offset:24576\nv_mfma_f32_16x16x32_bf16 v[28:31], v[60:63], v[52:55], v[28:31]\n" \
    "ds_read_b128 v[52:55], v9 offset:28672\ns_waitcnt lgkmcnt(3)\nv_mfma_f32_16x16x32_bf16 v[16:19], a[60:63], v[72:75], v[16:19]\n" \
    "buffer_load_dwordx4 v12, s[64:67], s85 offen offset:1024 sc1 lds\nv_mfma_f32_16x16x32_bf16 v[24:27], v[64:67], v[72:75], v[24:27]\nds_read_b128 v[60:63], v3 offset:17408\n" \
    "v_mfma_f32_16x16x32_bf16 v[20:23], a[60:63], v[56:59], v[20:23]\nds_read_b128 v[72:75], v10 offset:24576\nv_mfma_f32_16x16x32_bf16 v[28:31], v[64:67], v[56:59], v[28:31]\n" \
    "ds_read_b128 v[56:59], v10 offset:28672\ns_waitcnt lgkmcnt(3)\nv_mfma_f32_16x16x32_bf16 v[16:19], a[64:67], v[76:79], v[16:19]\n" \
    "buffer_store_dwordx4 v[252:255], v2, s[64:67], s74 offen offset:0 sc1\nv_mfma_f32_16x16x32_bf16 v[24:27], v[68:71], v[76:79], v[24:27]\n" \
    "ds_read_b128 v[64:67], v3 offset:18432\nv_mfma_f32_16x16x32_bf16 v[20:23], a[64:67], v[52:55], v[20:23]\nds_read_b128 v[76:79], v9 offset:25600\n" \
    "v_mfma_f32_16x16x32_bf16 v[28:31], v[68:71], v[52:55], v[28:31]\nds_read_b128 v[52:55], v9 offset:29696\ns_waitcnt lgkmcnt(3)\n" \
    "v_mfma_f32_16x16x32_bf16 v[16:19], a[68:71], v[72:75], v[16:19]\nv_mfma_f32_16x16x32_bf16 v[24:27], v[60:63], v[72:75], v[24:27]\nds_read_b128 v[68:71], v3 offset:19456\n" \
    "v_mfma_f32_16x16x32_bf16 v[20:23], a[68:71], v[56:59], v[20:23]\nds_read_b128 v[72:75], v10 offset:25600\nv_mfma_f32_16x16x32_bf16 v[28:31], v[60:63], v[56:59], v[28:31]\n" \
    "ds_read_b128 v[56:59], v10 offset:29696\ns_waitcnt lgkmcnt(3)\nv_mfma_f32_16x16x32_bf16 v[16:19], a[72:75], v[76:79], v[16:19]\n" \
    "buffer_store_dwordx4 v[252:255], v2, s[64:67], s74 offen offset:1024 sc1\nv_mfma_f32_16x16x32_bf16 v[24:27], v[64:67], v[76:79], v[24:27]\n" \
    "ds_read_b128 v[60:63], v3 offset:20480\nv_mfma_f32_16x16x32_bf16 v[20:23], a[72:75], v[52:55], v[20:23]\nds_read_b128 v[76:79], v9 offset:32768\n" \
    "v_mfma_f32_16x16x32_bf16 v[28:31], v[64:67], v[52:55], v[28:31]\nds_read_b128 v[52:55], v9 offset:36864\ns_waitcnt lgkmcnt(3)\n" \
    "v_mfma_f32_16x16x32_bf16 v[16:19], a[76:79], v[72:75], v[16:19]\nbuffer_store_dwordx4 v[252:255], v7, s[64:67], s82 offen sc1\ns_xor_b32 s82, s82, 0x10000\n" \
    "v_mfma_f32_16x16x32_bf16 v[24:27], v[68:71], v[72:75], v[24:27]\nds_read_b128 v[64:67], v3 offset:21504\nv_mfma_f32_16x16x32_bf16 v[20:23], a[76:79], v[56:59], v[20:23]\n" \
    "ds_read_b128 v[72:75], v10 offset:32768\nv_mfma_f32_16x16x32_bf16 v[28:31], v[68:71], v[56:59], v[28:31]\nds_read_b128 v[56:59], v10 offset:36864\ns_waitcnt lgkmcnt(3)\n" \
    "v_mfma_f32_16x16x32_bf16 v[16:19], a[80:83], v[76:79], v[16:19]\nbuffer_store_dwordx4 v[252:255], v7, s[64:67], s82 offen sc1\ns_xor_b32 s82, s82, 0x30000\n" \
    "v_mfma_f32_16x16x32_bf16 v[24:27], v[60:63], v[76:79], v[24:27]\nds_read_b128 v[68:71], v3 offset:22528\nv_mfma_f32_16x16x32_bf16 v[20:23], a[80:83], v[52:55], v[20:23]\n" \
    "ds_read_b128 v[76:79], v9 offset:33792\nv_mfma_f32_16x16x32_bf16 v[28:31], v[60:63], v[52:55], v[28:31]\nds_read_b128 v[52:55], v9 offset:37888\ns_waitcnt lgkmcnt(3)\n" \
    "v_mfma_f32_16x16x32_bf16 v[16:19], a[84:87], v[72:75], v[16:19]\nbuffer_store_dwordx4 v[252:255], v7, s[64:67], s82 offen sc1\ns_xor_b32 s82, s82, 0x10000\n" \
    "v_mfma_f32_16x16x32_bf16 v[24:27], v[64:67], v[72:75], v[24:27]\nds_read_b128 v[60:63], v3 offset:23552\nv_mfma_f32_16x16x32_bf16 v[20:23], a[84:87], v[56:59], v[20:23]\n" \
    "ds_read_b128 v[72:75], v10 offset:33792\nv_mfma_f32_16x16x32_bf16 v[28:31], v[64:67], v[56:59], v[28:31]\nds_read_b128 v[56:59], v10 offset:37888\ns_waitcnt lgkmcnt(3)\n" \
    "v_mfma_f32_16x16x32_bf16 v[16:19], a[88:91], v[76:79], v[16:19]\nbuffer_store_dwordx4 v[252:255], v7, s[64:67], s82 offen sc1\ns_xor_b32 s82, s82, 0x30000\n" \
    "v_mfma_f32_16x16x32_bf16 v[24:27], v[68:71], v[76:79], v[24:27]\ns_add_u32 s49, s49, 2\ns_waitcnt vmcnt(6)\ns_barrier\n" \
    "v_mfma_f32_16x16x32_bf16 v[20:23], a[88:91], v[52:55], v[20:23]\nds_read_b128 v[64:67], v9 offset:0\nv_mfma_f32_16x16x32_bf16 v[28:31], v[68:71], v[52:55], v[28:31]\n" \
    "ds_read_b128 v[52:55], v9 offset:4096\ns_add_u32 m0, s41, 16384\ns_waitcnt lgkmcnt(2)\nv_mfma_f32_16x16x32_bf16 v[16:19], a[92:95], v[72:75], v[16:19]\n" \
    "s_xor_b32 s85, s62, 512\ns_add_u32 s85, s85, s84\nv_mfma_f32_16x16x32_bf16 v[24:27], v[60:63], v[72:75], v[24:27]\nbuffer_load_dwordx4 v11, s[64:67], s85 offen sc1 lds\n" \
    "v_mfma_f32_16x16x32_bf16 v[20:23], a[92:95], v[56:59], v[20:23]\nds_read_b128 v[68:71], v10 offset:0\nv_mfma_f32_16x16x32_bf16 v[28:31], v[60:63], v[56:59], v[28:31]\n" \
    "ds_read_b128 v[56:59], v10 offset:4096\ns_waitcnt lgkmcnt(2)\nv_mfma_f32_16x16x32_bf16 v[16:19], a[96:99], v[64:67], v[16:19]\n" \
    "buffer_load_dwordx4 v12, s[64:67], s85 offen offset:1024 sc1 lds\ns_add_u32 m0, s41, 40960\nv_mfma_f32_16x16x32_bf16 v[24:27], a[104:107], v[64:67], v[24:27]\n" \
    "s_xor_b32 s85, s62, 768\ns_add_u32 s85, s85, s84\nv_mfma_f32_16x16x32_bf16 v[20:23], a[96:99], v[52:55], v[20:23]\nds_read_b128 v[60:63], v9 offset:1024\n" \
    "v_mfma_f32_16x16x32_bf16 v[28:31], a[104:107], v[52:55], v[28:31]\nds_read_b128 v[52:55], v9 offset:5120\ns_waitcnt lgkmcnt(2)\n" \
    "v_mfma_f32_16x16x32_bf16 v[16:19], a[100:103], v[68:71], v[16:19]\nbuffer_load_dwordx4 v11, s[64:67], s85 offen sc1 lds\n" \
    "v_mfma_f32_16x16x32_bf16 v[24:27], a[108:111], v[68:71], v[24:27]\nv_mfma_f32_16x16x32_bf16 v[20:23], a[100:103], v[56:59], v[20:23]\n" \
    "ds_read_b128 v[64:67], v10 offset:1024\nv_mfma_f32_16x16x32_bf16 v[28:31], a[108:111], v[56:59], v[28:31]\nds_read_b128 v[56:59], v10 offset:5120\ns_waitcnt lgkmcnt(2)\n" \
    "v_mfma_f32_16x16x32_bf16 v[16:19], a[112:115], v[60:63], v[16:19]\nbuffer_load_dwordx4 v12, s[64:67], s85 offen offset:1024 sc1 lds\n" \
    "v_mfma_f32_16x16x32_bf16 v[24:27], a[120:123], v[60:63], v[24:27]\nv_mfma_f32_16x16x32_bf16 v[20:23], a[112:115], v[52:55], v[20:23]\n" \
    "ds_read_b128 v[60:63], v9 offset:8192\nv_mfma_f32_16x16x32_bf16 v[28:31], a[120:123], v[52:55], v[28:31]\nds_read_b128 v[52:55], v9 offset:12288\ns_waitcnt lgkmcnt(2)\n" \
    "v_mfma_f32_16x16x32_bf16 v[16:19], a[116:119], v[64:67], v[16:19]\nv_mfma_f32_16x16x32_bf16 v[24:27], a[124:127], v[64:67], v[24:27]\n" \
    "v_mfma_f32_16x16x32_bf16 v[20:23], a[116:119], v[56:59], v[20:23]\nds_read_b128 v[64:67], v10 offset:8192\n" \
    "v_mfma_f32_16x16x32_bf16 v[28:31], a[124:127], v[56:59], v[28:31]\nds_read_b128 v[56:59], v10 offset:12288\ns_waitcnt lgkmcnt(2)\n" \
    "v_mfma_f32_16x16x32_bf16 v[16:19], a[128:131], v[60:63], v[16:19]\nv_mfma_f32_16x16x32_bf16 v[24:27], a[136:139], v[60:63], v[24:27]\n" \
    "v_mfma_f32_16x16x32_bf16 v[20:23], a[128:131], v[52:55], v[20:23]\nds_read_b128 v[60:63], v9 offset:9216\n" \
    "v_mfma_f32_16x16x32_bf16 v[28:31], a[136:139], v[52:55], v[28:31]\nds_read_b128 v[52:55], v9 offset:13312\ns_waitcnt lgkmcnt(2)\n" \
    "v_mfma_f32_16x16x32_bf16 v[16:19], a[132:135], v[64:67], v[16:19]\nds_read_b128 v[68:71], v10 offset:9216\n" \
    "v_mfma_f32_16x16x32_bf16 v[24:27], a[140:143], v[64:67], v[24:27]\nds_read_b128 v[64:67], v10 offset:13312\n" \
    "v_mfma_f32_16x16x32_bf16 v[20:23], a[132:135], v[56:59], v[20:23]\nv_mfma_f32_16x16x32_bf16 v[28:31], a[140:143], v[56:59], v[28:31]\ns_waitcnt lgkmcnt(2)\n" \
    "v_mfma_f32_16x16x32_bf16 v[16:19], a[144:147], v[60:63], v[16:19]\nv_mfma_f32_16x16x32_bf16 v[24:27], a[152:155], v[60:63], v[24:27]\ns_waitcnt lgkmcnt(0)\ns_barrier\n" \
    "v_mfma_f32_16x16x32_bf16 v[16:19], a[148:151], v[68:71], v[16:19]\ns_add_u32 m0, s41, 0\ns_xor_b32 s85, s62, 0\n" \
    "v_mfma_f32_16x16x32_bf16 v[24:27], a[156:159], v[68:71], v[24:27]\ns_add_u32 s85, s85, s84\nbuffer_load_dwordx4 v11, s[64:67], s85 offen sc1 lds\n" \
    "v_mfma_f32_16x16x32_bf16 v[20:23], a[144:147], v[52:55], v[20:23]\ns_nop 1\nv_cvt_pk_bf16_f32 v32, v16, v17\nv_cvt_pk_bf16_f32 v33, v18, v19\n" \
    "v_mfma_f32_16x16x32_bf16 v[28:31], a[152:155], v[52:55], v[28:31]\nv_cvt_pk_bf16_f32 v34, v24, v25\nv_cvt_pk_bf16_f32 v35, v26, v27\n" \
    "v_mfma_f32_16x16x32_bf16 v[20:23], a[148:151], v[64:67], v[20:23]\nds_write_b128 v14, v[32:35] offset:0\nv_mfma_f32_16x16x32_bf16 v[28:31], a[156:159], v[64:67], v[28:31]\n" \
    "buffer_load_dwordx4 v12, s[64:67], s85 offen offset:1024 sc1 lds\ns_add_u32 m0, s41, 8192\ns_xor_b32 s85, s62, 256\ns_add_u32 s85, s85, s84\n" \
    "buffer_load_dwordx4 v11, s[64:67], s85 offen sc1 lds\nbuffer_load_dwordx4 v12, s[64:67], s85 offen offset:1024 sc1 lds\nv_cvt_pk_bf16_f32 v36, v20, v21\n" \
    "v_cvt_pk_bf16_f32 v37, v22, v23\nv_cvt_pk_bf16_f32 v38, v28, v29\nv_cvt_pk_bf16_f32 v39, v30, v31\nds_write_b128 v14, v[36:39] offset:4096\ns_mov_b32 s40, s84\n" \
    "s_waitcnt vmcnt(0) lgkmcnt(0)\ns_memrealtime s[72:73]\ns_waitcnt lgkmcnt(0)\nv_mov_b32 v253, 1\nv_mov_b32 v252, s72\ns_or_b32 s69, s35, s36\n" \
    "s_cselect_b32 s69, 0x7fff0000, 0\ns_mov_b64 exec, 1\nbuffer_atomic_umax v252, off, s[64:67], s69 offset:260\nbuffer_atomic_add v253, off, s[64:67], s69 offset:256\n" \
    "s_mov_b64 exec, -1\ns_mov_b32 s16, 0\ns_cmp_eq_u32 s36, 0\ns_cselect_b32 s17, 511, -1\ns_mov_b32 s18, s44\ns_mov_b32 s72, s80\ns_barrier\n.Lv5_%=:\n" \
    "ds_read_b128 v[52:55], v6 offset:0\nds_read_b128 v[56:59], v6 offset:1024\nds_read_b128 v[60:63], v6 offset:2048\nv_mov_b64 v[32:33], 0\nv_mov_b64 v[34:35], 0\n" \
    "v_mov_b64 v[36:37], 0\nv_mov_b64 v[38:39], 0\ns_and_b32 s69, s50, 7\ns_lshl_b32 s69, s69, 21\ns_add_u32 s86, s39, s69\ns_lshr_b32 s69, s86, 8\ns_and_b32 s69, s69, 0x2000\n" \
    "s_xor_b32 s86, s86, s69\ns_waitcnt lgkmcnt(2)\nv_smfmac_f32_16x16x64_bf16 v[32:35], v[52:55], a[160:167], v0\nds_read_b128 v[64:67], v6 offset:3072\nv_mov_b64 v[40:41], 0\n" \
    "v_smfmac_f32_16x16x64_bf16 v[36:39], v[52:55], a[176:183], v0\nds_read_b128 v[68:71], v3 offset:0\ns_waitcnt lgkmcnt(3)\nv_mov_b64 v[42:43], 0\n" \
    "v_smfmac_f32_16x16x64_bf16 v[32:35], v[56:59], a[168:175], v0\nds_read_b128 v[72:75], v9 offset:0\nv_mov_b64 v[44:45], 0\n" \
    "v_smfmac_f32_16x16x64_bf16 v[36:39], v[56:59], a[184:191], v0\nds_read_b128 v[76:79], v9 offset:4096\ns_waitcnt lgkmcnt(4)\nv_mov_b64 v[46:47], 0\n" \
    "v_smfmac_f32_16x16x64_bf16 v[32:35], v[60:63], a[192:199], v0\ns_xor_b32 s87, s86, 0x40000\ns_xor_b32 s88, s86, 0x80000\n" \
    "v_smfmac_f32_16x16x64_bf16 v[36:39], v[60:63], a[208:215], v0\nds_read_b128 v[80:83], v3 offset:1024\ns_xor_b32 s89, s86, 0xc0000\ns_waitcnt lgkmcnt(4)\n" \
    "v_smfmac_f32_16x16x64_bf16 v[32:35], v[64:67], a[200:207], v0\nds_read_b128 v[84:87], v10 offset:0\ns_add_u32 s50, s50, 2\n" \
    "v_smfmac_f32_16x16x64_bf16 v[36:39], v[64:67], a[216:223], v0\nds_read_b128 v[88:91], v10 offset:4096\ns_add_u32 m0, s41, 24576\ns_waitcnt lgkmcnt(3)\n" \
    "v_mfma_f32_16x16x32_bf16 v[16:19], a[0:3], v[72:75], 0\ns_xor_b32 s85, s62, 1024\nv_cvt_pk_bf16_f32 v48, v32, v33\nv_cvt_pk_bf16_f32 v50, v34, v35\n" \
    "v_mfma_f32_16x16x32_bf16 v[24:27], v[68:71], v[72:75], 0\ns_add_u32 s85, s85, s40\nv_mfma_f32_16x16x32_bf16 v[20:23], a[0:3], v[76:79], 0\nv_cvt_pk_bf16_f32 v49, v36, v37\n" \
    "v_cvt_pk_bf16_f32 v51, v38, v39\nv_mfma_f32_16x16x32_bf16 v[28:31], v[68:71], v[76:79], 0\ns_waitcnt lgkmcnt(0)\nv_permlane16_swap_b32 v48, v49\n" \
    "v_mfma_f32_16x16x32_bf16 v[16:19], a[4:7], v[84:87], v[16:19]\nbuffer_load_dwordx4 v11, s[64:67], s85 offen sc1 lds\n" \
    "v_smfmac_f32_16x16x64_bf16 v[40:43], v[52:55], a[224:231], v0\nv_permlane16_swap_b32 v50, v51\nv_smfmac_f32_16x16x64_bf16 v[44:47], v[52:55], a[240:247], v0\n" \
    "v_mov_b64 v[32:33], 0\nv_smfmac_f32_16x16x64_bf16 v[40:43], v[56:59], a[232:239], v0\nbuffer_load_dwordx4 v12, s[64:67], s85 offen offset:1024 sc1 lds\n" \
    "v_smfmac_f32_16x16x64_bf16 v[44:47], v[56:59], a[248:255], v0\ns_add_u32 m0, s41, 32768\nv_mov_b64 v[34:35], 0\n" \
    "v_smfmac_f32_16x16x64_bf16 v[40:43], v[60:63], v[92:99], v0\nds_read_b128 v[68:71], v3 offset:2048\nv_mov_b64 v[36:37], 0\n" \
    "v_smfmac_f32_16x16x64_bf16 v[44:47], v[60:63], v[108:115], v0\nds_read_b128 v[72:75], v9 offset:1024\nv_mov_b64 v[38:39], 0\n" \
    "v_smfmac_f32_16x16x64_bf16 v[40:43], v[64:67], v[100:107], v0\nds_read_b128 v[76:79], v9 offset:5120\ns_xor_b32 s85, s62, 1280\n" \
    "v_smfmac_f32_16x16x64_bf16 v[44:47], v[64:67], v[116:123], v0\nbuffer_store_dwordx4 v[48:51], v8, s[64:67], s86 offen offset:0 sc1\ns_add_u32 s85, s85, s40\n" \
    "v_mfma_f32_16x16x32_bf16 v[24:27], v[80:83], v[84:87], v[24:27]\ns_nop 1\nv_cvt_pk_bf16_f32 v48, v40, v41\nv_cvt_pk_bf16_f32 v50, v42, v43\n" \
    "v_mfma_f32_16x16x32_bf16 v[20:23], a[4:7], v[88:91], v[20:23]\nv_cvt_pk_bf16_f32 v49, v44, v45\nv_cvt_pk_bf16_f32 v51, v46, v47\n" \
    "v_mfma_f32_16x16x32_bf16 v[28:31], v[80:83], v[88:91], v[28:31]\ns_waitcnt lgkmcnt(0)\nv_permlane16_swap_b32 v48, v49\n" \
    "v_mfma_f32_16x16x32_bf16 v[16:19], a[8:11], v[72:75], v[16:19]\nbuffer_load_dwordx4 v11, s[64:67], s85 offen sc1 lds\n" \
    "v_mfma_f32_16x16x32_bf16 v[24:27], v[68:71], v[72:75], v[24:27]\nv_permlane16_swap_b32 v50, v51\nv_smfmac_f32_16x16x64_bf16 v[32:35], v[52:55], v[124:131], v0\n" \
    "v_mov_b64 v[40:41], 0\nv_smfmac_f32_16x16x64_bf16 v[36:39], v[52:55], v[140:147], v0\nbuffer_load_dwordx4 v12, s[64:67], s85 offen offset:1024 sc1 lds\n" \
    "v_smfmac_f32_16x16x64_bf16 v[32:35], v[56:59], v[132:139], v0\ns_sub_u32 s20, s72, s80\nv_mov_b64 v[42:43], 0\n" \
    "v_smfmac_f32_16x16x64_bf16 v[36:39], v[56:59], v[148:155], v0\nds_read_b128 v[72:75], v3 offset:3072\nv_mov_b64 v[44:45], 0\n" \
    "v_smfmac_f32_16x16x64_bf16 v[32:35], v[60:63], v[156:163], v0\nds_read_b128 v[80:83], v10 offset:1024\nv_mov_b64 v[46:47], 0\n" \
    "v_smfmac_f32_16x16x64_bf16 v[36:39], v[60:63], v[172:179], v0\nds_read_b128 v[84:87], v10 offset:5120\ns_add_u32 s20, s20, 6\n" \
    "v_smfmac_f32_16x16x64_bf16 v[32:35], v[64:67], v[164:171], v0\nbuffer_store_dwordx4 v[48:51], v8, s[64:67], s87 offen offset:0 sc1\ns_min_u32 s21, s20, s16\n" \
    "v_smfmac_f32_16x16x64_bf16 v[36:39], v[64:67], v[180:187], v0\ns_mov_b32 s16, s20\ns_cmp_gt_u32 s21, 156\ns_cselect_b32 s21, 0, s21\n" \
    "v_mfma_f32_16x16x32_bf16 v[20:23], a[8:11], v[76:79], v[20:23]\ns_lshr_b32 s20, s35, 2\nv_cvt_pk_bf16_f32 v48, v32, v33\nv_cvt_pk_bf16_f32 v50, v34, v35\n" \
    "v_mfma_f32_16x16x32_bf16 v[28:31], v[68:71], v[76:79], v[28:31]\ns_lshl_b32 s22, s20, 8\ns_waitcnt lgkmcnt(0)\nv_cvt_pk_bf16_f32 v49, v36, v37\n" \
    "v_mfma_f32_16x16x32_bf16 v[16:19], a[12:15], v[80:83], v[16:19]\ns_or_b32 s21, s21, s22\nv_cvt_pk_bf16_f32 v51, v38, v39\nv_permlane16_swap_b32 v48, v49\n" \
    "v_mfma_f32_16x16x32_bf16 v[24:27], v[72:75], v[80:83], v[24:27]\ns_and_b32 s20, s20, 1\nv_permlane16_swap_b32 v50, v51\n" \
    "v_mfma_f32_16x16x32_bf16 v[20:23], a[12:15], v[84:87], v[20:23]\ns_lshl_b32 s20, s20, 2\nv_mov_b64 v[32:33], 0\nv_mov_b64 v[34:35], 0\n" \
    "v_smfmac_f32_16x16x64_bf16 v[40:43], v[52:55], v[188:195], v0\ns_cmp_gt_u32 s21, s17\ns_cselect_b32 s20, s20, 0x7fff0000\nv_mov_b64 v[36:37], 0\n" \
    "v_smfmac_f32_16x16x64_bf16 v[44:47], v[52:55], v[204:211], v0\ns_max_u32 s17, s17, s21\nv_mov_b64 v[38:39], 0\n" \
    "v_smfmac_f32_16x16x64_bf16 v[40:43], v[56:59], v[196:203], v0\nds_read_b128 v[52:55], v3 offset:4096\nv_mov_b32 v15, s21\ns_mov_b64 exec, 1\n" \
    "buffer_atomic_umax v15, off, s[64:67], s20 offset:768\ns_mov_b64 exec, -1\nv_smfmac_f32_16x16x64_bf16 v[44:47], v[56:59], v[212:219], v0\n" \
    "ds_read_b128 v[56:59], v9 offset:8192\ns_add_u32 s84, s40, 0x20000\nv_smfmac_f32_16x16x64_bf16 v[40:43], v[60:63], v[220:227], v0\nds_read_b128 v[68:71], v9 offset:12288\n" \
    "s_cmp_lt_u32 s49, 62\ns_cselect_b32 s84, s84, s40\nv_smfmac_f32_16x16x64_bf16 v[44:47], v[60:63], v[236:243], v0\n" \
    "buffer_store_dwordx4 v[48:51], v8, s[64:67], s88 offen offset:0 sc1\nv_smfmac_f32_16x16x64_bf16 v[40:43], v[64:67], v[228:235], v0\nv_mov_b64 v[252:253], -1\n" \
    "v_smfmac_f32_16x16x64_bf16 v[44:47], v[64:67], v[244:251], v0\ns_nop 0\nv_mov_b64 v[254:255], -1\nv_mfma_f32_16x16x32_bf16 v[28:31], v[72:75], v[84:87], v[28:31]\n" \
    "ds_read_b128 v[60:63], v6 offset:128\ns_and_b32 s69, s49, 15\ns_waitcnt lgkmcnt(1)\nv_mfma_f32_16x16x32_bf16 v[16:19], a[16:19], v[56:59], v[16:19]\n" \
    "s_lshl_b32 s69, s69, 19\nv_cvt_pk_bf16_f32 v48, v40, v41\nv_cvt_pk_bf16_f32 v50, v42, v43\nv_mfma_f32_16x16x32_bf16 v[24:27], v[52:55], v[56:59], v[24:27]\n" \
    "ds_read_b128 v[56:59], v6 offset:1152\nv_cvt_pk_bf16_f32 v49, v44, v45\nv_cvt_pk_bf16_f32 v51, v46, v47\nv_mfma_f32_16x16x32_bf16 v[20:23], a[16:19], v[68:71], v[20:23]\n" \
    "s_add_u32 s82, s38, s69\nv_permlane16_swap_b32 v48, v49\nv_mfma_f32_16x16x32_bf16 v[28:31], v[52:55], v[68:71], v[28:31]\nds_read_b128 v[52:55], v6 offset:2176\n" \
    "s_waitcnt lgkmcnt(2)\nv_permlane16_swap_b32 v50, v51\nv_smfmac_f32_16x16x64_bf16 v[32:35], v[60:63], a[160:167], v0\nds_read_b128 v[64:67], v6 offset:3200\n" \
    "v_mov_b64 v[40:41], 0\nv_smfmac_f32_16x16x64_bf16 v[36:39], v[60:63], a[176:183], v0\nds_read_b128 v[68:71], v3 offset:5120\ns_waitcnt lgkmcnt(3)\nv_mov_b64 v[42:43], 0\n" \
    "v_smfmac_f32_16x16x64_bf16 v[32:35], v[56:59], a[168:175], v0\nds_read_b128 v[72:75], v10 offset:8192\nv_mov_b64 v[44:45], 0\n" \
    "v_smfmac_f32_16x16x64_bf16 v[36:39], v[56:59], a[184:191], v0\nds_read_b128 v[76:79], v10 offset:12288\ns_waitcnt lgkmcnt(4)\nv_mov_b64 v[46:47], 0\n" \
    "v_smfmac_f32_16x16x64_bf16 v[32:35], v[52:55], a[192:199], v0\nbuffer_store_dwordx4 v[48:51], v8, s[64:67], s89 offen offset:0 sc1\ns_lshr_b32 s69, s82, 8\n" \
    "v_smfmac_f32_16x16x64_bf16 v[36:39], v[52:55], a[208:215], v0\nds_read_b128 v[80:83], v3 offset:6144\ns_and_b32 s69, s69, 0x2000\ns_waitcnt lgkmcnt(4)\n" \
    "v_smfmac_f32_16x16x64_bf16 v[32:35], v[64:67], a[200:207], v0\nds_read_b128 v[84:87], v9 offset:9216\ns_xor_b32 s82, s82, s69\n" \
    "v_smfmac_f32_16x16x64_bf16 v[36:39], v[64:67], a[216:223], v0\nds_read_b128 v[88:91], v9 offset:13312\ns_and_b32 s69, s35, 15\ns_waitcnt lgkmcnt(3)\n" \
    "v_mfma_f32_16x16x32_bf16 v[16:19], a[20:23], v[72:75], v[16:19]\ns_lshl_b32 s69, s69, 19\nv_cvt_pk_bf16_f32 v48, v32, v33\nv_cvt_pk_bf16_f32 v50, v34, v35\n" \
    "v_mfma_f32_16x16x32_bf16 v[24:27], v[68:71], v[72:75], v[24:27]\ns_add_u32 s0, s38, s69\nv_cvt_pk_bf16_f32 v49, v36, v37\nv_cvt_pk_bf16_f32 v51, v38, v39\n" \
    "v_mfma_f32_16x16x32_bf16 v[20:23], a[20:23], v[76:79], v[20:23]\ns_lshr_b32 s69, s0, 8\nv_permlane16_swap_b32 v48, v49\n" \
    "v_mfma_f32_16x16x32_bf16 v[28:31], v[68:71], v[76:79], v[28:31]\ns_and_b32 s69, s69, 0x2000\ns_waitcnt lgkmcnt(0)\nv_permlane16_swap_b32 v50, v51\n" \
    "v_mfma_f32_16x16x32_bf16 v[16:19], a[24:27], v[84:87], v[16:19]\ns_xor_b32 s0, s0, s69\nv_mov_b64 v[32:33], 0\nv_mov_b64 v[34:35], 0\n" \
    "v_smfmac_f32_16x16x64_bf16 v[40:43], v[60:63], a[224:231], v0\ns_xor_b32 s1, s0, 0x10000\nv_mov_b64 v[36:37], 0\n" \
    "v_smfmac_f32_16x16x64_bf16 v[44:47], v[60:63], a[240:247], v0\ns_xor_b32 s2, s0, 0x20000\nv_mov_b64 v[38:39], 0\n" \
    "v_smfmac_f32_16x16x64_bf16 v[40:43], v[56:59], a[232:239], v0\nbuffer_store_dwordx4 v[48:51], v8, s[64:67], s86 offen offset:256 sc1\ns_xor_b32 s3, s0, 0x30000\n" \
    "v_smfmac_f32_16x16x64_bf16 v[44:47], v[56:59], a[248:255], v0\nv_lshrrev_b32 v15, 1, v2\nv_smfmac_f32_16x16x64_bf16 v[40:43], v[52:55], v[92:99], v0\n" \
    "ds_read_b128 v[68:71], v3 offset:7168\nv_add_u32 v15, s60, v15\nv_smfmac_f32_16x16x64_bf16 v[44:47], v[52:55], v[108:115], v0\nds_read_b128 v[72:75], v10 offset:9216\n" \
    "v_smfmac_f32_16x16x64_bf16 v[40:43], v[64:67], v[100:107], v0\nds_read_b128 v[76:79], v10 offset:13312\nv_smfmac_f32_16x16x64_bf16 v[44:47], v[64:67], v[116:123], v0\n" \
    "ds_write_b64 v15, v[252:253]\ns_and_b32 s69, s49, 255\nv_mfma_f32_16x16x32_bf16 v[24:27], v[80:83], v[84:87], v[24:27]\ns_lshl_b32 s69, s69, 19\ns_add_u32 s74, s63, s69\n" \
    "v_mfma_f32_16x16x32_bf16 v[20:23], a[24:27], v[88:91], v[20:23]\ns_cmp_lt_u32 s49, 64\ns_cselect_b32 s74, s74, 0x7fff0000\nv_cvt_pk_bf16_f32 v48, v40, v41\n" \
    "v_mfma_f32_16x16x32_bf16 v[28:31], v[80:83], v[88:91], v[28:31]\ns_waitcnt lgkmcnt(1)\nv_cvt_pk_bf16_f32 v50, v42, v43\nv_cvt_pk_bf16_f32 v49, v44, v45\n" \
    "v_mfma_f32_16x16x32_bf16 v[16:19], a[28:31], v[72:75], v[16:19]\nv_cvt_pk_bf16_f32 v51, v46, v47\nv_permlane16_swap_b32 v48, v49\n" \
    "v_mfma_f32_16x16x32_bf16 v[24:27], v[68:71], v[72:75], v[24:27]\nv_permlane16_swap_b32 v50, v51\nv_smfmac_f32_16x16x64_bf16 v[32:35], v[60:63], v[124:131], v0\n" \
    "v_mov_b64 v[40:41], 0\nv_smfmac_f32_16x16x64_bf16 v[36:39], v[60:63], v[140:147], v0\nv_mov_b64 v[42:43], 0\nv_smfmac_f32_16x16x64_bf16 v[32:35], v[56:59], v[132:139], v0\n" \
    "buffer_store_dwordx4 v[252:255], v2, s[64:67], s74 offen offset:0 sc1\nv_smfmac_f32_16x16x64_bf16 v[36:39], v[56:59], v[148:155], v0\n" \
    "ds_read_b128 v[72:75], v3 offset:8192\nv_mov_b64 v[44:45], 0\nv_smfmac_f32_16x16x64_bf16 v[32:35], v[52:55], v[156:163], v0\nds_read_b128 v[80:83], v9 offset:16384\n" \
    "v_mov_b64 v[46:47], 0\nv_smfmac_f32_16x16x64_bf16 v[36:39], v[52:55], v[172:179], v0\nds_read_b128 v[84:87], v9 offset:20480\n" \
    "v_smfmac_f32_16x16x64_bf16 v[32:35], v[64:67], v[164:171], v0\nbuffer_store_dwordx4 v[48:51], v8, s[64:67], s87 offen offset:256 sc1\n" \
    "v_smfmac_f32_16x16x64_bf16 v[36:39], v[64:67], v[180:187], v0\ns_nop 1\nv_mfma_f32_16x16x32_bf16 v[20:23], a[28:31], v[76:79], v[20:23]\ns_nop 1\n" \
    "v_mfma_f32_16x16x32_bf16 v[28:31], v[68:71], v[76:79], v[28:31]\ns_waitcnt lgkmcnt(0)\nv_cvt_pk_bf16_f32 v48, v32, v33\nv_cvt_pk_bf16_f32 v50, v34, v35\n" \
    "v_mfma_f32_16x16x32_bf16 v[16:19], a[32:35], v[80:83], v[16:19]\nbuffer_store_dwordx4 v[252:255], v2, s[64:67], s74 offen offset:1024 sc1\n" \
    "v_mfma_f32_16x16x32_bf16 v[24:27], v[72:75], v[80:83], v[24:27]\nv_cvt_pk_bf16_f32 v49, v36, v37\nv_cvt_pk_bf16_f32 v51, v38, v39\n" \
    "v_mfma_f32_16x16x32_bf16 v[20:23], a[32:35], v[84:87], v[20:23]\nv_permlane16_swap_b32 v48, v49\nv_smfmac_f32_16x16x64_bf16 v[40:43], v[60:63], v[188:195], v0\n" \
    "v_permlane16_swap_b32 v50, v51\nv_smfmac_f32_16x16x64_bf16 v[44:47], v[60:63], v[204:211], v0\nv_mov_b64 v[32:33], 0\n" \
    "v_smfmac_f32_16x16x64_bf16 v[40:43], v[56:59], v[196:203], v0\nds_read_b128 v[60:63], v3 offset:9216\nv_mov_b64 v[34:35], 0\n" \
    "v_smfmac_f32_16x16x64_bf16 v[44:47], v[56:59], v[212:219], v0\nds_read_b128 v[56:59], v10 offset:16384\nv_mov_b64 v[36:37], 0\n" \
    "v_smfmac_f32_16x16x64_bf16 v[40:43], v[52:55], v[220:227], v0\nds_read_b128 v[68:71], v10 offset:20480\nv_mov_b64 v[38:39], 0\n" \
    "v_smfmac_f32_16x16x64_bf16 v[44:47], v[52:55], v[236:243], v0\nbuffer_store_dwordx4 v[48:51], v8, s[64:67], s88 offen offset:256 sc1\n" \
    "v_smfmac_f32_16x16x64_bf16 v[40:43], v[64:67], v[228:235], v0\nv_smfmac_f32_16x16x64_bf16 v[44:47], v[64:67], v[244:251], v0\ns_nop 1\n" \
    "v_mfma_f32_16x16x32_bf16 v[28:31], v[72:75], v[84:87], v[28:31]\nds_read_b128 v[52:55], v6 offset:4096\ns_nop 0\ns_waitcnt lgkmcnt(1)\n" \
    "v_mfma_f32_16x16x32_bf16 v[16:19], a[36:39], v[56:59], v[16:19]\nv_cvt_pk_bf16_f32 v48, v40, v41\nv_cvt_pk_bf16_f32 v50, v42, v43\n" \
    "v_mfma_f32_16x16x32_bf16 v[24:27], v[60:63], v[56:59], v[24:27]\nds_read_b128 v[56:59], v6 offset:5120\nv_cvt_pk_bf16_f32 v49, v44, v45\nv_cvt_pk_bf16_f32 v51, v46, v47\n" \
    "v_mfma_f32_16x16x32_bf16 v[20:23], a[36:39], v[68:71], v[20:23]\nv_permlane16_swap_b32 v48, v49\nv_mfma_f32_16x16x32_bf16 v[28:31], v[60:63], v[68:71], v[28:31]\n" \
    "ds_read_b128 v[60:63], v6 offset:6144\ns_waitcnt lgkmcnt(2)\nv_permlane16_swap_b32 v50, v51\nv_smfmac_f32_16x16x64_bf16 v[32:35], v[52:55], a[160:167], v0\n" \
    "ds_read_b128 v[64:67], v6 offset:7168\nv_mov_b64 v[40:41], 0\nv_smfmac_f32_16x16x64_bf16 v[36:39], v[52:55], a[176:183], v0\nds_read_b128 v[68:71], v3 offset:10240\n" \
    "s_waitcnt lgkmcnt(3)\nv_mov_b64 v[42:43], 0\nv_smfmac_f32_16x16x64_bf16 v[32:35], v[56:59], a[168:175], v0\nds_read_b128 v[72:75], v9 offset:17408\nv_mov_b64 v[44:45], 0\n" \
    "v_smfmac_f32_16x16x64_bf16 v[36:39], v[56:59], a[184:191], v0\nds_read_b128 v[76:79], v9 offset:21504\ns_waitcnt lgkmcnt(4)\nv_mov_b64 v[46:47], 0\n" \
    "v_smfmac_f32_16x16x64_bf16 v[32:35], v[60:63], a[192:199], v0\nbuffer_store_dwordx4 v[48:51], v8, s[64:67], s89 offen offset:256 sc1\n" \
    "v_smfmac_f32_16x16x64_bf16 v[36:39], v[60:63], a[208:215], v0\nds_read_b128 v[80:83], v3 offset:11264\ns_waitcnt lgkmcnt(4)\n" \
    "v_smfmac_f32_16x16x64_bf16 v[32:35], v[64:67], a[200:207], v0\nds_read_b128 v[84:87], v10 offset:17408\nv_smfmac_f32_16x16x64_bf16 v[36:39], v[64:67], a[216:223], v0\n" \
    "ds_read_b128 v[88:91], v10 offset:21504\ns_nop 0\ns_waitcnt lgkmcnt(3)\nv_mfma_f32_16x16x32_bf16 v[16:19], a[40:43], v[72:75], v[16:19]\n" \
    "buffer_store_dwordx4 v[252:255], v7, s[64:67], s82 offen sc1\ns_xor_b32 s82, s82, 0x10000\nv_mfma_f32_16x16x32_bf16 v[24:27], v[68:71], v[72:75], v[24:27]\n" \
    "v_cvt_pk_bf16_f32 v48, v32, v33\nv_cvt_pk_bf16_f32 v50, v34, v35\nv_mfma_f32_16x16x32_bf16 v[20:23], a[40:43], v[76:79], v[20:23]\nv_cvt_pk_bf16_f32 v49, v36, v37\n" \
    "v_cvt_pk_bf16_f32 v51, v38, v39\nv_mfma_f32_16x16x32_bf16 v[28:31], v[68:71], v[76:79], v[28:31]\ns_waitcnt lgkmcnt(0)\nv_permlane16_swap_b32 v48, v49\n" \
    "v_mfma_f32_16x16x32_bf16 v[16:19], a[44:47], v[84:87], v[16:19]\nv_permlane16_swap_b32 v50, v51\nv_smfmac_f32_16x16x64_bf16 v[40:43], v[52:55], a[224:231], v0\n" \
    "v_mov_b64 v[32:33], 0\nv_smfmac_f32_16x16x64_bf16 v[44:47], v[52:55], a[240:247], v0\nv_mov_b64 v[34:35], 0\nv_smfmac_f32_16x16x64_bf16 v[40:43], v[56:59], a[232:239], v0\n" \
    "v_mov_b64 v[36:37], 0\nv_smfmac_f32_16x16x64_bf16 v[44:47], v[56:59], a[248:255], v0\nv_mov_b64 v[38:39], 0\nv_smfmac_f32_16x16x64_bf16 v[40:43], v[60:63], v[92:99], v0\n" \
    "ds_read_b128 v[68:71], v3 offset:12288\ns_waitcnt vmcnt(11)\ns_barrier\nv_smfmac_f32_16x16x64_bf16 v[44:47], v[60:63], v[108:115], v0\n" \
    "ds_read_b128 v[72:75], v9 offset:40960\nv_smfmac_f32_16x16x64_bf16 v[40:43], v[64:67], v[100:107], v0\nds_read_b128 v[76:79], v9 offset:45056\ns_add_u32 m0, s41, 0\n" \
    "v_smfmac_f32_16x16x64_bf16 v[44:47], v[64:67], v[116:123], v0\nbuffer_store_dwordx4 v[48:51], v8, s[64:67], s86 offen offset:512 sc1\ns_xor_b32 s85, s62, 1536\n" \
    "v_mfma_f32_16x16x32_bf16 v[24:27], v[80:83], v[84:87], v[24:27]\ns_add_u32 s85, s85, s40\ns_nop 0\nv_cvt_pk_bf16_f32 v48, v40, v41\n" \
    "v_mfma_f32_16x16x32_bf16 v[20:23], a[44:47], v[88:91], v[20:23]\nv_cvt_pk_bf16_f32 v50, v42, v43\nv_cvt_pk_bf16_f32 v49, v44, v45\n" \
    "v_mfma_f32_16x16x32_bf16 v[28:31], v[80:83], v[88:91], v[28:31]\ns_waitcnt lgkmcnt(0)\nv_cvt_pk_bf16_f32 v51, v46, v47\nv_permlane16_swap_b32 v48, v49\n" \
    "v_mfma_f32_16x16x32_bf16 v[16:19], a[48:51], v[72:75], v[16:19]\nbuffer_load_dwordx4 v11, s[64:67], s85 offen sc1 lds\n" \
    "v_mfma_f32_16x16x32_bf16 v[24:27], v[68:71], v[72:75], v[24:27]\nv_permlane16_swap_b32 v50, v51\nv_smfmac_f32_16x16x64_bf16 v[32:35], v[52:55], v[124:131], v0\n" \
    "v_mov_b64 v[40:41], 0\nv_smfmac_f32_16x16x64_bf16 v[36:39], v[52:55], v[140:147], v0\nbuffer_load_dwordx4 v12, s[64:67], s85 offen offset:1024 sc1 lds\n" \
    "v_smfmac_f32_16x16x64_bf16 v[32:35], v[56:59], v[132:139], v0\ns_add_u32 m0, s41, 8192\nv_mov_b64 v[42:43], 0\n" \
    "v_smfmac_f32_16x16x64_bf16 v[36:39], v[56:59], v[148:155], v0\nds_read_b128 v[72:75], v3 offset:13312\nv_mov_b64 v[44:45], 0\n" \
    "v_smfmac_f32_16x16x64_bf16 v[32:35], v[60:63], v[156:163], v0\nds_read_b128 v[80:83], v10 offset:40960\nv_mov_b64 v[46:47], 0\n" \
    "v_smfmac_f32_16x16x64_bf16 v[36:39], v[60:63], v[172:179], v0\nds_read_b128 v[84:87], v10 offset:45056\ns_xor_b32 s85, s62, 1792\n" \
    "v_smfmac_f32_16x16x64_bf16 v[32:35], v[64:67], v[164:171], v0\nbuffer_store_dwordx4 v[48:51], v8, s[64:67], s87 offen offset:512 sc1\ns_add_u32 s85, s85, s40\n" \
    "v_smfmac_f32_16x16x64_bf16 v[36:39], v[64:67], v[180:187], v0\ns_nop 1\nv_mfma_f32_16x16x32_bf16 v[20:23], a[48:51], v[76:79], v[20:23]\ns_nop 1\n" \
    "v_cvt_pk_bf16_f32 v48, v32, v33\nv_cvt_pk_bf16_f32 v50, v34, v35\nv_mfma_f32_16x16x32_bf16 v[28:31], v[68:71], v[76:79], v[28:31]\ns_waitcnt lgkmcnt(0)\n" \
    "v_cvt_pk_bf16_f32 v49, v36, v37\nv_cvt_pk_bf16_f32 v51, v38, v39\nv_mfma_f32_16x16x32_bf16 v[16:19], a[52:55], v[80:83], v[16:19]\n" \
    "buffer_load_dwordx4 v11, s[64:67], s85 offen sc1 lds\nv_mfma_f32_16x16x32_bf16 v[24:27], v[72:75], v[80:83], v[24:27]\nv_permlane16_swap_b32 v48, v49\n" \
    "v_mfma_f32_16x16x32_bf16 v[20:23], a[52:55], v[84:87], v[20:23]\nv_permlane16_swap_b32 v50, v51\nv_smfmac_f32_16x16x64_bf16 v[40:43], v[52:55], v[188:195], v0\n" \
    "buffer_load_dwordx4 v12, s[64:67], s85 offen offset:1024 sc1 lds\nv_smfmac_f32_16x16x64_bf16 v[44:47], v[52:55], v[204:211], v0\nv_mov_b64 v[32:33], 0\n" \
    "v_smfmac_f32_16x16x64_bf16 v[40:43], v[56:59], v[196:203], v0\nds_read_b128 v[52:55], v3 offset:14336\nv_mov_b64 v[34:35], 0\n" \
    "v_smfmac_f32_16x16x64_bf16 v[44:47], v[56:59], v[212:219], v0\nds_read_b128 v[56:59], v9 offset:41984\nv_mov_b64 v[36:37], 0\n" \
    "v_smfmac_f32_16x16x64_bf16 v[40:43], v[60:63], v[220:227], v0\nds_read_b128 v[68:71], v9 offset:46080\nv_mov_b64 v[38:39], 0\n" \
    "v_smfmac_f32_16x16x64_bf16 v[44:47], v[60:63], v[236:243], v0\nbuffer_store_dwordx4 v[48:51], v8, s[64:67], s88 offen offset:512 sc1\n" \
    "v_smfmac_f32_16x16x64_bf16 v[40:43], v[64:67], v[228:235], v0\nv_smfmac_f32_16x16x64_bf16 v[44:47], v[64:67], v[244:251], v0\ns_nop 1\n" \
    "v_mfma_f32_16x16x32_bf16 v[28:31], v[72:75], v[84:87], v[28:31]\nds_read_b128 v[60:63], v6 offset:4224\ns_nop 0\ns_waitcnt lgkmcnt(1)\n" \
    "v_mfma_f32_16x16x32_bf16 v[16:19], a[56:59], v[56:59], v[16:19]\nbuffer_store_dwordx4 v[252:255], v7, s[64:67], s82 offen sc1\n" \
    "v_mfma_f32_16x16x32_bf16 v[24:27], v[52:55], v[56:59], v[24:27]\nds_read_b128 v[56:59], v6 offset:5248\nv_cvt_pk_bf16_f32 v48, v40, v41\nv_cvt_pk_bf16_f32 v50, v42, v43\n" \
    "v_mfma_f32_16x16x32_bf16 v[20:23], a[56:59], v[68:71], v[20:23]\ns_xor_b32 s82, s82, 0x30000\nv_cvt_pk_bf16_f32 v49, v44, v45\nv_cvt_pk_bf16_f32 v51, v46, v47\n" \
    "v_mfma_f32_16x16x32_bf16 v[28:31], v[52:55], v[68:71], v[28:31]\nds_read_b128 v[52:55], v6 offset:6272\ns_waitcnt lgkmcnt(2)\nv_permlane16_swap_b32 v48, v49\n" \
    "v_smfmac_f32_16x16x64_bf16 v[32:35], v[60:63], a[160:167], v0\nds_read_b128 v[64:67], v6 offset:7296\nv_permlane16_swap_b32 v50, v51\n" \
    "v_smfmac_f32_16x16x64_bf16 v[36:39], v[60:63], a[176:183], v0\nds_read_b128 v[68:71], v3 offset:15360\ns_waitcnt lgkmcnt(3)\nv_mov_b64 v[40:41], 0\n" \
    "v_smfmac_f32_16x16x64_bf16 v[32:35], v[56:59], a[168:175], v0\nds_read_b128 v[72:75], v10 offset:41984\nv_mov_b64 v[42:43], 0\n" \
    "v_smfmac_f32_16x16x64_bf16 v[36:39], v[56:59], a[184:191], v0\nds_read_b128 v[76:79], v10 offset:46080\ns_waitcnt lgkmcnt(4)\nv_mov_b64 v[44:45], 0\n" \
    "v_smfmac_f32_16x16x64_bf16 v[32:35], v[52:55], a[192:199], v0\nv_mov_b64 v[46:47], 0\nv_smfmac_f32_16x16x64_bf16 v[36:39], v[52:55], a[208:215], v0\n" \
    "ds_read_b128 v[80:83], v3 offset:16384\ns_waitcnt lgkmcnt(4)\nv_smfmac_f32_16x16x64_bf16 v[32:35], v[64:67], a[200:207], v0\nds_read_b128 v[84:87], v9 offset:24576\n" \
    "v_smfmac_f32_16x16x64_bf16 v[36:39], v[64:67], a[216:223], v0\nds_read_b128 v[88:91], v9 offset:28672\ns_waitcnt lgkmcnt(3)\n" \
    "v_mfma_f32_16x16x32_bf16 v[16:19], a[60:63], v[72:75], v[16:19]\nbuffer_store_dwordx4 v[48:51], v8, s[64:67], s89 offen offset:512 sc1\ns_nop 0\n" \
    "v_mfma_f32_16x16x32_bf16 v[24:27], v[68:71], v[72:75], v[24:27]\nv_cvt_pk_bf16_f32 v48, v32, v33\nv_cvt_pk_bf16_f32 v50, v34, v35\n" \
    "v_mfma_f32_16x16x32_bf16 v[20:23], a[60:63], v[76:79], v[20:23]\nv_cvt_pk_bf16_f32 v49, v36, v37\nv_cvt_pk_bf16_f32 v51, v38, v39\n" \
    "v_mfma_f32_16x16x32_bf16 v[28:31], v[68:71], v[76:79], v[28:31]\ns_waitcnt lgkmcnt(0)\nv_permlane16_swap_b32 v48, v49\n" \
    "v_mfma_f32_16x16x32_bf16 v[16:19], a[64:67], v[84:87], v[16:19]\nv_permlane16_swap_b32 v50, v51\nv_smfmac_f32_16x16x64_bf16 v[40:43], v[60:63], a[224:231], v0\n" \
    "buffer_store_dwordx4 v[252:255], v7, s[64:67], s82 offen sc1\nv_smfmac_f32_16x16x64_bf16 v[44:47], v[60:63], a[240:247], v0\ns_xor_b32 s82, s82, 0x10000\n" \
    "v_mov_b64 v[32:33], 0\nv_smfmac_f32_16x16x64_bf16 v[40:43], v[56:59], a[232:239], v0\nv_mov_b64 v[34:35], 0\nv_smfmac_f32_16x16x64_bf16 v[44:47], v[56:59], a[248:255], v0\n" \
    "v_mov_b64 v[36:37], 0\nv_smfmac_f32_16x16x64_bf16 v[40:43], v[52:55], v[92:99], v0\nds_read_b128 v[68:71], v3 offset:17408\nv_mov_b64 v[38:39], 0\n" \
    "v_smfmac_f32_16x16x64_bf16 v[44:47], v[52:55], v[108:115], v0\nds_read_b128 v[72:75], v10 offset:24576\nv_smfmac_f32_16x16x64_bf16 v[40:43], v[64:67], v[100:107], v0\n" \
    "ds_read_b128 v[76:79], v10 offset:28672\nv_smfmac_f32_16x16x64_bf16 v[44:47], v[64:67], v[116:123], v0\n" \
    "buffer_store_dwordx4 v[48:51], v8, s[64:67], s86 offen offset:768 sc1\ns_nop 0\nv_mfma_f32_16x16x32_bf16 v[24:27], v[80:83], v[84:87], v[24:27]\ns_nop 1\n" \
    "v_mfma_f32_16x16x32_bf16 v[20:23], a[64:67], v[88:91], v[20:23]\nv_cvt_pk_bf16_f32 v48, v40, v41\nv_cvt_pk_bf16_f32 v50, v42, v43\n" \
    "v_mfma_f32_16x16x32_bf16 v[28:31], v[80:83], v[88:91], v[28:31]\ns_waitcnt lgkmcnt(0)\nv_cvt_pk_bf16_f32 v49, v44, v45\nv_cvt_pk_bf16_f32 v51, v46, v47\n" \
    "v_mfma_f32_16x16x32_bf16 v[16:19], a[68:71], v[72:75], v[16:19]\nv_permlane16_swap_b32 v48, v49\nv_mfma_f32_16x16x32_bf16 v[24:27], v[68:71], v[72:75], v[24:27]\n" \
    "v_permlane16_swap_b32 v50, v51\nv_smfmac_f32_16x16x64_bf16 v[32:35], v[60:63], v[124:131], v0\nv_mov_b64 v[40:41], 0\n" \
    "v_smfmac_f32_16x16x64_bf16 v[36:39], v[60:63], v[140:147], v0\nv_mov_b64 v[42:43], 0\nv_smfmac_f32_16x16x64_bf16 v[32:35], v[56:59], v[132:139], v0\nv_mov_b64 v[44:45], 0\n" \
    "v_smfmac_f32_16x16x64_bf16 v[36:39], v[56:59], v[148:155], v0\nds_read_b128 v[72:75], v3 offset:18432\nv_mov_b64 v[46:47], 0\n" \
    "v_smfmac_f32_16x16x64_bf16 v[32:35], v[52:55], v[156:163], v0\nds_read_b128 v[80:83], v9 offset:25600\nv_smfmac_f32_16x16x64_bf16 v[36:39], v[52:55], v[172:179], v0\n" \
    "ds_read_b128 v[84:87], v9 offset:29696\nv_smfmac_f32_16x16x64_bf16 v[32:35], v[64:67], v[164:171], v0\n" \
    "buffer_store_dwordx4 v[48:51], v8, s[64:67], s87 offen offset:768 sc1\nv_smfmac_f32_16x16x64_bf16 v[36:39], v[64:67], v[180:187], v0\ns_nop 1\n" \
    "v_mfma_f32_16x16x32_bf16 v[20:23], a[68:71], v[76:79], v[20:23]\ns_nop 1\nv_mfma_f32_16x16x32_bf16 v[28:31], v[68:71], v[76:79], v[28:31]\ns_waitcnt lgkmcnt(0)\n" \
    "v_cvt_pk_bf16_f32 v48, v32, v33\nv_cvt_pk_bf16_f32 v50, v34, v35\nv_mfma_f32_16x16x32_bf16 v[16:19], a[72:75], v[80:83], v[16:19]\n" \
    "buffer_store_dwordx4 v[252:255], v7, s[64:67], s82 offen sc1\nv_mfma_f32_16x16x32_bf16 v[24:27], v[72:75], v[80:83], v[24:27]\ns_xor_b32 s82, s82, 0x30000\n" \
    "v_cvt_pk_bf16_f32 v49, v36, v37\nv_cvt_pk_bf16_f32 v51, v38, v39\nv_mfma_f32_16x16x32_bf16 v[20:23], a[72:75], v[84:87], v[20:23]\ns_add_u32 s49, s49, 2\n" \
    "v_permlane16_swap_b32 v48, v49\nv_smfmac_f32_16x16x64_bf16 v[40:43], v[60:63], v[188:195], v0\nv_permlane16_swap_b32 v50, v51\n" \
    "v_smfmac_f32_16x16x64_bf16 v[44:47], v[60:63], v[204:211], v0\nbuffer_store_dwordx4 v[48:51], v8, s[64:67], s88 offen offset:768 sc1\n" \
    "v_smfmac_f32_16x16x64_bf16 v[40:43], v[56:59], v[196:203], v0\nds_read_b128 v[60:63], v3 offset:19456\nv_smfmac_f32_16x16x64_bf16 v[44:47], v[56:59], v[212:219], v0\n" \
    "ds_read_b128 v[56:59], v10 offset:25600\nv_smfmac_f32_16x16x64_bf16 v[40:43], v[52:55], v[220:227], v0\nds_read_b128 v[68:71], v10 offset:29696\n" \
    "v_smfmac_f32_16x16x64_bf16 v[44:47], v[52:55], v[236:243], v0\nbuffer_load_dword v13, off, s[64:67], s42 sc1\ns_and_b32 s69, s35, 15\n" \
    "v_smfmac_f32_16x16x64_bf16 v[40:43], v[64:67], v[228:235], v0\nds_read_b128 v[52:55], v3 offset:20480\ns_lshl_b32 s69, s69, 17\n" \
    "v_smfmac_f32_16x16x64_bf16 v[44:47], v[64:67], v[244:251], v0\nds_read_b128 v[64:67], v9 offset:32768\ns_add_u32 s81, s37, s69\n" \
    "v_mfma_f32_16x16x32_bf16 v[28:31], v[72:75], v[84:87], v[28:31]\nds_read_b128 v[72:75], v9 offset:36864\ns_lshr_b32 s69, s81, 8\ns_waitcnt lgkmcnt(3)\n" \
    "v_mfma_f32_16x16x32_bf16 v[16:19], a[76:79], v[56:59], v[16:19]\ns_and_b32 s69, s69, 0x2000\nv_cvt_pk_bf16_f32 v48, v40, v41\nv_cvt_pk_bf16_f32 v50, v42, v43\n" \
    "v_mfma_f32_16x16x32_bf16 v[24:27], v[60:63], v[56:59], v[24:27]\nds_read_b128 v[56:59], v3 offset:21504\nv_cvt_pk_bf16_f32 v49, v44, v45\nv_cvt_pk_bf16_f32 v51, v46, v47\n" \
    "v_mfma_f32_16x16x32_bf16 v[20:23], a[76:79], v[68:71], v[20:23]\nds_read_b128 v[76:79], v10 offset:32768\nv_permlane16_swap_b32 v48, v49\n" \
    "v_mfma_f32_16x16x32_bf16 v[28:31], v[60:63], v[68:71], v[28:31]\nds_read_b128 v[60:63], v10 offset:36864\ns_waitcnt lgkmcnt(3)\nv_permlane16_swap_b32 v50, v51\n" \
    "v_mfma_f32_16x16x32_bf16 v[16:19], a[80:83], v[64:67], v[16:19]\nbuffer_store_dwordx4 v[48:51], v8, s[64:67], s89 offen offset:768 sc1\ns_xor_b32 s81, s81, s69\n" \
    "v_mfma_f32_16x16x32_bf16 v[24:27], v[52:55], v[64:67], v[24:27]\nds_read_b128 v[64:67], v3 offset:22528\ns_mov_b32 s68, 0\n" \
    "v_mfma_f32_16x16x32_bf16 v[20:23], a[80:83], v[72:75], v[20:23]\nds_read_b128 v[68:71], v9 offset:33792\nv_mfma_f32_16x16x32_bf16 v[28:31], v[52:55], v[72:75], v[28:31]\n" \
    "ds_read_b128 v[52:55], v9 offset:37888\ns_waitcnt lgkmcnt(3)\nv_mfma_f32_16x16x32_bf16 v[16:19], a[84:87], v[76:79], v[16:19]\n" \
    "v_mfma_f32_16x16x32_bf16 v[24:27], v[56:59], v[76:79], v[24:27]\nds_read_b128 v[72:75], v3 offset:23552\nv_mfma_f32_16x16x32_bf16 v[20:23], a[84:87], v[60:63], v[20:23]\n" \
    "ds_read_b128 v[76:79], v10 offset:33792\nv_mfma_f32_16x16x32_bf16 v[28:31], v[56:59], v[60:63], v[28:31]\nds_read_b128 v[56:59], v10 offset:37888\ns_waitcnt lgkmcnt(3)\n" \
    "v_mfma_f32_16x16x32_bf16 v[16:19], a[88:91], v[68:71], v[16:19]\nv_mfma_f32_16x16x32_bf16 v[24:27], v[64:67], v[68:71], v[24:27]\ns_waitcnt vmcnt(10)\ns_barrier\n" \
    "v_mfma_f32_16x16x32_bf16 v[20:23], a[88:91], v[52:55], v[20:23]\nds_read_b128 v[60:63], v9 offset:0\nv_mfma_f32_16x16x32_bf16 v[28:31], v[64:67], v[52:55], v[28:31]\n" \
    "ds_read_b128 v[52:55], v9 offset:4096\ns_add_u32 m0, s41, 16384\ns_waitcnt lgkmcnt(2)\nv_mfma_f32_16x16x32_bf16 v[16:19], a[92:95], v[76:79], v[16:19]\n" \
    "s_xor_b32 s85, s62, 512\ns_add_u32 s85, s85, s84\nv_mfma_f32_16x16x32_bf16 v[24:27], v[72:75], v[76:79], v[24:27]\nbuffer_load_dwordx4 v11, s[64:67], s85 offen sc1 lds\n" \
    "v_mfma_f32_16x16x32_bf16 v[20:23], a[92:95], v[56:59], v[20:23]\nds_read_b128 v[64:67], v10 offset:0\nv_mfma_f32_16x16x32_bf16 v[28:31], v[72:75], v[56:59], v[28:31]\n" \
    "ds_read_b128 v[56:59], v10 offset:4096\ns_waitcnt lgkmcnt(2)\nv_mfma_f32_16x16x32_bf16 v[16:19], a[96:99], v[60:63], v[16:19]\n" \
    "buffer_load_dwordx4 v12, s[64:67], s85 offen offset:1024 sc1 lds\ns_add_u32 m0, s41, 40960\nv_mfma_f32_16x16x32_bf16 v[24:27], a[104:107], v[60:63], v[24:27]\n" \
    "s_xor_b32 s85, s62, 768\ns_add_u32 s85, s85, s84\nv_mfma_f32_16x16x32_bf16 v[20:23], a[96:99], v[52:55], v[20:23]\nds_read_b128 v[60:63], v9 offset:1024\n" \
    "v_mfma_f32_16x16x32_bf16 v[28:31], a[104:107], v[52:55], v[28:31]\nds_read_b128 v[52:55], v9 offset:5120\ns_waitcnt lgkmcnt(2)\n" \
    "v_mfma_f32_16x16x32_bf16 v[16:19], a[100:103], v[64:67], v[16:19]\nbuffer_load_dwordx4 v11, s[64:67], s85 offen sc1 lds\n" \
    "v_mfma_f32_16x16x32_bf16 v[24:27], a[108:111], v[64:67], v[24:27]\nv_mfma_f32_16x16x32_bf16 v[20:23], a[100:103], v[56:59], v[20:23]\n" \
    "ds_read_b128 v[64:67], v10 offset:1024\nv_mfma_f32_16x16x32_bf16 v[28:31], a[108:111], v[56:59], v[28:31]\nds_read_b128 v[56:59], v10 offset:5120\ns_waitcnt lgkmcnt(2)\n" \
    "v_mfma_f32_16x16x32_bf16 v[16:19], a[112:115], v[60:63], v[16:19]\nbuffer_load_dwordx4 v12, s[64:67], s85 offen offset:1024 sc1 lds\ns_waitcnt vmcnt(5)\n" \
    "v_mfma_f32_16x16x32_bf16 v[24:27], a[120:123], v[60:63], v[24:27]\nv_readfirstlane_b32 s74, v13\nv_mfma_f32_16x16x32_bf16 v[20:23], a[112:115], v[52:55], v[20:23]\n" \
    "ds_read_b128 v[60:63], v9 offset:8192\ns_and_b32 s75, s74, 127\nv_mfma_f32_16x16x32_bf16 v[28:31], a[120:123], v[52:55], v[28:31]\nds_read_b128 v[52:55], v9 offset:12288\n" \
    "s_lshr_b32 s69, s74, 8\ns_waitcnt lgkmcnt(2)\nv_mfma_f32_16x16x32_bf16 v[16:19], a[116:119], v[64:67], v[16:19]\ns_sub_u32 s69, s69, s80\ns_bfe_i32 s69, s69, 0x180000\n" \
    "v_mfma_f32_16x16x32_bf16 v[24:27], a[124:127], v[64:67], v[24:27]\ns_cmp_eq_u32 s75, 0\ns_cselect_b32 s69, 0, s69\n" \
    "v_mfma_f32_16x16x32_bf16 v[20:23], a[116:119], v[56:59], v[20:23]\nds_read_b128 v[64:67], v10 offset:8192\ns_add_u32 s80, s80, s69\n" \
    "v_mfma_f32_16x16x32_bf16 v[28:31], a[124:127], v[56:59], v[28:31]\nds_read_b128 v[56:59], v10 offset:12288\ns_lshl_b32 s74, s74, 5\ns_waitcnt lgkmcnt(2)\n" \
    "v_mfma_f32_16x16x32_bf16 v[16:19], a[128:131], v[60:63], v[16:19]\ns_and_b32 s74, s74, 0x1000\ns_andn2_b32 s81, s81, 0x1000\n" \
    "v_mfma_f32_16x16x32_bf16 v[24:27], a[136:139], v[60:63], v[24:27]\ns_or_b32 s81, s81, s74\nv_mfma_f32_16x16x32_bf16 v[20:23], a[128:131], v[52:55], v[20:23]\n" \
    "ds_read_b128 v[60:63], v9 offset:9216\nv_mfma_f32_16x16x32_bf16 v[28:31], a[136:139], v[52:55], v[28:31]\nds_read_b128 v[52:55], v9 offset:13312\ns_waitcnt lgkmcnt(2)\n" \
    "v_mfma_f32_16x16x32_bf16 v[16:19], a[132:135], v[64:67], v[16:19]\nds_read_b128 v[68:71], v10 offset:9216\n" \
    "v_mfma_f32_16x16x32_bf16 v[24:27], a[140:143], v[64:67], v[24:27]\nds_read_b128 v[64:67], v10 offset:13312\n" \
    "v_mfma_f32_16x16x32_bf16 v[20:23], a[132:135], v[56:59], v[20:23]\nv_mfma_f32_16x16x32_bf16 v[28:31], a[140:143], v[56:59], v[28:31]\ns_waitcnt lgkmcnt(2)\n" \
    "v_mfma_f32_16x16x32_bf16 v[16:19], a[144:147], v[60:63], v[16:19]\nv_mfma_f32_16x16x32_bf16 v[24:27], a[152:155], v[60:63], v[24:27]\ns_waitcnt lgkmcnt(0)\ns_barrier\n" \
    "v_mfma_f32_16x16x32_bf16 v[16:19], a[148:151], v[68:71], v[16:19]\ns_add_u32 m0, s41, 0\ns_xor_b32 s85, s62, 0\n" \
    "v_mfma_f32_16x16x32_bf16 v[24:27], a[156:159], v[68:71], v[24:27]\ns_add_u32 s85, s85, s84\nbuffer_load_dwordx4 v11, s[64:67], s85 offen sc1 lds\n" \
    "v_mfma_f32_16x16x32_bf16 v[20:23], a[144:147], v[52:55], v[20:23]\ns_nop 1\nv_cvt_pk_bf16_f32 v32, v16, v17\nv_cvt_pk_bf16_f32 v33, v18, v19\n" \
    "v_mfma_f32_16x16x32_bf16 v[28:31], a[152:155], v[52:55], v[28:31]\nv_cvt_pk_bf16_f32 v34, v24, v25\nv_cvt_pk_bf16_f32 v35, v26, v27\n" \
    "v_mfma_f32_16x16x32_bf16 v[20:23], a[148:151], v[64:67], v[20:23]\nds_write_b128 v14, v[32:35] offset:0\nv_mfma_f32_16x16x32_bf16 v[28:31], a[156:159], v[64:67], v[28:31]\n" \
    "buffer_load_dwordx4 v12, s[64:67], s85 offen offset:1024 sc1 lds\ns_add_u32 m0, s41, 8192\ns_xor_b32 s85, s62, 256\ns_add_u32 s85, s85, s84\n" \
    "buffer_load_dwordx4 v11, s[64:67], s85 offen sc1 lds\nbuffer_load_dwordx4 v12, s[64:67], s85 offen offset:1024 sc1 lds\ns_mov_b32 s40, s84\ns_sub_u32 s69, s50, s35\n" \
    "s_max_u32 s76, s35, 1\ns_add_u32 s69, s69, s76\ns_sub_u32 s69, s69, s35\ns_cmp_eq_u32 s69, 2\ns_cbranch_scc1 .Lv12_%=\n.Lv4_%=:\n.Lv13_%=:\ns_cmp_lt_u32 s75, s35\n" \
    "s_cbranch_scc1 .Lv14_%=\ns_cmp_eq_u32 s36, 1\ns_cbranch_scc1 .Lv9_%=\ns_cmp_eq_u32 s36, 2\ns_cbranch_scc1 .Lv10_%=\ns_cmp_eq_u32 s36, 3\ns_cbranch_scc1 .Lv11_%=\n.Lv8_%=:\n" \
    ".Lv15_%=:\ns_memrealtime s[72:73]\ns_waitcnt lgkmcnt(0)\ns_mov_b32 s68, 0\ns_sub_u32 s76, s80, s45\ns_sub_u32 s69, s72, s76\ns_cmp_lt_i32 s69, -8\ns_cbranch_scc1 .Lv18_%=\n" \
    "s_xor_b32 s24, s81, 0\ns_xor_b32 s25, s81, 1024\ns_xor_b32 s26, s81, 2048\ns_xor_b32 s27, s81, 3072\n.Lv20_%=:\ns_memrealtime s[72:73]\ns_waitcnt lgkmcnt(0)\n" \
    "s_sub_u32 s72, s72, s80\ns_cmp_lt_i32 s72, 0\ns_cbranch_scc1 .Lv20_%=\nbuffer_load_dwordx4 v[72:75], v2, s[64:67], s24 offen sc1\nv_cvt_pk_bf16_f32 v36, v20, v21\n" \
    "v_cvt_pk_bf16_f32 v37, v22, v23\nv_cvt_pk_bf16_f32 v38, v28, v29\nbuffer_load_dwordx4 v[76:79], v2, s[64:67], s25 offen sc1\nv_cvt_pk_bf16_f32 v39, v30, v31\n" \
    "ds_write_b128 v14, v[36:39] offset:4096\nds_read_b128 v[24:27], v3 offset:0\nbuffer_load_dwordx4 v[80:83], v2, s[64:67], s26 offen sc1\n" \
    "ds_read_b128 v[28:31], v3 offset:1024\nds_read_b128 v[32:35], v3 offset:8192\nds_read_b128 v[36:39], v3 offset:9216\n" \
    "buffer_load_dwordx4 v[84:87], v2, s[64:67], s27 offen sc1\nds_read_b128 v[40:43], v3 offset:16384\nds_read_b128 v[44:47], v3 offset:17408\n" \
    "ds_read_b128 v[48:51], v3 offset:2048\nds_read_b128 v[52:55], v3 offset:3072\nds_read_b128 v[56:59], v3 offset:4096\nds_read_b128 v[60:63], v3 offset:5120\n" \
    "ds_read_b128 v[64:67], v3 offset:6144\nds_read_b128 v[68:71], v3 offset:7168\n.Lv19_%=:\nv_mov_b64 v[16:17], 0\nv_mov_b64 v[18:19], 0\nv_mov_b64 v[20:21], 0\n" \
    "v_mov_b64 v[22:23], 0\ns_waitcnt vmcnt(3) lgkmcnt(10)\ns_nop 0\nv_smfmac_f32_16x16x64_bf16 v[20:23], v[72:75], v[24:31], v0\nds_write_b128 v1, v[72:75] offset:24576\n" \
    "v_max3_u32 v252, v72, v73, v74\nv_smfmac_f32_16x16x64_bf16 v[16:19], v[72:75], a[0:7], v0\ns_waitcnt vmcnt(2)\nds_write_b128 v1, v[76:79] offset:28672\n" \
    "s_waitcnt lgkmcnt(10)\nv_max3_u32 v252, v252, v75, v76\nv_max3_u32 v252, v252, v77, v78\nv_cmp_u_f32 vcc, v252, v79\n" \
    "v_smfmac_f32_16x16x64_bf16 v[20:23], v[76:79], v[32:39], v0\ns_cbranch_vccnz .Lv26_%=\n.Lv22_%=:\nv_smfmac_f32_16x16x64_bf16 v[16:19], v[76:79], a[32:39], v0\n" \
    "ds_read_b128 v[24:27], v3 offset:10240\nds_read_b128 v[28:31], v3 offset:11264\ns_waitcnt vmcnt(1)\nv_max3_u32 v252, v80, v81, v82\nv_cmp_u_f32 vcc, v252, v83\ns_nop 0\n" \
    "s_cbranch_vccnz .Lv27_%=\n.Lv23_%=:\ns_waitcnt lgkmcnt(10)\nv_smfmac_f32_16x16x64_bf16 v[20:23], v[80:83], v[40:47], v0\nds_write_b128 v1, v[80:83] offset:32768\n" \
    "ds_read_b128 v[36:39], v3 offset:13312\ns_waitcnt lgkmcnt(4)\ns_barrier\nds_read_b128 v[88:91], v1 offset:25600\nds_read_b128 v[40:43], v1 offset:26624\n" \
    "v_smfmac_f32_16x16x64_bf16 v[16:19], v[80:83], a[64:71], v0\nds_read_b128 v[32:35], v3 offset:12288\nds_read_b128 v[44:47], v1 offset:27648\ns_waitcnt vmcnt(0)\n" \
    "v_max3_u32 v252, v84, v85, v86\nv_cmp_u_f32 vcc, v252, v87\ns_nop 0\ns_cbranch_vccnz .Lv28_%=\n.Lv24_%=:\nv_smfmac_f32_16x16x64_bf16 v[20:23], v[84:87], a[104:111], v0\n" \
    "ds_write_b128 v1, v[84:87] offset:36864\nv_smfmac_f32_16x16x64_bf16 v[16:19], v[84:87], a[96:103], v0\nds_read_b128 v[72:75], v1 offset:29696\ns_waitcnt lgkmcnt(5)\n" \
    "v_smfmac_f32_16x16x64_bf16 v[20:23], v[88:91], v[48:55], v0\nds_read_b128 v[48:51], v1 offset:30720\nv_smfmac_f32_16x16x64_bf16 v[16:19], v[88:91], a[8:15], v0\n" \
    "ds_read_b128 v[52:55], v1 offset:31744\ns_waitcnt lgkmcnt(6)\nv_smfmac_f32_16x16x64_bf16 v[20:23], v[40:43], v[56:63], v0\nds_read_b128 v[56:59], v3 offset:14336\n" \
    "s_waitcnt lgkmcnt(4)\ns_barrier\nv_smfmac_f32_16x16x64_bf16 v[16:19], v[40:43], a[16:23], v0\nds_read_b128 v[60:63], v3 offset:15360\n" \
    "v_smfmac_f32_16x16x64_bf16 v[20:23], v[44:47], v[64:71], v0\nds_read_b128 v[40:43], v1 offset:33792\nv_smfmac_f32_16x16x64_bf16 v[16:19], v[44:47], a[24:31], v0\n" \
    "ds_read_b128 v[64:67], v3 offset:18432\ns_waitcnt lgkmcnt(6)\nv_smfmac_f32_16x16x64_bf16 v[20:23], v[72:75], v[24:31], v0\nds_read_b128 v[68:71], v3 offset:19456\n" \
    "v_smfmac_f32_16x16x64_bf16 v[16:19], v[72:75], a[40:47], v0\nds_read_b128 v[44:47], v1 offset:34816\ns_waitcnt lgkmcnt(7)\n" \
    "v_smfmac_f32_16x16x64_bf16 v[20:23], v[48:51], v[32:39], v0\nds_read_b128 v[24:27], v3 offset:20480\nv_smfmac_f32_16x16x64_bf16 v[16:19], v[48:51], a[48:55], v0\n" \
    "ds_read_b128 v[28:31], v3 offset:21504\ns_waitcnt lgkmcnt(6)\nv_smfmac_f32_16x16x64_bf16 v[20:23], v[52:55], v[56:63], v0\nds_read_b128 v[48:51], v1 offset:35840\n" \
    "v_smfmac_f32_16x16x64_bf16 v[16:19], v[52:55], a[56:63], v0\nds_read_b128 v[32:35], v3 offset:22528\ns_waitcnt lgkmcnt(5)\n" \
    "v_smfmac_f32_16x16x64_bf16 v[20:23], v[40:43], v[64:71], v0\nds_read_b128 v[36:39], v3 offset:23552\nv_smfmac_f32_16x16x64_bf16 v[16:19], v[40:43], a[72:79], v0\n" \
    "ds_read_b128 v[40:43], v1 offset:37888\ns_waitcnt lgkmcnt(4)\nv_smfmac_f32_16x16x64_bf16 v[20:23], v[44:47], v[24:31], v0\nds_read_b128 v[24:27], v1 offset:38912\n" \
    "v_smfmac_f32_16x16x64_bf16 v[16:19], v[44:47], a[80:87], v0\nds_read_b128 v[28:31], v1 offset:39936\ns_waitcnt lgkmcnt(3)\n" \
    "v_smfmac_f32_16x16x64_bf16 v[20:23], v[48:51], v[32:39], v0\ns_waitcnt lgkmcnt(2)\nv_smfmac_f32_16x16x64_bf16 v[20:23], v[40:43], a[120:127], v0\ns_waitcnt lgkmcnt(1)\n" \
    "v_smfmac_f32_16x16x64_bf16 v[20:23], v[24:27], a[136:143], v0\ns_waitcnt lgkmcnt(0)\nv_smfmac_f32_16x16x64_bf16 v[20:23], v[28:31], a[152:159], v0\ns_nop 2\n" \
    "v_smfmac_f32_16x16x64_bf16 v[16:19], v[48:51], a[88:95], v0\ns_nop 2\nv_smfmac_f32_16x16x64_bf16 v[16:19], v[40:43], a[112:119], v0\nv_permlane32_swap_b32 v20, v22\n" \
    "v_smfmac_f32_16x16x64_bf16 v[16:19], v[24:27], a[128:135], v0\nv_permlane32_swap_b32 v21, v23\nv_smfmac_f32_16x16x64_bf16 v[16:19], v[28:31], a[144:151], v0\n" \
    "v_add_f32 v20, v20, v22\nv_add_f32 v21, v21, v23\nv_cvt_pk_bf16_f32 v20, v20, v21\nds_write_b16 v4, v20 offset:8\nds_write_b16_d16_hi v4, v20 offset:24\n" \
    "v_mov_b32 v15, v15\nv_mov_b32 v15, v15\nv_mov_b32 v15, v15\nv_permlane32_swap_b32 v16, v18\nv_permlane32_swap_b32 v17, v19\nv_pk_add_f32 v[16:17], v[16:17], v[18:19]\n" \
    "v_cvt_pk_bf16_f32 v16, v16, v17\nds_write_b16 v4, v16 offset:0\nds_write_b16_d16_hi v4, v16 offset:16\ns_branch .Lv7_%=\n.Lv9_%=:\n.Lv31_%=:\ns_memrealtime s[72:73]\n" \
    "s_waitcnt lgkmcnt(0)\ns_mov_b32 s68, 0\ns_sub_u32 s76, s80, s45\ns_sub_u32 s69, s72, s76\ns_cmp_lt_i32 s69, -8\ns_cbranch_scc1 .Lv34_%=\ns_xor_b32 s24, s81, 0\n" \
    "s_xor_b32 s25, s81, 1024\ns_xor_b32 s26, s81, 2048\ns_xor_b32 s27, s81, 3072\n.Lv36_%=:\ns_memrealtime s[72:73]\ns_waitcnt lgkmcnt(0)\ns_sub_u32 s72, s72, s80\n" \
    "s_cmp_lt_i32 s72, 0\ns_cbranch_scc1 .Lv36_%=\nbuffer_load_dwordx4 v[72:75], v2, s[64:67], s24 offen sc1\nv_cvt_pk_bf16_f32 v36, v20, v21\nv_cvt_pk_bf16_f32 v37, v22, v23\n" \
    "v_cvt_pk_bf16_f32 v38, v28, v29\nbuffer_load_dwordx4 v[76:79], v2, s[64:67], s25 offen sc1\nv_cvt_pk_bf16_f32 v39, v30, v31\nds_write_b128 v14, v[36:39] offset:4096\n" \
    "ds_read_b128 v[24:27], v3 offset:2048\nbuffer_load_dwordx4 v[80:83], v2, s[64:67], s26 offen sc1\nds_read_b128 v[28:31], v3 offset:3072\n" \
    "ds_read_b128 v[32:35], v3 offset:10240\nds_read_b128 v[36:39], v3 offset:11264\nbuffer_load_dwordx4 v[84:87], v2, s[64:67], s27 offen sc1\n" \
    "ds_read_b128 v[40:43], v3 offset:18432\nds_read_b128 v[44:47], v3 offset:19456\nds_read_b128 v[48:51], v3 offset:0\nds_read_b128 v[52:55], v3 offset:1024\n" \
    "ds_read_b128 v[56:59], v3 offset:4096\nds_read_b128 v[60:63], v3 offset:5120\nds_read_b128 v[64:67], v3 offset:6144\nds_read_b128 v[68:71], v3 offset:7168\n.Lv35_%=:\n" \
    "v_mov_b64 v[16:17], 0\nv_mov_b64 v[18:19], 0\nv_mov_b64 v[20:21], 0\nv_mov_b64 v[22:23], 0\ns_waitcnt vmcnt(3) lgkmcnt(10)\ns_nop 0\n" \
    "v_smfmac_f32_16x16x64_bf16 v[20:23], v[72:75], v[24:31], v0\nds_write_b128 v1, v[72:75] offset:25600\nv_max3_u32 v252, v72, v73, v74\n" \
    "v_smfmac_f32_16x16x64_bf16 v[16:19], v[72:75], a[8:15], v0\ns_waitcnt vmcnt(2)\nds_write_b128 v1, v[76:79] offset:29696\ns_waitcnt lgkmcnt(10)\n" \
    "v_max3_u32 v252, v252, v75, v76\nv_max3_u32 v252, v252, v77, v78\nv_cmp_u_f32 vcc, v252, v79\nv_smfmac_f32_16x16x64_bf16 v[20:23], v[76:79], v[32:39], v0\n" \
    "s_cbranch_vccnz .Lv42_%=\n.Lv38_%=:\nv_smfmac_f32_16x16x64_bf16 v[16:19], v[76:79], a[40:47], v0\nds_read_b128 v[24:27], v3 offset:8192\n" \
    "ds_read_b128 v[28:31], v3 offset:9216\ns_waitcnt vmcnt(1)\nv_max3_u32 v252, v80, v81, v82\nv_cmp_u_f32 vcc, v252, v83\ns_nop 0\ns_cbranch_vccnz .Lv43_%=\n.Lv39_%=:\n" \
    "s_waitcnt lgkmcnt(10)\nv_smfmac_f32_16x16x64_bf16 v[20:23], v[80:83], v[40:47], v0\nds_write_b128 v1, v[80:83] offset:33792\nds_read_b128 v[36:39], v3 offset:13312\n" \
    "s_waitcnt lgkmcnt(4)\ns_barrier\nds_read_b128 v[88:91], v1 offset:24576\nds_read_b128 v[40:43], v1 offset:26624\n" \
    "v_smfmac_f32_16x16x64_bf16 v[16:19], v[80:83], a[72:79], v0\nds_read_b128 v[32:35], v3 offset:12288\nds_read_b128 v[44:47], v1 offset:27648\ns_waitcnt vmcnt(0)\n" \
    "v_max3_u32 v252, v84, v85, v86\nv_cmp_u_f32 vcc, v252, v87\ns_nop 0\ns_cbranch_vccnz .Lv44_%=\n.Lv40_%=:\nv_smfmac_f32_16x16x64_bf16 v[20:23], v[84:87], a[120:127], v0\n" \
    "ds_write_b128 v1, v[84:87] offset:37888\nv_smfmac_f32_16x16x64_bf16 v[16:19], v[84:87], a[112:119], v0\nds_read_b128 v[72:75], v1 offset:28672\ns_waitcnt lgkmcnt(5)\n" \
    "v_smfmac_f32_16x16x64_bf16 v[20:23], v[88:91], v[48:55], v0\nds_read_b128 v[48:51], v1 offset:30720\nv_smfmac_f32_16x16x64_bf16 v[16:19], v[88:91], a[0:7], v0\n" \
    "ds_read_b128 v[52:55], v1 offset:31744\ns_waitcnt lgkmcnt(6)\nv_smfmac_f32_16x16x64_bf16 v[20:23], v[40:43], v[56:63], v0\nds_read_b128 v[56:59], v3 offset:14336\n" \
    "s_waitcnt lgkmcnt(4)\ns_barrier\nv_smfmac_f32_16x16x64_bf16 v[16:19], v[40:43], a[16:23], v0\nds_read_b128 v[60:63], v3 offset:15360\n" \
    "v_smfmac_f32_16x16x64_bf16 v[20:23], v[44:47], v[64:71], v0\nds_read_b128 v[40:43], v1 offset:32768\nv_smfmac_f32_16x16x64_bf16 v[16:19], v[44:47], a[24:31], v0\n" \
    "ds_read_b128 v[64:67], v3 offset:16384\ns_waitcnt lgkmcnt(6)\nv_smfmac_f32_16x16x64_bf16 v[20:23], v[72:75], v[24:31], v0\nds_read_b128 v[68:71], v3 offset:17408\n" \
    "v_smfmac_f32_16x16x64_bf16 v[16:19], v[72:75], a[32:39], v0\nds_read_b128 v[44:47], v1 offset:34816\ns_waitcnt lgkmcnt(7)\n" \
    "v_smfmac_f32_16x16x64_bf16 v[20:23], v[48:51], v[32:39], v0\nds_read_b128 v[24:27], v3 offset:20480\nv_smfmac_f32_16x16x64_bf16 v[16:19], v[48:51], a[48:55], v0\n" \
    "ds_read_b128 v[28:31], v3 offset:21504\ns_waitcnt lgkmcnt(6)\nv_smfmac_f32_16x16x64_bf16 v[20:23], v[52:55], v[56:63], v0\nds_read_b128 v[48:51], v1 offset:35840\n" \
    "v_smfmac_f32_16x16x64_bf16 v[16:19], v[52:55], a[56:63], v0\nds_read_b128 v[32:35], v3 offset:22528\ns_waitcnt lgkmcnt(5)\n" \
    "v_smfmac_f32_16x16x64_bf16 v[20:23], v[40:43], v[64:71], v0\nds_read_b128 v[36:39], v3 offset:23552\nv_smfmac_f32_16x16x64_bf16 v[16:19], v[40:43], a[64:71], v0\n" \
    "ds_read_b128 v[40:43], v1 offset:36864\ns_waitcnt lgkmcnt(4)\nv_smfmac_f32_16x16x64_bf16 v[20:23], v[44:47], v[24:31], v0\nds_read_b128 v[24:27], v1 offset:38912\n" \
    "v_smfmac_f32_16x16x64_bf16 v[16:19], v[44:47], a[80:87], v0\nds_read_b128 v[28:31], v1 offset:39936\ns_waitcnt lgkmcnt(3)\n" \
    "v_smfmac_f32_16x16x64_bf16 v[20:23], v[48:51], v[32:39], v0\ns_waitcnt lgkmcnt(2)\nv_smfmac_f32_16x16x64_bf16 v[20:23], v[40:43], a[104:111], v0\ns_waitcnt lgkmcnt(1)\n" \
    "v_smfmac_f32_16x16x64_bf16 v[20:23], v[24:27], a[136:143], v0\ns_waitcnt lgkmcnt(0)\nv_smfmac_f32_16x16x64_bf16 v[20:23], v[28:31], a[152:159], v0\ns_nop 2\n" \
    "v_smfmac_f32_16x16x64_bf16 v[16:19], v[48:51], a[88:95], v0\ns_nop 2\nv_smfmac_f32_16x16x64_bf16 v[16:19], v[40:43], a[96:103], v0\nv_permlane32_swap_b32 v20, v22\n" \
    "v_smfmac_f32_16x16x64_bf16 v[16:19], v[24:27], a[128:135], v0\nv_permlane32_swap_b32 v21, v23\nv_smfmac_f32_16x16x64_bf16 v[16:19], v[28:31], a[144:151], v0\n" \
    "v_add_f32 v20, v20, v22\nv_add_f32 v21, v21, v23\nv_cvt_pk_bf16_f32 v20, v20, v21\nds_write_b16 v4, v20 offset:8\nds_write_b16_d16_hi v4, v20 offset:24\n" \
    "v_mov_b32 v15, v15\nv_mov_b32 v15, v15\nv_mov_b32 v15, v15\nv_permlane32_swap_b32 v16, v18\nv_permlane32_swap_b32 v17, v19\nv_pk_add_f32 v[16:17], v[16:17], v[18:19]\n" \
    "v_cvt_pk_bf16_f32 v16, v16, v17\nds_write_b16 v4, v16 offset:0\nds_write_b16_d16_hi v4, v16 offset:16\ns_branch .Lv7_%=\n.Lv10_%=:\n.Lv47_%=:\ns_memrealtime s[72:73]\n" \
    "s_waitcnt lgkmcnt(0)\ns_mov_b32 s68, 0\ns_sub_u32 s76, s80, s45\ns_sub_u32 s69, s72, s76\ns_cmp_lt_i32 s69, -8\ns_cbranch_scc1 .Lv50_%=\ns_xor_b32 s24, s81, 0\n" \
    "s_xor_b32 s25, s81, 1024\ns_xor_b32 s26, s81, 2048\ns_xor_b32 s27, s81, 3072\n.Lv52_%=:\ns_memrealtime s[72:73]\ns_waitcnt lgkmcnt(0)\ns_sub_u32 s72, s72, s80\n" \
    "s_cmp_lt_i32 s72, 0\ns_cbranch_scc1 .Lv52_%=\nbuffer_load_dwordx4 v[72:75], v2, s[64:67], s24 offen sc1\nv_cvt_pk_bf16_f32 v36, v20, v21\nv_cvt_pk_bf16_f32 v37, v22, v23\n" \
    "v_cvt_pk_bf16_f32 v38, v28, v29\nbuffer_load_dwordx4 v[76:79], v2, s[64:67], s25 offen sc1\nv_cvt_pk_bf16_f32 v39, v30, v31\nds_write_b128 v14, v[36:39] offset:4096\n" \
    "ds_read_b128 v[24:27], v3 offset:4096\nbuffer_load_dwordx4 v[80:83], v2, s[64:67], s26 offen sc1\nds_read_b128 v[28:31], v3 offset:5120\n" \
    "ds_read_b128 v[32:35], v3 offset:12288\nds_read_b128 v[36:39], v3 offset:13312\nbuffer_load_dwordx4 v[84:87], v2, s[64:67], s27 offen sc1\n" \
    "ds_read_b128 v[40:43], v3 offset:20480\nds_read_b128 v[44:47], v3 offset:21504\nds_read_b128 v[48:51], v3 offset:0\nds_read_b128 v[52:55], v3 offset:1024\n" \
    "ds_read_b128 v[56:59], v3 offset:2048\nds_read_b128 v[60:63], v3 offset:3072\nds_read_b128 v[64:67], v3 offset:6144\nds_read_b128 v[68:71], v3 offset:7168\n.Lv51_%=:\n" \
    "v_mov_b64 v[16:17], 0\nv_mov_b64 v[18:19], 0\nv_mov_b64 v[20:21], 0\nv_mov_b64 v[22:23], 0\ns_waitcnt vmcnt(3) lgkmcnt(10)\ns_nop 0\n" \
    "v_smfmac_f32_16x16x64_bf16 v[20:23], v[72:75], v[24:31], v0\nds_write_b128 v1, v[72:75] offset:26624\nv_max3_u32 v252, v72, v73, v74\n" \
    "v_smfmac_f32_16x16x64_bf16 v[16:19], v[72:75], a[16:23], v0\ns_waitcnt vmcnt(2)\nds_write_b128 v1, v[76:79] offset:30720\ns_waitcnt lgkmcnt(10)\n" \
    "v_max3_u32 v252, v252, v75, v76\nv_max3_u32 v252, v252, v77, v78\nv_cmp_u_f32 vcc, v252, v79\nv_smfmac_f32_16x16x64_bf16 v[20:23], v[76:79], v[32:39], v0\n" \
    "s_cbranch_vccnz .Lv58_%=\n.Lv54_%=:\nv_smfmac_f32_16x16x64_bf16 v[16:19], v[76:79], a[48:55], v0\nds_read_b128 v[24:27], v3 offset:8192\n" \
    "ds_read_b128 v[28:31], v3 offset:9216\ns_waitcnt vmcnt(1)\nv_max3_u32 v252, v80, v81, v82\nv_cmp_u_f32 vcc, v252, v83\ns_nop 0\ns_cbranch_vccnz .Lv59_%=\n.Lv55_%=:\n" \
    "s_waitcnt lgkmcnt(10)\nv_smfmac_f32_16x16x64_bf16 v[20:23], v[80:83], v[40:47], v0\nds_write_b128 v1, v[80:83] offset:34816\nds_read_b128 v[36:39], v3 offset:11264\n" \
    "s_waitcnt lgkmcnt(4)\ns_barrier\nds_read_b128 v[88:91], v1 offset:24576\nds_read_b128 v[40:43], v1 offset:25600\n" \
    "v_smfmac_f32_16x16x64_bf16 v[16:19], v[80:83], a[80:87], v0\nds_read_b128 v[32:35], v3 offset:10240\nds_read_b128 v[44:47], v1 offset:27648\ns_waitcnt vmcnt(0)\n" \
    "v_max3_u32 v252, v84, v85, v86\nv_cmp_u_f32 vcc, v252, v87\ns_nop 0\ns_cbranch_vccnz .Lv60_%=\n.Lv56_%=:\nv_smfmac_f32_16x16x64_bf16 v[20:23], v[84:87], a[136:143], v0\n" \
    "ds_write_b128 v1, v[84:87] offset:38912\nv_smfmac_f32_16x16x64_bf16 v[16:19], v[84:87], a[128:135], v0\nds_read_b128 v[72:75], v1 offset:28672\ns_waitcnt lgkmcnt(5)\n" \
    "v_smfmac_f32_16x16x64_bf16 v[20:23], v[88:91], v[48:55], v0\nds_read_b128 v[48:51], v1 offset:29696\nv_smfmac_f32_16x16x64_bf16 v[16:19], v[88:91], a[0:7], v0\n" \
    "ds_read_b128 v[52:55], v1 offset:31744\ns_waitcnt lgkmcnt(6)\nv_smfmac_f32_16x16x64_bf16 v[20:23], v[40:43], v[56:63], v0\nds_read_b128 v[56:59], v3 offset:14336\n" \
    "s_waitcnt lgkmcnt(4)\ns_barrier\nv_smfmac_f32_16x16x64_bf16 v[16:19], v[40:43], a[8:15], v0\nds_read_b128 v[60:63], v3 offset:15360\n" \
    "v_smfmac_f32_16x16x64_bf16 v[20:23], v[44:47], v[64:71], v0\nds_read_b128 v[40:43], v1 offset:32768\nv_smfmac_f32_16x16x64_bf16 v[16:19], v[44:47], a[24:31], v0\n" \
    "ds_read_b128 v[64:67], v3 offset:16384\ns_waitcnt lgkmcnt(6)\nv_smfmac_f32_16x16x64_bf16 v[20:23], v[72:75], v[24:31], v0\nds_read_b128 v[68:71], v3 offset:17408\n" \
    "v_smfmac_f32_16x16x64_bf16 v[16:19], v[72:75], a[32:39], v0\nds_read_b128 v[44:47], v1 offset:33792\ns_waitcnt lgkmcnt(7)\n" \
    "v_smfmac_f32_16x16x64_bf16 v[20:23], v[48:51], v[32:39], v0\nds_read_b128 v[24:27], v3 offset:18432\nv_smfmac_f32_16x16x64_bf16 v[16:19], v[48:51], a[40:47], v0\n" \
    "ds_read_b128 v[28:31], v3 offset:19456\ns_waitcnt lgkmcnt(6)\nv_smfmac_f32_16x16x64_bf16 v[20:23], v[52:55], v[56:63], v0\nds_read_b128 v[48:51], v1 offset:35840\n" \
    "v_smfmac_f32_16x16x64_bf16 v[16:19], v[52:55], a[56:63], v0\nds_read_b128 v[32:35], v3 offset:22528\ns_waitcnt lgkmcnt(5)\n" \
    "v_smfmac_f32_16x16x64_bf16 v[20:23], v[40:43], v[64:71], v0\nds_read_b128 v[36:39], v3 offset:23552\nv_smfmac_f32_16x16x64_bf16 v[16:19], v[40:43], a[64:71], v0\n" \
    "ds_read_b128 v[40:43], v1 offset:36864\ns_waitcnt lgkmcnt(4)\nv_smfmac_f32_16x16x64_bf16 v[20:23], v[44:47], v[24:31], v0\nds_read_b128 v[24:27], v1 offset:37888\n" \
    "v_smfmac_f32_16x16x64_bf16 v[16:19], v[44:47], a[72:79], v0\nds_read_b128 v[28:31], v1 offset:39936\ns_waitcnt lgkmcnt(3)\n" \
    "v_smfmac_f32_16x16x64_bf16 v[20:23], v[48:51], v[32:39], v0\ns_waitcnt lgkmcnt(2)\nv_smfmac_f32_16x16x64_bf16 v[20:23], v[40:43], a[104:111], v0\ns_waitcnt lgkmcnt(1)\n" \
    "v_smfmac_f32_16x16x64_bf16 v[20:23], v[24:27], a[120:127], v0\ns_waitcnt lgkmcnt(0)\nv_smfmac_f32_16x16x64_bf16 v[20:23], v[28:31], a[152:159], v0\ns_nop 2\n" \
    "v_smfmac_f32_16x16x64_bf16 v[16:19], v[48:51], a[88:95], v0\ns_nop 2\nv_smfmac_f32_16x16x64_bf16 v[16:19], v[40:43], a[96:103], v0\nv_permlane32_swap_b32 v20, v22\n" \
    "v_smfmac_f32_16x16x64_bf16 v[16:19], v[24:27], a[112:119], v0\nv_permlane32_swap_b32 v21, v23\nv_smfmac_f32_16x16x64_bf16 v[16:19], v[28:31], a[144:151], v0\n" \
    "v_add_f32 v20, v20, v22\nv_add_f32 v21, v21, v23\nv_cvt_pk_bf16_f32 v20, v20, v21\nds_write_b16 v4, v20 offset:8\nds_write_b16_d16_hi v4, v20 offset:24\n" \
    "v_mov_b32 v15, v15\nv_mov_b32 v15, v15\nv_mov_b32 v15, v15\nv_permlane32_swap_b32 v16, v18\nv_permlane32_swap_b32 v17, v19\nv_pk_add_f32 v[16:17], v[16:17], v[18:19]\n" \
    "v_cvt_pk_bf16_f32 v16, v16, v17\nds_write_b16 v4, v16 offset:0\nds_write_b16_d16_hi v4, v16 offset:16\ns_branch .Lv7_%=\n.Lv11_%=:\n.Lv63_%=:\ns_memrealtime s[72:73]\n" \
    "s_waitcnt lgkmcnt(0)\ns_mov_b32 s68, 0\ns_sub_u32 s76, s80, s45\ns_sub_u32 s69, s72, s76\ns_cmp_lt_i32 s69, -8\ns_cbranch_scc1 .Lv66_%=\ns_xor_b32 s24, s81, 0\n" \
    "s_xor_b32 s25, s81, 1024\ns_xor_b32 s26, s81, 2048\ns_xor_b32 s27, s81, 3072\n.Lv68_%=:\ns_memrealtime s[72:73]\ns_waitcnt lgkmcnt(0)\ns_sub_u32 s72, s72, s80\n" \
    "s_cmp_lt_i32 s72, 0\ns_cbranch_scc1 .Lv68_%=\nbuffer_load_dwordx4 v[72:75], v2, s[64:67], s24 offen sc1\nv_cvt_pk_bf16_f32 v36, v20, v21\nv_cvt_pk_bf16_f32 v37, v22, v23\n" \
    "v_cvt_pk_bf16_f32 v38, v28, v29\nbuffer_load_dwordx4 v[76:79], v2, s[64:67], s25 offen sc1\nv_cvt_pk_bf16_f32 v39, v30, v31\nds_write_b128 v14, v[36:39] offset:4096\n" \
    "ds_read_b128 v[24:27], v3 offset:6144\nbuffer_load_dwordx4 v[80:83], v2, s[64:67], s26 offen sc1\nds_read_b128 v[28:31], v3 offset:7168\n" \
    "ds_read_b128 v[32:35], v3 offset:14336\nds_read_b128 v[36:39], v3 offset:15360\nbuffer_load_dwordx4 v[84:87], v2, s[64:67], s27 offen sc1\n" \
    "ds_read_b128 v[40:43], v3 offset:22528\nds_read_b128 v[44:47], v3 offset:23552\nds_read_b128 v[48:51], v3 offset:0\nds_read_b128 v[52:55], v3 offset:1024\n" \
    "ds_read_b128 v[56:59], v3 offset:2048\nds_read_b128 v[60:63], v3 offset:3072\nds_read_b128 v[64:67], v3 offset:4096\nds_read_b128 v[68:71], v3 offset:5120\n.Lv67_%=:\n" \
    "v_mov_b64 v[16:17], 0\nv_mov_b64 v[18:19], 0\nv_mov_b64 v[20:21], 0\nv_mov_b64 v[22:23], 0\ns_waitcnt vmcnt(3) lgkmcnt(10)\ns_nop 0\n" \
    "v_smfmac_f32_16x16x64_bf16 v[20:23], v[72:75], v[24:31], v0\nds_write_b128 v1, v[72:75] offset:27648\nv_max3_u32 v252, v72, v73, v74\n" \
    "v_smfmac_f32_16x16x64_bf16 v[16:19], v[72:75], a[24:31], v0\ns_waitcnt vmcnt(2)\nds_write_b128 v1, v[76:79] offset:31744\ns_waitcnt lgkmcnt(10)\n" \
    "v_max3_u32 v252, v252, v75, v76\nv_max3_u32 v252, v252, v77, v78\nv_cmp_u_f32 vcc, v252, v79\nv_smfmac_f32_16x16x64_bf16 v[20:23], v[76:79], v[32:39], v0\n" \
    "s_cbranch_vccnz .Lv74_%=\n.Lv70_%=:\nv_smfmac_f32_16x16x64_bf16 v[16:19], v[76:79], a[56:63], v0\nds_read_b128 v[24:27], v3 offset:8192\n" \
    "ds_read_b128 v[28:31], v3 offset:9216\ns_waitcnt vmcnt(1)\nv_max3_u32 v252, v80, v81, v82\nv_cmp_u_f32 vcc, v252, v83\ns_nop 0\ns_cbranch_vccnz .Lv75_%=\n.Lv71_%=:\n" \
    "s_waitcnt lgkmcnt(10)\nv_smfmac_f32_16x16x64_bf16 v[20:23], v[80:83], v[40:47], v0\nds_write_b128 v1, v[80:83] offset:35840\nds_read_b128 v[36:39], v3 offset:11264\n" \
    "s_waitcnt lgkmcnt(4)\ns_barrier\nds_read_b128 v[88:91], v1 offset:24576\nds_read_b128 v[40:43], v1 offset:25600\n" \
    "v_smfmac_f32_16x16x64_bf16 v[16:19], v[80:83], a[88:95], v0\nds_read_b128 v[32:35], v3 offset:10240\nds_read_b128 v[44:47], v1 offset:26624\ns_waitcnt vmcnt(0)\n" \
    "v_max3_u32 v252, v84, v85, v86\nv_cmp_u_f32 vcc, v252, v87\ns_nop 0\ns_cbranch_vccnz .Lv76_%=\n.Lv72_%=:\nv_smfmac_f32_16x16x64_bf16 v[20:23], v[84:87], a[152:159], v0\n" \
    "ds_write_b128 v1, v[84:87] offset:39936\nv_smfmac_f32_16x16x64_bf16 v[16:19], v[84:87], a[144:151], v0\nds_read_b128 v[72:75], v1 offset:28672\ns_waitcnt lgkmcnt(5)\n" \
    "v_smfmac_f32_16x16x64_bf16 v[20:23], v[88:91], v[48:55], v0\nds_read_b128 v[48:51], v1 offset:29696\nv_smfmac_f32_16x16x64_bf16 v[16:19], v[88:91], a[0:7], v0\n" \
    "ds_read_b128 v[52:55], v1 offset:30720\ns_waitcnt lgkmcnt(6)\nv_smfmac_f32_16x16x64_bf16 v[20:23], v[40:43], v[56:63], v0\nds_read_b128 v[56:59], v3 offset:12288\n" \
    "s_waitcnt lgkmcnt(4)\ns_barrier\nv_smfmac_f32_16x16x64_bf16 v[16:19], v[40:43], a[8:15], v0\nds_read_b128 v[60:63], v3 offset:13312\n" \
    "v_smfmac_f32_16x16x64_bf16 v[20:23], v[44:47], v[64:71], v0\nds_read_b128 v[40:43], v1 offset:32768\nv_smfmac_f32_16x16x64_bf16 v[16:19], v[44:47], a[16:23], v0\n" \
    "ds_read_b128 v[64:67], v3 offset:16384\ns_waitcnt lgkmcnt(6)\nv_smfmac_f32_16x16x64_bf16 v[20:23], v[72:75], v[24:31], v0\nds_read_b128 v[68:71], v3 offset:17408\n" \
    "v_smfmac_f32_16x16x64_bf16 v[16:19], v[72:75], a[32:39], v0\nds_read_b128 v[44:47], v1 offset:33792\ns_waitcnt lgkmcnt(7)\n" \
    "v_smfmac_f32_16x16x64_bf16 v[20:23], v[48:51], v[32:39], v0\nds_read_b128 v[24:27], v3 offset:18432\nv_smfmac_f32_16x16x64_bf16 v[16:19], v[48:51], a[40:47], v0\n" \
    "ds_read_b128 v[28:31], v3 offset:19456\ns_waitcnt lgkmcnt(6)\nv_smfmac_f32_16x16x64_bf16 v[20:23], v[52:55], v[56:63], v0\nds_read_b128 v[48:51], v1 offset:34816\n" \
    "v_smfmac_f32_16x16x64_bf16 v[16:19], v[52:55], a[48:55], v0\nds_read_b128 v[32:35], v3 offset:20480\ns_waitcnt lgkmcnt(5)\n" \
    "v_smfmac_f32_16x16x64_bf16 v[20:23], v[40:43], v[64:71], v0\nds_read_b128 v[36:39], v3 offset:21504\nv_smfmac_f32_16x16x64_bf16 v[16:19], v[40:43], a[64:71], v0\n" \
    "ds_read_b128 v[40:43], v1 offset:36864\ns_waitcnt lgkmcnt(4)\nv_smfmac_f32_16x16x64_bf16 v[20:23], v[44:47], v[24:31], v0\nds_read_b128 v[24:27], v1 offset:37888\n" \
    "v_smfmac_f32_16x16x64_bf16 v[16:19], v[44:47], a[72:79], v0\nds_read_b128 v[28:31], v1 offset:38912\ns_waitcnt lgkmcnt(3)\n" \
    "v_smfmac_f32_16x16x64_bf16 v[20:23], v[48:51], v[32:39], v0\ns_waitcnt lgkmcnt(2)\nv_smfmac_f32_16x16x64_bf16 v[20:23], v[40:43], a[104:111], v0\ns_waitcnt lgkmcnt(1)\n" \
    "v_smfmac_f32_16x16x64_bf16 v[20:23], v[24:27], a[120:127], v0\ns_waitcnt lgkmcnt(0)\nv_smfmac_f32_16x16x64_bf16 v[20:23], v[28:31], a[136:143], v0\ns_nop 2\n" \
    "v_smfmac_f32_16x16x64_bf16 v[16:19], v[48:51], a[80:87], v0\ns_nop 2\nv_smfmac_f32_16x16x64_bf16 v[16:19], v[40:43], a[96:103], v0\nv_permlane32_swap_b32 v20, v22\n" \
    "v_smfmac_f32_16x16x64_bf16 v[16:19], v[24:27], a[112:119], v0\nv_permlane32_swap_b32 v21, v23\nv_smfmac_f32_16x16x64_bf16 v[16:19], v[28:31], a[128:135], v0\n" \
    "v_add_f32 v20, v20, v22\nv_add_f32 v21, v21, v23\nv_cvt_pk_bf16_f32 v20, v20, v21\nds_write_b16 v4, v20 offset:8\nds_write_b16_d16_hi v4, v20 offset:24\n" \
    "v_mov_b32 v15, v15\nv_mov_b32 v15, v15\nv_mov_b32 v15, v15\nv_permlane32_swap_b32 v16, v18\nv_permlane32_swap_b32 v17, v19\nv_pk_add_f32 v[16:17], v[16:17], v[18:19]\n" \
    "v_cvt_pk_bf16_f32 v16, v16, v17\nds_write_b16 v4, v16 offset:0\nds_write_b16_d16_hi v4, v16 offset:16\nv_mov_b32 v15, v15\nv_mov_b32 v15, v15\n.Lv7_%=:\n.Lv79_%=:\n" \
    "v_mov_b64 v[54:55], 0\nv_mov_b64 v[52:53], 0\nv_mov_b64 v[50:51], 0\nv_mov_b64 v[48:49], 0\nv_mov_b64 v[70:71], 0\nv_mov_b64 v[68:69], 0\nv_mov_b64 v[66:67], 0\n" \
    "v_mov_b64 v[64:65], 0\nds_read_b128 v[32:35], v5 offset:0\nds_read_b128 v[36:39], v5 offset:512\nds_read_b128 v[40:43], v5 offset:1024\n" \
    "ds_read_b128 v[44:47], v5 offset:1536\nv_mov_b64 v[30:31], 0\nv_mov_b64 v[28:29], 0\nv_mov_b64 v[26:27], 0\nv_mov_b64 v[24:25], 0\nv_mov_b64 v[22:23], 0\n" \
    "v_mov_b64 v[20:21], 0\nv_mov_b64 v[18:19], 0\nv_mov_b64 v[16:17], 0\ns_waitcnt lgkmcnt(3)\ns_nop 0\nv_smfmac_f32_16x16x64_bf16 v[16:19], v[32:35], a[160:167], v0\n" \
    "v_smfmac_f32_16x16x64_bf16 v[20:23], v[32:35], a[176:183], v0\ns_waitcnt lgkmcnt(2)\nv_smfmac_f32_16x16x64_bf16 v[16:19], v[36:39], a[168:175], v0\n" \
    "v_smfmac_f32_16x16x64_bf16 v[20:23], v[36:39], a[184:191], v0\ns_waitcnt lgkmcnt(1)\nv_smfmac_f32_16x16x64_bf16 v[16:19], v[40:43], a[192:199], v0\n" \
    "v_smfmac_f32_16x16x64_bf16 v[20:23], v[40:43], a[208:215], v0\ns_waitcnt lgkmcnt(0)\nv_smfmac_f32_16x16x64_bf16 v[16:19], v[44:47], a[200:207], v0\n" \
    "v_smfmac_f32_16x16x64_bf16 v[20:23], v[44:47], a[216:223], v0\ns_nop 2\nv_smfmac_f32_16x16x64_bf16 v[24:27], v[32:35], a[224:231], v0\ns_nop 2\n" \
    "v_cmp_u_f32 s[4:5], v16, v17\nv_smfmac_f32_16x16x64_bf16 v[28:31], v[32:35], a[240:247], v0\nv_cmp_u_f32 vcc, v18, v19\n" \
    "v_smfmac_f32_16x16x64_bf16 v[24:27], v[36:39], a[232:239], v0\nv_cvt_pk_bf16_f32 v72, v16, v17\nv_smfmac_f32_16x16x64_bf16 v[28:31], v[36:39], a[248:255], v0\n" \
    "v_cvt_pk_bf16_f32 v74, v18, v19\nv_smfmac_f32_16x16x64_bf16 v[24:27], v[40:43], v[92:99], v0\ns_or_b64 vcc, vcc, s[4:5]\ns_cbranch_scc1 .Lv80_%=\n.Lv81_%=:\n" \
    "v_cvt_pk_bf16_f32 v73, v20, v21\nv_smfmac_f32_16x16x64_bf16 v[28:31], v[40:43], v[108:115], v0\nv_cvt_pk_bf16_f32 v75, v22, v23\n" \
    "v_smfmac_f32_16x16x64_bf16 v[24:27], v[44:47], v[100:107], v0\nv_permlane16_swap_b32 v72, v73\nv_smfmac_f32_16x16x64_bf16 v[28:31], v[44:47], v[116:123], v0\n" \
    "v_permlane16_swap_b32 v74, v75\nv_smfmac_f32_16x16x64_bf16 v[64:67], v[32:35], v[124:131], v0\nbuffer_store_dwordx4 v[72:75], v7, s[64:67], s0 offen offset:0 sc1\ns_nop 1\n" \
    "v_smfmac_f32_16x16x64_bf16 v[68:71], v[32:35], v[140:147], v0\nv_cvt_pk_bf16_f32 v76, v24, v25\nv_smfmac_f32_16x16x64_bf16 v[64:67], v[36:39], v[132:139], v0\n" \
    "v_cvt_pk_bf16_f32 v78, v26, v27\nv_smfmac_f32_16x16x64_bf16 v[68:71], v[36:39], v[148:155], v0\nv_cvt_pk_bf16_f32 v77, v28, v29\n" \
    "v_smfmac_f32_16x16x64_bf16 v[64:67], v[40:43], v[156:163], v0\nv_cvt_pk_bf16_f32 v79, v30, v31\nv_smfmac_f32_16x16x64_bf16 v[68:71], v[40:43], v[172:179], v0\n" \
    "v_permlane16_swap_b32 v76, v77\nv_smfmac_f32_16x16x64_bf16 v[64:67], v[44:47], v[164:171], v0\nv_permlane16_swap_b32 v78, v79\n" \
    "v_smfmac_f32_16x16x64_bf16 v[68:71], v[44:47], v[180:187], v0\nbuffer_store_dwordx4 v[76:79], v7, s[64:67], s1 offen offset:0 sc1\ns_nop 1\n" \
    "v_smfmac_f32_16x16x64_bf16 v[48:51], v[32:35], v[188:195], v0\ns_nop 1\nv_cvt_pk_bf16_f32 v72, v64, v65\nv_smfmac_f32_16x16x64_bf16 v[52:55], v[32:35], v[204:211], v0\n" \
    "v_cvt_pk_bf16_f32 v74, v66, v67\nv_smfmac_f32_16x16x64_bf16 v[48:51], v[36:39], v[196:203], v0\nv_cvt_pk_bf16_f32 v73, v68, v69\n" \
    "v_smfmac_f32_16x16x64_bf16 v[52:55], v[36:39], v[212:219], v0\nv_cvt_pk_bf16_f32 v75, v70, v71\nv_smfmac_f32_16x16x64_bf16 v[48:51], v[40:43], v[220:227], v0\n" \
    "v_permlane16_swap_b32 v72, v73\nv_smfmac_f32_16x16x64_bf16 v[52:55], v[40:43], v[236:243], v0\nv_permlane16_swap_b32 v74, v75\n" \
    "v_smfmac_f32_16x16x64_bf16 v[48:51], v[44:47], v[228:235], v0\nbuffer_store_dwordx4 v[72:75], v7, s[64:67], s2 offen offset:0 sc1\n" \
    "v_smfmac_f32_16x16x64_bf16 v[52:55], v[44:47], v[244:251], v0\ns_nop 5\nv_cvt_pk_bf16_f32 v76, v48, v49\nv_cvt_pk_bf16_f32 v78, v50, v51\nv_cvt_pk_bf16_f32 v77, v52, v53\n" \
    "v_cvt_pk_bf16_f32 v79, v54, v55\ns_nop 0\nv_permlane16_swap_b32 v76, v77\nv_permlane16_swap_b32 v78, v79\n" \
    "buffer_store_dwordx4 v[76:79], v7, s[64:67], s3 offen offset:0 sc1\ns_barrier\ns_memrealtime s[72:73]\nv_mov_b32 v15, s50\ns_mov_b64 exec, 1\n" \
    "buffer_store_dword v15, off, s[64:67], s48 sc1\ns_mov_b64 exec, -1\ns_xor_b32 s48, s48, 0x8000\ns_add_u32 s35, s35, 2\ns_waitcnt lgkmcnt(0)\ns_cmp_lt_u32 s35, 62\n" \
    "s_cbranch_scc1 .Lv5_%=\ns_cmp_lt_u32 s35, 64\ns_cbranch_scc0 .Lv6_%=\nv_mov_b64 v[252:253], -1\ns_and_b32 s69, s35, 15\ns_lshl_b32 s69, s69, 19\ns_add_u32 s0, s38, s69\n" \
    "s_lshr_b32 s69, s0, 8\ns_and_b32 s69, s69, 0x2000\ns_xor_b32 s0, s0, s69\ns_xor_b32 s1, s0, 0x10000\ns_xor_b32 s2, s0, 0x20000\ns_xor_b32 s3, s0, 0x30000\n" \
    "buffer_load_dword v13, off, s[64:67], s42 sc1\ns_and_b32 s69, s35, 15\ns_lshl_b32 s69, s69, 17\ns_add_u32 s81, s37, s69\ns_lshr_b32 s69, s81, 8\n" \
    "s_and_b32 s69, s69, 0x2000\ns_xor_b32 s81, s81, s69\ns_mov_b32 s68, 0\ns_waitcnt vmcnt(0)\nv_readfirstlane_b32 s74, v13\ns_nop 0\ns_and_b32 s75, s74, 127\n" \
    "s_lshr_b32 s69, s74, 8\ns_sub_u32 s69, s69, s80\ns_bfe_i32 s69, s69, 0x180000\ns_cmp_eq_u32 s75, 0\ns_cselect_b32 s69, 0, s69\ns_add_u32 s80, s80, s69\n" \
    "s_lshl_b32 s74, s74, 5\ns_and_b32 s74, s74, 0x1000\ns_andn2_b32 s81, s81, 0x1000\ns_or_b32 s81, s81, s74\nv_lshrrev_b32 v15, 1, v2\nv_add_u32 v15, s60, v15\n" \
    "ds_write_b64 v15, v[252:253]\ns_branch .Lv4_%=\n.Lv6_%=:\ns_endpgm\n.Lv12_%=:\ns_nop 7\nv_cvt_pk_bf16_f32 v36, v20, v21\nv_cvt_pk_bf16_f32 v37, v22, v23\n" \
    "v_cvt_pk_bf16_f32 v38, v28, v29\nv_cvt_pk_bf16_f32 v39, v30, v31\nds_write_b128 v14, v[36:39] offset:4096\ns_waitcnt vmcnt(0) lgkmcnt(0)\nv_mov_b32 v15, s50\n" \
    "s_mov_b64 exec, 1\nbuffer_store_dword v15, off, s[64:67], s48 sc1\ns_mov_b64 exec, -1\ns_xor_b32 s48, s48, 0x8000\ns_barrier\ns_branch .Lv5_%=\n.Lv14_%=:\ns_sleep 6\n" \
    "buffer_load_dword v13, off, s[64:67], s42 sc1\ns_waitcnt vmcnt(0)\nv_readfirstlane_b32 s74, v13\ns_nop 0\ns_and_b32 s75, s74, 127\ns_lshr_b32 s69, s74, 8\n" \
    "s_sub_u32 s69, s69, s80\ns_bfe_i32 s69, s69, 0x180000\ns_cmp_eq_u32 s75, 0\ns_cselect_b32 s69, 0, s69\ns_add_u32 s80, s80, s69\ns_lshl_b32 s74, s74, 5\n" \
    "s_and_b32 s74, s74, 0x1000\ns_andn2_b32 s81, s81, 0x1000\ns_or_b32 s81, s81, s74\ns_add_u32 s68, s68, 1\ns_and_b32 s69, s68, 63\ns_cbranch_scc1 .Lv13_%=\n" \
    "s_cmp_gt_u32 s68, 0x400000\ns_cbranch_scc1 .Lv1_%=\nbuffer_load_dword v253, off, s[64:67], 0 sc1\ns_waitcnt vmcnt(0)\nv_readfirstlane_b32 s69, v253\ns_nop 0\n" \
    "s_cmp_eq_u32 s69, 1\ns_cbranch_scc0 .Lv13_%=\ns_endpgm\n.Lv18_%=:\ns_memrealtime s[72:73]\ns_waitcnt lgkmcnt(0)\ns_sub_u32 s72, s72, s76\ns_cmp_lt_i32 s72, 0\n" \
    "s_cbranch_scc1 .Lv18_%=\nbuffer_load_dword v13, off, s[64:67], s42 sc1\ns_waitcnt vmcnt(0)\nv_readfirstlane_b32 s74, v13\ns_nop 0\ns_and_b32 s75, s74, 127\n" \
    "s_lshr_b32 s69, s74, 8\ns_sub_u32 s69, s69, s80\ns_bfe_i32 s69, s69, 0x180000\ns_cmp_eq_u32 s75, 0\ns_cselect_b32 s69, 0, s69\ns_add_u32 s80, s80, s69\n" \
    "s_lshl_b32 s74, s74, 5\ns_and_b32 s74, s74, 0x1000\ns_andn2_b32 s81, s81, 0x1000\ns_or_b32 s81, s81, s74\ns_branch .Lv15_%=\n.Lv26_%=:\nv_mov_b32 v252, 0\n" \
    "v_max3_u32 v252, v252, v72, v73\nv_max3_u32 v252, v252, v74, v75\nv_max3_u32 v252, v252, v76, v77\nv_max3_u32 v252, v252, v78, v79\nv_cmp_eq_u32 vcc, -1, v252\ns_nop 0\n" \
    "s_cbranch_vccz .Lv22_%=\ns_add_u32 s80, s80, s43\ns_xor_b32 s81, s81, 0x1000\ns_xor_b32 s24, s81, 0\ns_xor_b32 s25, s81, 1024\ns_xor_b32 s26, s81, 2048\n" \
    "s_xor_b32 s27, s81, 3072\ns_add_u32 s68, s68, 1\ns_and_b32 s69, s68, 63\ns_cbranch_scc1 .Lv16_%=\ns_cmp_gt_u32 s68, 0x400000\ns_cbranch_scc1 .Lv1_%=\n" \
    "buffer_load_dword v253, off, s[64:67], 0 sc1\ns_waitcnt vmcnt(0)\nv_readfirstlane_b32 s69, v253\ns_nop 0\ns_cmp_eq_u32 s69, 1\ns_cbranch_scc0 .Lv16_%=\ns_endpgm\n" \
    ".Lv16_%=:\ns_memrealtime s[72:73]\ns_waitcnt lgkmcnt(0)\ns_sub_u32 s72, s72, s80\ns_cmp_lt_i32 s72, 0\ns_cbranch_scc1 .Lv16_%=\n" \
    "buffer_load_dwordx4 v[72:75], v2, s[64:67], s24 offen sc1\ns_nop 7\nbuffer_load_dwordx4 v[76:79], v2, s[64:67], s25 offen sc1\ns_nop 7\n" \
    "buffer_load_dwordx4 v[80:83], v2, s[64:67], s26 offen sc1\ns_nop 7\nbuffer_load_dwordx4 v[84:87], v2, s[64:67], s27 offen sc1\ns_nop 7\ns_branch .Lv19_%=\n.Lv27_%=:\n" \
    "v_mov_b32 v252, 0\nv_max3_u32 v252, v252, v80, v81\nv_max3_u32 v252, v252, v82, v83\nv_cmp_eq_u32 vcc, -1, v252\ns_nop 0\ns_cbranch_vccz .Lv23_%=\ns_add_u32 s80, s80, s43\n" \
    "s_xor_b32 s81, s81, 0x1000\ns_xor_b32 s24, s81, 0\ns_xor_b32 s25, s81, 1024\ns_xor_b32 s26, s81, 2048\ns_xor_b32 s27, s81, 3072\n.Lv29_%=:\ns_memrealtime s[72:73]\n" \
    "s_waitcnt vmcnt(0) lgkmcnt(0)\ns_sub_u32 s72, s72, s80\ns_cmp_lt_i32 s72, 0\ns_cbranch_scc1 .Lv29_%=\nbuffer_load_dwordx4 v[80:83], v2, s[64:67], s26 offen sc1\n" \
    "buffer_load_dwordx4 v[84:87], v2, s[64:67], s27 offen sc1\ns_waitcnt vmcnt(0)\ns_add_u32 s68, s68, 1\ns_and_b32 s69, s68, 63\ns_cbranch_scc1 .Lv27_%=\n" \
    "s_cmp_gt_u32 s68, 0x400000\ns_cbranch_scc1 .Lv1_%=\nbuffer_load_dword v253, off, s[64:67], 0 sc1\ns_waitcnt vmcnt(0)\nv_readfirstlane_b32 s69, v253\ns_nop 0\n" \
    "s_cmp_eq_u32 s69, 1\ns_cbranch_scc0 .Lv27_%=\ns_endpgm\n.Lv28_%=:\nv_mov_b32 v252, 0\nv_max3_u32 v252, v252, v84, v85\nv_max3_u32 v252, v252, v86, v87\n" \
    "v_cmp_eq_u32 vcc, -1, v252\ns_nop 0\ns_cbranch_vccz .Lv24_%=\ns_add_u32 s80, s80, s43\ns_xor_b32 s81, s81, 0x1000\ns_xor_b32 s24, s81, 0\ns_xor_b32 s25, s81, 1024\n" \
    "s_xor_b32 s26, s81, 2048\ns_xor_b32 s27, s81, 3072\n.Lv30_%=:\ns_memrealtime s[72:73]\ns_waitcnt vmcnt(0) lgkmcnt(0)\ns_sub_u32 s72, s72, s80\ns_cmp_lt_i32 s72, 0\n" \
    "s_cbranch_scc1 .Lv30_%=\nbuffer_load_dwordx4 v[84:87], v2, s[64:67], s27 offen sc1\ns_waitcnt vmcnt(0)\ns_add_u32 s68, s68, 1\ns_and_b32 s69, s68, 63\n" \
    "s_cbranch_scc1 .Lv28_%=\ns_cmp_gt_u32 s68, 0x400000\ns_cbranch_scc1 .Lv1_%=\nbuffer_load_dword v253, off, s[64:67], 0 sc1\ns_waitcnt vmcnt(0)\n" \
    "v_readfirstlane_b32 s69, v253\ns_nop 0\ns_cmp_eq_u32 s69, 1\ns_cbranch_scc0 .Lv28_%=\ns_endpgm\n.Lv34_%=:\ns_memrealtime s[72:73]\ns_waitcnt lgkmcnt(0)\n" \
    "s_sub_u32 s72, s72, s76\ns_cmp_lt_i32 s72, 0\ns_cbranch_scc1 .Lv34_%=\nbuffer_load_dword v13, off, s[64:67], s42 sc1\ns_waitcnt vmcnt(0)\nv_readfirstlane_b32 s74, v13\n" \
    "s_nop 0\ns_and_b32 s75, s74, 127\ns_lshr_b32 s69, s74, 8\ns_sub_u32 s69, s69, s80\ns_bfe_i32 s69, s69, 0x180000\ns_cmp_eq_u32 s75, 0\ns_cselect_b32 s69, 0, s69\n" \
    "s_add_u32 s80, s80, s69\ns_lshl_b32 s74, s74, 5\ns_and_b32 s74, s74, 0x1000\ns_andn2_b32 s81, s81, 0x1000\ns_or_b32 s81, s81, s74\ns_branch .Lv31_%=\n.Lv42_%=:\n" \
    "v_mov_b32 v252, 0\nv_max3_u32 v252, v252, v72, v73\nv_max3_u32 v252, v252, v74, v75\nv_max3_u32 v252, v252, v76, v77\nv_max3_u32 v252, v252, v78, v79\n" \
    "v_cmp_eq_u32 vcc, -1, v252\ns_nop 0\ns_cbranch_vccz .Lv38_%=\ns_add_u32 s80, s80, s43\ns_xor_b32 s81, s81, 0x1000\ns_xor_b32 s24, s81, 0\ns_xor_b32 s25, s81, 1024\n" \
    "s_xor_b32 s26, s81, 2048\ns_xor_b32 s27, s81, 3072\ns_add_u32 s68, s68, 1\ns_and_b32 s69, s68, 63\ns_cbranch_scc1 .Lv32_%=\ns_cmp_gt_u32 s68, 0x400000\n" \
    "s_cbranch_scc1 .Lv1_%=\nbuffer_load_dword v253, off, s[64:67], 0 sc1\ns_waitcnt vmcnt(0)\nv_readfirstlane_b32 s69, v253\ns_nop 0\ns_cmp_eq_u32 s69, 1\n" \
    "s_cbranch_scc0 .Lv32_%=\ns_endpgm\n.Lv32_%=:\ns_memrealtime s[72:73]\ns_waitcnt lgkmcnt(0)\ns_sub_u32 s72, s72, s80\ns_cmp_lt_i32 s72, 0\ns_cbranch_scc1 .Lv32_%=\n" \
    "buffer_load_dwordx4 v[72:75], v2, s[64:67], s24 offen sc1\ns_nop 7\nbuffer_load_dwordx4 v[76:79], v2, s[64:67], s25 offen sc1\ns_nop 7\n" \
    "buffer_load_dwordx4 v[80:83], v2, s[64:67], s26 offen sc1\ns_nop 7\nbuffer_load_dwordx4 v[84:87], v2, s[64:67], s27 offen sc1\ns_nop 7\ns_branch .Lv35_%=\n.Lv43_%=:\n" \
    "v_mov_b32 v252, 0\nv_max3_u32 v252, v252, v80, v81\nv_max3_u32 v252, v252, v82, v83\nv_cmp_eq_u32 vcc, -1, v252\ns_nop 0\ns_cbranch_vccz .Lv39_%=\ns_add_u32 s80, s80, s43\n" \
    "s_xor_b32 s81, s81, 0x1000\ns_xor_b32 s24, s81, 0\ns_xor_b32 s25, s81, 1024\ns_xor_b32 s26, s81, 2048\ns_xor_b32 s27, s81, 3072\n.Lv45_%=:\ns_memrealtime s[72:73]\n" \
    "s_waitcnt vmcnt(0) lgkmcnt(0)\ns_sub_u32 s72, s72, s80\ns_cmp_lt_i32 s72, 0\ns_cbranch_scc1 .Lv45_%=\nbuffer_load_dwordx4 v[80:83], v2, s[64:67], s26 offen sc1\n" \
    "buffer_load_dwordx4 v[84:87], v2, s[64:67], s27 offen sc1\ns_waitcnt vmcnt(0)\ns_add_u32 s68, s68, 1\ns_and_b32 s69, s68, 63\ns_cbranch_scc1 .Lv43_%=\n" \
    "s_cmp_gt_u32 s68, 0x400000\ns_cbranch_scc1 .Lv1_%=\nbuffer_load_dword v253, off, s[64:67], 0 sc1\ns_waitcnt vmcnt(0)\nv_readfirstlane_b32 s69, v253\ns_nop 0\n" \
    "s_cmp_eq_u32 s69, 1\ns_cbranch_scc0 .Lv43_%=\ns_endpgm\n.Lv44_%=:\nv_mov_b32 v252, 0\nv_max3_u32 v252, v252, v84, v85\nv_max3_u32 v252, v252, v86, v87\n" \
    "v_cmp_eq_u32 vcc, -1, v252\ns_nop 0\ns_cbranch_vccz .Lv40_%=\ns_add_u32 s80, s80, s43\ns_xor_b32 s81, s81, 0x1000\ns_xor_b32 s24, s81, 0\ns_xor_b32 s25, s81, 1024\n" \
    "s_xor_b32 s26, s81, 2048\ns_xor_b32 s27, s81, 3072\n.Lv46_%=:\ns_memrealtime s[72:73]\ns_waitcnt vmcnt(0) lgkmcnt(0)\ns_sub_u32 s72, s72, s80\ns_cmp_lt_i32 s72, 0\n" \
    "s_cbranch_scc1 .Lv46_%=\nbuffer_load_dwordx4 v[84:87], v2, s[64:67], s27 offen sc1\ns_waitcnt vmcnt(0)\ns_add_u32 s68, s68, 1\ns_and_b32 s69, s68, 63\n" \
    "s_cbranch_scc1 .Lv44_%=\ns_cmp_gt_u32 s68, 0x400000\ns_cbranch_scc1 .Lv1_%=\nbuffer_load_dword v253, off, s[64:67], 0 sc1\ns_waitcnt vmcnt(0)\n" \
    "v_readfirstlane_b32 s69, v253\ns_nop 0\ns_cmp_eq_u32 s69, 1\ns_cbranch_scc0 .Lv44_%=\ns_endpgm\n.Lv50_%=:\ns_memrealtime s[72:73]\ns_waitcnt lgkmcnt(0)\n" \
    "s_sub_u32 s72, s72, s76\ns_cmp_lt_i32 s72, 0\ns_cbranch_scc1 .Lv50_%=\nbuffer_load_dword v13, off, s[64:67], s42 sc1\ns_waitcnt vmcnt(0)\nv_readfirstlane_b32 s74, v13\n" \
    "s_nop 0\ns_and_b32 s75, s74, 127\ns_lshr_b32 s69, s74, 8\ns_sub_u32 s69, s69, s80\ns_bfe_i32 s69, s69, 0x180000\ns_cmp_eq_u32 s75, 0\ns_cselect_b32 s69, 0, s69\n" \
    "s_add_u32 s80, s80, s69\ns_lshl_b32 s74, s74, 5\ns_and_b32 s74, s74, 0x1000\ns_andn2_b32 s81, s81, 0x1000\ns_or_b32 s81, s81, s74\ns_branch .Lv47_%=\n.Lv58_%=:\n" \
    "v_mov_b32 v252, 0\nv_max3_u32 v252, v252, v72, v73\nv_max3_u32 v252, v252, v74, v75\nv_max3_u32 v252, v252, v76, v77\nv_max3_u32 v252, v252, v78, v79\n" \
    "v_cmp_eq_u32 vcc, -1, v252\ns_nop 0\ns_cbranch_vccz .Lv54_%=\ns_add_u32 s80, s80, s43\ns_xor_b32 s81, s81, 0x1000\ns_xor_b32 s24, s81, 0\ns_xor_b32 s25, s81, 1024\n" \
    "s_xor_b32 s26, s81, 2048\ns_xor_b32 s27, s81, 3072\ns_add_u32 s68, s68, 1\ns_and_b32 s69, s68, 63\ns_cbranch_scc1 .Lv48_%=\ns_cmp_gt_u32 s68, 0x400000\n" \
    "s_cbranch_scc1 .Lv1_%=\nbuffer_load_dword v253, off, s[64:67], 0 sc1\ns_waitcnt vmcnt(0)\nv_readfirstlane_b32 s69, v253\ns_nop 0\ns_cmp_eq_u32 s69, 1\n" \
    "s_cbranch_scc0 .Lv48_%=\ns_endpgm\n.Lv48_%=:\ns_memrealtime s[72:73]\ns_waitcnt lgkmcnt(0)\ns_sub_u32 s72, s72, s80\ns_cmp_lt_i32 s72, 0\ns_cbranch_scc1 .Lv48_%=\n" \
    "buffer_load_dwordx4 v[72:75], v2, s[64:67], s24 offen sc1\ns_nop 7\nbuffer_load_dwordx4 v[76:79], v2, s[64:67], s25 offen sc1\ns_nop 7\n" \
    "buffer_load_dwordx4 v[80:83], v2, s[64:67], s26 offen sc1\ns_nop 7\nbuffer_load_dwordx4 v[84:87], v2, s[64:67], s27 offen sc1\ns_nop 7\ns_branch .Lv51_%=\n.Lv59_%=:\n" \
    "v_mov_b32 v252, 0\nv_max3_u32 v252, v252, v80, v81\nv_max3_u32 v252, v252, v82, v83\nv_cmp_eq_u32 vcc, -1, v252\ns_nop 0\ns_cbranch_vccz .Lv55_%=\ns_add_u32 s80, s80, s43\n" \
    "s_xor_b32 s81, s81, 0x1000\ns_xor_b32 s24, s81, 0\ns_xor_b32 s25, s81, 1024\ns_xor_b32 s26, s81, 2048\ns_xor_b32 s27, s81, 3072\n.Lv61_%=:\ns_memrealtime s[72:73]\n" \
    "s_waitcnt vmcnt(0) lgkmcnt(0)\ns_sub_u32 s72, s72, s80\ns_cmp_lt_i32 s72, 0\ns_cbranch_scc1 .Lv61_%=\nbuffer_load_dwordx4 v[80:83], v2, s[64:67], s26 offen sc1\n" \
    "buffer_load_dwordx4 v[84:87], v2, s[64:67], s27 offen sc1\ns_waitcnt vmcnt(0)\ns_add_u32 s68, s68, 1\ns_and_b32 s69, s68, 63\ns_cbranch_scc1 .Lv59_%=\n" \
    "s_cmp_gt_u32 s68, 0x400000\ns_cbranch_scc1 .Lv1_%=\nbuffer_load_dword v253, off, s[64:67], 0 sc1\ns_waitcnt vmcnt(0)\nv_readfirstlane_b32 s69, v253\ns_nop 0\n" \
    "s_cmp_eq_u32 s69, 1\ns_cbranch_scc0 .Lv59_%=\ns_endpgm\n.Lv60_%=:\nv_mov_b32 v252, 0\nv_max3_u32 v252, v252, v84, v85\nv_max3_u32 v252, v252, v86, v87\n" \
    "v_cmp_eq_u32 vcc, -1, v252\ns_nop 0\ns_cbranch_vccz .Lv56_%=\ns_add_u32 s80, s80, s43\ns_xor_b32 s81, s81, 0x1000\ns_xor_b32 s24, s81, 0\ns_xor_b32 s25, s81, 1024\n" \
    "s_xor_b32 s26, s81, 2048\ns_xor_b32 s27, s81, 3072\n.Lv62_%=:\ns_memrealtime s[72:73]\ns_waitcnt vmcnt(0) lgkmcnt(0)\ns_sub_u32 s72, s72, s80\ns_cmp_lt_i32 s72, 0\n" \
    "s_cbranch_scc1 .Lv62_%=\nbuffer_load_dwordx4 v[84:87], v2, s[64:67], s27 offen sc1\ns_waitcnt vmcnt(0)\ns_add_u32 s68, s68, 1\ns_and_b32 s69, s68, 63\n" \
    "s_cbranch_scc1 .Lv60_%=\ns_cmp_gt_u32 s68, 0x400000\ns_cbranch_scc1 .Lv1_%=\nbuffer_load_dword v253, off, s[64:67], 0 sc1\ns_waitcnt vmcnt(0)\n" \
    "v_readfirstlane_b32 s69, v253\ns_nop 0\ns_cmp_eq_u32 s69, 1\ns_cbranch_scc0 .Lv60_%=\ns_endpgm\n.Lv66_%=:\ns_memrealtime s[72:73]\ns_waitcnt lgkmcnt(0)\n" \
    "s_sub_u32 s72, s72, s76\ns_cmp_lt_i32 s72, 0\ns_cbranch_scc1 .Lv66_%=\nbuffer_load_dword v13, off, s[64:67], s42 sc1\ns_waitcnt vmcnt(0)\nv_readfirstlane_b32 s74, v13\n" \
    "s_nop 0\ns_and_b32 s75, s74, 127\ns_lshr_b32 s69, s74, 8\ns_sub_u32 s69, s69, s80\ns_bfe_i32 s69, s69, 0x180000\ns_cmp_eq_u32 s75, 0\ns_cselect_b32 s69, 0, s69\n" \
    "s_add_u32 s80, s80, s69\ns_lshl_b32 s74, s74, 5\ns_and_b32 s74, s74, 0x1000\ns_andn2_b32 s81, s81, 0x1000\ns_or_b32 s81, s81, s74\ns_branch .Lv63_%=\n.Lv74_%=:\n" \
    "v_mov_b32 v252, 0\nv_max3_u32 v252, v252, v72, v73\nv_max3_u32 v252, v252, v74, v75\nv_max3_u32 v252, v252, v76, v77\nv_max3_u32 v252, v252, v78, v79\n" \
    "v_cmp_eq_u32 vcc, -1, v252\ns_nop 0\ns_cbranch_vccz .Lv70_%=\ns_add_u32 s80, s80, s43\ns_xor_b32 s81, s81, 0x1000\ns_xor_b32 s24, s81, 0\ns_xor_b32 s25, s81, 1024\n" \
    "s_xor_b32 s26, s81, 2048\ns_xor_b32 s27, s81, 3072\ns_add_u32 s68, s68, 1\ns_and_b32 s69, s68, 63\ns_cbranch_scc1 .Lv64_%=\ns_cmp_gt_u32 s68, 0x400000\n" \
    "s_cbranch_scc1 .Lv1_%=\nbuffer_load_dword v253, off, s[64:67], 0 sc1\ns_waitcnt vmcnt(0)\nv_readfirstlane_b32 s69, v253\ns_nop 0\ns_cmp_eq_u32 s69, 1\n" \
    "s_cbranch_scc0 .Lv64_%=\ns_endpgm\n.Lv64_%=:\ns_memrealtime s[72:73]\ns_waitcnt lgkmcnt(0)\ns_sub_u32 s72, s72, s80\ns_cmp_lt_i32 s72, 0\ns_cbranch_scc1 .Lv64_%=\n" \
    "buffer_load_dwordx4 v[72:75], v2, s[64:67], s24 offen sc1\ns_nop 7\nbuffer_load_dwordx4 v[76:79], v2, s[64:67], s25 offen sc1\ns_nop 7\n" \
    "buffer_load_dwordx4 v[80:83], v2, s[64:67], s26 offen sc1\ns_nop 7\nbuffer_load_dwordx4 v[84:87], v2, s[64:67], s27 offen sc1\ns_nop 7\ns_branch .Lv67_%=\n.Lv75_%=:\n" \
    "v_mov_b32 v252, 0\nv_max3_u32 v252, v252, v80, v81\nv_max3_u32 v252, v252, v82, v83\nv_cmp_eq_u32 vcc, -1, v252\ns_nop 0\ns_cbranch_vccz .Lv71_%=\ns_add_u32 s80, s80, s43\n" \
    "s_xor_b32 s81, s81, 0x1000\ns_xor_b32 s24, s81, 0\ns_xor_b32 s25, s81, 1024\ns_xor_b32 s26, s81, 2048\ns_xor_b32 s27, s81, 3072\n.Lv77_%=:\ns_memrealtime s[72:73]\n" \
    "s_waitcnt vmcnt(0) lgkmcnt(0)\ns_sub_u32 s72, s72, s80\ns_cmp_lt_i32 s72, 0\ns_cbranch_scc1 .Lv77_%=\nbuffer_load_dwordx4 v[80:83], v2, s[64:67], s26 offen sc1\n" \
    "buffer_load_dwordx4 v[84:87], v2, s[64:67], s27 offen sc1\ns_waitcnt vmcnt(0)\ns_add_u32 s68, s68, 1\ns_and_b32 s69, s68, 63\ns_cbranch_scc1 .Lv75_%=\n" \
    "s_cmp_gt_u32 s68, 0x400000\ns_cbranch_scc1 .Lv1_%=\nbuffer_load_dword v253, off, s[64:67], 0 sc1\ns_waitcnt vmcnt(0)\nv_readfirstlane_b32 s69, v253\ns_nop 0\n" \
    "s_cmp_eq_u32 s69, 1\ns_cbranch_scc0 .Lv75_%=\ns_endpgm\n.Lv76_%=:\nv_mov_b32 v252, 0\nv_max3_u32 v252, v252, v84, v85\nv_max3_u32 v252, v252, v86, v87\n" \
    "v_cmp_eq_u32 vcc, -1, v252\ns_nop 0\ns_cbranch_vccz .Lv72_%=\ns_add_u32 s80, s80, s43\ns_xor_b32 s81, s81, 0x1000\ns_xor_b32 s24, s81, 0\ns_xor_b32 s25, s81, 1024\n" \
    "s_xor_b32 s26, s81, 2048\ns_xor_b32 s27, s81, 3072\n.Lv78_%=:\ns_memrealtime s[72:73]\ns_waitcnt vmcnt(0) lgkmcnt(0)\ns_sub_u32 s72, s72, s80\ns_cmp_lt_i32 s72, 0\n" \
    "s_cbranch_scc1 .Lv78_%=\nbuffer_load_dwordx4 v[84:87], v2, s[64:67], s27 offen sc1\ns_waitcnt vmcnt(0)\ns_add_u32 s68, s68, 1\ns_and_b32 s69, s68, 63\n" \
    "s_cbranch_scc1 .Lv76_%=\ns_cmp_gt_u32 s68, 0x400000\ns_cbranch_scc1 .Lv1_%=\nbuffer_load_dword v253, off, s[64:67], 0 sc1\ns_waitcnt vmcnt(0)\n" \
    "v_readfirstlane_b32 s69, v253\ns_nop 0\ns_cmp_eq_u32 s69, 1\ns_cbranch_scc0 .Lv76_%=\ns_endpgm\n.Lv80_%=:\nv_mov_b32 v252, 0\nv_pk_max_u16 v252, v252, v32\n" \
    "v_pk_max_u16 v252, v252, v33\nv_pk_max_u16 v252, v252, v34\nv_pk_max_u16 v252, v252, v35\nv_pk_max_u16 v252, v252, v36\nv_pk_max_u16 v252, v252, v37\n" \
    "v_pk_max_u16 v252, v252, v38\nv_pk_max_u16 v252, v252, v39\nv_pk_max_u16 v252, v252, v40\nv_pk_max_u16 v252, v252, v41\nv_pk_max_u16 v252, v252, v42\n" \
    "v_pk_max_u16 v252, v252, v43\nv_pk_max_u16 v252, v252, v44\nv_pk_max_u16 v252, v252, v45\nv_pk_max_u16 v252, v252, v46\nv_pk_max_u16 v252, v252, v47\n" \
    "v_pk_max_u16 v252, v252, v252 op_sel:[0,1] op_sel_hi:[1,0]\nv_cmp_eq_u32 vcc, -1, v252\ns_nop 0\ns_cbranch_vccz .Lv81_%=\ns_add_u32 s68, s68, 1\ns_and_b32 s69, s68, 63\n" \
    "s_cbranch_scc1 .Lv79_%=\ns_cmp_gt_u32 s68, 0x400000\ns_cbranch_scc1 .Lv1_%=\nbuffer_load_dword v253, off, s[64:67], 0 sc1\ns_waitcnt vmcnt(0)\n" \
    "v_readfirstlane_b32 s69, v253\ns_nop 0\ns_cmp_eq_u32 s69, 1\ns_cbranch_scc0 .Lv79_%=\ns_endpgm\n.Lv1_%=:\nv_mov_b32 v253, 1\nbuffer_store_dword v253, off, s[64:67], 0 sc1\n" \
    "s_endpgm\n"
#define ASM_R \
    "s_mov_b32 s81, s56\n.Lr2_%=:\nglobal_load_dwordx4 v[224:227], v7, s[54:55]\ns_mov_b32 s68, 0\ns_cmp_eq_u32 s32, 7\ns_cselect_b32 s79, 64, 0x7fff0000\n" \
    "s_and_b32 s69, s32, 7\ns_lshl_b32 s69, s69, 21\ns_add_u32 s82, s42, s69\ns_lshr_b32 s69, s82, 8\ns_and_b32 s69, s69, 0x2000\ns_xor_b32 s82, s82, s69\n" \
    "s_and_b32 s69, s32, 15\ns_lshl_b32 s69, s69, 19\ns_add_u32 s83, s43, s69\ns_lshr_b32 s69, s83, 8\ns_and_b32 s69, s69, 0x2000\ns_xor_b32 s83, s83, s69\n" \
    "s_and_b32 s69, s32, 15\ns_lshl_b32 s69, s69, 18\ns_add_u32 s84, s44, s69\ns_lshr_b32 s69, s84, 8\ns_and_b32 s69, s69, 0x2000\ns_xor_b32 s84, s84, s69\n" \
    "s_and_b32 s69, s32, 15\ns_lshl_b32 s69, s69, 18\ns_add_u32 s94, s45, s69\ns_lshr_b32 s69, s94, 8\ns_and_b32 s69, s69, 0x2000\ns_xor_b32 s94, s94, s69\n" \
    "s_add_u32 s69, s32, 1\ns_and_b32 s69, s69, 15\ns_lshl_b32 s69, s69, 17\ns_add_u32 s92, s46, s69\ns_and_b32 s69, s32, 63\ns_lshl_b32 s69, s69, 19\ns_add_u32 s93, s47, s69\n" \
    "s_mov_b32 s68, 0\ns_cmp_eq_u32 s32, 0\ns_cbranch_scc1 .Lr6_%=\ns_sub_u32 s77, s32, 1\ns_max_u32 s77, s77, 1\n.Lr4_%=:\nds_read_b32 v253, v10\ns_waitcnt lgkmcnt(0)\n" \
    "v_readfirstlane_b32 s74, v253\ns_nop 0\ns_and_b32 s75, s74, 127\ns_cmp_lt_u32 s75, s77\ns_cbranch_scc1 .Lr5_%=\ns_cmp_ge_u32 s75, s32\ns_cbranch_scc1 .Lr6_%=\n" \
    "s_memrealtime s[72:73]\ns_waitcnt lgkmcnt(0)\ns_lshr_b32 s69, s74, 8\ns_sub_u32 s69, s69, s72\ns_bfe_i32 s69, s69, 0x180000\ns_add_u32 s78, s72, s69\n" \
    "s_sub_u32 s76, s78, 60\n.Lr26_%=:\ns_memrealtime s[72:73]\ns_waitcnt lgkmcnt(0)\ns_sub_u32 s72, s72, s76\ns_cmp_lt_i32 s72, 0\ns_cbranch_scc1 .Lr26_%=\n" \
    "buffer_load_dword v254, v8, s[64:67], 0 offen sc1\ns_waitcnt vmcnt(0)\nv_cmp_ge_u32 vcc, s32, v254\ns_nop 0\ns_cmp_eq_u64 vcc, 0\ns_cselect_b32 s76, 4, s58\n" \
    "s_add_u32 s76, s76, s78\n.Lr27_%=:\ns_memrealtime s[72:73]\ns_waitcnt lgkmcnt(0)\ns_sub_u32 s72, s72, s76\ns_cmp_lt_i32 s72, 0\ns_cbranch_scc1 .Lr27_%=\n" \
    "s_cbranch_vccz .Lr15_%=\n.Lr6_%=:\nbuffer_load_dword v254, v8, s[64:67], 0 offen sc1\ns_waitcnt vmcnt(0)\nv_cmp_ge_u32 vcc, s32, v254\ns_nop 0\ns_cbranch_vccnz .Lr7_%=\n" \
    ".Lr15_%=:\nbuffer_load_dwordx4 a[0:3], v11, s[64:67], s82 offen offset:0 sc1\nbuffer_load_dwordx4 a[16:19], v11, s[64:67], s82 offen offset:512 sc1\n" \
    "buffer_load_dwordx4 a[4:7], v11, s[64:67], s82 offen offset:2048 sc1\nbuffer_load_dwordx4 a[20:23], v11, s[64:67], s82 offen offset:2560 sc1\ns_xor_b32 s82, s82, 4096\n" \
    "buffer_load_dwordx4 a[8:11], v11, s[64:67], s82 offen offset:0 sc1\nbuffer_load_dwordx4 a[24:27], v11, s[64:67], s82 offen offset:512 sc1\n" \
    "buffer_load_dwordx4 a[12:15], v11, s[64:67], s82 offen offset:2048 sc1\nbuffer_load_dwordx4 a[28:31], v11, s[64:67], s82 offen offset:2560 sc1\ns_xor_b32 s82, s82, 20480\n" \
    "buffer_load_dwordx4 a[32:35], v11, s[64:67], s82 offen offset:0 sc1\nbuffer_load_dwordx4 a[48:51], v11, s[64:67], s82 offen offset:512 sc1\n" \
    "buffer_load_dwordx4 a[36:39], v11, s[64:67], s82 offen offset:2048 sc1\nbuffer_load_dwordx4 a[52:55], v11, s[64:67], s82 offen offset:2560 sc1\ns_xor_b32 s82, s82, 4096\n" \
    "buffer_load_dwordx4 a[40:43], v11, s[64:67], s82 offen offset:0 sc1\nbuffer_load_dwordx4 a[56:59], v11, s[64:67], s82 offen offset:512 sc1\n" \
    "buffer_load_dwordx4 a[44:47], v11, s[64:67], s82 offen offset:2048 sc1\nbuffer_load_dwordx4 a[60:63], v11, s[64:67], s82 offen offset:2560 sc1\ns_xor_b32 s82, s82, 53248\n" \
    "buffer_load_dwordx4 a[64:67], v11, s[64:67], s82 offen offset:0 sc1\nbuffer_load_dwordx4 a[80:83], v11, s[64:67], s82 offen offset:512 sc1\n" \
    "buffer_load_dwordx4 a[68:71], v11, s[64:67], s82 offen offset:2048 sc1\nbuffer_load_dwordx4 a[84:87], v11, s[64:67], s82 offen offset:2560 sc1\ns_xor_b32 s82, s82, 4096\n" \
    "buffer_load_dwordx4 a[72:75], v11, s[64:67], s82 offen offset:0 sc1\nbuffer_load_dwordx4 a[88:91], v11, s[64:67], s82 offen offset:512 sc1\n" \
    "buffer_load_dwordx4 a[76:79], v11, s[64:67], s82 offen offset:2048 sc1\nbuffer_load_dwordx4 a[92:95], v11, s[64:67], s82 offen offset:2560 sc1\ns_xor_b32 s82, s82, 20480\n" \
    "buffer_load_dwordx4 a[96:99], v11, s[64:67], s82 offen offset:0 sc1\nbuffer_load_dwordx4 a[112:115], v11, s[64:67], s82 offen offset:512 sc1\n" \
    "buffer_load_dwordx4 a[100:103], v11, s[64:67], s82 offen offset:2048 sc1\nbuffer_load_dwordx4 a[116:119], v11, s[64:67], s82 offen offset:2560 sc1\n" \
    "s_xor_b32 s82, s82, 4096\nbuffer_load_dwordx4 a[104:107], v11, s[64:67], s82 offen offset:0 sc1\nbuffer_load_dwordx4 a[120:123], v11, s[64:67], s82 offen offset:512 sc1\n" \
    "buffer_load_dwordx4 a[108:111], v11, s[64:67], s82 offen offset:2048 sc1\nbuffer_load_dwordx4 a[124:127], v11, s[64:67], s82 offen offset:2560 sc1\n" \
    "buffer_load_dword v255, off, s[64:67], s79 offset:64 sc1\ns_waitcnt vmcnt(0)\n.Lr25_%=:\nv_readfirstlane_b32 s74, v255\ns_lshr_b32 s69, s79, 16\ns_add_u32 s74, s74, s69\n" \
    "s_cmp_lt_u32 s74, 64\ns_cbranch_scc1 .Lr24_%=\ns_cmp_eq_u32 s32, 0\ns_cbranch_scc1 .Lr20_%=\n.Lr13_%=:\nds_read_b32 v253, v10\ns_waitcnt lgkmcnt(0)\n" \
    "v_readfirstlane_b32 s74, v253\ns_nop 0\ns_and_b32 s75, s74, 127\ns_cmp_lt_u32 s75, s32\ns_cbranch_scc1 .Lr14_%=\ns_and_b32 s57, s74, 128\ns_memrealtime s[72:73]\n" \
    "s_waitcnt lgkmcnt(0)\ns_lshr_b32 s69, s74, 8\ns_sub_u32 s69, s69, s72\ns_bfe_i32 s69, s69, 0x180000\ns_add_u32 s56, s72, s69\n.Lr3_%=:\ns_sub_u32 s76, s56, s59\n.Lr29_%=:\n" \
    "s_memrealtime s[72:73]\ns_waitcnt lgkmcnt(0)\ns_sub_u32 s72, s72, s76\ns_cmp_lt_i32 s72, 0\ns_cbranch_scc1 .Lr29_%=\ns_cmp_gt_i32 s72, 28\ns_cbranch_scc1 .Lr22_%=\n" \
    "v_bfe_u32 v24, v1, 2, 7\nbuffer_load_dword v24, v24, s[64:67], s60 offen sc1\ns_sub_u32 s76, s56, s34\ns_add_u32 s76, s76, 24\ns_lshl_b32 s76, s76, 8\n" \
    "s_lshl_b32 s77, s38, 8\ns_waitcnt vmcnt(0)\nv_and_b32 v25, 127, v24\nv_cmp_eq_u32 s[74:75], s32, v25\nv_subrev_u32 v25, s76, v24\nv_cmp_lt_i32 vcc, 0, v25\ns_nop 0\n" \
    "s_and_b64 vcc, vcc, s[74:75]\ns_cbranch_scc0 .Lr22_%=\ns_cmp_eq_u32 s32, 0\ns_cbranch_scc1 .Lr22_%=\n.Lr23_%=:\ns_add_u32 s56, s56, s38\ns_xor_b32 s57, s57, 128\n" \
    "v_subrev_u32 v25, s77, v25\nv_cmp_lt_i32 vcc, 0, v25\ns_nop 0\ns_and_b64 vcc, vcc, s[74:75]\ns_cbranch_scc1 .Lr23_%=\ns_add_u32 s76, s56, s35\ns_cmp_eq_u32 s48, 0\n" \
    "s_cbranch_scc1 .Lr30_%=\ns_lshl_b32 s74, s76, 8\ns_add_u32 s74, s74, s57\ns_add_u32 s74, s74, s32\ns_add_u32 s74, s74, 1\nv_mov_b32 v253, s74\ns_xor_b32 s69, s48, 512\n" \
    "s_mov_b64 exec, 1\nbuffer_store_dword v253, off, s[64:67], s69 sc1\ns_mov_b64 exec, -1\n.Lr30_%=:\n.Lr22_%=:\ns_lshl_b32 s69, s57, 7\ns_xor_b32 s84, s84, s69\n" \
    "s_lshl_b32 s69, s57, 5\ns_xor_b32 s92, s92, s69\ns_xor_b32 s16, s83, 0\ns_xor_b32 s17, s83, 2048\ns_xor_b32 s18, s83, 4096\ns_xor_b32 s19, s83, 6144\ns_mov_b32 s68, 0\n" \
    ".Lr8_%=:\ns_xor_b32 s85, s84, 0x8000\ns_xor_b32 s86, s84, 0x10000\ns_xor_b32 s87, s84, 0x18000\ns_lshl_b32 s69, s81, 1\ns_xor_b32 s88, s84, s69\ns_xor_b32 s89, s85, s69\n" \
    "s_xor_b32 s90, s86, s69\ns_xor_b32 s91, s87, s69\ns_sub_u32 s76, s56, s36\n.Lr31_%=:\ns_memrealtime s[72:73]\ns_waitcnt lgkmcnt(0)\ns_sub_u32 s72, s72, s76\n" \
    "s_cmp_lt_i32 s72, 0\ns_cbranch_scc1 .Lr31_%=\nbuffer_load_dwordx4 v[32:35], v1, s[64:67], s16 offen offset:0 sc1\n" \
    "buffer_load_dwordx4 v[36:39], v1, s[64:67], s16 offen offset:1024 sc1\nbuffer_load_dwordx4 v[40:43], v1, s[64:67], s17 offen offset:0 sc1\n" \
    "buffer_load_dwordx4 v[44:47], v1, s[64:67], s17 offen offset:1024 sc1\nbuffer_load_dwordx4 v[48:51], v1, s[64:67], s18 offen offset:0 sc1\n" \
    "buffer_load_dwordx4 v[52:55], v1, s[64:67], s18 offen offset:1024 sc1\ns_cmp_lg_u32 s32, 63\ns_cbranch_scc0 .Lr10_%=\n.Lr34_%=:\ns_memrealtime s[72:73]\n" \
    "s_waitcnt lgkmcnt(0)\ns_sub_u32 s72, s72, s56\ns_cmp_lt_i32 s72, 0\ns_cbranch_scc1 .Lr34_%=\nbuffer_load_dwordx4 v[68:71], v1, s[64:67], s84 offen offset:0 sc1\n" \
    "buffer_load_dwordx4 v[72:75], v1, s[64:67], s84 offen offset:1024 sc1\nbuffer_load_dwordx4 v[76:79], v1, s[64:67], s84 offen offset:2048 sc1\n" \
    "buffer_load_dwordx4 v[80:83], v1, s[64:67], s84 offen offset:3072 sc1\nbuffer_load_dwordx4 v[88:91], v1, s[64:67], s85 offen offset:0 sc1\n" \
    "buffer_load_dwordx4 v[92:95], v1, s[64:67], s85 offen offset:1024 sc1\nbuffer_load_dwordx4 v[96:99], v1, s[64:67], s85 offen offset:2048 sc1\n" \
    "buffer_load_dwordx4 v[100:103], v1, s[64:67], s85 offen offset:3072 sc1\nbuffer_load_dwordx4 v[108:111], v1, s[64:67], s86 offen offset:0 sc1\n" \
    "buffer_load_dwordx4 v[112:115], v1, s[64:67], s86 offen offset:1024 sc1\nbuffer_load_dwordx4 v[56:59], v1, s[64:67], s19 offen offset:0 sc1\n" \
    "buffer_load_dwordx4 v[60:63], v1, s[64:67], s19 offen offset:1024 sc1\nbuffer_load_dwordx4 v[116:119], v1, s[64:67], s86 offen offset:2048 sc1\n" \
    "buffer_load_dwordx4 v[120:123], v1, s[64:67], s86 offen offset:3072 sc1\nbuffer_load_dwordx4 v[128:131], v1, s[64:67], s87 offen offset:0 sc1\n" \
    "buffer_load_dwordx4 v[132:135], v1, s[64:67], s87 offen offset:1024 sc1\nbuffer_load_dwordx4 v[136:139], v1, s[64:67], s87 offen offset:2048 sc1\n" \
    "buffer_load_dwordx4 v[140:143], v1, s[64:67], s87 offen offset:3072 sc1\nbuffer_load_dwordx4 v[64:67], v2, s[64:67], s88 offen offset:0 sc1\n" \
    "buffer_load_dwordx4 v[84:87], v2, s[64:67], s89 offen offset:0 sc1\nbuffer_load_dwordx4 v[104:107], v2, s[64:67], s90 offen offset:0 sc1\n" \
    "buffer_load_dwordx4 v[124:127], v2, s[64:67], s91 offen offset:0 sc1\nv_mov_b64 v[24:25], 0\nv_mov_b64 v[26:27], 0\ns_xor_b32 s77, s92, 0x1000\ns_waitcnt vmcnt(26)\n" \
    "v_permlane16_swap_b32 v32, v33\nv_permlane16_swap_b32 v34, v35\nv_permlane16_swap_b32 v36, v37\nv_permlane16_swap_b32 v38, v39\ns_waitcnt vmcnt(24)\n" \
    "v_permlane16_swap_b32 v40, v41\nv_permlane16_swap_b32 v42, v43\nv_permlane16_swap_b32 v44, v45\nv_permlane16_swap_b32 v46, v47\ns_waitcnt vmcnt(22)\n" \
    "v_permlane16_swap_b32 v48, v49\nv_permlane16_swap_b32 v50, v51\nv_permlane16_swap_b32 v52, v53\nv_permlane16_swap_b32 v54, v55\ns_waitcnt vmcnt(21)\n" \
    "v_smfmac_f32_16x16x64_bf16 v[24:27], v[68:71], a[0:7], v0\ns_waitcnt vmcnt(20)\nv_smfmac_f32_16x16x64_bf16 v[24:27], v[72:75], a[8:15], v0\ns_waitcnt vmcnt(19)\n" \
    "v_smfmac_f32_16x16x64_bf16 v[24:27], v[76:79], a[16:23], v0\ns_waitcnt vmcnt(18)\nv_smfmac_f32_16x16x64_bf16 v[24:27], v[80:83], a[24:31], v0\ns_waitcnt vmcnt(17)\n" \
    "v_smfmac_f32_16x16x64_bf16 v[24:27], v[88:91], a[32:39], v0\ns_waitcnt vmcnt(16)\nv_smfmac_f32_16x16x64_bf16 v[24:27], v[92:95], a[40:47], v0\ns_waitcnt vmcnt(15)\n" \
    "v_smfmac_f32_16x16x64_bf16 v[24:27], v[96:99], a[48:55], v0\ns_waitcnt vmcnt(14)\nv_smfmac_f32_16x16x64_bf16 v[24:27], v[100:103], a[56:63], v0\ns_waitcnt vmcnt(13)\n" \
    "v_smfmac_f32_16x16x64_bf16 v[24:27], v[108:111], a[64:71], v0\ns_waitcnt vmcnt(12)\nv_smfmac_f32_16x16x64_bf16 v[24:27], v[112:115], a[72:79], v0\ns_waitcnt vmcnt(9)\n" \
    "v_permlane16_swap_b32 v56, v57\nv_smfmac_f32_16x16x64_bf16 v[24:27], v[116:119], a[80:87], v0\ns_waitcnt vmcnt(8)\nv_permlane16_swap_b32 v58, v59\n" \
    "v_smfmac_f32_16x16x64_bf16 v[24:27], v[120:123], a[88:95], v0\ns_waitcnt vmcnt(7)\nv_permlane16_swap_b32 v60, v61\n" \
    "v_smfmac_f32_16x16x64_bf16 v[24:27], v[128:131], a[96:103], v0\ns_waitcnt vmcnt(6)\nv_permlane16_swap_b32 v62, v63\n" \
    "v_smfmac_f32_16x16x64_bf16 v[24:27], v[132:135], a[104:111], v0\ns_waitcnt vmcnt(5)\nv_smfmac_f32_16x16x64_bf16 v[24:27], v[136:139], a[112:119], v0\ns_waitcnt vmcnt(4)\n" \
    "v_smfmac_f32_16x16x64_bf16 v[24:27], v[140:143], a[120:127], v0\ns_waitcnt vmcnt(3)\nv_smfmac_f32_16x16x64_bf16 v[24:27], v[64:67], v[32:39], v0\ns_waitcnt vmcnt(2)\n" \
    "v_smfmac_f32_16x16x64_bf16 v[24:27], v[84:87], v[40:47], v0\ns_waitcnt vmcnt(1)\nv_smfmac_f32_16x16x64_bf16 v[24:27], v[104:107], v[48:55], v0\ns_waitcnt vmcnt(0)\n" \
    "v_smfmac_f32_16x16x64_bf16 v[24:27], v[124:127], v[56:63], v0\ns_nop 7\nv_permlane32_swap_b32 v24, v26\nv_permlane32_swap_b32 v25, v27\n" \
    "v_pk_add_f32 v[24:25], v[24:25], v[26:27]\nv_cvt_pk_bf16_f32 v26, v24, v25\nds_write_b32 v3, v26\nds_read_b64_tr_b16 v[28:29], v4\n" \
    "ds_read_b64_tr_b16 v[30:31], v4 offset:64\nv_cmp_u_f32 vcc, v24, v25\ns_nop 0\ns_cbranch_vccnz .Lr17_%=\n.Lr16_%=:\ns_waitcnt lgkmcnt(0)\n" \
    "buffer_store_dwordx4 v[28:31], v6, s[64:67], s92 offen sc1\nbuffer_store_dwordx4 v[28:31], v6, s[64:67], s77 offen sc1\n.Lr10_%=:\ns_memrealtime s[72:73]\n" \
    "s_waitcnt lgkmcnt(0)\ns_add_u32 s76, s56, s35\n.Lr36_%=:\ns_sub_u32 s69, s76, s72\ns_cmp_ge_i32 s69, s37\ns_cbranch_scc1 .Lr37_%=\ns_add_u32 s76, s76, s38\n" \
    "s_xor_b32 s57, s57, 128\ns_branch .Lr36_%=\n.Lr37_%=:\ns_add_u32 s76, s76, s34\ns_add_u32 s76, s76, s41\ns_cmp_ge_u32 s32, 57\ns_cselect_b32 s41, -8, 0\n" \
    "s_lshl_b32 s74, s76, 8\ns_add_u32 s74, s74, s57\ns_add_u32 s74, s74, s32\ns_add_u32 s74, s74, 1\nv_mov_b32 v253, s74\nds_write_b32 v10, v253\ns_sub_u32 s78, s76, s40\n" \
    "s_add_u32 s76, s76, s35\ns_cmp_ge_u32 s32, 60\ns_cselect_b32 s79, 60, s39\ns_cmp_eq_u32 s32, 63\ns_cselect_b32 s79, s61, s79\ns_add_u32 s79, s79, s56\n" \
    "s_cmp_ge_u32 s32, 62\ns_cselect_b32 s76, s79, s76\ns_cmp_eq_u32 s48, 0\ns_cbranch_scc1 .Lr38_%=\ns_lshl_b32 s74, s76, 8\ns_add_u32 s74, s74, s57\ns_add_u32 s74, s74, s32\n" \
    "s_add_u32 s74, s74, 2\nv_mov_b32 v253, s74\ns_xor_b32 s69, s48, 0\ns_mov_b64 exec, 1\nbuffer_store_dword v253, off, s[64:67], s69 sc1\ns_mov_b64 exec, -1\n.Lr38_%=:\n" \
    "s_xor_b32 s69, s49, 0x20000\ns_add_u32 s0, s94, s69\ns_xor_b32 s1, s0, 0x4000\ns_xor_b32 s2, s0, 0x8000\ns_xor_b32 s3, s0, 0xc000\ns_xor_b32 s69, s49, 0x30000\n" \
    "s_add_u32 s4, s94, s69\ns_xor_b32 s5, s4, 0x4000\ns_xor_b32 s6, s4, 0x8000\ns_xor_b32 s7, s4, 0xc000\ns_xor_b32 s69, s49, 0x0\ns_add_u32 s8, s94, s69\n" \
    "s_xor_b32 s9, s8, 0x4000\ns_xor_b32 s10, s8, 0x8000\ns_xor_b32 s11, s8, 0xc000\ns_xor_b32 s69, s49, 0x10000\ns_add_u32 s12, s94, s69\ns_xor_b32 s13, s12, 0x4000\n" \
    "s_xor_b32 s14, s12, 0x8000\ns_xor_b32 s15, s12, 0xc000\ns_mov_b32 s68, 0\ns_mov_b32 s80, 0\n.Lr39_%=:\ns_memrealtime s[72:73]\ns_waitcnt lgkmcnt(0)\n" \
    "s_sub_u32 s72, s72, s79\ns_cmp_lt_i32 s72, 0\ns_cbranch_scc1 .Lr39_%=\ns_cmp_lg_u32 s32, 63\ns_cbranch_scc1 .Lr40_%=\n" \
    "buffer_load_dwordx4 v[56:59], v1, s[64:67], s19 offen offset:0 sc1\nbuffer_load_dwordx4 v[60:63], v1, s[64:67], s19 offen offset:1024 sc1\n.Lr40_%=:\n.Lr11_%=:\n" \
    "v_mov_b64 v[24:25], 0\nv_mov_b64 v[26:27], 0\nv_mov_b64 v[28:29], 0\nv_mov_b64 v[30:31], 0\nv_mov_b64 v[228:229], 0\nv_mov_b64 v[230:231], 0\nv_mov_b64 v[232:233], 0\n" \
    "v_mov_b64 v[234:235], 0\nv_mov_b32 v252, 0\ns_add_u32 s76, s94, 5120\nbuffer_load_dword v250, off, s[64:67], s76 sc1\ns_lshr_b32 s69, s32, 2\ns_cmp_eq_u32 s69, 1\n" \
    "s_cselect_b32 s35, s63, s35\nbuffer_load_dwordx4 v[64:67], v2, s[64:67], s0 offen offset:0 sc1\ns_xor_b32 s0, s0, s81\n" \
    "buffer_load_dwordx4 v[84:87], v2, s[64:67], s1 offen offset:0 sc1\ns_xor_b32 s1, s1, s81\nbuffer_load_dwordx4 v[104:107], v2, s[64:67], s2 offen offset:0 sc1\n" \
    "s_xor_b32 s2, s2, s81\nbuffer_load_dwordx4 v[124:127], v2, s[64:67], s3 offen offset:0 sc1\ns_xor_b32 s3, s3, s81\n" \
    "buffer_load_dwordx4 v[144:147], v2, s[64:67], s4 offen offset:0 sc1\ns_xor_b32 s4, s4, s81\nbuffer_load_dwordx4 v[148:151], v2, s[64:67], s5 offen offset:0 sc1\n" \
    "s_xor_b32 s5, s5, s81\nbuffer_load_dwordx4 v[152:155], v2, s[64:67], s6 offen offset:0 sc1\ns_xor_b32 s6, s6, s81\n" \
    "buffer_load_dwordx4 v[156:159], v2, s[64:67], s7 offen offset:0 sc1\ns_xor_b32 s7, s7, s81\nbuffer_load_dwordx4 v[160:163], v2, s[64:67], s8 offen offset:0 sc1\n" \
    "s_xor_b32 s8, s8, s81\nbuffer_load_dwordx4 v[164:167], v2, s[64:67], s9 offen offset:0 sc1\ns_xor_b32 s9, s9, s81\n" \
    "buffer_load_dwordx4 v[168:171], v2, s[64:67], s10 offen offset:0 sc1\ns_xor_b32 s10, s10, s81\nbuffer_load_dwordx4 v[172:175], v2, s[64:67], s11 offen offset:0 sc1\n" \
    "s_xor_b32 s11, s11, s81\nbuffer_load_dwordx4 v[176:179], v2, s[64:67], s12 offen offset:0 sc1\ns_xor_b32 s12, s12, s81\n" \
    "buffer_load_dwordx4 v[180:183], v2, s[64:67], s13 offen offset:0 sc1\ns_xor_b32 s13, s13, s81\nbuffer_load_dwordx4 v[184:187], v2, s[64:67], s14 offen offset:0 sc1\n" \
    "s_xor_b32 s14, s14, s81\nbuffer_load_dwordx4 v[188:191], v2, s[64:67], s15 offen offset:0 sc1\ns_xor_b32 s15, s15, s81\n" \
    "buffer_load_dwordx4 v[68:71], v1, s[64:67], s0 offen offset:0 sc1\nbuffer_load_dwordx4 v[72:75], v1, s[64:67], s0 offen offset:1024 sc1\n" \
    "buffer_load_dwordx4 v[76:79], v1, s[64:67], s0 offen offset:2048 sc1\nbuffer_load_dwordx4 v[80:83], v1, s[64:67], s0 offen offset:3072 sc1\n" \
    "buffer_load_dwordx4 v[88:91], v1, s[64:67], s1 offen offset:0 sc1\nbuffer_load_dwordx4 v[92:95], v1, s[64:67], s1 offen offset:1024 sc1\n" \
    "buffer_load_dwordx4 v[96:99], v1, s[64:67], s1 offen offset:2048 sc1\nbuffer_load_dwordx4 v[100:103], v1, s[64:67], s1 offen offset:3072 sc1\n" \
    "buffer_load_dwordx4 v[108:111], v1, s[64:67], s2 offen offset:0 sc1\nbuffer_load_dwordx4 v[112:115], v1, s[64:67], s2 offen offset:1024 sc1\n" \
    "buffer_load_dwordx4 v[116:119], v1, s[64:67], s2 offen offset:2048 sc1\nbuffer_load_dwordx4 v[120:123], v1, s[64:67], s2 offen offset:3072 sc1\n" \
    "buffer_load_dwordx4 v[128:131], v1, s[64:67], s3 offen offset:0 sc1\nbuffer_load_dwordx4 v[132:135], v1, s[64:67], s3 offen offset:1024 sc1\n" \
    "buffer_load_dwordx4 v[136:139], v1, s[64:67], s3 offen offset:2048 sc1\nbuffer_load_dwordx4 v[140:143], v1, s[64:67], s3 offen offset:3072 sc1\ns_sub_u32 s69, s32, s68\n" \
    "s_cmp_lg_u32 s69, 63\ns_cbranch_scc1 .Lr42_%=\ns_waitcnt vmcnt(32)\nv_permlane16_swap_b32 v32, v33\nv_permlane16_swap_b32 v34, v35\nv_permlane16_swap_b32 v36, v37\n" \
    "v_permlane16_swap_b32 v38, v39\nv_permlane16_swap_b32 v40, v41\nv_permlane16_swap_b32 v42, v43\nv_permlane16_swap_b32 v44, v45\nv_permlane16_swap_b32 v46, v47\n" \
    "v_permlane16_swap_b32 v48, v49\nv_permlane16_swap_b32 v50, v51\nv_permlane16_swap_b32 v52, v53\nv_permlane16_swap_b32 v54, v55\nv_permlane16_swap_b32 v56, v57\n" \
    "v_permlane16_swap_b32 v58, v59\nv_permlane16_swap_b32 v60, v61\nv_permlane16_swap_b32 v62, v63\n.Lr42_%=:\ns_waitcnt vmcnt(30)\n" \
    "v_smfmac_f32_16x16x64_bf16 v[24:27], v[64:67], v[32:39], v0\nbuffer_load_dwordx4 a[128:131], v1, s[64:67], s4 offen offset:0 sc1\nv_readfirstlane_b32 s34, v250\n" \
    "v_smfmac_f32_16x16x64_bf16 v[24:27], v[84:87], v[40:47], v0\nbuffer_load_dwordx4 a[132:135], v1, s[64:67], s4 offen offset:1024 sc1\ns_waitcnt vmcnt(30)\n" \
    "v_smfmac_f32_16x16x64_bf16 v[24:27], v[104:107], v[48:55], v0\nbuffer_load_dwordx4 a[136:139], v1, s[64:67], s4 offen offset:2048 sc1\nv_max3_u32 v250, v250, v64, v84\n" \
    "v_smfmac_f32_16x16x64_bf16 v[24:27], v[124:127], v[56:63], v0\nbuffer_load_dwordx4 a[140:143], v1, s[64:67], s4 offen offset:3072 sc1\ns_waitcnt vmcnt(30)\n" \
    "v_max3_u32 v250, v250, v104, v124\nv_smfmac_f32_16x16x64_bf16 v[28:31], v[144:147], v[32:39], v0\nbuffer_load_dwordx4 a[144:147], v1, s[64:67], s5 offen offset:0 sc1\n" \
    "v_smfmac_f32_16x16x64_bf16 v[28:31], v[148:151], v[40:47], v0\nbuffer_load_dwordx4 a[148:151], v1, s[64:67], s5 offen offset:1024 sc1\ns_waitcnt vmcnt(30)\n" \
    "v_smfmac_f32_16x16x64_bf16 v[28:31], v[152:155], v[48:55], v0\nbuffer_load_dwordx4 a[152:155], v1, s[64:67], s5 offen offset:2048 sc1\nv_max3_u32 v250, v250, v144, v148\n" \
    "v_smfmac_f32_16x16x64_bf16 v[28:31], v[156:159], v[56:63], v0\nbuffer_load_dwordx4 a[156:159], v1, s[64:67], s5 offen offset:3072 sc1\ns_waitcnt vmcnt(30)\n" \
    "v_max3_u32 v250, v250, v152, v156\nv_smfmac_f32_16x16x64_bf16 v[228:231], v[160:163], v[32:39], v0\nbuffer_load_dwordx4 a[160:163], v1, s[64:67], s6 offen offset:0 sc1\n" \
    "v_smfmac_f32_16x16x64_bf16 v[228:231], v[164:167], v[40:47], v0\nbuffer_load_dwordx4 a[164:167], v1, s[64:67], s6 offen offset:1024 sc1\ns_waitcnt vmcnt(30)\n" \
    "v_smfmac_f32_16x16x64_bf16 v[228:231], v[168:171], v[48:55], v0\nbuffer_load_dwordx4 a[168:171], v1, s[64:67], s6 offen offset:2048 sc1\nv_max3_u32 v250, v250, v160, v164\n" \
    "v_smfmac_f32_16x16x64_bf16 v[228:231], v[172:175], v[56:63], v0\nbuffer_load_dwordx4 a[172:175], v1, s[64:67], s6 offen offset:3072 sc1\ns_waitcnt vmcnt(30)\n" \
    "v_max3_u32 v250, v250, v168, v172\nv_smfmac_f32_16x16x64_bf16 v[232:235], v[176:179], v[32:39], v0\nbuffer_load_dwordx4 a[176:179], v1, s[64:67], s7 offen offset:0 sc1\n" \
    "v_smfmac_f32_16x16x64_bf16 v[232:235], v[180:183], v[40:47], v0\nbuffer_load_dwordx4 a[180:183], v1, s[64:67], s7 offen offset:1024 sc1\ns_waitcnt vmcnt(30)\n" \
    "v_smfmac_f32_16x16x64_bf16 v[232:235], v[184:187], v[48:55], v0\nbuffer_load_dwordx4 a[184:187], v1, s[64:67], s7 offen offset:2048 sc1\nv_max3_u32 v250, v250, v176, v180\n" \
    "v_smfmac_f32_16x16x64_bf16 v[232:235], v[188:191], v[56:63], v0\nbuffer_load_dwordx4 a[188:191], v1, s[64:67], s7 offen offset:3072 sc1\ns_waitcnt vmcnt(30)\n" \
    "v_max3_u32 v250, v250, v184, v188\nv_smfmac_f32_16x16x64_bf16 v[24:27], v[68:71], a[0:7], v0\nbuffer_load_dwordx4 a[192:195], v1, s[64:67], s8 offen offset:0 sc1\n" \
    "v_cmp_eq_u32 vcc, -1, v250\nv_smfmac_f32_16x16x64_bf16 v[24:27], v[72:75], a[8:15], v0\nbuffer_load_dwordx4 a[196:199], v1, s[64:67], s8 offen offset:1024 sc1\n" \
    "s_waitcnt vmcnt(30)\nv_smfmac_f32_16x16x64_bf16 v[24:27], v[76:79], a[16:23], v0\nbuffer_load_dwordx4 a[200:203], v1, s[64:67], s9 offen offset:0 sc1\n" \
    "s_cbranch_vccnz .Lr41_%=\nv_smfmac_f32_16x16x64_bf16 v[24:27], v[80:83], a[24:31], v0\nbuffer_load_dwordx4 a[204:207], v1, s[64:67], s9 offen offset:1024 sc1\n" \
    "s_waitcnt vmcnt(30)\nv_smfmac_f32_16x16x64_bf16 v[24:27], v[88:91], a[32:39], v0\nbuffer_load_dwordx4 a[208:211], v1, s[64:67], s10 offen offset:0 sc1\n" \
    "v_smfmac_f32_16x16x64_bf16 v[24:27], v[92:95], a[40:47], v0\nbuffer_load_dwordx4 a[212:215], v1, s[64:67], s10 offen offset:1024 sc1\ns_waitcnt vmcnt(30)\n" \
    "v_smfmac_f32_16x16x64_bf16 v[24:27], v[96:99], a[48:55], v0\nbuffer_load_dwordx4 a[216:219], v1, s[64:67], s11 offen offset:0 sc1\n" \
    "v_smfmac_f32_16x16x64_bf16 v[24:27], v[100:103], a[56:63], v0\nbuffer_load_dwordx4 a[220:223], v1, s[64:67], s11 offen offset:1024 sc1\ns_waitcnt vmcnt(30)\n" \
    "v_smfmac_f32_16x16x64_bf16 v[24:27], v[108:111], a[64:71], v0\nbuffer_load_dwordx4 a[224:227], v1, s[64:67], s12 offen offset:0 sc1\n" \
    "v_smfmac_f32_16x16x64_bf16 v[24:27], v[112:115], a[72:79], v0\nbuffer_load_dwordx4 a[228:231], v1, s[64:67], s12 offen offset:1024 sc1\ns_waitcnt vmcnt(30)\n" \
    "v_smfmac_f32_16x16x64_bf16 v[24:27], v[116:119], a[80:87], v0\nbuffer_load_dwordx4 a[232:235], v1, s[64:67], s13 offen offset:0 sc1\n" \
    "v_smfmac_f32_16x16x64_bf16 v[24:27], v[120:123], a[88:95], v0\nbuffer_load_dwordx4 a[236:239], v1, s[64:67], s13 offen offset:1024 sc1\ns_waitcnt vmcnt(30)\n" \
    "v_smfmac_f32_16x16x64_bf16 v[24:27], v[128:131], a[96:103], v0\nbuffer_load_dwordx4 a[240:243], v1, s[64:67], s14 offen offset:0 sc1\n" \
    "v_smfmac_f32_16x16x64_bf16 v[24:27], v[132:135], a[104:111], v0\nbuffer_load_dwordx4 a[244:247], v1, s[64:67], s14 offen offset:1024 sc1\ns_waitcnt vmcnt(30)\n" \
    "v_smfmac_f32_16x16x64_bf16 v[24:27], v[136:139], a[112:119], v0\nbuffer_load_dwordx4 a[248:251], v1, s[64:67], s15 offen offset:0 sc1\n" \
    "v_smfmac_f32_16x16x64_bf16 v[24:27], v[140:143], a[120:127], v0\nbuffer_load_dwordx4 a[252:255], v1, s[64:67], s15 offen offset:1024 sc1\ns_nop 1\ns_waitcnt vmcnt(30)\n" \
    "v_smfmac_f32_16x16x64_bf16 v[28:31], a[128:131], a[0:7], v0\ns_nop 2\nv_permlane32_swap_b32 v24, v26\nv_smfmac_f32_16x16x64_bf16 v[28:31], a[132:135], a[8:15], v0\n" \
    "s_waitcnt vmcnt(28)\nv_permlane32_swap_b32 v25, v27\nv_smfmac_f32_16x16x64_bf16 v[28:31], a[136:139], a[16:23], v0\nv_add_f32 v24, v24, v26\n" \
    "v_smfmac_f32_16x16x64_bf16 v[28:31], a[140:143], a[24:31], v0\ns_waitcnt vmcnt(26)\nv_add_f32 v25, v25, v27\nv_smfmac_f32_16x16x64_bf16 v[28:31], a[144:147], a[32:39], v0\n" \
    "v_fma_f32 v252, v24, v25, v252\nv_smfmac_f32_16x16x64_bf16 v[28:31], a[148:151], a[40:47], v0\ns_waitcnt vmcnt(24)\nv_cvt_pk_bf16_f32 v24, v24, v25\n" \
    "v_smfmac_f32_16x16x64_bf16 v[28:31], a[152:155], a[48:55], v0\nds_write_b32 v3, v24 offset:512\nv_smfmac_f32_16x16x64_bf16 v[28:31], a[156:159], a[56:63], v0\n" \
    "s_waitcnt vmcnt(22)\nv_smfmac_f32_16x16x64_bf16 v[28:31], a[160:163], a[64:71], v0\nv_smfmac_f32_16x16x64_bf16 v[28:31], a[164:167], a[72:79], v0\ns_waitcnt vmcnt(20)\n" \
    "v_smfmac_f32_16x16x64_bf16 v[28:31], a[168:171], a[80:87], v0\nv_smfmac_f32_16x16x64_bf16 v[28:31], a[172:175], a[88:95], v0\ns_waitcnt vmcnt(18)\n" \
    "v_smfmac_f32_16x16x64_bf16 v[28:31], a[176:179], a[96:103], v0\nv_smfmac_f32_16x16x64_bf16 v[28:31], a[180:183], a[104:111], v0\ns_waitcnt vmcnt(16)\n" \
    "v_smfmac_f32_16x16x64_bf16 v[28:31], a[184:187], a[112:119], v0\nv_smfmac_f32_16x16x64_bf16 v[28:31], a[188:191], a[120:127], v0\ns_nop 2\ns_waitcnt vmcnt(14)\n" \
    "v_smfmac_f32_16x16x64_bf16 v[228:231], a[192:195], a[0:7], v0\ns_nop 2\nv_permlane32_swap_b32 v28, v30\nv_smfmac_f32_16x16x64_bf16 v[228:231], a[196:199], a[8:15], v0\n" \
    "s_waitcnt vmcnt(12)\nv_permlane32_swap_b32 v29, v31\nv_smfmac_f32_16x16x64_bf16 v[228:231], a[200:203], a[32:39], v0\nv_add_f32 v28, v28, v30\n" \
    "v_smfmac_f32_16x16x64_bf16 v[228:231], a[204:207], a[40:47], v0\ns_waitcnt vmcnt(10)\nv_add_f32 v29, v29, v31\n" \
    "v_smfmac_f32_16x16x64_bf16 v[228:231], a[208:211], a[64:71], v0\nv_fma_f32 v252, v28, v29, v252\nv_smfmac_f32_16x16x64_bf16 v[228:231], a[212:215], a[72:79], v0\n" \
    "s_waitcnt vmcnt(8)\nv_cvt_pk_bf16_f32 v28, v28, v29\nv_smfmac_f32_16x16x64_bf16 v[228:231], a[216:219], a[96:103], v0\nds_write_b32 v3, v28 offset:768\n" \
    "v_smfmac_f32_16x16x64_bf16 v[228:231], a[220:223], a[104:111], v0\ns_nop 2\ns_waitcnt vmcnt(6)\nv_smfmac_f32_16x16x64_bf16 v[232:235], a[224:227], a[0:7], v0\ns_nop 2\n" \
    "v_permlane32_swap_b32 v228, v230\nv_smfmac_f32_16x16x64_bf16 v[232:235], a[228:231], a[8:15], v0\ns_waitcnt vmcnt(4)\nv_permlane32_swap_b32 v229, v231\n" \
    "v_smfmac_f32_16x16x64_bf16 v[232:235], a[232:235], a[32:39], v0\nv_add_f32 v228, v228, v230\nv_smfmac_f32_16x16x64_bf16 v[232:235], a[236:239], a[40:47], v0\n" \
    "s_waitcnt vmcnt(2)\nv_add_f32 v229, v229, v231\nv_smfmac_f32_16x16x64_bf16 v[232:235], a[240:243], a[64:71], v0\nv_fma_f32 v252, v228, v229, v252\n" \
    "v_smfmac_f32_16x16x64_bf16 v[232:235], a[244:247], a[72:79], v0\ns_waitcnt vmcnt(0)\nv_cvt_pk_bf16_f32 v228, v228, v229\n" \
    "v_smfmac_f32_16x16x64_bf16 v[232:235], a[248:251], a[96:103], v0\nds_write_b32 v3, v228 offset:0\nv_smfmac_f32_16x16x64_bf16 v[232:235], a[252:255], a[104:111], v0\n" \
    "s_nop 7\nv_permlane32_swap_b32 v232, v234\nv_permlane32_swap_b32 v233, v235\nv_add_f32 v232, v232, v234\nv_add_f32 v233, v233, v235\nv_fma_f32 v252, v232, v233, v252\n" \
    "v_cvt_pk_bf16_f32 v232, v232, v233\nv_cmp_u_f32 vcc, v252, v252\nds_write_b32 v3, v232 offset:256\nds_read_b64_tr_b16 v[24:25], v5\n" \
    "ds_read_b64_tr_b16 v[26:27], v5 offset:64\ns_cbranch_vccnz .Lr12_%=\n.Lr18_%=:\ns_cmp_lt_u32 s32, 4\ns_cbranch_scc1 .Lr43_%=\n.Lr44_%=:\ns_memrealtime s[72:73]\n" \
    "v_lshlrev_b32 v72, 16, v224\nv_and_b32 v73, 0xffff0000, v224\nv_lshlrev_b32 v74, 16, v225\nv_and_b32 v75, 0xffff0000, v225\nv_lshlrev_b32 v76, 16, v226\n" \
    "v_and_b32 v77, 0xffff0000, v226\nv_lshlrev_b32 v78, 16, v227\nv_and_b32 v79, 0xffff0000, v227\ns_xor_b32 s77, s93, 0x2000\ns_waitcnt lgkmcnt(0)\ns_sub_u32 s78, s72, s78\n" \
    "s_cmp_lt_u32 s32, 60\ns_cselect_b32 s78, 0, s78\ns_cmp_ge_u32 s32, 62\ns_cselect_b32 s78, -1, s78\nv_lshlrev_b32 v64, 16, v24\nv_and_b32 v65, 0xffff0000, v24\n" \
    "v_lshlrev_b32 v66, 16, v25\nv_and_b32 v67, 0xffff0000, v25\nv_lshlrev_b32 v68, 16, v26\nv_and_b32 v69, 0xffff0000, v26\nv_lshlrev_b32 v70, 16, v27\n" \
    "v_and_b32 v71, 0xffff0000, v27\nv_mul_f32 v64, v64, v16\nv_mul_f32 v65, v65, v17\nv_mul_f32 v66, v66, v18\nv_mul_f32 v67, v67, v19\nv_mul_f32 v68, v68, v20\n" \
    "v_mul_f32 v69, v69, v21\nv_mul_f32 v70, v70, v22\nv_mul_f32 v71, v71, v23\nv_cvt_pk_bf16_f32 v64, 0, v64\nv_cvt_pk_bf16_f32 v65, 0, v65\nv_cvt_pk_bf16_f32 v66, 0, v66\n" \
    "v_cvt_pk_bf16_f32 v67, 0, v67\nv_cvt_pk_bf16_f32 v68, 0, v68\nv_cvt_pk_bf16_f32 v69, 0, v69\nv_cvt_pk_bf16_f32 v70, 0, v70\nv_cvt_pk_bf16_f32 v71, 0, v71\n" \
    "v_add_f32 v64, v64, v72\nv_add_f32 v65, v65, v73\nv_add_f32 v66, v66, v74\nv_add_f32 v67, v67, v75\nv_add_f32 v68, v68, v76\nv_add_f32 v69, v69, v77\n" \
    "v_add_f32 v70, v70, v78\nv_add_f32 v71, v71, v79\nv_cvt_pk_bf16_f32 v24, v64, v65\nv_cvt_pk_bf16_f32 v25, v66, v67\nv_cvt_pk_bf16_f32 v26, v68, v69\n" \
    "v_cvt_pk_bf16_f32 v27, v70, v71\nv_min_u32 v28, -2, v24\nv_min_u32 v29, -2, v25\nv_min_u32 v30, -2, v26\nv_min_u32 v31, -2, v27\ns_cmp_ge_i32 s78, 0\n" \
    "s_cbranch_scc1 .Lr45_%=\nbuffer_store_dwordx4 v[28:31], v1, s[64:67], s93 offen sc1\nbuffer_store_dwordx4 v[28:31], v1, s[64:67], s77 offen sc1\n.Lr45_%=:\n" \
    "v_lshlrev_b32 v64, 16, v24\nv_and_b32 v65, 0xffff0000, v24\nv_lshlrev_b32 v66, 16, v25\nv_and_b32 v67, 0xffff0000, v25\nv_lshlrev_b32 v68, 16, v26\n" \
    "v_and_b32 v69, 0xffff0000, v26\nv_lshlrev_b32 v70, 16, v27\nv_and_b32 v71, 0xffff0000, v27\nv_add_f32 v72, v64, v65\nv_add_f32 v73, v66, v67\nv_add_f32 v74, v68, v69\n" \
    "v_add_f32 v75, v70, v71\nv_add_f32 v72, v72, v73\nv_add_f32 v74, v74, v75\nv_add_f32 v76, v72, v74\nv_add_f32 v77, v72, v74\ns_nop 1\nv_permlane32_swap_b32 v76, v77\n" \
    "v_add_f32 v76, v76, v77\nv_mul_f32 v76, 0x3d800000, v76\nv_sub_f32 v64, v64, v76\nv_sub_f32 v65, v65, v76\nv_sub_f32 v66, v66, v76\nv_sub_f32 v67, v67, v76\n" \
    "v_sub_f32 v68, v68, v76\nv_sub_f32 v69, v69, v76\nv_sub_f32 v70, v70, v76\nv_sub_f32 v71, v71, v76\nv_mul_f32 v72, v64, v64\nv_mul_f32 v73, v65, v65\n" \
    "v_fmac_f32 v72, v66, v66\nv_fmac_f32 v73, v67, v67\nv_fmac_f32 v72, v68, v68\nv_fmac_f32 v73, v69, v69\nv_fmac_f32 v72, v70, v70\nv_fmac_f32 v73, v71, v71\n" \
    "v_add_f32 v74, v72, v73\nv_add_f32 v75, v72, v73\ns_nop 1\nv_permlane32_swap_b32 v74, v75\nv_add_f32 v77, v74, v75\nv_min_u32 v76, -2, v76\nv_min_u32 v77, -2, v77\n" \
    "s_cmp_lt_i32 s78, 0\ns_cbranch_scc1 .Lr46_%=\ns_add_u32 s76, s32, 2\n.Lr48_%=:\nds_read_b32 v253, v10\ns_waitcnt lgkmcnt(0)\nv_readfirstlane_b32 s74, v253\ns_nop 0\n" \
    "s_and_b32 s75, s74, 127\ns_cmp_lt_u32 s75, s76\ns_cbranch_scc1 .Lr49_%=\nbuffer_store_dwordx4 v[28:31], v1, s[64:67], s93 offen sc1\n" \
    "buffer_store_dwordx4 v[28:31], v1, s[64:67], s77 offen sc1\n.Lr46_%=:\nbuffer_store_dwordx2 v[76:77], v9, s[64:67], s93 offen sc1\ns_cmp_ge_u32 s32, 60\n" \
    "s_cbranch_scc1 .Lr50_%=\ns_add_u32 s76, s32, 2\n.Lr52_%=:\nds_read_b32 v253, v10\ns_waitcnt lgkmcnt(0)\nv_readfirstlane_b32 s74, v253\ns_nop 0\ns_and_b32 s75, s74, 127\n" \
    "s_cmp_lt_u32 s75, s76\ns_cbranch_scc1 .Lr53_%=\ns_cmp_ge_u32 s32, 56\ns_cbranch_scc1 .Lr50_%=\nv_mov_b64 v[80:81], -1\nv_mov_b64 v[82:83], -1\ns_add_u32 s69, s32, 9\n" \
    "s_and_b32 s69, s69, 15\ns_lshl_b32 s69, s69, 17\ns_add_u32 s92, s46, s69\ns_xor_b32 s74, s92, 0x1000\nbuffer_store_dwordx4 v[80:83], v6, s[64:67], s92 offen sc1\n" \
    "buffer_store_dwordx4 v[80:83], v6, s[64:67], s74 offen sc1\n.Lr50_%=:\nv_add_u32 v7, 0x40000, v7\ns_add_u32 s32, s32, 4\ns_cmp_lt_u32 s32, 64\ns_cbranch_scc1 .Lr2_%=\n" \
    "s_endpgm\n.Lr24_%=:\ns_sleep 16\nbuffer_load_dword v255, off, s[64:67], s79 offset:64 sc1\ns_waitcnt vmcnt(0)\ns_add_u32 s68, s68, 1\ns_and_b32 s69, s68, 63\n" \
    "s_cbranch_scc1 .Lr25_%=\ns_cmp_gt_u32 s68, 0x400000\ns_cbranch_scc1 .Lr1_%=\nbuffer_load_dword v253, off, s[64:67], 0 sc1\ns_waitcnt vmcnt(0)\n" \
    "v_readfirstlane_b32 s69, v253\ns_nop 0\ns_cmp_eq_u32 s69, 1\ns_cbranch_scc0 .Lr25_%=\ns_endpgm\n.Lr5_%=:\ns_sleep 2\ns_add_u32 s68, s68, 1\ns_and_b32 s69, s68, 63\n" \
    "s_cbranch_scc1 .Lr4_%=\ns_cmp_gt_u32 s68, 0x400000\ns_cbranch_scc1 .Lr1_%=\nbuffer_load_dword v253, off, s[64:67], 0 sc1\ns_waitcnt vmcnt(0)\n" \
    "v_readfirstlane_b32 s69, v253\ns_nop 0\ns_cmp_eq_u32 s69, 1\ns_cbranch_scc0 .Lr4_%=\ns_endpgm\n.Lr7_%=:\ns_sleep 6\ns_add_u32 s68, s68, 1\ns_and_b32 s69, s68, 63\n" \
    "s_cbranch_scc1 .Lr6_%=\ns_cmp_gt_u32 s68, 0x400000\ns_cbranch_scc1 .Lr1_%=\nbuffer_load_dword v253, off, s[64:67], 0 sc1\ns_waitcnt vmcnt(0)\n" \
    "v_readfirstlane_b32 s69, v253\ns_nop 0\ns_cmp_eq_u32 s69, 1\ns_cbranch_scc0 .Lr6_%=\ns_endpgm\n.Lr14_%=:\ns_sleep 2\ns_add_u32 s68, s68, 1\ns_and_b32 s69, s68, 63\n" \
    "s_cbranch_scc1 .Lr13_%=\ns_cmp_gt_u32 s68, 0x400000\ns_cbranch_scc1 .Lr1_%=\nbuffer_load_dword v253, off, s[64:67], 0 sc1\ns_waitcnt vmcnt(0)\n" \
    "v_readfirstlane_b32 s69, v253\ns_nop 0\ns_cmp_eq_u32 s69, 1\ns_cbranch_scc0 .Lr13_%=\ns_endpgm\n.Lr20_%=:\nbuffer_load_dword v253, off, s[64:67], s60 offset:128 sc1\n" \
    "s_waitcnt vmcnt(0)\nv_readfirstlane_b32 s74, v253\ns_nop 0\ns_cmp_eq_u32 s74, 0\ns_cbranch_scc1 .Lr21_%=\ns_andn2_b32 s56, s74, 3\ns_add_u32 s76, s56, s35\n" \
    "s_cmp_eq_u32 s48, 0\ns_cbranch_scc1 .Lr28_%=\ns_lshl_b32 s74, s76, 8\ns_add_u32 s74, s74, s57\ns_add_u32 s74, s74, s32\ns_add_u32 s74, s74, 1\nv_mov_b32 v253, s74\n" \
    "s_xor_b32 s69, s48, 512\ns_mov_b64 exec, 1\nbuffer_store_dword v253, off, s[64:67], s69 sc1\ns_mov_b64 exec, -1\n.Lr28_%=:\ns_branch .Lr3_%=\n.Lr21_%=:\ns_sleep 4\n" \
    "s_add_u32 s68, s68, 1\ns_and_b32 s69, s68, 63\ns_cbranch_scc1 .Lr20_%=\ns_cmp_gt_u32 s68, 0x400000\ns_cbranch_scc1 .Lr1_%=\nbuffer_load_dword v253, off, s[64:67], 0 sc1\n" \
    "s_waitcnt vmcnt(0)\nv_readfirstlane_b32 s69, v253\ns_nop 0\ns_cmp_eq_u32 s69, 1\ns_cbranch_scc0 .Lr20_%=\ns_endpgm\n.Lr17_%=:\ns_cmp_lt_u32 s68, 3\ns_cbranch_scc1 .Lr9_%=\n" \
    "v_max3_u32 v252, v32, v33, v34\nv_max3_u32 v253, v35, v36, v37\nv_max3_u32 v252, v252, v38, v39\nv_max3_u32 v253, v253, v40, v41\nv_max3_u32 v252, v252, v42, v43\n" \
    "v_max3_u32 v253, v253, v44, v45\nv_max3_u32 v252, v252, v46, v47\nv_max3_u32 v253, v253, v48, v49\nv_max3_u32 v252, v252, v50, v51\nv_max3_u32 v253, v253, v52, v53\n" \
    "v_max3_u32 v252, v252, v54, v55\nv_max3_u32 v253, v253, v56, v57\nv_max3_u32 v252, v252, v58, v59\nv_max3_u32 v253, v253, v60, v61\nv_max3_u32 v252, v252, v62, v63\n" \
    "v_max3_u32 v252, v252, v64, v65\nv_max3_u32 v253, v253, v66, v67\nv_max3_u32 v252, v252, v68, v69\nv_max3_u32 v253, v253, v70, v71\nv_max3_u32 v252, v252, v72, v73\n" \
    "v_max3_u32 v253, v253, v74, v75\nv_max3_u32 v252, v252, v76, v77\nv_max3_u32 v253, v253, v78, v79\nv_max3_u32 v252, v252, v80, v81\nv_max3_u32 v253, v253, v82, v83\n" \
    "v_max3_u32 v252, v252, v84, v85\nv_max3_u32 v253, v253, v86, v87\nv_max3_u32 v252, v252, v88, v89\nv_max3_u32 v253, v253, v90, v91\nv_max3_u32 v252, v252, v92, v93\n" \
    "v_max3_u32 v253, v253, v94, v95\nv_max3_u32 v252, v252, v96, v97\nv_max3_u32 v253, v253, v98, v99\nv_max3_u32 v252, v252, v100, v101\nv_max3_u32 v253, v253, v102, v103\n" \
    "v_max3_u32 v252, v252, v104, v105\nv_max3_u32 v253, v253, v106, v107\nv_max3_u32 v252, v252, v108, v109\nv_max3_u32 v253, v253, v110, v111\n" \
    "v_max3_u32 v252, v252, v112, v113\nv_max3_u32 v253, v253, v114, v115\nv_max3_u32 v252, v252, v116, v117\nv_max3_u32 v253, v253, v118, v119\n" \
    "v_max3_u32 v252, v252, v120, v121\nv_max3_u32 v253, v253, v122, v123\nv_max3_u32 v252, v252, v124, v125\nv_max3_u32 v253, v253, v126, v127\n" \
    "v_max3_u32 v252, v252, v128, v129\nv_max3_u32 v253, v253, v130, v131\nv_max3_u32 v252, v252, v132, v133\nv_max3_u32 v253, v253, v134, v135\n" \
    "v_max3_u32 v252, v252, v136, v137\nv_max3_u32 v253, v253, v138, v139\nv_max3_u32 v252, v252, v140, v141\nv_max3_u32 v253, v253, v142, v143\nv_max_u32 v252, v252, v253\n" \
    "v_cmp_eq_u32 vcc, -1, v252\ns_nop 0\ns_cbranch_vccz .Lr16_%=\n.Lr9_%=:\ns_add_u32 s56, s56, s38\ns_xor_b32 s57, s57, 128\ns_xor_b32 s84, s84, 0x4000\n" \
    "s_xor_b32 s92, s92, 0x1000\ns_add_u32 s76, s56, s35\ns_cmp_eq_u32 s48, 0\ns_cbranch_scc1 .Lr35_%=\ns_lshl_b32 s74, s76, 8\ns_add_u32 s74, s74, s57\n" \
    "s_add_u32 s74, s74, s32\ns_add_u32 s74, s74, 1\nv_mov_b32 v253, s74\ns_xor_b32 s69, s48, 512\ns_mov_b64 exec, 1\nbuffer_store_dword v253, off, s[64:67], s69 sc1\n" \
    "s_mov_b64 exec, -1\n.Lr35_%=:\ns_add_u32 s68, s68, 1\ns_and_b32 s69, s68, 63\ns_cbranch_scc1 .Lr8_%=\ns_cmp_gt_u32 s68, 0x400000\ns_cbranch_scc1 .Lr1_%=\n" \
    "buffer_load_dword v253, off, s[64:67], 0 sc1\ns_waitcnt vmcnt(0)\nv_readfirstlane_b32 s69, v253\ns_nop 0\ns_cmp_eq_u32 s69, 1\ns_cbranch_scc0 .Lr8_%=\ns_endpgm\n.Lr41_%=:\n" \
    "s_waitcnt vmcnt(0)\ns_xor_b32 s0, s0, s81\ns_xor_b32 s1, s1, s81\ns_xor_b32 s2, s2, s81\ns_xor_b32 s3, s3, s81\ns_xor_b32 s4, s4, s81\ns_xor_b32 s5, s5, s81\n" \
    "s_xor_b32 s6, s6, s81\ns_xor_b32 s7, s7, s81\ns_xor_b32 s8, s8, s81\ns_xor_b32 s9, s9, s81\ns_xor_b32 s10, s10, s81\ns_xor_b32 s11, s11, s81\ns_xor_b32 s12, s12, s81\n" \
    "s_xor_b32 s13, s13, s81\ns_xor_b32 s14, s14, s81\ns_xor_b32 s15, s15, s81\ns_add_u32 s68, s68, 1\ns_and_b32 s69, s68, 63\ns_cbranch_scc1 .Lr11_%=\n" \
    "s_cmp_gt_u32 s68, 0x400000\ns_cbranch_scc1 .Lr1_%=\nbuffer_load_dword v253, off, s[64:67], 0 sc1\ns_waitcnt vmcnt(0)\nv_readfirstlane_b32 s69, v253\ns_nop 0\n" \
    "s_cmp_eq_u32 s69, 1\ns_cbranch_scc0 .Lr11_%=\ns_endpgm\n.Lr12_%=:\ns_cmp_lg_u32 s80, 0\ns_cbranch_scc1 .Lr18_%=\ns_sleep 4\n" \
    "buffer_load_dwordx4 v[32:35], v1, s[64:67], s16 offen offset:0 sc1\nbuffer_load_dwordx4 v[36:39], v1, s[64:67], s16 offen offset:1024 sc1\n" \
    "buffer_load_dwordx4 v[40:43], v1, s[64:67], s17 offen offset:0 sc1\nbuffer_load_dwordx4 v[44:47], v1, s[64:67], s17 offen offset:1024 sc1\n" \
    "buffer_load_dwordx4 v[48:51], v1, s[64:67], s18 offen offset:0 sc1\nbuffer_load_dwordx4 v[52:55], v1, s[64:67], s18 offen offset:1024 sc1\n" \
    "buffer_load_dwordx4 v[56:59], v1, s[64:67], s19 offen offset:0 sc1\nbuffer_load_dwordx4 v[60:63], v1, s[64:67], s19 offen offset:1024 sc1\ns_waitcnt vmcnt(0)\n" \
    "v_permlane16_swap_b32 v32, v33\nv_permlane16_swap_b32 v34, v35\nv_permlane16_swap_b32 v36, v37\nv_permlane16_swap_b32 v38, v39\nv_permlane16_swap_b32 v40, v41\n" \
    "v_permlane16_swap_b32 v42, v43\nv_permlane16_swap_b32 v44, v45\nv_permlane16_swap_b32 v46, v47\nv_permlane16_swap_b32 v48, v49\nv_permlane16_swap_b32 v50, v51\n" \
    "v_permlane16_swap_b32 v52, v53\nv_permlane16_swap_b32 v54, v55\nv_permlane16_swap_b32 v56, v57\nv_permlane16_swap_b32 v58, v59\nv_permlane16_swap_b32 v60, v61\n" \
    "v_permlane16_swap_b32 v62, v63\nv_max3_u32 v252, v32, v33, v34\nv_max3_u32 v253, v35, v36, v37\nv_max3_u32 v252, v252, v38, v39\nv_max3_u32 v253, v253, v40, v41\n" \
    "v_max3_u32 v252, v252, v42, v43\nv_max3_u32 v253, v253, v44, v45\nv_max3_u32 v252, v252, v46, v47\nv_max3_u32 v253, v253, v48, v49\nv_max3_u32 v252, v252, v50, v51\n" \
    "v_max3_u32 v253, v253, v52, v53\nv_max3_u32 v252, v252, v54, v55\nv_max3_u32 v253, v253, v56, v57\nv_max3_u32 v252, v252, v58, v59\nv_max3_u32 v253, v253, v60, v61\n" \
    "v_max3_u32 v252, v252, v62, v63\ns_mov_b32 s76, s94\n.Lr19_%=:\ns_add_u32 s84, s76, 0x0\ns_add_u32 s85, s76, 0x4000\ns_add_u32 s86, s76, 0x8000\n" \
    "s_add_u32 s87, s76, 0xc000\nbuffer_load_dwordx4 v[64:67], v2, s[64:67], s84 offen offset:0 sc1\nbuffer_load_dwordx4 v[68:71], v1, s[64:67], s84 offen offset:0 sc1\n" \
    "buffer_load_dwordx4 v[72:75], v1, s[64:67], s84 offen offset:1024 sc1\nbuffer_load_dwordx4 v[76:79], v1, s[64:67], s84 offen offset:2048 sc1\n" \
    "buffer_load_dwordx4 v[80:83], v1, s[64:67], s84 offen offset:3072 sc1\nbuffer_load_dwordx4 v[84:87], v2, s[64:67], s85 offen offset:0 sc1\n" \
    "buffer_load_dwordx4 v[88:91], v1, s[64:67], s85 offen offset:0 sc1\nbuffer_load_dwordx4 v[92:95], v1, s[64:67], s85 offen offset:1024 sc1\n" \
    "buffer_load_dwordx4 v[96:99], v1, s[64:67], s85 offen offset:2048 sc1\nbuffer_load_dwordx4 v[100:103], v1, s[64:67], s85 offen offset:3072 sc1\n" \
    "buffer_load_dwordx4 v[104:107], v2, s[64:67], s86 offen offset:0 sc1\nbuffer_load_dwordx4 v[108:111], v1, s[64:67], s86 offen offset:0 sc1\n" \
    "buffer_load_dwordx4 v[112:115], v1, s[64:67], s86 offen offset:1024 sc1\nbuffer_load_dwordx4 v[116:119], v1, s[64:67], s86 offen offset:2048 sc1\n" \
    "buffer_load_dwordx4 v[120:123], v1, s[64:67], s86 offen offset:3072 sc1\nbuffer_load_dwordx4 v[124:127], v2, s[64:67], s87 offen offset:0 sc1\n" \
    "buffer_load_dwordx4 v[128:131], v1, s[64:67], s87 offen offset:0 sc1\nbuffer_load_dwordx4 v[132:135], v1, s[64:67], s87 offen offset:1024 sc1\n" \
    "buffer_load_dwordx4 v[136:139], v1, s[64:67], s87 offen offset:2048 sc1\nbuffer_load_dwordx4 v[140:143], v1, s[64:67], s87 offen offset:3072 sc1\ns_waitcnt vmcnt(0)\n" \
    "v_max3_u32 v252, v252, v64, v65\nv_max3_u32 v253, v253, v66, v67\nv_max3_u32 v252, v252, v68, v69\nv_max3_u32 v253, v253, v70, v71\nv_max3_u32 v252, v252, v72, v73\n" \
    "v_max3_u32 v253, v253, v74, v75\nv_max3_u32 v252, v252, v76, v77\nv_max3_u32 v253, v253, v78, v79\nv_max3_u32 v252, v252, v80, v81\nv_max3_u32 v253, v253, v82, v83\n" \
    "v_max3_u32 v252, v252, v84, v85\nv_max3_u32 v253, v253, v86, v87\nv_max3_u32 v252, v252, v88, v89\nv_max3_u32 v253, v253, v90, v91\nv_max3_u32 v252, v252, v92, v93\n" \
    "v_max3_u32 v253, v253, v94, v95\nv_max3_u32 v252, v252, v96, v97\nv_max3_u32 v253, v253, v98, v99\nv_max3_u32 v252, v252, v100, v101\nv_max3_u32 v253, v253, v102, v103\n" \
    "v_max3_u32 v252, v252, v104, v105\nv_max3_u32 v253, v253, v106, v107\nv_max3_u32 v252, v252, v108, v109\nv_max3_u32 v253, v253, v110, v111\n" \
    "v_max3_u32 v252, v252, v112, v113\nv_max3_u32 v253, v253, v114, v115\nv_max3_u32 v252, v252, v116, v117\nv_max3_u32 v253, v253, v118, v119\n" \
    "v_max3_u32 v252, v252, v120, v121\nv_max3_u32 v253, v253, v122, v123\nv_max3_u32 v252, v252, v124, v125\nv_max3_u32 v253, v253, v126, v127\n" \
    "v_max3_u32 v252, v252, v128, v129\nv_max3_u32 v253, v253, v130, v131\nv_max3_u32 v252, v252, v132, v133\nv_max3_u32 v253, v253, v134, v135\n" \
    "v_max3_u32 v252, v252, v136, v137\nv_max3_u32 v253, v253, v138, v139\nv_max3_u32 v252, v252, v140, v141\nv_max3_u32 v253, v253, v142, v143\ns_add_u32 s76, s76, 0x10000\n" \
    "s_sub_u32 s69, s76, s94\ns_cmp_lt_u32 s69, 0x40000\ns_cbranch_scc1 .Lr19_%=\nv_max_u32 v252, v252, v253\nv_cmp_eq_u32 vcc, -1, v252\ns_nop 0\ns_cmp_eq_u64 vcc, 0\n" \
    "s_cselect_b32 s80, 1, 0\ns_xor_b32 s0, s0, s81\ns_xor_b32 s1, s1, s81\ns_xor_b32 s2, s2, s81\ns_xor_b32 s3, s3, s81\ns_xor_b32 s4, s4, s81\ns_xor_b32 s5, s5, s81\n" \
    "s_xor_b32 s6, s6, s81\ns_xor_b32 s7, s7, s81\ns_xor_b32 s8, s8, s81\ns_xor_b32 s9, s9, s81\ns_xor_b32 s10, s10, s81\ns_xor_b32 s11, s11, s81\ns_xor_b32 s12, s12, s81\n" \
    "s_xor_b32 s13, s13, s81\ns_xor_b32 s14, s14, s81\ns_xor_b32 s15, s15, s81\ns_add_u32 s68, s68, 1\ns_and_b32 s69, s68, 63\ns_cbranch_scc1 .Lr11_%=\n" \
    "s_cmp_gt_u32 s68, 0x400000\ns_cbranch_scc1 .Lr1_%=\nbuffer_load_dword v253, off, s[64:67], 0 sc1\ns_waitcnt vmcnt(0)\nv_readfirstlane_b32 s69, v253\ns_nop 0\n" \
    "s_cmp_eq_u32 s69, 1\ns_cbranch_scc0 .Lr11_%=\ns_endpgm\n.Lr43_%=:\nbuffer_load_dwordx4 v[16:19], v12, s[64:67], 0 offen offset:0 sc1\n" \
    "buffer_load_dwordx4 v[20:23], v12, s[64:67], 0 offen offset:16 sc1\ns_waitcnt vmcnt(0)\nv_max3_u32 v252, v16, v17, v18\nv_max3_u32 v253, v19, v20, v21\n" \
    "v_max3_u32 v252, v252, v22, v23\nv_max_u32 v252, v252, v253\nv_cmp_eq_u32 vcc, -1, v252\ns_nop 0\ns_cbranch_vccz .Lr44_%=\ns_sleep 8\ns_add_u32 s68, s68, 1\n" \
    "s_and_b32 s69, s68, 63\ns_cbranch_scc1 .Lr43_%=\ns_cmp_gt_u32 s68, 0x400000\ns_cbranch_scc1 .Lr1_%=\nbuffer_load_dword v253, off, s[64:67], 0 sc1\ns_waitcnt vmcnt(0)\n" \
    "v_readfirstlane_b32 s69, v253\ns_nop 0\ns_cmp_eq_u32 s69, 1\ns_cbranch_scc0 .Lr43_%=\ns_endpgm\n.Lr49_%=:\ns_sleep 2\ns_add_u32 s68, s68, 1\ns_and_b32 s69, s68, 63\n" \
    "s_cbranch_scc1 .Lr48_%=\ns_cmp_gt_u32 s68, 0x400000\ns_cbranch_scc1 .Lr1_%=\nbuffer_load_dword v253, off, s[64:67], 0 sc1\ns_waitcnt vmcnt(0)\n" \
    "v_readfirstlane_b32 s69, v253\ns_nop 0\ns_cmp_eq_u32 s69, 1\ns_cbranch_scc0 .Lr48_%=\ns_endpgm\n.Lr53_%=:\ns_sleep 2\ns_add_u32 s68, s68, 1\ns_and_b32 s69, s68, 63\n" \
    "s_cbranch_scc1 .Lr52_%=\ns_cmp_gt_u32 s68, 0x400000\ns_cbranch_scc1 .Lr1_%=\nbuffer_load_dword v253, off, s[64:67], 0 sc1\ns_waitcnt vmcnt(0)\n" \
    "v_readfirstlane_b32 s69, v253\ns_nop 0\ns_cmp_eq_u32 s69, 1\ns_cbranch_scc0 .Lr52_%=\ns_endpgm\n.Lr1_%=:\nv_mov_b32 v253, 1\nbuffer_store_dword v253, off, s[64:67], 0 sc1\n" \
    "s_endpgm\n"
#define ASM_F1 \
    "v_mov_b32 v128, 0x3f803f80\nv_mov_b32 v129, 0x3f803f80\nv_mov_b32 v130, 0x3f803f80\nv_mov_b32 v131, 0x3f803f80\nds_read_b128 v[64:67], v12 offset:0\n" \
    "ds_read_b128 v[68:71], v12 offset:16\nds_read_b128 v[72:75], v13 offset:0\nds_read_b128 v[80:83], v12 offset:64\nds_read_b128 v[84:87], v12 offset:80\n" \
    "ds_read_b128 v[88:91], v13 offset:32\nds_read_b128 v[96:99], v12 offset:128\nds_read_b128 v[100:103], v12 offset:144\nds_read_b128 v[104:107], v13 offset:64\n" \
    "v_accvgpr_read_b32 v112, a0\nv_accvgpr_read_b32 v113, a1\nv_accvgpr_read_b32 v114, a2\nv_accvgpr_read_b32 v115, a3\nv_lshlrev_b32 v116, 16, v112\n" \
    "v_and_b32 v117, 0xffff0000, v112\nv_lshlrev_b32 v118, 16, v113\nv_and_b32 v119, 0xffff0000, v113\nv_lshlrev_b32 v120, 16, v114\nv_and_b32 v121, 0xffff0000, v114\n" \
    "v_lshlrev_b32 v122, 16, v115\nv_and_b32 v123, 0xffff0000, v115\ns_waitcnt lgkmcnt(6)\nv_mul_f32 v116, v116, v64\nv_mul_f32 v117, v117, v65\nv_mul_f32 v118, v118, v66\n" \
    "v_mul_f32 v119, v119, v67\nv_mul_f32 v120, v120, v68\nv_mul_f32 v121, v121, v69\nv_mul_f32 v122, v122, v70\nv_mul_f32 v123, v123, v71\n" \
    "v_mfma_f32_32x32x16_bf16 v[32:47], a[0:3], v[72:75], v[32:47]\nv_cvt_pk_bf16_f32 v132, v116, v117\nv_cvt_pk_bf16_f32 v133, v118, v119\nv_cvt_pk_bf16_f32 v134, v120, v121\n" \
    "v_cvt_pk_bf16_f32 v135, v122, v123\nv_accvgpr_write_b32 a0, v132\nv_accvgpr_write_b32 a1, v133\nv_accvgpr_write_b32 a2, v134\nv_accvgpr_write_b32 a3, v135\n" \
    "v_mfma_f32_32x32x16_bf16 v[16:31], v[132:135], v[128:131], 0\nds_read_b128 v[64:67], v12 offset:192\nds_read_b128 v[68:71], v12 offset:208\n" \
    "ds_read_b128 v[72:75], v13 offset:96\nv_accvgpr_read_b32 v112, a4\nv_accvgpr_read_b32 v113, a5\nv_accvgpr_read_b32 v114, a6\nv_accvgpr_read_b32 v115, a7\n" \
    "v_lshlrev_b32 v116, 16, v112\nv_and_b32 v117, 0xffff0000, v112\nv_lshlrev_b32 v118, 16, v113\nv_and_b32 v119, 0xffff0000, v113\nv_lshlrev_b32 v120, 16, v114\n" \
    "v_and_b32 v121, 0xffff0000, v114\nv_lshlrev_b32 v122, 16, v115\nv_and_b32 v123, 0xffff0000, v115\ns_waitcnt lgkmcnt(6)\nv_mul_f32 v116, v116, v80\n" \
    "v_mul_f32 v117, v117, v81\nv_mul_f32 v118, v118, v82\nv_mul_f32 v119, v119, v83\nv_mul_f32 v120, v120, v84\nv_mul_f32 v121, v121, v85\nv_mul_f32 v122, v122, v86\n" \
    "v_mul_f32 v123, v123, v87\nv_mfma_f32_32x32x16_bf16 v[32:47], a[4:7], v[88:91], v[32:47]\nv_cvt_pk_bf16_f32 v136, v116, v117\nv_cvt_pk_bf16_f32 v137, v118, v119\n" \
    "v_cvt_pk_bf16_f32 v138, v120, v121\nv_cvt_pk_bf16_f32 v139, v122, v123\nv_accvgpr_write_b32 a4, v136\nv_accvgpr_write_b32 a5, v137\nv_accvgpr_write_b32 a6, v138\n" \
    "v_accvgpr_write_b32 a7, v139\nv_mfma_f32_32x32x16_bf16 v[16:31], v[136:139], v[128:131], v[16:31]\nds_read_b128 v[80:83], v12 offset:256\n" \
    "ds_read_b128 v[84:87], v12 offset:272\nds_read_b128 v[88:91], v13 offset:128\nv_accvgpr_read_b32 v112, a8\nv_accvgpr_read_b32 v113, a9\nv_accvgpr_read_b32 v114, a10\n" \
    "v_accvgpr_read_b32 v115, a11\nv_lshlrev_b32 v116, 16, v112\nv_and_b32 v117, 0xffff0000, v112\nv_lshlrev_b32 v118, 16, v113\nv_and_b32 v119, 0xffff0000, v113\n" \
    "v_lshlrev_b32 v120, 16, v114\nv_and_b32 v121, 0xffff0000, v114\nv_lshlrev_b32 v122, 16, v115\nv_and_b32 v123, 0xffff0000, v115\ns_waitcnt lgkmcnt(6)\n" \
    "v_mul_f32 v116, v116, v96\nv_mul_f32 v117, v117, v97\nv_mul_f32 v118, v118, v98\nv_mul_f32 v119, v119, v99\nv_mul_f32 v120, v120, v100\nv_mul_f32 v121, v121, v101\n" \
    "v_mul_f32 v122, v122, v102\nv_mul_f32 v123, v123, v103\nv_mfma_f32_32x32x16_bf16 v[32:47], a[8:11], v[104:107], v[32:47]\nv_cvt_pk_bf16_f32 v132, v116, v117\n" \
    "v_cvt_pk_bf16_f32 v133, v118, v119\nv_cvt_pk_bf16_f32 v134, v120, v121\nv_cvt_pk_bf16_f32 v135, v122, v123\nv_accvgpr_write_b32 a8, v132\nv_accvgpr_write_b32 a9, v133\n" \
    "v_accvgpr_write_b32 a10, v134\nv_accvgpr_write_b32 a11, v135\nv_mfma_f32_32x32x16_bf16 v[16:31], v[132:135], v[128:131], v[16:31]\nds_read_b128 v[96:99], v12 offset:320\n" \
    "ds_read_b128 v[100:103], v12 offset:336\nds_read_b128 v[104:107], v13 offset:160\nv_accvgpr_read_b32 v112, a12\nv_accvgpr_read_b32 v113, a13\nv_accvgpr_read_b32 v114, a14\n" \
    "v_accvgpr_read_b32 v115, a15\nv_lshlrev_b32 v116, 16, v112\nv_and_b32 v117, 0xffff0000, v112\nv_lshlrev_b32 v118, 16, v113\nv_and_b32 v119, 0xffff0000, v113\n" \
    "v_lshlrev_b32 v120, 16, v114\nv_and_b32 v121, 0xffff0000, v114\nv_lshlrev_b32 v122, 16, v115\nv_and_b32 v123, 0xffff0000, v115\ns_waitcnt lgkmcnt(6)\n" \
    "v_mul_f32 v116, v116, v64\nv_mul_f32 v117, v117, v65\nv_mul_f32 v118, v118, v66\nv_mul_f32 v119, v119, v67\nv_mul_f32 v120, v120, v68\nv_mul_f32 v121, v121, v69\n" \
    "v_mul_f32 v122, v122, v70\nv_mul_f32 v123, v123, v71\nv_mfma_f32_32x32x16_bf16 v[32:47], a[12:15], v[72:75], v[32:47]\nv_cvt_pk_bf16_f32 v136, v116, v117\n" \
    "v_cvt_pk_bf16_f32 v137, v118, v119\nv_cvt_pk_bf16_f32 v138, v120, v121\nv_cvt_pk_bf16_f32 v139, v122, v123\nv_accvgpr_write_b32 a12, v136\nv_accvgpr_write_b32 a13, v137\n" \
    "v_accvgpr_write_b32 a14, v138\nv_accvgpr_write_b32 a15, v139\nv_mfma_f32_32x32x16_bf16 v[16:31], v[136:139], v[128:131], v[16:31]\nds_read_b128 v[64:67], v12 offset:384\n" \
    "ds_read_b128 v[68:71], v12 offset:400\nds_read_b128 v[72:75], v13 offset:192\nv_accvgpr_read_b32 v112, a16\nv_accvgpr_read_b32 v113, a17\nv_accvgpr_read_b32 v114, a18\n" \
    "v_accvgpr_read_b32 v115, a19\nv_lshlrev_b32 v116, 16, v112\nv_and_b32 v117, 0xffff0000, v112\nv_lshlrev_b32 v118, 16, v113\nv_and_b32 v119, 0xffff0000, v113\n" \
    "v_lshlrev_b32 v120, 16, v114\nv_and_b32 v121, 0xffff0000, v114\nv_lshlrev_b32 v122, 16, v115\nv_and_b32 v123, 0xffff0000, v115\ns_waitcnt lgkmcnt(6)\n" \
    "v_mul_f32 v116, v116, v80\nv_mul_f32 v117, v117, v81\nv_mul_f32 v118, v118, v82\nv_mul_f32 v119, v119, v83\nv_mul_f32 v120, v120, v84\nv_mul_f32 v121, v121, v85\n" \
    "v_mul_f32 v122, v122, v86\nv_mul_f32 v123, v123, v87\nv_mfma_f32_32x32x16_bf16 v[32:47], a[16:19], v[88:91], v[32:47]\nv_cvt_pk_bf16_f32 v132, v116, v117\n" \
    "v_cvt_pk_bf16_f32 v133, v118, v119\nv_cvt_pk_bf16_f32 v134, v120, v121\nv_cvt_pk_bf16_f32 v135, v122, v123\nv_accvgpr_write_b32 a16, v132\nv_accvgpr_write_b32 a17, v133\n" \
    "v_accvgpr_write_b32 a18, v134\nv_accvgpr_write_b32 a19, v135\nv_mfma_f32_32x32x16_bf16 v[16:31], v[132:135], v[128:131], v[16:31]\nds_read_b128 v[80:83], v12 offset:448\n" \
    "ds_read_b128 v[84:87], v12 offset:464\nds_read_b128 v[88:91], v13 offset:224\nv_accvgpr_read_b32 v112, a20\nv_accvgpr_read_b32 v113, a21\nv_accvgpr_read_b32 v114, a22\n" \
    "v_accvgpr_read_b32 v115, a23\nv_lshlrev_b32 v116, 16, v112\nv_and_b32 v117, 0xffff0000, v112\nv_lshlrev_b32 v118, 16, v113\nv_and_b32 v119, 0xffff0000, v113\n" \
    "v_lshlrev_b32 v120, 16, v114\nv_and_b32 v121, 0xffff0000, v114\nv_lshlrev_b32 v122, 16, v115\nv_and_b32 v123, 0xffff0000, v115\ns_waitcnt lgkmcnt(6)\n" \
    "v_mul_f32 v116, v116, v96\nv_mul_f32 v117, v117, v97\nv_mul_f32 v118, v118, v98\nv_mul_f32 v119, v119, v99\nv_mul_f32 v120, v120, v100\nv_mul_f32 v121, v121, v101\n" \
    "v_mul_f32 v122, v122, v102\nv_mul_f32 v123, v123, v103\nv_mfma_f32_32x32x16_bf16 v[32:47], a[20:23], v[104:107], v[32:47]\nv_cvt_pk_bf16_f32 v136, v116, v117\n" \
    "v_cvt_pk_bf16_f32 v137, v118, v119\nv_cvt_pk_bf16_f32 v138, v120, v121\nv_cvt_pk_bf16_f32 v139, v122, v123\nv_accvgpr_write_b32 a20, v136\nv_accvgpr_write_b32 a21, v137\n" \
    "v_accvgpr_write_b32 a22, v138\nv_accvgpr_write_b32 a23, v139\nv_mfma_f32_32x32x16_bf16 v[16:31], v[136:139], v[128:131], v[16:31]\nds_read_b128 v[96:99], v12 offset:512\n" \
    "ds_read_b128 v[100:103], v12 offset:528\nds_read_b128 v[104:107], v13 offset:256\nv_accvgpr_read_b32 v112, a24\nv_accvgpr_read_b32 v113, a25\nv_accvgpr_read_b32 v114, a26\n" \
    "v_accvgpr_read_b32 v115, a27\nv_lshlrev_b32 v116, 16, v112\nv_and_b32 v117, 0xffff0000, v112\nv_lshlrev_b32 v118, 16, v113\nv_and_b32 v119, 0xffff0000, v113\n" \
    "v_lshlrev_b32 v120, 16, v114\nv_and_b32 v121, 0xffff0000, v114\nv_lshlrev_b32 v122, 16, v115\nv_and_b32 v123, 0xffff0000, v115\ns_waitcnt lgkmcnt(6)\n" \
    "v_mul_f32 v116, v116, v64\nv_mul_f32 v117, v117, v65\nv_mul_f32 v118, v118, v66\nv_mul_f32 v119, v119, v67\nv_mul_f32 v120, v120, v68\nv_mul_f32 v121, v121, v69\n" \
    "v_mul_f32 v122, v122, v70\nv_mul_f32 v123, v123, v71\nv_mfma_f32_32x32x16_bf16 v[32:47], a[24:27], v[72:75], v[32:47]\nv_cvt_pk_bf16_f32 v132, v116, v117\n" \
    "v_cvt_pk_bf16_f32 v133, v118, v119\nv_cvt_pk_bf16_f32 v134, v120, v121\nv_cvt_pk_bf16_f32 v135, v122, v123\nv_accvgpr_write_b32 a24, v132\nv_accvgpr_write_b32 a25, v133\n" \
    "v_accvgpr_write_b32 a26, v134\nv_accvgpr_write_b32 a27, v135\nv_mfma_f32_32x32x16_bf16 v[16:31], v[132:135], v[128:131], v[16:31]\nds_read_b128 v[64:67], v12 offset:576\n" \
    "ds_read_b128 v[68:71], v12 offset:592\nds_read_b128 v[72:75], v13 offset:288\nv_accvgpr_read_b32 v112, a28\nv_accvgpr_read_b32 v113, a29\nv_accvgpr_read_b32 v114, a30\n" \
    "v_accvgpr_read_b32 v115, a31\nv_lshlrev_b32 v116, 16, v112\nv_and_b32 v117, 0xffff0000, v112\nv_lshlrev_b32 v118, 16, v113\nv_and_b32 v119, 0xffff0000, v113\n" \
    "v_lshlrev_b32 v120, 16, v114\nv_and_b32 v121, 0xffff0000, v114\nv_lshlrev_b32 v122, 16, v115\nv_and_b32 v123, 0xffff0000, v115\ns_waitcnt lgkmcnt(6)\n" \
    "v_mul_f32 v116, v116, v80\nv_mul_f32 v117, v117, v81\nv_mul_f32 v118, v118, v82\nv_mul_f32 v119, v119, v83\nv_mul_f32 v120, v120, v84\nv_mul_f32 v121, v121, v85\n" \
    "v_mul_f32 v122, v122, v86\nv_mul_f32 v123, v123, v87\nv_mfma_f32_32x32x16_bf16 v[32:47], a[28:31], v[88:91], v[32:47]\nv_cvt_pk_bf16_f32 v136, v116, v117\n" \
    "v_cvt_pk_bf16_f32 v137, v118, v119\nv_cvt_pk_bf16_f32 v138, v120, v121\nv_cvt_pk_bf16_f32 v139, v122, v123\nv_accvgpr_write_b32 a28, v136\nv_accvgpr_write_b32 a29, v137\n" \
    "v_accvgpr_write_b32 a30, v138\nv_accvgpr_write_b32 a31, v139\nv_mfma_f32_32x32x16_bf16 v[16:31], v[136:139], v[128:131], v[16:31]\nds_read_b128 v[80:83], v12 offset:640\n" \
    "ds_read_b128 v[84:87], v12 offset:656\nds_read_b128 v[88:91], v13 offset:320\nv_accvgpr_read_b32 v112, a32\nv_accvgpr_read_b32 v113, a33\nv_accvgpr_read_b32 v114, a34\n" \
    "v_accvgpr_read_b32 v115, a35\nv_lshlrev_b32 v116, 16, v112\nv_and_b32 v117, 0xffff0000, v112\nv_lshlrev_b32 v118, 16, v113\nv_and_b32 v119, 0xffff0000, v113\n" \
    "v_lshlrev_b32 v120, 16, v114\nv_and_b32 v121, 0xffff0000, v114\nv_lshlrev_b32 v122, 16, v115\nv_and_b32 v123, 0xffff0000, v115\ns_waitcnt lgkmcnt(6)\n" \
    "v_mul_f32 v116, v116, v96\nv_mul_f32 v117, v117, v97\nv_mul_f32 v118, v118, v98\nv_mul_f32 v119, v119, v99\nv_mul_f32 v120, v120, v100\nv_mul_f32 v121, v121, v101\n" \
    "v_mul_f32 v122, v122, v102\nv_mul_f32 v123, v123, v103\nv_mfma_f32_32x32x16_bf16 v[32:47], a[32:35], v[104:107], v[32:47]\nv_cvt_pk_bf16_f32 v132, v116, v117\n" \
    "v_cvt_pk_bf16_f32 v133, v118, v119\nv_cvt_pk_bf16_f32 v134, v120, v121\nv_cvt_pk_bf16_f32 v135, v122, v123\nv_accvgpr_write_b32 a32, v132\nv_accvgpr_write_b32 a33, v133\n" \
    "v_accvgpr_write_b32 a34, v134\nv_accvgpr_write_b32 a35, v135\nv_mfma_f32_32x32x16_bf16 v[16:31], v[132:135], v[128:131], v[16:31]\nds_read_b128 v[96:99], v12 offset:704\n" \
    "ds_read_b128 v[100:103], v12 offset:720\nds_read_b128 v[104:107], v13 offset:352\nv_accvgpr_read_b32 v112, a36\nv_accvgpr_read_b32 v113, a37\nv_accvgpr_read_b32 v114, a38\n" \
    "v_accvgpr_read_b32 v115, a39\nv_lshlrev_b32 v116, 16, v112\nv_and_b32 v117, 0xffff0000, v112\nv_lshlrev_b32 v118, 16, v113\nv_and_b32 v119, 0xffff0000, v113\n" \
    "v_lshlrev_b32 v120, 16, v114\nv_and_b32 v121, 0xffff0000, v114\nv_lshlrev_b32 v122, 16, v115\nv_and_b32 v123, 0xffff0000, v115\ns_waitcnt lgkmcnt(6)\n" \
    "v_mul_f32 v116, v116, v64\nv_mul_f32 v117, v117, v65\nv_mul_f32 v118, v118, v66\nv_mul_f32 v119, v119, v67\nv_mul_f32 v120, v120, v68\nv_mul_f32 v121, v121, v69\n" \
    "v_mul_f32 v122, v122, v70\nv_mul_f32 v123, v123, v71\nv_mfma_f32_32x32x16_bf16 v[32:47], a[36:39], v[72:75], v[32:47]\nv_cvt_pk_bf16_f32 v136, v116, v117\n" \
    "v_cvt_pk_bf16_f32 v137, v118, v119\nv_cvt_pk_bf16_f32 v138, v120, v121\nv_cvt_pk_bf16_f32 v139, v122, v123\nv_accvgpr_write_b32 a36, v136\nv_accvgpr_write_b32 a37, v137\n" \
    "v_accvgpr_write_b32 a38, v138\nv_accvgpr_write_b32 a39, v139\nv_mfma_f32_32x32x16_bf16 v[16:31], v[136:139], v[128:131], v[16:31]\nds_read_b128 v[64:67], v12 offset:768\n" \
    "ds_read_b128 v[68:71], v12 offset:784\nds_read_b128 v[72:75], v13 offset:384\nv_accvgpr_read_b32 v112, a40\nv_accvgpr_read_b32 v113, a41\nv_accvgpr_read_b32 v114, a42\n" \
    "v_accvgpr_read_b32 v115, a43\nv_lshlrev_b32 v116, 16, v112\nv_and_b32 v117, 0xffff0000, v112\nv_lshlrev_b32 v118, 16, v113\nv_and_b32 v119, 0xffff0000, v113\n" \
    "v_lshlrev_b32 v120, 16, v114\nv_and_b32 v121, 0xffff0000, v114\nv_lshlrev_b32 v122, 16, v115\nv_and_b32 v123, 0xffff0000, v115\ns_waitcnt lgkmcnt(6)\n" \
    "v_mul_f32 v116, v116, v80\nv_mul_f32 v117, v117, v81\nv_mul_f32 v118, v118, v82\nv_mul_f32 v119, v119, v83\nv_mul_f32 v120, v120, v84\nv_mul_f32 v121, v121, v85\n" \
    "v_mul_f32 v122, v122, v86\nv_mul_f32 v123, v123, v87\nv_mfma_f32_32x32x16_bf16 v[32:47], a[40:43], v[88:91], v[32:47]\nv_cvt_pk_bf16_f32 v132, v116, v117\n" \
    "v_cvt_pk_bf16_f32 v133, v118, v119\nv_cvt_pk_bf16_f32 v134, v120, v121\nv_cvt_pk_bf16_f32 v135, v122, v123\nv_accvgpr_write_b32 a40, v132\nv_accvgpr_write_b32 a41, v133\n" \
    "v_accvgpr_write_b32 a42, v134\nv_accvgpr_write_b32 a43, v135\nv_mfma_f32_32x32x16_bf16 v[16:31], v[132:135], v[128:131], v[16:31]\nds_read_b128 v[80:83], v12 offset:832\n" \
    "ds_read_b128 v[84:87], v12 offset:848\nds_read_b128 v[88:91], v13 offset:416\nv_accvgpr_read_b32 v112, a44\nv_accvgpr_read_b32 v113, a45\nv_accvgpr_read_b32 v114, a46\n" \
    "v_accvgpr_read_b32 v115, a47\nv_lshlrev_b32 v116, 16, v112\nv_and_b32 v117, 0xffff0000, v112\nv_lshlrev_b32 v118, 16, v113\nv_and_b32 v119, 0xffff0000, v113\n" \
    "v_lshlrev_b32 v120, 16, v114\nv_and_b32 v121, 0xffff0000, v114\nv_lshlrev_b32 v122, 16, v115\nv_and_b32 v123, 0xffff0000, v115\ns_waitcnt lgkmcnt(6)\n" \
    "v_mul_f32 v116, v116, v96\nv_mul_f32 v117, v117, v97\nv_mul_f32 v118, v118, v98\nv_mul_f32 v119, v119, v99\nv_mul_f32 v120, v120, v100\nv_mul_f32 v121, v121, v101\n" \
    "v_mul_f32 v122, v122, v102\nv_mul_f32 v123, v123, v103\nv_mfma_f32_32x32x16_bf16 v[32:47], a[44:47], v[104:107], v[32:47]\nv_cvt_pk_bf16_f32 v136, v116, v117\n" \
    "v_cvt_pk_bf16_f32 v137, v118, v119\nv_cvt_pk_bf16_f32 v138, v120, v121\nv_cvt_pk_bf16_f32 v139, v122, v123\nv_accvgpr_write_b32 a44, v136\nv_accvgpr_write_b32 a45, v137\n" \
    "v_accvgpr_write_b32 a46, v138\nv_accvgpr_write_b32 a47, v139\nv_mfma_f32_32x32x16_bf16 v[16:31], v[136:139], v[128:131], v[16:31]\nds_read_b128 v[96:99], v12 offset:896\n" \
    "ds_read_b128 v[100:103], v12 offset:912\nds_read_b128 v[104:107], v13 offset:448\nv_accvgpr_read_b32 v112, a48\nv_accvgpr_read_b32 v113, a49\nv_accvgpr_read_b32 v114, a50\n" \
    "v_accvgpr_read_b32 v115, a51\nv_lshlrev_b32 v116, 16, v112\nv_and_b32 v117, 0xffff0000, v112\nv_lshlrev_b32 v118, 16, v113\nv_and_b32 v119, 0xffff0000, v113\n" \
    "v_lshlrev_b32 v120, 16, v114\nv_and_b32 v121, 0xffff0000, v114\nv_lshlrev_b32 v122, 16, v115\nv_and_b32 v123, 0xffff0000, v115\ns_waitcnt lgkmcnt(6)\n" \
    "v_mul_f32 v116, v116, v64\nv_mul_f32 v117, v117, v65\nv_mul_f32 v118, v118, v66\nv_mul_f32 v119, v119, v67\nv_mul_f32 v120, v120, v68\nv_mul_f32 v121, v121, v69\n" \
    "v_mul_f32 v122, v122, v70\nv_mul_f32 v123, v123, v71\nv_mfma_f32_32x32x16_bf16 v[32:47], a[48:51], v[72:75], v[32:47]\nv_cvt_pk_bf16_f32 v132, v116, v117\n" \
    "v_cvt_pk_bf16_f32 v133, v118, v119\nv_cvt_pk_bf16_f32 v134, v120, v121\nv_cvt_pk_bf16_f32 v135, v122, v123\nv_accvgpr_write_b32 a48, v132\nv_accvgpr_write_b32 a49, v133\n" \
    "v_accvgpr_write_b32 a50, v134\nv_accvgpr_write_b32 a51, v135\nv_mfma_f32_32x32x16_bf16 v[16:31], v[132:135], v[128:131], v[16:31]\nds_read_b128 v[64:67], v12 offset:960\n" \
    "ds_read_b128 v[68:71], v12 offset:976\nds_read_b128 v[72:75], v13 offset:480\nv_accvgpr_read_b32 v112, a52\nv_accvgpr_read_b32 v113, a53\nv_accvgpr_read_b32 v114, a54\n" \
    "v_accvgpr_read_b32 v115, a55\nv_lshlrev_b32 v116, 16, v112\nv_and_b32 v117, 0xffff0000, v112\nv_lshlrev_b32 v118, 16, v113\nv_and_b32 v119, 0xffff0000, v113\n" \
    "v_lshlrev_b32 v120, 16, v114\nv_and_b32 v121, 0xffff0000, v114\nv_lshlrev_b32 v122, 16, v115\nv_and_b32 v123, 0xffff0000, v115\ns_waitcnt lgkmcnt(6)\n" \
    "v_mul_f32 v116, v116, v80\nv_mul_f32 v117, v117, v81\nv_mul_f32 v118, v118, v82\nv_mul_f32 v119, v119, v83\nv_mul_f32 v120, v120, v84\nv_mul_f32 v121, v121, v85\n" \
    "v_mul_f32 v122, v122, v86\nv_mul_f32 v123, v123, v87\nv_mfma_f32_32x32x16_bf16 v[32:47], a[52:55], v[88:91], v[32:47]\nv_cvt_pk_bf16_f32 v136, v116, v117\n" \
    "v_cvt_pk_bf16_f32 v137, v118, v119\nv_cvt_pk_bf16_f32 v138, v120, v121\nv_cvt_pk_bf16_f32 v139, v122, v123\nv_accvgpr_write_b32 a52, v136\nv_accvgpr_write_b32 a53, v137\n" \
    "v_accvgpr_write_b32 a54, v138\nv_accvgpr_write_b32 a55, v139\nv_mfma_f32_32x32x16_bf16 v[16:31], v[136:139], v[128:131], v[16:31]\nds_read_b128 v[80:83], v12 offset:1024\n" \
    "ds_read_b128 v[84:87], v12 offset:1040\nds_read_b128 v[88:91], v13 offset:512\nv_accvgpr_read_b32 v112, a56\nv_accvgpr_read_b32 v113, a57\nv_accvgpr_read_b32 v114, a58\n" \
    "v_accvgpr_read_b32 v115, a59\nv_lshlrev_b32 v116, 16, v112\nv_and_b32 v117, 0xffff0000, v112\nv_lshlrev_b32 v118, 16, v113\nv_and_b32 v119, 0xffff0000, v113\n" \
    "v_lshlrev_b32 v120, 16, v114\nv_and_b32 v121, 0xffff0000, v114\nv_lshlrev_b32 v122, 16, v115\nv_and_b32 v123, 0xffff0000, v115\ns_waitcnt lgkmcnt(6)\n" \
    "v_mul_f32 v116, v116, v96\nv_mul_f32 v117, v117, v97\nv_mul_f32 v118, v118, v98\nv_mul_f32 v119, v119, v99\nv_mul_f32 v120, v120, v100\nv_mul_f32 v121, v121, v101\n" \
    "v_mul_f32 v122, v122, v102\nv_mul_f32 v123, v123, v103\nv_mfma_f32_32x32x16_bf16 v[32:47], a[56:59], v[104:107], v[32:47]\nv_cvt_pk_bf16_f32 v132, v116, v117\n" \
    "v_cvt_pk_bf16_f32 v133, v118, v119\nv_cvt_pk_bf16_f32 v134, v120, v121\nv_cvt_pk_bf16_f32 v135, v122, v123\nv_accvgpr_write_b32 a56, v132\nv_accvgpr_write_b32 a57, v133\n" \
    "v_accvgpr_write_b32 a58, v134\nv_accvgpr_write_b32 a59, v135\nv_mfma_f32_32x32x16_bf16 v[16:31], v[132:135], v[128:131], v[16:31]\nds_read_b128 v[96:99], v12 offset:1088\n" \
    "ds_read_b128 v[100:103], v12 offset:1104\nds_read_b128 v[104:107], v13 offset:544\nv_accvgpr_read_b32 v112, a60\nv_accvgpr_read_b32 v113, a61\n" \
    "v_accvgpr_read_b32 v114, a62\nv_accvgpr_read_b32 v115, a63\nv_lshlrev_b32 v116, 16, v112\nv_and_b32 v117, 0xffff0000, v112\nv_lshlrev_b32 v118, 16, v113\n" \
    "v_and_b32 v119, 0xffff0000, v113\nv_lshlrev_b32 v120, 16, v114\nv_and_b32 v121, 0xffff0000, v114\nv_lshlrev_b32 v122, 16, v115\nv_and_b32 v123, 0xffff0000, v115\n" \
    "s_waitcnt lgkmcnt(6)\nv_mul_f32 v116, v116, v64\nv_mul_f32 v117, v117, v65\nv_mul_f32 v118, v118, v66\nv_mul_f32 v119, v119, v67\nv_mul_f32 v120, v120, v68\n" \
    "v_mul_f32 v121, v121, v69\nv_mul_f32 v122, v122, v70\nv_mul_f32 v123, v123, v71\nv_mfma_f32_32x32x16_bf16 v[32:47], a[60:63], v[72:75], v[32:47]\n" \
    "v_cvt_pk_bf16_f32 v136, v116, v117\nv_cvt_pk_bf16_f32 v137, v118, v119\nv_cvt_pk_bf16_f32 v138, v120, v121\nv_cvt_pk_bf16_f32 v139, v122, v123\n" \
    "v_accvgpr_write_b32 a60, v136\nv_accvgpr_write_b32 a61, v137\nv_accvgpr_write_b32 a62, v138\nv_accvgpr_write_b32 a63, v139\n" \
    "v_mfma_f32_32x32x16_bf16 v[16:31], v[136:139], v[128:131], v[16:31]\nds_read_b128 v[64:67], v12 offset:1152\nds_read_b128 v[68:71], v12 offset:1168\n" \
    "ds_read_b128 v[72:75], v13 offset:576\nv_accvgpr_read_b32 v112, a64\nv_accvgpr_read_b32 v113, a65\nv_accvgpr_read_b32 v114, a66\nv_accvgpr_read_b32 v115, a67\n" \
    "v_lshlrev_b32 v116, 16, v112\nv_and_b32 v117, 0xffff0000, v112\nv_lshlrev_b32 v118, 16, v113\nv_and_b32 v119, 0xffff0000, v113\nv_lshlrev_b32 v120, 16, v114\n" \
    "v_and_b32 v121, 0xffff0000, v114\nv_lshlrev_b32 v122, 16, v115\nv_and_b32 v123, 0xffff0000, v115\ns_waitcnt lgkmcnt(6)\nv_mul_f32 v116, v116, v80\n" \
    "v_mul_f32 v117, v117, v81\nv_mul_f32 v118, v118, v82\nv_mul_f32 v119, v119, v83\nv_mul_f32 v120, v120, v84\nv_mul_f32 v121, v121, v85\nv_mul_f32 v122, v122, v86\n" \
    "v_mul_f32 v123, v123, v87\nv_mfma_f32_32x32x16_bf16 v[32:47], a[64:67], v[88:91], v[32:47]\nv_cvt_pk_bf16_f32 v132, v116, v117\nv_cvt_pk_bf16_f32 v133, v118, v119\n" \
    "v_cvt_pk_bf16_f32 v134, v120, v121\nv_cvt_pk_bf16_f32 v135, v122, v123\nv_accvgpr_write_b32 a64, v132\nv_accvgpr_write_b32 a65, v133\nv_accvgpr_write_b32 a66, v134\n" \
    "v_accvgpr_write_b32 a67, v135\nv_mfma_f32_32x32x16_bf16 v[16:31], v[132:135], v[128:131], v[16:31]\nds_read_b128 v[80:83], v12 offset:1216\n" \
    "ds_read_b128 v[84:87], v12 offset:1232\nds_read_b128 v[88:91], v13 offset:608\nv_accvgpr_read_b32 v112, a68\nv_accvgpr_read_b32 v113, a69\nv_accvgpr_read_b32 v114, a70\n" \
    "v_accvgpr_read_b32 v115, a71\nv_lshlrev_b32 v116, 16, v112\nv_and_b32 v117, 0xffff0000, v112\nv_lshlrev_b32 v118, 16, v113\nv_and_b32 v119, 0xffff0000, v113\n" \
    "v_lshlrev_b32 v120, 16, v114\nv_and_b32 v121, 0xffff0000, v114\nv_lshlrev_b32 v122, 16, v115\nv_and_b32 v123, 0xffff0000, v115\ns_waitcnt lgkmcnt(6)\n" \
    "v_mul_f32 v116, v116, v96\nv_mul_f32 v117, v117, v97\nv_mul_f32 v118, v118, v98\nv_mul_f32 v119, v119, v99\nv_mul_f32 v120, v120, v100\nv_mul_f32 v121, v121, v101\n" \
    "v_mul_f32 v122, v122, v102\nv_mul_f32 v123, v123, v103\nv_mfma_f32_32x32x16_bf16 v[32:47], a[68:71], v[104:107], v[32:47]\nv_cvt_pk_bf16_f32 v136, v116, v117\n" \
    "v_cvt_pk_bf16_f32 v137, v118, v119\nv_cvt_pk_bf16_f32 v138, v120, v121\nv_cvt_pk_bf16_f32 v139, v122, v123\nv_accvgpr_write_b32 a68, v136\nv_accvgpr_write_b32 a69, v137\n" \
    "v_accvgpr_write_b32 a70, v138\nv_accvgpr_write_b32 a71, v139\nv_mfma_f32_32x32x16_bf16 v[16:31], v[136:139], v[128:131], v[16:31]\nds_read_b128 v[96:99], v12 offset:1280\n" \
    "ds_read_b128 v[100:103], v12 offset:1296\nds_read_b128 v[104:107], v13 offset:640\nv_accvgpr_read_b32 v112, a72\nv_accvgpr_read_b32 v113, a73\n" \
    "v_accvgpr_read_b32 v114, a74\nv_accvgpr_read_b32 v115, a75\nv_lshlrev_b32 v116, 16, v112\nv_and_b32 v117, 0xffff0000, v112\nv_lshlrev_b32 v118, 16, v113\n" \
    "v_and_b32 v119, 0xffff0000, v113\nv_lshlrev_b32 v120, 16, v114\nv_and_b32 v121, 0xffff0000, v114\nv_lshlrev_b32 v122, 16, v115\nv_and_b32 v123, 0xffff0000, v115\n" \
    "s_waitcnt lgkmcnt(6)\nv_mul_f32 v116, v116, v64\nv_mul_f32 v117, v117, v65\nv_mul_f32 v118, v118, v66\nv_mul_f32 v119, v119, v67\nv_mul_f32 v120, v120, v68\n" \
    "v_mul_f32 v121, v121, v69\nv_mul_f32 v122, v122, v70\nv_mul_f32 v123, v123, v71\nv_mfma_f32_32x32x16_bf16 v[32:47], a[72:75], v[72:75], v[32:47]\n" \
    "v_cvt_pk_bf16_f32 v132, v116, v117\nv_cvt_pk_bf16_f32 v133, v118, v119\nv_cvt_pk_bf16_f32 v134, v120, v121\nv_cvt_pk_bf16_f32 v135, v122, v123\n" \
    "v_accvgpr_write_b32 a72, v132\nv_accvgpr_write_b32 a73, v133\nv_accvgpr_write_b32 a74, v134\nv_accvgpr_write_b32 a75, v135\n" \
    "v_mfma_f32_32x32x16_bf16 v[16:31], v[132:135], v[128:131], v[16:31]\nds_read_b128 v[64:67], v12 offset:1344\nds_read_b128 v[68:71], v12 offset:1360\n" \
    "ds_read_b128 v[72:75], v13 offset:672\nv_accvgpr_read_b32 v112, a76\nv_accvgpr_read_b32 v113, a77\nv_accvgpr_read_b32 v114, a78\nv_accvgpr_read_b32 v115, a79\n" \
    "v_lshlrev_b32 v116, 16, v112\nv_and_b32 v117, 0xffff0000, v112\nv_lshlrev_b32 v118, 16, v113\nv_and_b32 v119, 0xffff0000, v113\nv_lshlrev_b32 v120, 16, v114\n" \
    "v_and_b32 v121, 0xffff0000, v114\nv_lshlrev_b32 v122, 16, v115\nv_and_b32 v123, 0xffff0000, v115\ns_waitcnt lgkmcnt(6)\nv_mul_f32 v116, v116, v80\n" \
    "v_mul_f32 v117, v117, v81\nv_mul_f32 v118, v118, v82\nv_mul_f32 v119, v119, v83\nv_mul_f32 v120, v120, v84\nv_mul_f32 v121, v121, v85\nv_mul_f32 v122, v122, v86\n" \
    "v_mul_f32 v123, v123, v87\nv_mfma_f32_32x32x16_bf16 v[32:47], a[76:79], v[88:91], v[32:47]\nv_cvt_pk_bf16_f32 v136, v116, v117\nv_cvt_pk_bf16_f32 v137, v118, v119\n" \
    "v_cvt_pk_bf16_f32 v138, v120, v121\nv_cvt_pk_bf16_f32 v139, v122, v123\nv_accvgpr_write_b32 a76, v136\nv_accvgpr_write_b32 a77, v137\nv_accvgpr_write_b32 a78, v138\n" \
    "v_accvgpr_write_b32 a79, v139\nv_mfma_f32_32x32x16_bf16 v[16:31], v[136:139], v[128:131], v[16:31]\nds_read_b128 v[80:83], v12 offset:1408\n" \
    "ds_read_b128 v[84:87], v12 offset:1424\nds_read_b128 v[88:91], v13 offset:704\nv_accvgpr_read_b32 v112, a80\nv_accvgpr_read_b32 v113, a81\nv_accvgpr_read_b32 v114, a82\n" \
    "v_accvgpr_read_b32 v115, a83\nv_lshlrev_b32 v116, 16, v112\nv_and_b32 v117, 0xffff0000, v112\nv_lshlrev_b32 v118, 16, v113\nv_and_b32 v119, 0xffff0000, v113\n" \
    "v_lshlrev_b32 v120, 16, v114\nv_and_b32 v121, 0xffff0000, v114\nv_lshlrev_b32 v122, 16, v115\nv_and_b32 v123, 0xffff0000, v115\ns_waitcnt lgkmcnt(6)\n" \
    "v_mul_f32 v116, v116, v96\nv_mul_f32 v117, v117, v97\nv_mul_f32 v118, v118, v98\nv_mul_f32 v119, v119, v99\nv_mul_f32 v120, v120, v100\nv_mul_f32 v121, v121, v101\n" \
    "v_mul_f32 v122, v122, v102\nv_mul_f32 v123, v123, v103\nv_mfma_f32_32x32x16_bf16 v[32:47], a[80:83], v[104:107], v[32:47]\nv_cvt_pk_bf16_f32 v132, v116, v117\n" \
    "v_cvt_pk_bf16_f32 v133, v118, v119\nv_cvt_pk_bf16_f32 v134, v120, v121\nv_cvt_pk_bf16_f32 v135, v122, v123\nv_accvgpr_write_b32 a80, v132\nv_accvgpr_write_b32 a81, v133\n" \
    "v_accvgpr_write_b32 a82, v134\nv_accvgpr_write_b32 a83, v135\nv_mfma_f32_32x32x16_bf16 v[16:31], v[132:135], v[128:131], v[16:31]\nds_read_b128 v[96:99], v12 offset:1472\n" \
    "ds_read_b128 v[100:103], v12 offset:1488\nds_read_b128 v[104:107], v13 offset:736\nv_accvgpr_read_b32 v112, a84\nv_accvgpr_read_b32 v113, a85\n" \
    "v_accvgpr_read_b32 v114, a86\nv_accvgpr_read_b32 v115, a87\nv_lshlrev_b32 v116, 16, v112\nv_and_b32 v117, 0xffff0000, v112\nv_lshlrev_b32 v118, 16, v113\n" \
    "v_and_b32 v119, 0xffff0000, v113\nv_lshlrev_b32 v120, 16, v114\nv_and_b32 v121, 0xffff0000, v114\nv_lshlrev_b32 v122, 16, v115\nv_and_b32 v123, 0xffff0000, v115\n" \
    "s_waitcnt lgkmcnt(6)\nv_mul_f32 v116, v116, v64\nv_mul_f32 v117, v117, v65\nv_mul_f32 v118, v118, v66\nv_mul_f32 v119, v119, v67\nv_mul_f32 v120, v120, v68\n" \
    "v_mul_f32 v121, v121, v69\nv_mul_f32 v122, v122, v70\nv_mul_f32 v123, v123, v71\nv_mfma_f32_32x32x16_bf16 v[32:47], a[84:87], v[72:75], v[32:47]\n" \
    "v_cvt_pk_bf16_f32 v136, v116, v117\nv_cvt_pk_bf16_f32 v137, v118, v119\nv_cvt_pk_bf16_f32 v138, v120, v121\nv_cvt_pk_bf16_f32 v139, v122, v123\n" \
    "v_accvgpr_write_b32 a84, v136\nv_accvgpr_write_b32 a85, v137\nv_accvgpr_write_b32 a86, v138\nv_accvgpr_write_b32 a87, v139\n" \
    "v_mfma_f32_32x32x16_bf16 v[16:31], v[136:139], v[128:131], v[16:31]\nds_read_b128 v[64:67], v12 offset:1536\nds_read_b128 v[68:71], v12 offset:1552\n" \
    "ds_read_b128 v[72:75], v13 offset:768\nv_accvgpr_read_b32 v112, a88\nv_accvgpr_read_b32 v113, a89\nv_accvgpr_read_b32 v114, a90\nv_accvgpr_read_b32 v115, a91\n" \
    "v_lshlrev_b32 v116, 16, v112\nv_and_b32 v117, 0xffff0000, v112\nv_lshlrev_b32 v118, 16, v113\nv_and_b32 v119, 0xffff0000, v113\nv_lshlrev_b32 v120, 16, v114\n" \
    "v_and_b32 v121, 0xffff0000, v114\nv_lshlrev_b32 v122, 16, v115\nv_and_b32 v123, 0xffff0000, v115\ns_waitcnt lgkmcnt(6)\nv_mul_f32 v116, v116, v80\n" \
    "v_mul_f32 v117, v117, v81\nv_mul_f32 v118, v118, v82\nv_mul_f32 v119, v119, v83\nv_mul_f32 v120, v120, v84\nv_mul_f32 v121, v121, v85\nv_mul_f32 v122, v122, v86\n" \
    "v_mul_f32 v123, v123, v87\nv_mfma_f32_32x32x16_bf16 v[32:47], a[88:91], v[88:91], v[32:47]\nv_cvt_pk_bf16_f32 v132, v116, v117\nv_cvt_pk_bf16_f32 v133, v118, v119\n" \
    "v_cvt_pk_bf16_f32 v134, v120, v121\nv_cvt_pk_bf16_f32 v135, v122, v123\nv_accvgpr_write_b32 a88, v132\nv_accvgpr_write_b32 a89, v133\nv_accvgpr_write_b32 a90, v134\n" \
    "v_accvgpr_write_b32 a91, v135\nv_mfma_f32_32x32x16_bf16 v[16:31], v[132:135], v[128:131], v[16:31]\nds_read_b128 v[80:83], v12 offset:1600\n" \
    "ds_read_b128 v[84:87], v12 offset:1616\nds_read_b128 v[88:91], v13 offset:800\nv_accvgpr_read_b32 v112, a92\nv_accvgpr_read_b32 v113, a93\nv_accvgpr_read_b32 v114, a94\n" \
    "v_accvgpr_read_b32 v115, a95\nv_lshlrev_b32 v116, 16, v112\nv_and_b32 v117, 0xffff0000, v112\nv_lshlrev_b32 v118, 16, v113\nv_and_b32 v119, 0xffff0000, v113\n" \
    "v_lshlrev_b32 v120, 16, v114\nv_and_b32 v121, 0xffff0000, v114\nv_lshlrev_b32 v122, 16, v115\nv_and_b32 v123, 0xffff0000, v115\ns_waitcnt lgkmcnt(6)\n" \
    "v_mul_f32 v116, v116, v96\nv_mul_f32 v117, v117, v97\nv_mul_f32 v118, v118, v98\nv_mul_f32 v119, v119, v99\nv_mul_f32 v120, v120, v100\nv_mul_f32 v121, v121, v101\n" \
    "v_mul_f32 v122, v122, v102\nv_mul_f32 v123, v123, v103\nv_mfma_f32_32x32x16_bf16 v[32:47], a[92:95], v[104:107], v[32:47]\nv_cvt_pk_bf16_f32 v136, v116, v117\n" \
    "v_cvt_pk_bf16_f32 v137, v118, v119\nv_cvt_pk_bf16_f32 v138, v120, v121\nv_cvt_pk_bf16_f32 v139, v122, v123\nv_accvgpr_write_b32 a92, v136\nv_accvgpr_write_b32 a93, v137\n" \
    "v_accvgpr_write_b32 a94, v138\nv_accvgpr_write_b32 a95, v139\nv_mfma_f32_32x32x16_bf16 v[16:31], v[136:139], v[128:131], v[16:31]\nds_read_b128 v[96:99], v12 offset:1664\n" \
    "ds_read_b128 v[100:103], v12 offset:1680\nds_read_b128 v[104:107], v13 offset:832\nv_accvgpr_read_b32 v112, a96\nv_accvgpr_read_b32 v113, a97\n" \
    "v_accvgpr_read_b32 v114, a98\nv_accvgpr_read_b32 v115, a99\nv_lshlrev_b32 v116, 16, v112\nv_and_b32 v117, 0xffff0000, v112\nv_lshlrev_b32 v118, 16, v113\n" \
    "v_and_b32 v119, 0xffff0000, v113\nv_lshlrev_b32 v120, 16, v114\nv_and_b32 v121, 0xffff0000, v114\nv_lshlrev_b32 v122, 16, v115\nv_and_b32 v123, 0xffff0000, v115\n" \
    "s_waitcnt lgkmcnt(6)\nv_mul_f32 v116, v116, v64\nv_mul_f32 v117, v117, v65\nv_mul_f32 v118, v118, v66\nv_mul_f32 v119, v119, v67\nv_mul_f32 v120, v120, v68\n" \
    "v_mul_f32 v121, v121, v69\nv_mul_f32 v122, v122, v70\nv_mul_f32 v123, v123, v71\nv_mfma_f32_32x32x16_bf16 v[32:47], a[96:99], v[72:75], v[32:47]\n" \
    "v_cvt_pk_bf16_f32 v132, v116, v117\nv_cvt_pk_bf16_f32 v133, v118, v119\nv_cvt_pk_bf16_f32 v134, v120, v121\nv_cvt_pk_bf16_f32 v135, v122, v123\n" \
    "v_accvgpr_write_b32 a96, v132\nv_accvgpr_write_b32 a97, v133\nv_accvgpr_write_b32 a98, v134\nv_accvgpr_write_b32 a99, v135\n" \
    "v_mfma_f32_32x32x16_bf16 v[16:31], v[132:135], v[128:131], v[16:31]\nds_read_b128 v[64:67], v12 offset:1728\nds_read_b128 v[68:71], v12 offset:1744\n" \
    "ds_read_b128 v[72:75], v13 offset:864\nv_accvgpr_read_b32 v112, a100\nv_accvgpr_read_b32 v113, a101\nv_accvgpr_read_b32 v114, a102\nv_accvgpr_read_b32 v115, a103\n" \
    "v_lshlrev_b32 v116, 16, v112\nv_and_b32 v117, 0xffff0000, v112\nv_lshlrev_b32 v118, 16, v113\nv_and_b32 v119, 0xffff0000, v113\nv_lshlrev_b32 v120, 16, v114\n" \
    "v_and_b32 v121, 0xffff0000, v114\nv_lshlrev_b32 v122, 16, v115\nv_and_b32 v123, 0xffff0000, v115\ns_waitcnt lgkmcnt(6)\nv_mul_f32 v116, v116, v80\n" \
    "v_mul_f32 v117, v117, v81\nv_mul_f32 v118, v118, v82\nv_mul_f32 v119, v119, v83\nv_mul_f32 v120, v120, v84\nv_mul_f32 v121, v121, v85\nv_mul_f32 v122, v122, v86\n" \
    "v_mul_f32 v123, v123, v87\nv_mfma_f32_32x32x16_bf16 v[32:47], a[100:103], v[88:91], v[32:47]\nv_cvt_pk_bf16_f32 v136, v116, v117\nv_cvt_pk_bf16_f32 v137, v118, v119\n" \
    "v_cvt_pk_bf16_f32 v138, v120, v121\nv_cvt_pk_bf16_f32 v139, v122, v123\nv_accvgpr_write_b32 a100, v136\nv_accvgpr_write_b32 a101, v137\nv_accvgpr_write_b32 a102, v138\n" \
    "v_accvgpr_write_b32 a103, v139\nv_mfma_f32_32x32x16_bf16 v[16:31], v[136:139], v[128:131], v[16:31]\nds_read_b128 v[80:83], v12 offset:1792\n" \
    "ds_read_b128 v[84:87], v12 offset:1808\nds_read_b128 v[88:91], v13 offset:896\nv_accvgpr_read_b32 v112, a104\nv_accvgpr_read_b32 v113, a105\nv_accvgpr_read_b32 v114, a106\n" \
    "v_accvgpr_read_b32 v115, a107\nv_lshlrev_b32 v116, 16, v112\nv_and_b32 v117, 0xffff0000, v112\nv_lshlrev_b32 v118, 16, v113\nv_and_b32 v119, 0xffff0000, v113\n" \
    "v_lshlrev_b32 v120, 16, v114\nv_and_b32 v121, 0xffff0000, v114\nv_lshlrev_b32 v122, 16, v115\nv_and_b32 v123, 0xffff0000, v115\ns_waitcnt lgkmcnt(6)\n" \
    "v_mul_f32 v116, v116, v96\nv_mul_f32 v117, v117, v97\nv_mul_f32 v118, v118, v98\nv_mul_f32 v119, v119, v99\nv_mul_f32 v120, v120, v100\nv_mul_f32 v121, v121, v101\n" \
    "v_mul_f32 v122, v122, v102\nv_mul_f32 v123, v123, v103\nv_mfma_f32_32x32x16_bf16 v[32:47], a[104:107], v[104:107], v[32:47]\nv_cvt_pk_bf16_f32 v132, v116, v117\n" \
    "v_cvt_pk_bf16_f32 v133, v118, v119\nv_cvt_pk_bf16_f32 v134, v120, v121\nv_cvt_pk_bf16_f32 v135, v122, v123\nv_accvgpr_write_b32 a104, v132\nv_accvgpr_write_b32 a105, v133\n" \
    "v_accvgpr_write_b32 a106, v134\nv_accvgpr_write_b32 a107, v135\nv_mfma_f32_32x32x16_bf16 v[16:31], v[132:135], v[128:131], v[16:31]\n" \
    "ds_read_b128 v[96:99], v12 offset:1856\nds_read_b128 v[100:103], v12 offset:1872\nds_read_b128 v[104:107], v13 offset:928\nv_accvgpr_read_b32 v112, a108\n" \
    "v_accvgpr_read_b32 v113, a109\nv_accvgpr_read_b32 v114, a110\nv_accvgpr_read_b32 v115, a111\nv_lshlrev_b32 v116, 16, v112\nv_and_b32 v117, 0xffff0000, v112\n" \
    "v_lshlrev_b32 v118, 16, v113\nv_and_b32 v119, 0xffff0000, v113\nv_lshlrev_b32 v120, 16, v114\nv_and_b32 v121, 0xffff0000, v114\nv_lshlrev_b32 v122, 16, v115\n" \
    "v_and_b32 v123, 0xffff0000, v115\ns_waitcnt lgkmcnt(6)\nv_mul_f32 v116, v116, v64\nv_mul_f32 v117, v117, v65\nv_mul_f32 v118, v118, v66\nv_mul_f32 v119, v119, v67\n" \
    "v_mul_f32 v120, v120, v68\nv_mul_f32 v121, v121, v69\nv_mul_f32 v122, v122, v70\nv_mul_f32 v123, v123, v71\n" \
    "v_mfma_f32_32x32x16_bf16 v[32:47], a[108:111], v[72:75], v[32:47]\nv_cvt_pk_bf16_f32 v136, v116, v117\nv_cvt_pk_bf16_f32 v137, v118, v119\n" \
    "v_cvt_pk_bf16_f32 v138, v120, v121\nv_cvt_pk_bf16_f32 v139, v122, v123\nv_accvgpr_write_b32 a108, v136\nv_accvgpr_write_b32 a109, v137\nv_accvgpr_write_b32 a110, v138\n" \
    "v_accvgpr_write_b32 a111, v139\nv_mfma_f32_32x32x16_bf16 v[16:31], v[136:139], v[128:131], v[16:31]\nds_read_b128 v[64:67], v12 offset:1920\n" \
    "ds_read_b128 v[68:71], v12 offset:1936\nds_read_b128 v[72:75], v13 offset:960\nv_accvgpr_read_b32 v112, a112\nv_accvgpr_read_b32 v113, a113\nv_accvgpr_read_b32 v114, a114\n" \
    "v_accvgpr_read_b32 v115, a115\nv_lshlrev_b32 v116, 16, v112\nv_and_b32 v117, 0xffff0000, v112\nv_lshlrev_b32 v118, 16, v113\nv_and_b32 v119, 0xffff0000, v113\n" \
    "v_lshlrev_b32 v120, 16, v114\nv_and_b32 v121, 0xffff0000, v114\nv_lshlrev_b32 v122, 16, v115\nv_and_b32 v123, 0xffff0000, v115\ns_waitcnt lgkmcnt(6)\n" \
    "v_mul_f32 v116, v116, v80\nv_mul_f32 v117, v117, v81\nv_mul_f32 v118, v118, v82\nv_mul_f32 v119, v119, v83\nv_mul_f32 v120, v120, v84\nv_mul_f32 v121, v121, v85\n" \
    "v_mul_f32 v122, v122, v86\nv_mul_f32 v123, v123, v87\nv_mfma_f32_32x32x16_bf16 v[32:47], a[112:115], v[88:91], v[32:47]\nv_cvt_pk_bf16_f32 v132, v116, v117\n" \
    "v_cvt_pk_bf16_f32 v133, v118, v119\nv_cvt_pk_bf16_f32 v134, v120, v121\nv_cvt_pk_bf16_f32 v135, v122, v123\nv_accvgpr_write_b32 a112, v132\nv_accvgpr_write_b32 a113, v133\n" \
    "v_accvgpr_write_b32 a114, v134\nv_accvgpr_write_b32 a115, v135\nv_mfma_f32_32x32x16_bf16 v[16:31], v[132:135], v[128:131], v[16:31]\n" \
    "ds_read_b128 v[80:83], v12 offset:1984\nds_read_b128 v[84:87], v12 offset:2000\nds_read_b128 v[88:91], v13 offset:992\nv_accvgpr_read_b32 v112, a116\n" \
    "v_accvgpr_read_b32 v113, a117\nv_accvgpr_read_b32 v114, a118\nv_accvgpr_read_b32 v115, a119\nv_lshlrev_b32 v116, 16, v112\nv_and_b32 v117, 0xffff0000, v112\n" \
    "v_lshlrev_b32 v118, 16, v113\nv_and_b32 v119, 0xffff0000, v113\nv_lshlrev_b32 v120, 16, v114\nv_and_b32 v121, 0xffff0000, v114\nv_lshlrev_b32 v122, 16, v115\n" \
    "v_and_b32 v123, 0xffff0000, v115\ns_waitcnt lgkmcnt(6)\nv_mul_f32 v116, v116, v96\nv_mul_f32 v117, v117, v97\nv_mul_f32 v118, v118, v98\nv_mul_f32 v119, v119, v99\n" \
    "v_mul_f32 v120, v120, v100\nv_mul_f32 v121, v121, v101\nv_mul_f32 v122, v122, v102\nv_mul_f32 v123, v123, v103\n" \
    "v_mfma_f32_32x32x16_bf16 v[32:47], a[116:119], v[104:107], v[32:47]\nv_cvt_pk_bf16_f32 v136, v116, v117\nv_cvt_pk_bf16_f32 v137, v118, v119\n" \
    "v_cvt_pk_bf16_f32 v138, v120, v121\nv_cvt_pk_bf16_f32 v139, v122, v123\nv_accvgpr_write_b32 a116, v136\nv_accvgpr_write_b32 a117, v137\nv_accvgpr_write_b32 a118, v138\n" \
    "v_accvgpr_write_b32 a119, v139\nv_mfma_f32_32x32x16_bf16 v[16:31], v[136:139], v[128:131], v[16:31]\nds_read_b128 v[96:99], v12 offset:2048\n" \
    "ds_read_b128 v[100:103], v12 offset:2064\nds_read_b128 v[104:107], v13 offset:1024\nv_accvgpr_read_b32 v112, a120\nv_accvgpr_read_b32 v113, a121\n" \
    "v_accvgpr_read_b32 v114, a122\nv_accvgpr_read_b32 v115, a123\nv_lshlrev_b32 v116, 16, v112\nv_and_b32 v117, 0xffff0000, v112\nv_lshlrev_b32 v118, 16, v113\n" \
    "v_and_b32 v119, 0xffff0000, v113\nv_lshlrev_b32 v120, 16, v114\nv_and_b32 v121, 0xffff0000, v114\nv_lshlrev_b32 v122, 16, v115\nv_and_b32 v123, 0xffff0000, v115\n" \
    "s_waitcnt lgkmcnt(6)\nv_mul_f32 v116, v116, v64\nv_mul_f32 v117, v117, v65\nv_mul_f32 v118, v118, v66\nv_mul_f32 v119, v119, v67\nv_mul_f32 v120, v120, v68\n" \
    "v_mul_f32 v121, v121, v69\nv_mul_f32 v122, v122, v70\nv_mul_f32 v123, v123, v71\nv_mfma_f32_32x32x16_bf16 v[32:47], a[120:123], v[72:75], v[32:47]\n" \
    "v_cvt_pk_bf16_f32 v132, v116, v117\nv_cvt_pk_bf16_f32 v133, v118, v119\nv_cvt_pk_bf16_f32 v134, v120, v121\nv_cvt_pk_bf16_f32 v135, v122, v123\n" \
    "v_accvgpr_write_b32 a120, v132\nv_accvgpr_write_b32 a121, v133\nv_accvgpr_write_b32 a122, v134\nv_accvgpr_write_b32 a123, v135\n" \
    "v_mfma_f32_32x32x16_bf16 v[16:31], v[132:135], v[128:131], v[16:31]\nds_read_b128 v[64:67], v12 offset:2112\nds_read_b128 v[68:71], v12 offset:2128\n" \
    "ds_read_b128 v[72:75], v13 offset:1056\nv_accvgpr_read_b32 v112, a124\nv_accvgpr_read_b32 v113, a125\nv_accvgpr_read_b32 v114, a126\nv_accvgpr_read_b32 v115, a127\n" \
    "v_lshlrev_b32 v116, 16, v112\nv_and_b32 v117, 0xffff0000, v112\nv_lshlrev_b32 v118, 16, v113\nv_and_b32 v119, 0xffff0000, v113\nv_lshlrev_b32 v120, 16, v114\n" \
    "v_and_b32 v121, 0xffff0000, v114\nv_lshlrev_b32 v122, 16, v115\nv_and_b32 v123, 0xffff0000, v115\ns_waitcnt lgkmcnt(6)\nv_mul_f32 v116, v116, v80\n" \
    "v_mul_f32 v117, v117, v81\nv_mul_f32 v118, v118, v82\nv_mul_f32 v119, v119, v83\nv_mul_f32 v120, v120, v84\nv_mul_f32 v121, v121, v85\nv_mul_f32 v122, v122, v86\n" \
    "v_mul_f32 v123, v123, v87\nv_mfma_f32_32x32x16_bf16 v[32:47], a[124:127], v[88:91], v[32:47]\nv_cvt_pk_bf16_f32 v136, v116, v117\nv_cvt_pk_bf16_f32 v137, v118, v119\n" \
    "v_cvt_pk_bf16_f32 v138, v120, v121\nv_cvt_pk_bf16_f32 v139, v122, v123\nv_accvgpr_write_b32 a124, v136\nv_accvgpr_write_b32 a125, v137\nv_accvgpr_write_b32 a126, v138\n" \
    "v_accvgpr_write_b32 a127, v139\nv_mfma_f32_32x32x16_bf16 v[16:31], v[136:139], v[128:131], v[16:31]\nds_read_b128 v[80:83], v12 offset:2176\n" \
    "ds_read_b128 v[84:87], v12 offset:2192\nds_read_b128 v[88:91], v13 offset:1088\nv_accvgpr_read_b32 v112, a128\nv_accvgpr_read_b32 v113, a129\n" \
    "v_accvgpr_read_b32 v114, a130\nv_accvgpr_read_b32 v115, a131\nv_lshlrev_b32 v116, 16, v112\nv_and_b32 v117, 0xffff0000, v112\nv_lshlrev_b32 v118, 16, v113\n" \
    "v_and_b32 v119, 0xffff0000, v113\nv_lshlrev_b32 v120, 16, v114\nv_and_b32 v121, 0xffff0000, v114\nv_lshlrev_b32 v122, 16, v115\nv_and_b32 v123, 0xffff0000, v115\n" \
    "s_waitcnt lgkmcnt(6)\nv_mul_f32 v116, v116, v96\nv_mul_f32 v117, v117, v97\nv_mul_f32 v118, v118, v98\nv_mul_f32 v119, v119, v99\nv_mul_f32 v120, v120, v100\n" \
    "v_mul_f32 v121, v121, v101\nv_mul_f32 v122, v122, v102\nv_mul_f32 v123, v123, v103\nv_mfma_f32_32x32x16_bf16 v[32:47], a[128:131], v[104:107], v[32:47]\n" \
    "v_cvt_pk_bf16_f32 v132, v116, v117\nv_cvt_pk_bf16_f32 v133, v118, v119\nv_cvt_pk_bf16_f32 v134, v120, v121\nv_cvt_pk_bf16_f32 v135, v122, v123\n" \
    "v_accvgpr_write_b32 a128, v132\nv_accvgpr_write_b32 a129, v133\nv_accvgpr_write_b32 a130, v134\nv_accvgpr_write_b32 a131, v135\n" \
    "v_mfma_f32_32x32x16_bf16 v[16:31], v[132:135], v[128:131], v[16:31]\nds_read_b128 v[96:99], v12 offset:2240\nds_read_b128 v[100:103], v12 offset:2256\n" \
    "ds_read_b128 v[104:107], v13 offset:1120\nv_accvgpr_read_b32 v112, a132\nv_accvgpr_read_b32 v113, a133\nv_accvgpr_read_b32 v114, a134\nv_accvgpr_read_b32 v115, a135\n" \
    "v_lshlrev_b32 v116, 16, v112\nv_and_b32 v117, 0xffff0000, v112\nv_lshlrev_b32 v118, 16, v113\nv_and_b32 v119, 0xffff0000, v113\nv_lshlrev_b32 v120, 16, v114\n" \
    "v_and_b32 v121, 0xffff0000, v114\nv_lshlrev_b32 v122, 16, v115\nv_and_b32 v123, 0xffff0000, v115\ns_waitcnt lgkmcnt(6)\nv_mul_f32 v116, v116, v64\n" \
    "v_mul_f32 v117, v117, v65\nv_mul_f32 v118, v118, v66\nv_mul_f32 v119, v119, v67\nv_mul_f32 v120, v120, v68\nv_mul_f32 v121, v121, v69\nv_mul_f32 v122, v122, v70\n" \
    "v_mul_f32 v123, v123, v71\nv_mfma_f32_32x32x16_bf16 v[32:47], a[132:135], v[72:75], v[32:47]\nv_cvt_pk_bf16_f32 v136, v116, v117\nv_cvt_pk_bf16_f32 v137, v118, v119\n" \
    "v_cvt_pk_bf16_f32 v138, v120, v121\nv_cvt_pk_bf16_f32 v139, v122, v123\nv_accvgpr_write_b32 a132, v136\nv_accvgpr_write_b32 a133, v137\nv_accvgpr_write_b32 a134, v138\n" \
    "v_accvgpr_write_b32 a135, v139\nv_mfma_f32_32x32x16_bf16 v[16:31], v[136:139], v[128:131], v[16:31]\nds_read_b128 v[64:67], v12 offset:2304\n" \
    "ds_read_b128 v[68:71], v12 offset:2320\nds_read_b128 v[72:75], v13 offset:1152\nv_accvgpr_read_b32 v112, a136\nv_accvgpr_read_b32 v113, a137\n" \
    "v_accvgpr_read_b32 v114, a138\nv_accvgpr_read_b32 v115, a139\nv_lshlrev_b32 v116, 16, v112\nv_and_b32 v117, 0xffff0000, v112\nv_lshlrev_b32 v118, 16, v113\n" \
    "v_and_b32 v119, 0xffff0000, v113\nv_lshlrev_b32 v120, 16, v114\nv_and_b32 v121, 0xffff0000, v114\nv_lshlrev_b32 v122, 16, v115\nv_and_b32 v123, 0xffff0000, v115\n" \
    "s_waitcnt lgkmcnt(6)\nv_mul_f32 v116, v116, v80\nv_mul_f32 v117, v117, v81\nv_mul_f32 v118, v118, v82\nv_mul_f32 v119, v119, v83\nv_mul_f32 v120, v120, v84\n" \
    "v_mul_f32 v121, v121, v85\nv_mul_f32 v122, v122, v86\nv_mul_f32 v123, v123, v87\nv_mfma_f32_32x32x16_bf16 v[32:47], a[136:139], v[88:91], v[32:47]\n" \
    "v_cvt_pk_bf16_f32 v132, v116, v117\nv_cvt_pk_bf16_f32 v133, v118, v119\nv_cvt_pk_bf16_f32 v134, v120, v121\nv_cvt_pk_bf16_f32 v135, v122, v123\n" \
    "v_accvgpr_write_b32 a136, v132\nv_accvgpr_write_b32 a137, v133\nv_accvgpr_write_b32 a138, v134\nv_accvgpr_write_b32 a139, v135\n" \
    "v_mfma_f32_32x32x16_bf16 v[16:31], v[132:135], v[128:131], v[16:31]\nds_read_b128 v[80:83], v12 offset:2368\nds_read_b128 v[84:87], v12 offset:2384\n" \
    "ds_read_b128 v[88:91], v13 offset:1184\nv_accvgpr_read_b32 v112, a140\nv_accvgpr_read_b32 v113, a141\nv_accvgpr_read_b32 v114, a142\nv_accvgpr_read_b32 v115, a143\n" \
    "v_lshlrev_b32 v116, 16, v112\nv_and_b32 v117, 0xffff0000, v112\nv_lshlrev_b32 v118, 16, v113\nv_and_b32 v119, 0xffff0000, v113\nv_lshlrev_b32 v120, 16, v114\n" \
    "v_and_b32 v121, 0xffff0000, v114\nv_lshlrev_b32 v122, 16, v115\nv_and_b32 v123, 0xffff0000, v115\ns_waitcnt lgkmcnt(6)\nv_mul_f32 v116, v116, v96\n" \
    "v_mul_f32 v117, v117, v97\nv_mul_f32 v118, v118, v98\nv_mul_f32 v119, v119, v99\nv_mul_f32 v120, v120, v100\nv_mul_f32 v121, v121, v101\nv_mul_f32 v122, v122, v102\n" \
    "v_mul_f32 v123, v123, v103\nv_mfma_f32_32x32x16_bf16 v[32:47], a[140:143], v[104:107], v[32:47]\nv_cvt_pk_bf16_f32 v136, v116, v117\nv_cvt_pk_bf16_f32 v137, v118, v119\n" \
    "v_cvt_pk_bf16_f32 v138, v120, v121\nv_cvt_pk_bf16_f32 v139, v122, v123\nv_accvgpr_write_b32 a140, v136\nv_accvgpr_write_b32 a141, v137\nv_accvgpr_write_b32 a142, v138\n" \
    "v_accvgpr_write_b32 a143, v139\nv_mfma_f32_32x32x16_bf16 v[16:31], v[136:139], v[128:131], v[16:31]\nds_read_b128 v[96:99], v12 offset:2432\n" \
    "ds_read_b128 v[100:103], v12 offset:2448\nds_read_b128 v[104:107], v13 offset:1216\nv_accvgpr_read_b32 v112, a144\nv_accvgpr_read_b32 v113, a145\n" \
    "v_accvgpr_read_b32 v114, a146\nv_accvgpr_read_b32 v115, a147\nv_lshlrev_b32 v116, 16, v112\nv_and_b32 v117, 0xffff0000, v112\nv_lshlrev_b32 v118, 16, v113\n" \
    "v_and_b32 v119, 0xffff0000, v113\nv_lshlrev_b32 v120, 16, v114\nv_and_b32 v121, 0xffff0000, v114\nv_lshlrev_b32 v122, 16, v115\nv_and_b32 v123, 0xffff0000, v115\n" \
    "s_waitcnt lgkmcnt(6)\nv_mul_f32 v116, v116, v64\nv_mul_f32 v117, v117, v65\nv_mul_f32 v118, v118, v66\nv_mul_f32 v119, v119, v67\nv_mul_f32 v120, v120, v68\n" \
    "v_mul_f32 v121, v121, v69\nv_mul_f32 v122, v122, v70\nv_mul_f32 v123, v123, v71\nv_mfma_f32_32x32x16_bf16 v[32:47], a[144:147], v[72:75], v[32:47]\n" \
    "v_cvt_pk_bf16_f32 v132, v116, v117\nv_cvt_pk_bf16_f32 v133, v118, v119\nv_cvt_pk_bf16_f32 v134, v120, v121\nv_cvt_pk_bf16_f32 v135, v122, v123\n" \
    "v_accvgpr_write_b32 a144, v132\nv_accvgpr_write_b32 a145, v133\nv_accvgpr_write_b32 a146, v134\nv_accvgpr_write_b32 a147, v135\n" \
    "v_mfma_f32_32x32x16_bf16 v[16:31], v[132:135], v[128:131], v[16:31]\nds_read_b128 v[64:67], v12 offset:2496\nds_read_b128 v[68:71], v12 offset:2512\n" \
    "ds_read_b128 v[72:75], v13 offset:1248\nv_accvgpr_read_b32 v112, a148\nv_accvgpr_read_b32 v113, a149\nv_accvgpr_read_b32 v114, a150\nv_accvgpr_read_b32 v115, a151\n" \
    "v_lshlrev_b32 v116, 16, v112\nv_and_b32 v117, 0xffff0000, v112\nv_lshlrev_b32 v118, 16, v113\nv_and_b32 v119, 0xffff0000, v113\nv_lshlrev_b32 v120, 16, v114\n" \
    "v_and_b32 v121, 0xffff0000, v114\nv_lshlrev_b32 v122, 16, v115\nv_and_b32 v123, 0xffff0000, v115\ns_waitcnt lgkmcnt(6)\nv_mul_f32 v116, v116, v80\n" \
    "v_mul_f32 v117, v117, v81\nv_mul_f32 v118, v118, v82\nv_mul_f32 v119, v119, v83\nv_mul_f32 v120, v120, v84\nv_mul_f32 v121, v121, v85\nv_mul_f32 v122, v122, v86\n" \
    "v_mul_f32 v123, v123, v87\nv_mfma_f32_32x32x16_bf16 v[32:47], a[148:151], v[88:91], v[32:47]\nv_cvt_pk_bf16_f32 v136, v116, v117\nv_cvt_pk_bf16_f32 v137, v118, v119\n" \
    "v_cvt_pk_bf16_f32 v138, v120, v121\nv_cvt_pk_bf16_f32 v139, v122, v123\nv_accvgpr_write_b32 a148, v136\nv_accvgpr_write_b32 a149, v137\nv_accvgpr_write_b32 a150, v138\n" \
    "v_accvgpr_write_b32 a151, v139\nv_mfma_f32_32x32x16_bf16 v[16:31], v[136:139], v[128:131], v[16:31]\nds_read_b128 v[80:83], v12 offset:2560\n" \
    "ds_read_b128 v[84:87], v12 offset:2576\nds_read_b128 v[88:91], v13 offset:1280\nv_accvgpr_read_b32 v112, a152\nv_accvgpr_read_b32 v113, a153\n" \
    "v_accvgpr_read_b32 v114, a154\nv_accvgpr_read_b32 v115, a155\nv_lshlrev_b32 v116, 16, v112\nv_and_b32 v117, 0xffff0000, v112\nv_lshlrev_b32 v118, 16, v113\n" \
    "v_and_b32 v119, 0xffff0000, v113\nv_lshlrev_b32 v120, 16, v114\nv_and_b32 v121, 0xffff0000, v114\nv_lshlrev_b32 v122, 16, v115\nv_and_b32 v123, 0xffff0000, v115\n" \
    "s_waitcnt lgkmcnt(6)\nv_mul_f32 v116, v116, v96\nv_mul_f32 v117, v117, v97\nv_mul_f32 v118, v118, v98\nv_mul_f32 v119, v119, v99\nv_mul_f32 v120, v120, v100\n" \
    "v_mul_f32 v121, v121, v101\nv_mul_f32 v122, v122, v102\nv_mul_f32 v123, v123, v103\nv_mfma_f32_32x32x16_bf16 v[32:47], a[152:155], v[104:107], v[32:47]\n" \
    "v_cvt_pk_bf16_f32 v132, v116, v117\nv_cvt_pk_bf16_f32 v133, v118, v119\nv_cvt_pk_bf16_f32 v134, v120, v121\nv_cvt_pk_bf16_f32 v135, v122, v123\n" \
    "v_accvgpr_write_b32 a152, v132\nv_accvgpr_write_b32 a153, v133\nv_accvgpr_write_b32 a154, v134\nv_accvgpr_write_b32 a155, v135\n" \
    "v_mfma_f32_32x32x16_bf16 v[16:31], v[132:135], v[128:131], v[16:31]\nds_read_b128 v[96:99], v12 offset:2624\nds_read_b128 v[100:103], v12 offset:2640\n" \
    "ds_read_b128 v[104:107], v13 offset:1312\nv_accvgpr_read_b32 v112, a156\nv_accvgpr_read_b32 v113, a157\nv_accvgpr_read_b32 v114, a158\nv_accvgpr_read_b32 v115, a159\n" \
    "v_lshlrev_b32 v116, 16, v112\nv_and_b32 v117, 0xffff0000, v112\nv_lshlrev_b32 v118, 16, v113\nv_and_b32 v119, 0xffff0000, v113\nv_lshlrev_b32 v120, 16, v114\n" \
    "v_and_b32 v121, 0xffff0000, v114\nv_lshlrev_b32 v122, 16, v115\nv_and_b32 v123, 0xffff0000, v115\ns_waitcnt lgkmcnt(6)\nv_mul_f32 v116, v116, v64\n" \
    "v_mul_f32 v117, v117, v65\nv_mul_f32 v118, v118, v66\nv_mul_f32 v119, v119, v67\nv_mul_f32 v120, v120, v68\nv_mul_f32 v121, v121, v69\nv_mul_f32 v122, v122, v70\n" \
    "v_mul_f32 v123, v123, v71\nv_mfma_f32_32x32x16_bf16 v[32:47], a[156:159], v[72:75], v[32:47]\nv_cvt_pk_bf16_f32 v136, v116, v117\nv_cvt_pk_bf16_f32 v137, v118, v119\n" \
    "v_cvt_pk_bf16_f32 v138, v120, v121\nv_cvt_pk_bf16_f32 v139, v122, v123\nv_accvgpr_write_b32 a156, v136\nv_accvgpr_write_b32 a157, v137\nv_accvgpr_write_b32 a158, v138\n" \
    "v_accvgpr_write_b32 a159, v139\nv_mfma_f32_32x32x16_bf16 v[16:31], v[136:139], v[128:131], v[16:31]\nds_read_b128 v[64:67], v12 offset:2688\n" \
    "ds_read_b128 v[68:71], v12 offset:2704\nds_read_b128 v[72:75], v13 offset:1344\nv_accvgpr_read_b32 v112, a160\nv_accvgpr_read_b32 v113, a161\n" \
    "v_accvgpr_read_b32 v114, a162\nv_accvgpr_read_b32 v115, a163\nv_lshlrev_b32 v116, 16, v112\nv_and_b32 v117, 0xffff0000, v112\nv_lshlrev_b32 v118, 16, v113\n" \
    "v_and_b32 v119, 0xffff0000, v113\nv_lshlrev_b32 v120, 16, v114\nv_and_b32 v121, 0xffff0000, v114\nv_lshlrev_b32 v122, 16, v115\nv_and_b32 v123, 0xffff0000, v115\n" \
    "s_waitcnt lgkmcnt(6)\nv_mul_f32 v116, v116, v80\nv_mul_f32 v117, v117, v81\nv_mul_f32 v118, v118, v82\nv_mul_f32 v119, v119, v83\nv_mul_f32 v120, v120, v84\n" \
    "v_mul_f32 v121, v121, v85\nv_mul_f32 v122, v122, v86\nv_mul_f32 v123, v123, v87\nv_mfma_f32_32x32x16_bf16 v[32:47], a[160:163], v[88:91], v[32:47]\n" \
    "v_cvt_pk_bf16_f32 v132, v116, v117\nv_cvt_pk_bf16_f32 v133, v118, v119\nv_cvt_pk_bf16_f32 v134, v120, v121\nv_cvt_pk_bf16_f32 v135, v122, v123\n" \
    "v_accvgpr_write_b32 a160, v132\nv_accvgpr_write_b32 a161, v133\nv_accvgpr_write_b32 a162, v134\nv_accvgpr_write_b32 a163, v135\n" \
    "v_mfma_f32_32x32x16_bf16 v[16:31], v[132:135], v[128:131], v[16:31]\nds_read_b128 v[80:83], v12 offset:2752\nds_read_b128 v[84:87], v12 offset:2768\n" \
    "ds_read_b128 v[88:91], v13 offset:1376\nv_accvgpr_read_b32 v112, a164\nv_accvgpr_read_b32 v113, a165\nv_accvgpr_read_b32 v114, a166\nv_accvgpr_read_b32 v115, a167\n" \
    "v_lshlrev_b32 v116, 16, v112\nv_and_b32 v117, 0xffff0000, v112\nv_lshlrev_b32 v118, 16, v113\nv_and_b32 v119, 0xffff0000, v113\nv_lshlrev_b32 v120, 16, v114\n" \
    "v_and_b32 v121, 0xffff0000, v114\nv_lshlrev_b32 v122, 16, v115\nv_and_b32 v123, 0xffff0000, v115\ns_waitcnt lgkmcnt(6)\nv_mul_f32 v116, v116, v96\n" \
    "v_mul_f32 v117, v117, v97\nv_mul_f32 v118, v118, v98\nv_mul_f32 v119, v119, v99\nv_mul_f32 v120, v120, v100\nv_mul_f32 v121, v121, v101\nv_mul_f32 v122, v122, v102\n" \
    "v_mul_f32 v123, v123, v103\nv_mfma_f32_32x32x16_bf16 v[32:47], a[164:167], v[104:107], v[32:47]\nv_cvt_pk_bf16_f32 v136, v116, v117\nv_cvt_pk_bf16_f32 v137, v118, v119\n" \
    "v_cvt_pk_bf16_f32 v138, v120, v121\nv_cvt_pk_bf16_f32 v139, v122, v123\nv_accvgpr_write_b32 a164, v136\nv_accvgpr_write_b32 a165, v137\nv_accvgpr_write_b32 a166, v138\n" \
    "v_accvgpr_write_b32 a167, v139\nv_mfma_f32_32x32x16_bf16 v[16:31], v[136:139], v[128:131], v[16:31]\nds_read_b128 v[96:99], v12 offset:2816\n" \
    "ds_read_b128 v[100:103], v12 offset:2832\nds_read_b128 v[104:107], v13 offset:1408\nv_accvgpr_read_b32 v112, a168\nv_accvgpr_read_b32 v113, a169\n" \
    "v_accvgpr_read_b32 v114, a170\nv_accvgpr_read_b32 v115, a171\nv_lshlrev_b32 v116, 16, v112\nv_and_b32 v117, 0xffff0000, v112\nv_lshlrev_b32 v118, 16, v113\n" \
    "v_and_b32 v119, 0xffff0000, v113\nv_lshlrev_b32 v120, 16, v114\nv_and_b32 v121, 0xffff0000, v114\nv_lshlrev_b32 v122, 16, v115\nv_and_b32 v123, 0xffff0000, v115\n" \
    "s_waitcnt lgkmcnt(6)\nv_mul_f32 v116, v116, v64\nv_mul_f32 v117, v117, v65\nv_mul_f32 v118, v118, v66\nv_mul_f32 v119, v119, v67\nv_mul_f32 v120, v120, v68\n" \
    "v_mul_f32 v121, v121, v69\nv_mul_f32 v122, v122, v70\nv_mul_f32 v123, v123, v71\nv_mfma_f32_32x32x16_bf16 v[32:47], a[168:171], v[72:75], v[32:47]\n" \
    "v_cvt_pk_bf16_f32 v132, v116, v117\nv_cvt_pk_bf16_f32 v133, v118, v119\nv_cvt_pk_bf16_f32 v134, v120, v121\nv_cvt_pk_bf16_f32 v135, v122, v123\n" \
    "v_accvgpr_write_b32 a168, v132\nv_accvgpr_write_b32 a169, v133\nv_accvgpr_write_b32 a170, v134\nv_accvgpr_write_b32 a171, v135\n" \
    "v_mfma_f32_32x32x16_bf16 v[16:31], v[132:135], v[128:131], v[16:31]\nds_read_b128 v[64:67], v12 offset:2880\nds_read_b128 v[68:71], v12 offset:2896\n" \
    "ds_read_b128 v[72:75], v13 offset:1440\nv_accvgpr_read_b32 v112, a172\nv_accvgpr_read_b32 v113, a173\nv_accvgpr_read_b32 v114, a174\nv_accvgpr_read_b32 v115, a175\n" \
    "v_lshlrev_b32 v116, 16, v112\nv_and_b32 v117, 0xffff0000, v112\nv_lshlrev_b32 v118, 16, v113\nv_and_b32 v119, 0xffff0000, v113\nv_lshlrev_b32 v120, 16, v114\n" \
    "v_and_b32 v121, 0xffff0000, v114\nv_lshlrev_b32 v122, 16, v115\nv_and_b32 v123, 0xffff0000, v115\ns_waitcnt lgkmcnt(6)\nv_mul_f32 v116, v116, v80\n" \
    "v_mul_f32 v117, v117, v81\nv_mul_f32 v118, v118, v82\nv_mul_f32 v119, v119, v83\nv_mul_f32 v120, v120, v84\nv_mul_f32 v121, v121, v85\nv_mul_f32 v122, v122, v86\n" \
    "v_mul_f32 v123, v123, v87\nv_mfma_f32_32x32x16_bf16 v[32:47], a[172:175], v[88:91], v[32:47]\nv_cvt_pk_bf16_f32 v136, v116, v117\nv_cvt_pk_bf16_f32 v137, v118, v119\n" \
    "v_cvt_pk_bf16_f32 v138, v120, v121\nv_cvt_pk_bf16_f32 v139, v122, v123\nv_accvgpr_write_b32 a172, v136\nv_accvgpr_write_b32 a173, v137\nv_accvgpr_write_b32 a174, v138\n" \
    "v_accvgpr_write_b32 a175, v139\nv_mfma_f32_32x32x16_bf16 v[16:31], v[136:139], v[128:131], v[16:31]\nds_read_b128 v[80:83], v12 offset:2944\n" \
    "ds_read_b128 v[84:87], v12 offset:2960\nds_read_b128 v[88:91], v13 offset:1472\nv_accvgpr_read_b32 v112, a176\nv_accvgpr_read_b32 v113, a177\n" \
    "v_accvgpr_read_b32 v114, a178\nv_accvgpr_read_b32 v115, a179\nv_lshlrev_b32 v116, 16, v112\nv_and_b32 v117, 0xffff0000, v112\nv_lshlrev_b32 v118, 16, v113\n" \
    "v_and_b32 v119, 0xffff0000, v113\nv_lshlrev_b32 v120, 16, v114\nv_and_b32 v121, 0xffff0000, v114\nv_lshlrev_b32 v122, 16, v115\nv_and_b32 v123, 0xffff0000, v115\n" \
    "s_waitcnt lgkmcnt(6)\nv_mul_f32 v116, v116, v96\nv_mul_f32 v117, v117, v97\nv_mul_f32 v118, v118, v98\nv_mul_f32 v119, v119, v99\nv_mul_f32 v120, v120, v100\n" \
    "v_mul_f32 v121, v121, v101\nv_mul_f32 v122, v122, v102\nv_mul_f32 v123, v123, v103\nv_mfma_f32_32x32x16_bf16 v[32:47], a[176:179], v[104:107], v[32:47]\n" \
    "v_cvt_pk_bf16_f32 v132, v116, v117\nv_cvt_pk_bf16_f32 v133, v118, v119\nv_cvt_pk_bf16_f32 v134, v120, v121\nv_cvt_pk_bf16_f32 v135, v122, v123\n" \
    "v_accvgpr_write_b32 a176, v132\nv_accvgpr_write_b32 a177, v133\nv_accvgpr_write_b32 a178, v134\nv_accvgpr_write_b32 a179, v135\n" \
    "v_mfma_f32_32x32x16_bf16 v[16:31], v[132:135], v[128:131], v[16:31]\nds_read_b128 v[96:99], v12 offset:3008\nds_read_b128 v[100:103], v12 offset:3024\n" \
    "ds_read_b128 v[104:107], v13 offset:1504\nv_accvgpr_read_b32 v112, a180\nv_accvgpr_read_b32 v113, a181\nv_accvgpr_read_b32 v114, a182\nv_accvgpr_read_b32 v115, a183\n" \
    "v_lshlrev_b32 v116, 16, v112\nv_and_b32 v117, 0xffff0000, v112\nv_lshlrev_b32 v118, 16, v113\nv_and_b32 v119, 0xffff0000, v113\nv_lshlrev_b32 v120, 16, v114\n" \
    "v_and_b32 v121, 0xffff0000, v114\nv_lshlrev_b32 v122, 16, v115\nv_and_b32 v123, 0xffff0000, v115\ns_waitcnt lgkmcnt(6)\nv_mul_f32 v116, v116, v64\n" \
    "v_mul_f32 v117, v117, v65\nv_mul_f32 v118, v118, v66\nv_mul_f32 v119, v119, v67\nv_mul_f32 v120, v120, v68\nv_mul_f32 v121, v121, v69\nv_mul_f32 v122, v122, v70\n" \
    "v_mul_f32 v123, v123, v71\nv_mfma_f32_32x32x16_bf16 v[32:47], a[180:183], v[72:75], v[32:47]\nv_cvt_pk_bf16_f32 v136, v116, v117\nv_cvt_pk_bf16_f32 v137, v118, v119\n" \
    "v_cvt_pk_bf16_f32 v138, v120, v121\nv_cvt_pk_bf16_f32 v139, v122, v123\nv_accvgpr_write_b32 a180, v136\nv_accvgpr_write_b32 a181, v137\nv_accvgpr_write_b32 a182, v138\n" \
    "v_accvgpr_write_b32 a183, v139\nv_mfma_f32_32x32x16_bf16 v[16:31], v[136:139], v[128:131], v[16:31]\nds_read_b128 v[64:67], v12 offset:3072\n" \
    "ds_read_b128 v[68:71], v12 offset:3088\nds_read_b128 v[72:75], v13 offset:1536\nv_accvgpr_read_b32 v112, a184\nv_accvgpr_read_b32 v113, a185\n" \
    "v_accvgpr_read_b32 v114, a186\nv_accvgpr_read_b32 v115, a187\nv_lshlrev_b32 v116, 16, v112\nv_and_b32 v117, 0xffff0000, v112\nv_lshlrev_b32 v118, 16, v113\n" \
    "v_and_b32 v119, 0xffff0000, v113\nv_lshlrev_b32 v120, 16, v114\nv_and_b32 v121, 0xffff0000, v114\nv_lshlrev_b32 v122, 16, v115\nv_and_b32 v123, 0xffff0000, v115\n" \
    "s_waitcnt lgkmcnt(6)\nv_mul_f32 v116, v116, v80\nv_mul_f32 v117, v117, v81\nv_mul_f32 v118, v118, v82\nv_mul_f32 v119, v119, v83\nv_mul_f32 v120, v120, v84\n" \
    "v_mul_f32 v121, v121, v85\nv_mul_f32 v122, v122, v86\nv_mul_f32 v123, v123, v87\nv_mfma_f32_32x32x16_bf16 v[32:47], a[184:187], v[88:91], v[32:47]\n" \
    "v_cvt_pk_bf16_f32 v132, v116, v117\nv_cvt_pk_bf16_f32 v133, v118, v119\nv_cvt_pk_bf16_f32 v134, v120, v121\nv_cvt_pk_bf16_f32 v135, v122, v123\n" \
    "v_accvgpr_write_b32 a184, v132\nv_accvgpr_write_b32 a185, v133\nv_accvgpr_write_b32 a186, v134\nv_accvgpr_write_b32 a187, v135\n" \
    "v_mfma_f32_32x32x16_bf16 v[16:31], v[132:135], v[128:131], v[16:31]\nds_read_b128 v[80:83], v12 offset:3136\nds_read_b128 v[84:87], v12 offset:3152\n" \
    "ds_read_b128 v[88:91], v13 offset:1568\nv_accvgpr_read_b32 v112, a188\nv_accvgpr_read_b32 v113, a189\nv_accvgpr_read_b32 v114, a190\nv_accvgpr_read_b32 v115, a191\n" \
    "v_lshlrev_b32 v116, 16, v112\nv_and_b32 v117, 0xffff0000, v112\nv_lshlrev_b32 v118, 16, v113\nv_and_b32 v119, 0xffff0000, v113\nv_lshlrev_b32 v120, 16, v114\n" \
    "v_and_b32 v121, 0xffff0000, v114\nv_lshlrev_b32 v122, 16, v115\nv_and_b32 v123, 0xffff0000, v115\ns_waitcnt lgkmcnt(6)\nv_mul_f32 v116, v116, v96\n" \
    "v_mul_f32 v117, v117, v97\nv_mul_f32 v118, v118, v98\nv_mul_f32 v119, v119, v99\nv_mul_f32 v120, v120, v100\nv_mul_f32 v121, v121, v101\nv_mul_f32 v122, v122, v102\n" \
    "v_mul_f32 v123, v123, v103\nv_mfma_f32_32x32x16_bf16 v[32:47], a[188:191], v[104:107], v[32:47]\nv_cvt_pk_bf16_f32 v136, v116, v117\nv_cvt_pk_bf16_f32 v137, v118, v119\n" \
    "v_cvt_pk_bf16_f32 v138, v120, v121\nv_cvt_pk_bf16_f32 v139, v122, v123\nv_accvgpr_write_b32 a188, v136\nv_accvgpr_write_b32 a189, v137\nv_accvgpr_write_b32 a190, v138\n" \
    "v_accvgpr_write_b32 a191, v139\nv_mfma_f32_32x32x16_bf16 v[16:31], v[136:139], v[128:131], v[16:31]\nds_read_b128 v[96:99], v12 offset:3200\n" \
    "ds_read_b128 v[100:103], v12 offset:3216\nds_read_b128 v[104:107], v13 offset:1600\nv_accvgpr_read_b32 v112, a192\nv_accvgpr_read_b32 v113, a193\n" \
    "v_accvgpr_read_b32 v114, a194\nv_accvgpr_read_b32 v115, a195\nv_lshlrev_b32 v116, 16, v112\nv_and_b32 v117, 0xffff0000, v112\nv_lshlrev_b32 v118, 16, v113\n" \
    "v_and_b32 v119, 0xffff0000, v113\nv_lshlrev_b32 v120, 16, v114\nv_and_b32 v121, 0xffff0000, v114\nv_lshlrev_b32 v122, 16, v115\nv_and_b32 v123, 0xffff0000, v115\n" \
    "s_waitcnt lgkmcnt(6)\nv_mul_f32 v116, v116, v64\nv_mul_f32 v117, v117, v65\nv_mul_f32 v118, v118, v66\nv_mul_f32 v119, v119, v67\nv_mul_f32 v120, v120, v68\n" \
    "v_mul_f32 v121, v121, v69\nv_mul_f32 v122, v122, v70\nv_mul_f32 v123, v123, v71\nv_mfma_f32_32x32x16_bf16 v[32:47], a[192:195], v[72:75], v[32:47]\n" \
    "v_cvt_pk_bf16_f32 v132, v116, v117\nv_cvt_pk_bf16_f32 v133, v118, v119\nv_cvt_pk_bf16_f32 v134, v120, v121\nv_cvt_pk_bf16_f32 v135, v122, v123\n" \
    "v_accvgpr_write_b32 a192, v132\nv_accvgpr_write_b32 a193, v133\nv_accvgpr_write_b32 a194, v134\nv_accvgpr_write_b32 a195, v135\n" \
    "v_mfma_f32_32x32x16_bf16 v[16:31], v[132:135], v[128:131], v[16:31]\nds_read_b128 v[64:67], v12 offset:3264\nds_read_b128 v[68:71], v12 offset:3280\n" \
    "ds_read_b128 v[72:75], v13 offset:1632\nv_accvgpr_read_b32 v112, a196\nv_accvgpr_read_b32 v113, a197\nv_accvgpr_read_b32 v114, a198\nv_accvgpr_read_b32 v115, a199\n" \
    "v_lshlrev_b32 v116, 16, v112\nv_and_b32 v117, 0xffff0000, v112\nv_lshlrev_b32 v118, 16, v113\nv_and_b32 v119, 0xffff0000, v113\nv_lshlrev_b32 v120, 16, v114\n" \
    "v_and_b32 v121, 0xffff0000, v114\nv_lshlrev_b32 v122, 16, v115\nv_and_b32 v123, 0xffff0000, v115\ns_waitcnt lgkmcnt(6)\nv_mul_f32 v116, v116, v80\n" \
    "v_mul_f32 v117, v117, v81\nv_mul_f32 v118, v118, v82\nv_mul_f32 v119, v119, v83\nv_mul_f32 v120, v120, v84\nv_mul_f32 v121, v121, v85\nv_mul_f32 v122, v122, v86\n" \
    "v_mul_f32 v123, v123, v87\nv_mfma_f32_32x32x16_bf16 v[32:47], a[196:199], v[88:91], v[32:47]\nv_cvt_pk_bf16_f32 v136, v116, v117\nv_cvt_pk_bf16_f32 v137, v118, v119\n" \
    "v_cvt_pk_bf16_f32 v138, v120, v121\nv_cvt_pk_bf16_f32 v139, v122, v123\nv_accvgpr_write_b32 a196, v136\nv_accvgpr_write_b32 a197, v137\nv_accvgpr_write_b32 a198, v138\n" \
    "v_accvgpr_write_b32 a199, v139\nv_mfma_f32_32x32x16_bf16 v[16:31], v[136:139], v[128:131], v[16:31]\nds_read_b128 v[80:83], v12 offset:3328\n" \
    "ds_read_b128 v[84:87], v12 offset:3344\nds_read_b128 v[88:91], v13 offset:1664\nv_accvgpr_read_b32 v112, a200\nv_accvgpr_read_b32 v113, a201\n" \
    "v_accvgpr_read_b32 v114, a202\nv_accvgpr_read_b32 v115, a203\nv_lshlrev_b32 v116, 16, v112\nv_and_b32 v117, 0xffff0000, v112\nv_lshlrev_b32 v118, 16, v113\n" \
    "v_and_b32 v119, 0xffff0000, v113\nv_lshlrev_b32 v120, 16, v114\nv_and_b32 v121, 0xffff0000, v114\nv_lshlrev_b32 v122, 16, v115\nv_and_b32 v123, 0xffff0000, v115\n" \
    "s_waitcnt lgkmcnt(6)\nv_mul_f32 v116, v116, v96\nv_mul_f32 v117, v117, v97\nv_mul_f32 v118, v118, v98\nv_mul_f32 v119, v119, v99\nv_mul_f32 v120, v120, v100\n" \
    "v_mul_f32 v121, v121, v101\nv_mul_f32 v122, v122, v102\nv_mul_f32 v123, v123, v103\nv_mfma_f32_32x32x16_bf16 v[32:47], a[200:203], v[104:107], v[32:47]\n" \
    "v_cvt_pk_bf16_f32 v132, v116, v117\nv_cvt_pk_bf16_f32 v133, v118, v119\nv_cvt_pk_bf16_f32 v134, v120, v121\nv_cvt_pk_bf16_f32 v135, v122, v123\n" \
    "v_accvgpr_write_b32 a200, v132\nv_accvgpr_write_b32 a201, v133\nv_accvgpr_write_b32 a202, v134\nv_accvgpr_write_b32 a203, v135\n" \
    "v_mfma_f32_32x32x16_bf16 v[16:31], v[132:135], v[128:131], v[16:31]\nds_read_b128 v[96:99], v12 offset:3392\nds_read_b128 v[100:103], v12 offset:3408\n" \
    "ds_read_b128 v[104:107], v13 offset:1696\nv_accvgpr_read_b32 v112, a204\nv_accvgpr_read_b32 v113, a205\nv_accvgpr_read_b32 v114, a206\nv_accvgpr_read_b32 v115, a207\n" \
    "v_lshlrev_b32 v116, 16, v112\nv_and_b32 v117, 0xffff0000, v112\nv_lshlrev_b32 v118, 16, v113\nv_and_b32 v119, 0xffff0000, v113\nv_lshlrev_b32 v120, 16, v114\n" \
    "v_and_b32 v121, 0xffff0000, v114\nv_lshlrev_b32 v122, 16, v115\nv_and_b32 v123, 0xffff0000, v115\ns_waitcnt lgkmcnt(6)\nv_mul_f32 v116, v116, v64\n" \
    "v_mul_f32 v117, v117, v65\nv_mul_f32 v118, v118, v66\nv_mul_f32 v119, v119, v67\nv_mul_f32 v120, v120, v68\nv_mul_f32 v121, v121, v69\nv_mul_f32 v122, v122, v70\n" \
    "v_mul_f32 v123, v123, v71\nv_mfma_f32_32x32x16_bf16 v[32:47], a[204:207], v[72:75], v[32:47]\nv_cvt_pk_bf16_f32 v136, v116, v117\nv_cvt_pk_bf16_f32 v137, v118, v119\n" \
    "v_cvt_pk_bf16_f32 v138, v120, v121\nv_cvt_pk_bf16_f32 v139, v122, v123\nv_accvgpr_write_b32 a204, v136\nv_accvgpr_write_b32 a205, v137\nv_accvgpr_write_b32 a206, v138\n" \
    "v_accvgpr_write_b32 a207, v139\nv_mfma_f32_32x32x16_bf16 v[16:31], v[136:139], v[128:131], v[16:31]\nds_read_b128 v[64:67], v12 offset:3456\n" \
    "ds_read_b128 v[68:71], v12 offset:3472\nds_read_b128 v[72:75], v13 offset:1728\nv_accvgpr_read_b32 v112, a208\nv_accvgpr_read_b32 v113, a209\n" \
    "v_accvgpr_read_b32 v114, a210\nv_accvgpr_read_b32 v115, a211\nv_lshlrev_b32 v116, 16, v112\nv_and_b32 v117, 0xffff0000, v112\nv_lshlrev_b32 v118, 16, v113\n" \
    "v_and_b32 v119, 0xffff0000, v113\nv_lshlrev_b32 v120, 16, v114\nv_and_b32 v121, 0xffff0000, v114\nv_lshlrev_b32 v122, 16, v115\nv_and_b32 v123, 0xffff0000, v115\n" \
    "s_waitcnt lgkmcnt(6)\nv_mul_f32 v116, v116, v80\nv_mul_f32 v117, v117, v81\nv_mul_f32 v118, v118, v82\nv_mul_f32 v119, v119, v83\nv_mul_f32 v120, v120, v84\n" \
    "v_mul_f32 v121, v121, v85\nv_mul_f32 v122, v122, v86\nv_mul_f32 v123, v123, v87\nv_mfma_f32_32x32x16_bf16 v[32:47], a[208:211], v[88:91], v[32:47]\n" \
    "v_cvt_pk_bf16_f32 v132, v116, v117\nv_cvt_pk_bf16_f32 v133, v118, v119\nv_cvt_pk_bf16_f32 v134, v120, v121\nv_cvt_pk_bf16_f32 v135, v122, v123\n" \
    "v_accvgpr_write_b32 a208, v132\nv_accvgpr_write_b32 a209, v133\nv_accvgpr_write_b32 a210, v134\nv_accvgpr_write_b32 a211, v135\n" \
    "v_mfma_f32_32x32x16_bf16 v[16:31], v[132:135], v[128:131], v[16:31]\nds_read_b128 v[80:83], v12 offset:3520\nds_read_b128 v[84:87], v12 offset:3536\n" \
    "ds_read_b128 v[88:91], v13 offset:1760\nv_accvgpr_read_b32 v112, a212\nv_accvgpr_read_b32 v113, a213\nv_accvgpr_read_b32 v114, a214\nv_accvgpr_read_b32 v115, a215\n" \
    "v_lshlrev_b32 v116, 16, v112\nv_and_b32 v117, 0xffff0000, v112\nv_lshlrev_b32 v118, 16, v113\nv_and_b32 v119, 0xffff0000, v113\nv_lshlrev_b32 v120, 16, v114\n" \
    "v_and_b32 v121, 0xffff0000, v114\nv_lshlrev_b32 v122, 16, v115\nv_and_b32 v123, 0xffff0000, v115\ns_waitcnt lgkmcnt(6)\nv_mul_f32 v116, v116, v96\n" \
    "v_mul_f32 v117, v117, v97\nv_mul_f32 v118, v118, v98\nv_mul_f32 v119, v119, v99\nv_mul_f32 v120, v120, v100\nv_mul_f32 v121, v121, v101\nv_mul_f32 v122, v122, v102\n" \
    "v_mul_f32 v123, v123, v103\nv_mfma_f32_32x32x16_bf16 v[32:47], a[212:215], v[104:107], v[32:47]\nv_cvt_pk_bf16_f32 v136, v116, v117\nv_cvt_pk_bf16_f32 v137, v118, v119\n" \
    "v_cvt_pk_bf16_f32 v138, v120, v121\nv_cvt_pk_bf16_f32 v139, v122, v123\nv_accvgpr_write_b32 a212, v136\nv_accvgpr_write_b32 a213, v137\nv_accvgpr_write_b32 a214, v138\n" \
    "v_accvgpr_write_b32 a215, v139\nv_mfma_f32_32x32x16_bf16 v[16:31], v[136:139], v[128:131], v[16:31]\nds_read_b128 v[96:99], v12 offset:3584\n" \
    "ds_read_b128 v[100:103], v12 offset:3600\nds_read_b128 v[104:107], v13 offset:1792\nv_accvgpr_read_b32 v112, a216\nv_accvgpr_read_b32 v113, a217\n" \
    "v_accvgpr_read_b32 v114, a218\nv_accvgpr_read_b32 v115, a219\nv_lshlrev_b32 v116, 16, v112\nv_and_b32 v117, 0xffff0000, v112\nv_lshlrev_b32 v118, 16, v113\n" \
    "v_and_b32 v119, 0xffff0000, v113\nv_lshlrev_b32 v120, 16, v114\nv_and_b32 v121, 0xffff0000, v114\nv_lshlrev_b32 v122, 16, v115\nv_and_b32 v123, 0xffff0000, v115\n" \
    "s_waitcnt lgkmcnt(6)\nv_mul_f32 v116, v116, v64\nv_mul_f32 v117, v117, v65\nv_mul_f32 v118, v118, v66\nv_mul_f32 v119, v119, v67\nv_mul_f32 v120, v120, v68\n" \
    "v_mul_f32 v121, v121, v69\nv_mul_f32 v122, v122, v70\nv_mul_f32 v123, v123, v71\nv_mfma_f32_32x32x16_bf16 v[32:47], a[216:219], v[72:75], v[32:47]\n" \
    "v_cvt_pk_bf16_f32 v132, v116, v117\nv_cvt_pk_bf16_f32 v133, v118, v119\nv_cvt_pk_bf16_f32 v134, v120, v121\nv_cvt_pk_bf16_f32 v135, v122, v123\n" \
    "v_accvgpr_write_b32 a216, v132\nv_accvgpr_write_b32 a217, v133\nv_accvgpr_write_b32 a218, v134\nv_accvgpr_write_b32 a219, v135\n" \
    "v_mfma_f32_32x32x16_bf16 v[16:31], v[132:135], v[128:131], v[16:31]\nds_read_b128 v[64:67], v12 offset:3648\nds_read_b128 v[68:71], v12 offset:3664\n" \
    "ds_read_b128 v[72:75], v13 offset:1824\nv_accvgpr_read_b32 v112, a220\nv_accvgpr_read_b32 v113, a221\nv_accvgpr_read_b32 v114, a222\nv_accvgpr_read_b32 v115, a223\n" \
    "v_lshlrev_b32 v116, 16, v112\nv_and_b32 v117, 0xffff0000, v112\nv_lshlrev_b32 v118, 16, v113\nv_and_b32 v119, 0xffff0000, v113\nv_lshlrev_b32 v120, 16, v114\n" \
    "v_and_b32 v121, 0xffff0000, v114\nv_lshlrev_b32 v122, 16, v115\nv_and_b32 v123, 0xffff0000, v115\ns_waitcnt lgkmcnt(6)\nv_mul_f32 v116, v116, v80\n" \
    "v_mul_f32 v117, v117, v81\nv_mul_f32 v118, v118, v82\nv_mul_f32 v119, v119, v83\nv_mul_f32 v120, v120, v84\nv_mul_f32 v121, v121, v85\nv_mul_f32 v122, v122, v86\n" \
    "v_mul_f32 v123, v123, v87\nv_mfma_f32_32x32x16_bf16 v[32:47], a[220:223], v[88:91], v[32:47]\nv_cvt_pk_bf16_f32 v136, v116, v117\nv_cvt_pk_bf16_f32 v137, v118, v119\n" \
    "v_cvt_pk_bf16_f32 v138, v120, v121\nv_cvt_pk_bf16_f32 v139, v122, v123\nv_accvgpr_write_b32 a220, v136\nv_accvgpr_write_b32 a221, v137\nv_accvgpr_write_b32 a222, v138\n" \
    "v_accvgpr_write_b32 a223, v139\nv_mfma_f32_32x32x16_bf16 v[16:31], v[136:139], v[128:131], v[16:31]\nds_read_b128 v[80:83], v12 offset:3712\n" \
    "ds_read_b128 v[84:87], v12 offset:3728\nds_read_b128 v[88:91], v13 offset:1856\nv_accvgpr_read_b32 v112, a224\nv_accvgpr_read_b32 v113, a225\n" \
    "v_accvgpr_read_b32 v114, a226\nv_accvgpr_read_b32 v115, a227\nv_lshlrev_b32 v116, 16, v112\nv_and_b32 v117, 0xffff0000, v112\nv_lshlrev_b32 v118, 16, v113\n" \
    "v_and_b32 v119, 0xffff0000, v113\nv_lshlrev_b32 v120, 16, v114\nv_and_b32 v121, 0xffff0000, v114\nv_lshlrev_b32 v122, 16, v115\nv_and_b32 v123, 0xffff0000, v115\n" \
    "s_waitcnt lgkmcnt(6)\nv_mul_f32 v116, v116, v96\nv_mul_f32 v117, v117, v97\nv_mul_f32 v118, v118, v98\nv_mul_f32 v119, v119, v99\nv_mul_f32 v120, v120, v100\n" \
    "v_mul_f32 v121, v121, v101\nv_mul_f32 v122, v122, v102\nv_mul_f32 v123, v123, v103\nv_mfma_f32_32x32x16_bf16 v[32:47], a[224:227], v[104:107], v[32:47]\n" \
    "v_cvt_pk_bf16_f32 v132, v116, v117\nv_cvt_pk_bf16_f32 v133, v118, v119\nv_cvt_pk_bf16_f32 v134, v120, v121\nv_cvt_pk_bf16_f32 v135, v122, v123\n" \
    "v_accvgpr_write_b32 a224, v132\nv_accvgpr_write_b32 a225, v133\nv_accvgpr_write_b32 a226, v134\nv_accvgpr_write_b32 a227, v135\n" \
    "v_mfma_f32_32x32x16_bf16 v[16:31], v[132:135], v[128:131], v[16:31]\nds_read_b128 v[96:99], v12 offset:3776\nds_read_b128 v[100:103], v12 offset:3792\n" \
    "ds_read_b128 v[104:107], v13 offset:1888\nv_accvgpr_read_b32 v112, a228\nv_accvgpr_read_b32 v113, a229\nv_accvgpr_read_b32 v114, a230\nv_accvgpr_read_b32 v115, a231\n" \
    "v_lshlrev_b32 v116, 16, v112\nv_and_b32 v117, 0xffff0000, v112\nv_lshlrev_b32 v118, 16, v113\nv_and_b32 v119, 0xffff0000, v113\nv_lshlrev_b32 v120, 16, v114\n" \
    "v_and_b32 v121, 0xffff0000, v114\nv_lshlrev_b32 v122, 16, v115\nv_and_b32 v123, 0xffff0000, v115\ns_waitcnt lgkmcnt(6)\nv_mul_f32 v116, v116, v64\n" \
    "v_mul_f32 v117, v117, v65\nv_mul_f32 v118, v118, v66\nv_mul_f32 v119, v119, v67\nv_mul_f32 v120, v120, v68\nv_mul_f32 v121, v121, v69\nv_mul_f32 v122, v122, v70\n" \
    "v_mul_f32 v123, v123, v71\nv_mfma_f32_32x32x16_bf16 v[32:47], a[228:231], v[72:75], v[32:47]\nv_cvt_pk_bf16_f32 v136, v116, v117\nv_cvt_pk_bf16_f32 v137, v118, v119\n" \
    "v_cvt_pk_bf16_f32 v138, v120, v121\nv_cvt_pk_bf16_f32 v139, v122, v123\nv_accvgpr_write_b32 a228, v136\nv_accvgpr_write_b32 a229, v137\nv_accvgpr_write_b32 a230, v138\n" \
    "v_accvgpr_write_b32 a231, v139\nv_mfma_f32_32x32x16_bf16 v[16:31], v[136:139], v[128:131], v[16:31]\nds_read_b128 v[64:67], v12 offset:3840\n" \
    "ds_read_b128 v[68:71], v12 offset:3856\nds_read_b128 v[72:75], v13 offset:1920\nv_accvgpr_read_b32 v112, a232\nv_accvgpr_read_b32 v113, a233\n" \
    "v_accvgpr_read_b32 v114, a234\nv_accvgpr_read_b32 v115, a235\nv_lshlrev_b32 v116, 16, v112\nv_and_b32 v117, 0xffff0000, v112\nv_lshlrev_b32 v118, 16, v113\n" \
    "v_and_b32 v119, 0xffff0000, v113\nv_lshlrev_b32 v120, 16, v114\nv_and_b32 v121, 0xffff0000, v114\nv_lshlrev_b32 v122, 16, v115\nv_and_b32 v123, 0xffff0000, v115\n" \
    "s_waitcnt lgkmcnt(6)\nv_mul_f32 v116, v116, v80\nv_mul_f32 v117, v117, v81\nv_mul_f32 v118, v118, v82\nv_mul_f32 v119, v119, v83\nv_mul_f32 v120, v120, v84\n" \
    "v_mul_f32 v121, v121, v85\nv_mul_f32 v122, v122, v86\nv_mul_f32 v123, v123, v87\nv_mfma_f32_32x32x16_bf16 v[32:47], a[232:235], v[88:91], v[32:47]\n" \
    "v_cvt_pk_bf16_f32 v132, v116, v117\nv_cvt_pk_bf16_f32 v133, v118, v119\nv_cvt_pk_bf16_f32 v134, v120, v121\nv_cvt_pk_bf16_f32 v135, v122, v123\n" \
    "v_accvgpr_write_b32 a232, v132\nv_accvgpr_write_b32 a233, v133\nv_accvgpr_write_b32 a234, v134\nv_accvgpr_write_b32 a235, v135\n" \
    "v_mfma_f32_32x32x16_bf16 v[16:31], v[132:135], v[128:131], v[16:31]\nds_read_b128 v[80:83], v12 offset:3904\nds_read_b128 v[84:87], v12 offset:3920\n" \
    "ds_read_b128 v[88:91], v13 offset:1952\nv_accvgpr_read_b32 v112, a236\nv_accvgpr_read_b32 v113, a237\nv_accvgpr_read_b32 v114, a238\nv_accvgpr_read_b32 v115, a239\n" \
    "v_lshlrev_b32 v116, 16, v112\nv_and_b32 v117, 0xffff0000, v112\nv_lshlrev_b32 v118, 16, v113\nv_and_b32 v119, 0xffff0000, v113\nv_lshlrev_b32 v120, 16, v114\n" \
    "v_and_b32 v121, 0xffff0000, v114\nv_lshlrev_b32 v122, 16, v115\nv_and_b32 v123, 0xffff0000, v115\ns_waitcnt lgkmcnt(6)\nv_mul_f32 v116, v116, v96\n" \
    "v_mul_f32 v117, v117, v97\nv_mul_f32 v118, v118, v98\nv_mul_f32 v119, v119, v99\nv_mul_f32 v120, v120, v100\nv_mul_f32 v121, v121, v101\nv_mul_f32 v122, v122, v102\n" \
    "v_mul_f32 v123, v123, v103\nv_mfma_f32_32x32x16_bf16 v[32:47], a[236:239], v[104:107], v[32:47]\nv_cvt_pk_bf16_f32 v136, v116, v117\nv_cvt_pk_bf16_f32 v137, v118, v119\n" \
    "v_cvt_pk_bf16_f32 v138, v120, v121\nv_cvt_pk_bf16_f32 v139, v122, v123\nv_accvgpr_write_b32 a236, v136\nv_accvgpr_write_b32 a237, v137\nv_accvgpr_write_b32 a238, v138\n" \
    "v_accvgpr_write_b32 a239, v139\nv_mfma_f32_32x32x16_bf16 v[16:31], v[136:139], v[128:131], v[16:31]\nds_read_b128 v[96:99], v12 offset:3968\n" \
    "ds_read_b128 v[100:103], v12 offset:3984\nds_read_b128 v[104:107], v13 offset:1984\nv_accvgpr_read_b32 v112, a240\nv_accvgpr_read_b32 v113, a241\n" \
    "v_accvgpr_read_b32 v114, a242\nv_accvgpr_read_b32 v115, a243\nv_lshlrev_b32 v116, 16, v112\nv_and_b32 v117, 0xffff0000, v112\nv_lshlrev_b32 v118, 16, v113\n" \
    "v_and_b32 v119, 0xffff0000, v113\nv_lshlrev_b32 v120, 16, v114\nv_and_b32 v121, 0xffff0000, v114\nv_lshlrev_b32 v122, 16, v115\nv_and_b32 v123, 0xffff0000, v115\n" \
    "s_waitcnt lgkmcnt(6)\nv_mul_f32 v116, v116, v64\nv_mul_f32 v117, v117, v65\nv_mul_f32 v118, v118, v66\nv_mul_f32 v119, v119, v67\nv_mul_f32 v120, v120, v68\n" \
    "v_mul_f32 v121, v121, v69\nv_mul_f32 v122, v122, v70\nv_mul_f32 v123, v123, v71\nv_mfma_f32_32x32x16_bf16 v[32:47], a[240:243], v[72:75], v[32:47]\n" \
    "v_cvt_pk_bf16_f32 v132, v116, v117\nv_cvt_pk_bf16_f32 v133, v118, v119\nv_cvt_pk_bf16_f32 v134, v120, v121\nv_cvt_pk_bf16_f32 v135, v122, v123\n" \
    "v_accvgpr_write_b32 a240, v132\nv_accvgpr_write_b32 a241, v133\nv_accvgpr_write_b32 a242, v134\nv_accvgpr_write_b32 a243, v135\n" \
    "v_mfma_f32_32x32x16_bf16 v[16:31], v[132:135], v[128:131], v[16:31]\nds_read_b128 v[64:67], v12 offset:4032\nds_read_b128 v[68:71], v12 offset:4048\n" \
    "ds_read_b128 v[72:75], v13 offset:2016\nv_accvgpr_read_b32 v112, a244\nv_accvgpr_read_b32 v113, a245\nv_accvgpr_read_b32 v114, a246\nv_accvgpr_read_b32 v115, a247\n" \
    "v_lshlrev_b32 v116, 16, v112\nv_and_b32 v117, 0xffff0000, v112\nv_lshlrev_b32 v118, 16, v113\nv_and_b32 v119, 0xffff0000, v113\nv_lshlrev_b32 v120, 16, v114\n" \
    "v_and_b32 v121, 0xffff0000, v114\nv_lshlrev_b32 v122, 16, v115\nv_and_b32 v123, 0xffff0000, v115\ns_waitcnt lgkmcnt(6)\nv_mul_f32 v116, v116, v80\n" \
    "v_mul_f32 v117, v117, v81\nv_mul_f32 v118, v118, v82\nv_mul_f32 v119, v119, v83\nv_mul_f32 v120, v120, v84\nv_mul_f32 v121, v121, v85\nv_mul_f32 v122, v122, v86\n" \
    "v_mul_f32 v123, v123, v87\nv_mfma_f32_32x32x16_bf16 v[32:47], a[244:247], v[88:91], v[32:47]\nv_cvt_pk_bf16_f32 v136, v116, v117\nv_cvt_pk_bf16_f32 v137, v118, v119\n" \
    "v_cvt_pk_bf16_f32 v138, v120, v121\nv_cvt_pk_bf16_f32 v139, v122, v123\nv_accvgpr_write_b32 a244, v136\nv_accvgpr_write_b32 a245, v137\nv_accvgpr_write_b32 a246, v138\n" \
    "v_accvgpr_write_b32 a247, v139\nv_mfma_f32_32x32x16_bf16 v[16:31], v[136:139], v[128:131], v[16:31]\nv_accvgpr_read_b32 v112, a248\nv_accvgpr_read_b32 v113, a249\n" \
    "v_accvgpr_read_b32 v114, a250\nv_accvgpr_read_b32 v115, a251\nv_lshlrev_b32 v116, 16, v112\nv_and_b32 v117, 0xffff0000, v112\nv_lshlrev_b32 v118, 16, v113\n" \
    "v_and_b32 v119, 0xffff0000, v113\nv_lshlrev_b32 v120, 16, v114\nv_and_b32 v121, 0xffff0000, v114\nv_lshlrev_b32 v122, 16, v115\nv_and_b32 v123, 0xffff0000, v115\n" \
    "s_waitcnt lgkmcnt(3)\nv_mul_f32 v116, v116, v96\nv_mul_f32 v117, v117, v97\nv_mul_f32 v118, v118, v98\nv_mul_f32 v119, v119, v99\nv_mul_f32 v120, v120, v100\n" \
    "v_mul_f32 v121, v121, v101\nv_mul_f32 v122, v122, v102\nv_mul_f32 v123, v123, v103\nv_mfma_f32_32x32x16_bf16 v[32:47], a[248:251], v[104:107], v[32:47]\n" \
    "v_cvt_pk_bf16_f32 v132, v116, v117\nv_cvt_pk_bf16_f32 v133, v118, v119\nv_cvt_pk_bf16_f32 v134, v120, v121\nv_cvt_pk_bf16_f32 v135, v122, v123\n" \
    "v_accvgpr_write_b32 a248, v132\nv_accvgpr_write_b32 a249, v133\nv_accvgpr_write_b32 a250, v134\nv_accvgpr_write_b32 a251, v135\n" \
    "v_mfma_f32_32x32x16_bf16 v[16:31], v[132:135], v[128:131], v[16:31]\nv_accvgpr_read_b32 v112, a252\nv_accvgpr_read_b32 v113, a253\nv_accvgpr_read_b32 v114, a254\n" \
    "v_accvgpr_read_b32 v115, a255\nv_lshlrev_b32 v116, 16, v112\nv_and_b32 v117, 0xffff0000, v112\nv_lshlrev_b32 v118, 16, v113\nv_and_b32 v119, 0xffff0000, v113\n" \
    "v_lshlrev_b32 v120, 16, v114\nv_and_b32 v121, 0xffff0000, v114\nv_lshlrev_b32 v122, 16, v115\nv_and_b32 v123, 0xffff0000, v115\ns_waitcnt lgkmcnt(0)\n" \
    "v_mul_f32 v116, v116, v64\nv_mul_f32 v117, v117, v65\nv_mul_f32 v118, v118, v66\nv_mul_f32 v119, v119, v67\nv_mul_f32 v120, v120, v68\nv_mul_f32 v121, v121, v69\n" \
    "v_mul_f32 v122, v122, v70\nv_mul_f32 v123, v123, v71\nv_mfma_f32_32x32x16_bf16 v[32:47], a[252:255], v[72:75], v[32:47]\nv_cvt_pk_bf16_f32 v136, v116, v117\n" \
    "v_cvt_pk_bf16_f32 v137, v118, v119\nv_cvt_pk_bf16_f32 v138, v120, v121\nv_cvt_pk_bf16_f32 v139, v122, v123\nv_accvgpr_write_b32 a252, v136\nv_accvgpr_write_b32 a253, v137\n" \
    "v_accvgpr_write_b32 a254, v138\nv_accvgpr_write_b32 a255, v139\nv_mfma_f32_32x32x16_bf16 v[16:31], v[136:139], v[128:131], v[16:31]\ns_mov_b32 s90, 0x7fff0000\n" \
    "s_mov_b32 s91, 0x7fff0000\ns_mov_b32 s92, 0x7fff0000\ns_mov_b32 s93, 0x7fff0000\ns_mov_b32 s80, 0\ns_mov_b32 s81, 0\ns_mov_b32 s68, 0\n.Lf2_%=:\ns_xor_b32 s85, s38, 512\n" \
    "s_and_b32 s69, s32, 63\ns_lshl_b32 s69, s69, 19\ns_add_u32 s82, s36, s69\ns_lshr_b32 s69, s82, 8\ns_and_b32 s69, s69, 0x2000\ns_xor_b32 s82, s82, s69\n" \
    "s_add_u32 s69, s32, 1\ns_and_b32 s69, s69, 63\ns_lshl_b32 s69, s69, 19\ns_add_u32 s83, s36, s69\ns_lshr_b32 s69, s83, 8\ns_and_b32 s69, s69, 0x2000\n" \
    "s_xor_b32 s83, s83, s69\ns_add_u32 s84, s37, s80\ns_cmp_eq_u32 s81, 1\ns_cbranch_scc1 .Lf6_%=\ns_add_u32 s77, s32, 2\ns_cmp_eq_u32 s81, 2\ns_cbranch_scc1 .Lf5_%=\n" \
    ".Lf3_%=:\nbuffer_load_dword v150, off, s[64:67], s38 sc1\ns_waitcnt vmcnt(0)\nv_and_b32 v112, 127, v150\nv_cmp_gt_u32 vcc, s77, v112\ns_nop 0\ns_cbranch_vccnz .Lf4_%=\n" \
    ".Lf5_%=:\nv_and_b32 v112, 127, v150\n.Lf11_%=:\ns_memrealtime s[72:73]\ns_waitcnt lgkmcnt(0)\ns_cmp_ge_u32 s77, 62\ns_cselect_b32 s69, s61, s34\ns_cmp_ge_u32 s77, 64\n" \
    "s_cselect_b32 s69, s60, s69\ns_sub_u32 s72, s72, s69\ns_lshl_b32 s72, s72, 8\nv_sub_u32 v113, s72, v150\nv_cmp_gt_i32 s[74:75], 0, v113\nv_cmp_eq_u32 vcc, s77, v112\n" \
    "s_nop 0\ns_and_b64 vcc, vcc, s[74:75]\ns_cbranch_scc1 .Lf11_%=\n.Lf12_%=:\nbuffer_load_dword v151, v2, s[64:67], s82 offen sc1\ns_add_u32 m0, s84, 0\n" \
    "s_add_u32 s69, s82, s48\nbuffer_load_dwordx4 v0, s[64:67], s69 offen offset:0 sc1 lds\nbuffer_load_dwordx4 v0, s[64:67], s69 offen offset:1024 sc1 lds\n" \
    "s_add_u32 m0, s84, 8192\ns_add_u32 s69, s82, s49\nbuffer_load_dwordx4 v0, s[64:67], s69 offen offset:0 sc1 lds\n" \
    "buffer_load_dwordx4 v0, s[64:67], s69 offen offset:1024 sc1 lds\ns_add_u32 m0, s84, 16384\ns_add_u32 s69, s82, s50\n" \
    "buffer_load_dwordx4 v0, s[64:67], s69 offen offset:0 sc1 lds\nbuffer_load_dwordx4 v0, s[64:67], s69 offen offset:1024 sc1 lds\n.Lf6_%=:\ns_waitcnt vmcnt(6)\n" \
    "v_cmp_eq_u32 vcc, -1, v151\ns_nop 0\ns_cbranch_vccnz .Lf13_%=\ns_cmp_lg_u32 s33, 0\ns_cbranch_scc1 .Lf15_%=\ns_memrealtime s[72:73]\ns_waitcnt lgkmcnt(0)\n" \
    "s_lshl_b32 s72, s72, 8\ns_add_u32 s72, s72, s32\ns_add_u32 s72, s72, 1\nv_mov_b32 v112, s72\ns_mov_b64 exec, 1\nbuffer_store_dword v112, off, s[64:67], s39 sc1\n" \
    "s_mov_b64 exec, -1\n.Lf15_%=:\ns_add_u32 s77, s32, 3\ns_add_u32 m0, s84, 24576\ns_add_u32 s69, s82, s51\nbuffer_load_dwordx4 v0, s[64:67], s69 offen offset:0 sc1 lds\n" \
    "buffer_load_dwordx4 v0, s[64:67], s69 offen offset:1024 sc1 lds\ns_waitcnt vmcnt(6)\ns_barrier\nds_read_b128 v[64:67], v1 offset:0\nds_read_b128 v[68:71], v1 offset:1024\n" \
    "ds_read_b128 v[72:75], v1 offset:2048\nds_read_b128 v[76:79], v1 offset:3072\ns_waitcnt lgkmcnt(3)\nv_mfma_f32_32x32x16_bf16 v[48:63], a[0:3], v[64:67], 0\n" \
    "ds_read_b128 v[80:83], v1 offset:4096\ns_add_u32 s69, s82, s41\nbuffer_load_dwordx4 v[96:99], v0, s[64:67], s69 offen offset:0 sc1\ns_waitcnt lgkmcnt(3)\n" \
    "v_mfma_f32_32x32x16_bf16 v[48:63], a[4:7], v[68:71], v[48:63]\nds_read_b128 v[84:87], v1 offset:5120\n" \
    "buffer_load_dwordx4 v[100:103], v0, s[64:67], s69 offen offset:1024 sc1\ns_waitcnt lgkmcnt(3)\nv_mfma_f32_32x32x16_bf16 v[48:63], a[8:11], v[72:75], v[48:63]\n" \
    "ds_read_b128 v[88:91], v1 offset:6144\nbuffer_load_dwordx4 v[104:107], v0, s[64:67], s69 offen offset:2048 sc1\ns_waitcnt lgkmcnt(3)\n" \
    "v_mfma_f32_32x32x16_bf16 v[48:63], a[12:15], v[76:79], v[48:63]\nds_read_b128 v[92:95], v1 offset:7168\n" \
    "buffer_load_dwordx4 v[108:111], v0, s[64:67], s69 offen offset:3072 sc1\ns_waitcnt vmcnt(8) lgkmcnt(3)\ns_barrier\n" \
    "v_mfma_f32_32x32x16_bf16 v[48:63], a[16:19], v[80:83], v[48:63]\nds_read_b128 v[64:67], v1 offset:8192\ns_add_u32 m0, s84, 32768\ns_add_u32 s69, s82, s52\n" \
    "buffer_load_dwordx4 v0, s[64:67], s69 offen offset:0 sc1 lds\ns_waitcnt lgkmcnt(3)\nv_mfma_f32_32x32x16_bf16 v[48:63], a[20:23], v[84:87], v[48:63]\n" \
    "ds_read_b128 v[68:71], v1 offset:9216\ns_waitcnt lgkmcnt(3)\nv_mfma_f32_32x32x16_bf16 v[48:63], a[24:27], v[88:91], v[48:63]\nds_read_b128 v[72:75], v1 offset:10240\n" \
    "buffer_load_dwordx4 v0, s[64:67], s69 offen offset:1024 sc1 lds\ns_waitcnt lgkmcnt(3)\nv_mfma_f32_32x32x16_bf16 v[48:63], a[28:31], v[92:95], v[48:63]\n" \
    "ds_read_b128 v[76:79], v1 offset:11264\ns_add_u32 m0, s84, 40960\ns_add_u32 s69, s82, s53\nbuffer_load_dwordx4 v0, s[64:67], s69 offen offset:0 sc1 lds\n" \
    "s_waitcnt lgkmcnt(3)\nv_mfma_f32_32x32x16_bf16 v[48:63], a[32:35], v[64:67], v[48:63]\nds_read_b128 v[80:83], v1 offset:12288\ns_waitcnt lgkmcnt(3)\n" \
    "v_mfma_f32_32x32x16_bf16 v[48:63], a[36:39], v[68:71], v[48:63]\nds_read_b128 v[84:87], v1 offset:13312\nbuffer_load_dwordx4 v0, s[64:67], s69 offen offset:1024 sc1 lds\n" \
    "s_waitcnt lgkmcnt(3)\nv_mfma_f32_32x32x16_bf16 v[48:63], a[40:43], v[72:75], v[48:63]\nds_read_b128 v[88:91], v1 offset:14336\ns_add_u32 m0, s84, 49152\n" \
    "s_add_u32 s69, s82, s54\nbuffer_load_dwordx4 v0, s[64:67], s69 offen offset:0 sc1 lds\ns_waitcnt lgkmcnt(3)\n" \
    "v_mfma_f32_32x32x16_bf16 v[48:63], a[44:47], v[76:79], v[48:63]\nds_read_b128 v[92:95], v1 offset:15360\ns_waitcnt vmcnt(9) lgkmcnt(3)\ns_barrier\n" \
    "v_mfma_f32_32x32x16_bf16 v[48:63], a[48:51], v[80:83], v[48:63]\nds_read_b128 v[64:67], v1 offset:16384\nbuffer_load_dwordx4 v0, s[64:67], s69 offen offset:1024 sc1 lds\n" \
    "s_waitcnt lgkmcnt(3)\nv_mfma_f32_32x32x16_bf16 v[48:63], a[52:55], v[84:87], v[48:63]\nds_read_b128 v[68:71], v1 offset:17408\ns_add_u32 m0, s84, 57344\n" \
    "s_add_u32 s69, s82, s55\nbuffer_load_dwordx4 v0, s[64:67], s69 offen offset:0 sc1 lds\ns_waitcnt lgkmcnt(3)\n" \
    "v_mfma_f32_32x32x16_bf16 v[48:63], a[56:59], v[88:91], v[48:63]\nds_read_b128 v[72:75], v1 offset:18432\ns_waitcnt lgkmcnt(3)\n" \
    "v_mfma_f32_32x32x16_bf16 v[48:63], a[60:63], v[92:95], v[48:63]\nds_read_b128 v[76:79], v1 offset:19456\nbuffer_load_dwordx4 v0, s[64:67], s69 offen offset:1024 sc1 lds\n" \
    "s_waitcnt lgkmcnt(3)\nv_mfma_f32_32x32x16_bf16 v[48:63], a[64:67], v[64:67], v[48:63]\nds_read_b128 v[80:83], v1 offset:20480\ns_and_b32 s69, s32, 63\n" \
    "s_lshl_b32 s69, s69, 19\ns_add_u32 s82, s40, s69\ns_lshr_b32 s69, s82, 8\ns_and_b32 s69, s69, 0x2000\ns_waitcnt lgkmcnt(3)\n" \
    "v_mfma_f32_32x32x16_bf16 v[48:63], a[68:71], v[68:71], v[48:63]\nds_read_b128 v[84:87], v1 offset:21504\ns_xor_b32 s82, s82, s69\ns_waitcnt vmcnt(8)\ns_waitcnt lgkmcnt(3)\n" \
    "v_readfirstlane_b32 s76, v150\nv_max3_u32 v116, v96, v97, v98\nv_max3_u32 v116, v116, v99, v100\nv_mfma_f32_32x32x16_bf16 v[48:63], a[72:75], v[72:75], v[48:63]\n" \
    "ds_read_b128 v[88:91], v1 offset:22528\ns_and_b32 s76, s76, 127\ns_waitcnt lgkmcnt(3)\nv_max3_u32 v116, v116, v101, v102\nv_max3_u32 v116, v116, v103, v104\n" \
    "v_max3_u32 v116, v116, v105, v106\nv_mfma_f32_32x32x16_bf16 v[48:63], a[76:79], v[76:79], v[48:63]\nds_read_b128 v[92:95], v1 offset:23552\ns_waitcnt lgkmcnt(3)\n" \
    "v_max3_u32 v116, v116, v107, v108\nv_max3_u32 v116, v116, v109, v110\nv_max_u32 v116, v116, v111\nv_mfma_f32_32x32x16_bf16 v[48:63], a[80:83], v[80:83], v[48:63]\n" \
    "ds_read_b128 v[64:67], v1 offset:24576\ns_waitcnt lgkmcnt(3)\nv_cmp_eq_u32 s[70:71], -1, v116\nv_sub_f32 v114, v96, v100\nv_add_f32 v96, v96, v100\n" \
    "v_mfma_f32_32x32x16_bf16 v[48:63], a[84:87], v[84:87], v[48:63]\nds_read_b128 v[68:71], v1 offset:25600\ns_waitcnt lgkmcnt(3)\nv_add_f32 v97, v97, v101\n" \
    "v_mul_f32 v114, v114, v114\nv_mul_f32 v96, 0.5, v96\nv_mfma_f32_32x32x16_bf16 v[48:63], a[88:91], v[88:91], v[48:63]\nds_read_b128 v[72:75], v1 offset:26624\n" \
    "s_waitcnt lgkmcnt(3)\nv_fmac_f32 v97, 0x41000000, v114\nv_sub_f32 v114, v104, v108\nv_add_f32 v104, v104, v108\n" \
    "v_mfma_f32_32x32x16_bf16 v[48:63], a[92:95], v[92:95], v[48:63]\nds_read_b128 v[76:79], v1 offset:27648\ns_waitcnt lgkmcnt(3)\nv_add_f32 v105, v105, v109\n" \
    "v_mul_f32 v114, v114, v114\nv_mul_f32 v104, 0.5, v104\nv_mfma_f32_32x32x16_bf16 v[48:63], a[96:99], v[64:67], v[48:63]\nds_read_b128 v[80:83], v1 offset:28672\n" \
    "s_waitcnt lgkmcnt(3)\nv_fmac_f32 v105, 0x41000000, v114\nv_sub_f32 v114, v96, v104\nv_add_f32 v96, v96, v104\n" \
    "v_mfma_f32_32x32x16_bf16 v[48:63], a[100:103], v[68:71], v[48:63]\nds_read_b128 v[84:87], v1 offset:29696\ns_waitcnt lgkmcnt(3)\nv_add_f32 v97, v97, v105\n" \
    "v_mul_f32 v114, v114, v114\nv_mul_f32 v96, 0.5, v96\nv_mfma_f32_32x32x16_bf16 v[48:63], a[104:107], v[72:75], v[48:63]\nds_read_b128 v[88:91], v1 offset:30720\n" \
    "s_waitcnt lgkmcnt(3)\nv_fmac_f32 v97, 0x41800000, v114\nv_sub_f32 v114, v98, v102\nv_add_f32 v98, v98, v102\n" \
    "v_mfma_f32_32x32x16_bf16 v[48:63], a[108:111], v[76:79], v[48:63]\nds_read_b128 v[92:95], v1 offset:31744\ns_waitcnt vmcnt(4) lgkmcnt(3)\ns_barrier\n" \
    "v_add_f32 v99, v99, v103\nv_mul_f32 v114, v114, v114\nv_mul_f32 v98, 0.5, v98\nv_mfma_f32_32x32x16_bf16 v[48:63], a[112:115], v[80:83], v[48:63]\n" \
    "ds_read_b128 v[64:67], v1 offset:32768\ns_waitcnt lgkmcnt(3)\nv_fmac_f32 v99, 0x41000000, v114\nv_sub_f32 v114, v106, v110\nv_add_f32 v106, v106, v110\n" \
    "v_mfma_f32_32x32x16_bf16 v[48:63], a[116:119], v[84:87], v[48:63]\nds_read_b128 v[68:71], v1 offset:33792\ns_waitcnt lgkmcnt(3)\nv_add_f32 v107, v107, v111\n" \
    "v_mul_f32 v114, v114, v114\nv_mul_f32 v106, 0.5, v106\nv_mfma_f32_32x32x16_bf16 v[48:63], a[120:123], v[88:91], v[48:63]\nds_read_b128 v[72:75], v1 offset:34816\n" \
    "s_waitcnt lgkmcnt(3)\nv_fmac_f32 v107, 0x41000000, v114\nv_sub_f32 v114, v98, v106\nv_add_f32 v98, v98, v106\n" \
    "v_mfma_f32_32x32x16_bf16 v[48:63], a[124:127], v[92:95], v[48:63]\nds_read_b128 v[76:79], v1 offset:35840\ns_waitcnt lgkmcnt(3)\nv_add_f32 v99, v99, v107\n" \
    "v_mul_f32 v114, v114, v114\nv_mul_f32 v98, 0.5, v98\nv_mfma_f32_32x32x16_bf16 v[48:63], a[128:131], v[64:67], v[48:63]\nds_read_b128 v[80:83], v1 offset:36864\n" \
    "s_waitcnt lgkmcnt(3)\nv_fmac_f32 v99, 0x41800000, v114\nv_mov_b64 v[100:101], v[96:97]\nv_mov_b64 v[102:103], v[98:99]\n" \
    "v_mfma_f32_32x32x16_bf16 v[48:63], a[132:135], v[68:71], v[48:63]\nds_read_b128 v[84:87], v1 offset:37888\ns_waitcnt lgkmcnt(3)\nv_permlane16_swap_b32 v96, v100\n" \
    "v_permlane16_swap_b32 v97, v101\nv_mfma_f32_32x32x16_bf16 v[48:63], a[136:139], v[72:75], v[48:63]\nds_read_b128 v[88:91], v1 offset:38912\ns_waitcnt lgkmcnt(3)\n" \
    "v_permlane16_swap_b32 v98, v102\nv_permlane16_swap_b32 v99, v103\nv_mfma_f32_32x32x16_bf16 v[48:63], a[140:143], v[76:79], v[48:63]\n" \
    "ds_read_b128 v[92:95], v1 offset:39936\ns_waitcnt lgkmcnt(3)\nv_sub_f32 v114, v96, v100\nv_add_f32 v96, v96, v100\nv_add_f32 v97, v97, v101\n" \
    "v_mfma_f32_32x32x16_bf16 v[48:63], a[144:147], v[80:83], v[48:63]\nds_read_b128 v[64:67], v1 offset:40960\ns_waitcnt lgkmcnt(3)\nv_mul_f32 v114, v114, v114\n" \
    "v_mul_f32 v96, 0.5, v96\nv_fmac_f32 v97, 0x42000000, v114\nv_mfma_f32_32x32x16_bf16 v[48:63], a[148:151], v[84:87], v[48:63]\nds_read_b128 v[68:71], v1 offset:41984\n" \
    "s_waitcnt lgkmcnt(3)\nv_sub_f32 v114, v98, v102\nv_add_f32 v98, v98, v102\nv_add_f32 v99, v99, v103\nv_mfma_f32_32x32x16_bf16 v[48:63], a[152:155], v[88:91], v[48:63]\n" \
    "ds_read_b128 v[72:75], v1 offset:43008\ns_waitcnt lgkmcnt(3)\nv_mul_f32 v114, v114, v114\nv_mul_f32 v98, 0.5, v98\nv_fmac_f32 v99, 0x42000000, v114\n" \
    "v_mfma_f32_32x32x16_bf16 v[48:63], a[156:159], v[92:95], v[48:63]\nds_read_b128 v[76:79], v1 offset:44032\ns_waitcnt lgkmcnt(3)\nv_mov_b64 v[100:101], v[96:97]\n" \
    "v_mov_b64 v[102:103], v[98:99]\nv_mfma_f32_32x32x16_bf16 v[48:63], a[160:163], v[64:67], v[48:63]\nds_read_b128 v[80:83], v1 offset:45056\ns_waitcnt lgkmcnt(3)\n" \
    "v_permlane32_swap_b32 v96, v100\nv_permlane32_swap_b32 v97, v101\nv_mfma_f32_32x32x16_bf16 v[48:63], a[164:167], v[68:71], v[48:63]\n" \
    "ds_read_b128 v[84:87], v1 offset:46080\ns_waitcnt lgkmcnt(3)\nv_permlane32_swap_b32 v98, v102\nv_permlane32_swap_b32 v99, v103\n" \
    "v_mfma_f32_32x32x16_bf16 v[48:63], a[168:171], v[72:75], v[48:63]\nds_read_b128 v[88:91], v1 offset:47104\ns_waitcnt lgkmcnt(3)\nv_sub_f32 v114, v96, v100\n" \
    "v_add_f32 v96, v96, v100\nv_add_f32 v97, v97, v101\nv_mfma_f32_32x32x16_bf16 v[48:63], a[172:175], v[76:79], v[48:63]\nds_read_b128 v[92:95], v1 offset:48128\n" \
    "s_waitcnt vmcnt(0) lgkmcnt(3)\ns_barrier\nv_mul_f32 v114, v114, v114\nv_mul_f32 v96, 0.5, v96\nv_fmac_f32 v97, 0x42800000, v114\n" \
    "v_mfma_f32_32x32x16_bf16 v[48:63], a[176:179], v[80:83], v[48:63]\nds_read_b128 v[64:67], v1 offset:49152\ns_waitcnt lgkmcnt(3)\nv_sub_f32 v114, v98, v102\n" \
    "v_add_f32 v98, v98, v102\nv_add_f32 v99, v99, v103\nv_mfma_f32_32x32x16_bf16 v[48:63], a[180:183], v[84:87], v[48:63]\nds_read_b128 v[68:71], v1 offset:50176\n" \
    "s_waitcnt lgkmcnt(3)\nv_mul_f32 v114, v114, v114\nv_mul_f32 v98, 0.5, v98\nv_fmac_f32 v99, 0x42800000, v114\n" \
    "v_mfma_f32_32x32x16_bf16 v[48:63], a[184:187], v[88:91], v[48:63]\nds_read_b128 v[72:75], v1 offset:51200\ns_cmp_gt_u32 s76, s77\ns_cbranch_scc1 .Lf16_%=\n" \
    "buffer_load_dword v150, off, s[64:67], s85 sc1\n.Lf16_%=:\ns_cmp_lg_u64 s[70:71], 0\ns_cselect_b32 s78, -1, 0\ns_waitcnt lgkmcnt(3)\nv_min_u32 v96, -2, v96\n" \
    "v_min_u32 v98, -2, v98\nv_or_b32 v96, s78, v96\nv_mfma_f32_32x32x16_bf16 v[48:63], a[188:191], v[92:95], v[48:63]\nds_read_b128 v[76:79], v1 offset:52224\n" \
    "s_waitcnt lgkmcnt(3)\nv_or_b32 v98, s78, v98\nv_mfma_f32_32x32x16_bf16 v[48:63], a[192:195], v[64:67], v[48:63]\nds_read_b128 v[80:83], v1 offset:53248\n" \
    "ds_write_b128 v3, v[96:99]\ns_waitcnt lgkmcnt(4)\nv_mfma_f32_32x32x16_bf16 v[48:63], a[196:199], v[68:71], v[48:63]\nds_read_b128 v[84:87], v1 offset:54272\n" \
    "s_waitcnt lgkmcnt(4)\nv_mfma_f32_32x32x16_bf16 v[48:63], a[200:203], v[72:75], v[48:63]\nds_read_b128 v[88:91], v1 offset:55296\ns_waitcnt lgkmcnt(2)\ns_barrier\n" \
    "ds_read_b64 v[96:97], v4 offset:0\nds_read_b64 v[98:99], v4 offset:256\nds_read_b64 v[100:101], v4 offset:512\n" \
    "v_mfma_f32_32x32x16_bf16 v[48:63], a[204:207], v[76:79], v[48:63]\nds_read_b128 v[92:95], v1 offset:56320\nds_read_b64 v[102:103], v4 offset:768\n" \
    "v_mfma_f32_32x32x16_bf16 v[48:63], a[208:211], v[80:83], v[48:63]\nds_read_b128 v[64:67], v1 offset:57344\ns_waitcnt lgkmcnt(7)\n" \
    "v_mfma_f32_32x32x16_bf16 v[48:63], a[212:215], v[84:87], v[48:63]\nds_read_b128 v[68:71], v1 offset:58368\ns_waitcnt lgkmcnt(2)\nv_max3_u32 v116, v96, v98, v100\n" \
    "v_max_u32 v116, v116, v102\nv_cmp_eq_u32 s[70:71], -1, v116\nv_mfma_f32_32x32x16_bf16 v[48:63], a[216:219], v[88:91], v[48:63]\nds_read_b128 v[72:75], v1 offset:59392\n" \
    "v_sub_f32 v114, v96, v98\nv_add_f32 v96, v96, v98\nv_add_f32 v97, v97, v99\nv_mfma_f32_32x32x16_bf16 v[48:63], a[220:223], v[92:95], v[48:63]\n" \
    "ds_read_b128 v[76:79], v1 offset:60416\ns_waitcnt lgkmcnt(3)\nv_mul_f32 v114, v114, v114\nv_mul_f32 v96, 0.5, v96\nv_fmac_f32 v97, 0x43000000, v114\n" \
    "v_mfma_f32_32x32x16_bf16 v[48:63], a[224:227], v[64:67], v[48:63]\nds_read_b128 v[80:83], v1 offset:61440\ns_waitcnt lgkmcnt(3)\nv_sub_f32 v114, v100, v102\n" \
    "v_add_f32 v100, v100, v102\nv_add_f32 v101, v101, v103\nv_mfma_f32_32x32x16_bf16 v[48:63], a[228:231], v[68:71], v[48:63]\nds_read_b128 v[84:87], v1 offset:62464\n" \
    "s_waitcnt lgkmcnt(3)\nv_mul_f32 v114, v114, v114\nv_mul_f32 v100, 0.5, v100\nv_fmac_f32 v101, 0x43000000, v114\n" \
    "v_mfma_f32_32x32x16_bf16 v[48:63], a[232:235], v[72:75], v[48:63]\nds_read_b128 v[88:91], v1 offset:63488\ns_waitcnt vmcnt(0)\ns_waitcnt lgkmcnt(3)\n" \
    "v_and_b32 v112, 127, v150\nv_cmp_gt_u32 vcc, s77, v112\nv_sub_f32 v114, v96, v100\nv_mfma_f32_32x32x16_bf16 v[48:63], a[236:239], v[76:79], v[48:63]\n" \
    "ds_read_b128 v[92:95], v1 offset:64512\ns_cmp_eq_u64 vcc, 0\ns_cselect_b32 s81, 1, 0\ns_waitcnt lgkmcnt(3)\nv_add_f32 v96, v96, v100\nv_add_f32 v97, v97, v101\n" \
    "v_mul_f32 v114, v114, v114\nv_mfma_f32_32x32x16_bf16 v[48:63], a[240:243], v[80:83], v[48:63]\ns_waitcnt lgkmcnt(2)\nv_mul_f32 v96, 0.5, v96\n" \
    "v_fmac_f32 v97, 0x43800000, v114\nv_mul_f32 v98, 0x3a800000, v97\nv_mfma_f32_32x32x16_bf16 v[48:63], a[244:247], v[84:87], v[48:63]\ns_waitcnt lgkmcnt(0)\n" \
    "s_memrealtime s[72:73]\nv_cvt_pk_bf16_f32 v96, 0, v96\nv_cvt_pk_bf16_f32 v98, 0, v98\nv_add_f32 v98, 0x3727c5ac, v98\n" \
    "v_mfma_f32_32x32x16_bf16 v[48:63], a[248:251], v[88:91], v[48:63]\nv_cvt_pk_bf16_f32 v98, 0, v98\nv_sqrt_f32 v98, v98\n" \
    "v_mfma_f32_32x32x16_bf16 v[48:63], a[252:255], v[92:95], v[48:63]\ns_waitcnt lgkmcnt(0)\ns_cmp_ge_u32 s77, 62\ns_cselect_b32 s69, s61, s34\ns_cmp_ge_u32 s77, 64\n" \
    "s_cselect_b32 s69, s60, s69\ns_sub_u32 s72, s72, s69\ns_lshl_b32 s72, s72, 8\nv_sub_u32 v113, s72, v150\nv_cmp_gt_i32 s[74:75], 0, v113\nv_cmp_eq_u32 vcc, s77, v112\n" \
    "s_nop 0\ns_and_b64 vcc, vcc, s[74:75]\ns_cselect_b32 s69, 1, 0\ns_lshl_b32 s81, s81, s69\nv_cvt_pk_bf16_f32 v98, 0, v98\nv_rcp_f32 v98, v98\ns_cmp_lg_u64 s[70:71], 0\n" \
    "s_cbranch_scc1 .Lf9_%=\nv_cmp_u_f32 vcc, v48, v48\ns_nop 0\ns_cbranch_vccnz .Lf7_%=\n.Lf8_%=:\ns_cmp_eq_u32 s81, 1\ns_cbranch_scc1 .Lf17_%=\n" \
    "v_pk_fma_f32 v[48:49], v[16:17], v[96:97], v[48:49] op_sel_hi:[1,0,1] neg_lo:[0,1,0] neg_hi:[0,1,0]\n" \
    "v_pk_fma_f32 v[50:51], v[18:19], v[96:97], v[50:51] op_sel_hi:[1,0,1] neg_lo:[0,1,0] neg_hi:[0,1,0]\n" \
    "v_pk_fma_f32 v[52:53], v[20:21], v[96:97], v[52:53] op_sel_hi:[1,0,1] neg_lo:[0,1,0] neg_hi:[0,1,0]\n" \
    "v_pk_fma_f32 v[54:55], v[22:23], v[96:97], v[54:55] op_sel_hi:[1,0,1] neg_lo:[0,1,0] neg_hi:[0,1,0]\nv_pk_fma_f32 v[48:49], v[48:49], v[98:99], v[32:33] op_sel_hi:[1,0,1]\n" \
    "v_pk_fma_f32 v[50:51], v[50:51], v[98:99], v[34:35] op_sel_hi:[1,0,1]\nv_pk_fma_f32 v[52:53], v[52:53], v[98:99], v[36:37] op_sel_hi:[1,0,1]\n" \
    "v_pk_fma_f32 v[54:55], v[54:55], v[98:99], v[38:39] op_sel_hi:[1,0,1]\nv_pk_mul_f32 v[72:73], v[48:49], v[48:49]\nv_pk_mul_f32 v[74:75], v[50:51], v[50:51]\n" \
    "v_pk_mul_f32 v[76:77], v[52:53], v[52:53]\nv_pk_mul_f32 v[78:79], v[54:55], v[54:55]\nv_pk_fma_f32 v[72:73], v[72:73], v[6:7], v[8:9]\n" \
    "v_pk_fma_f32 v[74:75], v[74:75], v[6:7], v[8:9]\nv_pk_fma_f32 v[76:77], v[76:77], v[6:7], v[8:9]\nv_pk_fma_f32 v[78:79], v[78:79], v[6:7], v[8:9]\n" \
    "v_pk_mul_f32 v[72:73], v[72:73], v[48:49]\nv_pk_mul_f32 v[74:75], v[74:75], v[50:51]\nv_pk_mul_f32 v[76:77], v[76:77], v[52:53]\nv_pk_mul_f32 v[78:79], v[78:79], v[54:55]\n" \
    "v_exp_f32 v72, v72\nv_exp_f32 v73, v73\nv_exp_f32 v74, v74\nv_exp_f32 v75, v75\nv_exp_f32 v76, v76\nv_exp_f32 v77, v77\nv_exp_f32 v78, v78\nv_exp_f32 v79, v79\n" \
    "v_pk_add_f32 v[72:73], v[72:73], v[10:11]\nv_pk_add_f32 v[74:75], v[74:75], v[10:11]\nv_pk_add_f32 v[76:77], v[76:77], v[10:11]\nv_pk_add_f32 v[78:79], v[78:79], v[10:11]\n" \
    "v_rcp_f32 v72, v72\nv_rcp_f32 v73, v73\nv_rcp_f32 v74, v74\nv_rcp_f32 v75, v75\nv_rcp_f32 v76, v76\nv_rcp_f32 v77, v77\nv_rcp_f32 v78, v78\nv_rcp_f32 v79, v79\n" \
    "v_pk_mul_f32 v[72:73], v[72:73], v[48:49]\nv_pk_mul_f32 v[74:75], v[74:75], v[50:51]\nv_pk_mul_f32 v[76:77], v[76:77], v[52:53]\nv_pk_mul_f32 v[78:79], v[78:79], v[54:55]\n" \
    "v_cvt_pk_bf16_f32 v64, v72, v73\nv_cvt_pk_bf16_f32 v65, v74, v75\nv_cvt_pk_bf16_f32 v66, v76, v77\nv_cvt_pk_bf16_f32 v67, v78, v79\nv_min_u32 v64, -2, v64\n" \
    "v_min_u32 v65, -2, v65\nv_min_u32 v66, -2, v66\nv_min_u32 v67, -2, v67\nbuffer_store_dwordx4 v[64:67], v0, s[64:67], s82 offen offset:0 sc1\n" \
    "v_pk_fma_f32 v[56:57], v[24:25], v[96:97], v[56:57] op_sel_hi:[1,0,1] neg_lo:[0,1,0] neg_hi:[0,1,0]\n" \
    "v_pk_fma_f32 v[58:59], v[26:27], v[96:97], v[58:59] op_sel_hi:[1,0,1] neg_lo:[0,1,0] neg_hi:[0,1,0]\n" \
    "v_pk_fma_f32 v[60:61], v[28:29], v[96:97], v[60:61] op_sel_hi:[1,0,1] neg_lo:[0,1,0] neg_hi:[0,1,0]\n" \
    "v_pk_fma_f32 v[62:63], v[30:31], v[96:97], v[62:63] op_sel_hi:[1,0,1] neg_lo:[0,1,0] neg_hi:[0,1,0]\nv_pk_fma_f32 v[56:57], v[56:57], v[98:99], v[40:41] op_sel_hi:[1,0,1]\n" \
    "v_pk_fma_f32 v[58:59], v[58:59], v[98:99], v[42:43] op_sel_hi:[1,0,1]\nv_pk_fma_f32 v[60:61], v[60:61], v[98:99], v[44:45] op_sel_hi:[1,0,1]\n" \
    "v_pk_fma_f32 v[62:63], v[62:63], v[98:99], v[46:47] op_sel_hi:[1,0,1]\nv_pk_mul_f32 v[72:73], v[56:57], v[56:57]\nv_pk_mul_f32 v[74:75], v[58:59], v[58:59]\n" \
    "v_pk_mul_f32 v[76:77], v[60:61], v[60:61]\nv_pk_mul_f32 v[78:79], v[62:63], v[62:63]\nv_pk_fma_f32 v[72:73], v[72:73], v[6:7], v[8:9]\n" \
    "v_pk_fma_f32 v[74:75], v[74:75], v[6:7], v[8:9]\nv_pk_fma_f32 v[76:77], v[76:77], v[6:7], v[8:9]\nv_pk_fma_f32 v[78:79], v[78:79], v[6:7], v[8:9]\n" \
    "v_pk_mul_f32 v[72:73], v[72:73], v[56:57]\nv_pk_mul_f32 v[74:75], v[74:75], v[58:59]\nv_pk_mul_f32 v[76:77], v[76:77], v[60:61]\nv_pk_mul_f32 v[78:79], v[78:79], v[62:63]\n" \
    "v_exp_f32 v72, v72\nv_exp_f32 v73, v73\nv_exp_f32 v74, v74\nv_exp_f32 v75, v75\nv_exp_f32 v76, v76\nv_exp_f32 v77, v77\nv_exp_f32 v78, v78\nv_exp_f32 v79, v79\n" \
    "v_pk_add_f32 v[72:73], v[72:73], v[10:11]\nv_pk_add_f32 v[74:75], v[74:75], v[10:11]\nv_pk_add_f32 v[76:77], v[76:77], v[10:11]\nv_pk_add_f32 v[78:79], v[78:79], v[10:11]\n" \
    "v_rcp_f32 v72, v72\nv_rcp_f32 v73, v73\nv_rcp_f32 v74, v74\nv_rcp_f32 v75, v75\nv_rcp_f32 v76, v76\nv_rcp_f32 v77, v77\nv_rcp_f32 v78, v78\nv_rcp_f32 v79, v79\n" \
    "v_pk_mul_f32 v[72:73], v[72:73], v[56:57]\nv_pk_mul_f32 v[74:75], v[74:75], v[58:59]\nv_pk_mul_f32 v[76:77], v[76:77], v[60:61]\nv_pk_mul_f32 v[78:79], v[78:79], v[62:63]\n" \
    "v_cvt_pk_bf16_f32 v68, v72, v73\nv_cvt_pk_bf16_f32 v69, v74, v75\nv_cvt_pk_bf16_f32 v70, v76, v77\nv_cvt_pk_bf16_f32 v71, v78, v79\nv_min_u32 v68, -2, v68\n" \
    "v_min_u32 v69, -2, v69\nv_min_u32 v70, -2, v70\nv_min_u32 v71, -2, v71\nbuffer_store_dwordx4 v[68:71], v0, s[64:67], s82 offen offset:1024 sc1\n.Lf18_%=:\n" \
    "s_mov_b32 s38, s85\ns_xor_b32 s39, s39, 128\ns_xor_b32 s80, s80, 0x10000\nv_xor_b32 v1, 0x10000, v1\ns_mov_b32 s68, 0\ns_add_u32 s32, s32, 1\ns_cmp_lt_u32 s32, 64\n" \
    "s_cbranch_scc1 .Lf2_%=\ns_endpgm\n.Lf4_%=:\ns_sleep 8\ns_add_u32 s68, s68, 1\ns_and_b32 s69, s68, 63\ns_cbranch_scc1 .Lf3_%=\ns_cmp_gt_u32 s68, 0x400000\n" \
    "s_cbranch_scc1 .Lf1_%=\nbuffer_load_dword v253, off, s[64:67], 0 sc1\ns_waitcnt vmcnt(0)\nv_readfirstlane_b32 s69, v253\ns_nop 0\ns_cmp_eq_u32 s69, 1\n" \
    "s_cbranch_scc0 .Lf3_%=\ns_endpgm\n.Lf13_%=:\ns_cmp_ge_u32 s32, 61\ns_cbranch_scc1 .Lf14_%=\ns_sleep 10\n.Lf14_%=:\ns_sleep 3\n" \
    "buffer_load_dword v151, v2, s[64:67], s82 offen sc1\ns_waitcnt vmcnt(0)\nv_cmp_eq_u32 vcc, -1, v151\ns_nop 0\ns_cbranch_vccz .Lf12_%=\ns_add_u32 s68, s68, 1\n" \
    "s_and_b32 s69, s68, 63\ns_cbranch_scc1 .Lf13_%=\ns_cmp_gt_u32 s68, 0x400000\ns_cbranch_scc1 .Lf1_%=\nbuffer_load_dword v253, off, s[64:67], 0 sc1\ns_waitcnt vmcnt(0)\n" \
    "v_readfirstlane_b32 s69, v253\ns_nop 0\ns_cmp_eq_u32 s69, 1\ns_cbranch_scc0 .Lf13_%=\ns_endpgm\n.Lf17_%=:\ns_xor_b32 s84, s84, 0x10000\n" \
    "buffer_load_dword v151, v2, s[64:67], s83 offen sc1\nv_pk_fma_f32 v[48:49], v[16:17], v[96:97], v[48:49] op_sel_hi:[1,0,1] neg_lo:[0,1,0] neg_hi:[0,1,0]\n" \
    "v_pk_fma_f32 v[50:51], v[18:19], v[96:97], v[50:51] op_sel_hi:[1,0,1] neg_lo:[0,1,0] neg_hi:[0,1,0]\n" \
    "v_pk_fma_f32 v[52:53], v[20:21], v[96:97], v[52:53] op_sel_hi:[1,0,1] neg_lo:[0,1,0] neg_hi:[0,1,0]\n" \
    "v_pk_fma_f32 v[54:55], v[22:23], v[96:97], v[54:55] op_sel_hi:[1,0,1] neg_lo:[0,1,0] neg_hi:[0,1,0]\nv_pk_fma_f32 v[48:49], v[48:49], v[98:99], v[32:33] op_sel_hi:[1,0,1]\n" \
    "v_pk_fma_f32 v[50:51], v[50:51], v[98:99], v[34:35] op_sel_hi:[1,0,1]\nv_pk_fma_f32 v[52:53], v[52:53], v[98:99], v[36:37] op_sel_hi:[1,0,1]\n" \
    "v_pk_fma_f32 v[54:55], v[54:55], v[98:99], v[38:39] op_sel_hi:[1,0,1]\nv_pk_mul_f32 v[72:73], v[48:49], v[48:49]\nv_pk_mul_f32 v[74:75], v[50:51], v[50:51]\n" \
    "v_pk_mul_f32 v[76:77], v[52:53], v[52:53]\nv_pk_mul_f32 v[78:79], v[54:55], v[54:55]\ns_add_u32 m0, s84, 0\ns_add_u32 s69, s83, s48\n" \
    "buffer_load_dwordx4 v0, s[64:67], s69 offen offset:0 sc1 lds\nv_pk_fma_f32 v[72:73], v[72:73], v[6:7], v[8:9]\nv_pk_fma_f32 v[74:75], v[74:75], v[6:7], v[8:9]\n" \
    "v_pk_fma_f32 v[76:77], v[76:77], v[6:7], v[8:9]\nv_pk_fma_f32 v[78:79], v[78:79], v[6:7], v[8:9]\nv_pk_mul_f32 v[72:73], v[72:73], v[48:49]\n" \
    "v_pk_mul_f32 v[74:75], v[74:75], v[50:51]\nv_pk_mul_f32 v[76:77], v[76:77], v[52:53]\nv_pk_mul_f32 v[78:79], v[78:79], v[54:55]\nv_exp_f32 v72, v72\nv_exp_f32 v73, v73\n" \
    "v_exp_f32 v74, v74\nv_exp_f32 v75, v75\nbuffer_load_dwordx4 v0, s[64:67], s69 offen offset:1024 sc1 lds\nv_exp_f32 v76, v76\nv_exp_f32 v77, v77\nv_exp_f32 v78, v78\n" \
    "v_exp_f32 v79, v79\nv_pk_add_f32 v[72:73], v[72:73], v[10:11]\nv_pk_add_f32 v[74:75], v[74:75], v[10:11]\nv_pk_add_f32 v[76:77], v[76:77], v[10:11]\n" \
    "v_pk_add_f32 v[78:79], v[78:79], v[10:11]\nv_rcp_f32 v72, v72\nv_rcp_f32 v73, v73\nv_rcp_f32 v74, v74\nv_rcp_f32 v75, v75\ns_add_u32 m0, s84, 8192\n" \
    "s_add_u32 s69, s83, s49\nbuffer_load_dwordx4 v0, s[64:67], s69 offen offset:0 sc1 lds\nv_rcp_f32 v76, v76\nv_rcp_f32 v77, v77\nv_rcp_f32 v78, v78\nv_rcp_f32 v79, v79\n" \
    "v_pk_mul_f32 v[72:73], v[72:73], v[48:49]\nv_pk_mul_f32 v[74:75], v[74:75], v[50:51]\nv_pk_mul_f32 v[76:77], v[76:77], v[52:53]\nv_pk_mul_f32 v[78:79], v[78:79], v[54:55]\n" \
    "v_cvt_pk_bf16_f32 v64, v72, v73\nv_cvt_pk_bf16_f32 v65, v74, v75\nv_cvt_pk_bf16_f32 v66, v76, v77\nv_cvt_pk_bf16_f32 v67, v78, v79\n" \
    "buffer_load_dwordx4 v0, s[64:67], s69 offen offset:1024 sc1 lds\nv_min_u32 v64, -2, v64\nv_min_u32 v65, -2, v65\nv_min_u32 v66, -2, v66\nv_min_u32 v67, -2, v67\n" \
    "buffer_store_dwordx4 v[64:67], v0, s[64:67], s82 offen offset:0 sc1\nv_pk_fma_f32 v[56:57], v[24:25], v[96:97], v[56:57] op_sel_hi:[1,0,1] neg_lo:[0,1,0] neg_hi:[0,1,0]\n" \
    "v_pk_fma_f32 v[58:59], v[26:27], v[96:97], v[58:59] op_sel_hi:[1,0,1] neg_lo:[0,1,0] neg_hi:[0,1,0]\n" \
    "v_pk_fma_f32 v[60:61], v[28:29], v[96:97], v[60:61] op_sel_hi:[1,0,1] neg_lo:[0,1,0] neg_hi:[0,1,0]\n" \
    "v_pk_fma_f32 v[62:63], v[30:31], v[96:97], v[62:63] op_sel_hi:[1,0,1] neg_lo:[0,1,0] neg_hi:[0,1,0]\nv_pk_fma_f32 v[56:57], v[56:57], v[98:99], v[40:41] op_sel_hi:[1,0,1]\n" \
    "v_pk_fma_f32 v[58:59], v[58:59], v[98:99], v[42:43] op_sel_hi:[1,0,1]\nv_pk_fma_f32 v[60:61], v[60:61], v[98:99], v[44:45] op_sel_hi:[1,0,1]\ns_add_u32 m0, s84, 16384\n" \
    "s_add_u32 s69, s83, s50\nbuffer_load_dwordx4 v0, s[64:67], s69 offen offset:0 sc1 lds\nv_pk_fma_f32 v[62:63], v[62:63], v[98:99], v[46:47] op_sel_hi:[1,0,1]\n" \
    "v_pk_mul_f32 v[72:73], v[56:57], v[56:57]\nv_pk_mul_f32 v[74:75], v[58:59], v[58:59]\nv_pk_mul_f32 v[76:77], v[60:61], v[60:61]\nv_pk_mul_f32 v[78:79], v[62:63], v[62:63]\n" \
    "v_pk_fma_f32 v[72:73], v[72:73], v[6:7], v[8:9]\nv_pk_fma_f32 v[74:75], v[74:75], v[6:7], v[8:9]\nv_pk_fma_f32 v[76:77], v[76:77], v[6:7], v[8:9]\n" \
    "v_pk_fma_f32 v[78:79], v[78:79], v[6:7], v[8:9]\nv_pk_mul_f32 v[72:73], v[72:73], v[56:57]\nv_pk_mul_f32 v[74:75], v[74:75], v[58:59]\n" \
    "v_pk_mul_f32 v[76:77], v[76:77], v[60:61]\nbuffer_load_dwordx4 v0, s[64:67], s69 offen offset:1024 sc1 lds\nv_pk_mul_f32 v[78:79], v[78:79], v[62:63]\nv_exp_f32 v72, v72\n" \
    "v_exp_f32 v73, v73\nv_exp_f32 v74, v74\nv_exp_f32 v75, v75\nv_exp_f32 v76, v76\nv_exp_f32 v77, v77\nv_exp_f32 v78, v78\nv_exp_f32 v79, v79\n" \
    "v_pk_add_f32 v[72:73], v[72:73], v[10:11]\nv_pk_add_f32 v[74:75], v[74:75], v[10:11]\nv_pk_add_f32 v[76:77], v[76:77], v[10:11]\nv_pk_add_f32 v[78:79], v[78:79], v[10:11]\n" \
    "v_rcp_f32 v72, v72\nv_rcp_f32 v73, v73\nv_rcp_f32 v74, v74\nv_rcp_f32 v75, v75\nv_rcp_f32 v76, v76\nv_rcp_f32 v77, v77\nv_rcp_f32 v78, v78\nv_rcp_f32 v79, v79\n" \
    "v_pk_mul_f32 v[72:73], v[72:73], v[56:57]\nv_pk_mul_f32 v[74:75], v[74:75], v[58:59]\nv_pk_mul_f32 v[76:77], v[76:77], v[60:61]\nv_pk_mul_f32 v[78:79], v[78:79], v[62:63]\n" \
    "v_cvt_pk_bf16_f32 v68, v72, v73\nv_cvt_pk_bf16_f32 v69, v74, v75\nv_cvt_pk_bf16_f32 v70, v76, v77\nv_cvt_pk_bf16_f32 v71, v78, v79\nv_min_u32 v68, -2, v68\n" \
    "v_min_u32 v69, -2, v69\nv_min_u32 v70, -2, v70\nv_min_u32 v71, -2, v71\nbuffer_store_dwordx4 v[68:71], v0, s[64:67], s82 offen offset:1024 sc1\ns_branch .Lf18_%=\n" \
    ".Lf7_%=:\nv_mov_b32 v120, 0\nv_mov_b32 v121, v1\ns_mov_b32 s76, 16\n.Lf10_%=:\nds_read_b128 v[124:127], v121 offset:0\nds_read_b128 v[128:131], v121 offset:1024\n" \
    "ds_read_b128 v[132:135], v121 offset:2048\nds_read_b128 v[136:139], v121 offset:3072\nv_add_u32 v121, 0x1000, v121\ns_waitcnt lgkmcnt(0)\n" \
    "v_max3_u32 v120, v120, v124, v125\nv_max3_u32 v120, v120, v126, v127\nv_max3_u32 v120, v120, v128, v129\nv_max3_u32 v120, v120, v130, v131\n" \
    "v_max3_u32 v120, v120, v132, v133\nv_max3_u32 v120, v120, v134, v135\nv_max3_u32 v120, v120, v136, v137\nv_max3_u32 v120, v120, v138, v139\ns_sub_u32 s76, s76, 1\n" \
    "s_cmp_lg_u32 s76, 0\ns_cbranch_scc1 .Lf10_%=\nv_cmp_eq_u32 vcc, -1, v120\ns_nop 0\ns_cbranch_vccz .Lf8_%=\n.Lf9_%=:\ns_mov_b32 s81, 0\ns_barrier\ns_sleep 4\n" \
    "s_add_u32 s68, s68, 1\ns_and_b32 s69, s68, 63\ns_cbranch_scc1 .Lf2_%=\ns_cmp_gt_u32 s68, 0x400000\ns_cbranch_scc1 .Lf1_%=\nbuffer_load_dword v253, off, s[64:67], 0 sc1\n" \
    "s_waitcnt vmcnt(0)\nv_readfirstlane_b32 s69, v253\ns_nop 0\ns_cmp_eq_u32 s69, 1\ns_cbranch_scc0 .Lf2_%=\ns_endpgm\n.Lf1_%=:\nv_mov_b32 v253, 1\n" \
    "buffer_store_dword v253, off, s[64:67], 0 sc1\ns_endpgm\n"
#define ASM_F2 \
    "s_mov_b32 s90, 0x7fff0000\ns_mov_b32 s91, 0x7fff0000\ns_mov_b32 s92, 0x7fff0000\ns_mov_b32 s93, 0x7fff0000\ns_mov_b32 s80, 0\ns_mov_b32 s81, 0\ns_mov_b32 s68, 0\n.Lg2_%=:\n" \
    "s_xor_b32 s85, s38, 128\ns_and_b32 s69, s32, 63\ns_lshl_b32 s69, s69, 19\ns_add_u32 s82, s36, s69\ns_lshr_b32 s69, s82, 8\ns_and_b32 s69, s69, 0x2000\n" \
    "s_xor_b32 s82, s82, s69\ns_add_u32 s69, s32, 1\ns_and_b32 s69, s69, 63\ns_lshl_b32 s69, s69, 19\ns_add_u32 s83, s36, s69\ns_lshr_b32 s69, s83, 8\n" \
    "s_and_b32 s69, s69, 0x2000\ns_xor_b32 s83, s83, s69\ns_add_u32 s84, s37, s80\ns_cmp_eq_u32 s81, 1\ns_cbranch_scc1 .Lg6_%=\ns_add_u32 s77, s32, 1\ns_cmp_eq_u32 s81, 2\n" \
    "s_cbranch_scc1 .Lg5_%=\n.Lg3_%=:\nbuffer_load_dword v150, v3, s[64:67], s38 offen sc1\ns_waitcnt vmcnt(0)\nv_and_b32 v112, 127, v150\nv_cmp_gt_u32 vcc, s77, v112\ns_nop 0\n" \
    "s_cbranch_vccnz .Lg4_%=\n.Lg5_%=:\nv_and_b32 v112, 127, v150\n.Lg11_%=:\ns_memrealtime s[72:73]\ns_waitcnt lgkmcnt(0)\ns_cmp_ge_u32 s77, 62\ns_cselect_b32 s69, s61, s34\n" \
    "s_cmp_ge_u32 s77, 64\ns_cselect_b32 s69, s60, s69\ns_sub_u32 s72, s72, s69\ns_lshl_b32 s72, s72, 8\nv_sub_u32 v113, s72, v150\nv_cmp_gt_i32 s[74:75], 0, v113\n" \
    "v_cmp_eq_u32 vcc, s77, v112\ns_nop 0\ns_and_b64 vcc, vcc, s[74:75]\ns_cbranch_scc1 .Lg11_%=\n.Lg12_%=:\nbuffer_load_dword v151, v2, s[64:67], s82 offen sc1\n" \
    "s_add_u32 m0, s84, 0\ns_add_u32 s69, s82, s48\nbuffer_load_dwordx4 v0, s[64:67], s69 offen offset:0 sc1 lds\n" \
    "buffer_load_dwordx4 v0, s[64:67], s69 offen offset:1024 sc1 lds\ns_add_u32 m0, s84, 8192\ns_add_u32 s69, s82, s49\n" \
    "buffer_load_dwordx4 v0, s[64:67], s69 offen offset:0 sc1 lds\nbuffer_load_dwordx4 v0, s[64:67], s69 offen offset:1024 sc1 lds\ns_add_u32 m0, s84, 16384\n" \
    "s_add_u32 s69, s82, s50\nbuffer_load_dwordx4 v0, s[64:67], s69 offen offset:0 sc1 lds\nbuffer_load_dwordx4 v0, s[64:67], s69 offen offset:1024 sc1 lds\n.Lg6_%=:\n" \
    "s_waitcnt vmcnt(6)\nv_cmp_eq_u32 vcc, -1, v151\ns_nop 0\ns_cbranch_vccnz .Lg13_%=\ns_add_u32 s77, s32, 2\ns_add_u32 m0, s84, 24576\ns_add_u32 s69, s82, s51\n" \
    "buffer_load_dwordx4 v0, s[64:67], s69 offen offset:0 sc1 lds\nbuffer_load_dwordx4 v0, s[64:67], s69 offen offset:1024 sc1 lds\ns_waitcnt vmcnt(6)\ns_barrier\n" \
    "ds_read_b128 v[64:67], v1 offset:0\nds_read_b128 v[68:71], v1 offset:1024\nds_read_b128 v[72:75], v1 offset:2048\nds_read_b128 v[76:79], v1 offset:3072\n" \
    "s_waitcnt lgkmcnt(3)\nv_mfma_f32_32x32x16_bf16 v[48:63], a[0:3], v[64:67], 0\nds_read_b128 v[80:83], v1 offset:4096\ns_add_u32 m0, s84, 32768\ns_add_u32 s69, s82, s52\n" \
    "buffer_load_dwordx4 v0, s[64:67], s69 offen offset:0 sc1 lds\ns_waitcnt lgkmcnt(3)\nv_mfma_f32_32x32x16_bf16 v[48:63], a[4:7], v[68:71], v[48:63]\n" \
    "ds_read_b128 v[84:87], v1 offset:5120\ns_waitcnt lgkmcnt(3)\nv_mfma_f32_32x32x16_bf16 v[48:63], a[8:11], v[72:75], v[48:63]\nds_read_b128 v[88:91], v1 offset:6144\n" \
    "buffer_load_dwordx4 v0, s[64:67], s69 offen offset:1024 sc1 lds\ns_waitcnt lgkmcnt(3)\nv_mfma_f32_32x32x16_bf16 v[48:63], a[12:15], v[76:79], v[48:63]\n" \
    "ds_read_b128 v[92:95], v1 offset:7168\ns_waitcnt vmcnt(6) lgkmcnt(3)\ns_barrier\nv_mfma_f32_32x32x16_bf16 v[48:63], a[16:19], v[80:83], v[48:63]\n" \
    "ds_read_b128 v[64:67], v1 offset:8192\ns_add_u32 m0, s84, 40960\ns_add_u32 s69, s82, s53\nbuffer_load_dwordx4 v0, s[64:67], s69 offen offset:0 sc1 lds\n" \
    "s_waitcnt lgkmcnt(3)\nv_mfma_f32_32x32x16_bf16 v[48:63], a[20:23], v[84:87], v[48:63]\nds_read_b128 v[68:71], v1 offset:9216\ns_waitcnt lgkmcnt(3)\n" \
    "v_mfma_f32_32x32x16_bf16 v[48:63], a[24:27], v[88:91], v[48:63]\nds_read_b128 v[72:75], v1 offset:10240\nbuffer_load_dwordx4 v0, s[64:67], s69 offen offset:1024 sc1 lds\n" \
    "s_waitcnt lgkmcnt(3)\nv_mfma_f32_32x32x16_bf16 v[48:63], a[28:31], v[92:95], v[48:63]\nds_read_b128 v[76:79], v1 offset:11264\ns_waitcnt lgkmcnt(3)\n" \
    "v_mfma_f32_32x32x16_bf16 v[48:63], a[32:35], v[64:67], v[48:63]\nds_read_b128 v[80:83], v1 offset:12288\ns_add_u32 m0, s84, 49152\ns_add_u32 s69, s82, s54\n" \
    "buffer_load_dwordx4 v0, s[64:67], s69 offen offset:0 sc1 lds\ns_waitcnt lgkmcnt(3)\nv_mfma_f32_32x32x16_bf16 v[48:63], a[36:39], v[68:71], v[48:63]\n" \
    "ds_read_b128 v[84:87], v1 offset:13312\ns_waitcnt lgkmcnt(3)\nv_mfma_f32_32x32x16_bf16 v[48:63], a[40:43], v[72:75], v[48:63]\nds_read_b128 v[88:91], v1 offset:14336\n" \
    "buffer_load_dwordx4 v0, s[64:67], s69 offen offset:1024 sc1 lds\ns_waitcnt lgkmcnt(3)\nv_mfma_f32_32x32x16_bf16 v[48:63], a[44:47], v[76:79], v[48:63]\n" \
    "ds_read_b128 v[92:95], v1 offset:15360\ns_waitcnt vmcnt(6) lgkmcnt(3)\ns_barrier\nv_mfma_f32_32x32x16_bf16 v[48:63], a[48:51], v[80:83], v[48:63]\n" \
    "ds_read_b128 v[64:67], v1 offset:16384\ns_and_b32 s69, s32, 3\ns_lshl_b32 s69, s69, 19\ns_add_u32 s86, s56, s69\ns_lshr_b32 s69, s86, 8\ns_and_b32 s69, s69, 0x2000\n" \
    "s_waitcnt lgkmcnt(3)\nv_mfma_f32_32x32x16_bf16 v[48:63], a[52:55], v[84:87], v[48:63]\nds_read_b128 v[68:71], v1 offset:17408\ns_xor_b32 s86, s86, s69\n" \
    "s_and_b32 s69, s32, 3\ns_lshl_b32 s69, s69, 19\ns_add_u32 s87, s57, s69\ns_lshr_b32 s69, s87, 8\ns_waitcnt lgkmcnt(3)\n" \
    "v_mfma_f32_32x32x16_bf16 v[48:63], a[56:59], v[88:91], v[48:63]\nds_read_b128 v[72:75], v1 offset:18432\ns_and_b32 s69, s69, 0x2000\ns_xor_b32 s87, s87, s69\n" \
    "s_and_b32 s69, s32, 3\ns_lshl_b32 s69, s69, 19\ns_add_u32 s88, s58, s69\ns_waitcnt lgkmcnt(3)\nv_mfma_f32_32x32x16_bf16 v[48:63], a[60:63], v[92:95], v[48:63]\n" \
    "ds_read_b128 v[76:79], v1 offset:19456\ns_lshr_b32 s69, s88, 8\ns_and_b32 s69, s69, 0x2000\ns_xor_b32 s88, s88, s69\ns_and_b32 s69, s32, 3\ns_lshl_b32 s69, s69, 19\n" \
    "s_waitcnt lgkmcnt(3)\nv_mfma_f32_32x32x16_bf16 v[48:63], a[64:67], v[64:67], v[48:63]\nds_read_b128 v[80:83], v1 offset:20480\ns_add_u32 s89, s59, s69\n" \
    "s_lshr_b32 s69, s89, 8\ns_and_b32 s69, s69, 0x2000\ns_xor_b32 s89, s89, s69\ns_bfe_u32 s78, s32, 0x10002\ns_waitcnt lgkmcnt(3)\n" \
    "v_mfma_f32_32x32x16_bf16 v[48:63], a[68:71], v[68:71], v[48:63]\nds_read_b128 v[84:87], v1 offset:21504\ns_waitcnt lgkmcnt(3)\n" \
    "v_mfma_f32_32x32x16_bf16 v[48:63], a[72:75], v[72:75], v[48:63]\nds_read_b128 v[88:91], v1 offset:22528\nbuffer_load_dwordx2 v[108:109], v4, s[64:67], s93 offen sc1\n" \
    "s_waitcnt lgkmcnt(3)\nv_mfma_f32_32x32x16_bf16 v[48:63], a[76:79], v[76:79], v[48:63]\nds_read_b128 v[92:95], v1 offset:23552\n" \
    "buffer_load_dwordx4 v[96:99], v0, s[64:67], s90 offen sc1\ns_waitcnt lgkmcnt(3)\nv_mfma_f32_32x32x16_bf16 v[48:63], a[80:83], v[80:83], v[48:63]\n" \
    "ds_read_b128 v[64:67], v1 offset:24576\nbuffer_load_dwordx4 v[100:103], v0, s[64:67], s91 offen sc1\ns_waitcnt lgkmcnt(3)\n" \
    "v_mfma_f32_32x32x16_bf16 v[48:63], a[84:87], v[84:87], v[48:63]\nds_read_b128 v[68:71], v1 offset:25600\nbuffer_load_dwordx4 v[104:107], v0, s[64:67], s92 offen sc1\n" \
    "s_waitcnt lgkmcnt(3)\nv_mfma_f32_32x32x16_bf16 v[48:63], a[88:91], v[88:91], v[48:63]\nds_read_b128 v[72:75], v1 offset:26624\ns_waitcnt lgkmcnt(3)\n" \
    "v_mfma_f32_32x32x16_bf16 v[48:63], a[92:95], v[92:95], v[48:63]\nds_read_b128 v[76:79], v1 offset:27648\ns_waitcnt lgkmcnt(3)\n" \
    "v_mfma_f32_32x32x16_bf16 v[48:63], a[96:99], v[64:67], v[48:63]\nds_read_b128 v[80:83], v1 offset:28672\ns_waitcnt lgkmcnt(3)\n" \
    "v_mfma_f32_32x32x16_bf16 v[48:63], a[100:103], v[68:71], v[48:63]\nds_read_b128 v[84:87], v1 offset:29696\ns_waitcnt lgkmcnt(3)\n" \
    "v_mfma_f32_32x32x16_bf16 v[48:63], a[104:107], v[72:75], v[48:63]\nds_read_b128 v[88:91], v1 offset:30720\ns_waitcnt lgkmcnt(3)\n" \
    "v_mfma_f32_32x32x16_bf16 v[48:63], a[108:111], v[76:79], v[48:63]\nds_read_b128 v[92:95], v1 offset:31744\ns_waitcnt vmcnt(6) lgkmcnt(3)\ns_barrier\n" \
    "v_mfma_f32_32x32x16_bf16 v[48:63], a[112:115], v[80:83], v[48:63]\nds_read_b128 v[64:67], v1 offset:32768\ns_waitcnt lgkmcnt(3)\n" \
    "v_mfma_f32_32x32x16_bf16 v[48:63], a[116:119], v[84:87], v[48:63]\nds_read_b128 v[68:71], v1 offset:33792\ns_waitcnt lgkmcnt(3)\n" \
    "v_mfma_f32_32x32x16_bf16 v[48:63], a[120:123], v[88:91], v[48:63]\nds_read_b128 v[72:75], v1 offset:34816\ns_waitcnt lgkmcnt(3)\n" \
    "v_mfma_f32_32x32x16_bf16 v[48:63], a[124:127], v[92:95], v[48:63]\nds_read_b128 v[76:79], v1 offset:35840\ns_waitcnt lgkmcnt(3)\n" \
    "v_mfma_f32_32x32x16_bf16 v[48:63], a[128:131], v[64:67], v[48:63]\nds_read_b128 v[80:83], v1 offset:36864\nbuffer_load_dword v150, v3, s[64:67], s85 offen sc1\n" \
    "s_waitcnt lgkmcnt(3)\nv_mfma_f32_32x32x16_bf16 v[48:63], a[132:135], v[68:71], v[48:63]\nds_read_b128 v[84:87], v1 offset:37888\ns_waitcnt lgkmcnt(3)\n" \
    "v_mfma_f32_32x32x16_bf16 v[48:63], a[136:139], v[72:75], v[48:63]\nds_read_b128 v[88:91], v1 offset:38912\ns_waitcnt lgkmcnt(3)\n" \
    "v_mfma_f32_32x32x16_bf16 v[48:63], a[140:143], v[76:79], v[48:63]\nds_read_b128 v[92:95], v1 offset:39936\ns_waitcnt lgkmcnt(3)\n" \
    "v_mfma_f32_32x32x16_bf16 v[48:63], a[144:147], v[80:83], v[48:63]\nds_read_b128 v[64:67], v1 offset:40960\ns_waitcnt lgkmcnt(3)\n" \
    "v_mfma_f32_32x32x16_bf16 v[48:63], a[148:151], v[84:87], v[48:63]\nds_read_b128 v[68:71], v1 offset:41984\ns_waitcnt vmcnt(1)\ns_waitcnt lgkmcnt(3)\n" \
    "v_max_u32 v116, v108, v109\nv_cmp_eq_u32 s[70:71], -1, v116\nv_mfma_f32_32x32x16_bf16 v[48:63], a[152:155], v[88:91], v[48:63]\nds_read_b128 v[72:75], v1 offset:43008\n" \
    "s_waitcnt lgkmcnt(3)\nv_cndmask_b32 v116, 0, 1, s[70:71]\nv_xor_b32 v96, s79, v96\nv_xor_b32 v97, s79, v97\nv_xor_b32 v98, s79, v98\n" \
    "v_mfma_f32_32x32x16_bf16 v[48:63], a[156:159], v[92:95], v[48:63]\nds_read_b128 v[76:79], v1 offset:44032\ns_waitcnt lgkmcnt(3)\nv_xor_b32 v99, s79, v99\n" \
    "v_xor_b32 v100, s79, v100\nv_xor_b32 v101, s79, v101\nv_xor_b32 v102, s79, v102\nv_mfma_f32_32x32x16_bf16 v[48:63], a[160:163], v[64:67], v[48:63]\n" \
    "ds_read_b128 v[80:83], v1 offset:45056\ns_waitcnt lgkmcnt(3)\nv_xor_b32 v103, s79, v103\nv_xor_b32 v104, s79, v104\nv_xor_b32 v105, s79, v105\nv_xor_b32 v106, s79, v106\n" \
    "v_mfma_f32_32x32x16_bf16 v[48:63], a[164:167], v[68:71], v[48:63]\nds_read_b128 v[84:87], v1 offset:46080\ns_waitcnt lgkmcnt(3)\nv_xor_b32 v107, s79, v107\n" \
    "v_or3_b32 v116, v116, v96, v97\nv_or3_b32 v116, v116, v98, v99\nv_or3_b32 v116, v116, v100, v101\nv_mfma_f32_32x32x16_bf16 v[48:63], a[168:171], v[72:75], v[48:63]\n" \
    "ds_read_b128 v[88:91], v1 offset:47104\ns_waitcnt lgkmcnt(3)\nv_or3_b32 v116, v116, v102, v103\nv_or3_b32 v116, v116, v104, v105\nv_or3_b32 v116, v116, v106, v107\n" \
    "v_and_b32 v116, 1, v116\nv_mfma_f32_32x32x16_bf16 v[48:63], a[172:175], v[76:79], v[48:63]\nds_read_b128 v[92:95], v1 offset:48128\ns_waitcnt lgkmcnt(3)\ns_barrier\n" \
    "v_cmp_eq_u32 s[70:71], 1, v116\nv_add_f32 v96, v96, v16\nv_add_f32 v97, v97, v17\nv_add_f32 v98, v98, v18\n" \
    "v_mfma_f32_32x32x16_bf16 v[48:63], a[176:179], v[80:83], v[48:63]\nds_read_b128 v[64:67], v1 offset:49152\ns_waitcnt lgkmcnt(3)\nv_add_f32 v99, v99, v19\n" \
    "v_add_f32 v100, v100, v104\nv_add_f32 v101, v101, v105\nv_add_f32 v102, v102, v106\nv_mfma_f32_32x32x16_bf16 v[48:63], a[180:183], v[84:87], v[48:63]\n" \
    "ds_read_b128 v[68:71], v1 offset:50176\ns_waitcnt lgkmcnt(3)\nv_add_f32 v103, v103, v107\nv_add_f32 v96, v96, v100\nv_add_f32 v97, v97, v101\nv_add_f32 v98, v98, v102\n" \
    "v_mfma_f32_32x32x16_bf16 v[48:63], a[184:187], v[88:91], v[48:63]\nds_read_b128 v[72:75], v1 offset:51200\ns_waitcnt lgkmcnt(3)\nv_add_f32 v99, v99, v103\n" \
    "v_cvt_pk_bf16_f32 v96, 0, v96\nv_cvt_pk_bf16_f32 v97, 0, v97\nv_cvt_pk_bf16_f32 v98, 0, v98\nv_mfma_f32_32x32x16_bf16 v[48:63], a[188:191], v[92:95], v[48:63]\n" \
    "ds_read_b128 v[76:79], v1 offset:52224\ns_waitcnt lgkmcnt(3)\nv_cvt_pk_bf16_f32 v99, 0, v99\nv_add_f32 v96, v96, v8\nv_add_f32 v97, v97, v9\nv_add_f32 v98, v98, v10\n" \
    "v_mfma_f32_32x32x16_bf16 v[48:63], a[192:195], v[64:67], v[48:63]\nds_read_b128 v[80:83], v1 offset:53248\ns_waitcnt lgkmcnt(3)\nv_add_f32 v99, v99, v11\n" \
    "v_cvt_pk_bf16_f32 v96, 0, v96\nv_cvt_pk_bf16_f32 v97, 0, v97\nv_cvt_pk_bf16_f32 v98, 0, v98\nv_mfma_f32_32x32x16_bf16 v[48:63], a[196:199], v[68:71], v[48:63]\n" \
    "ds_read_b128 v[84:87], v1 offset:54272\ns_waitcnt lgkmcnt(3)\nv_cvt_pk_bf16_f32 v99, 0, v99\nv_mul_f32 v96, v96, v12\nv_mul_f32 v97, v97, v13\nv_mul_f32 v98, v98, v14\n" \
    "v_mfma_f32_32x32x16_bf16 v[48:63], a[200:203], v[72:75], v[48:63]\nds_read_b128 v[88:91], v1 offset:55296\ns_waitcnt vmcnt(0)\ns_waitcnt lgkmcnt(3)\n" \
    "v_and_b32 v112, 127, v150\nv_cmp_gt_u32 vcc, s77, v112\nv_mul_f32 v99, v99, v15\nv_cvt_pk_bf16_f32 v96, 0, v96\n" \
    "v_mfma_f32_32x32x16_bf16 v[48:63], a[204:207], v[76:79], v[48:63]\nds_read_b128 v[92:95], v1 offset:56320\ns_cmp_eq_u64 vcc, 0\ns_cselect_b32 s81, 1, 0\n" \
    "s_waitcnt lgkmcnt(3)\nv_cvt_pk_bf16_f32 v97, 0, v97\nv_cvt_pk_bf16_f32 v98, 0, v98\nv_cvt_pk_bf16_f32 v99, 0, v99\nv_lshlrev_b32 v100, 16, v108\n" \
    "v_mfma_f32_32x32x16_bf16 v[48:63], a[208:211], v[80:83], v[48:63]\ns_waitcnt lgkmcnt(2)\nv_and_b32 v101, 0xffff0000, v108\nv_lshlrev_b32 v102, 16, v109\n" \
    "v_and_b32 v103, 0xffff0000, v109\nv_add_f32 v96, v96, v100\nv_mfma_f32_32x32x16_bf16 v[48:63], a[212:215], v[84:87], v[48:63]\ns_waitcnt lgkmcnt(0)\n" \
    "s_memrealtime s[72:73]\nv_add_f32 v97, v97, v101\nv_add_f32 v98, v98, v102\nv_add_f32 v99, v99, v103\nv_cvt_pk_bf16_f32 v116, v96, v97\n" \
    "v_mfma_f32_32x32x16_bf16 v[48:63], a[216:219], v[88:91], v[48:63]\nv_cvt_pk_bf16_f32 v117, v98, v99\nv_mfma_f32_32x32x16_bf16 v[48:63], a[220:223], v[92:95], v[48:63]\n" \
    "s_waitcnt lgkmcnt(0)\ns_cmp_ge_u32 s77, 62\ns_cselect_b32 s69, s61, s34\ns_cmp_ge_u32 s77, 64\ns_cselect_b32 s69, s60, s69\ns_sub_u32 s72, s72, s69\n" \
    "s_lshl_b32 s72, s72, 8\nv_sub_u32 v113, s72, v150\nv_cmp_gt_i32 s[74:75], 0, v113\nv_cmp_eq_u32 vcc, s77, v112\ns_nop 0\ns_and_b64 vcc, vcc, s[74:75]\n" \
    "s_cselect_b32 s69, 1, 0\ns_lshl_b32 s81, s81, s69\ns_nop 7\ns_cmp_eq_u32 s90, 0x7fff0000\ns_cbranch_scc1 .Lg16_%=\ns_cmp_lg_u64 s[70:71], 0\ns_cbranch_scc1 .Lg17_%=\n" \
    ".Lg18_%=:\nglobal_store_dwordx2 v5, v[116:117], s[46:47]\nv_add_u32 v5, 0x10000, v5\ns_mov_b32 s90, 0x7fff0000\ns_mov_b32 s91, 0x7fff0000\ns_mov_b32 s92, 0x7fff0000\n" \
    "s_mov_b32 s93, 0x7fff0000\n.Lg16_%=:\ns_nop 1\nv_cmp_u_f32 vcc, v48, v48\ns_nop 0\ns_cbranch_vccnz .Lg7_%=\n.Lg8_%=:\nv_and_or_b32 v52, v52, -2, s78\n" \
    "v_and_or_b32 v53, v53, -2, s78\nv_and_or_b32 v54, v54, -2, s78\nv_and_or_b32 v55, v55, -2, s78\nbuffer_store_dwordx4 v[52:55], v0, s[64:67], s87 offen sc1\n" \
    "v_and_or_b32 v56, v56, -2, s78\nv_and_or_b32 v57, v57, -2, s78\nv_and_or_b32 v58, v58, -2, s78\nv_and_or_b32 v59, v59, -2, s78\n" \
    "buffer_store_dwordx4 v[56:59], v0, s[64:67], s88 offen sc1\nv_and_or_b32 v60, v60, -2, s78\nv_and_or_b32 v61, v61, -2, s78\nv_and_or_b32 v62, v62, -2, s78\n" \
    "v_and_or_b32 v63, v63, -2, s78\nbuffer_store_dwordx4 v[60:63], v0, s[64:67], s89 offen sc1\ns_cmp_lg_u32 s81, 1\ns_cbranch_scc1 .Lg21_%=\ns_xor_b32 s84, s84, 0x10000\n" \
    "buffer_load_dword v151, v2, s[64:67], s83 offen sc1\ns_add_u32 m0, s84, 0\ns_add_u32 s69, s83, s48\nbuffer_load_dwordx4 v0, s[64:67], s69 offen offset:0 sc1 lds\n" \
    "buffer_load_dwordx4 v0, s[64:67], s69 offen offset:1024 sc1 lds\ns_add_u32 m0, s84, 8192\ns_add_u32 s69, s83, s49\n" \
    "buffer_load_dwordx4 v0, s[64:67], s69 offen offset:0 sc1 lds\nbuffer_load_dwordx4 v0, s[64:67], s69 offen offset:1024 sc1 lds\ns_add_u32 m0, s84, 16384\n" \
    "s_add_u32 s69, s83, s50\nbuffer_load_dwordx4 v0, s[64:67], s69 offen offset:0 sc1 lds\nbuffer_load_dwordx4 v0, s[64:67], s69 offen offset:1024 sc1 lds\n.Lg21_%=:\n" \
    "v_mov_b64 v[16:17], v[48:49]\nv_mov_b64 v[18:19], v[50:51]\ns_add_u32 s90, s86, 0x1000\ns_add_u32 s91, s86, 0x4000\ns_add_u32 s92, s86, 0x5000\ns_mov_b32 s93, s82\n" \
    "s_mov_b32 s79, s78\ns_sub_u32 s69, s32, 1\ns_cmp_lt_u32 s69, 62\ns_cbranch_scc1 .Lg15_%=\ns_memrealtime s[72:73]\ns_waitcnt lgkmcnt(0)\ns_add_u32 s76, s72, s35\n.Lg22_%=:\n" \
    "s_memrealtime s[72:73]\ns_waitcnt lgkmcnt(0)\ns_sub_u32 s72, s72, s76\ns_cmp_lt_i32 s72, 0\ns_cbranch_scc1 .Lg22_%=\n.Lg19_%=:\n" \
    "buffer_load_dwordx2 v[108:109], v4, s[64:67], s93 offen sc1\nbuffer_load_dwordx4 v[96:99], v0, s[64:67], s90 offen sc1\n" \
    "buffer_load_dwordx4 v[100:103], v0, s[64:67], s91 offen sc1\nbuffer_load_dwordx4 v[104:107], v0, s[64:67], s92 offen sc1\ns_waitcnt vmcnt(0)\nv_max_u32 v116, v108, v109\n" \
    "v_cmp_eq_u32 s[70:71], -1, v116\ns_nop 1\nv_cndmask_b32 v116, 0, 1, s[70:71]\nv_xor_b32 v96, s79, v96\nv_xor_b32 v97, s79, v97\nv_xor_b32 v98, s79, v98\n" \
    "v_xor_b32 v99, s79, v99\nv_xor_b32 v100, s79, v100\nv_xor_b32 v101, s79, v101\nv_xor_b32 v102, s79, v102\nv_xor_b32 v103, s79, v103\nv_xor_b32 v104, s79, v104\n" \
    "v_xor_b32 v105, s79, v105\nv_xor_b32 v106, s79, v106\nv_xor_b32 v107, s79, v107\nv_or3_b32 v116, v116, v96, v97\nv_or3_b32 v116, v116, v98, v99\n" \
    "v_or3_b32 v116, v116, v100, v101\nv_or3_b32 v116, v116, v102, v103\nv_or3_b32 v116, v116, v104, v105\nv_or3_b32 v116, v116, v106, v107\nv_and_b32 v116, 1, v116\n" \
    "v_cmp_eq_u32 s[70:71], 1, v116\nv_pk_add_f32 v[96:97], v[96:97], v[16:17]\nv_pk_add_f32 v[98:99], v[98:99], v[18:19]\nv_pk_add_f32 v[100:101], v[100:101], v[104:105]\n" \
    "v_pk_add_f32 v[102:103], v[102:103], v[106:107]\nv_pk_add_f32 v[96:97], v[96:97], v[100:101]\nv_pk_add_f32 v[98:99], v[98:99], v[102:103]\nv_cvt_pk_bf16_f32 v96, 0, v96\n" \
    "v_cvt_pk_bf16_f32 v97, 0, v97\nv_cvt_pk_bf16_f32 v98, 0, v98\nv_cvt_pk_bf16_f32 v99, 0, v99\nv_pk_add_f32 v[96:97], v[96:97], v[8:9]\n" \
    "v_pk_add_f32 v[98:99], v[98:99], v[10:11]\nv_cvt_pk_bf16_f32 v96, 0, v96\nv_cvt_pk_bf16_f32 v97, 0, v97\nv_cvt_pk_bf16_f32 v98, 0, v98\nv_cvt_pk_bf16_f32 v99, 0, v99\n" \
    "v_pk_mul_f32 v[96:97], v[96:97], v[12:13]\nv_pk_mul_f32 v[98:99], v[98:99], v[14:15]\nv_cvt_pk_bf16_f32 v96, 0, v96\nv_cvt_pk_bf16_f32 v97, 0, v97\n" \
    "v_cvt_pk_bf16_f32 v98, 0, v98\nv_cvt_pk_bf16_f32 v99, 0, v99\nv_lshlrev_b32 v100, 16, v108\nv_and_b32 v101, 0xffff0000, v108\nv_lshlrev_b32 v102, 16, v109\n" \
    "v_and_b32 v103, 0xffff0000, v109\nv_pk_add_f32 v[96:97], v[96:97], v[100:101]\nv_pk_add_f32 v[98:99], v[98:99], v[102:103]\nv_cvt_pk_bf16_f32 v116, v96, v97\n" \
    "v_cvt_pk_bf16_f32 v117, v98, v99\ns_cmp_lg_u64 s[70:71], 0\ns_cbranch_scc1 .Lg20_%=\nglobal_store_dwordx2 v5, v[116:117], s[46:47]\nv_add_u32 v5, 0x10000, v5\n" \
    "s_mov_b32 s90, 0x7fff0000\ns_mov_b32 s91, 0x7fff0000\ns_mov_b32 s92, 0x7fff0000\ns_mov_b32 s93, 0x7fff0000\n.Lg15_%=:\ns_mov_b32 s38, s85\ns_xor_b32 s39, s39, 128\n" \
    "s_xor_b32 s80, s80, 0x10000\nv_xor_b32 v1, 0x10000, v1\ns_mov_b32 s68, 0\ns_add_u32 s32, s32, 1\ns_cmp_lt_u32 s32, 64\ns_cbranch_scc1 .Lg2_%=\ns_endpgm\n.Lg4_%=:\n" \
    "s_sleep 8\ns_add_u32 s68, s68, 1\ns_and_b32 s69, s68, 63\ns_cbranch_scc1 .Lg3_%=\ns_cmp_gt_u32 s68, 0x400000\ns_cbranch_scc1 .Lg1_%=\n" \
    "buffer_load_dword v253, off, s[64:67], 0 sc1\ns_waitcnt vmcnt(0)\nv_readfirstlane_b32 s69, v253\ns_nop 0\ns_cmp_eq_u32 s69, 1\ns_cbranch_scc0 .Lg3_%=\ns_endpgm\n.Lg13_%=:\n" \
    "s_cmp_ge_u32 s32, 61\ns_cbranch_scc1 .Lg14_%=\ns_sleep 10\n.Lg14_%=:\ns_sleep 3\nbuffer_load_dword v151, v2, s[64:67], s82 offen sc1\ns_waitcnt vmcnt(0)\n" \
    "v_cmp_eq_u32 vcc, -1, v151\ns_nop 0\ns_cbranch_vccz .Lg12_%=\ns_add_u32 s68, s68, 1\ns_and_b32 s69, s68, 63\ns_cbranch_scc1 .Lg13_%=\ns_cmp_gt_u32 s68, 0x400000\n" \
    "s_cbranch_scc1 .Lg1_%=\nbuffer_load_dword v253, off, s[64:67], 0 sc1\ns_waitcnt vmcnt(0)\nv_readfirstlane_b32 s69, v253\ns_nop 0\ns_cmp_eq_u32 s69, 1\n" \
    "s_cbranch_scc0 .Lg13_%=\ns_endpgm\n.Lg17_%=:\ns_sleep 4\nbuffer_load_dwordx2 v[108:109], v4, s[64:67], s93 offen sc1\n" \
    "buffer_load_dwordx4 v[96:99], v0, s[64:67], s90 offen sc1\nbuffer_load_dwordx4 v[100:103], v0, s[64:67], s91 offen sc1\n" \
    "buffer_load_dwordx4 v[104:107], v0, s[64:67], s92 offen sc1\ns_waitcnt vmcnt(0)\nv_max_u32 v116, v108, v109\nv_cmp_eq_u32 s[70:71], -1, v116\ns_nop 1\n" \
    "v_cndmask_b32 v116, 0, 1, s[70:71]\nv_xor_b32 v96, s79, v96\nv_xor_b32 v97, s79, v97\nv_xor_b32 v98, s79, v98\nv_xor_b32 v99, s79, v99\nv_xor_b32 v100, s79, v100\n" \
    "v_xor_b32 v101, s79, v101\nv_xor_b32 v102, s79, v102\nv_xor_b32 v103, s79, v103\nv_xor_b32 v104, s79, v104\nv_xor_b32 v105, s79, v105\nv_xor_b32 v106, s79, v106\n" \
    "v_xor_b32 v107, s79, v107\nv_or3_b32 v116, v116, v96, v97\nv_or3_b32 v116, v116, v98, v99\nv_or3_b32 v116, v116, v100, v101\nv_or3_b32 v116, v116, v102, v103\n" \
    "v_or3_b32 v116, v116, v104, v105\nv_or3_b32 v116, v116, v106, v107\nv_and_b32 v116, 1, v116\nv_cmp_eq_u32 s[70:71], 1, v116\nv_pk_add_f32 v[96:97], v[96:97], v[16:17]\n" \
    "v_pk_add_f32 v[98:99], v[98:99], v[18:19]\nv_pk_add_f32 v[100:101], v[100:101], v[104:105]\nv_pk_add_f32 v[102:103], v[102:103], v[106:107]\n" \
    "v_pk_add_f32 v[96:97], v[96:97], v[100:101]\nv_pk_add_f32 v[98:99], v[98:99], v[102:103]\nv_cvt_pk_bf16_f32 v96, 0, v96\nv_cvt_pk_bf16_f32 v97, 0, v97\n" \
    "v_cvt_pk_bf16_f32 v98, 0, v98\nv_cvt_pk_bf16_f32 v99, 0, v99\nv_pk_add_f32 v[96:97], v[96:97], v[8:9]\nv_pk_add_f32 v[98:99], v[98:99], v[10:11]\n" \
    "v_cvt_pk_bf16_f32 v96, 0, v96\nv_cvt_pk_bf16_f32 v97, 0, v97\nv_cvt_pk_bf16_f32 v98, 0, v98\nv_cvt_pk_bf16_f32 v99, 0, v99\nv_pk_mul_f32 v[96:97], v[96:97], v[12:13]\n" \
    "v_pk_mul_f32 v[98:99], v[98:99], v[14:15]\nv_cvt_pk_bf16_f32 v96, 0, v96\nv_cvt_pk_bf16_f32 v97, 0, v97\nv_cvt_pk_bf16_f32 v98, 0, v98\nv_cvt_pk_bf16_f32 v99, 0, v99\n" \
    "v_lshlrev_b32 v100, 16, v108\nv_and_b32 v101, 0xffff0000, v108\nv_lshlrev_b32 v102, 16, v109\nv_and_b32 v103, 0xffff0000, v109\n" \
    "v_pk_add_f32 v[96:97], v[96:97], v[100:101]\nv_pk_add_f32 v[98:99], v[98:99], v[102:103]\nv_cvt_pk_bf16_f32 v116, v96, v97\nv_cvt_pk_bf16_f32 v117, v98, v99\n" \
    "s_cmp_lg_u64 s[70:71], 0\ns_cbranch_scc0 .Lg18_%=\ns_add_u32 s68, s68, 1\ns_and_b32 s69, s68, 63\ns_cbranch_scc1 .Lg17_%=\ns_cmp_gt_u32 s68, 0x400000\n" \
    "s_cbranch_scc1 .Lg1_%=\nbuffer_load_dword v253, off, s[64:67], 0 sc1\ns_waitcnt vmcnt(0)\nv_readfirstlane_b32 s69, v253\ns_nop 0\ns_cmp_eq_u32 s69, 1\n" \
    "s_cbranch_scc0 .Lg17_%=\ns_endpgm\n.Lg20_%=:\ns_sleep 4\ns_add_u32 s68, s68, 1\ns_and_b32 s69, s68, 63\ns_cbranch_scc1 .Lg19_%=\ns_cmp_gt_u32 s68, 0x400000\n" \
    "s_cbranch_scc1 .Lg1_%=\nbuffer_load_dword v253, off, s[64:67], 0 sc1\ns_waitcnt vmcnt(0)\nv_readfirstlane_b32 s69, v253\ns_nop 0\ns_cmp_eq_u32 s69, 1\n" \
    "s_cbranch_scc0 .Lg19_%=\ns_endpgm\n.Lg7_%=:\nv_mov_b32 v120, 0\nv_mov_b32 v121, v1\ns_mov_b32 s76, 14\n.Lg10_%=:\nds_read_b128 v[124:127], v121 offset:0\n" \
    "ds_read_b128 v[128:131], v121 offset:1024\nds_read_b128 v[132:135], v121 offset:2048\nds_read_b128 v[136:139], v121 offset:3072\nv_add_u32 v121, 0x1000, v121\n" \
    "s_waitcnt lgkmcnt(0)\nv_max3_u32 v120, v120, v124, v125\nv_max3_u32 v120, v120, v126, v127\nv_max3_u32 v120, v120, v128, v129\nv_max3_u32 v120, v120, v130, v131\n" \
    "v_max3_u32 v120, v120, v132, v133\nv_max3_u32 v120, v120, v134, v135\nv_max3_u32 v120, v120, v136, v137\nv_max3_u32 v120, v120, v138, v139\ns_sub_u32 s76, s76, 1\n" \
    "s_cmp_lg_u32 s76, 0\ns_cbranch_scc1 .Lg10_%=\nv_cmp_eq_u32 vcc, -1, v120\ns_nop 0\ns_cbranch_vccz .Lg8_%=\n.Lg9_%=:\ns_mov_b32 s81, 0\ns_barrier\ns_sleep 4\n" \
    "s_add_u32 s68, s68, 1\ns_and_b32 s69, s68, 63\ns_cbranch_scc1 .Lg2_%=\ns_cmp_gt_u32 s68, 0x400000\ns_cbranch_scc1 .Lg1_%=\nbuffer_load_dword v253, off, s[64:67], 0 sc1\n" \
    "s_waitcnt vmcnt(0)\nv_readfirstlane_b32 s69, v253\ns_nop 0\ns_cmp_eq_u32 s69, 1\ns_cbranch_scc0 .Lg2_%=\ns_endpgm\n.Lg1_%=:\nv_mov_b32 v253, 1\n" \
    "buffer_store_dword v253, off, s[64:67], 0 sc1\ns_endpgm\n"
typedef unsigned v32u __attribute__((ext_vector_type(32)));
typedef unsigned v16u __attribute__((ext_vector_type(16)));
#define NX ((xg >> 2) * 2 + (i8 & 1))
#define NF 1
#define KX (NF ? (xg >> 2) << 10 | set << 9 : set << 10)
#define HX (head >> 3)
template <bool isV>
__device__ __forceinline__ void s1_main(const Args& a, unsigned char* lds, const int xg, const int ii) {
    const int wave = a.pad0, head = xg * 4 + (ii >> 2), set = (ii >> 1) & 1;
    char* const ws = a.ws;
    unsigned* const ctl = (unsigned*)(ws + O_CTL);
    const unsigned pubb = isV ? O_UM + (wave & 1) * 262144 + head * 256 : O_EP + ((wave & 1) * 4 + (head >> 3)) * 32768;
    const unsigned psb = isV ? O_UX + (wave & 1) * MiB + (head >> 3) * 16384 + (head & 7) * 1024 : O_PS + (wave * 4 + (head >> 3)) * 16384;
    v16bf w[52], pend[12];
    v4u rX = mkrs(isV ? a.Wv : wave < 2 ? a.Wq : a.Wk, DM * HD * 2), rO = mkrs(a.Wo, DM * HD * 2);
    unsigned bo[4];
#pragma unroll
    for (int i = 0; i < 4; ++i) {
        const int lane = lane_id(), row = lane >> 3, ch = (lane & 7) ^ ((2 * (i & 1) + (row >> 2)) << 1);
        bo[i] = i < 2 ? (((isV ? 64 * (wave >> 1) : 0) + row) * HD + head * DH + 64 * (wave & 1) + 8 * ch) * 2 : ((head * DH + row) * DM + 256 * wave + 8 * ch) * 2;
    }
    asm volatile("s_nop 4" : "+s"(rX), "+s"(rO));
    const int kx = KX, hx = HX;
    unsigned go;
    {
        const int lane = lane_id(), l15 = lane & 15, gb = lane >> 4;
        auto bufp = [&](int u, int wv) { const int b = (u + 4) % 5; return lds + (b < 3 ? L_W + wv * 24576 + b * 8192 : L_H + wv * 16384 + (b - 3) * 8192); };
        auto issue = [&](auto uu) {
            constexpr int u = decltype(uu)::v, c = u;
            constexpr bool wo = isV && c >= 8;
            const unsigned d = (unsigned)(size_t)(LAS unsigned char*)bufp(u, wave);
#pragma unroll
            for (int r8 = 0; r8 < 8; ++r8)
                bdma<(NTW & (u < 4 ? 1 : u < 8 ? 2 : 64))>(wo ? rO : rX, wo ? bo[2 + (r8 & 1)] : bo[r8 & 1], wo ? ((64 * (c & 1) + 8 * r8) * DM + 64 * (((c - 8) >> 1) ^ hx)) * 2 : (((isV ? 128 : 64) * c + 8 * r8) ^ (kx >> 1)) * HD * 2, d + r8 * 1024);
        };
        const unsigned q = l15 >> 2, p = lane & 3;
        const unsigned row0 = 16 * (gb >> 1) + 4 * (gb & 1) + (q & 1) + 8 * (q >> 1);
        const unsigned sw = ((gb & 1) | ((q >> 1) << 1)) << 1;
        auto flush = [&](int j) {
            const v8u v = __builtin_bit_cast(v8u, pend[j]);
            unsigned char* d = lds + L_W + wave * 24576 + j * 2048 + lane * 16;
            *(LAS v4u*)d = (v4u){v[0], v[1], v[2], v[3]};
            *(LAS v4u*)(d + 1024) = (v4u){v[4], v[5], v[6], v[7]};
        };
        sfor<0, 16>([&](auto cc) {
            constexpr int u = decltype(cc)::v, c = u;
            asm volatile("s_waitcnt vmcnt(%0)\n\ts_barrier" :: "n"(u == 0 ? 32 : u < 13 ? 24 : 8 * (15 - u) + (u == 15)) : "memory");
            if constexpr (u == 14) asm volatile("s_nop 4\n\tglobal_load_dword %0, %1, %2 sc1" : "=&v"(go) : "v"(O_GO + blockIdx.x * 128), "s"(ws));
            if constexpr (u > 0 && u + 4 < 16) issue(IC<u + 4>{});
            if (u >= 12 && u < 15) { flush(4 * u - 48); flush(4 * u - 47); flush(4 * u - 46); flush(4 * u - 45); }
            v16bf t[4];
#pragma unroll
            for (int n = 0; n < 4; ++n) {
                constexpr bool pk = isV && c >= 8;
                const unsigned char* buf = bufp(u, pk ? wave : (n & 2) + (wave >> 1));
                const unsigned o = pk ? (32 * (n & 1) + 16 * (gb & 1) + 4 * (gb >> 1) + (q & 1)) * 128 + (((4 * (n >> 1) + 2 * (q >> 1) + (p >> 1)) ^ (gb & 2)) << 4) + (p & 1) * 8
                                      : row0 * 128 + (((4 * (wave & 1) + 2 * (n & 1) + (p >> 1)) ^ sw) << 4) + (p & 1) * 8;
                const unsigned o2 = pk ? (o ^ 64) + 8 * 128 : o + 32 * 128;
                const v2u r0 = tr_rd(buf, o), r1 = tr_rd(buf, o + 2 * 128), r2 = tr_rd(buf, o2), r3 = tr_rd(buf, o2 + 2 * 128);
                const v8u v = {r0.x, r0.y, r1.x, r1.y, r2.x, r2.y, r3.x, r3.y};
                t[n] = __builtin_bit_cast(v16bf, v);
            }
            asm volatile("s_waitcnt lgkmcnt(0)" ::: "memory");
            sfor<0, 4>([&](auto nn) {
                constexpr int n = decltype(nn)::v, j = lsl(isV, c, n), r = ridx(isV, c, n);
                if constexpr (j >= 0) { pend[j] = t[n]; asm volatile("" : "+v"(pend[j])); }
                else {
                    w[r] = t[n];
                    if constexpr (r < 32) asm volatile("" : "+a"(w[r])); else asm volatile("" : "+v"(w[r]));
                }
            });
        });
    }
    RSTAMP(74, 0);
    asm volatile("s_waitcnt vmcnt(0)\n\tv_readfirstlane_b32 %0, %1" : "=s"(go) : "v"(go));
    const unsigned pol = (go ? go : wait_go(ws)) & 1;
    lds_barrier();
    RSTAMP(74, 1);
    const unsigned lb0 = (unsigned)(size_t)(LAS unsigned char*)lds;
    const unsigned lane = lane_id(), l15 = lane & 15, g = lane >> 4, q = l15 >> 2, p = lane & 3, l16 = lane * 16, to = g * 128 + (l15 & 7) * 16, xb = ((g & 1) << 1) | (g >> 1);
    const unsigned r = 8 * wave + (lane >> 3), ao = r * 2048 + 16 * ((lane & 7) ^ ((r >> 1) & 7)) + 1024, ra = lb0 + L_H + (l15 >> 3) * 2048 + (l15 & 7) * 128;
    const v16u c0 = {(lane & 8) ? 0xEEEEu : 0x4444u, lb0 + L_H + l16, l16, lb0 + L_W + wave * 24576 + l16, lb0 + L_T + wave * 512 + q * 128 + (g & 1) * 64 + (g >> 1) * 32 + p * 2,
                     lb0 + (isV ? L_T + to : L_QX + (wave >> 1) * 4096 + (wave & 1) * 128 + g * 128 + to), isV ? lb0 + L_QX + g * 128 + to : lb0 + L_T + to,
                     isV ? xb * 16384 + l15 * 16 : g < 2 ? (l15 >> 3) * 2048 + (head & 7) * 256 + to : lane < 40 ? 4096 + (head & 7) * 128 + l15 * 16 : lane == 40 && head + wave == 0 ? 5120 : 0x7FFF0000u, isV ? xb * 65536 + l15 * 16 : lb0 + L_T + 2048 + (to ^ (l15 >> 3) * 64),
                     ra + 16 * (g ^ (l15 >> 1)), ra + 16 * ((4 + g) ^ (l15 >> 1)), ao, ao - 896,
                     0u, lb0 + (isV ? L_QX : L_KX) + wave * 1024 + l16, 0u};
    const unsigned sd = ((pol ^ (wave >> 1)) & 1) * 8192;
    PARS;
    const v16u s0 = {0x3e0293eeu, 0x3db504f3u, 0x3fb8aa3bu, (unsigned)set, (unsigned)wave, O_E1 + (((xg >> 1) & 1) * 4 + wave) * 16384 + ((pol ^ (xg >> 2)) & 1) * 8192 + 2 * kx, pubb + sd + (isV ? hx * 65536 : 0), psb + (isV ? sd + hx * 262144 : 0),
                     O_H1 - 1024 + set * 65536, lb0 + L_H + wave * 2048, hw(pol, xg) + H_C + 512 * set, par(P_R), par(P_A), par(P_X), lb0 + L_ST + (wave == STAMP_WAVE ? 0 : MiB), lb0 + L_ST};
    u64 cm[4];
#pragma unroll
    for (int i = 0; i < 4; ++i) cm[i] = __ballot(16 * (l15 >> 3) + 4 * g + i <= 8 * wave + (l15 & 7));
    const u64 dbg = DBG(a, blockIdx.x);
    const v16u s1 = {isV ? O_FL + set * 16384 + sd + (wave & 1) * 4096 + head * 128 : head + wave ? 0x7FFF0000u : 0u, isV ? set : par(P_M), isV ? set : (unsigned)cm[0], (unsigned)(cm[0] >> 32), (unsigned)cm[1], (unsigned)(cm[1] >> 32),
                     (unsigned)cm[2], (unsigned)(cm[2] >> 32), (unsigned)cm[3], (unsigned)(cm[3] >> 32), (unsigned)dbg, (unsigned)(dbg >> 32), lb0 + L_T + wave * 512, ii < 4 ? hw(pol, xg) + H_D + 4 * wave : 0u, (unsigned)kx,
                     head < (isV ? 20 : 28) ? O_D + (isV ? D_X2 : 0) + head * 8192 + wave * 2048 : 0x70000000u};
    const v4u rws = mkrs(ws, WS_END);
    v32u ww[13];
#pragma unroll
    for (int i = 0; i < 13; ++i) {
        const v8u x0 = __builtin_bit_cast(v8u, w[4 * i]), x1 = __builtin_bit_cast(v8u, w[4 * i + 1]), x2 = __builtin_bit_cast(v8u, w[4 * i + 2]), x3 = __builtin_bit_cast(v8u, w[4 * i + 3]);
        ww[i] = __builtin_shufflevector(__builtin_shufflevector(x0, x1, 0, 1, 2, 3, 4, 5, 6, 7, 8, 9, 10, 11, 12, 13, 14, 15), __builtin_shufflevector(x2, x3, 0, 1, 2, 3, 4, 5, 6, 7, 8, 9, 10, 11, 12, 13, 14, 15),
                                        0, 1, 2, 3, 4, 5, 6, 7, 8, 9, 10, 11, 12, 13, 14, 15, 16, 17, 18, 19, 20, 21, 22, 23, 24, 25, 26, 27, 28, 29, 30, 31);
    }
#define S1_CALL(TXT) asm volatile("s_nop 4\n" TXT :: "{v[0:15]}"(c0), "{s[32:47]}"(s0), "{s[48:63]}"(s1), "{s[64:67]}"(rws), \
              "{a[0:31]}"(ww[0]), "{a[32:63]}"(ww[1]), "{a[64:95]}"(ww[2]), "{a[96:127]}"(ww[3]), "{a[128:159]}"(ww[4]), "{a[160:191]}"(ww[5]), "{a[192:223]}"(ww[6]), "{a[224:255]}"(ww[7]), \
              "{v[92:123]}"(ww[8]), "{v[124:155]}"(ww[9]), "{v[156:187]}"(ww[10]), "{v[188:219]}"(ww[11]), "{v[220:251]}"(ww[12]) : "memory", "s95", "v253")
    if constexpr (isV) S1_CALL(ASM_V); else S1_CALL(ASM_Q);
}

__device__ __forceinline__ float gelu_tanh(float v) {
    const float e = v * (-2.302208198f - 0.1029432397f * (v * v));
    return v * __builtin_amdgcn_rcpf(1.f + __builtin_amdgcn_exp2f(e));
}
__device__ __forceinline__ v4u unsent4(v4u v) { return (v4u){unsent(v.x), unsent(v.y), unsent(v.z), unsent(v.w)}; }
__device__ __forceinline__ unsigned rt_now() { return (unsigned)__builtin_amdgcn_s_memrealtime(); }
__device__ __forceinline__ void rt_delay(unsigned ticks  , unsigned t0 = rt_now()) {
    while ((unsigned)__builtin_amdgcn_s_memrealtime() - t0 < ticks) __builtin_amdgcn_s_sleep(2);
}
__device__ __forceinline__ float wave_sum(float x) {
    x += __uint_as_float(__builtin_amdgcn_update_dpp(0, __float_as_uint(x), 0xB1, 0xF, 0xF, false));
    x += __uint_as_float(__builtin_amdgcn_update_dpp(0, __float_as_uint(x), 0x4E, 0xF, 0xF, false));
    x += __uint_as_float(__builtin_amdgcn_update_dpp(0, __float_as_uint(x), 0x124, 0xF, 0xF, false));
    x += __uint_as_float(__builtin_amdgcn_update_dpp(0, __float_as_uint(x), 0x128, 0xF, 0xF, false));
    return red32_add(red16_add(x));
}
__device__ __forceinline__ void ln_row(const v4u (&xr)[2], const f32x4 (&c1)[4], const f32x4 (&sh)[4], v4u (&o)[2]) {
    f32x2 v[8];
    f32x2 s2 = {0.f, 0.f};
#pragma unroll
    for (int j = 0; j < 8; ++j) { v[j] = (f32x2){lo16f(xr[j >> 2][j & 3]), hi16f(xr[j >> 2][j & 3])}; s2 += v[j]; }
    const float mean = wave_sum(s2[0] + s2[1]) * (1.f / DM);
    f32x2 q2 = {0.f, 0.f};
#pragma unroll
    for (int j = 0; j < 8; ++j) { const f32x2 d = v[j] - mean; q2 += d * d; }
    const float vs = wave_sum(q2[0] + q2[1]);
    const float mb = rbf(mean), vb = rbf(vs * (1.f / DM));
    const float rsd = 1.f / rbf(sqrtf(rbf(vb + 1e-5f)));
    auto r2 = [](f32x2 z) { const unsigned u = pack2(z[0], z[1]); return (f32x2){lo16f(u), hi16f(u)}; };
#pragma unroll
    for (int j = 0; j < 8; ++j) {
        const f32x2 cc = {c1[j >> 1][2 * (j & 1)], c1[j >> 1][2 * (j & 1) + 1]}, ss = {sh[j >> 1][2 * (j & 1)], sh[j >> 1][2 * (j & 1) + 1]};
        const f32x2 y = r2(r2(r2(v[j] - mb) * rsd) * cc) + ss;
        o[j >> 2][j & 3] = pack2(y[0], y[1]);
    }
}
template <int N> __device__ __forceinline__ void ld_mod(rsrc_t rs, unsigned* ctl, const int (&col)[N], f32x4* m) {
    for (unsigned spins = 0;;) {
        unsigned mx = 0;
#pragma unroll
        for (int i = 0; i < N; ++i) { const v4u v = bld16(rs, O_MOD + col[i] * 4); mx = max(mx, umax4(v)); m[i] = __builtin_bit_cast(f32x4, v); }
        if (!__any(mx == SENT)) break;
        __builtin_amdgcn_s_sleep(6);
        if (spin_fail(spins, ctl + C_ABORT)) asm volatile("s_endpgm");
    }
}
__device__ __forceinline__ void set_m0(unsigned v) { asm volatile("s_mov_b32 m0, %0" :: "s"(v) : "m0"); }
__device__ __forceinline__ void mod8(rsrc_t rs, int col, f32x4 (&m)[2]) {
    f32x4 p[4][2];
#pragma unroll
    for (int k = 0; k < 8; ++k) p[k >> 1][k & 1] = __builtin_bit_cast(f32x4, bld16(rs, O_MODP + ((k >> 1) * 2048 + col + 4 * (k & 1)) * 4));
#pragma unroll
    for (int h = 0; h < 2; ++h) {
        const f32x4 t = (p[0][h] + p[1][h]) + (p[2][h] + p[3][h]);
#pragma unroll
        for (int i = 0; i < 4; ++i) { float v = rbf(t[i]); if (col >= DM) v = rbf(1.f + v); m[h][i] = v; }
    }
}
template <int N, class F> __device__ __forceinline__ void mod_cols(const Args& a, rsrc_t rs, unsigned char* lds, int cg, int k0, F&& mid) {
    const int tid = otid();
    v4u wv[N];
    const v2u cr = *(const v2u*)(a.c + 4 * tid);
#pragma unroll
    for (int i = 0; i < N; ++i) {
        const v4u* p = (const v4u*)(a.w_mod + (size_t)(k0 + (tid >> 3) + 32 * i) * 6144 + cg * 64 + 8 * (tid & 7));
        wv[i] = NTW & 4 ? __builtin_nontemporal_load(p) : *p;
    }
    SB(); mid(); SB();
    f32x4 sv = {lo16f(cr.x), hi16f(cr.x), lo16f(cr.y), hi16f(cr.y)};
#pragma unroll
    for (int i = 0; i < 4; ++i) sv[i] = rbf(sv[i] / (1.f + __expf(-sv[i])));
    *(LAS f32x4*)(lds + 16384 + tid * 16) = sv;
    lds_barrier();
    f32x2 acc[4] = {{0.f, 0.f}, {0.f, 0.f}, {0.f, 0.f}, {0.f, 0.f}};
#pragma unroll
    for (int i = 0; i < N; ++i) {
        const float sv = *(const LAS float*)(lds + 16384 + (k0 + (tid >> 3) + 32 * i) * 4);
#pragma unroll
        for (int j = 0; j < 4; ++j) acc[j] += sv * (f32x2){lo16f(wv[i][j]), hi16f(wv[i][j])};
    }
    unsigned char* red = lds + 20480;
    *(LAS f32x4*)(red + (tid >> 3) * 272 + (tid & 7) * 32) = (f32x4){acc[0][0], acc[0][1], acc[1][0], acc[1][1]};
    *(LAS f32x4*)(red + (tid >> 3) * 272 + (tid & 7) * 32 + 16) = (f32x4){acc[2][0], acc[2][1], acc[3][0], acc[3][1]};
    lds_barrier();
    if (tid < 64) {
        f32x4 t = {0.f, 0.f, 0.f, 0.f};
#pragma unroll
        for (int i = 0; i < 8; ++i) t += *(const LAS f32x4*)(red + ((tid >> 4) * 8 + i) * 272 + (tid & 15) * 16);
#pragma unroll
        for (int i = 0; i < 4; ++i) { float v = red32_add(red16_add(t[i])); if (N == 32) { v = rbf(v); if ((cg >> 4) == 4) v = rbf(1.f + v); } t[i] = v; }
        if (tid < 16) bst16(rs, (N == 32 ? O_MOD : O_MODP + k0 * 32) + (cg * 64 + 4 * tid) * 4, unsent4(__builtin_bit_cast(v4u, t)));
    }
}
#define TD_A 300
#define TD_L 1000
#define TD_M 850
#define TD_G 120
constexpr int NARM0 = 2216, NARM1 = 4712;
__device__ __forceinline__ unsigned arm0(unsigned v) {
    if (v < 104) return (v < 64 ? O_FL : O_HW - 65536) + v * 1024;
    if ((v -= 104) < 768) return (v < 384 ? O_EP : O_PS - MiB / 2) + v / 6 * 8192 + v % 6 * 1024;
    if ((v -= 768) < 1024) return O_E1 + 131072 + v * 1024;
    if ((v -= 1024) < 320) return O_D + D_X2 + v / 160 * D_SLOT + v % 160 * 1024;
    if ((v -= 320) < 448) return O_D + v / 224 * D_SLOT + v % 224 * 1024;
    return O_SL + (v - 448) * 1024;
}
__device__ __forceinline__ void f_start(const Args& a, rsrc_t rs, unsigned char* lds, int idx  , unsigned t0) {
    const int tid = otid(), lane = tid & 63, wave = a.pad0;
    unsigned* const ctl = (unsigned*)(a.ws + O_CTL);
    const int r0 = 4 * idx + wave;
    const bool boss = idx == 63;
    unsigned gv = wave >> 1;
    auto serve = [&](bool all) { if (gv && (all || !ldg4_sc1(a.ws, O_GO + tid * 128))) __hip_atomic_store((gu32*)(a.ws + O_GO + tid * 128), gv, RLX_AGENT); };
    if (boss) bst16(rs, tid * 16, (v4u){0u, 0u, 0u, 0u});
    v4u xr[6][2];
    auto ldx = [&](int k, int row) { for (int c = 0; c < 2; ++c) xr[k][c] = *(const v4u*)(a.x + (size_t)row * DM + 512 * c + 8 * lane); };
    SB();
    mod_cols<8>(a, rs, lds, idx & 31, (idx >> 5) * 256, [&]() {
        if (boss) { asm volatile("s_waitcnt vmcnt(8)\n\ts_barrier" ::: "memory"); rt_delay(TD_G, t0); serve(true); }
        ldx(0, r0); if (idx == 55) ldx(1, r0 + 16);
    });
    RSTAMP(73, 1);
    if (boss) serve(false); else if (wave == 0) { wait_vm0(); wait_go(a.ws); }
    drained(ctl + C_M + 32 * (idx & 7));
    RSTAMP(73, 2);
    if (idx >= 64) {
        for (unsigned v = r0 - 256; v < NARM0; v += 256) bst16(rs, arm0(v) + lane * 16, v < 80 ? (v4u){0u, 0u, 0u, 0u} : sentv());
        if (idx < 80) {
            const unsigned u = wave * 256 + ((idx >> 1) & 3) * 64 + lane;
            const v4u m = unsent4(*(const v4u*)(a.mem0 + (u & 7) * DM + (u >> 4) * 16 + ((u >> 3) & 1) * 8));
            bst16(rs, O_E1 + (idx - 64) * 8192 + tid * 16, m); bst16(rs, O_E1 + (idx - 64) * 8192 + 4096 + tid * 16, m);
        }
    }
    if (idx == 59) {
        unsigned best[2] = {~0u, ~0u};
        for (int r = 0; r < NPR; ++r) {
            const u64 t0 = __builtin_readcyclecounter();
            asm volatile("s_mov_b64 exec, 1\n\ts_nop 4\n\tglobal_store_dword %0, %1, %2 sc1\n\ts_waitcnt vmcnt(0)\n\ts_mov_b64 exec, -1" :: "v"(O_POL + 256 + ((r ^ wave) & 1) * 8192 + wave * 1280), "v"(0u), "s"(a.ws) : "memory");
            const unsigned d = (unsigned)(__builtin_readcyclecounter() - t0);
            if ((r ^ wave) & 1) best[1] = min(best[1], d); else best[0] = min(best[0], d);
        }
        *(LAS v2u*)(lds + 40960 + wave * 8) = (v2u){best[0], best[1]};
        lds_barrier();
        const v4u p = lds16(lds + 40960), q = lds16(lds + 40976);
        const bool second = min(min(p.y, p.w), min(q.y, q.w)) < min(min(p.x, p.z), min(q.x, q.z));
        if (tid == 0) __hip_atomic_store((gu32*)(a.ws + O_POL), (second ^ (__builtin_amdgcn_s_getreg((3 << 11) | 20) >> 2)) & 1, RLX_AGENT);
    }
    RSTAMP(72, 0);
    for (unsigned spins = 0; idx != 59 && __any(lane < 8 && __hip_atomic_load((gu32*)(ctl + C_M + 32 * (lane & 7)), RLX_AGENT) < 16u);) {
        if (boss) serve(false);
        __builtin_amdgcn_s_sleep(8);
        if (spin_fail(spins, ctl + C_ABORT)) asm volatile("s_endpgm");
    }
    RSTAMP(73, 0);
    f32x4 c1[4], sh[4];
    auto prep = [&]() {
        f32x4 m[2];
        mod8(rs, 8 * tid, m);
        *(LAS f32x4*)(lds + 32 * tid) = m[0]; *(LAS f32x4*)(lds + 32 * tid + 16) = m[1];
        lds_barrier();
#pragma unroll
        for (int i = 0; i < 4; ++i) { const int col = 512 * (i >> 1) + 8 * lane + 4 * (i & 1); sh[i] = *(const LAS f32x4*)(lds + col * 4); c1[i] = *(const LAS f32x4*)(lds + (DM + col) * 4); }
    };
    prep();
    auto row = [&](int k, int r) {
        v4u o[2];
        ln_row(xr[k], c1, sh, o);
#pragma unroll
        for (int c = 0; c < 2; ++c) {
            const bool od = lane & 1;
            const unsigned y0 = __builtin_amdgcn_update_dpp(0, od ? o[c].x : o[c].z, 0xB1, 0xF, 0xF, false), y1 = __builtin_amdgcn_update_dpp(0, od ? o[c].y : o[c].w, 0xB1, 0xF, 0xF, false);
            bst16(rs, O_H1 + (unsigned)(r * DM + 512 * c + 8 * lane) * 2, od ? (v4u){y0, o[c].z, y1, o[c].w} : (v4u){o[c].x, y0, o[c].y, y1});
        }
    };
    if (idx != 59) row(0, r0);
    if (idx == 55) row(1, r0 + 16);
    drained(ctl + C_A + 32 * (idx & 7));
    RSTAMP(75, 2);
    if (boss) { gv = 2 + wait_set<4>(a.ws, ctl); serve(true); }
    rt_delay(idx >= 64 ? TD_M : TD_L, t0);
    if (idx >= 64) {
#pragma unroll
        for (int k = 0; k < 6; ++k) ldx(k, 6 * r0 - 1024 + k);
        for (unsigned v = NARM0 + r0 - 256; v < NARM1; v += 256) bst16(rs, arm0(v) + lane * 16, sentv());
#pragma unroll 1
        for (int k = 0; k < 6; ++k) {
            row(0, 6 * r0 - 1024 + k);
#pragma unroll
            for (int i = 0; i < 10; ++i) xr[i >> 1][i & 1] = xr[(i >> 1) + 1][i & 1];
        }
        drained(ctl + C_H);
        return;
    }
    const unsigned pol = wait_set(a.ws, ctl);
    if (idx < 32) {
#pragma unroll 1
        for (int h = 0; h < 2; ++h) { mod_cols<32>(a, rs, lds, h ? (idx < 16 ? idx + 32 : idx + 64) : idx + 48, 0, []() {}); lds_barrier(); }
    }
    if (idx == 51 && wave == 0) {
        PARS;
        if (!wave_wait<8>(ctl + C_Q, 1, 1, 128, ctl + C_ABORT)) return;
        const unsigned now = rt_now(), d = __hip_atomic_load((gu32*)(ctl + C_Q + 1), RLX_AGENT) + par(P_T0) - now;
        if (lane < 8) __hip_atomic_store((gu32*)(a.ws + O_HW + H_D + 128 + lane * 2048), (now + (d < par(P_T0) && d > 80 ? d : 80)) | 1, RLX_AGENT);
    }
    if (boss) {
        for (unsigned spins = 0; (__builtin_amdgcn_readfirstlane(ldg4_sc1(a.ws, hw(pol, 7) + H_C + 512)) & 127) < 3;) {
            serve(false);
            __builtin_amdgcn_s_sleep(64);
            if (spin_fail(spins, ctl + C_ABORT)) break;
        }
    }
}
#define TD_W 1300
__device__ __forceinline__ unsigned r_ew(unsigned pol, unsigned lane) { return ((lane >> 4) & 1) * 65536 + (((O_E1 >> 21) ^ pol ^ (lane >> 5)) & 1) * 8192 + (lane & 15) * 16; }
__device__ __forceinline__ unsigned r_e1(int cb) { return O_E1 + ((cb >> 2) & 3) * 16384 + (cb >> 4) * 1024 + (cb & 3) * 256; }
__device__ __forceinline__ unsigned r_db(int cb) { return D_X2 + (cb >> 3) * 16384 + (cb & 7) * 1024; }
__device__ __forceinline__ unsigned r_st(int cb, unsigned lane) { return D_ST + (cb >> 5) * 16384 + (lane >> 5) * 8192 + (cb & 31) * 256 + (lane & 31) * 8; }
__device__ __forceinline__ void r_main(const Args& a, rsrc_t rs, unsigned char* lds, int xg, int i8, unsigned* ctl, unsigned t0) {
    const unsigned lane = lane_id(), wave = a.pad0, l15 = lane & 15, g = lane >> 4, q = l15 >> 2, p = lane & 3, cb = 8 * xg + i8, ng = (cb >> 2) & 3, nx = NX;
    const unsigned pol = wait_set(a.ws, ctl), lb = (unsigned)(size_t)(LAS unsigned char*)lds;
    *(volatile LAS unsigned*)(lds + 4096) = 0u;
    lds_barrier();
    if (threadIdx.x == 0) __hip_atomic_fetch_add((gu32*)(ctl + C_Q), 1u, RLX_AGENT);
    RSTAMP(74, 0);
    const v16u c0 = {(lane & 8) ? 0xEEEEu : 0x4444u, lane * 16, 4096 + lane * 16, lb + wave * 1024 + l15 * 16 + (g & 1) * 8 + (g >> 1) * 4,
                     lb + wave * 1024 + (8 * (p >> 1) + q) * 16 + (p & 1) * 8, lb + wave * 1024 + ((2 * (g & 1) + (p >> 1)) ^ (cb & 1)) * 256 + (8 * (g >> 1) + q) * 16 + (p & 1) * 8,
                     r_ew(pol, lane), (wave * 32 + (lane & 31)) * 2048 + cb * 32 + (lane >> 5) * 16, nearg(pol, O_FL + wave * 16384, xg >> 2) + ((cb >> 4) & 1) * 4096 + (lane & 31) * 128, r_st(cb, lane) - r_db(cb), lb + 4096, (g >> 1) * 1024 + (g & 1) * 256 + l15 * 16,
                     O_MOD + (2 * DM + 16 * cb + 8 * (lane >> 5)) * 4, (lane >> 3) * 16384 + 4096 + (lane & 7) * 128, 0u, 0u};
    const unsigned sd = ((pol ^ (xg >> 2)) & 1) * 8192;
    PARS;
    const v16u s0 = {wave, wave, par(P_A), par(P_B), par(P_EU), par(P_DMIN), par(P_R), par(P_TP), par(P_TU), wave == 0 ? par(P_A0) : wave == 1 ? par(P_A1) : 0,
                     O_UX + (cb & 31) * 65536 + sd + nx * 16384, O_UM + (cb & 31) * 16384 + sd + ng * 2048, O_EP + ((xg >> 1) & 1) * 131072 + sd + nx * 32768, O_PS + sd, r_e1(cb), O_D + r_db(cb)};
    const u64 dbg = DBG(a, blockIdx.x);
    const v8u s1 = {i8 ? 0u : hw(pol, xg) + H_C + 512 * (wave & 1), (cb & 1) * 65536 + ng * 16384, lb + 8192 + wave * 32, lb + 8192, (unsigned)dbg, (unsigned)(dbg >> 32), (unsigned)(u64)a.x, (unsigned)((u64)a.x >> 32)};
    const v4u rws = mkrs(a.ws, WS_END);
    const v8u s2 = {(ng ^ nx) * 16384, 0u, par(P_TF), par(P_X2), hw(pol, xg) + H_D, par(P_TT), t0, par(P_B2)};
    asm volatile("s_nop 4\n" ASM_R :: "{v[0:15]}"(c0), "{s[32:47]}"(s0), "{s[48:55]}"(s1), "{s[56:63]}"(s2), "{s[64:67]}"(rws) : "memory", "s95", "v253", "a255");
}

typedef __attribute__((ext_vector_type(16))) float f32x16;
template <bool is2> __device__ __forceinline__ void f_main(const Args& a, rsrc_t rs, unsigned char* lds, int xg, int kq, int cg, unsigned* ctl) {
    constexpr int L_C = 131072, L_S = L_C + 8192, NG = is2 ? 7 : 8, LD = is2 ? DM : FF;
    const int tid = otid(), lane = tid & 63, wave = a.pad0, l15 = lane & 15, g = lane >> 4;
    const unsigned lb = (unsigned)(size_t)(LAS unsigned char*)lds, q = l15 >> 2, p = lane & 3, l16 = lane * 16;
    const unsigned pol = __builtin_amdgcn_readfirstlane(ldg4_sc1(a.ws, O_POL));
    auto gr = [&](int G) { const int x = G + 2 * ((blockIdx.x >> 3) & 3); return x >= NG ? x - NG : x; };
    lds_barrier();
    v4u w[64];
    f32x16 u2;
    {
        v4u rW = mkrs(is2 ? a.w2 : a.w1, DM * FF * 2);
        unsigned bo[2];
#pragma unroll
        for (int i = 0; i < 2; ++i) bo[i] = (g * LD + 16 * ((l15 >> 1) ^ (g | i << 2)) + 8 * (lane & 1)) * 2;
        asm volatile("s_nop 4" : "+s"(rW));
        auto issue = [&](int G) {
            const unsigned r0 = 896 * kq + 128 * gr(G) + 32 * wave;
#pragma unroll
            for (int j = 0; j < 8; ++j) bdma<(NTW & 8)>(rW, bo[(j >> 1) & 1], ((r0 + 4 * j) * LD + 128 * cg) * 2, lb + (G & 3) * 32768 + wave * 8192 + j * 1024);
        };
        issue(0); issue(1); issue(2); issue(3);
        const unsigned ox = is2 ? (4 * (g >> 1) + q) * 256 + 32 * ((2 * (kq ^ (2 * (g & 1) + (p >> 1))) + (wave >> 1)) ^ q) + 16 * (wave & 1) + 8 * (p & 1)
                                : (8 * (g >> 1) + q) * 256 + 32 * ((2 * wave + (g & 1)) ^ (q | (g >> 1) << 2)) + 8 * p;
        sfor<0, NG>([&](auto gg) {
            constexpr int G = decltype(gg)::v;
            asm volatile("s_waitcnt vmcnt(%0)\n\ts_barrier" :: "n"(G < NG - 3 ? 24 : 8 * (NG - 1 - G)) : "memory");
#pragma unroll
            for (int k = 0; k < 8; ++k) {
                const unsigned o = (G & 3) * 32768 + k * 4096;
                const v2u lo = tr_rd(lds, o + ox), hi = tr_rd(lds, o + (is2 ? (ox ^ 128) + 2048 : ox + 1024));
                w[8 * G + k] = (v4u){lo.x, lo.y, hi.x, hi.y};
            }
#pragma unroll
            for (int k = 8 * G; k < 8 * G + 8; ++k) asm volatile("" : "+a"(w[k]));
            lds_barrier();
            if (G + 4 < NG) issue(G + 4);
        });
    }
    const unsigned sd = ((pol ^ (xg >> 2)) & 1) * 8192;
    v16u c0 = {l16, lb + l16, is2 ? min(lane, 6) * 16384 + wave * 2048 + 2044 : D_ST - D_X2 + (l15 >> 3) * 16384 + ((l15 & 6) * 4 + 2 * wave + (lane & 1)) * 256, lb + L_S + wave * 256 + l15 * 16, lb + L_S + (lane & 31) * 8, 0u, 0xbdd2d3e8u, 0xbdd2d3e8u, 0xc0135761u, 0xc0135761u, 0x3f800000u, 0x3f800000u, 0u, 0u, 0u, 0u};
    if (is2) {
        const int col = 128 * cg + 32 * kq + 8 * wave + 4 * (lane >> 5);
        const v2u br = *(const v2u*)(a.b2 + col);
        f32x4 gv;
        ld_mod<1>(rs, ctl, {5 * DM + col}, &gv);
        const int f1 = 7 * (kq & 1) + min(lane, 6);
        c0[3] = hw(pol, (xg & 4) + (f1 >> 2)) + H_P + 256 * (f1 & 3); c0[4] = r_db(col >> 4) - (kq & 1) * 7 * 16384 + (wave & 1) * 512 + (lane & 31) * 16 + (lane >> 5) * 8; c0[5] = ((lane & 31) * DM + col) * 2;
        c0[8] = br.x << 16; c0[9] = br.x & 0xFFFF0000u; c0[10] = br.y << 16; c0[11] = br.y & 0xFFFF0000u;
#pragma unroll
        for (int i = 0; i < 4; ++i) c0[12 + i] = __float_as_uint(gv[i]);
    } else {
#pragma unroll
        for (int h = 0; h < 4; ++h) {
            const v2u br = *(const v2u*)(a.b1 + 128 * cg + 32 * wave + 8 * h + 4 * (g >> 1));
            u2[4 * h] = lo16f(br.x); u2[4 * h + 1] = hi16f(br.x); u2[4 * h + 2] = lo16f(br.y); u2[4 * h + 3] = hi16f(br.y);
        }
        f32x4 md[2];
        const int k0 = 128 * gr(tid >> 5) + 4 * (tid & 31);
        ld_mod<2>(rs, ctl, {4 * DM + k0, 3 * DM + k0}, md);
        *(LAS f32x4*)(lds + L_C + 16 * tid) = md[0]; *(LAS v2u*)(lds + L_C + 4096 + 8 * tid) = (v2u){pack2(md[1][0], md[1][1]), pack2(md[1][2], md[1][3])};
        lds_barrier();
        c0[12] = lb + L_C + 32 * (g >> 1); c0[13] = lb + L_C + 4096 + 16 * (g >> 1);
    }
    if (!wave_wait<16>(ctl + C_H, 1, 1, 64, ctl + C_ABORT)) asm volatile("s_endpgm");
    RSTAMP(74, 0);
    const u64 dbg = DBG(a, blockIdx.x);
    PARS;
    const v16u s0 = {0u, (unsigned)wave, is2 ? par(P_T1) : -par(P_TZ), par(P_TS), O_D + (is2 ? (kq & 1) * 7 * 16384 : D_X2) + sd, lb + wave * 2048, is2 ? 0u : hw(pol, xg) + H_C, hw(pol, xg) + H_P + 256 * ((blockIdx.x >> 3) & 3),
                     O_D + (cg >= 14 ? cg - 14 : cg) * 16384 + wave * 2048 + sd, D_ST - D_X2 + (wave >> 1) * 16384 + (wave & 1) * 4096, lb + L_S + 2048 + (wave == STAMP_WAVE ? 0 : MiB), lb + L_S + 2048,
                     (unsigned)dbg, (unsigned)(dbg >> 32), (unsigned)(u64)a.out, (unsigned)((u64)a.out >> 32)};
    v16u s1;
#pragma unroll
    for (int i = 0; i < 16; ++i) {
        const int o = kq ^ (i & 3);
        s1[i] = i < 8 ? gr(i) * 16384 + wave * 2048 : O_SL + ((cg * 2 + (o & 1)) * 2 + ((i >> 1) & 1)) * 16384 + ((pol ^ (o >> 1)) & 1) * 8192 + (i & 1) * 4096 + wave * 1024;
    }
    s1[12] = is2 ? par(P_T2) : par(P_TY); s1[13] = is2 ? par(P_T2) + 10 : -par(P_TW);
    const v4u rws = mkrs(a.ws, WS_END);
    v32u ww[8];
#pragma unroll
    for (int i = 0; i < NG; ++i) {
        const v16u x0 = __builtin_shufflevector(__builtin_shufflevector(w[8 * i], w[8 * i + 1], 0, 1, 2, 3, 4, 5, 6, 7), __builtin_shufflevector(w[8 * i + 2], w[8 * i + 3], 0, 1, 2, 3, 4, 5, 6, 7), 0, 1, 2, 3, 4, 5, 6, 7, 8, 9, 10, 11, 12, 13, 14, 15);
        const v16u x1 = __builtin_shufflevector(__builtin_shufflevector(w[8 * i + 4], w[8 * i + 5], 0, 1, 2, 3, 4, 5, 6, 7), __builtin_shufflevector(w[8 * i + 6], w[8 * i + 7], 0, 1, 2, 3, 4, 5, 6, 7), 0, 1, 2, 3, 4, 5, 6, 7, 8, 9, 10, 11, 12, 13, 14, 15);
        ww[i] = __builtin_shufflevector(x0, x1, 0, 1, 2, 3, 4, 5, 6, 7, 8, 9, 10, 11, 12, 13, 14, 15, 16, 17, 18, 19, 20, 21, 22, 23, 24, 25, 26, 27, 28, 29, 30, 31);
    }
#define F_IN "{v[0:15]}"(c0), "{s[32:47]}"(s0), "{s[48:63]}"(s1), "{s[64:67]}"(rws), "{a[0:31]}"(ww[0]), "{a[32:63]}"(ww[1]), "{a[64:95]}"(ww[2]), "{a[96:127]}"(ww[3]), "{a[128:159]}"(ww[4]), "{a[160:191]}"(ww[5]), "{a[192:223]}"(ww[6])
    if constexpr (is2) asm volatile("s_nop 4\n" ASM_F2 :: F_IN : "memory", "s95");
    else asm volatile("s_nop 7\ns_nop 7\n" ASM_F1 :: F_IN, "{a[224:255]}"(ww[7]), "{v[32:47]}"(u2) : "memory", "s95");
}

#define NTS ""
__global__ void __launch_bounds__(256, 1) k_main(Args) {
    extern __shared__ __attribute__((aligned(16))) unsigned char lds[];
    const int xg = blockIdx.x & 7, ii = blockIdx.x >> 3;
    int wv = __builtin_amdgcn_readfirstlane(threadIdx.x >> 6); asm volatile("" : "+s"(wv));
    asm volatile("s_load_dwordx8 s[8:15], %0, 0\n s_load_dword s16, %0, 0x68\n s_getpc_b64 s[4:5]\n s_bfe_u32 s3, %1, 0x20003\n s_lshl_b32 s3, s3, 15\n v_lshl_add_u32 v1, %2, 7, s3\n v_min_u32 v1, %4, v1\n"
                 "s_mul_i32 s17, %3, 24576\n s_mov_b32 m0, s17\n s_cmp_gt_u32 %1, 127\n global_load_lds_dword v1, s[4:5]\n s_cbranch_scc1 3f\n"
                 "v_and_b32 v1, 63, %2\n v_lshrrev_b32 v2, 3, v1\n v_lshrrev_b32 v3, 4, v1\n v_and_b32 v3, 2, v3\n v_xor_b32 v1, v1, v3\n v_and_b32 v1, 7, v1\n v_lshlrev_b32 v1, 4, v1\n v_lshl_add_u32 v1, v2, 13, v1\n"
                 "s_and_b32 s3, %1, 7\n s_lshl_b32 s3, s3, 2\n s_lshr_b32 s18, %1, 5\n s_add_u32 s3, s3, s18\n s_lshl_b32 s3, s3, 1\n s_and_b32 s18, %3, 1\n s_add_u32 s3, s3, s18\n v_lshl_add_u32 v1, s3, 7, v1\n v_xor_b32 v2, 64, v1\n"
                 "s_bfe_u32 s19, %1, 0x10004\n s_lshl_b32 s19, s19, 22 - %6\n s_and_b32 s18, %1, 4 * %6\n s_lshl_b32 s18, s18, 20\n s_or_b32 s19, s19, s18\n s_lshl_b32 s20, %3, 14\n s_add_u32 s20, s20, %5\n s_add_u32 m0, s20, 0x2000\n s_bfe_u32 s21, %1, 0x10003\n s_lshl_b32 s21, s21, 19\n s_lshr_b32 s18, %3, 1\n s_mul_i32 s22, s18, s21\n"
                 "s_mov_b32 s6, 0x800000\n s_mov_b32 s7, 0x20000\n s_mov_b32 s23, 0\n s_waitcnt lgkmcnt(0)\n s_cmp_lg_u32 s18, 0\n s_cselect_b64 s[4:5], s[10:11], s[8:9]\n s_cmp_lg_u32 s21, 0\n s_cselect_b64 s[4:5], s[12:13], s[4:5]\n s_and_b32 s5, s5, 0xffff\n"
                 "1: s_xor_b32 s3, s22, s19\n buffer_load_dwordx4 v1, s[4:7], s3 offen " NTS "lds\n s_add_u32 m0, m0, 0x400\n s_add_u32 s3, s3, 0x10000\n buffer_load_dwordx4 v2, s[4:7], s3 offen " NTS "lds\n s_add_u32 m0, m0, 0x400\n s_add_u32 s22, s22, 0x20000\n"
                 "s_add_u32 s23, s23, 1\n s_and_b32 s3, s23, 3\n s_cbranch_scc1 1b\n s_add_u32 s22, s22, s21\n s_cmp_eq_u32 s23, 4\n s_cselect_b32 m0, s17, m0\n s_cmp_eq_u32 s23, 16\n s_cselect_b32 m0, s20, m0\n s_cmp_lt_u32 s23, 20\n s_cbranch_scc1 1b\n"
                 "3: s_waitcnt lgkmcnt(0)"
                 :: "s"(__builtin_amdgcn_kernarg_segment_ptr()), "s"(blockIdx.x), "v"(threadIdx.x), "s"(wv), "n"(CODE_KB * 1024 - 4), "n"(L_H), "n"(NF)
                 : "memory", "scc", "m0", "v1", "v2", "v3", "s3", "s4", "s5", "s6", "s7", "s8", "s9", "s10", "s11", "s12", "s13", "s14", "s15", "s16", "s17", "s18", "s19", "s20", "s21", "s22", "s23");
    const unsigned t0 = rt_now();
#define MODE_ blockIdx.x
    auto args = [&]() { PARS; Args r = *(const Args*)pp_; r.pad0 = wv; r.mode = MODE_; return r; };
    const Args a = args();
    if (threadIdx.x == 0) __hip_atomic_store((gu32*)(a.ws + O_GO + blockIdx.x * 128), 0u, RLX_AGENT);
    RSTAMP(75, 0);
    if (ii < 16) {
        if (ii & 1) s1_main<true>(args(), lds, xg, ii); else s1_main<false>(args(), lds, xg, ii);
        return;
    }
    const rsrc_t rs = __builtin_amdgcn_make_buffer_rsrc((void*)a.ws, (short)0, (int)WS_END, 0x00020000);
    unsigned* const ctl = (unsigned*)(a.ws + O_CTL);
    const int i4 = ii & 3, cg = 4 * (xg & 3) + i4;
    f_start(args(), rs, lds, (blockIdx.x - 64) & 127, t0);
    if (ii < 24) {
        rt_delay(TD_W - 200, t0);
        if (wv < 2) {
            v4u r = wv ? mkrs(a.x, S_TOK * DM * 2) : mkrs(a.ws, WS_END);
            asm volatile("s_mov_b32 m0, 0x8000\n\ts_nop 4\n\tbuffer_load_dword %0, %1, 0 offen sc1 lds" :: "v"(wv ? lane_id() * 65536 : lane_id() * 2 * MiB + (ii - 16) * 262144 + 7168), "s"(r) : "m0");
        }
        rt_delay(TD_W, t0);
        r_main(args(), rs, lds, xg, ii - 16, ctl, t0);
        return;
    }
    RSTAMP(75, 1);
    if (ii >= 28 && cg >= 14) return;
    if (ii < 28) f_main<true>(args(), rs, lds, xg, xg >> 1, 4 * (xg & 1) + i4, ctl);
    else f_main<false>(args(), rs, lds, xg, 0, 14 * (xg >> 2) + cg, ctl);
}

extern "C" void kernel_launch(void* const* d_in, const int* in_sizes, int n_in,
                              void* d_out, int out_size, void* d_ws, size_t ws_size,
                              hipStream_t stream) {
    Args a;
    a.x = (const u16*)d_in[0]; a.mem0 = (const u16*)d_in[1]; a.Wq = (const u16*)d_in[2]; a.Wk = (const u16*)d_in[3];
    a.Wv = (const u16*)d_in[4]; a.Wo = (const u16*)d_in[5];
    a.w1 = (const u16*)d_in[8]; a.b1 = (const u16*)d_in[9]; a.w2 = (const u16*)d_in[10]; a.b2 = (const u16*)d_in[11];
    a.out = (u16*)d_out; a.ws = (char*)d_ws + (size_t)WSB * MiB; a.mode = 0; a.pad0 = 0;
    a.c = (const u16*)d_in[12]; a.w_mod = (const u16*)d_in[13];
    hipFuncSetAttribute((const void*)k_main, hipFuncAttributeMaxDynamicSharedMemorySize, LDS_BYTES);
    k_main<<<256, 256, LDS_BYTES, stream>>>(a);
}
```
